```python
import torch

def _softmax(x, axis=-1):
    return torch.softmax(x, dim=axis)

def _gelu(x):
    return torch.nn.functional.gelu(x, approximate='tanh')

def _silu(x):
    return torch.nn.functional.silu(x)

def _sigmoid(x):
    return torch.sigmoid(x)

def _layernorm(x, axis=-1, eps=1e-05):
    m = x.mean(dim=axis, keepdim=True)
    v = x.var(dim=axis, keepdim=True, unbiased=False)
    return (x - m) / torch.sqrt(v + eps)

def _scatter_add(tgt, idx, src, axis=0):
    return tgt.clone().index_add_(axis, idx, src)

def _segment_sum(src, seg, n):
    out = torch.zeros((n,) + src.shape[1:], dtype=src.dtype)
    return out.index_add_(0, seg, src)

def _pad(x, pw):
    flat = tuple((v for pair in reversed(pw) for v in pair))
    return torch.nn.functional.pad(x, flat)
kernel_name = 'adaLN_cond__sliding_block_cached__glu_gated_swiglu__decode'

def setup_inputs():

    _w = lambda a: (lambda t: t.to(torch.bfloat16) if t.is_floating_point() else t)(torch.as_tensor(a))

    def _A_setup():
        B, Sq, D = (2, 1, 512)
        Hq, Hkv, Sk, Dh = (32, 32, 16384, 128)
        assert Hq % Hkv == 0, (Hq, Hkv)
        return {'x': _w((lambda *s: torch.randn(*s, dtype=torch.bfloat16) * 0.05)(B, Sq, D)), 'k_cache': _w((lambda *s: torch.randn(*s, dtype=torch.bfloat16) * 3.2)(B, Hkv, Sk, Dh)), 'v_cache': _w((lambda *s: torch.randn(*s, dtype=torch.bfloat16) * 3.2)(B, Hkv, Sk, Dh)), 'wq': _w((lambda *s: torch.randn(*s, dtype=torch.bfloat16) * 3.2)(D, Hq * Dh) * D ** (-0.5)), 'wo': _w((lambda *s: torch.randn(*s, dtype=torch.bfloat16) * 3.2)(Hq * Dh, D) * (Hq * Dh) ** (-0.5))}
    _A_in = _A_setup()

    def _B_setup():
        B, S, D, F = (2, 1, 512, 1376)
        return {'x': _w((lambda *s: torch.randn(*s, dtype=torch.bfloat16) * 0.05)(B, S, D)), 'w1': _w((lambda *s: torch.randn(*s, dtype=torch.bfloat16) * 1.6)(D, F) * (2.0 / D) ** 0.5), 'w3': _w((lambda *s: torch.randn(*s, dtype=torch.bfloat16) * 1.6)(D, F) * (2.0 / D) ** 0.5), 'w2': _w((lambda *s: torch.randn(*s, dtype=torch.bfloat16) * 1.6)(F, D) * (2.0 / F) ** 0.5)}
    _B_in = _B_setup()
    return {'A_x': _A_in['x'], 'A_k_cache': _A_in['k_cache'], 'A_v_cache': _A_in['v_cache'], 'A_wq': _A_in['wq'], 'A_wo': _A_in['wo'], 'B_x': _B_in['x'], 'B_w1': _B_in['w1'], 'B_w3': _B_in['w3'], 'B_w2': _B_in['w2'], 'c': _w((lambda *s: torch.randn(*s, dtype=torch.bfloat16) * 0.2)(2, 512) * 8.0), 'w_mod': _w((lambda *s: torch.randn(*s, dtype=torch.bfloat16) * 0.2)(512, 6 * 512) * 512 ** (-0.5) * 8.0)}

def reference(A_x, A_k_cache, A_v_cache, A_wq, A_wo, B_x, B_w1, B_w3, B_w2, c, w_mod):

    def _A_ref(x, k_cache, v_cache, wq, wo):
        B, Sq, D = x.shape
        Hq, Dh = (32, 128)
        Hkv, Sk = (k_cache.shape[1], k_cache.shape[2])
        W = min(4096, max(256, Sk // 4))
        rep = Hq // Hkv
        q = (lambda x, *d: x.permute(*d))((x @ wq).reshape(B, Sq, Hq, Dh), 0, 2, 1, 3)
        k = (lambda x, n, axis: torch.repeat_interleave(x, n, dim=axis))(k_cache[:, :, Sk - W:, :], rep, axis=1)
        v = (lambda x, n, axis: torch.repeat_interleave(x, n, dim=axis))(v_cache[:, :, Sk - W:, :], rep, axis=1)
        scores = torch.einsum('bhqd,bhkd->bhqk', q, k) * Dh ** (-0.5)
        p = _softmax(scores, axis=-1)
        o = torch.einsum('bhqk,bhkd->bhqd', p, v)
        return (lambda x, *d: x.permute(*d))(o, 0, 2, 1, 3).reshape(B, Sq, Hq * Dh) @ wo

    def _B_ref(x, w1, w3, w2):
        gate = torch.einsum('bsd,df->bsf', x, w1)
        up = torch.einsum('bsd,df->bsf', x, w3)
        h = _silu(gate) * up
        out = torch.einsum('bsf,fd->bsd', h, w2)
        return out
    x = A_x
    mod = (_silu(c) @ w_mod).reshape(c.shape[0], 6, 1, -1)
    sa, ca, ga, sb, cb, gb = (mod[:, 0].clone(), mod[:, 1].clone(), mod[:, 2].clone(), mod[:, 3].clone(), mod[:, 4].clone(), mod[:, 5].clone())
    h = _layernorm(x, axis=-1) * (1 + ca) + sa
    x = x + ga * _A_ref(h, A_k_cache, A_v_cache, A_wq, A_wo)
    h = _layernorm(x, axis=-1) * (1 + cb) + sb
    x = x + gb * _B_ref(h, B_w1, B_w3, B_w2)
    return x
```

```cpp
#define PROBE_MUL 10
#include <hip/hip_runtime.h>
#include <stdint.h>

typedef unsigned int u32;
typedef unsigned short u16;
typedef unsigned long long u64;
typedef __attribute__((ext_vector_type(4))) unsigned u32x4;
typedef __attribute__((ext_vector_type(2))) unsigned u32x2;
typedef __attribute__((ext_vector_type(2))) __bf16 bf16x2;
typedef __attribute__((ext_vector_type(4))) float f32x4;
typedef __attribute__((ext_vector_type(2))) float f32x2;
typedef __amdgpu_buffer_rsrc_t rsrc_t;

constexpr int NH = 32, SK = 16384, DH = 128, WIN = 4096, FF = 1376;
struct Prm { int E0, ES, E1, E2, E2C, EU, TEND, DT_UP, L0, L1, X1, X2, X3, X4, NE, FE, M0, M1, M2, M3, M4, GS, GX, N1, N2, GA, WD, DT_WO, LW, LU, EW, PM; };
constexpr Prm PRM0 = {32, 24, 32, 32, 56, 64, 2030, 60, 30, 25, 92, 86, 131, 88, 4, 12, 5, 8, 21, 4, 32, 30, 56, 28, 70, 12, 4, 100, 45, 15, 8, 0x24310};
#define PRM_ARG
constexpr int NL = 37;
constexpr int NCMIN = 192;
constexpr int WK = 8, WV = 5;
constexpr int WUSE = 12, LNS = 6;
constexpr int QP = 14, QD = 4;
constexpr int VQ = 6;
constexpr int NSL = (NL + 7) / 8;
constexpr int VS = VQ + NSL;
constexpr int MLS = VS + 2;
constexpr int VSIDE = MLS - 5, UPREP = MLS + 8;
constexpr int SIDE = NCMIN / 8 - 4;
constexpr int CPF = 18;
constexpr int TBP = 3, TBL = 10, TBU = 15;
constexpr int cons_end(int v) {
  const int a = v - WV + 1, c = (v - MLS) * 4;
  const int m = a < c ? a : c;
  return m < 0 ? 0 : (m > NL ? NL : m);
}
struct Tab { u32 e[512]; };
constexpr Tab make_tab(const Prm& P) {
  Tab T{};
  for (int bid = 0; bid < 256; ++bid) {
    const int xcd = bid & 7, idx = bid >> 3, part = idx & 3, i8 = idx >> 2, wi = i8 * 8 + xcd;
    const bool has_mod = wi < 48, has_up = wi >= 21;
    const int cb0 = (i8 + 5) & 7, cb1 = (i8 + 4) & 7, ms = wi - 20;
    const int mi = wi < 8 ? wi : (wi < 16 ? 8 + ((xcd + 4) & 7) : (wi < 20 ? (wi - 14) * 8 + 3 : 16 + ms / 7 * 8 + ms % 7 + (ms % 7 >= 3 ? 1 : 0)));
    const int ui = xcd * 5 + (xcd > 5 ? xcd - 5 : 0) + (i8 >= 3 ? (xcd >= 5 ? 1 : 0) + i8 - 3 : 0);
    const int wd = cb0 == 3 ? 0 : P.WD;
    const int e[4] = {P.E0 - 4 * wd + (cb0 == 3 ? P.ES : 0), P.E1 + (cb1 == 3 ? P.ES : 0), has_mod ? (wi < 16 ? P.E2C : P.E2) + ((mi & 7) == 3 ? P.ES : 0) : 0, has_up ? P.EU : 0};
    const int ew = wd ? P.EW : 0, pa = (1024 + e[0] + ew + e[1] + e[2] + e[3]) / 4;
    int c[5] = {0, 0, 0, 0, 128};
    for (int k = 0; k < 3; ++k) c[k + 1] = c[k] + (pa - e[k] - (k == 0 ? ew : 0) + 4) / 8;
    const int nl = c[part + 1] - c[part], wst = e[part] / 4;
    const bool has_w = part < 2 || (part == 2 ? has_mod : has_up);
    const int blk = !has_w ? 0 : (part == 0 ? cb0 : (part == 1 ? xcd * 8 + cb1 : (part == 2 ? mi : ui)));
    const int tau = (part == 3 && has_up ? P.TEND - P.DT_UP : (part == 0 && wd ? P.TEND - P.DT_WO : P.TEND)) * 256 / (2 * nl + wst);
    T.e[2 * bid] = (u32)(c[part] | nl << 7 | blk << 13 | (has_w ? 1 : 0) << 19 | (part == 0 ? wd : 0) << 20);
    T.e[2 * bid + 1] = (u32)(tau | (tau * wst / (part == 0 ? 8 - wd : 8)) << 16);
  }
  return T;
}
constexpr int step_range(const Prm& P, bool mx_) {
  int mx = 0, mn = 1 << 20;
  const Tab T = make_tab(P);
  for (int bid = 0; bid < 256; ++bid) { const int n = T.e[2 * bid] >> 7 & 63; mx = n > mx ? n : mx; mn = n < mn ? n : mn; }
  return mx_ ? mx : mn;
}
__constant__ const Tab TAB = make_tab(PRM0);
static_assert(step_range(PRM0, true) <= NL && step_range(PRM0, false) >= NCMIN / 8, "a wave's key range does not fit its load steps");
static_assert(cons_end(SIDE + WV + 1) <= NCMIN / 8 && cons_end(UPREP) <= NCMIN / 8 && SIDE > MLS + 3, "steps every wave runs");
constexpr int NUP = FF / 32;
constexpr int SPIN_MAX = 1 << 17;

constexpr u32 WS_EPOCH = 0;
constexpr u32 G_TB = 1024;
constexpr u32 G_NT = 2048;
constexpr u32 G_MOD = 4096;
constexpr u32 G_Q = G_MOD + 3072 * 8;
constexpr u32 G_ML = G_Q + 4096 * 8;
constexpr u32 G_P = G_ML + 256 * 16;
constexpr u32 G_XP = G_P + 256 * 256;
constexpr u32 G_OP = G_XP + 8 * 512 * 4;
constexpr u32 WS_END = G_OP + 43 * 512 * 8;
constexpr u32 SENT = 0xffffffffu;
constexpr u32 OOB = 0x40000000u;
#define WST 0

constexpr int AUX_NT = 2, AUX_SC1 = 16;

__device__ __forceinline__ float lo_f(u32 v) { return __uint_as_float(v << 16); }
__device__ __forceinline__ float hi_f(u32 v) { return __uint_as_float(v & 0xffff0000u); }
__device__ __forceinline__ float rbf(float x) { return (float)(__bf16)x; }
__device__ __forceinline__ u32 pack_bf(float lo, float hi) {
  bf16x2 r; r[0] = (__bf16)lo; r[1] = (__bf16)hi; return __builtin_bit_cast(u32, r);
}
__device__ __forceinline__ float dot2(u32 k, u32 q, float acc) {
  return __builtin_amdgcn_fdot2_f32_bf16(__builtin_bit_cast(bf16x2, k), __builtin_bit_cast(bf16x2, q), acc, false);
}
template <int CTRL> __device__ __forceinline__ float dpp(float x) {
  return __builtin_bit_cast(float, __builtin_amdgcn_mov_dpp(__builtin_bit_cast(int, x), CTRL, 0xf, 0xf, true));
}
constexpr int XOR1 = 0xB1, XOR2 = 0x4E, XOR7 = 0x141, XOR8 = 0x128;
__device__ __forceinline__ float swap32_add(float a, float b) {
  auto t = __builtin_amdgcn_permlane32_swap(__float_as_uint(a), __float_as_uint(b), false, false);
  return __uint_as_float(t[0]) + __uint_as_float(t[1]);
}
__device__ __forceinline__ float swap16_add(float a, float b) {
  auto t = __builtin_amdgcn_permlane16_swap(__float_as_uint(a), __float_as_uint(b), false, false);
  return __uint_as_float(t[0]) + __uint_as_float(t[1]);
}
__device__ __forceinline__ float wave_sum(float x) {
  x += dpp<XOR1>(x); x += dpp<XOR2>(x); x += dpp<XOR7>(x); x += dpp<XOR8>(x);
  x = swap16_add(x, x);
  return swap32_add(x, x);
}
__device__ __forceinline__ float wave_max(float x) {
  x = fmaxf(x, dpp<XOR1>(x)); x = fmaxf(x, dpp<XOR2>(x)); x = fmaxf(x, dpp<XOR7>(x)); x = fmaxf(x, dpp<XOR8>(x));
  auto s = __builtin_amdgcn_permlane16_swap(__float_as_uint(x), __float_as_uint(x), false, false);
  x = fmaxf(__uint_as_float(s[0]), __uint_as_float(s[1]));
  auto t = __builtin_amdgcn_permlane32_swap(__float_as_uint(x), __float_as_uint(x), false, false);
  return fmaxf(__uint_as_float(t[0]), __uint_as_float(t[1]));
}
template <int CTRL> __device__ __forceinline__ float sel_add(float lo, float hi, bool up) {
  const float t1 = lo + dpp<CTRL>(lo), t2 = hi + dpp<CTRL>(hi);
  return up ? t2 : t1;
}
template <int I> __device__ __forceinline__ float bc16(float x) {
  return __builtin_bit_cast(float, __builtin_amdgcn_update_dpp(0, __builtin_bit_cast(int, x), 0x150 + I, 0xf, 0xf, false));
}
__device__ __forceinline__ float bcast16(float x, int i) {
  switch (i) {
    case 0: return bc16<0>(x); case 1: return bc16<1>(x); case 2: return bc16<2>(x); case 3: return bc16<3>(x);
    case 4: return bc16<4>(x); case 5: return bc16<5>(x); case 6: return bc16<6>(x); case 7: return bc16<7>(x);
    case 8: return bc16<8>(x); case 9: return bc16<9>(x); case 10: return bc16<10>(x); case 11: return bc16<11>(x);
    case 12: return bc16<12>(x); case 13: return bc16<13>(x); case 14: return bc16<14>(x); default: return bc16<15>(x);
  }
}
__device__ __forceinline__ float silu_f(float x) { return x * __builtin_amdgcn_rcpf(1.f + __expf(-x)); }

template <int AUX> __device__ __forceinline__ u32x4 ld16(rsrc_t r, u32 voff, u32 soff) {
  return __builtin_amdgcn_raw_buffer_load_b128(r, voff, soff, AUX);
}
template <int AUX> __device__ __forceinline__ u32x2 ld8(rsrc_t r, u32 voff, u32 soff) {
  return __builtin_amdgcn_raw_buffer_load_b64(r, voff, soff, AUX);
}
__device__ __forceinline__ void st8_sc1(rsrc_t r, u32 voff, u32 v, u32 tag) {
  __builtin_amdgcn_raw_buffer_store_b64(u32x2{v, tag}, r, voff, 0, AUX_SC1);
}
__device__ __forceinline__ void st16_sc1(rsrc_t r, u32 voff, u32 v0, u32 v1, u32 tag) {
  __builtin_amdgcn_raw_buffer_store_b128(u32x4{v0, tag, v1, tag}, r, voff, 0, AUX_SC1);
}
__device__ __forceinline__ rsrc_t mk_rsrc(const void* p, u32 bytes) {
  return __builtin_amdgcn_make_buffer_rsrc(const_cast<void*>(p), 0, bytes, 0x00020000);
}
#define WAIT_VM(n) __builtin_amdgcn_s_waitcnt(((n) & 15) | (((n) >> 4) << 14) | (7 << 4) | (15 << 8))
#define SB() __builtin_amdgcn_sched_barrier(0)
#define CFENCE() asm volatile("" ::: "memory")
__device__ __forceinline__ bool all_ok(bool ok) { return __builtin_amdgcn_ballot_w64(!ok) == 0ull; }

template <int N> __device__ __forceinline__ void gather16(rsrc_t ws, u32 voff, u32 stride, u32 tag, u32x4 (&g)[N]) {
#pragma nounroll
  for (int spin = 0; spin < SPIN_MAX; ++spin) {
    CFENCE();
    bool ok = true;
#pragma unroll
    for (int i = 0; i < N; ++i) g[i] = ld16<AUX_SC1>(ws, voff + i * stride, 0);
#pragma unroll
    for (int i = 0; i < N; ++i) ok &= (g[i][1] == tag) & (g[i][3] == tag);
    if (all_ok(ok)) break;
    __builtin_amdgcn_s_sleep(8);
  }
}
template <int N> __device__ __forceinline__ void gather8(rsrc_t ws, u32 voff, u32 stride, u32 tag, u32 (&g)[N], bool act = true) {
#pragma nounroll
  for (int spin = 0; spin < SPIN_MAX; ++spin) {
    CFENCE();
    bool ok = true;
    u32x2 t[N];
#pragma unroll
    for (int i = 0; i < N; ++i) t[i] = ld8<AUX_SC1>(ws, voff + i * stride, 0);
#pragma unroll
    for (int i = 0; i < N; ++i) { ok &= (t[i][1] == tag); g[i] = t[i][0]; }
    if (all_ok(ok | !act)) break;
    __builtin_amdgcn_s_sleep(8);
  }
}

struct Smem {
  float red[8][128];
  float ml[8][2];
  float r2[64 * 20];
  u16 hb[3 * 1024];
  float stat[8][64];
  float red2[8][128];
  u32 dummy[64];
  u32x2 bc;
  u32x4 zero[4];
  u32 mflag[8];
  u32x4 wbuf[8][8][64];
  __attribute__((aligned(256))) u16 w2b[32][512];
  u16 actl[2][32];
};

#define PROBE_K 102
#define PROBE_OP 2
#define PROBE_PART -1
#define STAMP(k) do { if ((k) == PROBE_K && tid == 0) pst = wall_clock64(); } while (0)
#define WSTAMP(k) do {} while (0)
#define FINISH() do { STAMP(8); if (tid == 0) { __hip_atomic_store(reinterpret_cast<u64*>(ws + (1u << 20)) + bid * 2, pt0, __ATOMIC_RELAXED, __HIP_MEMORY_SCOPE_AGENT); \
   __hip_atomic_store(reinterpret_cast<u64*>(ws + (1u << 20)) + bid * 2 + 1, (PROBE_PART < 0 || part == PROBE_PART) ? pst : 0ull, __ATOMIC_RELAXED, __HIP_MEMORY_SCOPE_AGENT); } \
   if (bid == 8 && wave == 0) probe_end(ws, pt0, lane); return; } while (0)
#define PIN(x) asm volatile("" : "+v"(x))
#define LDS_BARRIER() do { __builtin_amdgcn_s_waitcnt(0xc07f); __builtin_amdgcn_s_barrier(); CFENCE(); } while (0)
#define WAVE_SYNC() do { __builtin_amdgcn_fence(__ATOMIC_RELEASE, "wavefront"); __builtin_amdgcn_wave_barrier(); __builtin_amdgcn_fence(__ATOMIC_ACQUIRE, "wavefront"); } while (0)

__device__ __forceinline__ void dma16(rsrc_t r, u32 voff, u32 soff, u32 lds_dst) {
  u32 keep;
  asm volatile("s_mov_b32 %0, m0\n\ts_mov_b32 m0, %4\n\ts_nop 4\n\tbuffer_load_dwordx4 %1, %2, %3 offen nt lds\n\ts_mov_b32 m0, %0"
               : "=&s"(keep) : "v"(voff), "s"(r), "s"(soff), "s"(lds_dst) : "memory");
}
__device__ __forceinline__ u32 perm_lo(u32 a, u32 b) { return __builtin_amdgcn_perm(b, a, 0x05040100u); }
__device__ __forceinline__ u32 perm_hi(u32 a, u32 b) { return __builtin_amdgcn_perm(b, a, 0x07060302u); }
__device__ __forceinline__ float rdl(float x, int l) { return __int_as_float(__builtin_amdgcn_readlane(__float_as_int(x), l)); }

typedef __attribute__((ext_vector_type(8))) __bf16 bf16x8;
__device__ __forceinline__ f32x4 mfma16(u32x4 a, u32x4 b, f32x4 c) {
  return __builtin_amdgcn_mfma_f32_16x16x32_bf16(__builtin_bit_cast(bf16x8, a), __builtin_bit_cast(bf16x8, b), c, 0, 0, 0);
}
__device__ __forceinline__ float sum8(const float* p) {
  const f32x4 a = *reinterpret_cast<const f32x4*>(p), b = *reinterpret_cast<const f32x4*>(p + 4);
  return ((a[0] + b[0]) + (a[1] + b[1])) + ((a[2] + b[2]) + (a[3] + b[3]));
}
__device__ __forceinline__ float ln_ri(float var) {
  return __builtin_amdgcn_rcpf(rbf(__builtin_amdgcn_sqrtf(rbf(rbf(fmaxf(var, 0.f)) + 1e-5f))));
}
__device__ __forceinline__ void ln_stats2(const float (&x0)[8], const float (&x1)[8], float& mb0, float& ri0, float& mb1, float& ri1) {
  const float k0 = rdl(x0[0], 0), k1 = rdl(x1[0], 0);
  float s0 = 0.f, s1 = 0.f, q0 = 0.f, q1 = 0.f;
#pragma unroll
  for (int k = 0; k < 8; ++k) {
    const float d0 = x0[k] - k0, d1 = x1[k] - k1;
    s0 += d0; s1 += d1; q0 = fmaf(d0, d0, q0); q1 = fmaf(d1, d1, q1);
  }
  float t = swap16_add(swap32_add(s0, s1), swap32_add(q0, q1));
  t += dpp<XOR1>(t); t += dpp<XOR2>(t); t += dpp<XOR7>(t); t += dpp<XOR8>(t);
  auto u = __builtin_amdgcn_permlane16_swap(__float_as_uint(t), __float_as_uint(t), false, false);
  const float ms = __uint_as_float(u[0]) * (1.f / 512.f), mq = __uint_as_float(u[1]) * (1.f / 512.f);
  const float ri = ln_ri(fmaf(-ms, ms, mq));
  mb0 = rbf(k0 + rdl(ms, 0)); mb1 = rbf(k1 + rdl(ms, 32));
  ri0 = rdl(ri, 0); ri1 = rdl(ri, 32);
}
template <int I> struct IC { static constexpr int v = I; };
template <int I, int N, class F> __device__ __forceinline__ void sfor(F&& f) {
  if constexpr (I < N) { if (f(IC<I>{})) sfor<I + 1, N>(f); }
}
typedef __attribute__((ext_vector_type(4))) short s16x4;
typedef __attribute__((address_space(3))) s16x4* lds_s16x4p;
__device__ __forceinline__ u32x2 tr_read(u32 lds_addr) {
  return __builtin_bit_cast(u32x2, __builtin_amdgcn_ds_read_tr16_b64_v4i16((lds_s16x4p)lds_addr));
}
__device__ __forceinline__ u32 next_tag(u32 e) { return ((e | 0x40004000u) + 1u) & ~0x40004000u; }
__device__ __forceinline__ float u2f(u32 v) { return __uint_as_float(v); }
#define LDSP(T, a) ((__attribute__((address_space(3))) T*)(a))
template <class T> __device__ __forceinline__ u32 lds_addr(T* p) { u32 a = (u32)(uintptr_t)p; PIN(a); return a; }

__device__ __forceinline__ void probe_end(unsigned char* ws, u64 pt0, int lane) {
  while (wall_clock64() < pt0 + 5500ull) __builtin_amdgcn_s_sleep(8);
  u64 a[4], c2[4];
  for (int i = 0; i < 4; ++i) {
    a[i] = __hip_atomic_load(reinterpret_cast<u64*>(ws + (1u << 20)) + (i * 64 + lane) * 2, __ATOMIC_RELAXED, __HIP_MEMORY_SCOPE_AGENT);
    c2[i] = __hip_atomic_load(reinterpret_cast<u64*>(ws + (1u << 20)) + (i * 64 + lane) * 2 + 1, __ATOMIC_RELAXED, __HIP_MEMORY_SCOPE_AGENT);
  }
  int mn0 = 1 << 30, mx1 = -(1 << 30), mn1 = 1 << 30, sum = 0, cnt = 0;
  for (int i = 0; i < 4; ++i) {
    const int d0 = (int)(a[i] - pt0);
    mn0 = min(mn0, d0);
    if (c2[i] != 0ull) { const int d1 = (int)(c2[i] - pt0); mx1 = max(mx1, d1); mn1 = min(mn1, d1); sum += d1; cnt += 1; }
  }
  for (int o = 1; o < 64; o <<= 1) { mn0 = min(mn0, __shfl_xor(mn0, o)); mx1 = max(mx1, __shfl_xor(mx1, o)); mn1 = min(mn1, __shfl_xor(mn1, o)); sum += __shfl_xor(sum, o); cnt += __shfl_xor(cnt, o); }
  int X = PROBE_OP == 0 ? mx1 : (PROBE_OP == 1 ? sum / max(cnt, 1) : mn1);
  if (PROBE_K == 102) X = mn0;
  X = mn0 + (X - mn0) * PROBE_MUL;
  const u64 tg = pt0 + (u64)(long long)(X + 8000);
  for (int i = 0; i < 400000 && wall_clock64() < tg; ++i) __builtin_amdgcn_s_sleep(1);
}
__global__ void __launch_bounds__(512, 2) fused_kernel(const u16* __restrict__ x, const u16* __restrict__ kc, const u16* __restrict__ vc,
                                                       const u16* __restrict__ wq, const u16* __restrict__ wo, const u16* __restrict__ w1,
                                                       const u16* __restrict__ w3, const u16* __restrict__ w2, const u16* __restrict__ cnd,
                                                       const u16* __restrict__ wmod, unsigned char* ws, u16* __restrict__ out PRM_ARG) {
  __shared__ __attribute__((aligned(256))) Smem sm;
  constexpr Prm P = PRM0;
  const int TEND = P.TEND;
  const u64 pt0 = wall_clock64(); u64 pst = (PROBE_K == 100 || PROBE_K == 102) ? pt0 : 0;
  const u32 t0 = (u32)pt0;
  const int bid = blockIdx.x, xcd = bid & 7, idx = bid >> 3, part = idx & 3;
  const int tid = threadIdx.x, lane = tid & 63, wave = __builtin_amdgcn_readfirstlane(tid >> 6);
  u32 tch2 = 0;
  if ((idx | 4) == 7) {
    u64 pc; u32 len;
    asm volatile("s_getpc_b64 %0\n.Lcpf_first:\n\ts_mov_b32 %1, .Lend_a-.Lcpf_first\n\ts_max_i32 %1, %1, .Lend_b-.Lcpf_first\n\ts_max_i32 %1, %1, .Lend_c-.Lcpf_first\n\ts_max_i32 %1, %1, .Lend_d-.Lcpf_first\n\ts_max_i32 %1, %1, .Lend_e-.Lcpf_first" : "=s"(pc), "=s"(len) :: "scc");
    tch2 = __builtin_amdgcn_raw_buffer_load_b32(mk_rsrc((const void*)pc, len), tid * 64, 0, 0);
  }
  SB();
  const bool p3 = part == 3;
  typedef __attribute__((address_space(4))) const u64* kap_t;
  const u64 wa = ((kap_t)__builtin_amdgcn_kernarg_segment_ptr())[((0x5934 >> (part * 4)) & 15) + (p3 ? wave & 1 : 0)];
  const u32 te0 = TAB.e[2 * bid], te1 = TAB.e[2 * bid + 1];
  u32 pitch = (u32)(0x005600c001000020ull >> (part * 16) & 0xffff) * 32u;
  const int sh = p3 ? 2 : 3;
  u32 wv0 = (u32)((p3 ? (wave >> 1) * 128 : wave * 64) + (part == 0 ? xcd * 512 : 0) + (lane >> sh)) * pitch + ((lane & (p3 ? 3 : 7)) ^ (lane >> (p3 ? 5 : 3))) * 16u;
  const u32 wimg = (u32)(uintptr_t)(&sm.wbuf[0][0][0]);
  u32 wsz = (part == 0 ? 4096u : 512u) * pitch, wld = wimg + (p3 ? (wave & 1) * 32768 + (wave >> 1) * 8192 : wave * 8192);
  asm volatile("" : "+v"(wv0), "+s"(wsz), "+s"(wld), "+s"(pitch));
  asm volatile("" :: "s"(x), "s"(kc), "s"(vc), "s"(w2), "s"(cnd), "s"(ws), "s"(out), "s"(wa), "s"(te0), "s"(te1));
  const int bl0 = te0 & 127, nl = te0 >> 7 & 63, blk = te0 >> 13 & 63;
  const bool has_w = te0 >> 19 & 1;
  const rsrc_t rw = mk_rsrc((const void*)wa, wsz);
  const u32 wstep = pitch << (6 - sh), wflip = p3 ? 32u : 0u;
  u32 wso = blk << (sh + 4);
  int wleft = 8;
  auto piece = [&]() {
    dma16(rw, wv0, wso, wld);
    wso += wstep; wld += 1024;
    wv0 ^= (wleft & 1) ? wflip : 0u;
    --wleft;
  };
  const u32 tka = (u32)wall_clock64();
  if (has_w) piece();
  SB();
  STAMP(23);
  const int b = idx >> 2 & 1, head = xcd * 4 + (((idx >> 3) & 1) << 1 | idx >> 4);
  const rsrc_t wsr = mk_rsrc(ws, WS_END);
  const int g = lane >> 4, c = lane & 15;
  const int i8 = idx >> 2, wi = i8 * 8 + xcd;
  const bool is_up = p3 && has_w, modc = part == 2 && wi < 16;
  const size_t base = ((size_t)(b * NH + head) * SK + (SK - WIN)) * DH;
  const rsrc_t rk = mk_rsrc(kc + base, WIN * DH * 2), rv = mk_rsrc(vc + base, WIN * DH * 2);
  u32 sot;
  {
    const u32 s0 = bl0 + lane;
    u32 sb = s0 & 96u;
#pragma unroll
    for (int k = 0; k < 5; ++k) sb |= (s0 >> k & 1u) << (P.PM >> (4 * k) & 7);
    const u32 src = (sb >> 5 & 3u) | (u32)(b * 32 + head) << 2;
    sb ^= (__builtin_popcount(src & P.M0) & 1) | (__builtin_popcount(src & P.M1) & 1) << 1 | (__builtin_popcount(src & P.M2) & 1) << 2 | (__builtin_popcount(src & P.M3) & 1) << 3 | (__builtin_popcount(src & P.M4) & 1) << 4;
    sot = (sb & 7u) << 10 | (u32)wave << 13 | (sb >> 3) << 16;
    sot = lane < nl ? sot : OOB;
    PIN(sot);
  }
  u32x4 kr[NL];
  int tau = te1 & 0xffff;
  const int tauw = te1 >> 16;
  int due = 0, now = 0;
  auto gate = [&]() {
    while (now < due) { now = (int)(((u32)wall_clock64() - t0) << 8); if (now < due) __builtin_amdgcn_s_sleep(1); }
    due += tau;
  };
  auto until = [&](int t) { while ((int)(((u32)wall_clock64() - t0) << 8) < t); };
  const u32 pslot = G_P + ((b * 32 + head) * 4 + part) * 256;
  const rsrc_t rx = mk_rsrc(part == 2 ? cnd : x, (part == 1 || (part == 2 && has_w)) ? 2048u : 0u);
  u32x2 ep; u32 xs0, xs1, tch1; u32x4 xr0, xr1;
  auto small_ops = [&]() {
    ep = ld8<AUX_SC1>(wsr, wave == 7 ? WS_EPOCH : OOB, 0);
    xs0 = __builtin_amdgcn_raw_buffer_load_b16(rx, tid * 2, 0, 0); xs1 = __builtin_amdgcn_raw_buffer_load_b16(rx, tid * 2, 1024, 0);
    xr0 = ld16<0>(rx, part == 1 ? lane * 16 : OOB, 0); xr1 = ld16<0>(rx, part == 1 ? lane * 16 : OOB, 1024);
    tch1 = __builtin_amdgcn_raw_buffer_load_b32(mk_rsrc(w2 + (size_t)blk * 32 * 512, is_up ? 64u : 0u), tid == 384 ? 0 : OOB, 0, AUX_NT);
  };
  if (modc) __builtin_amdgcn_raw_buffer_store_b128(u32x4{SENT, SENT, SENT, SENT}, wsr, tid < 320 ? G_P + wi * 5120 + tid * 16 : OOB, 0, AUX_SC1);
  if (has_w) {
    if (modc) now = 1 << 30;
#pragma nounroll
    while (wleft > (int)(te0 >> 20)) { due += tauw; gate(); due -= tau; piece(); }
    due += tauw; now = 0;
  }
  SB();
  u32 tag = 0;
  u32x4 bf[8];
  auto wo_prep = [&](int s0 = 0, int s1 = 2) {
    const u32 wi_ = wimg + wave * 8192;
    const int q = (lane >> 2) & 3, pp = lane & 3;
#pragma unroll
    for (int s = s0; s < s1; ++s) {
#pragma unroll
      for (int t = 0; t < 4; ++t) {
        const int r0 = s * 32 + g * 8 + q, r1 = r0 + 4, ch = t * 2 + (pp >> 1);
        const u32x2 lo = tr_read(wi_ + r0 * 128 + ((ch ^ (r0 & 7)) * 16) + (pp & 1) * 8), hi = tr_read(wi_ + r1 * 128 + ((ch ^ (r1 & 7)) * 16) + (pp & 1) * 8);
        bf[s * 4 + t] = u32x4{lo[0], lo[1], hi[0], hi[1]};
      }
    }
  };
  auto gemv_go = [&](u32 hh) {
    sm.hb[tid] = (u16)hh; sm.hb[512 + tid] = (u16)(hh >> 16);
    WAVE_SYNC();
    const u32x4* a = (c & 11) == 0 ? reinterpret_cast<const u32x4*>(&sm.hb[(c >> 2) * 512 + wave * 64 + g * 8]) : &sm.zero[0];
    const u32x4 a0 = a[0], a1 = a[(c & 11) == 0 ? 4 : 0];
    const f32x4 z = {0.f, 0.f, 0.f, 0.f};
    f32x4 d[4];
#pragma unroll
    for (int t = 0; t < 4; ++t) d[t] = mfma16(a0, bf[t], z);
#pragma unroll
    for (int t = 0; t < 4; ++t) d[t] = mfma16(a1, bf[4 + t], d[t]);
    if (lane < 32) {
#pragma unroll
      for (int t = 0; t < 4; ++t) sm.r2[(t * 16 + c) * 20 + g * 8 + wave] = d[t][0];
    }
  };
  auto mod_stage = [&]() {
    wo_prep();
    gemv_go(pack_bf(silu_f(lo_f(xs0)), silu_f(lo_f(xs1))));
    __builtin_amdgcn_s_waitcnt(0xc07f);
    if (lane == 0) *LDSP(volatile u32, (u32)(uintptr_t)&sm.mflag[wave]) = 1u;
    if (wave == 7) {
#pragma nounroll
      for (int spin = 0; spin < SPIN_MAX; ++spin) {
        const u32 f = *LDSP(volatile u32, (u32)(uintptr_t)&sm.mflag[lane & 7]);
        if (all_ok(f != 0u)) break;
        __builtin_amdgcn_s_sleep(1);
      }
      CFENCE();
      st8_sc1(wsr, G_MOD + (blk * 64 + lane) * 8, pack_bf(sum8(&sm.r2[lane * 20]), sum8(&sm.r2[lane * 20 + 8])), tag);
    }
    STAMP(9);
  };
  if (lane < 4) sm.zero[lane] = u32x4{0u, 0u, 0u, 0u};
  if (part == 2 && has_w) {
    if (tid < 8) sm.mflag[tid] = 0u;
    LDS_BARRIER();
    if (__builtin_expect(modc, 0)) {
      small_ops();
      asm volatile("s_waitcnt vmcnt(0)" ::: "memory");
      tag = next_tag(__builtin_amdgcn_readfirstlane(ep[0]));
      mod_stage();
      __builtin_amdgcn_s_barrier();
    }
  }
  SB();
  const int fd = wi * 8 + (lane & 7);
  const int ub = wave >> 2, ud = (wave & 3) * 128 + c * 8 + g * 2;
  u32x4 sg[3]; u32 sx2;
  auto side_loads = [&]() {
    const bool fin = part == 1 && wave == 0;
    sg[0] = ld16<AUX_SC1>(wsr, is_up ? G_MOD + (1024 + ud) * 8 : (fin ? G_MOD + (1024 + fd) * 8 : OOB), 0);
    sg[1] = ld16<AUX_SC1>(wsr, is_up ? G_MOD + (1536 + ud) * 8 : (fin ? G_MOD + (2560 + fd) * 8 : OOB), 0);
    sg[2] = ld16<AUX_SC1>(wsr, is_up ? G_MOD + (2048 + ud) * 8 : OOB, 0);
    sx2 = __builtin_amdgcn_raw_buffer_load_b32(mk_rsrc(x, (is_up || fin) ? 2048u : 0u), is_up ? ub * 1024 + ud * 2 : (g & 1) * 1024 + (fd >> 1) * 4, 0, 0);
  };
  auto side_pin = [&]() { PIN(sg[0]); PIN(sg[1]); PIN(sg[2]); PIN(sx2); };
  const u32 mlo = G_ML + (head * 2 + b) * 64 + (lane & 3) * 16;
  float fmrg = 0.f;
  bool fok = wave != 0;
  auto merge_factor = [&](const u32x4& ml) {
    const float mi = u2f(ml[0]);
    float M = fmaxf(mi, dpp<XOR1>(mi)); M = fmaxf(M, dpp<XOR2>(M));
    const float e = __expf(mi - M);
    float L = e * u2f(ml[2]);
    L += dpp<XOR1>(L); L += dpp<XOR2>(L);
    fmrg = rdl(e * __builtin_amdgcn_rcpf(L), part);
  };
  f32x2 ga = {0.f, 0.f}, xin = {0.f, 0.f}, cc1 = {0.f, 0.f};
  float Af = 0.f, Bf = 0.f;
  const int hpos = ub * 512 + ud;
  auto up_prep = [&]() {
    if (!all_ok((sg[0][1] == tag) & (sg[0][3] == tag) & (sg[1][1] == tag) & (sg[1][3] == tag) & (sg[2][1] == tag) & (sg[2][3] == tag))) gather16<3>(wsr, G_MOD + (1024 + ud) * 8, 512 * 8, tag, sg);
    ga = f32x2{ub ? hi_f(sg[0][0]) : lo_f(sg[0][0]), ub ? hi_f(sg[0][2]) : lo_f(sg[0][2])};
    cc1 = f32x2{rbf(1.f + (ub ? hi_f(sg[2][0]) : lo_f(sg[2][0]))), rbf(1.f + (ub ? hi_f(sg[2][2]) : lo_f(sg[2][2])))};
    xin = f32x2{lo_f(sx2), hi_f(sx2)};
    u32* hp = reinterpret_cast<u32*>(&sm.hb[hpos]);
    hp[512] = pack_bf(cc1[0], cc1[1]);
    hp[1024] = ub ? perm_hi(sg[1][0], sg[1][2]) : perm_lo(sg[1][0], sg[1][2]);
  };
  auto up_frags = [&]() {
    const int pp = lane & 3;
    const u32 a = wimg + (pp & 1) * 32768 + ((pp >> 1) * 32 + g * 8 + ((lane >> 2) & 3)) * 64 + (((wave >> 1) ^ ((g & 1) | (pp & 2))) * 16) + (wave & 1) * 8;
#pragma unroll
    for (int rb = 0; rb < 8; ++rb) {
      const u32x2 lo = tr_read(a + rb * 4096), hi = tr_read(a + rb * 4096 + 256);
      bf[rb] = u32x4{lo[0], lo[1], hi[0], hi[1]};
    }
  };
  const u32x4* ap = reinterpret_cast<const u32x4*>(&sm.hb[((lane >> 3) & 1) * 512 + (lane & 1) * 32 + g * 8]);
  const bool par = lane & 8;
  float l0 = 0.f, l1 = 0.f;
  u32x2 gmd[2], tbg;
  int t1 = 0;
#pragma unroll
  for (int i = NCMIN / 8; i < NL; ++i) kr[i] = u32x4{0u, 0u, 0u, 0u};
  sfor<0, NL>([&](auto ic) {
    constexpr int i = ic.v;
    if (i >= NCMIN / 8 && i >= nl) return false;
    const u32 so = __builtin_amdgcn_readlane(sot, i);
    gate();
    SB();
    kr[i] = ld16<AUX_NT>(rk, lane * 16, so);
    if constexpr (i == 0) small_ops();
    if constexpr (i == TBL) tbg = ld8<AUX_SC1>(wsr, wave == 7 ? G_TB + xcd * 128 + (lane & 7) * 8 : OOB, 0);
    if constexpr (i == QP) { gmd[0] = ld8<AUX_SC1>(wsr, part == 1 ? G_MOD + tid * 8 : OOB, 0); gmd[1] = ld8<AUX_SC1>(wsr, part == 1 ? G_MOD + tid * 8 : OOB, 4096); }
    SB();
    SB();
    if (i >= WK) { WAIT_VM(WK); SB(); }
    if (i == 0) STAMP(25);
    if (i == 8) STAMP(12);
    if (i == 20) STAMP(21);
    if (i == 25) STAMP(22);
    if (i == TBP) {
      if (part == 3 && i8 == 0 && wave == 7) { tag = next_tag(__builtin_amdgcn_readfirstlane(ep[0])); if (lane < 8) st8_sc1(wsr, G_TB + lane * 128 + xcd * 8, t0 + (((tka - t0) * P.GA) >> 3) - P.GS, tag); }
      SB();
    }
    if (i == TBU) {
      if (wave == 7) {
        const bool ok = tbg[1] == tag;
        float lf = ok ? (float)min(max((int)(t0 - tbg[0]), -300), 300) : 0.f, nf = ok ? 1.f : 0.f;
        lf += dpp<XOR1>(lf); nf += dpp<XOR1>(nf); lf += dpp<XOR2>(lf); nf += dpp<XOR2>(nf); lf += dpp<XOR7>(lf); nf += dpp<XOR7>(nf);
        if (lane == 0) sm.bc = u32x2{tag, t0 - (u32)(int)(lf * __builtin_amdgcn_rcpf(fmaxf(nf, 1.f)))};
      }
      LDS_BARRIER();
      const u32x2 bc = sm.bc;
      tag = __builtin_amdgcn_readfirstlane(bc[0]);
      t1 = (__builtin_amdgcn_readfirstlane((int)(bc[1] - t0)) + TEND + P.X1) * 256;
      tau -= __builtin_amdgcn_readfirstlane((int)((float)(int)(t0 - bc[1]) * 256.f * __builtin_amdgcn_rcpf((float)(2 * nl - TBU - 2))));
      SB();
    }
    if (i == LNS) {
      tag = next_tag(__builtin_amdgcn_readfirstlane(ep[0]));
      if (part == 1) {
        float x0[8], x1[8], mb0, ri0, mb1, ri1;
#pragma unroll
        for (int k = 0; k < 4; ++k) { x0[2 * k] = lo_f(xr0[k]); x0[2 * k + 1] = hi_f(xr0[k]); x1[2 * k] = lo_f(xr1[k]); x1[2 * k + 1] = hi_f(xr1[k]); }
        ln_stats2(x0, x1, mb0, ri0, mb1, ri1);
        l0 = rbf(rbf(lo_f(xs0) - mb0) * ri0); l1 = rbf(rbf(lo_f(xs1) - mb1) * ri1);
      }
      PIN(l0); PIN(l1);
      SB();
    }
    if (i == WUSE) {
      PIN(tch1); PIN(tch2);
      if ((part == 1 || (part == 2 && has_w && !modc))) {
        WAIT_VM(WK); CFENCE();
        if (part == 2) mod_stage(); else wo_prep();
      }
#pragma unroll
      for (int k = 0; k < 8; ++k) PIN(bf[k]);
      SB();
    }
    if (i == QP + QD) {
      if (part == 1) {
        u32 gm[2] = {gmd[0][0], gmd[1][0]};
        if (!all_ok((gmd[0][1] == tag) & (gmd[1][1] == tag))) { STAMP(11); gather8<2>(wsr, G_MOD + tid * 8, 512 * 8, tag, gm); }
        STAMP(5);
        gemv_go(pack_bf(rbf(l0 * rbf(1.f + lo_f(gm[1]))) + lo_f(gm[0]), rbf(l1 * rbf(1.f + hi_f(gm[1]))) + hi_f(gm[0])));
      }
      SB();
    }
    if (i == QP + QD + 1) {
      if (part == 1) {
        LDS_BARRIER();
        if (wave == 0) {
          const int bb = lane >> 5, jq = lane & 31;
          st8_sc1(wsr, G_Q + (bb * 2048 + blk * 32 + jq) * 8, pack_bf(sum8(&sm.r2[2 * jq * 20 + bb * 8]), sum8(&sm.r2[(2 * jq + 1) * 20 + bb * 8])), tag);
        }
        STAMP(6);
      }
      SB();
    }
    return true;
  });
  PIN(sot);
  const u32 qoff = G_Q + ((b * 32 + head) * 64 + c * 4) * 8;
  u32x4 qg[2], vr[NL], fl2[3];
  u32 qq[4];
  constexpr int NB = (NL + 15) / 16;
  float xlo, sc[NB], pr[NB], m = 0.f;
  f32x4 acc = {0.f, 0.f, 0.f, 0.f};
  u32 pb[NB], pm[4], cpf;
  const u32 adst0 = lds_addr(lane < 32 ? &sm.red[wave][c * 8 + g * 4] : &sm.r2[(lane - 32) * 4]);
  u32 po, rdst, rsrc, xo;
  auto wo_addr = [&]() {
    po = G_P + (((c >> 2 & 1) * 32 + xcd * 4 + (wave >> 1)) * 4 + (c & 3)) * 256 + (wave & 1) * 128 + (c >> 3) * 64 + g * 16;
    rdst = lane < 32 ? (u32)(uintptr_t)&sm.red2[wave][0] + lane * 16 : (u32)(uintptr_t)&sm.r2[0] + (lane - 32) * 16;
    rsrc = (u32)(uintptr_t)&sm.red2[0][((wave & 1) * 16 + (lane & 15)) * 4 + (lane >> 4)];
    xo = (lane & 1) ? OOB : G_XP + xcd * 2048 + (wave & 1) * 1024 + (blk * 64 + lane) * 2;
    PIN(po); PIN(rdst); PIN(rsrc); PIN(xo);
  };
  u32 pso = wave == 0 ? pslot + lane * 4 : OOB;
  PIN(pso);
  const u32 nadd = (te0 >> 20) ? P.LW : (is_up ? P.LU : 0);
  u32 nto = tid == 0 ? G_NT + bid * 8 : OOB;
  PIN(nto);
  auto notices = [&](auto nc_, u32 off, u32x4 (&gn)[nc_.v]) -> int {
    constexpr int N = nc_.v;
#pragma nounroll
    for (int spin = 0; spin < SPIN_MAX; ++spin) {
      bool ok = true;
#pragma unroll
      for (int i = 0; i < N; ++i) ok &= (gn[i][1] == tag) & (gn[i][3] == tag);
      if (all_ok(ok)) break;
      __builtin_amdgcn_s_sleep(10);
      CFENCE();
#pragma unroll
      for (int i = 0; i < N; ++i) gn[i] = ld16<AUX_SC1>(wsr, off, i * 1024);
    }
    int mx = 0;
#pragma unroll
    for (int i = 0; i < N; ++i) mx = max(mx, max((int)(gn[i][0] - t0), (int)(gn[i][2] - t0)));
    return __builtin_amdgcn_readfirstlane((int)wave_max((float)min(mx, 1 << 20)));
  };
  const int side = nl >= SIDE + 13 ? SIDE + 8 : (nl >= SIDE + 9 ? SIDE + 4 : SIDE);
  sfor<0, NL + WV>([&](auto ic) {
    constexpr int v = ic.v;
    if (cons_end(v - 1) >= NCMIN / 8 && cons_end(v - 1) >= nl) return false;
    if constexpr (v < NL) {
      const u32 so = __builtin_amdgcn_readlane(sot, v);
      if constexpr (v >= 12) { if (v == nl - P.NE) tau = (tau * P.FE) >> 4; }
      if constexpr (v >= NCMIN / 8) { if (v < nl) gate(); } else gate();
      SB();
      vr[v] = ld16<AUX_NT>(rv, lane * 16, so);
      SB();
      SB();
    }
    if constexpr (v == NCMIN / 8 - 1) {
#pragma unroll
      for (int i = NCMIN / 8; i < NL; ++i) asm volatile("" : "=v"(vr[i]));
      asm volatile("" : "=v"(fl2[1]), "=v"(fl2[2]));
    }
    if constexpr (v == VQ - 4) { qg[0] = ld16<AUX_SC1>(wsr, qoff, 0); qg[1] = ld16<AUX_SC1>(wsr, qoff + 16, 0); }
    if constexpr (v == VSIDE) side_loads();
    if constexpr (v == CPF) {
      u64 pc; u32 dlt, len;
      asm volatile("s_getpc_b64 %0\n.Lcpf_here:\n\ts_mov_b32 %1, .Ltail_start-.Lcpf_here\n\ts_mov_b32 %2, .Lend_a-.Ltail_start\n\ts_max_i32 %2, %2, .Lend_b-.Ltail_start\n\ts_max_i32 %2, %2, .Lend_c-.Ltail_start\n\ts_max_i32 %2, %2, .Lend_d-.Ltail_start\n\ts_max_i32 %2, %2, .Lend_e-.Ltail_start" : "=s"(pc), "=s"(dlt), "=s"(len) :: "scc");
      pc += dlt;
      const rsrc_t rc = mk_rsrc((const void*)pc, (part == 3 && i8 == 1) ? len : 0u);
      cpf = __builtin_amdgcn_raw_buffer_load_b32(rc, tid * 64, 0, 0);
    }
    if constexpr (v == CPF + WV + 1) PIN(cpf);
    if constexpr (v == SIDE || v == SIDE + 4 || v == SIDE + 8) fl2[(v - SIDE) / 4] = ld16<AUX_SC1>(wsr, (wave == 0 && v == side) ? mlo : OOB, 0);
    SB();
    if constexpr (v < WV) { WAIT_VM(WK); SB(); } else if constexpr (v < NL && cons_end(v) < v - WV + 1) { WAIT_VM(WV); SB(); }
    if constexpr (v == VQ) {
      if (!all_ok((qg[0][1] == tag) & (qg[0][3] == tag) & (qg[1][1] == tag) & (qg[1][3] == tag))) { STAMP(20); gather16<2>(wsr, qoff, 16, tag, qg); }
      STAMP(1); WSTAMP(1);
      qq[0] = qg[0][0]; qq[1] = qg[0][2]; qq[2] = qg[1][0]; qq[3] = qg[1][2];
    }
    if constexpr (v >= VQ && v < VS) {
      constexpr int sl = v - VQ, bt = sl >> 1;
      float xs[8];
#pragma unroll
      for (int i = 0; i < 8; ++i) {
        float d = 0.f;
        if (sl * 8 + i < NL) {
#pragma unroll
          for (int k = 0; k < 4; ++k) d = dot2(kr[sl * 8 + i][k], qq[k], d);
        }
        xs[i] = d;
      }
#pragma unroll
      for (int h = 0; h < 4; ++h) xs[h] = sel_add<XOR7>(xs[h], xs[h + 4], c & 4);
#pragma unroll
      for (int h = 0; h < 2; ++h) xs[h] = sel_add<XOR2>(xs[h], xs[h + 2], c & 2);
      xs[0] = sel_add<XOR1>(xs[0], xs[1], c & 1);
      if constexpr ((sl & 1) || sl == NSL - 1) {
        const float t = (sl & 1) ? sel_add<XOR8>(xlo, xs[0], c & 8) : xs[0] + dpp<XOR8>(xs[0]);
        const float s = rbf(rbf(t) * 0.08838834764831845f);
        sc[bt] = (bt * 16 + c < nl) ? s : -__builtin_inff();
        PIN(sc[bt]);
      } else {
        xlo = xs[0];
        PIN(xlo);
      }
    }
    if constexpr (v == VS) {
      float mloc = sc[0];
#pragma unroll
      for (int bt = 1; bt < NB; ++bt) mloc = fmaxf(mloc, sc[bt]);
      m = fmaxf(wave_max(mloc), -3.0e38f);
      float ls = 0.f;
#pragma unroll
      for (int bt = 0; bt < NB; ++bt) { pr[bt] = __expf(sc[bt] - m); ls += pr[bt]; }
      const float l = wave_sum(ls);
      if (lane == 0) { sm.ml[wave][0] = m; sm.ml[wave][1] = l; }
      STAMP(2); WSTAMP(2);
    }
    sfor<cons_end(v - 1), cons_end(v)>([&](auto jc) {
      constexpr int i = jc.v;
      if (i < NCMIN / 8 || i < nl) {
        const u32 pv = __float_as_uint(bcast16(u2f(pb[i >> 4]), i & 15));
        acc = mfma16(u32x4{pv & pm[0], pv & pm[1], pv & pm[2], pv & pm[3]}, vr[i], acc);
      }
      PIN(acc);
      return true;
    });
    if constexpr (v == MLS - 1) { side_pin(); if (is_up) up_prep(); PIN(ga); PIN(xin); PIN(cc1); }
    if constexpr (v == UPREP - 1) wo_addr();
    if constexpr (v == UPREP) {
#pragma unroll
      for (int k = 0; k < 8; ++k) asm volatile("" : "=v"(bf[k]));
      if (part == 0) wo_prep(0, 1); else if (is_up) up_frags();
#pragma unroll
      for (int k = 0; k < 8; ++k) PIN(bf[k]);
    }
    if constexpr (v == UPREP + 1) {
      if (is_up) {
        const u32x4* a1 = ap + 128 + ((lane >> 1) & 1) * 128;
        f32x4 d0 = {0.f, 0.f, 0.f, 0.f}, d1 = {0.f, 0.f, 0.f, 0.f};
#pragma unroll
        for (int rb = 0; rb < 4; ++rb) { d0 = mfma16(a1[rb * 8], bf[rb], d0); d1 = mfma16(a1[(rb + 4) * 8], bf[rb + 4], d1); }
        Af = par ? d0[1] + d1[1] : d0[0] + d1[0]; Bf = par ? d0[3] + d1[3] : d0[2] + d1[2];
        Af += dpp<XOR8>(Af); Bf += dpp<XOR8>(Bf);
      }
      PIN(Af); PIN(Bf);
    }
    if constexpr (v == SIDE + WV + 1 || v == SIDE + WV + 5 || v == SIDE + WV + 9) {
      if (wave == 0 && v == side + WV + 1) {
        const u32x4 ml = fl2[(v - SIDE - WV - 1) / 4];
        fok = all_ok((ml[1] == tag) & (ml[3] == tag));
        merge_factor(ml);
      }
    }
    if (v % 6 == 4 && v >= 10 && v < 30) STAMP(15 + v / 6);
    if constexpr (v == MLS) {
      LDS_BARRIER();
      float mm[8], ll[8];
#pragma unroll
      for (int w = 0; w < 8; ++w) { const f32x2 t = *reinterpret_cast<const f32x2*>(&sm.ml[w][0]); mm[w] = t[0]; ll[w] = t[1]; }
      float Mwg = mm[0], Lwg = 0.f;
#pragma unroll
      for (int w = 1; w < 8; ++w) Mwg = fmaxf(Mwg, mm[w]);
      if (wave == 0) {
#pragma unroll
        for (int w = 0; w < 8; ++w) Lwg = fmaf(__expf(mm[w] - Mwg), ll[w], Lwg);
        if (lane == 0) st16_sc1(wsr, G_ML + ((head * 2 + b) * 4 + part) * 16, __float_as_uint(Mwg), __float_as_uint(Lwg), tag);
      }
      const float fw = __expf(m - Mwg);
#pragma unroll
      for (int bt = 0; bt < NB; ++bt) { pb[bt] = pack_bf(pr[bt] * fw, pr[bt] * fw); PIN(pb[bt]); }
#pragma unroll
      for (int k = 0; k < 4; ++k) { pm[k] = (c >> 1) == k ? ((c & 1) ? 0xffff0000u : 0x0000ffffu) : 0u; PIN(pm[k]); }
    }
    SB();
    return true;
  });
  asm volatile(".Ltail_start:");
  *LDSP(f32x4, adst0) = acc;
  STAMP(14); WSTAMP(14);
  if (!fok) {
    u32x4 ml;
#pragma nounroll
    for (int spin = 0; spin < SPIN_MAX; ++spin) {
      CFENCE();
      ml = ld16<AUX_SC1>(wsr, mlo, 0);
      if (all_ok((ml[1] == tag) & (ml[3] == tag))) break;
    }
    merge_factor(ml);
  }
  STAMP(15);
  LDS_BARRIER();
  STAMP(3);
  {
    u32 pw = 0;
    if (wave == 0) {
      f32x2 t[8];
#pragma unroll
      for (int w = 0; w < 8; ++w) t[w] = *reinterpret_cast<const f32x2*>(&sm.red[w][2 * lane]);
      const f32x2 o = ((t[0] + t[1]) + (t[2] + t[3])) + ((t[4] + t[5]) + (t[6] + t[7]));
      pw = min(pack_bf(o[0] * fmrg, o[1] * fmrg), SENT - 1u);
    }
    __builtin_amdgcn_raw_buffer_store_b32(pw, wsr, pso, 0, AUX_SC1);
    st8_sc1(wsr, nto, (u32)wall_clock64() + nadd, tag);
    SB();
#pragma unroll
    for (int i = NCMIN / 8; i < NL; ++i) PIN(vr[i]);
    PIN(fl2[0]); PIN(fl2[1]); PIN(fl2[2]);
  }
  STAMP(4);

  if (part == 2) FINISH();
  if (part == 0) {
    __builtin_amdgcn_s_barrier();
#pragma nounroll
    while (wleft > 0) piece();
    until(t1);
    STAMP(11);
    CFENCE();
    u32x4 og[2];
    og[0] = ld16<AUX_SC1>(wsr, po, 0);
    SB();
    WAIT_VM(1); CFENCE();
    wo_prep(1, 2);
#pragma unroll
    for (int k = 4; k < 8; ++k) PIN(bf[k]);
    SB();
    f32x4 d[4];
    auto wo_mm = [&]() {
      const f32x4 z = {0.f, 0.f, 0.f, 0.f};
#pragma unroll
      for (int t = 0; t < 4; ++t) { d[t] = mfma16(og[0], bf[t], z); og[1][t] = __float_as_uint(dpp<XOR8>(u2f(og[0][t]))); }
      SB();
#pragma unroll
      for (int t = 0; t < 4; ++t) d[t] = mfma16(og[1], bf[4 + t], d[t]);
    };
    auto p_bad = [&]() { return !all_ok(max(max(og[0][0], og[0][1]), max(og[0][2], og[0][3])) != SENT); };
    wo_mm();
    if (__builtin_expect(p_bad(), 0)) {
#pragma nounroll
      for (int spin = 0; spin < SPIN_MAX; ++spin) {
        __builtin_amdgcn_s_sleep(1);
        CFENCE();
        og[0] = ld16<AUX_SC1>(wsr, po, 0);
        if (!p_bad()) break;
      }
      wo_mm();
      SB();
      asm volatile(".Lend_d:");
    }
    STAMP(5); WSTAMP(5);
    {
      SB();
      f32x4 y; f32x2 h[4];
#pragma unroll
      for (int t = 0; t < 4; ++t) { h[t] = d[t].xy + d[t].zw; PIN(h[t]); }
#pragma unroll
      for (int t = 0; t < 4; ++t) { y[t] = h[t][0] + h[t][1]; PIN(y[t]); }
      *LDSP(f32x4, rdst) = y;
    }
    STAMP(16);
    LDS_BARRIER();
    STAMP(17);
    if (wave < 2) {
      f32x2 v[4];
#pragma unroll
      for (int w = 0; w < 4; ++w) v[w] = f32x2{LDSP(float, rsrc)[w * 256], LDSP(float, rsrc)[w * 256 + 128]};
      const f32x2 h = (v[0] + v[1]) + (v[2] + v[3]);
      const float y = h[0] + h[1];
      __builtin_amdgcn_raw_buffer_store_b32(min(pack_bf(y, dpp<XOR1>(y)), SENT - 1u), wsr, xo, 0, AUX_SC1);
      asm volatile(".Lend_a:");
    }
    STAMP(6);
    FINISH();
  }
  if (part == 3) {
    if (!has_w) FINISH();
    u32x4 b2[4];
    {
      __builtin_amdgcn_s_barrier();
      const rsrc_t r2 = mk_rsrc(w2 + (size_t)(blk * 32 + wave * 4) * 512, 4096u);
      const u32 l2 = (u32)(uintptr_t)(&sm.w2b[wave * 4][0]);
#pragma unroll
      for (int n = 0; n < 4; ++n) dma16(r2, (lane ^ (n * 2) ^ ((wave & 2) << 2)) * 16, n * 1024, l2 + n * 1024);
    }
    const u32 tdst = lds_addr(&sm.hb[hpos]);
    const u32 sdst = lds_addr(&sm.stat[wave][lane]);
    const u32 ssrc = lds_addr(&sm.stat[(g >> 1) * 4 + ((lane >> 1) & 3)][(lane & 1) * 16]);
    const u32 adst = lds_addr((c < 4 && (g & 1) == 0) ? &sm.actl[g >> 1][wave * 4 + c] : reinterpret_cast<u16*>(&sm.dummy[lane]));
    const u32 asrc = lds_addr(&sm.actl[lane & 1][g * 8]);
    const u32 apa = lds_addr(&sm.hb[(c >> 3) * 512 + (c & 3) * 64 + ((c >> 2) & 1) * 32 + g * 8]);
    u32 xoff = G_XP + g * 2048 + ub * 1024 + (wave & 3) * 256 + c * 16;
    u32x4 one;
#pragma unroll
    for (int k = 0; k < 4; ++k) one[k] = ((c & 2) == 0 && (c >> 2) == k) ? ((c & 1) ? 0x3f800000u : 0x00003f80u) : 0u;
    PIN(one);
    u32 ooff = G_OP + (blk * 512 + tid) * 8;
    float Afp = Af, Bfp = Bf;
    PIN(xoff); PIN(ooff); PIN(Afp); PIN(Bfp);
    PIN(ga); PIN(xin); PIN(cc1);
    u32 w2a = (u32)(uintptr_t)(&sm.w2b[0][0]) + (g * 8 + ((lane >> 2) & 3)) * 1024 + (wave ^ (g & 1)) * 128 + (lane & 15) * 8;
    PIN(w2a);
    if (wave == 1) {
      due = t1 + P.N1 * 256; gate();
      u32x4 gn[2] = {ld16<AUX_SC1>(wsr, G_NT + lane * 16, 0), ld16<AUX_SC1>(wsr, G_NT + lane * 16, 1024)};
      const int tn = notices(IC<2>{}, G_NT + lane * 16, gn) * 256;
      due = (tn > t1 + 2 * 256 ? max(tn, t1 + P.L0 * 256) + P.L1 * 256 : t1) + P.X2 * 256;
      until(due);
    }
    asm volatile("s_waitcnt vmcnt(0)" ::: "memory");
    STAMP(9);
    __builtin_amdgcn_s_barrier();
    STAMP(11);
    CFENCE();
    u32x4 xg[2] = {ld16<AUX_SC1>(wsr, xoff, 0), ld16<AUX_SC1>(wsr, xoff, 8192)};
    SB();
#pragma unroll
    for (int t = 0; t < 4; ++t) {
      const u32x2 lo = tr_read(w2a ^ (t * 32)), hi = tr_read((w2a ^ (t * 32)) + 4096);
      b2[t] = u32x4{lo[0], lo[1], hi[0], hi[1]};
    }
#pragma unroll
    for (int t = 0; t < 4; ++t) PIN(b2[t]);
    SB();
    const f32x4 z4 = {0.f, 0.f, 0.f, 0.f};
    auto xp_bad = [&]() { return !all_ok(max(max(max(xg[0][0], xg[0][1]), max(xg[0][2], xg[0][3])), max(max(xg[1][0], xg[1][1]), max(xg[1][2], xg[1][3]))) != SENT); };
    f32x4 v = mfma16(one, xg[1], mfma16(one, xg[0], z4));
    if (__builtin_expect(xp_bad(), 0)) {
#pragma nounroll
      for (int spin = 0; spin < SPIN_MAX; ++spin) {
        __builtin_amdgcn_s_sleep(1);
        CFENCE();
        xg[0] = ld16<AUX_SC1>(wsr, xoff, 0); xg[1] = ld16<AUX_SC1>(wsr, xoff, 8192);
        if (!xp_bad()) break;
      }
      v = mfma16(one, xg[1], mfma16(one, xg[0], z4));
      SB();
      asm volatile(".Lend_e:");
    }
    STAMP(5); WSTAMP(5);
    {
      const float a0 = v[0], a1 = v[1];
      const float x10 = fmaf(ga[0], a0, xin[0]), x11 = fmaf(ga[1], a1, xin[1]);
      *LDSP(u32, tdst) = pack_bf(x10 * cc1[0], x11 * cc1[1]);
      float u = swap32_add(x10 + x11, fmaf(x10, x10, x11 * x11));
      u += dpp<XOR1>(u); u += dpp<XOR2>(u); u += dpp<XOR7>(u); u += dpp<XOR8>(u);
      *LDSP(float, sdst) = u;
    }
    STAMP(16);
    LDS_BARRIER();
    STAMP(17);
    {
      const f32x4 z = {0.f, 0.f, 0.f, 0.f};
      const u32x4 af0 = LDSP(u32x4, apa)[0], af1 = LDSP(u32x4, apa)[32];
      float s = LDSP(float, ssrc)[0], q = LDSP(float, ssrc)[32];
      SB();
      f32x4 d[4];
#pragma unroll
      for (int j = 0; j < 4; ++j) d[j] = mfma16(af0, bf[j], z);
#pragma unroll
      for (int j = 0; j < 4; ++j) d[j] = mfma16(af1, bf[4 + j], d[j]);
      s += dpp<XOR1>(s); q += dpp<XOR1>(q); s += dpp<XOR2>(s); q += dpp<XOR2>(q); s += dpp<XOR7>(s); q += dpp<XOR7>(q);
      const float mean = s * (1.f / 512.f);
      const float var = fmaf(q, 1.f / 512.f, -mean * mean);
      const float ri = __builtin_amdgcn_rsqf(fmaxf(var, 0.f) + 1e-5f);
      const float cf = fmaf(-ri * mean, Afp, Bfp);
      float y = (d[0][0] + d[1][1]) + (d[2][2] + d[3][3]);
      y = __builtin_bit_cast(float, __builtin_amdgcn_update_dpp(__builtin_bit_cast(int, y), __builtin_bit_cast(int, y), XOR8, 0xa, 0xf, false));
      y = swap16_add(y, y);
      y = fmaf(y, ri, cf);
      const float up = dpp<0x104>(y);
      *LDSP(u16, adst) = (u16)pack_bf(silu_f(y) * up, 0.f);
    }
    STAMP(18);
    LDS_BARRIER();
    STAMP(10);
    {
      const u32x4 af = *LDSP(u32x4, asrc);
      const f32x4 z = {0.f, 0.f, 0.f, 0.f};
      const f32x4 d0 = mfma16(af, b2[0], z), d1 = mfma16(af, b2[1], z), d2 = mfma16(af, b2[2], z), d3 = mfma16(af, b2[3], z);
      const bool g1 = g & 1, g2 = g & 2;
      const float e0 = g1 ? d1[0] : d0[0], e1 = g1 ? d1[1] : d0[1], f0 = g1 ? d3[0] : d2[0], f1 = g1 ? d3[1] : d2[1];
      st8_sc1(wsr, ooff, pack_bf(g2 ? f0 : e0, g2 ? f1 : e1), tag);
    }
    asm volatile(".Lend_b:");
    STAMP(6);
    FINISH();
  }
  if (wave == 0) {
    const int d = fd, ul = lane >> 3;
    u32 gm[2] = {sg[0][0], sg[1][0]};
    if (!all_ok((sg[0][1] == tag) & (sg[1][1] == tag))) gather8<2>(wsr, G_MOD + (1024 + d) * 8, 1536 * 8, tag, gm);
    u32 xa0, xa1;
    {
      due = t1 + P.N2 * 256; gate();
      u32x4 gn[2] = {ld16<AUX_SC1>(wsr, G_NT + lane * 16, 0), ld16<AUX_SC1>(wsr, G_NT + lane * 16, 1024)};
      const int tn = notices(IC<2>{}, G_NT + lane * 16, gn) * 256;
      due = (tn > t1 + 2 * 256 ? max(tn, t1 + P.L0 * 256) + P.L1 * 256 : t1) + P.X3 * 256;
      until(due);
    }
#pragma nounroll
    for (int spin = 0; spin < SPIN_MAX; ++spin) {
      CFENCE();
      xa0 = __builtin_amdgcn_raw_buffer_load_b32(wsr, G_XP + ul * 2048 + (d >> 1) * 4, 0, AUX_SC1);
      xa1 = __builtin_amdgcn_raw_buffer_load_b32(wsr, G_XP + ul * 2048 + (d >> 1) * 4, 1024, AUX_SC1);
      if (all_ok(max(xa0, xa1) != SENT)) break;
      __builtin_amdgcn_s_sleep(1);
    }
    float a0 = (d & 1) ? hi_f(xa0) : lo_f(xa0), a1 = (d & 1) ? hi_f(xa1) : lo_f(xa1);
    a0 += dpp<XOR8>(a0); a1 += dpp<XOR8>(a1);
    a0 = swap16_add(a0, a0); a1 = swap16_add(a1, a1);
    a0 = swap32_add(a0, a0); a1 = swap32_add(a1, a1);
    const bool hb = g & 1;
    float xv = rbf(((d & 1) ? hi_f(sx2) : lo_f(sx2)) + rbf((hb ? hi_f(gm[0]) : lo_f(gm[0])) * rbf(hb ? a1 : a0)));
    float gv = hb ? hi_f(gm[1]) : lo_f(gm[1]);
    PIN(xv); PIN(gv);
    if (wi == 0 && lane == 0) __builtin_amdgcn_raw_buffer_store_b32(tag, wsr, WS_EPOCH, 0, AUX_SC1);
    STAMP(10);
    due = max(due + P.X4 * 256, (int)(((u32)wall_clock64() - t0) << 8) + P.GX * 256);
    const bool last = ul + 40 < NUP;
    u32 o0 = G_OP + (ul * 512 + d) * 8, o5 = last ? o0 : OOB, tg5 = last ? tag : 0u;
    const rsrc_t rout = mk_rsrc(out, 2048u);
    u32 oo = (lane & 40) == 0 ? (g * 512 + d) * 2 : OOB;
    const u32 pk = c == 0 ? 0x00003f80u : (c == 4 ? 0x3f800000u : 0u);
    u32x4 one = {pk, 0u, pk, 0u};
    PIN(o0); PIN(o5); PIN(tg5); PIN(oo); PIN(one);
    __builtin_amdgcn_raw_buffer_store_b16((u16)pack_bf(xv, 0.f), rout, oo, 0, AUX_SC1);
    until(due);
    f32x4 sv;
#pragma nounroll
    for (int spin = 0; spin < SPIN_MAX; ++spin) {
      CFENCE();
      u32x2 og[6];
#pragma unroll
      for (int k = 0; k < 5; ++k) og[k] = ld8<AUX_SC1>(wsr, o0, k * 32768);
      og[5] = ld8<AUX_SC1>(wsr, o5, 5 * 32768);
      sv = f32x4{0.f, 0.f, 0.f, 0.f};
#pragma unroll
      for (int k = 0; k < 3; ++k) sv = mfma16(one, u32x4{og[2 * k][0], og[2 * k][1], og[2 * k + 1][0], og[2 * k + 1][1]}, sv);
      const u32 bad = ((og[0][1] ^ tag) | (og[1][1] ^ tag) | (og[2][1] ^ tag)) | ((og[3][1] ^ tag) | (og[4][1] ^ tag) | (og[5][1] ^ tg5));
      PIN(sv);
      if (all_ok(bad == 0u)) break;
      __builtin_amdgcn_s_sleep(1);
    }
    STAMP(7);
    const float s = sv[0] + dpp<XOR8>(sv[0]);
    __builtin_amdgcn_raw_buffer_store_b16((u16)pack_bf(fmaf(gv, s, xv), 0.f), rout, oo, 0, AUX_SC1);
    asm volatile(".Lend_c:");
  }
  FINISH();
}

extern "C" void kernel_launch(void* const* d_in, const int* in_sizes, int n_in,
                              void* d_out, int out_size, void* d_ws, size_t ws_size,
                              hipStream_t stream) {
  fused_kernel<<<256, 512, 0, stream>>>((const u16*)d_in[0], (const u16*)d_in[1], (const u16*)d_in[2], (const u16*)d_in[3], (const u16*)d_in[4],
                                        (const u16*)d_in[6], (const u16*)d_in[7], (const u16*)d_in[8], (const u16*)d_in[9], (const u16*)d_in[10],
                                        (unsigned char*)d_ws, (u16*)d_out
    );
}
```

```cpp
#define PROBE_MUL 10
#include <hip/hip_runtime.h>
#include <stdint.h>

typedef unsigned int u32;
typedef unsigned short u16;
typedef unsigned long long u64;
typedef __attribute__((ext_vector_type(4))) unsigned u32x4;
typedef __attribute__((ext_vector_type(2))) unsigned u32x2;
typedef __attribute__((ext_vector_type(2))) __bf16 bf16x2;
typedef __attribute__((ext_vector_type(4))) float f32x4;
typedef __attribute__((ext_vector_type(2))) float f32x2;
typedef __amdgpu_buffer_rsrc_t rsrc_t;

constexpr int NH = 32, SK = 16384, DH = 128, WIN = 4096, FF = 1376;
struct Prm { int E0, ES, E1, E2, E2C, EU, TEND, DT_UP, L0, L1, X1, X2, X3, X4, NE, FE, M0, M1, M2, M3, M4, GS, GX, N1, N2, GA, WD, DT_WO, LW, LU, EW, PM, NF; };
constexpr Prm PRM0 = {32, 24, 32, 32, 56, 64, 2030, 60, 30, 25, 92, 86, 131, 88, 4, 12, 5, 8, 21, 4, 32, 30, 56, 28, 70, 12, 4, 100, 45, 15, 8, 0x24310, 2};
#define PRM_ARG
constexpr int NL = 37;
constexpr int NCMIN = 192;
constexpr int WK = 8, WV = 5;
constexpr int WUSE = 12, LNS = 7;
constexpr int QP = 14, QD = 4;
constexpr int VQ = 6;
constexpr int NSL = (NL + 7) / 8;
constexpr int VS = VQ + NSL;
constexpr int MLS = VS + 2;
constexpr int VSIDE = MLS - 5, UPREP = MLS + 8;
constexpr int SIDE = NCMIN / 8 - 4;
constexpr int CPF = 18;
constexpr int SMO = 1;
constexpr int TBP = 4, TBL = 11, TBU = 15;
constexpr int cons_end(int v) {
  const int a = v - WV + 1, c = (v - MLS) * 4;
  const int m = a < c ? a : c;
  return m < 0 ? 0 : (m > NL ? NL : m);
}
struct Tab { u32 e[512]; };
constexpr Tab make_tab(const Prm& P) {
  Tab T{};
  for (int bid = 0; bid < 256; ++bid) {
    const int xcd = bid & 7, part = bid >> 4 & 3, i8 = (bid >> 3 & 1) | bid >> 6 << 1, wi = i8 * 8 + xcd;
    const bool has_mod = wi < 48, has_up = wi >= 21;
    const int cb0 = (i8 + 5) & 7, cb1 = (i8 + 4) & 7, ms = wi - 20;
    const int mi = wi < 8 ? wi : (wi < 16 ? 8 + ((xcd + 4) & 7) : (wi < 20 ? (wi - 14) * 8 + 3 : 16 + ms / 7 * 8 + ms % 7 + (ms % 7 >= 3 ? 1 : 0)));
    const int ui = xcd * 5 + (xcd > 5 ? xcd - 5 : 0) + (i8 >= 3 ? (xcd >= 5 ? 1 : 0) + i8 - 3 : 0);
    const int wd = cb0 == 3 ? 0 : P.WD;
    const int e[4] = {P.E0 - 4 * wd + (cb0 == 3 ? P.ES : 0), P.E1 + (cb1 == 3 ? P.ES : 0), has_mod ? (wi < 16 ? P.E2C : P.E2) + ((mi & 7) == 3 ? P.ES : 0) : 0, has_up ? P.EU : 0};
    const int ew = wd ? P.EW : 0, pa = (1024 + e[0] + ew + e[1] + e[2] + e[3]) / 4;
    int c[5] = {0, 0, 0, 0, 124};
    for (int k = 0; k < 3; ++k) c[k + 1] = c[k] + (pa - e[k] - (k == 0 ? ew : 0) + 4) / 8 - 1;
    const int nl = c[part + 1] - c[part] + 1, wst = e[part] / 4;
    const bool has_w = part < 2 || (part == 2 ? has_mod : has_up);
    const int blk = !has_w ? 0 : (part == 0 ? cb0 : (part == 1 ? xcd * 8 + cb1 : (part == 2 ? mi : ui)));
    const int tau = (part == 3 && has_up ? P.TEND - P.DT_UP : (part == 0 && wd ? P.TEND - P.DT_WO : P.TEND)) * 256 / (2 * nl + wst);
    T.e[2 * bid] = (u32)(c[part] | nl << 7 | blk << 13 | (has_w ? 1 : 0) << 19 | (part == 0 ? wd : 0) << 20);
    T.e[2 * bid + 1] = (u32)(tau | (tau * wst / (part == 0 ? 8 - wd : 8)) << 16);
  }
  return T;
}
constexpr int step_range(const Prm& P, bool mx_) {
  int mx = 0, mn = 1 << 20;
  const Tab T = make_tab(P);
  for (int bid = 0; bid < 256; ++bid) { const int n = T.e[2 * bid] >> 7 & 63; mx = n > mx ? n : mx; mn = n < mn ? n : mn; }
  return mx_ ? mx : mn;
}
extern "C" __attribute__((used, visibility("protected"))) __constant__ const Tab wg_tab = make_tab(PRM0);
static_assert(step_range(PRM0, true) <= NL && step_range(PRM0, false) >= NCMIN / 8, "a wave's key range does not fit its load steps");
static_assert(cons_end(SIDE + WV + 1) <= NCMIN / 8 && cons_end(UPREP) <= NCMIN / 8 && SIDE > MLS + 3, "steps every wave runs");
constexpr int NUP = FF / 32;
constexpr int SPIN_MAX = 1 << 17;

constexpr u32 WS_EPOCH = 0;
constexpr u32 G_TB = 1024;
constexpr u32 G_NT = 2048;
constexpr u32 G_MOD = 4096;
constexpr u32 G_Q = G_MOD + 3072 * 8;
constexpr u32 G_ML = G_Q + 4096 * 8;
constexpr u32 G_P = G_ML + 256 * 16;
constexpr u32 G_XP = G_P + 256 * 256;
constexpr u32 G_OP = G_XP + 8 * 512 * 4;
constexpr u32 WS_END = G_OP + 43 * 512 * 8;
constexpr u32 SENT = 0xffffffffu;
constexpr u32 OOB = 0x40000000u;
#define WST 0

constexpr int AUX_NT = 2, AUX_SC1 = 16;

__device__ __forceinline__ float lo_f(u32 v) { return __uint_as_float(v << 16); }
__device__ __forceinline__ float hi_f(u32 v) { return __uint_as_float(v & 0xffff0000u); }
__device__ __forceinline__ float rbf(float x) { return (float)(__bf16)x; }
__device__ __forceinline__ u32 pack_bf(float lo, float hi) {
  bf16x2 r; r[0] = (__bf16)lo; r[1] = (__bf16)hi; return __builtin_bit_cast(u32, r);
}
__device__ __forceinline__ float dot2(u32 k, u32 q, float acc) {
  return __builtin_amdgcn_fdot2_f32_bf16(__builtin_bit_cast(bf16x2, k), __builtin_bit_cast(bf16x2, q), acc, false);
}
template <int CTRL> __device__ __forceinline__ float dpp(float x) {
  return __builtin_bit_cast(float, __builtin_amdgcn_mov_dpp(__builtin_bit_cast(int, x), CTRL, 0xf, 0xf, true));
}
constexpr int XOR1 = 0xB1, XOR2 = 0x4E, XOR7 = 0x141, XOR8 = 0x128;
__device__ __forceinline__ float swap32_add(float a, float b) {
  auto t = __builtin_amdgcn_permlane32_swap(__float_as_uint(a), __float_as_uint(b), false, false);
  return __uint_as_float(t[0]) + __uint_as_float(t[1]);
}
__device__ __forceinline__ float swap16_add(float a, float b) {
  auto t = __builtin_amdgcn_permlane16_swap(__float_as_uint(a), __float_as_uint(b), false, false);
  return __uint_as_float(t[0]) + __uint_as_float(t[1]);
}
__device__ __forceinline__ float wave_sum(float x) {
  x += dpp<XOR1>(x); x += dpp<XOR2>(x); x += dpp<XOR7>(x); x += dpp<XOR8>(x);
  x = swap16_add(x, x);
  return swap32_add(x, x);
}
__device__ __forceinline__ float wave_max(float x) {
  x = fmaxf(x, dpp<XOR1>(x)); x = fmaxf(x, dpp<XOR2>(x)); x = fmaxf(x, dpp<XOR7>(x)); x = fmaxf(x, dpp<XOR8>(x));
  auto s = __builtin_amdgcn_permlane16_swap(__float_as_uint(x), __float_as_uint(x), false, false);
  x = fmaxf(__uint_as_float(s[0]), __uint_as_float(s[1]));
  auto t = __builtin_amdgcn_permlane32_swap(__float_as_uint(x), __float_as_uint(x), false, false);
  return fmaxf(__uint_as_float(t[0]), __uint_as_float(t[1]));
}
template <int CTRL> __device__ __forceinline__ float sel_add(float lo, float hi, bool up) {
  const float t1 = lo + dpp<CTRL>(lo), t2 = hi + dpp<CTRL>(hi);
  return up ? t2 : t1;
}
template <int I> __device__ __forceinline__ float bc16(float x) {
  return __builtin_bit_cast(float, __builtin_amdgcn_update_dpp(0, __builtin_bit_cast(int, x), 0x150 + I, 0xf, 0xf, false));
}
__device__ __forceinline__ float bcast16(float x, int i) {
  switch (i) {
    case 0: return bc16<0>(x); case 1: return bc16<1>(x); case 2: return bc16<2>(x); case 3: return bc16<3>(x);
    case 4: return bc16<4>(x); case 5: return bc16<5>(x); case 6: return bc16<6>(x); case 7: return bc16<7>(x);
    case 8: return bc16<8>(x); case 9: return bc16<9>(x); case 10: return bc16<10>(x); case 11: return bc16<11>(x);
    case 12: return bc16<12>(x); case 13: return bc16<13>(x); case 14: return bc16<14>(x); default: return bc16<15>(x);
  }
}
__device__ __forceinline__ float silu_f(float x) { return x * __builtin_amdgcn_rcpf(1.f + __expf(-x)); }

template <int AUX> __device__ __forceinline__ u32x4 ld16(rsrc_t r, u32 voff, u32 soff) {
  return __builtin_amdgcn_raw_buffer_load_b128(r, voff, soff, AUX);
}
template <int AUX> __device__ __forceinline__ u32x2 ld8(rsrc_t r, u32 voff, u32 soff) {
  return __builtin_amdgcn_raw_buffer_load_b64(r, voff, soff, AUX);
}
__device__ __forceinline__ void st8_sc1(rsrc_t r, u32 voff, u32 v, u32 tag) {
  __builtin_amdgcn_raw_buffer_store_b64(u32x2{v, tag}, r, voff, 0, AUX_SC1);
}
__device__ __forceinline__ void st16_sc1(rsrc_t r, u32 voff, u32 v0, u32 v1, u32 tag) {
  __builtin_amdgcn_raw_buffer_store_b128(u32x4{v0, tag, v1, tag}, r, voff, 0, AUX_SC1);
}
__device__ __forceinline__ rsrc_t mk_rsrc(const void* p, u32 bytes) {
  return __builtin_amdgcn_make_buffer_rsrc(const_cast<void*>(p), 0, bytes, 0x00020000);
}
#define WAIT_VM(n) __builtin_amdgcn_s_waitcnt(((n) & 15) | (((n) >> 4) << 14) | (7 << 4) | (15 << 8))
#define SB() __builtin_amdgcn_sched_barrier(0)
#define CFENCE() asm volatile("" ::: "memory")
__device__ __forceinline__ bool all_ok(bool ok) { return __builtin_amdgcn_ballot_w64(!ok) == 0ull; }

template <int N> __device__ __forceinline__ void gather16(rsrc_t ws, u32 voff, u32 stride, u32 tag, u32x4 (&g)[N]) {
#pragma nounroll
  for (int spin = 0; spin < SPIN_MAX; ++spin) {
    CFENCE();
    bool ok = true;
#pragma unroll
    for (int i = 0; i < N; ++i) g[i] = ld16<AUX_SC1>(ws, voff + i * stride, 0);
#pragma unroll
    for (int i = 0; i < N; ++i) ok &= (g[i][1] == tag) & (g[i][3] == tag);
    if (all_ok(ok)) break;
    __builtin_amdgcn_s_sleep(8);
  }
}
template <int N> __device__ __forceinline__ void gather8(rsrc_t ws, u32 voff, u32 stride, u32 tag, u32 (&g)[N], bool act = true) {
#pragma nounroll
  for (int spin = 0; spin < SPIN_MAX; ++spin) {
    CFENCE();
    bool ok = true;
    u32x2 t[N];
#pragma unroll
    for (int i = 0; i < N; ++i) t[i] = ld8<AUX_SC1>(ws, voff + i * stride, 0);
#pragma unroll
    for (int i = 0; i < N; ++i) { ok &= (t[i][1] == tag); g[i] = t[i][0]; }
    if (all_ok(ok | !act)) break;
    __builtin_amdgcn_s_sleep(8);
  }
}

struct Smem {
  float red[8][128];
  float ml[8][2];
  float r2[64 * 20];
  u16 hb[3 * 1024];
  float stat[8][64];
  float red2[8][128];
  u32 dummy[64];
  u32x2 bc;
  u32x4 zero[4];
  u32 mflag[8];
  u32x4 wbuf[8][8][64];
  __attribute__((aligned(256))) u16 w2b[32][512];
  u16 actl[2][32];
};

#define PROBE_K 102
#define PROBE_OP 2
#define PROBE_PART -1
#define STAMP(k) do { if ((k) == PROBE_K && tid == 0) pst = wall_clock64(); } while (0)
#define WSTAMP(k) do {} while (0)
#define FINISH() do { STAMP(8); if (tid == 0) { __hip_atomic_store(reinterpret_cast<u64*>(ws + (1u << 20)) + bid * 2, pt0, __ATOMIC_RELAXED, __HIP_MEMORY_SCOPE_AGENT); \
   __hip_atomic_store(reinterpret_cast<u64*>(ws + (1u << 20)) + bid * 2 + 1, (PROBE_PART < 0 || part == PROBE_PART) ? pst : 0ull, __ATOMIC_RELAXED, __HIP_MEMORY_SCOPE_AGENT); } \
   if (bid == 8 && wave == 0) probe_end(ws, pt0, lane); return; } while (0)
#define PIN(x) asm volatile("" : "+v"(x))
#define LDS_BARRIER() do { __builtin_amdgcn_s_waitcnt(0xc07f); __builtin_amdgcn_s_barrier(); CFENCE(); } while (0)
#define WAVE_SYNC() do { __builtin_amdgcn_fence(__ATOMIC_RELEASE, "wavefront"); __builtin_amdgcn_wave_barrier(); __builtin_amdgcn_fence(__ATOMIC_ACQUIRE, "wavefront"); } while (0)

__device__ __forceinline__ void dma16(rsrc_t r, u32 voff, u32 soff, u32 lds_dst) {
  u32 keep;
  asm volatile("s_mov_b32 %0, m0\n\ts_mov_b32 m0, %4\n\ts_nop 4\n\tbuffer_load_dwordx4 %1, %2, %3 offen nt lds\n\ts_mov_b32 m0, %0"
               : "=&s"(keep) : "v"(voff), "s"(r), "s"(soff), "s"(lds_dst) : "memory");
}
__device__ __forceinline__ u32 perm_lo(u32 a, u32 b) { return __builtin_amdgcn_perm(b, a, 0x05040100u); }
__device__ __forceinline__ u32 perm_hi(u32 a, u32 b) { return __builtin_amdgcn_perm(b, a, 0x07060302u); }
__device__ __forceinline__ float rdl(float x, int l) { return __int_as_float(__builtin_amdgcn_readlane(__float_as_int(x), l)); }

typedef __attribute__((ext_vector_type(8))) __bf16 bf16x8;
__device__ __forceinline__ f32x4 mfma16(u32x4 a, u32x4 b, f32x4 c) {
  return __builtin_amdgcn_mfma_f32_16x16x32_bf16(__builtin_bit_cast(bf16x8, a), __builtin_bit_cast(bf16x8, b), c, 0, 0, 0);
}
__device__ __forceinline__ float sum8(const float* p) {
  const f32x4 a = *reinterpret_cast<const f32x4*>(p), b = *reinterpret_cast<const f32x4*>(p + 4);
  return ((a[0] + b[0]) + (a[1] + b[1])) + ((a[2] + b[2]) + (a[3] + b[3]));
}
__device__ __forceinline__ float ln_ri(float var) {
  return __builtin_amdgcn_rcpf(rbf(__builtin_amdgcn_sqrtf(rbf(rbf(fmaxf(var, 0.f)) + 1e-5f))));
}
__device__ __forceinline__ void ln_stats2(const float (&x0)[8], const float (&x1)[8], float& mb0, float& ri0, float& mb1, float& ri1) {
  const float k0 = rdl(x0[0], 0), k1 = rdl(x1[0], 0);
  float s0 = 0.f, s1 = 0.f, q0 = 0.f, q1 = 0.f;
#pragma unroll
  for (int k = 0; k < 8; ++k) {
    const float d0 = x0[k] - k0, d1 = x1[k] - k1;
    s0 += d0; s1 += d1; q0 = fmaf(d0, d0, q0); q1 = fmaf(d1, d1, q1);
  }
  float t = swap16_add(swap32_add(s0, s1), swap32_add(q0, q1));
  t += dpp<XOR1>(t); t += dpp<XOR2>(t); t += dpp<XOR7>(t); t += dpp<XOR8>(t);
  auto u = __builtin_amdgcn_permlane16_swap(__float_as_uint(t), __float_as_uint(t), false, false);
  const float ms = __uint_as_float(u[0]) * (1.f / 512.f), mq = __uint_as_float(u[1]) * (1.f / 512.f);
  const float ri = ln_ri(fmaf(-ms, ms, mq));
  mb0 = rbf(k0 + rdl(ms, 0)); mb1 = rbf(k1 + rdl(ms, 32));
  ri0 = rdl(ri, 0); ri1 = rdl(ri, 32);
}
template <int I> struct IC { static constexpr int v = I; };
template <int I, int N, class F> __device__ __forceinline__ void sfor(F&& f) {
  if constexpr (I < N) { if (f(IC<I>{})) sfor<I + 1, N>(f); }
}
typedef __attribute__((ext_vector_type(4))) short s16x4;
typedef __attribute__((address_space(3))) s16x4* lds_s16x4p;
__device__ __forceinline__ u32x2 tr_read(u32 lds_addr) {
  return __builtin_bit_cast(u32x2, __builtin_amdgcn_ds_read_tr16_b64_v4i16((lds_s16x4p)lds_addr));
}
__device__ __forceinline__ u32 next_tag(u32 e) { return ((e | 0x40004000u) + 1u) & ~0x40004000u; }
__device__ __forceinline__ float u2f(u32 v) { return __uint_as_float(v); }
#define LDSP(T, a) ((__attribute__((address_space(3))) T*)(a))
template <class T> __device__ __forceinline__ u32 lds_addr(T* p) { u32 a = (u32)(uintptr_t)p; PIN(a); return a; }

#define TAB_LOAD "s_add_u32 s30, s28, wg_tab@rel32@lo+8\ns_addc_u32 s31, s29, wg_tab@rel32@hi+16\ns_load_dwordx2 %[te], s[30:31], %[sa] offset:0x0\n"
__device__ __forceinline__ void probe_end(unsigned char* ws, u64 pt0, int lane) {
  while (wall_clock64() < pt0 + 5500ull) __builtin_amdgcn_s_sleep(8);
  u64 a[4], c2[4];
  for (int i = 0; i < 4; ++i) {
    a[i] = __hip_atomic_load(reinterpret_cast<u64*>(ws + (1u << 20)) + (i * 64 + lane) * 2, __ATOMIC_RELAXED, __HIP_MEMORY_SCOPE_AGENT);
    c2[i] = __hip_atomic_load(reinterpret_cast<u64*>(ws + (1u << 20)) + (i * 64 + lane) * 2 + 1, __ATOMIC_RELAXED, __HIP_MEMORY_SCOPE_AGENT);
  }
  int mn0 = 1 << 30, mx1 = -(1 << 30), mn1 = 1 << 30, sum = 0, cnt = 0;
  for (int i = 0; i < 4; ++i) {
    const int d0 = (int)(a[i] - pt0);
    mn0 = min(mn0, d0);
    if (c2[i] != 0ull) { const int d1 = (int)(c2[i] - pt0); mx1 = max(mx1, d1); mn1 = min(mn1, d1); sum += d1; cnt += 1; }
  }
  for (int o = 1; o < 64; o <<= 1) { mn0 = min(mn0, __shfl_xor(mn0, o)); mx1 = max(mx1, __shfl_xor(mx1, o)); mn1 = min(mn1, __shfl_xor(mn1, o)); sum += __shfl_xor(sum, o); cnt += __shfl_xor(cnt, o); }
  int X = PROBE_OP == 0 ? mx1 : (PROBE_OP == 1 ? sum / max(cnt, 1) : mn1);
  if (PROBE_K == 102) X = mn0;
  X = mn0 + (X - mn0) * PROBE_MUL;
  const u64 tg = pt0 + (u64)(long long)(X + 8000);
  for (int i = 0; i < 400000 && wall_clock64() < tg; ++i) __builtin_amdgcn_s_sleep(1);
}
__global__ void __launch_bounds__(512, 2) fused_kernel(const u16* kc_, const u16* vc_, const u16* wo_, const u16* wq_, const u16* wmod_, const u16* w1_, const u16* w3_,
                                                       unsigned char* ws_, const u16* x_, const u16* cnd_, const u16* w2_, u16* out_ PRM_ARG) {
  __shared__ __attribute__((aligned(256))) Smem sm;
  const u64 a_ka = (u64)__builtin_amdgcn_kernarg_segment_ptr();
  const int bid = blockIdx.x, tid = threadIdx.x;
  u64 wa, t64, te; int tid0;
  u32 ar[14];
  u32 tch2, sa_, sb_, va_;
  u32x4 kr[NL], vr[NL];
  asm volatile(
    "s_load_dwordx16 s[4:19], %[ka], 0x0\ns_load_dwordx8 s[20:27], %[ka], 0x40\n"
    "s_getpc_b64 s[28:29]\n.Lcpf_first:\n"
    "s_lshl_b32 %[sa], %[bid], 3\n"
    TAB_LOAD
    "s_memrealtime %[t]\n"
    "s_cmp_gt_u32 %[bid], 15\ns_cbranch_scc1 .Lno_cpf\n"
    "s_mov_b32 %[sa], .Lend_a-.Lcpf_first\ns_max_i32 %[sa], %[sa], .Lend_b-.Lcpf_first\ns_max_i32 %[sa], %[sa], .Lend_c-.Lcpf_first\ns_max_i32 %[sa], %[sa], .Lend_d-.Lcpf_first\ns_max_i32 %[sa], %[sa], .Lend_e-.Lcpf_first\n"
    "v_lshlrev_b32 %[va], 6, %[tid]\nv_min_u32 %[va], %[sa], %[va]\nglobal_load_dword %[pf], %[va], s[28:29]\n"
    ".Lno_cpf:\n"
    "s_and_b32 %[sa], %[bid], 15\ns_lshr_b32 %[sb], %[bid], 6\ns_lshl2_add_u32 %[sa], %[sa], %[sb]\ns_lshl_b32 %[sa], %[sa], 22\ns_xor_b32 %[sa], %[sa], 0xff0000\n"
    "s_and_b32 %[sb], %[bid], 48\nv_lshl_add_u32 %[va], %[sb], 5, %[tid]\nv_lshl_add_u32 %[va], %[va], 4, %[sa]\n"
    "v_readfirstlane_b32 %[t0], %[tid]\n"
    "s_waitcnt lgkmcnt(0)\nglobal_load_dwordx4 %[k0], %[va], s[4:5] nt\nglobal_load_dwordx4 %[v0], %[va], s[6:7] nt\n"
    "s_bitcmp1_b32 %[t0], 6\ns_cselect_b64 s[14:15], s[16:17], s[14:15]\n"
    "s_bitcmp1_b32 %[bid], 4\ns_cselect_b64 s[8:9], s[10:11], s[8:9]\ns_cselect_b64 s[12:13], s[14:15], s[12:13]\ns_bitcmp1_b32 %[bid], 5\ns_cselect_b64 %[wa], s[12:13], s[8:9]"
    : "=&{s4}"(ar[0]), "=&{s5}"(ar[1]), "=&{s6}"(ar[2]), "=&{s7}"(ar[3]), "=&{s18}"(ar[4]), "=&{s19}"(ar[5]), "=&{s20}"(ar[6]), "=&{s21}"(ar[7]), "=&{s22}"(ar[8]), "=&{s23}"(ar[9]),
      "=&{s24}"(ar[10]), "=&{s25}"(ar[11]), "=&{s26}"(ar[12]), "=&{s27}"(ar[13]), [wa] "=&s"(wa), [t0] "=&s"(tid0), [t] "=&s"(t64), [te] "=&s"(te), [sa] "=&s"(sa_), [sb] "=&s"(sb_),
      [va] "=&v"(va_), [pf] "=&v"(tch2), [k0] "=&v"(kr[0]), [v0] "=&v"(vr[0])
    : [ka] "s"(a_ka), [bid] "s"(bid), [tid] "v"(tid) : "s8", "s9", "s10", "s11", "s12", "s13", "s14", "s15", "s16", "s17", "s28", "s29", "s30", "s31", "scc", "memory");
  asm volatile("" : "+s"(ar[0]), "+s"(ar[1]), "+s"(ar[2]), "+s"(ar[3]), "+s"(ar[4]), "+s"(ar[5]), "+s"(ar[6]), "+s"(ar[7]), "+s"(ar[8]), "+s"(ar[9]), "+s"(ar[10]), "+s"(ar[11]), "+s"(ar[12]), "+s"(ar[13]));
  asm volatile("" : "+s"(wa), "+s"(te), "+s"(t64), "+s"(tid0));
  const u32 tka = (u32)wall_clock64();
  const u64 pt0 = t64; u64 pst = (PROBE_K == 100 || PROBE_K == 102) ? pt0 : (PROBE_K == 101 ? pt0 + (u32)(tka - (u32)t64) : 0);
  const u32 t0 = (u32)t64, te0 = (u32)te, te1 = (u32)(te >> 32);
  __builtin_assume(tid >= 0 && tid < 512 && tid0 >= 0 && tid0 < 512 && bid >= 0 && bid < 256);
  constexpr Prm P = PRM0;
  const int TEND = P.TEND;
  const int xcd = bid & 7, part = bid >> 4 & 3;
  const int lane = tid & 63, wave = tid0 >> 6;
  SB();
  const bool p3 = part == 3;
  u32 pitch = (u32)(0x005600c001000020ull >> (part * 16) & 0xffff) * 32u;
  const int sh = p3 ? 2 : 3;
  u32 wv0 = (u32)((p3 ? (wave >> 1) * 128 : wave * 64) + (part == 0 ? xcd * 512 : 0) + (lane >> sh)) * pitch + ((lane & (p3 ? 3 : 7)) ^ (lane >> (p3 ? 5 : 3))) * 16u;
  const u32 wimg = (u32)(uintptr_t)(&sm.wbuf[0][0][0]);
  u32 wsz = (part == 0 ? 4096u : 512u) * pitch, wld = wimg + (p3 ? (wave & 1) * 32768 + (wave >> 1) * 8192 : wave * 8192);
  PIN(wv0);
  asm volatile("" : "+s"(wsz), "+s"(wld), "+s"(pitch));
  auto karg = [&](int i) { return ar[2 * i] | (u64)ar[2 * i + 1] << 32; };
  const u64 a_kc = karg(0), a_vc = karg(1);
  unsigned char* ws = (unsigned char*)karg(2);
  const u16 *x = (const u16*)karg(3), *cnd = (const u16*)karg(4), *w2 = (const u16*)karg(5);
  u16* out = (u16*)karg(6);
  const rsrc_t wsr = mk_rsrc(ws, WS_END);
  const int bl0 = te0 & 127, nl = te0 >> 7 & 63, blk = te0 >> 13 & 63;
  const bool has_w = te0 >> 19 & 1;
  const rsrc_t rw = mk_rsrc((const void*)wa, wsz);
  const u32 wstep = pitch << (6 - sh), wflip = p3 ? 32u : 0u;
  u32 wso = blk << (sh + 4);
  int wleft = 8;
  auto piece = [&]() {
    dma16(rw, wv0, wso, wld);
    wso += wstep; wld += 1024;
    wv0 ^= (wleft & 1) ? wflip : 0u;
    --wleft;
  };
  SB();
  STAMP(23);
  const int b = bid >> 3 & 1, head = xcd * 4 + (bid >> 6 ^ 3);
  const int g = lane >> 4, c = lane & 15;
  const int i8 = b | bid >> 6 << 1, wi = i8 * 8 + xcd;
  const bool is_up = p3 && has_w, modc = part == 2 && wi < 16;
  const size_t base = ((size_t)(b * NH + head) * SK + (SK - WIN)) * DH;
  const rsrc_t rk = mk_rsrc((const u16*)a_kc + base, WIN * DH * 2), rv = mk_rsrc((const u16*)a_vc + base, WIN * DH * 2);
  int tau = te1 & 0xffff;
  const int tauw = te1 >> 16;
  int due = P.NF * tau, now = 0;
  auto gate = [&]() {
    while (now < due) { now = (int)(((u32)wall_clock64() - t0) << 8); if (now < due) __builtin_amdgcn_s_sleep(1); }
    due += tau;
  };
  auto until = [&](int t) { while ((int)(((u32)wall_clock64() - t0) << 8) < t); };
  const u32 pslot = G_P + ((b * 32 + head) * 4 + part) * 256;
  u32x2 ep; u32 xs0, xs1, tch1; u32x4 xr0, xr1;
  const rsrc_t rx = mk_rsrc(part == 2 ? cnd : x, (part == 1 || (part == 2 && has_w)) ? 2048u : 0u);
  auto small_ops = [&]() {
    ep = ld8<AUX_SC1>(wsr, wave == 7 ? WS_EPOCH : OOB, 0);
    xs0 = __builtin_amdgcn_raw_buffer_load_b16(rx, tid * 2, 0, 0); xs1 = __builtin_amdgcn_raw_buffer_load_b16(rx, tid * 2, 1024, 0);
    xr0 = ld16<0>(rx, part == 1 ? lane * 16 : OOB, 0); xr1 = ld16<0>(rx, part == 1 ? lane * 16 : OOB, 1024);
    tch1 = __builtin_amdgcn_raw_buffer_load_b32(mk_rsrc(w2 + (size_t)blk * 32 * 512, is_up ? 64u : 0u), tid == 384 ? 0 : OOB, 0, AUX_NT);
  };
  if (has_w) {
    if (modc) now = 1 << 30;
#pragma nounroll
    while (wleft > (int)(te0 >> 20)) { gate(); due += tauw - tau; piece(); }
    now = 0;
  }
  SB();
  u32 sot;
  {
    const u32 s1 = bl0 + lane - 1, s0 = s1 + (s1 >= 108 ? 4 : 0);
    u32 sb = s0 & 96u;
#pragma unroll
    for (int k = 0; k < 5; ++k) sb |= (s0 >> k & 1u) << (P.PM >> (4 * k) & 7);
    const u32 src = (sb >> 5 & 3u) | (u32)(b * 32 + head) << 2;
    sb ^= ((__builtin_popcount(src & P.M0) & 1) | (__builtin_popcount(src & P.M1) & 1) << 1 | (__builtin_popcount(src & P.M2) & 1) << 2 | (__builtin_popcount(src & P.M3) & 1) << 3 | (__builtin_popcount(src & P.M4) & 1) << 4) & (s1 >= 96 ? 7u : 31u);
    sot = (sb & 7u) << 10 | (u32)wave << 13 | (sb >> 3) << 16;
    sot = s1 >= 120 ? (s1 + 4) << 13 | (u32)wave << 10 : sot;
    sot = lane < nl ? sot : OOB;
    PIN(sot);
  }
  u32 tag = 0;
  u32x4 bf[8];
  auto wo_prep = [&](int s0 = 0, int s1 = 2) {
    const u32 wi_ = wimg + wave * 8192;
    const int q = (lane >> 2) & 3, pp = lane & 3;
#pragma unroll
    for (int s = s0; s < s1; ++s) {
#pragma unroll
      for (int t = 0; t < 4; ++t) {
        const int r0 = s * 32 + g * 8 + q, r1 = r0 + 4, ch = t * 2 + (pp >> 1);
        const u32x2 lo = tr_read(wi_ + r0 * 128 + ((ch ^ (r0 & 7)) * 16) + (pp & 1) * 8), hi = tr_read(wi_ + r1 * 128 + ((ch ^ (r1 & 7)) * 16) + (pp & 1) * 8);
        bf[s * 4 + t] = u32x4{lo[0], lo[1], hi[0], hi[1]};
      }
    }
  };
  auto gemv_go = [&](u32 hh) {
    sm.hb[tid] = (u16)hh; sm.hb[512 + tid] = (u16)(hh >> 16);
    WAVE_SYNC();
    const u32x4* a = (c & 11) == 0 ? reinterpret_cast<const u32x4*>(&sm.hb[(c >> 2) * 512 + wave * 64 + g * 8]) : &sm.zero[0];
    const u32x4 a0 = a[0], a1 = a[(c & 11) == 0 ? 4 : 0];
    const f32x4 z = {0.f, 0.f, 0.f, 0.f};
    f32x4 d[4];
#pragma unroll
    for (int t = 0; t < 4; ++t) d[t] = mfma16(a0, bf[t], z);
#pragma unroll
    for (int t = 0; t < 4; ++t) d[t] = mfma16(a1, bf[4 + t], d[t]);
    if (lane < 32) {
#pragma unroll
      for (int t = 0; t < 4; ++t) sm.r2[(t * 16 + c) * 20 + g * 8 + wave] = d[t][0];
    }
  };
  auto mod_stage = [&]() {
    wo_prep();
    gemv_go(pack_bf(silu_f(lo_f(xs0)), silu_f(lo_f(xs1))));
    __builtin_amdgcn_s_waitcnt(0xc07f);
    if (lane == 0) *LDSP(volatile u32, (u32)(uintptr_t)&sm.mflag[wave]) = 1u;
    if (wave == 7) {
#pragma nounroll
      for (int spin = 0; spin < SPIN_MAX; ++spin) {
        const u32 f = *LDSP(volatile u32, (u32)(uintptr_t)&sm.mflag[lane & 7]);
        if (all_ok(f != 0u)) break;
        __builtin_amdgcn_s_sleep(1);
      }
      CFENCE();
      st8_sc1(wsr, G_MOD + (blk * 64 + lane) * 8, pack_bf(sum8(&sm.r2[lane * 20]), sum8(&sm.r2[lane * 20 + 8])), tag);
    }
    STAMP(9);
  };
  if (lane < 4) sm.zero[lane] = u32x4{0u, 0u, 0u, 0u};
  if (part == 2 && has_w) {
    if (tid < 8) sm.mflag[tid] = 0u;
    LDS_BARRIER();
    if (__builtin_expect(modc, 0)) {
      small_ops();
      __builtin_amdgcn_raw_buffer_store_b128(u32x4{SENT, SENT, SENT, SENT}, wsr, tid < 320 ? G_P + wi * 5120 + tid * 16 : OOB, 0, AUX_SC1);
      asm volatile("s_waitcnt vmcnt(0)" ::: "memory");
      tag = next_tag(__builtin_amdgcn_readfirstlane(ep[0]));
      mod_stage();
      __builtin_amdgcn_s_barrier();
    }
  }
  SB();
  const int fd = wi * 8 + (lane & 7);
  const int ub = wave >> 2, ud = (wave & 3) * 128 + c * 8 + g * 2;
  u32x4 sg[3]; u32 sx2;
  auto side_loads = [&]() {
    const bool fin = part == 1 && wave == 0;
    sg[0] = ld16<AUX_SC1>(wsr, is_up ? G_MOD + (1024 + ud) * 8 : (fin ? G_MOD + (1024 + fd) * 8 : OOB), 0);
    sg[1] = ld16<AUX_SC1>(wsr, is_up ? G_MOD + (1536 + ud) * 8 : (fin ? G_MOD + (2560 + fd) * 8 : OOB), 0);
    sg[2] = ld16<AUX_SC1>(wsr, is_up ? G_MOD + (2048 + ud) * 8 : OOB, 0);
    sx2 = __builtin_amdgcn_raw_buffer_load_b32(mk_rsrc(x, (is_up || fin) ? 2048u : 0u), is_up ? ub * 1024 + ud * 2 : (g & 1) * 1024 + (fd >> 1) * 4, 0, 0);
  };
  auto side_pin = [&]() { PIN(sg[0]); PIN(sg[1]); PIN(sg[2]); PIN(sx2); };
  const u32 mlo = G_ML + (head * 2 + b) * 64 + (lane & 3) * 16;
  float fmrg = 0.f;
  bool fok = wave != 0;
  auto merge_factor = [&](const u32x4& ml) {
    const float mi = u2f(ml[0]);
    float M = fmaxf(mi, dpp<XOR1>(mi)); M = fmaxf(M, dpp<XOR2>(M));
    const float e = __expf(mi - M);
    float L = e * u2f(ml[2]);
    L += dpp<XOR1>(L); L += dpp<XOR2>(L);
    fmrg = rdl(e * __builtin_amdgcn_rcpf(L), part);
  };
  f32x2 ga = {0.f, 0.f}, xin = {0.f, 0.f}, cc1 = {0.f, 0.f};
  float Af = 0.f, Bf = 0.f;
  const int hpos = ub * 512 + ud;
  auto up_prep = [&]() {
    if (!all_ok((sg[0][1] == tag) & (sg[0][3] == tag) & (sg[1][1] == tag) & (sg[1][3] == tag) & (sg[2][1] == tag) & (sg[2][3] == tag))) gather16<3>(wsr, G_MOD + (1024 + ud) * 8, 512 * 8, tag, sg);
    ga = f32x2{ub ? hi_f(sg[0][0]) : lo_f(sg[0][0]), ub ? hi_f(sg[0][2]) : lo_f(sg[0][2])};
    cc1 = f32x2{rbf(1.f + (ub ? hi_f(sg[2][0]) : lo_f(sg[2][0]))), rbf(1.f + (ub ? hi_f(sg[2][2]) : lo_f(sg[2][2])))};
    xin = f32x2{lo_f(sx2), hi_f(sx2)};
    u32* hp = reinterpret_cast<u32*>(&sm.hb[hpos]);
    hp[512] = pack_bf(cc1[0], cc1[1]);
    hp[1024] = ub ? perm_hi(sg[1][0], sg[1][2]) : perm_lo(sg[1][0], sg[1][2]);
  };
  auto up_frags = [&]() {
    const int pp = lane & 3;
    const u32 a = wimg + (pp & 1) * 32768 + ((pp >> 1) * 32 + g * 8 + ((lane >> 2) & 3)) * 64 + (((wave >> 1) ^ ((g & 1) | (pp & 2))) * 16) + (wave & 1) * 8;
#pragma unroll
    for (int rb = 0; rb < 8; ++rb) {
      const u32x2 lo = tr_read(a + rb * 4096), hi = tr_read(a + rb * 4096 + 256);
      bf[rb] = u32x4{lo[0], lo[1], hi[0], hi[1]};
    }
  };
  const u32x4* ap = reinterpret_cast<const u32x4*>(&sm.hb[((lane >> 3) & 1) * 512 + (lane & 1) * 32 + g * 8]);
  const bool par = lane & 8;
  float l0 = 0.f, l1 = 0.f;
  u32x2 gmd[2], tbg;
  int t1 = 0;
#pragma unroll
  for (int i = NCMIN / 8; i < NL; ++i) kr[i] = u32x4{0u, 0u, 0u, 0u};
  sfor<0, NL>([&](auto ic) {
    constexpr int i = ic.v;
    if (i >= NCMIN / 8 && i >= nl) return false;
    if constexpr (i == 0) return true;
    const u32 so = __builtin_amdgcn_readlane(sot, i);
    gate();
    SB();
    kr[i] = ld16<AUX_NT>(rk, lane * 16, so);
    if constexpr (i == SMO) small_ops();
    if constexpr (i == TBL) tbg = ld8<AUX_SC1>(wsr, wave == 7 ? G_TB + xcd * 128 + (lane & 7) * 8 : OOB, 0);
    if constexpr (i == QP) { gmd[0] = ld8<AUX_SC1>(wsr, part == 1 ? G_MOD + tid * 8 : OOB, 0); gmd[1] = ld8<AUX_SC1>(wsr, part == 1 ? G_MOD + tid * 8 : OOB, 4096); }
    SB();
    SB();
    if (i >= WK) { WAIT_VM(WK); SB(); }
    if (i == 1) STAMP(25);
    if (i == 8) STAMP(12);
    if (i == 20) STAMP(21);
    if (i == 25) STAMP(22);
    if (i == TBP) {
      if (part == 3 && i8 == 0 && wave == 7) { tag = next_tag(__builtin_amdgcn_readfirstlane(ep[0])); if (lane < 8) st8_sc1(wsr, G_TB + lane * 128 + xcd * 8, t0 + (((tka - t0) * P.GA) >> 3) - P.GS, tag); }
      SB();
    }
    if (i == TBU) {
      if (wave == 7) {
        const bool ok = tbg[1] == tag;
        float lf = ok ? (float)min(max((int)(t0 - tbg[0]), -300), 300) : 0.f, nf = ok ? 1.f : 0.f;
        lf += dpp<XOR1>(lf); nf += dpp<XOR1>(nf); lf += dpp<XOR2>(lf); nf += dpp<XOR2>(nf); lf += dpp<XOR7>(lf); nf += dpp<XOR7>(nf);
        if (lane == 0) sm.bc = u32x2{tag, t0 - (u32)(int)(lf * __builtin_amdgcn_rcpf(fmaxf(nf, 1.f)))};
      }
      LDS_BARRIER();
      const u32x2 bc = sm.bc;
      tag = __builtin_amdgcn_readfirstlane(bc[0]);
      t1 = (__builtin_amdgcn_readfirstlane((int)(bc[1] - t0)) + TEND + P.X1) * 256;
      tau -= __builtin_amdgcn_readfirstlane((int)((float)(int)(t0 - bc[1]) * 256.f * __builtin_amdgcn_rcpf((float)(2 * nl - TBU - 3))));
      SB();
    }
    if (i == LNS) {
      tag = next_tag(__builtin_amdgcn_readfirstlane(ep[0]));
      if (part == 1) {
        float x0[8], x1[8], mb0, ri0, mb1, ri1;
#pragma unroll
        for (int k = 0; k < 4; ++k) { x0[2 * k] = lo_f(xr0[k]); x0[2 * k + 1] = hi_f(xr0[k]); x1[2 * k] = lo_f(xr1[k]); x1[2 * k + 1] = hi_f(xr1[k]); }
        ln_stats2(x0, x1, mb0, ri0, mb1, ri1);
        l0 = rbf(rbf(lo_f(xs0) - mb0) * ri0); l1 = rbf(rbf(lo_f(xs1) - mb1) * ri1);
      }
      PIN(l0); PIN(l1);
      SB();
    }
    if (i == WUSE) {
      PIN(tch1); PIN(tch2);
      if ((part == 1 || (part == 2 && has_w && !modc))) {
        WAIT_VM(WK); CFENCE();
        if (part == 2) mod_stage(); else wo_prep();
      }
#pragma unroll
      for (int k = 0; k < 8; ++k) PIN(bf[k]);
      SB();
    }
    if (i == QP + QD) {
      if (part == 1) {
        u32 gm[2] = {gmd[0][0], gmd[1][0]};
        if (!all_ok((gmd[0][1] == tag) & (gmd[1][1] == tag))) { STAMP(11); gather8<2>(wsr, G_MOD + tid * 8, 512 * 8, tag, gm); }
        STAMP(5);
        gemv_go(pack_bf(rbf(l0 * rbf(1.f + lo_f(gm[1]))) + lo_f(gm[0]), rbf(l1 * rbf(1.f + hi_f(gm[1]))) + hi_f(gm[0])));
      }
      SB();
    }
    if (i == QP + QD + 1) {
      if (part == 1) {
        LDS_BARRIER();
        if (wave == 0) {
          const int bb = lane >> 5, jq = lane & 31;
          st8_sc1(wsr, G_Q + (bb * 2048 + blk * 32 + jq) * 8, pack_bf(sum8(&sm.r2[2 * jq * 20 + bb * 8]), sum8(&sm.r2[(2 * jq + 1) * 20 + bb * 8])), tag);
        }
        STAMP(6);
      }
      SB();
    }
    return true;
  });
  PIN(sot);
  const u32 qoff = G_Q + ((b * 32 + head) * 64 + c * 4) * 8;
  u32x4 qg[2], fl2[3];
  u32 qq[4];
  constexpr int NB = (NL + 15) / 16;
  float xlo, sc[NB], pr[NB], m = 0.f;
  f32x4 acc = {0.f, 0.f, 0.f, 0.f};
  u32 pb[NB], pm[4], cpf;
  const u32 adst0 = lds_addr(lane < 32 ? &sm.red[wave][c * 8 + g * 4] : &sm.r2[(lane - 32) * 4]);
  u32 po, rdst, rsrc, xo;
  auto wo_addr = [&]() {
    po = G_P + (((c >> 2 & 1) * 32 + xcd * 4 + (wave >> 1)) * 4 + (c & 3)) * 256 + (wave & 1) * 128 + (c >> 3) * 64 + g * 16;
    rdst = lane < 32 ? (u32)(uintptr_t)&sm.red2[wave][0] + lane * 16 : (u32)(uintptr_t)&sm.r2[0] + (lane - 32) * 16;
    rsrc = (u32)(uintptr_t)&sm.red2[0][((wave & 1) * 16 + (lane & 15)) * 4 + (lane >> 4)];
    xo = (lane & 1) ? OOB : G_XP + xcd * 2048 + (wave & 1) * 1024 + (blk * 64 + lane) * 2;
    PIN(po); PIN(rdst); PIN(rsrc); PIN(xo);
  };
  u32 pso = wave == 0 ? pslot + lane * 4 : OOB;
  PIN(pso);
  const u32 nadd = (te0 >> 20) ? P.LW : (is_up ? P.LU : 0);
  u32 nto = tid == 0 ? G_NT + bid * 8 : OOB;
  PIN(nto);
  auto notices = [&](auto nc_, u32 off, u32x4 (&gn)[nc_.v]) -> int {
    constexpr int N = nc_.v;
#pragma nounroll
    for (int spin = 0; spin < SPIN_MAX; ++spin) {
      bool ok = true;
#pragma unroll
      for (int i = 0; i < N; ++i) ok &= (gn[i][1] == tag) & (gn[i][3] == tag);
      if (all_ok(ok)) break;
      __builtin_amdgcn_s_sleep(10);
      CFENCE();
#pragma unroll
      for (int i = 0; i < N; ++i) gn[i] = ld16<AUX_SC1>(wsr, off, i * 1024);
    }
    int mx = 0;
#pragma unroll
    for (int i = 0; i < N; ++i) mx = max(mx, max((int)(gn[i][0] - t0), (int)(gn[i][2] - t0)));
    return __builtin_amdgcn_readfirstlane((int)wave_max((float)min(mx, 1 << 20)));
  };
  const int side = nl >= SIDE + 13 ? SIDE + 8 : (nl >= SIDE + 9 ? SIDE + 4 : SIDE);
  sfor<0, NL + WV>([&](auto ic) {
    constexpr int v = ic.v;
    if (cons_end(v - 1) >= NCMIN / 8 && cons_end(v - 1) >= nl) return false;
    if constexpr (v > 0 && v < NL) {
      const u32 so = __builtin_amdgcn_readlane(sot, v);
      if constexpr (v >= 12) { if (v == nl - P.NE) tau = (tau * P.FE) >> 4; }
      if constexpr (v >= NCMIN / 8) { if (v < nl) gate(); } else gate();
      SB();
      vr[v] = ld16<AUX_NT>(rv, lane * 16, so);
      SB();
      SB();
    }
    if constexpr (v == NCMIN / 8 - 1) {
#pragma unroll
      for (int i = NCMIN / 8; i < NL; ++i) asm volatile("" : "=v"(vr[i]));
      asm volatile("" : "=v"(fl2[1]), "=v"(fl2[2]));
    }
    if constexpr (v == VQ - 4) { qg[0] = ld16<AUX_SC1>(wsr, qoff, 0); qg[1] = ld16<AUX_SC1>(wsr, qoff + 16, 0); }
    if constexpr (v == VSIDE) side_loads();
    if constexpr (v == CPF) {
      u64 pc; u32 dlt, len;
      asm volatile("s_getpc_b64 %0\n.Lcpf_here:\n\ts_mov_b32 %1, .Ltail_start-.Lcpf_here\n\ts_mov_b32 %2, .Lend_a-.Ltail_start\n\ts_max_i32 %2, %2, .Lend_b-.Ltail_start\n\ts_max_i32 %2, %2, .Lend_c-.Ltail_start\n\ts_max_i32 %2, %2, .Lend_d-.Ltail_start\n\ts_max_i32 %2, %2, .Lend_e-.Ltail_start" : "=s"(pc), "=s"(dlt), "=s"(len) :: "scc");
      pc += dlt;
      const rsrc_t rc = mk_rsrc((const void*)pc, (part == 3 && i8 == 1) ? len : 0u);
      cpf = __builtin_amdgcn_raw_buffer_load_b32(rc, tid * 64, 0, 0);
    }
    if constexpr (v == CPF + WV + 1) PIN(cpf);
    if constexpr (v == SIDE || v == SIDE + 4 || v == SIDE + 8) fl2[(v - SIDE) / 4] = ld16<AUX_SC1>(wsr, (wave == 0 && v == side) ? mlo : OOB, 0);
    SB();
    if constexpr (v < WV) { WAIT_VM(WK); SB(); } else if constexpr (v < NL && cons_end(v) < v - WV + 1) { WAIT_VM(WV); SB(); }
    if constexpr (v == VQ) {
      if (!all_ok((qg[0][1] == tag) & (qg[0][3] == tag) & (qg[1][1] == tag) & (qg[1][3] == tag))) { STAMP(20); gather16<2>(wsr, qoff, 16, tag, qg); }
      STAMP(1); WSTAMP(1);
      qq[0] = qg[0][0]; qq[1] = qg[0][2]; qq[2] = qg[1][0]; qq[3] = qg[1][2];
    }
    if constexpr (v >= VQ && v < VS) {
      constexpr int sl = v - VQ, bt = sl >> 1;
      float xs[8];
#pragma unroll
      for (int i = 0; i < 8; ++i) {
        float d = 0.f;
        if (sl * 8 + i < NL) {
#pragma unroll
          for (int k = 0; k < 4; ++k) d = dot2(kr[sl * 8 + i][k], qq[k], d);
        }
        xs[i] = d;
      }
#pragma unroll
      for (int h = 0; h < 4; ++h) xs[h] = sel_add<XOR7>(xs[h], xs[h + 4], c & 4);
#pragma unroll
      for (int h = 0; h < 2; ++h) xs[h] = sel_add<XOR2>(xs[h], xs[h + 2], c & 2);
      xs[0] = sel_add<XOR1>(xs[0], xs[1], c & 1);
      if constexpr ((sl & 1) || sl == NSL - 1) {
        const float t = (sl & 1) ? sel_add<XOR8>(xlo, xs[0], c & 8) : xs[0] + dpp<XOR8>(xs[0]);
        const float s = rbf(rbf(t) * 0.08838834764831845f);
        sc[bt] = (bt * 16 + c < nl) ? s : -__builtin_inff();
        PIN(sc[bt]);
      } else {
        xlo = xs[0];
        PIN(xlo);
      }
    }
    if constexpr (v == VS) {
      float mloc = sc[0];
#pragma unroll
      for (int bt = 1; bt < NB; ++bt) mloc = fmaxf(mloc, sc[bt]);
      m = fmaxf(wave_max(mloc), -3.0e38f);
      float ls = 0.f;
#pragma unroll
      for (int bt = 0; bt < NB; ++bt) { pr[bt] = __expf(sc[bt] - m); ls += pr[bt]; }
      const float l = wave_sum(ls);
      if (lane == 0) { sm.ml[wave][0] = m; sm.ml[wave][1] = l; }
      STAMP(2); WSTAMP(2);
    }
    sfor<cons_end(v - 1), cons_end(v)>([&](auto jc) {
      constexpr int i = jc.v;
      if (i < NCMIN / 8 || i < nl) {
        const u32 pv = __float_as_uint(bcast16(u2f(pb[i >> 4]), i & 15));
        acc = mfma16(u32x4{pv & pm[0], pv & pm[1], pv & pm[2], pv & pm[3]}, vr[i], acc);
      }
      PIN(acc);
      return true;
    });
    if constexpr (v == MLS - 1) { side_pin(); if (is_up) up_prep(); PIN(ga); PIN(xin); PIN(cc1); }
    if constexpr (v == UPREP - 1) wo_addr();
    if constexpr (v == UPREP) {
#pragma unroll
      for (int k = 0; k < 8; ++k) asm volatile("" : "=v"(bf[k]));
      if (part == 0) wo_prep(0, 1); else if (is_up) up_frags();
#pragma unroll
      for (int k = 0; k < 8; ++k) PIN(bf[k]);
    }
    if constexpr (v == UPREP + 1) {
      if (is_up) {
        const u32x4* a1 = ap + 128 + ((lane >> 1) & 1) * 128;
        f32x4 d0 = {0.f, 0.f, 0.f, 0.f}, d1 = {0.f, 0.f, 0.f, 0.f};
#pragma unroll
        for (int rb = 0; rb < 4; ++rb) { d0 = mfma16(a1[rb * 8], bf[rb], d0); d1 = mfma16(a1[(rb + 4) * 8], bf[rb + 4], d1); }
        Af = par ? d0[1] + d1[1] : d0[0] + d1[0]; Bf = par ? d0[3] + d1[3] : d0[2] + d1[2];
        Af += dpp<XOR8>(Af); Bf += dpp<XOR8>(Bf);
      }
      PIN(Af); PIN(Bf);
    }
    if constexpr (v == SIDE + WV + 1 || v == SIDE + WV + 5 || v == SIDE + WV + 9) {
      if (wave == 0 && v == side + WV + 1) {
        const u32x4 ml = fl2[(v - SIDE - WV - 1) / 4];
        fok = all_ok((ml[1] == tag) & (ml[3] == tag));
        merge_factor(ml);
      }
    }
    if (v % 6 == 4 && v >= 10 && v < 30) STAMP(15 + v / 6);
    if constexpr (v == MLS) {
      LDS_BARRIER();
      float mm[8], ll[8];
#pragma unroll
      for (int w = 0; w < 8; ++w) { const f32x2 t = *reinterpret_cast<const f32x2*>(&sm.ml[w][0]); mm[w] = t[0]; ll[w] = t[1]; }
      float Mwg = mm[0], Lwg = 0.f;
#pragma unroll
      for (int w = 1; w < 8; ++w) Mwg = fmaxf(Mwg, mm[w]);
      if (wave == 0) {
#pragma unroll
        for (int w = 0; w < 8; ++w) Lwg = fmaf(__expf(mm[w] - Mwg), ll[w], Lwg);
        if (lane == 0) st16_sc1(wsr, G_ML + ((head * 2 + b) * 4 + part) * 16, __float_as_uint(Mwg), __float_as_uint(Lwg), tag);
      }
      const float fw = __expf(m - Mwg);
#pragma unroll
      for (int bt = 0; bt < NB; ++bt) { pb[bt] = pack_bf(pr[bt] * fw, pr[bt] * fw); PIN(pb[bt]); }
#pragma unroll
      for (int k = 0; k < 4; ++k) { pm[k] = (c >> 1) == k ? ((c & 1) ? 0xffff0000u : 0x0000ffffu) : 0u; PIN(pm[k]); }
    }
    SB();
    return true;
  });
  asm volatile(".Ltail_start:");
  *LDSP(f32x4, adst0) = acc;
  STAMP(14); WSTAMP(14);
  if (!fok) {
    u32x4 ml;
#pragma nounroll
    for (int spin = 0; spin < SPIN_MAX; ++spin) {
      CFENCE();
      ml = ld16<AUX_SC1>(wsr, mlo, 0);
      if (all_ok((ml[1] == tag) & (ml[3] == tag))) break;
    }
    merge_factor(ml);
  }
  STAMP(15);
  LDS_BARRIER();
  STAMP(3);
  {
    u32 pw = 0;
    if (wave == 0) {
      f32x2 t[8];
#pragma unroll
      for (int w = 0; w < 8; ++w) t[w] = *reinterpret_cast<const f32x2*>(&sm.red[w][2 * lane]);
      const f32x2 o = ((t[0] + t[1]) + (t[2] + t[3])) + ((t[4] + t[5]) + (t[6] + t[7]));
      pw = min(pack_bf(o[0] * fmrg, o[1] * fmrg), SENT - 1u);
    }
    __builtin_amdgcn_raw_buffer_store_b32(pw, wsr, pso, 0, AUX_SC1);
    st8_sc1(wsr, nto, (u32)wall_clock64() + nadd, tag);
    SB();
#pragma unroll
    for (int i = NCMIN / 8; i < NL; ++i) PIN(vr[i]);
    PIN(fl2[0]); PIN(fl2[1]); PIN(fl2[2]);
  }
  STAMP(4);

  if (part == 2) FINISH();
  if (part == 0) {
    __builtin_amdgcn_s_barrier();
#pragma nounroll
    while (wleft > 0) piece();
    until(t1);
    STAMP(11);
    CFENCE();
    u32x4 og[2];
    og[0] = ld16<AUX_SC1>(wsr, po, 0);
    SB();
    WAIT_VM(1); CFENCE();
    wo_prep(1, 2);
#pragma unroll
    for (int k = 4; k < 8; ++k) PIN(bf[k]);
    SB();
    f32x4 d[4];
    auto wo_mm = [&]() {
      const f32x4 z = {0.f, 0.f, 0.f, 0.f};
#pragma unroll
      for (int t = 0; t < 4; ++t) { d[t] = mfma16(og[0], bf[t], z); og[1][t] = __float_as_uint(dpp<XOR8>(u2f(og[0][t]))); }
      SB();
#pragma unroll
      for (int t = 0; t < 4; ++t) d[t] = mfma16(og[1], bf[4 + t], d[t]);
    };
    auto p_bad = [&]() { return !all_ok(max(max(og[0][0], og[0][1]), max(og[0][2], og[0][3])) != SENT); };
    wo_mm();
    if (__builtin_expect(p_bad(), 0)) {
#pragma nounroll
      for (int spin = 0; spin < SPIN_MAX; ++spin) {
        __builtin_amdgcn_s_sleep(1);
        CFENCE();
        og[0] = ld16<AUX_SC1>(wsr, po, 0);
        if (!p_bad()) break;
      }
      wo_mm();
      SB();
      asm volatile(".Lend_d:");
    }
    STAMP(5); WSTAMP(5);
    {
      SB();
      f32x4 y; f32x2 h[4];
#pragma unroll
      for (int t = 0; t < 4; ++t) { h[t] = d[t].xy + d[t].zw; PIN(h[t]); }
#pragma unroll
      for (int t = 0; t < 4; ++t) { y[t] = h[t][0] + h[t][1]; PIN(y[t]); }
      *LDSP(f32x4, rdst) = y;
    }
    STAMP(16);
    LDS_BARRIER();
    STAMP(17);
    if (wave < 2) {
      f32x2 v[4];
#pragma unroll
      for (int w = 0; w < 4; ++w) v[w] = f32x2{LDSP(float, rsrc)[w * 256], LDSP(float, rsrc)[w * 256 + 128]};
      const f32x2 h = (v[0] + v[1]) + (v[2] + v[3]);
      const float y = h[0] + h[1];
      __builtin_amdgcn_raw_buffer_store_b32(min(pack_bf(y, dpp<XOR1>(y)), SENT - 1u), wsr, xo, 0, AUX_SC1);
      asm volatile(".Lend_a:");
    }
    STAMP(6);
    FINISH();
  }
  if (part == 3) {
    if (!has_w) FINISH();
    u32x4 b2[4];
    {
      __builtin_amdgcn_s_barrier();
      const rsrc_t r2 = mk_rsrc(w2 + (size_t)(blk * 32 + wave * 4) * 512, 4096u);
      const u32 l2 = (u32)(uintptr_t)(&sm.w2b[wave * 4][0]);
#pragma unroll
      for (int n = 0; n < 4; ++n) dma16(r2, (lane ^ (n * 2) ^ ((wave & 2) << 2)) * 16, n * 1024, l2 + n * 1024);
    }
    const u32 tdst = lds_addr(&sm.hb[hpos]);
    const u32 sdst = lds_addr(&sm.stat[wave][lane]);
    const u32 ssrc = lds_addr(&sm.stat[(g >> 1) * 4 + ((lane >> 1) & 3)][(lane & 1) * 16]);
    const u32 adst = lds_addr((c < 4 && (g & 1) == 0) ? &sm.actl[g >> 1][wave * 4 + c] : reinterpret_cast<u16*>(&sm.dummy[lane]));
    const u32 asrc = lds_addr(&sm.actl[lane & 1][g * 8]);
    const u32 apa = lds_addr(&sm.hb[(c >> 3) * 512 + (c & 3) * 64 + ((c >> 2) & 1) * 32 + g * 8]);
    u32 xoff = G_XP + g * 2048 + ub * 1024 + (wave & 3) * 256 + c * 16;
    u32x4 one;
#pragma unroll
    for (int k = 0; k < 4; ++k) one[k] = ((c & 2) == 0 && (c >> 2) == k) ? ((c & 1) ? 0x3f800000u : 0x00003f80u) : 0u;
    PIN(one);
    u32 ooff = G_OP + (blk * 512 + tid) * 8;
    float Afp = Af, Bfp = Bf;
    PIN(xoff); PIN(ooff); PIN(Afp); PIN(Bfp);
    PIN(ga); PIN(xin); PIN(cc1);
    u32 w2a = (u32)(uintptr_t)(&sm.w2b[0][0]) + (g * 8 + ((lane >> 2) & 3)) * 1024 + (wave ^ (g & 1)) * 128 + (lane & 15) * 8;
    PIN(w2a);
    if (wave == 1) {
      due = t1 + P.N1 * 256; gate();
      u32x4 gn[2] = {ld16<AUX_SC1>(wsr, G_NT + lane * 16, 0), ld16<AUX_SC1>(wsr, G_NT + lane * 16, 1024)};
      const int tn = notices(IC<2>{}, G_NT + lane * 16, gn) * 256;
      due = (tn > t1 + 2 * 256 ? max(tn, t1 + P.L0 * 256) + P.L1 * 256 : t1) + P.X2 * 256;
      until(due);
    }
    asm volatile("s_waitcnt vmcnt(0)" ::: "memory");
    STAMP(9);
    __builtin_amdgcn_s_barrier();
    STAMP(11);
    CFENCE();
    u32x4 xg[2] = {ld16<AUX_SC1>(wsr, xoff, 0), ld16<AUX_SC1>(wsr, xoff, 8192)};
    SB();
#pragma unroll
    for (int t = 0; t < 4; ++t) {
      const u32x2 lo = tr_read(w2a ^ (t * 32)), hi = tr_read((w2a ^ (t * 32)) + 4096);
      b2[t] = u32x4{lo[0], lo[1], hi[0], hi[1]};
    }
#pragma unroll
    for (int t = 0; t < 4; ++t) PIN(b2[t]);
    SB();
    const f32x4 z4 = {0.f, 0.f, 0.f, 0.f};
    auto xp_bad = [&]() { return !all_ok(max(max(max(xg[0][0], xg[0][1]), max(xg[0][2], xg[0][3])), max(max(xg[1][0], xg[1][1]), max(xg[1][2], xg[1][3]))) != SENT); };
    f32x4 v = mfma16(one, xg[1], mfma16(one, xg[0], z4));
    if (__builtin_expect(xp_bad(), 0)) {
#pragma nounroll
      for (int spin = 0; spin < SPIN_MAX; ++spin) {
        __builtin_amdgcn_s_sleep(1);
        CFENCE();
        xg[0] = ld16<AUX_SC1>(wsr, xoff, 0); xg[1] = ld16<AUX_SC1>(wsr, xoff, 8192);
        if (!xp_bad()) break;
      }
      v = mfma16(one, xg[1], mfma16(one, xg[0], z4));
      SB();
      asm volatile(".Lend_e:");
    }
    STAMP(5); WSTAMP(5);
    {
      const float a0 = v[0], a1 = v[1];
      const float x10 = fmaf(ga[0], a0, xin[0]), x11 = fmaf(ga[1], a1, xin[1]);
      *LDSP(u32, tdst) = pack_bf(x10 * cc1[0], x11 * cc1[1]);
      float u = swap32_add(x10 + x11, fmaf(x10, x10, x11 * x11));
      u += dpp<XOR1>(u); u += dpp<XOR2>(u); u += dpp<XOR7>(u); u += dpp<XOR8>(u);
      *LDSP(float, sdst) = u;
    }
    STAMP(16);
    LDS_BARRIER();
    STAMP(17);
    {
      const f32x4 z = {0.f, 0.f, 0.f, 0.f};
      const u32x4 af0 = LDSP(u32x4, apa)[0], af1 = LDSP(u32x4, apa)[32];
      float s = LDSP(float, ssrc)[0], q = LDSP(float, ssrc)[32];
      SB();
      f32x4 d[4];
#pragma unroll
      for (int j = 0; j < 4; ++j) d[j] = mfma16(af0, bf[j], z);
#pragma unroll
      for (int j = 0; j < 4; ++j) d[j] = mfma16(af1, bf[4 + j], d[j]);
      s += dpp<XOR1>(s); q += dpp<XOR1>(q); s += dpp<XOR2>(s); q += dpp<XOR2>(q); s += dpp<XOR7>(s); q += dpp<XOR7>(q);
      const float mean = s * (1.f / 512.f);
      const float var = fmaf(q, 1.f / 512.f, -mean * mean);
      const float ri = __builtin_amdgcn_rsqf(fmaxf(var, 0.f) + 1e-5f);
      const float cf = fmaf(-ri * mean, Afp, Bfp);
      float y = (d[0][0] + d[1][1]) + (d[2][2] + d[3][3]);
      y = __builtin_bit_cast(float, __builtin_amdgcn_update_dpp(__builtin_bit_cast(int, y), __builtin_bit_cast(int, y), XOR8, 0xa, 0xf, false));
      y = swap16_add(y, y);
      y = fmaf(y, ri, cf);
      const float up = dpp<0x104>(y);
      *LDSP(u16, adst) = (u16)pack_bf(silu_f(y) * up, 0.f);
    }
    STAMP(18);
    LDS_BARRIER();
    STAMP(10);
    {
      const u32x4 af = *LDSP(u32x4, asrc);
      const f32x4 z = {0.f, 0.f, 0.f, 0.f};
      const f32x4 d0 = mfma16(af, b2[0], z), d1 = mfma16(af, b2[1], z), d2 = mfma16(af, b2[2], z), d3 = mfma16(af, b2[3], z);
      const bool g1 = g & 1, g2 = g & 2;
      const float e0 = g1 ? d1[0] : d0[0], e1 = g1 ? d1[1] : d0[1], f0 = g1 ? d3[0] : d2[0], f1 = g1 ? d3[1] : d2[1];
      st8_sc1(wsr, ooff, pack_bf(g2 ? f0 : e0, g2 ? f1 : e1), tag);
    }
    asm volatile(".Lend_b:");
    STAMP(6);
    FINISH();
  }
  if (wave == 0) {
    const int d = fd, ul = lane >> 3;
    u32 gm[2] = {sg[0][0], sg[1][0]};
    if (!all_ok((sg[0][1] == tag) & (sg[1][1] == tag))) gather8<2>(wsr, G_MOD + (1024 + d) * 8, 1536 * 8, tag, gm);
    u32 xa0, xa1;
    {
      due = t1 + P.N2 * 256; gate();
      u32x4 gn[2] = {ld16<AUX_SC1>(wsr, G_NT + lane * 16, 0), ld16<AUX_SC1>(wsr, G_NT + lane * 16, 1024)};
      const int tn = notices(IC<2>{}, G_NT + lane * 16, gn) * 256;
      due = (tn > t1 + 2 * 256 ? max(tn, t1 + P.L0 * 256) + P.L1 * 256 : t1) + P.X3 * 256;
      until(due);
    }
#pragma nounroll
    for (int spin = 0; spin < SPIN_MAX; ++spin) {
      CFENCE();
      xa0 = __builtin_amdgcn_raw_buffer_load_b32(wsr, G_XP + ul * 2048 + (d >> 1) * 4, 0, AUX_SC1);
      xa1 = __builtin_amdgcn_raw_buffer_load_b32(wsr, G_XP + ul * 2048 + (d >> 1) * 4, 1024, AUX_SC1);
      if (all_ok(max(xa0, xa1) != SENT)) break;
      __builtin_amdgcn_s_sleep(1);
    }
    float a0 = (d & 1) ? hi_f(xa0) : lo_f(xa0), a1 = (d & 1) ? hi_f(xa1) : lo_f(xa1);
    a0 += dpp<XOR8>(a0); a1 += dpp<XOR8>(a1);
    a0 = swap16_add(a0, a0); a1 = swap16_add(a1, a1);
    a0 = swap32_add(a0, a0); a1 = swap32_add(a1, a1);
    const bool hb = g & 1;
    float xv = rbf(((d & 1) ? hi_f(sx2) : lo_f(sx2)) + rbf((hb ? hi_f(gm[0]) : lo_f(gm[0])) * rbf(hb ? a1 : a0)));
    float gv = hb ? hi_f(gm[1]) : lo_f(gm[1]);
    PIN(xv); PIN(gv);
    if (wi == 0 && lane == 0) __builtin_amdgcn_raw_buffer_store_b32(tag, wsr, WS_EPOCH, 0, AUX_SC1);
    STAMP(10);
    due = max(due + P.X4 * 256, (int)(((u32)wall_clock64() - t0) << 8) + P.GX * 256);
    const bool last = ul + 40 < NUP;
    u32 o0 = G_OP + (ul * 512 + d) * 8, o5 = last ? o0 : OOB, tg5 = last ? tag : 0u;
    const rsrc_t rout = mk_rsrc(out, 2048u);
    u32 oo = (lane & 40) == 0 ? (g * 512 + d) * 2 : OOB;
    const u32 pk = c == 0 ? 0x00003f80u : (c == 4 ? 0x3f800000u : 0u);
    u32x4 one = {pk, 0u, pk, 0u};
    PIN(o0); PIN(o5); PIN(tg5); PIN(oo); PIN(one);
    __builtin_amdgcn_raw_buffer_store_b16((u16)pack_bf(xv, 0.f), rout, oo, 0, AUX_SC1);
    until(due);
    f32x4 sv;
#pragma nounroll
    for (int spin = 0; spin < SPIN_MAX; ++spin) {
      CFENCE();
      u32x2 og[6];
#pragma unroll
      for (int k = 0; k < 5; ++k) og[k] = ld8<AUX_SC1>(wsr, o0, k * 32768);
      og[5] = ld8<AUX_SC1>(wsr, o5, 5 * 32768);
      sv = f32x4{0.f, 0.f, 0.f, 0.f};
#pragma unroll
      for (int k = 0; k < 3; ++k) sv = mfma16(one, u32x4{og[2 * k][0], og[2 * k][1], og[2 * k + 1][0], og[2 * k + 1][1]}, sv);
      const u32 bad = ((og[0][1] ^ tag) | (og[1][1] ^ tag) | (og[2][1] ^ tag)) | ((og[3][1] ^ tag) | (og[4][1] ^ tag) | (og[5][1] ^ tg5));
      PIN(sv);
      if (all_ok(bad == 0u)) break;
      __builtin_amdgcn_s_sleep(1);
    }
    STAMP(7);
    const float s = sv[0] + dpp<XOR8>(sv[0]);
    __builtin_amdgcn_raw_buffer_store_b16((u16)pack_bf(fmaf(gv, s, xv), 0.f), rout, oo, 0, AUX_SC1);
    asm volatile(".Lend_c:");
  }
  FINISH();
}

extern "C" void kernel_launch(void* const* d_in, const int* in_sizes, int n_in,
                              void* d_out, int out_size, void* d_ws, size_t ws_size,
                              hipStream_t stream) {
  fused_kernel<<<256, 512, 0, stream>>>((const u16*)d_in[1], (const u16*)d_in[2], (const u16*)d_in[4], (const u16*)d_in[3], (const u16*)d_in[10], (const u16*)d_in[6], (const u16*)d_in[7],
                                        (unsigned char*)d_ws, (const u16*)d_in[0], (const u16*)d_in[9], (const u16*)d_in[8], (u16*)d_out
    );
}
```

```cpp
#define PROBE_MUL 10
#include <hip/hip_runtime.h>
#include <stdint.h>

typedef unsigned int u32;
typedef unsigned short u16;
typedef unsigned long long u64;
typedef __attribute__((ext_vector_type(4))) unsigned u32x4;
typedef __attribute__((ext_vector_type(2))) unsigned u32x2;
typedef __attribute__((ext_vector_type(2))) __bf16 bf16x2;
typedef __attribute__((ext_vector_type(4))) float f32x4;
typedef __attribute__((ext_vector_type(2))) float f32x2;
typedef __amdgpu_buffer_rsrc_t rsrc_t;

constexpr int NH = 32, SK = 16384, DH = 128, WIN = 4096, FF = 1376;
struct Prm { int E0, ES, E1, E2, E2C, EU, TEND, DT_UP, L0, L1, X1, X2, X3, X4, NE, FE, M0, M1, M2, M3, M4, GS, GX, N1, N2, GA, WD, DT_WO, LW, LU, EW, PM, NF; };
constexpr Prm PRM0 = {32, 24, 32, 32, 56, 64, 2030, 60, 30, 25, 92, 86, 131, 88, 4, 12, 5, 8, 21, 4, 32, 30, 56, 28, 70, 12, 4, 100, 45, 15, 8, 0x24310, 2};
#define PRM_ARG
constexpr int NL = 37;
constexpr int NCMIN = 192;
constexpr int WK = 8, WV = 5;
constexpr int WUSE = 12, LNS = 7;
constexpr int QP = 14, QD = 4;
constexpr int VQ = 6;
constexpr int NSL = (NL + 7) / 8;
constexpr int VS = VQ + NSL;
constexpr int MLS = VS + 2;
constexpr int VSIDE = MLS - 5, UPREP = MLS + 8;
constexpr int SIDE = NCMIN / 8 - 4;
constexpr int CPF = 18;
constexpr int SMO = 1;
constexpr int TBP = 4, TBL = 11, TBU = 15;
constexpr int cons_end(int v) {
  const int a = v - WV + 1, c = (v - MLS) * 4;
  const int m = a < c ? a : c;
  return m < 0 ? 0 : (m > NL ? NL : m);
}
struct Tab { u32 e[512]; };
constexpr Tab make_tab(const Prm& P) {
  Tab T{};
  for (int bid = 0; bid < 256; ++bid) {
    const int xcd = bid & 7, part = bid >> 4 & 3, i8 = (bid >> 3 & 1) | bid >> 6 << 1, wi = i8 * 8 + xcd;
    const bool has_mod = wi < 48, has_up = wi >= 21;
    const int cb0 = (i8 + 5) & 7, cb1 = (i8 + 4) & 7, ms = wi - 20;
    const int mi = wi < 8 ? wi : (wi < 16 ? 8 + ((xcd + 4) & 7) : (wi < 20 ? (wi - 14) * 8 + 3 : 16 + ms / 7 * 8 + ms % 7 + (ms % 7 >= 3 ? 1 : 0)));
    const int ui = xcd * 5 + (xcd > 5 ? xcd - 5 : 0) + (i8 >= 3 ? (xcd >= 5 ? 1 : 0) + i8 - 3 : 0);
    const int wd = cb0 == 3 ? 0 : P.WD;
    const int e[4] = {P.E0 - 4 * wd + (cb0 == 3 ? P.ES : 0), P.E1 + (cb1 == 3 ? P.ES : 0), has_mod ? (wi < 16 ? P.E2C : P.E2) + ((mi & 7) == 3 ? P.ES : 0) : 0, has_up ? P.EU : 0};
    const int ew = wd ? P.EW : 0, pa = (1024 + e[0] + ew + e[1] + e[2] + e[3]) / 4;
    int c[5] = {0, 0, 0, 0, 124};
    for (int k = 0; k < 3; ++k) c[k + 1] = c[k] + (pa - e[k] - (k == 0 ? ew : 0) + 4) / 8 - 1;
    const int nl = c[part + 1] - c[part] + 1, wst = e[part] / 4;
    const bool has_w = part < 2 || (part == 2 ? has_mod : has_up);
    const int blk = !has_w ? 0 : (part == 0 ? cb0 : (part == 1 ? xcd * 8 + cb1 : (part == 2 ? mi : ui)));
    const int tau = (part == 3 && has_up ? P.TEND - P.DT_UP : (part == 0 && wd ? P.TEND - P.DT_WO : P.TEND)) * 256 / (2 * nl + wst);
    T.e[2 * bid] = (u32)(c[part] | nl << 7 | blk << 13 | (has_w ? 1 : 0) << 19 | (part == 0 ? wd : 0) << 20);
    T.e[2 * bid + 1] = (u32)(tau | (tau * wst / (part == 0 ? 8 - wd : 8)) << 16);
  }
  return T;
}
constexpr int step_range(const Prm& P, bool mx_) {
  int mx = 0, mn = 1 << 20;
  const Tab T = make_tab(P);
  for (int bid = 0; bid < 256; ++bid) { const int n = T.e[2 * bid] >> 7 & 63; mx = n > mx ? n : mx; mn = n < mn ? n : mn; }
  return mx_ ? mx : mn;
}
extern "C" __attribute__((used, visibility("protected"))) __constant__ const Tab wg_tab = make_tab(PRM0);
static_assert(step_range(PRM0, true) <= NL && step_range(PRM0, false) >= NCMIN / 8, "a wave's key range does not fit its load steps");
static_assert(cons_end(SIDE + WV + 1) <= NCMIN / 8 && cons_end(UPREP) <= NCMIN / 8 && SIDE > MLS + 3, "steps every wave runs");
constexpr int NUP = FF / 32;
constexpr int SPIN_MAX = 1 << 17;

constexpr u32 WS_EPOCH = 0;
constexpr u32 G_TB = 1024;
constexpr u32 G_NT = 2048;
constexpr u32 G_MOD = 4096;
constexpr u32 G_Q = G_MOD + 3072 * 8;
constexpr u32 G_ML = G_Q + 4096 * 8;
constexpr u32 G_P = G_ML + 256 * 16;
constexpr u32 G_XP = G_P + 256 * 256;
constexpr u32 G_OP = G_XP + 8 * 512 * 4;
constexpr u32 WS_END = G_OP + 43 * 512 * 8;
constexpr u32 SENT = 0xffffffffu;
constexpr u32 OOB = 0x40000000u;
#define WST 0

constexpr int AUX_NT = 2, AUX_SC1 = 16;

__device__ __forceinline__ float lo_f(u32 v) { return __uint_as_float(v << 16); }
__device__ __forceinline__ float hi_f(u32 v) { return __uint_as_float(v & 0xffff0000u); }
__device__ __forceinline__ float rbf(float x) { return (float)(__bf16)x; }
__device__ __forceinline__ u32 pack_bf(float lo, float hi) {
  bf16x2 r; r[0] = (__bf16)lo; r[1] = (__bf16)hi; return __builtin_bit_cast(u32, r);
}
__device__ __forceinline__ float dot2(u32 k, u32 q, float acc) {
  return __builtin_amdgcn_fdot2_f32_bf16(__builtin_bit_cast(bf16x2, k), __builtin_bit_cast(bf16x2, q), acc, false);
}
template <int CTRL> __device__ __forceinline__ float dpp(float x) {
  return __builtin_bit_cast(float, __builtin_amdgcn_mov_dpp(__builtin_bit_cast(int, x), CTRL, 0xf, 0xf, true));
}
constexpr int XOR1 = 0xB1, XOR2 = 0x4E, XOR7 = 0x141, XOR8 = 0x128;
__device__ __forceinline__ float swap32_add(float a, float b) {
  auto t = __builtin_amdgcn_permlane32_swap(__float_as_uint(a), __float_as_uint(b), false, false);
  return __uint_as_float(t[0]) + __uint_as_float(t[1]);
}
__device__ __forceinline__ float swap16_add(float a, float b) {
  auto t = __builtin_amdgcn_permlane16_swap(__float_as_uint(a), __float_as_uint(b), false, false);
  return __uint_as_float(t[0]) + __uint_as_float(t[1]);
}
__device__ __forceinline__ float wave_sum(float x) {
  x += dpp<XOR1>(x); x += dpp<XOR2>(x); x += dpp<XOR7>(x); x += dpp<XOR8>(x);
  x = swap16_add(x, x);
  return swap32_add(x, x);
}
__device__ __forceinline__ float wave_max(float x) {
  x = fmaxf(x, dpp<XOR1>(x)); x = fmaxf(x, dpp<XOR2>(x)); x = fmaxf(x, dpp<XOR7>(x)); x = fmaxf(x, dpp<XOR8>(x));
  auto s = __builtin_amdgcn_permlane16_swap(__float_as_uint(x), __float_as_uint(x), false, false);
  x = fmaxf(__uint_as_float(s[0]), __uint_as_float(s[1]));
  auto t = __builtin_amdgcn_permlane32_swap(__float_as_uint(x), __float_as_uint(x), false, false);
  return fmaxf(__uint_as_float(t[0]), __uint_as_float(t[1]));
}
template <int CTRL> __device__ __forceinline__ float sel_add(float lo, float hi, bool up) {
  const float t1 = lo + dpp<CTRL>(lo), t2 = hi + dpp<CTRL>(hi);
  return up ? t2 : t1;
}
template <int I> __device__ __forceinline__ float bc16(float x) {
  return __builtin_bit_cast(float, __builtin_amdgcn_update_dpp(0, __builtin_bit_cast(int, x), 0x150 + I, 0xf, 0xf, false));
}
__device__ __forceinline__ float bcast16(float x, int i) {
  switch (i) {
    case 0: return bc16<0>(x); case 1: return bc16<1>(x); case 2: return bc16<2>(x); case 3: return bc16<3>(x);
    case 4: return bc16<4>(x); case 5: return bc16<5>(x); case 6: return bc16<6>(x); case 7: return bc16<7>(x);
    case 8: return bc16<8>(x); case 9: return bc16<9>(x); case 10: return bc16<10>(x); case 11: return bc16<11>(x);
    case 12: return bc16<12>(x); case 13: return bc16<13>(x); case 14: return bc16<14>(x); default: return bc16<15>(x);
  }
}
__device__ __forceinline__ float silu_f(float x) { return x * __builtin_amdgcn_rcpf(1.f + __expf(-x)); }

template <int AUX> __device__ __forceinline__ u32x4 ld16(rsrc_t r, u32 voff, u32 soff) {
  return __builtin_amdgcn_raw_buffer_load_b128(r, voff, soff, AUX);
}
template <int AUX> __device__ __forceinline__ u32x2 ld8(rsrc_t r, u32 voff, u32 soff) {
  return __builtin_amdgcn_raw_buffer_load_b64(r, voff, soff, AUX);
}
__device__ __forceinline__ void st8_sc1(rsrc_t r, u32 voff, u32 v, u32 tag) {
  __builtin_amdgcn_raw_buffer_store_b64(u32x2{v, tag}, r, voff, 0, AUX_SC1);
}
__device__ __forceinline__ void st16_sc1(rsrc_t r, u32 voff, u32 v0, u32 v1, u32 tag) {
  __builtin_amdgcn_raw_buffer_store_b128(u32x4{v0, tag, v1, tag}, r, voff, 0, AUX_SC1);
}
__device__ __forceinline__ rsrc_t mk_rsrc(const void* p, u32 bytes) {
  return __builtin_amdgcn_make_buffer_rsrc(const_cast<void*>(p), 0, bytes, 0x00020000);
}
#define WAIT_VM(n) __builtin_amdgcn_s_waitcnt(((n) & 15) | (((n) >> 4) << 14) | (7 << 4) | (15 << 8))
#define SB() __builtin_amdgcn_sched_barrier(0)
#define CFENCE() asm volatile("" ::: "memory")
__device__ __forceinline__ bool all_ok(bool ok) { return __builtin_amdgcn_ballot_w64(!ok) == 0ull; }

template <int N> __device__ __forceinline__ void gather16(rsrc_t ws, u32 voff, u32 stride, u32 tag, u32x4 (&g)[N]) {
#pragma nounroll
  for (int spin = 0; spin < SPIN_MAX; ++spin) {
    CFENCE();
    bool ok = true;
#pragma unroll
    for (int i = 0; i < N; ++i) g[i] = ld16<AUX_SC1>(ws, voff + i * stride, 0);
#pragma unroll
    for (int i = 0; i < N; ++i) ok &= (g[i][1] == tag) & (g[i][3] == tag);
    if (all_ok(ok)) break;
    __builtin_amdgcn_s_sleep(8);
  }
}
template <int N> __device__ __forceinline__ void gather8(rsrc_t ws, u32 voff, u32 stride, u32 tag, u32 (&g)[N], bool act = true) {
#pragma nounroll
  for (int spin = 0; spin < SPIN_MAX; ++spin) {
    CFENCE();
    bool ok = true;
    u32x2 t[N];
#pragma unroll
    for (int i = 0; i < N; ++i) t[i] = ld8<AUX_SC1>(ws, voff + i * stride, 0);
#pragma unroll
    for (int i = 0; i < N; ++i) { ok &= (t[i][1] == tag); g[i] = t[i][0]; }
    if (all_ok(ok | !act)) break;
    __builtin_amdgcn_s_sleep(8);
  }
}

struct Smem {
  float red[8][128];
  float ml[8][2];
  float r2[64 * 20];
  u16 hb[3 * 1024];
  float stat[8][64];
  float red2[8][128];
  u32 dummy[64];
  u32x2 bc;
  u32x4 zero[4];
  u32 mflag[8];
  u32x4 wbuf[8][8][64];
  __attribute__((aligned(256))) u16 w2b[32][512];
  u16 actl[2][32];
};

#define PROBE_K 102
#define PROBE_OP 2
#define PROBE_PART -1
#define STAMP(k) do { if ((k) == PROBE_K && tid == 0) pst = wall_clock64(); } while (0)
#define WSTAMP(k) do {} while (0)
#define FINISH() do { STAMP(8); if (tid == 0) { __hip_atomic_store(reinterpret_cast<u64*>(ws + (1u << 20)) + bid * 2, pt0, __ATOMIC_RELAXED, __HIP_MEMORY_SCOPE_AGENT); \
   __hip_atomic_store(reinterpret_cast<u64*>(ws + (1u << 20)) + bid * 2 + 1, (PROBE_PART < 0 || part == PROBE_PART) ? pst : 0ull, __ATOMIC_RELAXED, __HIP_MEMORY_SCOPE_AGENT); } \
   if (bid == 8 && wave == 0) probe_end(ws, pt0, lane); return; } while (0)
#define PIN(x) asm volatile("" : "+v"(x))
#define LDS_BARRIER() do { __builtin_amdgcn_s_waitcnt(0xc07f); __builtin_amdgcn_s_barrier(); CFENCE(); } while (0)
#define WAVE_SYNC() do { __builtin_amdgcn_fence(__ATOMIC_RELEASE, "wavefront"); __builtin_amdgcn_wave_barrier(); __builtin_amdgcn_fence(__ATOMIC_ACQUIRE, "wavefront"); } while (0)

__device__ __forceinline__ void dma16(rsrc_t r, u32 voff, u32 soff, u32 lds_dst) {
  u32 keep;
  asm volatile("s_mov_b32 %0, m0\n\ts_mov_b32 m0, %4\n\ts_nop 4\n\tbuffer_load_dwordx4 %1, %2, %3 offen nt lds\n\ts_mov_b32 m0, %0"
               : "=&s"(keep) : "v"(voff), "s"(r), "s"(soff), "s"(lds_dst) : "memory");
}
__device__ __forceinline__ u32 perm_lo(u32 a, u32 b) { return __builtin_amdgcn_perm(b, a, 0x05040100u); }
__device__ __forceinline__ u32 perm_hi(u32 a, u32 b) { return __builtin_amdgcn_perm(b, a, 0x07060302u); }
__device__ __forceinline__ float rdl(float x, int l) { return __int_as_float(__builtin_amdgcn_readlane(__float_as_int(x), l)); }

typedef __attribute__((ext_vector_type(8))) __bf16 bf16x8;
__device__ __forceinline__ f32x4 mfma16(u32x4 a, u32x4 b, f32x4 c) {
  return __builtin_amdgcn_mfma_f32_16x16x32_bf16(__builtin_bit_cast(bf16x8, a), __builtin_bit_cast(bf16x8, b), c, 0, 0, 0);
}
__device__ __forceinline__ float sum8(const float* p) {
  const f32x4 a = *reinterpret_cast<const f32x4*>(p), b = *reinterpret_cast<const f32x4*>(p + 4);
  return ((a[0] + b[0]) + (a[1] + b[1])) + ((a[2] + b[2]) + (a[3] + b[3]));
}
__device__ __forceinline__ float ln_ri(float var) {
  return __builtin_amdgcn_rcpf(rbf(__builtin_amdgcn_sqrtf(rbf(rbf(fmaxf(var, 0.f)) + 1e-5f))));
}
__device__ __forceinline__ void ln_stats2(const float (&x0)[8], const float (&x1)[8], float& mb0, float& ri0, float& mb1, float& ri1) {
  const float k0 = rdl(x0[0], 0), k1 = rdl(x1[0], 0);
  float s0 = 0.f, s1 = 0.f, q0 = 0.f, q1 = 0.f;
#pragma unroll
  for (int k = 0; k < 8; ++k) {
    const float d0 = x0[k] - k0, d1 = x1[k] - k1;
    s0 += d0; s1 += d1; q0 = fmaf(d0, d0, q0); q1 = fmaf(d1, d1, q1);
  }
  float t = swap16_add(swap32_add(s0, s1), swap32_add(q0, q1));
  t += dpp<XOR1>(t); t += dpp<XOR2>(t); t += dpp<XOR7>(t); t += dpp<XOR8>(t);
  auto u = __builtin_amdgcn_permlane16_swap(__float_as_uint(t), __float_as_uint(t), false, false);
  const float ms = __uint_as_float(u[0]) * (1.f / 512.f), mq = __uint_as_float(u[1]) * (1.f / 512.f);
  const float ri = ln_ri(fmaf(-ms, ms, mq));
  mb0 = rbf(k0 + rdl(ms, 0)); mb1 = rbf(k1 + rdl(ms, 32));
  ri0 = rdl(ri, 0); ri1 = rdl(ri, 32);
}
template <int I> struct IC { static constexpr int v = I; };
template <int I, int N, class F> __device__ __forceinline__ void sfor(F&& f) {
  if constexpr (I < N) { if (f(IC<I>{})) sfor<I + 1, N>(f); }
}
typedef __attribute__((ext_vector_type(4))) short s16x4;
typedef __attribute__((address_space(3))) s16x4* lds_s16x4p;
__device__ __forceinline__ u32x2 tr_read(u32 lds_addr) {
  return __builtin_bit_cast(u32x2, __builtin_amdgcn_ds_read_tr16_b64_v4i16((lds_s16x4p)lds_addr));
}
__device__ __forceinline__ u32 next_tag(u32 e) { return ((e | 0x40004000u) + 1u) & ~0x40004000u; }
__device__ __forceinline__ float u2f(u32 v) { return __uint_as_float(v); }
#define LDSP(T, a) ((__attribute__((address_space(3))) T*)(a))
template <class T> __device__ __forceinline__ u32 lds_addr(T* p) { u32 a = (u32)(uintptr_t)p; PIN(a); return a; }

#define TAB_LOAD "s_add_u32 s30, s28, wg_tab@rel32@lo+4\ns_addc_u32 s31, s29, wg_tab@rel32@hi+12\nglobal_load_dwordx2 %[te], %[va], s[30:31]\n"
__device__ __forceinline__ void probe_end(unsigned char* ws, u64 pt0, int lane) {
  while (wall_clock64() < pt0 + 5500ull) __builtin_amdgcn_s_sleep(8);
  u64 a[4], c2[4];
  for (int i = 0; i < 4; ++i) {
    a[i] = __hip_atomic_load(reinterpret_cast<u64*>(ws + (1u << 20)) + (i * 64 + lane) * 2, __ATOMIC_RELAXED, __HIP_MEMORY_SCOPE_AGENT);
    c2[i] = __hip_atomic_load(reinterpret_cast<u64*>(ws + (1u << 20)) + (i * 64 + lane) * 2 + 1, __ATOMIC_RELAXED, __HIP_MEMORY_SCOPE_AGENT);
  }
  int mn0 = 1 << 30, mx1 = -(1 << 30), mn1 = 1 << 30, sum = 0, cnt = 0;
  for (int i = 0; i < 4; ++i) {
    const int d0 = (int)(a[i] - pt0);
    mn0 = min(mn0, d0);
    if (c2[i] != 0ull) { const int d1 = (int)(c2[i] - pt0); mx1 = max(mx1, d1); mn1 = min(mn1, d1); sum += d1; cnt += 1; }
  }
  for (int o = 1; o < 64; o <<= 1) { mn0 = min(mn0, __shfl_xor(mn0, o)); mx1 = max(mx1, __shfl_xor(mx1, o)); mn1 = min(mn1, __shfl_xor(mn1, o)); sum += __shfl_xor(sum, o); cnt += __shfl_xor(cnt, o); }
  int X = PROBE_OP == 0 ? mx1 : (PROBE_OP == 1 ? sum / max(cnt, 1) : mn1);
  if (PROBE_K == 102) X = mn0;
  X = mn0 + (X - mn0) * PROBE_MUL;
  const u64 tg = pt0 + (u64)(long long)(X + 8000);
  for (int i = 0; i < 400000 && wall_clock64() < tg; ++i) __builtin_amdgcn_s_sleep(1);
}
__global__ void __launch_bounds__(512, 2) fused_kernel(const u16* kc_, const u16* vc_, const u16* wo_, const u16* wq_, const u16* wmod_, const u16* w1_, const u16* w3_,
                                                       unsigned char* ws_, const u16* x_, const u16* cnd_, const u16* w2_, u16* out_ PRM_ARG) {
  __shared__ __attribute__((aligned(256))) Smem sm;
  const u64 a_ka = (u64)__builtin_amdgcn_kernarg_segment_ptr();
  const int bid = blockIdx.x, tid = threadIdx.x;
  u64 wa, t64; u32x2 te; int tid0;
  u32 ar[14];
  u32 tch2, sa_, sb_, va_;
  u32x4 kr[NL], vr[NL];
  asm volatile(
    "s_load_dwordx16 s[4:19], %[ka], 0x0\ns_load_dwordx8 s[20:27], %[ka], 0x40\n"
    "s_memrealtime %[t]\n"
    "v_lshlrev_b32 %[va], 3, %[bid]\n"
    "s_getpc_b64 s[28:29]\n.Lcpf_first:\n"
    TAB_LOAD
    "s_cmp_gt_u32 %[bid], 15\ns_cbranch_scc1 .Lno_cpf\n"
    "s_mov_b32 %[sa], .Lend_a-.Lcpf_first\ns_max_i32 %[sa], %[sa], .Lend_b-.Lcpf_first\ns_max_i32 %[sa], %[sa], .Lend_c-.Lcpf_first\ns_max_i32 %[sa], %[sa], .Lend_d-.Lcpf_first\ns_max_i32 %[sa], %[sa], .Lend_e-.Lcpf_first\n"
    "v_lshlrev_b32 %[va], 6, %[tid]\nv_min_u32 %[va], %[sa], %[va]\nglobal_load_dword %[pf], %[va], s[28:29]\n"
    ".Lno_cpf:\n"
    "s_and_b32 %[sa], %[bid], 15\ns_lshr_b32 %[sb], %[bid], 6\ns_lshl2_add_u32 %[sa], %[sa], %[sb]\ns_lshl_b32 %[sa], %[sa], 22\ns_xor_b32 %[sa], %[sa], 0xff0000\n"
    "s_and_b32 %[sb], %[bid], 48\nv_lshl_add_u32 %[va], %[sb], 5, %[tid]\nv_lshl_add_u32 %[va], %[va], 4, %[sa]\n"
    "v_readfirstlane_b32 %[t0], %[tid]\n"
    "s_waitcnt lgkmcnt(0)\nglobal_load_dwordx4 %[k0], %[va], s[4:5] nt\nglobal_load_dwordx4 %[v0], %[va], s[6:7] nt\n"
    "s_bitcmp1_b32 %[t0], 6\ns_cselect_b64 s[14:15], s[16:17], s[14:15]\n"
    "s_bitcmp1_b32 %[bid], 4\ns_cselect_b64 s[8:9], s[10:11], s[8:9]\ns_cselect_b64 s[12:13], s[14:15], s[12:13]\ns_bitcmp1_b32 %[bid], 5\ns_cselect_b64 %[wa], s[12:13], s[8:9]"
    : "=&{s4}"(ar[0]), "=&{s5}"(ar[1]), "=&{s6}"(ar[2]), "=&{s7}"(ar[3]), "=&{s18}"(ar[4]), "=&{s19}"(ar[5]), "=&{s20}"(ar[6]), "=&{s21}"(ar[7]), "=&{s22}"(ar[8]), "=&{s23}"(ar[9]),
      "=&{s24}"(ar[10]), "=&{s25}"(ar[11]), "=&{s26}"(ar[12]), "=&{s27}"(ar[13]), [wa] "=&s"(wa), [t0] "=&s"(tid0), [t] "=&s"(t64), [te] "=&v"(te), [sa] "=&s"(sa_), [sb] "=&s"(sb_),
      [va] "=&v"(va_), [pf] "=&v"(tch2), [k0] "=&v"(kr[0]), [v0] "=&v"(vr[0])
    : [ka] "s"(a_ka), [bid] "s"(bid), [tid] "v"(tid) : "s8", "s9", "s10", "s11", "s12", "s13", "s14", "s15", "s16", "s17", "s28", "s29", "s30", "s31", "scc", "memory");
  asm volatile("" : "+s"(ar[0]), "+s"(ar[1]), "+s"(ar[2]), "+s"(ar[3]), "+s"(ar[4]), "+s"(ar[5]), "+s"(ar[6]), "+s"(ar[7]), "+s"(ar[8]), "+s"(ar[9]), "+s"(ar[10]), "+s"(ar[11]), "+s"(ar[12]), "+s"(ar[13]));
  asm volatile("" : "+s"(wa), "+s"(t64), "+s"(tid0));
  const u32 tka = (u32)wall_clock64();
  const u64 pt0 = t64; u64 pst = (PROBE_K == 100 || PROBE_K == 102) ? pt0 : (PROBE_K == 101 ? pt0 + (u32)(tka - (u32)t64) : 0);
  const u32 t0 = (u32)t64, te0_ = 0;
  __builtin_assume(tid >= 0 && tid < 512 && tid0 >= 0 && tid0 < 512 && bid >= 0 && bid < 256);
  constexpr Prm P = PRM0;
  const int TEND = P.TEND;
  const int xcd = bid & 7, part = bid >> 4 & 3;
  const int lane = tid & 63, wave = tid0 >> 6;
  SB();
  const bool p3 = part == 3;
  u32 pitch = (u32)(0x005600c001000020ull >> (part * 16) & 0xffff) * 32u;
  const int sh = p3 ? 2 : 3;
  u32 wv0 = (u32)((p3 ? (wave >> 1) * 128 : wave * 64) + (part == 0 ? xcd * 512 : 0) + (lane >> sh)) * pitch + ((lane & (p3 ? 3 : 7)) ^ (lane >> (p3 ? 5 : 3))) * 16u;
  const u32 wimg = (u32)(uintptr_t)(&sm.wbuf[0][0][0]);
  u32 wsz = (part == 0 ? 4096u : 512u) * pitch, wld = wimg + (p3 ? (wave & 1) * 32768 + (wave >> 1) * 8192 : wave * 8192);
  PIN(wv0);
  asm volatile("" : "+s"(wsz), "+s"(wld), "+s"(pitch));
  auto karg = [&](int i) { return ar[2 * i] | (u64)ar[2 * i + 1] << 32; };
  const u64 a_kc = karg(0), a_vc = karg(1);
  unsigned char* ws = (unsigned char*)karg(2);
  const u16 *x = (const u16*)karg(3), *cnd = (const u16*)karg(4), *w2 = (const u16*)karg(5);
  u16* out = (u16*)karg(6);
  const rsrc_t wsr = mk_rsrc(ws, WS_END);
  asm volatile("s_waitcnt vmcnt(2)" : "+v"(te));
  const u32 te0 = __builtin_amdgcn_readfirstlane(te[0]), te1 = __builtin_amdgcn_readfirstlane(te[1]);
  const int bl0 = te0 & 127, nl = te0 >> 7 & 63, blk = te0 >> 13 & 63;
  const bool has_w = te0 >> 19 & 1;
  const rsrc_t rw = mk_rsrc((const void*)wa, wsz);
  const u32 wstep = pitch << (6 - sh), wflip = p3 ? 32u : 0u;
  u32 wso = blk << (sh + 4);
  int wleft = 8;
  auto piece = [&]() {
    dma16(rw, wv0, wso, wld);
    wso += wstep; wld += 1024;
    wv0 ^= (wleft & 1) ? wflip : 0u;
    --wleft;
  };
  SB();
  STAMP(23);
  const int b = bid >> 3 & 1, head = xcd * 4 + (bid >> 6 ^ 3);
  const int g = lane >> 4, c = lane & 15;
  const int i8 = b | bid >> 6 << 1, wi = i8 * 8 + xcd;
  const bool is_up = p3 && has_w, modc = part == 2 && wi < 16;
  const size_t base = ((size_t)(b * NH + head) * SK + (SK - WIN)) * DH;
  const rsrc_t rk = mk_rsrc((const u16*)a_kc + base, WIN * DH * 2), rv = mk_rsrc((const u16*)a_vc + base, WIN * DH * 2);
  int tau = te1 & 0xffff;
  const int tauw = te1 >> 16;
  int due = P.NF * tau, now = 0;
  auto gate = [&]() {
    while (now < due) { now = (int)(((u32)wall_clock64() - t0) << 8); if (now < due) __builtin_amdgcn_s_sleep(1); }
    due += tau;
  };
  auto until = [&](int t) { while ((int)(((u32)wall_clock64() - t0) << 8) < t); };
  const u32 pslot = G_P + ((b * 32 + head) * 4 + part) * 256;
  u32x2 ep; u32 xs0, xs1, tch1; u32x4 xr0, xr1;
  const rsrc_t rx = mk_rsrc(part == 2 ? cnd : x, (part == 1 || (part == 2 && has_w)) ? 2048u : 0u);
  auto small_ops = [&]() {
    ep = ld8<AUX_SC1>(wsr, wave == 7 ? WS_EPOCH : OOB, 0);
    xs0 = __builtin_amdgcn_raw_buffer_load_b16(rx, tid * 2, 0, 0); xs1 = __builtin_amdgcn_raw_buffer_load_b16(rx, tid * 2, 1024, 0);
    xr0 = ld16<0>(rx, part == 1 ? lane * 16 : OOB, 0); xr1 = ld16<0>(rx, part == 1 ? lane * 16 : OOB, 1024);
    tch1 = __builtin_amdgcn_raw_buffer_load_b32(mk_rsrc(w2 + (size_t)blk * 32 * 512, is_up ? 64u : 0u), tid == 384 ? 0 : OOB, 0, AUX_NT);
  };
  if (has_w) {
    if (modc) now = 1 << 30;
#pragma nounroll
    while (wleft > (int)(te0 >> 20)) { gate(); due += tauw - tau; piece(); }
    now = 0;
  }
  SB();
  u32 sot;
  {
    const u32 s1 = bl0 + lane - 1, s0 = s1 + (s1 >= 108 ? 4 : 0);
    u32 sb = s0 & 96u;
#pragma unroll
    for (int k = 0; k < 5; ++k) sb |= (s0 >> k & 1u) << (P.PM >> (4 * k) & 7);
    const u32 src = (sb >> 5 & 3u) | (u32)(b * 32 + head) << 2;
    sb ^= ((__builtin_popcount(src & P.M0) & 1) | (__builtin_popcount(src & P.M1) & 1) << 1 | (__builtin_popcount(src & P.M2) & 1) << 2 | (__builtin_popcount(src & P.M3) & 1) << 3 | (__builtin_popcount(src & P.M4) & 1) << 4) & (s1 >= 96 ? 7u : 31u);
    sot = (sb & 7u) << 10 | (u32)wave << 13 | (sb >> 3) << 16;
    sot = s1 >= 120 ? (s1 + 4) << 13 | (u32)wave << 10 : sot;
    sot = lane < nl ? sot : OOB;
    PIN(sot);
  }
  u32 tag = 0;
  u32x4 bf[8];
  auto wo_prep = [&](int s0 = 0, int s1 = 2) {
    const u32 wi_ = wimg + wave * 8192;
    const int q = (lane >> 2) & 3, pp = lane & 3;
#pragma unroll
    for (int s = s0; s < s1; ++s) {
#pragma unroll
      for (int t = 0; t < 4; ++t) {
        const int r0 = s * 32 + g * 8 + q, r1 = r0 + 4, ch = t * 2 + (pp >> 1);
        const u32x2 lo = tr_read(wi_ + r0 * 128 + ((ch ^ (r0 & 7)) * 16) + (pp & 1) * 8), hi = tr_read(wi_ + r1 * 128 + ((ch ^ (r1 & 7)) * 16) + (pp & 1) * 8);
        bf[s * 4 + t] = u32x4{lo[0], lo[1], hi[0], hi[1]};
      }
    }
  };
  auto gemv_go = [&](u32 hh) {
    sm.hb[tid] = (u16)hh; sm.hb[512 + tid] = (u16)(hh >> 16);
    WAVE_SYNC();
    const u32x4* a = (c & 11) == 0 ? reinterpret_cast<const u32x4*>(&sm.hb[(c >> 2) * 512 + wave * 64 + g * 8]) : &sm.zero[0];
    const u32x4 a0 = a[0], a1 = a[(c & 11) == 0 ? 4 : 0];
    const f32x4 z = {0.f, 0.f, 0.f, 0.f};
    f32x4 d[4];
#pragma unroll
    for (int t = 0; t < 4; ++t) d[t] = mfma16(a0, bf[t], z);
#pragma unroll
    for (int t = 0; t < 4; ++t) d[t] = mfma16(a1, bf[4 + t], d[t]);
    if (lane < 32) {
#pragma unroll
      for (int t = 0; t < 4; ++t) sm.r2[(t * 16 + c) * 20 + g * 8 + wave] = d[t][0];
    }
  };
  auto mod_stage = [&]() {
    wo_prep();
    gemv_go(pack_bf(silu_f(lo_f(xs0)), silu_f(lo_f(xs1))));
    __builtin_amdgcn_s_waitcnt(0xc07f);
    if (lane == 0) *LDSP(volatile u32, (u32)(uintptr_t)&sm.mflag[wave]) = 1u;
    if (wave == 7) {
#pragma nounroll
      for (int spin = 0; spin < SPIN_MAX; ++spin) {
        const u32 f = *LDSP(volatile u32, (u32)(uintptr_t)&sm.mflag[lane & 7]);
        if (all_ok(f != 0u)) break;
        __builtin_amdgcn_s_sleep(1);
      }
      CFENCE();
      st8_sc1(wsr, G_MOD + (blk * 64 + lane) * 8, pack_bf(sum8(&sm.r2[lane * 20]), sum8(&sm.r2[lane * 20 + 8])), tag);
    }
    STAMP(9);
  };
  if (lane < 4) sm.zero[lane] = u32x4{0u, 0u, 0u, 0u};
  if (part == 2 && has_w) {
    if (tid < 8) sm.mflag[tid] = 0u;
    LDS_BARRIER();
    if (__builtin_expect(modc, 0)) {
      small_ops();
      __builtin_amdgcn_raw_buffer_store_b128(u32x4{SENT, SENT, SENT, SENT}, wsr, tid < 320 ? G_P + wi * 5120 + tid * 16 : OOB, 0, AUX_SC1);
      asm volatile("s_waitcnt vmcnt(0)" ::: "memory");
      tag = next_tag(__builtin_amdgcn_readfirstlane(ep[0]));
      mod_stage();
      __builtin_amdgcn_s_barrier();
    }
  }
  SB();
  const int fd = wi * 8 + (lane & 7);
  const int ub = wave >> 2, ud = (wave & 3) * 128 + c * 8 + g * 2;
  u32x4 sg[3]; u32 sx2;
  auto side_loads = [&]() {
    const bool fin = part == 1 && wave == 0;
    sg[0] = ld16<AUX_SC1>(wsr, is_up ? G_MOD + (1024 + ud) * 8 : (fin ? G_MOD + (1024 + fd) * 8 : OOB), 0);
    sg[1] = ld16<AUX_SC1>(wsr, is_up ? G_MOD + (1536 + ud) * 8 : (fin ? G_MOD + (2560 + fd) * 8 : OOB), 0);
    sg[2] = ld16<AUX_SC1>(wsr, is_up ? G_MOD + (2048 + ud) * 8 : OOB, 0);
    sx2 = __builtin_amdgcn_raw_buffer_load_b32(mk_rsrc(x, (is_up || fin) ? 2048u : 0u), is_up ? ub * 1024 + ud * 2 : (g & 1) * 1024 + (fd >> 1) * 4, 0, 0);
  };
  auto side_pin = [&]() { PIN(sg[0]); PIN(sg[1]); PIN(sg[2]); PIN(sx2); };
  const u32 mlo = G_ML + (head * 2 + b) * 64 + (lane & 3) * 16;
  float fmrg = 0.f;
  bool fok = wave != 0;
  auto merge_factor = [&](const u32x4& ml) {
    const float mi = u2f(ml[0]);
    float M = fmaxf(mi, dpp<XOR1>(mi)); M = fmaxf(M, dpp<XOR2>(M));
    const float e = __expf(mi - M);
    float L = e * u2f(ml[2]);
    L += dpp<XOR1>(L); L += dpp<XOR2>(L);
    fmrg = rdl(e * __builtin_amdgcn_rcpf(L), part);
  };
  f32x2 ga = {0.f, 0.f}, xin = {0.f, 0.f}, cc1 = {0.f, 0.f};
  float Af = 0.f, Bf = 0.f;
  const int hpos = ub * 512 + ud;
  auto up_prep = [&]() {
    if (!all_ok((sg[0][1] == tag) & (sg[0][3] == tag) & (sg[1][1] == tag) & (sg[1][3] == tag) & (sg[2][1] == tag) & (sg[2][3] == tag))) gather16<3>(wsr, G_MOD + (1024 + ud) * 8, 512 * 8, tag, sg);
    ga = f32x2{ub ? hi_f(sg[0][0]) : lo_f(sg[0][0]), ub ? hi_f(sg[0][2]) : lo_f(sg[0][2])};
    cc1 = f32x2{rbf(1.f + (ub ? hi_f(sg[2][0]) : lo_f(sg[2][0]))), rbf(1.f + (ub ? hi_f(sg[2][2]) : lo_f(sg[2][2])))};
    xin = f32x2{lo_f(sx2), hi_f(sx2)};
    u32* hp = reinterpret_cast<u32*>(&sm.hb[hpos]);
    hp[512] = pack_bf(cc1[0], cc1[1]);
    hp[1024] = ub ? perm_hi(sg[1][0], sg[1][2]) : perm_lo(sg[1][0], sg[1][2]);
  };
  auto up_frags = [&]() {
    const int pp = lane & 3;
    const u32 a = wimg + (pp & 1) * 32768 + ((pp >> 1) * 32 + g * 8 + ((lane >> 2) & 3)) * 64 + (((wave >> 1) ^ ((g & 1) | (pp & 2))) * 16) + (wave & 1) * 8;
#pragma unroll
    for (int rb = 0; rb < 8; ++rb) {
      const u32x2 lo = tr_read(a + rb * 4096), hi = tr_read(a + rb * 4096 + 256);
      bf[rb] = u32x4{lo[0], lo[1], hi[0], hi[1]};
    }
  };
  const u32x4* ap = reinterpret_cast<const u32x4*>(&sm.hb[((lane >> 3) & 1) * 512 + (lane & 1) * 32 + g * 8]);
  const bool par = lane & 8;
  float l0 = 0.f, l1 = 0.f;
  u32x2 gmd[2], tbg;
  int t1 = 0;
#pragma unroll
  for (int i = NCMIN / 8; i < NL; ++i) kr[i] = u32x4{0u, 0u, 0u, 0u};
  sfor<0, NL>([&](auto ic) {
    constexpr int i = ic.v;
    if (i >= NCMIN / 8 && i >= nl) return false;
    if constexpr (i == 0) return true;
    const u32 so = __builtin_amdgcn_readlane(sot, i);
    gate();
    SB();
    kr[i] = ld16<AUX_NT>(rk, lane * 16, so);
    if constexpr (i == SMO) small_ops();
    if constexpr (i == TBL) tbg = ld8<AUX_SC1>(wsr, wave == 7 ? G_TB + xcd * 128 + (lane & 7) * 8 : OOB, 0);
    if constexpr (i == QP) { gmd[0] = ld8<AUX_SC1>(wsr, part == 1 ? G_MOD + tid * 8 : OOB, 0); gmd[1] = ld8<AUX_SC1>(wsr, part == 1 ? G_MOD + tid * 8 : OOB, 4096); }
    SB();
    SB();
    if (i >= WK) { WAIT_VM(WK); SB(); }
    if (i == 1) STAMP(25);
    if (i == 8) STAMP(12);
    if (i == 20) STAMP(21);
    if (i == 25) STAMP(22);
    if (i == TBP) {
      if (part == 3 && i8 == 0 && wave == 7) { tag = next_tag(__builtin_amdgcn_readfirstlane(ep[0])); if (lane < 8) st8_sc1(wsr, G_TB + lane * 128 + xcd * 8, t0 + (((tka - t0) * P.GA) >> 3) - P.GS, tag); }
      SB();
    }
    if (i == TBU) {
      if (wave == 7) {
        const bool ok = tbg[1] == tag;
        float lf = ok ? (float)min(max((int)(t0 - tbg[0]), -300), 300) : 0.f, nf = ok ? 1.f : 0.f;
        lf += dpp<XOR1>(lf); nf += dpp<XOR1>(nf); lf += dpp<XOR2>(lf); nf += dpp<XOR2>(nf); lf += dpp<XOR7>(lf); nf += dpp<XOR7>(nf);
        if (lane == 0) sm.bc = u32x2{tag, t0 - (u32)(int)(lf * __builtin_amdgcn_rcpf(fmaxf(nf, 1.f)))};
      }
      LDS_BARRIER();
      const u32x2 bc = sm.bc;
      tag = __builtin_amdgcn_readfirstlane(bc[0]);
      t1 = (__builtin_amdgcn_readfirstlane((int)(bc[1] - t0)) + TEND + P.X1) * 256;
      tau -= __builtin_amdgcn_readfirstlane((int)((float)(int)(t0 - bc[1]) * 256.f * __builtin_amdgcn_rcpf((float)(2 * nl - TBU - 3))));
      SB();
    }
    if (i == LNS) {
      tag = next_tag(__builtin_amdgcn_readfirstlane(ep[0]));
      if (part == 1) {
        float x0[8], x1[8], mb0, ri0, mb1, ri1;
#pragma unroll
        for (int k = 0; k < 4; ++k) { x0[2 * k] = lo_f(xr0[k]); x0[2 * k + 1] = hi_f(xr0[k]); x1[2 * k] = lo_f(xr1[k]); x1[2 * k + 1] = hi_f(xr1[k]); }
        ln_stats2(x0, x1, mb0, ri0, mb1, ri1);
        l0 = rbf(rbf(lo_f(xs0) - mb0) * ri0); l1 = rbf(rbf(lo_f(xs1) - mb1) * ri1);
      }
      PIN(l0); PIN(l1);
      SB();
    }
    if (i == WUSE) {
      PIN(tch1); PIN(tch2);
      if ((part == 1 || (part == 2 && has_w && !modc))) {
        WAIT_VM(WK); CFENCE();
        if (part == 2) mod_stage(); else wo_prep();
      }
#pragma unroll
      for (int k = 0; k < 8; ++k) PIN(bf[k]);
      SB();
    }
    if (i == QP + QD) {
      if (part == 1) {
        u32 gm[2] = {gmd[0][0], gmd[1][0]};
        if (!all_ok((gmd[0][1] == tag) & (gmd[1][1] == tag))) { STAMP(11); gather8<2>(wsr, G_MOD + tid * 8, 512 * 8, tag, gm); }
        STAMP(5);
        gemv_go(pack_bf(rbf(l0 * rbf(1.f + lo_f(gm[1]))) + lo_f(gm[0]), rbf(l1 * rbf(1.f + hi_f(gm[1]))) + hi_f(gm[0])));
      }
      SB();
    }
    if (i == QP + QD + 1) {
      if (part == 1) {
        LDS_BARRIER();
        if (wave == 0) {
          const int bb = lane >> 5, jq = lane & 31;
          st8_sc1(wsr, G_Q + (bb * 2048 + blk * 32 + jq) * 8, pack_bf(sum8(&sm.r2[2 * jq * 20 + bb * 8]), sum8(&sm.r2[(2 * jq + 1) * 20 + bb * 8])), tag);
        }
        STAMP(6);
      }
      SB();
    }
    return true;
  });
  PIN(sot);
  const u32 qoff = G_Q + ((b * 32 + head) * 64 + c * 4) * 8;
  u32x4 qg[2], fl2[3];
  u32 qq[4];
  constexpr int NB = (NL + 15) / 16;
  float xlo, sc[NB], pr[NB], m = 0.f;
  f32x4 acc = {0.f, 0.f, 0.f, 0.f};
  u32 pb[NB], pm[4], cpf;
  const u32 adst0 = lds_addr(lane < 32 ? &sm.red[wave][c * 8 + g * 4] : &sm.r2[(lane - 32) * 4]);
  u32 po, rdst, rsrc, xo;
  auto wo_addr = [&]() {
    po = G_P + (((c >> 2 & 1) * 32 + xcd * 4 + (wave >> 1)) * 4 + (c & 3)) * 256 + (wave & 1) * 128 + (c >> 3) * 64 + g * 16;
    rdst = lane < 32 ? (u32)(uintptr_t)&sm.red2[wave][0] + lane * 16 : (u32)(uintptr_t)&sm.r2[0] + (lane - 32) * 16;
    rsrc = (u32)(uintptr_t)&sm.red2[0][((wave & 1) * 16 + (lane & 15)) * 4 + (lane >> 4)];
    xo = (lane & 1) ? OOB : G_XP + xcd * 2048 + (wave & 1) * 1024 + (blk * 64 + lane) * 2;
    PIN(po); PIN(rdst); PIN(rsrc); PIN(xo);
  };
  u32 pso = wave == 0 ? pslot + lane * 4 : OOB;
  PIN(pso);
  const u32 nadd = (te0 >> 20) ? P.LW : (is_up ? P.LU : 0);
  u32 nto = tid == 0 ? G_NT + bid * 8 : OOB;
  PIN(nto);
  auto notices = [&](auto nc_, u32 off, u32x4 (&gn)[nc_.v]) -> int {
    constexpr int N = nc_.v;
#pragma nounroll
    for (int spin = 0; spin < SPIN_MAX; ++spin) {
      bool ok = true;
#pragma unroll
      for (int i = 0; i < N; ++i) ok &= (gn[i][1] == tag) & (gn[i][3] == tag);
      if (all_ok(ok)) break;
      __builtin_amdgcn_s_sleep(10);
      CFENCE();
#pragma unroll
      for (int i = 0; i < N; ++i) gn[i] = ld16<AUX_SC1>(wsr, off, i * 1024);
    }
    int mx = 0;
#pragma unroll
    for (int i = 0; i < N; ++i) mx = max(mx, max((int)(gn[i][0] - t0), (int)(gn[i][2] - t0)));
    return __builtin_amdgcn_readfirstlane((int)wave_max((float)min(mx, 1 << 20)));
  };
  const int side = nl >= SIDE + 13 ? SIDE + 8 : (nl >= SIDE + 9 ? SIDE + 4 : SIDE);
  sfor<0, NL + WV>([&](auto ic) {
    constexpr int v = ic.v;
    if (cons_end(v - 1) >= NCMIN / 8 && cons_end(v - 1) >= nl) return false;
    if constexpr (v > 0 && v < NL) {
      const u32 so = __builtin_amdgcn_readlane(sot, v);
      if constexpr (v >= 12) { if (v == nl - P.NE) tau = (tau * P.FE) >> 4; }
      if constexpr (v >= NCMIN / 8) { if (v < nl) gate(); } else gate();
      SB();
      vr[v] = ld16<AUX_NT>(rv, lane * 16, so);
      SB();
      SB();
    }
    if constexpr (v == NCMIN / 8 - 1) {
#pragma unroll
      for (int i = NCMIN / 8; i < NL; ++i) asm volatile("" : "=v"(vr[i]));
      asm volatile("" : "=v"(fl2[1]), "=v"(fl2[2]));
    }
    if constexpr (v == VQ - 4) { qg[0] = ld16<AUX_SC1>(wsr, qoff, 0); qg[1] = ld16<AUX_SC1>(wsr, qoff + 16, 0); }
    if constexpr (v == VSIDE) side_loads();
    if constexpr (v == CPF) {
      u64 pc; u32 dlt, len;
      asm volatile("s_getpc_b64 %0\n.Lcpf_here:\n\ts_mov_b32 %1, .Ltail_start-.Lcpf_here\n\ts_mov_b32 %2, .Lend_a-.Ltail_start\n\ts_max_i32 %2, %2, .Lend_b-.Ltail_start\n\ts_max_i32 %2, %2, .Lend_c-.Ltail_start\n\ts_max_i32 %2, %2, .Lend_d-.Ltail_start\n\ts_max_i32 %2, %2, .Lend_e-.Ltail_start" : "=s"(pc), "=s"(dlt), "=s"(len) :: "scc");
      pc += dlt;
      const rsrc_t rc = mk_rsrc((const void*)pc, (part == 3 && i8 == 1) ? len : 0u);
      cpf = __builtin_amdgcn_raw_buffer_load_b32(rc, tid * 64, 0, 0);
    }
    if constexpr (v == CPF + WV + 1) PIN(cpf);
    if constexpr (v == SIDE || v == SIDE + 4 || v == SIDE + 8) fl2[(v - SIDE) / 4] = ld16<AUX_SC1>(wsr, (wave == 0 && v == side) ? mlo : OOB, 0);
    SB();
    if constexpr (v < WV) { WAIT_VM(WK); SB(); } else if constexpr (v < NL && cons_end(v) < v - WV + 1) { WAIT_VM(WV); SB(); }
    if constexpr (v == VQ) {
      if (!all_ok((qg[0][1] == tag) & (qg[0][3] == tag) & (qg[1][1] == tag) & (qg[1][3] == tag))) { STAMP(20); gather16<2>(wsr, qoff, 16, tag, qg); }
      STAMP(1); WSTAMP(1);
      qq[0] = qg[0][0]; qq[1] = qg[0][2]; qq[2] = qg[1][0]; qq[3] = qg[1][2];
    }
    if constexpr (v >= VQ && v < VS) {
      constexpr int sl = v - VQ, bt = sl >> 1;
      float xs[8];
#pragma unroll
      for (int i = 0; i < 8; ++i) {
        float d = 0.f;
        if (sl * 8 + i < NL) {
#pragma unroll
          for (int k = 0; k < 4; ++k) d = dot2(kr[sl * 8 + i][k], qq[k], d);
        }
        xs[i] = d;
      }
#pragma unroll
      for (int h = 0; h < 4; ++h) xs[h] = sel_add<XOR7>(xs[h], xs[h + 4], c & 4);
#pragma unroll
      for (int h = 0; h < 2; ++h) xs[h] = sel_add<XOR2>(xs[h], xs[h + 2], c & 2);
      xs[0] = sel_add<XOR1>(xs[0], xs[1], c & 1);
      if constexpr ((sl & 1) || sl == NSL - 1) {
        const float t = (sl & 1) ? sel_add<XOR8>(xlo, xs[0], c & 8) : xs[0] + dpp<XOR8>(xs[0]);
        const float s = rbf(rbf(t) * 0.08838834764831845f);
        sc[bt] = (bt * 16 + c < nl) ? s : -__builtin_inff();
        PIN(sc[bt]);
      } else {
        xlo = xs[0];
        PIN(xlo);
      }
    }
    if constexpr (v == VS) {
      float mloc = sc[0];
#pragma unroll
      for (int bt = 1; bt < NB; ++bt) mloc = fmaxf(mloc, sc[bt]);
      m = fmaxf(wave_max(mloc), -3.0e38f);
      float ls = 0.f;
#pragma unroll
      for (int bt = 0; bt < NB; ++bt) { pr[bt] = __expf(sc[bt] - m); ls += pr[bt]; }
      const float l = wave_sum(ls);
      if (lane == 0) { sm.ml[wave][0] = m; sm.ml[wave][1] = l; }
      STAMP(2); WSTAMP(2);
    }
    sfor<cons_end(v - 1), cons_end(v)>([&](auto jc) {
      constexpr int i = jc.v;
      if (i < NCMIN / 8 || i < nl) {
        const u32 pv = __float_as_uint(bcast16(u2f(pb[i >> 4]), i & 15));
        acc = mfma16(u32x4{pv & pm[0], pv & pm[1], pv & pm[2], pv & pm[3]}, vr[i], acc);
      }
      PIN(acc);
      return true;
    });
    if constexpr (v == MLS - 1) { side_pin(); if (is_up) up_prep(); PIN(ga); PIN(xin); PIN(cc1); }
    if constexpr (v == UPREP - 1) wo_addr();
    if constexpr (v == UPREP) {
#pragma unroll
      for (int k = 0; k < 8; ++k) asm volatile("" : "=v"(bf[k]));
      if (part == 0) wo_prep(0, 1); else if (is_up) up_frags();
#pragma unroll
      for (int k = 0; k < 8; ++k) PIN(bf[k]);
    }
    if constexpr (v == UPREP + 1) {
      if (is_up) {
        const u32x4* a1 = ap + 128 + ((lane >> 1) & 1) * 128;
        f32x4 d0 = {0.f, 0.f, 0.f, 0.f}, d1 = {0.f, 0.f, 0.f, 0.f};
#pragma unroll
        for (int rb = 0; rb < 4; ++rb) { d0 = mfma16(a1[rb * 8], bf[rb], d0); d1 = mfma16(a1[(rb + 4) * 8], bf[rb + 4], d1); }
        Af = par ? d0[1] + d1[1] : d0[0] + d1[0]; Bf = par ? d0[3] + d1[3] : d0[2] + d1[2];
        Af += dpp<XOR8>(Af); Bf += dpp<XOR8>(Bf);
      }
      PIN(Af); PIN(Bf);
    }
    if constexpr (v == SIDE + WV + 1 || v == SIDE + WV + 5 || v == SIDE + WV + 9) {
      if (wave == 0 && v == side + WV + 1) {
        const u32x4 ml = fl2[(v - SIDE - WV - 1) / 4];
        fok = all_ok((ml[1] == tag) & (ml[3] == tag));
        merge_factor(ml);
      }
    }
    if (v % 6 == 4 && v >= 10 && v < 30) STAMP(15 + v / 6);
    if constexpr (v == MLS) {
      LDS_BARRIER();
      float mm[8], ll[8];
#pragma unroll
      for (int w = 0; w < 8; ++w) { const f32x2 t = *reinterpret_cast<const f32x2*>(&sm.ml[w][0]); mm[w] = t[0]; ll[w] = t[1]; }
      float Mwg = mm[0], Lwg = 0.f;
#pragma unroll
      for (int w = 1; w < 8; ++w) Mwg = fmaxf(Mwg, mm[w]);
      if (wave == 0) {
#pragma unroll
        for (int w = 0; w < 8; ++w) Lwg = fmaf(__expf(mm[w] - Mwg), ll[w], Lwg);
        if (lane == 0) st16_sc1(wsr, G_ML + ((head * 2 + b) * 4 + part) * 16, __float_as_uint(Mwg), __float_as_uint(Lwg), tag);
      }
      const float fw = __expf(m - Mwg);
#pragma unroll
      for (int bt = 0; bt < NB; ++bt) { pb[bt] = pack_bf(pr[bt] * fw, pr[bt] * fw); PIN(pb[bt]); }
#pragma unroll
      for (int k = 0; k < 4; ++k) { pm[k] = (c >> 1) == k ? ((c & 1) ? 0xffff0000u : 0x0000ffffu) : 0u; PIN(pm[k]); }
    }
    SB();
    return true;
  });
  asm volatile(".Ltail_start:");
  *LDSP(f32x4, adst0) = acc;
  STAMP(14); WSTAMP(14);
  if (!fok) {
    u32x4 ml;
#pragma nounroll
    for (int spin = 0; spin < SPIN_MAX; ++spin) {
      CFENCE();
      ml = ld16<AUX_SC1>(wsr, mlo, 0);
      if (all_ok((ml[1] == tag) & (ml[3] == tag))) break;
    }
    merge_factor(ml);
  }
  STAMP(15);
  LDS_BARRIER();
  STAMP(3);
  {
    u32 pw = 0;
    if (wave == 0) {
      f32x2 t[8];
#pragma unroll
      for (int w = 0; w < 8; ++w) t[w] = *reinterpret_cast<const f32x2*>(&sm.red[w][2 * lane]);
      const f32x2 o = ((t[0] + t[1]) + (t[2] + t[3])) + ((t[4] + t[5]) + (t[6] + t[7]));
      pw = min(pack_bf(o[0] * fmrg, o[1] * fmrg), SENT - 1u);
    }
    __builtin_amdgcn_raw_buffer_store_b32(pw, wsr, pso, 0, AUX_SC1);
    st8_sc1(wsr, nto, (u32)wall_clock64() + nadd, tag);
    SB();
#pragma unroll
    for (int i = NCMIN / 8; i < NL; ++i) PIN(vr[i]);
    PIN(fl2[0]); PIN(fl2[1]); PIN(fl2[2]);
  }
  STAMP(4);

  if (part == 2) FINISH();
  if (part == 0) {
    __builtin_amdgcn_s_barrier();
#pragma nounroll
    while (wleft > 0) piece();
    until(t1);
    STAMP(11);
    CFENCE();
    u32x4 og[2];
    og[0] = ld16<AUX_SC1>(wsr, po, 0);
    SB();
    WAIT_VM(1); CFENCE();
    wo_prep(1, 2);
#pragma unroll
    for (int k = 4; k < 8; ++k) PIN(bf[k]);
    SB();
    f32x4 d[4];
    auto wo_mm = [&]() {
      const f32x4 z = {0.f, 0.f, 0.f, 0.f};
#pragma unroll
      for (int t = 0; t < 4; ++t) { d[t] = mfma16(og[0], bf[t], z); og[1][t] = __float_as_uint(dpp<XOR8>(u2f(og[0][t]))); }
      SB();
#pragma unroll
      for (int t = 0; t < 4; ++t) d[t] = mfma16(og[1], bf[4 + t], d[t]);
    };
    auto p_bad = [&]() { return !all_ok(max(max(og[0][0], og[0][1]), max(og[0][2], og[0][3])) != SENT); };
    wo_mm();
    if (__builtin_expect(p_bad(), 0)) {
#pragma nounroll
      for (int spin = 0; spin < SPIN_MAX; ++spin) {
        __builtin_amdgcn_s_sleep(1);
        CFENCE();
        og[0] = ld16<AUX_SC1>(wsr, po, 0);
        if (!p_bad()) break;
      }
      wo_mm();
      SB();
      asm volatile(".Lend_d:");
    }
    STAMP(5); WSTAMP(5);
    {
      SB();
      f32x4 y; f32x2 h[4];
#pragma unroll
      for (int t = 0; t < 4; ++t) { h[t] = d[t].xy + d[t].zw; PIN(h[t]); }
#pragma unroll
      for (int t = 0; t < 4; ++t) { y[t] = h[t][0] + h[t][1]; PIN(y[t]); }
      *LDSP(f32x4, rdst) = y;
    }
    STAMP(16);
    LDS_BARRIER();
    STAMP(17);
    if (wave < 2) {
      f32x2 v[4];
#pragma unroll
      for (int w = 0; w < 4; ++w) v[w] = f32x2{LDSP(float, rsrc)[w * 256], LDSP(float, rsrc)[w * 256 + 128]};
      const f32x2 h = (v[0] + v[1]) + (v[2] + v[3]);
      const float y = h[0] + h[1];
      __builtin_amdgcn_raw_buffer_store_b32(min(pack_bf(y, dpp<XOR1>(y)), SENT - 1u), wsr, xo, 0, AUX_SC1);
      asm volatile(".Lend_a:");
    }
    STAMP(6);
    FINISH();
  }
  if (part == 3) {
    if (!has_w) FINISH();
    u32x4 b2[4];
    {
      __builtin_amdgcn_s_barrier();
      const rsrc_t r2 = mk_rsrc(w2 + (size_t)(blk * 32 + wave * 4) * 512, 4096u);
      const u32 l2 = (u32)(uintptr_t)(&sm.w2b[wave * 4][0]);
#pragma unroll
      for (int n = 0; n < 4; ++n) dma16(r2, (lane ^ (n * 2) ^ ((wave & 2) << 2)) * 16, n * 1024, l2 + n * 1024);
    }
    const u32 tdst = lds_addr(&sm.hb[hpos]);
    const u32 sdst = lds_addr(&sm.stat[wave][lane]);
    const u32 ssrc = lds_addr(&sm.stat[(g >> 1) * 4 + ((lane >> 1) & 3)][(lane & 1) * 16]);
    const u32 adst = lds_addr((c < 4 && (g & 1) == 0) ? &sm.actl[g >> 1][wave * 4 + c] : reinterpret_cast<u16*>(&sm.dummy[lane]));
    const u32 asrc = lds_addr(&sm.actl[lane & 1][g * 8]);
    const u32 apa = lds_addr(&sm.hb[(c >> 3) * 512 + (c & 3) * 64 + ((c >> 2) & 1) * 32 + g * 8]);
    u32 xoff = G_XP + g * 2048 + ub * 1024 + (wave & 3) * 256 + c * 16;
    u32x4 one;
#pragma unroll
    for (int k = 0; k < 4; ++k) one[k] = ((c & 2) == 0 && (c >> 2) == k) ? ((c & 1) ? 0x3f800000u : 0x00003f80u) : 0u;
    PIN(one);
    u32 ooff = G_OP + (blk * 512 + tid) * 8;
    float Afp = Af, Bfp = Bf;
    PIN(xoff); PIN(ooff); PIN(Afp); PIN(Bfp);
    PIN(ga); PIN(xin); PIN(cc1);
    u32 w2a = (u32)(uintptr_t)(&sm.w2b[0][0]) + (g * 8 + ((lane >> 2) & 3)) * 1024 + (wave ^ (g & 1)) * 128 + (lane & 15) * 8;
    PIN(w2a);
    if (wave == 1) {
      due = t1 + P.N1 * 256; gate();
      u32x4 gn[2] = {ld16<AUX_SC1>(wsr, G_NT + lane * 16, 0), ld16<AUX_SC1>(wsr, G_NT + lane * 16, 1024)};
      const int tn = notices(IC<2>{}, G_NT + lane * 16, gn) * 256;
      due = (tn > t1 + 2 * 256 ? max(tn, t1 + P.L0 * 256) + P.L1 * 256 : t1) + P.X2 * 256;
      until(due);
    }
    asm volatile("s_waitcnt vmcnt(0)" ::: "memory");
    STAMP(9);
    __builtin_amdgcn_s_barrier();
    STAMP(11);
    CFENCE();
    u32x4 xg[2] = {ld16<AUX_SC1>(wsr, xoff, 0), ld16<AUX_SC1>(wsr, xoff, 8192)};
    SB();
#pragma unroll
    for (int t = 0; t < 4; ++t) {
      const u32x2 lo = tr_read(w2a ^ (t * 32)), hi = tr_read((w2a ^ (t * 32)) + 4096);
      b2[t] = u32x4{lo[0], lo[1], hi[0], hi[1]};
    }
#pragma unroll
    for (int t = 0; t < 4; ++t) PIN(b2[t]);
    SB();
    const f32x4 z4 = {0.f, 0.f, 0.f, 0.f};
    auto xp_bad = [&]() { return !all_ok(max(max(max(xg[0][0], xg[0][1]), max(xg[0][2], xg[0][3])), max(max(xg[1][0], xg[1][1]), max(xg[1][2], xg[1][3]))) != SENT); };
    f32x4 v = mfma16(one, xg[1], mfma16(one, xg[0], z4));
    if (__builtin_expect(xp_bad(), 0)) {
#pragma nounroll
      for (int spin = 0; spin < SPIN_MAX; ++spin) {
        __builtin_amdgcn_s_sleep(1);
        CFENCE();
        xg[0] = ld16<AUX_SC1>(wsr, xoff, 0); xg[1] = ld16<AUX_SC1>(wsr, xoff, 8192);
        if (!xp_bad()) break;
      }
      v = mfma16(one, xg[1], mfma16(one, xg[0], z4));
      SB();
      asm volatile(".Lend_e:");
    }
    STAMP(5); WSTAMP(5);
    {
      const float a0 = v[0], a1 = v[1];
      const float x10 = fmaf(ga[0], a0, xin[0]), x11 = fmaf(ga[1], a1, xin[1]);
      *LDSP(u32, tdst) = pack_bf(x10 * cc1[0], x11 * cc1[1]);
      float u = swap32_add(x10 + x11, fmaf(x10, x10, x11 * x11));
      u += dpp<XOR1>(u); u += dpp<XOR2>(u); u += dpp<XOR7>(u); u += dpp<XOR8>(u);
      *LDSP(float, sdst) = u;
    }
    STAMP(16);
    LDS_BARRIER();
    STAMP(17);
    {
      const f32x4 z = {0.f, 0.f, 0.f, 0.f};
      const u32x4 af0 = LDSP(u32x4, apa)[0], af1 = LDSP(u32x4, apa)[32];
      float s = LDSP(float, ssrc)[0], q = LDSP(float, ssrc)[32];
      SB();
      f32x4 d[4];
#pragma unroll
      for (int j = 0; j < 4; ++j) d[j] = mfma16(af0, bf[j], z);
#pragma unroll
      for (int j = 0; j < 4; ++j) d[j] = mfma16(af1, bf[4 + j], d[j]);
      s += dpp<XOR1>(s); q += dpp<XOR1>(q); s += dpp<XOR2>(s); q += dpp<XOR2>(q); s += dpp<XOR7>(s); q += dpp<XOR7>(q);
      const float mean = s * (1.f / 512.f);
      const float var = fmaf(q, 1.f / 512.f, -mean * mean);
      const float ri = __builtin_amdgcn_rsqf(fmaxf(var, 0.f) + 1e-5f);
      const float cf = fmaf(-ri * mean, Afp, Bfp);
      float y = (d[0][0] + d[1][1]) + (d[2][2] + d[3][3]);
      y = __builtin_bit_cast(float, __builtin_amdgcn_update_dpp(__builtin_bit_cast(int, y), __builtin_bit_cast(int, y), XOR8, 0xa, 0xf, false));
      y = swap16_add(y, y);
      y = fmaf(y, ri, cf);
      const float up = dpp<0x104>(y);
      *LDSP(u16, adst) = (u16)pack_bf(silu_f(y) * up, 0.f);
    }
    STAMP(18);
    LDS_BARRIER();
    STAMP(10);
    {
      const u32x4 af = *LDSP(u32x4, asrc);
      const f32x4 z = {0.f, 0.f, 0.f, 0.f};
      const f32x4 d0 = mfma16(af, b2[0], z), d1 = mfma16(af, b2[1], z), d2 = mfma16(af, b2[2], z), d3 = mfma16(af, b2[3], z);
      const bool g1 = g & 1, g2 = g & 2;
      const float e0 = g1 ? d1[0] : d0[0], e1 = g1 ? d1[1] : d0[1], f0 = g1 ? d3[0] : d2[0], f1 = g1 ? d3[1] : d2[1];
      st8_sc1(wsr, ooff, pack_bf(g2 ? f0 : e0, g2 ? f1 : e1), tag);
    }
    asm volatile(".Lend_b:");
    STAMP(6);
    FINISH();
  }
  if (wave == 0) {
    const int d = fd, ul = lane >> 3;
    u32 gm[2] = {sg[0][0], sg[1][0]};
    if (!all_ok((sg[0][1] == tag) & (sg[1][1] == tag))) gather8<2>(wsr, G_MOD + (1024 + d) * 8, 1536 * 8, tag, gm);
    u32 xa0, xa1;
    {
      due = t1 + P.N2 * 256; gate();
      u32x4 gn[2] = {ld16<AUX_SC1>(wsr, G_NT + lane * 16, 0), ld16<AUX_SC1>(wsr, G_NT + lane * 16, 1024)};
      const int tn = notices(IC<2>{}, G_NT + lane * 16, gn) * 256;
      due = (tn > t1 + 2 * 256 ? max(tn, t1 + P.L0 * 256) + P.L1 * 256 : t1) + P.X3 * 256;
      until(due);
    }
#pragma nounroll
    for (int spin = 0; spin < SPIN_MAX; ++spin) {
      CFENCE();
      xa0 = __builtin_amdgcn_raw_buffer_load_b32(wsr, G_XP + ul * 2048 + (d >> 1) * 4, 0, AUX_SC1);
      xa1 = __builtin_amdgcn_raw_buffer_load_b32(wsr, G_XP + ul * 2048 + (d >> 1) * 4, 1024, AUX_SC1);
      if (all_ok(max(xa0, xa1) != SENT)) break;
      __builtin_amdgcn_s_sleep(1);
    }
    float a0 = (d & 1) ? hi_f(xa0) : lo_f(xa0), a1 = (d & 1) ? hi_f(xa1) : lo_f(xa1);
    a0 += dpp<XOR8>(a0); a1 += dpp<XOR8>(a1);
    a0 = swap16_add(a0, a0); a1 = swap16_add(a1, a1);
    a0 = swap32_add(a0, a0); a1 = swap32_add(a1, a1);
    const bool hb = g & 1;
    float xv = rbf(((d & 1) ? hi_f(sx2) : lo_f(sx2)) + rbf((hb ? hi_f(gm[0]) : lo_f(gm[0])) * rbf(hb ? a1 : a0)));
    float gv = hb ? hi_f(gm[1]) : lo_f(gm[1]);
    PIN(xv); PIN(gv);
    if (wi == 0 && lane == 0) __builtin_amdgcn_raw_buffer_store_b32(tag, wsr, WS_EPOCH, 0, AUX_SC1);
    STAMP(10);
    due = max(due + P.X4 * 256, (int)(((u32)wall_clock64() - t0) << 8) + P.GX * 256);
    const bool last = ul + 40 < NUP;
    u32 o0 = G_OP + (ul * 512 + d) * 8, o5 = last ? o0 : OOB, tg5 = last ? tag : 0u;
    const rsrc_t rout = mk_rsrc(out, 2048u);
    u32 oo = (lane & 40) == 0 ? (g * 512 + d) * 2 : OOB;
    const u32 pk = c == 0 ? 0x00003f80u : (c == 4 ? 0x3f800000u : 0u);
    u32x4 one = {pk, 0u, pk, 0u};
    PIN(o0); PIN(o5); PIN(tg5); PIN(oo); PIN(one);
    __builtin_amdgcn_raw_buffer_store_b16((u16)pack_bf(xv, 0.f), rout, oo, 0, AUX_SC1);
    until(due);
    f32x4 sv;
#pragma nounroll
    for (int spin = 0; spin < SPIN_MAX; ++spin) {
      CFENCE();
      u32x2 og[6];
#pragma unroll
      for (int k = 0; k < 5; ++k) og[k] = ld8<AUX_SC1>(wsr, o0, k * 32768);
      og[5] = ld8<AUX_SC1>(wsr, o5, 5 * 32768);
      sv = f32x4{0.f, 0.f, 0.f, 0.f};
#pragma unroll
      for (int k = 0; k < 3; ++k) sv = mfma16(one, u32x4{og[2 * k][0], og[2 * k][1], og[2 * k + 1][0], og[2 * k + 1][1]}, sv);
      const u32 bad = ((og[0][1] ^ tag) | (og[1][1] ^ tag) | (og[2][1] ^ tag)) | ((og[3][1] ^ tag) | (og[4][1] ^ tag) | (og[5][1] ^ tg5));
      PIN(sv);
      if (all_ok(bad == 0u)) break;
      __builtin_amdgcn_s_sleep(1);
    }
    STAMP(7);
    const float s = sv[0] + dpp<XOR8>(sv[0]);
    __builtin_amdgcn_raw_buffer_store_b16((u16)pack_bf(fmaf(gv, s, xv), 0.f), rout, oo, 0, AUX_SC1);
    asm volatile(".Lend_c:");
  }
  FINISH();
}

extern "C" void kernel_launch(void* const* d_in, const int* in_sizes, int n_in,
                              void* d_out, int out_size, void* d_ws, size_t ws_size,
                              hipStream_t stream) {
  fused_kernel<<<256, 512, 0, stream>>>((const u16*)d_in[1], (const u16*)d_in[2], (const u16*)d_in[4], (const u16*)d_in[3], (const u16*)d_in[10], (const u16*)d_in[6], (const u16*)d_in[7],
                                        (unsigned char*)d_ws, (const u16*)d_in[0], (const u16*)d_in[9], (const u16*)d_in[8], (u16*)d_out
    );
}
```

```cpp
#include <hip/hip_runtime.h>
#include <stdint.h>

typedef unsigned int u32;
typedef unsigned short u16;
typedef unsigned long long u64;
typedef __attribute__((ext_vector_type(4))) unsigned u32x4;
typedef __attribute__((ext_vector_type(2))) unsigned u32x2;
typedef __attribute__((ext_vector_type(2))) __bf16 bf16x2;
typedef __attribute__((ext_vector_type(4))) float f32x4;
typedef __attribute__((ext_vector_type(2))) float f32x2;
typedef __amdgpu_buffer_rsrc_t rsrc_t;

constexpr int NH = 32, SK = 16384, DH = 128, WIN = 4096, FF = 1376;
struct Prm { int E0, ES, E1, E2, E2C, EU, TEND, DT_UP, L0, L1, X1, X2, X3, X4, NE, FE, M0, M1, M2, M3, M4, GS, GX, N1, N2, GA, WD, DT_WO, LW, LU, EW, PM, NF; };
constexpr Prm PRM0 = {32, 24, 32, 32, 56, 64, 2010, 60, 30, 25, 92, 86, 131, 88, 4, 12, 5, 8, 21, 4, 32, 30, 56, 28, 70, 12, 4, 100, 45, 15, 8, 0x24310, 2};
#define PRM_ARG
constexpr int NL = 37;
constexpr int NCMIN = 192;
constexpr int WK = 8, WV = 5;
constexpr int WUSE = 12, LNS = 7;
constexpr int QP = 14, QD = 4;
constexpr int VQ = 6;
constexpr int NSL = (NL + 7) / 8;
constexpr int VS = VQ + NSL;
constexpr int MLS = VS + 2;
constexpr int VSIDE = MLS - 5, UPREP = MLS + 8;
constexpr int SIDE = NCMIN / 8 - 4;
constexpr int CPF = 18;
constexpr int SMO = 1;
constexpr int TBP = 4, TBL = 11, TBU = 15;
constexpr int cons_end(int v) {
  const int a = v - WV + 1, c = (v - MLS) * 4;
  const int m = a < c ? a : c;
  return m < 0 ? 0 : (m > NL ? NL : m);
}
struct Tab { u32 e[512]; };
constexpr Tab make_tab(const Prm& P) {
  Tab T{};
  for (int bid = 0; bid < 256; ++bid) {
    const int xcd = bid & 7, part = bid >> 4 & 3, i8 = (bid >> 3 & 1) | bid >> 6 << 1, wi = i8 * 8 + xcd;
    const bool has_mod = wi < 48, has_up = wi >= 21;
    const int cb0 = (i8 + 5) & 7, cb1 = (i8 + 4) & 7, ms = wi - 20;
    const int mi = wi < 8 ? wi : (wi < 16 ? 8 + ((xcd + 4) & 7) : (wi < 20 ? (wi - 14) * 8 + 3 : 16 + ms / 7 * 8 + ms % 7 + (ms % 7 >= 3 ? 1 : 0)));
    const int ui = xcd * 5 + (xcd > 5 ? xcd - 5 : 0) + (i8 >= 3 ? (xcd >= 5 ? 1 : 0) + i8 - 3 : 0);
    const int wd = cb0 == 3 ? 0 : P.WD;
    const int e[4] = {P.E0 - 4 * wd + (cb0 == 3 ? P.ES : 0), P.E1 + (cb1 == 3 ? P.ES : 0), has_mod ? (wi < 16 ? P.E2C : P.E2) + ((mi & 7) == 3 ? P.ES : 0) : 0, has_up ? P.EU : 0};
    const int ew = wd ? P.EW : 0, pa = (1024 + e[0] + ew + e[1] + e[2] + e[3]) / 4;
    int c[5] = {0, 0, 0, 0, 124};
    for (int k = 0; k < 3; ++k) c[k + 1] = c[k] + (pa - e[k] - (k == 0 ? ew : 0) + 4) / 8 - 1;
    const int nl = c[part + 1] - c[part] + 1, wst = e[part] / 4;
    const bool has_w = part < 2 || (part == 2 ? has_mod : has_up);
    const int blk = !has_w ? 0 : (part == 0 ? cb0 : (part == 1 ? xcd * 8 + cb1 : (part == 2 ? mi : ui)));
    const int tau = (part == 3 && has_up ? P.TEND - P.DT_UP : (part == 0 && wd ? P.TEND - P.DT_WO : P.TEND)) * 256 / (2 * nl + wst);
    T.e[2 * bid] = (u32)(c[part] | nl << 7 | blk << 13 | (has_w ? 1 : 0) << 19 | (part == 0 ? wd : 0) << 20);
    T.e[2 * bid + 1] = (u32)(tau | (tau * wst / (part == 0 ? 8 - wd : 8)) << 16);
  }
  return T;
}
constexpr int step_range(const Prm& P, bool mx_) {
  int mx = 0, mn = 1 << 20;
  const Tab T = make_tab(P);
  for (int bid = 0; bid < 256; ++bid) { const int n = T.e[2 * bid] >> 7 & 63; mx = n > mx ? n : mx; mn = n < mn ? n : mn; }
  return mx_ ? mx : mn;
}
extern "C" __attribute__((used, visibility("protected"))) __constant__ const Tab wg_tab = make_tab(PRM0);
static_assert(step_range(PRM0, true) <= NL && step_range(PRM0, false) >= NCMIN / 8, "a wave's key range does not fit its load steps");
static_assert(cons_end(SIDE + WV + 1) <= NCMIN / 8 && cons_end(UPREP) <= NCMIN / 8 && SIDE > MLS + 3, "steps every wave runs");
constexpr int NUP = FF / 32;
constexpr int SPIN_MAX = 1 << 17;

constexpr u32 WS_EPOCH = 0;
constexpr u32 G_TB = 1024;
constexpr u32 G_NT = 2048;
constexpr u32 G_MOD = 4096;
constexpr u32 G_Q = G_MOD + 3072 * 8;
constexpr u32 G_ML = G_Q + 4096 * 8;
constexpr u32 G_P = G_ML + 256 * 16;
constexpr u32 G_XP = G_P + 256 * 256;
constexpr u32 G_OP = G_XP + 8 * 512 * 4;
constexpr u32 WS_END = G_OP + 43 * 512 * 8;
constexpr u32 SENT = 0xffffffffu;
constexpr u32 OOB = 0x40000000u;
#define WST 0

constexpr int AUX_NT = 2, AUX_SC1 = 16;

__device__ __forceinline__ float lo_f(u32 v) { return __uint_as_float(v << 16); }
__device__ __forceinline__ float hi_f(u32 v) { return __uint_as_float(v & 0xffff0000u); }
__device__ __forceinline__ float rbf(float x) { return (float)(__bf16)x; }
__device__ __forceinline__ u32 pack_bf(float lo, float hi) {
  bf16x2 r; r[0] = (__bf16)lo; r[1] = (__bf16)hi; return __builtin_bit_cast(u32, r);
}
__device__ __forceinline__ float dot2(u32 k, u32 q, float acc) {
  return __builtin_amdgcn_fdot2_f32_bf16(__builtin_bit_cast(bf16x2, k), __builtin_bit_cast(bf16x2, q), acc, false);
}
template <int CTRL> __device__ __forceinline__ float dpp(float x) {
  return __builtin_bit_cast(float, __builtin_amdgcn_mov_dpp(__builtin_bit_cast(int, x), CTRL, 0xf, 0xf, true));
}
constexpr int XOR1 = 0xB1, XOR2 = 0x4E, XOR7 = 0x141, XOR8 = 0x128;
__device__ __forceinline__ float swap32_add(float a, float b) {
  auto t = __builtin_amdgcn_permlane32_swap(__float_as_uint(a), __float_as_uint(b), false, false);
  return __uint_as_float(t[0]) + __uint_as_float(t[1]);
}
__device__ __forceinline__ float swap16_add(float a, float b) {
  auto t = __builtin_amdgcn_permlane16_swap(__float_as_uint(a), __float_as_uint(b), false, false);
  return __uint_as_float(t[0]) + __uint_as_float(t[1]);
}
__device__ __forceinline__ float wave_sum(float x) {
  x += dpp<XOR1>(x); x += dpp<XOR2>(x); x += dpp<XOR7>(x); x += dpp<XOR8>(x);
  x = swap16_add(x, x);
  return swap32_add(x, x);
}
__device__ __forceinline__ float wave_max(float x) {
  x = fmaxf(x, dpp<XOR1>(x)); x = fmaxf(x, dpp<XOR2>(x)); x = fmaxf(x, dpp<XOR7>(x)); x = fmaxf(x, dpp<XOR8>(x));
  auto s = __builtin_amdgcn_permlane16_swap(__float_as_uint(x), __float_as_uint(x), false, false);
  x = fmaxf(__uint_as_float(s[0]), __uint_as_float(s[1]));
  auto t = __builtin_amdgcn_permlane32_swap(__float_as_uint(x), __float_as_uint(x), false, false);
  return fmaxf(__uint_as_float(t[0]), __uint_as_float(t[1]));
}
template <int CTRL> __device__ __forceinline__ float sel_add(float lo, float hi, bool up) {
  const float t1 = lo + dpp<CTRL>(lo), t2 = hi + dpp<CTRL>(hi);
  return up ? t2 : t1;
}
template <int I> __device__ __forceinline__ float bc16(float x) {
  return __builtin_bit_cast(float, __builtin_amdgcn_update_dpp(0, __builtin_bit_cast(int, x), 0x150 + I, 0xf, 0xf, false));
}
__device__ __forceinline__ float bcast16(float x, int i) {
  switch (i) {
    case 0: return bc16<0>(x); case 1: return bc16<1>(x); case 2: return bc16<2>(x); case 3: return bc16<3>(x);
    case 4: return bc16<4>(x); case 5: return bc16<5>(x); case 6: return bc16<6>(x); case 7: return bc16<7>(x);
    case 8: return bc16<8>(x); case 9: return bc16<9>(x); case 10: return bc16<10>(x); case 11: return bc16<11>(x);
    case 12: return bc16<12>(x); case 13: return bc16<13>(x); case 14: return bc16<14>(x); default: return bc16<15>(x);
  }
}
__device__ __forceinline__ float silu_f(float x) { return x * __builtin_amdgcn_rcpf(1.f + __expf(-x)); }

template <int AUX> __device__ __forceinline__ u32x4 ld16(rsrc_t r, u32 voff, u32 soff) {
  return __builtin_amdgcn_raw_buffer_load_b128(r, voff, soff, AUX);
}
template <int AUX> __device__ __forceinline__ u32x2 ld8(rsrc_t r, u32 voff, u32 soff) {
  return __builtin_amdgcn_raw_buffer_load_b64(r, voff, soff, AUX);
}
__device__ __forceinline__ void st8_sc1(rsrc_t r, u32 voff, u32 v, u32 tag) {
  __builtin_amdgcn_raw_buffer_store_b64(u32x2{v, tag}, r, voff, 0, AUX_SC1);
}
__device__ __forceinline__ void st16_sc1(rsrc_t r, u32 voff, u32 v0, u32 v1, u32 tag) {
  __builtin_amdgcn_raw_buffer_store_b128(u32x4{v0, tag, v1, tag}, r, voff, 0, AUX_SC1);
}
__device__ __forceinline__ rsrc_t mk_rsrc(const void* p, u32 bytes) {
  return __builtin_amdgcn_make_buffer_rsrc(const_cast<void*>(p), 0, bytes, 0x00020000);
}
#define WAIT_VM(n) __builtin_amdgcn_s_waitcnt(((n) & 15) | (((n) >> 4) << 14) | (7 << 4) | (15 << 8))
#define SB() __builtin_amdgcn_sched_barrier(0)
#define CFENCE() asm volatile("" ::: "memory")
__device__ __forceinline__ bool all_ok(bool ok) { return __builtin_amdgcn_ballot_w64(!ok) == 0ull; }

template <int N> __device__ __forceinline__ void gather16(rsrc_t ws, u32 voff, u32 stride, u32 tag, u32x4 (&g)[N]) {
#pragma nounroll
  for (int spin = 0; spin < SPIN_MAX; ++spin) {
    CFENCE();
    bool ok = true;
#pragma unroll
    for (int i = 0; i < N; ++i) g[i] = ld16<AUX_SC1>(ws, voff + i * stride, 0);
#pragma unroll
    for (int i = 0; i < N; ++i) ok &= (g[i][1] == tag) & (g[i][3] == tag);
    if (all_ok(ok)) break;
    __builtin_amdgcn_s_sleep(8);
  }
}
template <int N> __device__ __forceinline__ void gather8(rsrc_t ws, u32 voff, u32 stride, u32 tag, u32 (&g)[N], bool act = true) {
#pragma nounroll
  for (int spin = 0; spin < SPIN_MAX; ++spin) {
    CFENCE();
    bool ok = true;
    u32x2 t[N];
#pragma unroll
    for (int i = 0; i < N; ++i) t[i] = ld8<AUX_SC1>(ws, voff + i * stride, 0);
#pragma unroll
    for (int i = 0; i < N; ++i) { ok &= (t[i][1] == tag); g[i] = t[i][0]; }
    if (all_ok(ok | !act)) break;
    __builtin_amdgcn_s_sleep(8);
  }
}

struct Smem {
  float red[8][128];
  float ml[8][2];
  float r2[64 * 20];
  u16 hb[3 * 1024];
  float stat[8][64];
  float red2[8][128];
  u32 dummy[64];
  u32x2 bc;
  u32x4 zero[4];
  u32 mflag[8];
  u32x4 wbuf[8][8][64];
  __attribute__((aligned(256))) u16 w2b[32][512];
  u16 actl[2][32];
};

#define STAMP(k) do {} while (0)
#define WSTAMP(k) do {} while (0)
#define FINISH() return
#define PIN(x) asm volatile("" : "+v"(x))
#define LDS_BARRIER() do { __builtin_amdgcn_s_waitcnt(0xc07f); __builtin_amdgcn_s_barrier(); CFENCE(); } while (0)
#define WAVE_SYNC() do { __builtin_amdgcn_fence(__ATOMIC_RELEASE, "wavefront"); __builtin_amdgcn_wave_barrier(); __builtin_amdgcn_fence(__ATOMIC_ACQUIRE, "wavefront"); } while (0)

__device__ __forceinline__ void dma16(rsrc_t r, u32 voff, u32 soff, u32 lds_dst) {
  u32 keep;
  asm volatile("s_mov_b32 %0, m0\n\ts_mov_b32 m0, %4\n\ts_nop 4\n\tbuffer_load_dwordx4 %1, %2, %3 offen nt lds\n\ts_mov_b32 m0, %0"
               : "=&s"(keep) : "v"(voff), "s"(r), "s"(soff), "s"(lds_dst) : "memory");
}
__device__ __forceinline__ u32 perm_lo(u32 a, u32 b) { return __builtin_amdgcn_perm(b, a, 0x05040100u); }
__device__ __forceinline__ u32 perm_hi(u32 a, u32 b) { return __builtin_amdgcn_perm(b, a, 0x07060302u); }
__device__ __forceinline__ float rdl(float x, int l) { return __int_as_float(__builtin_amdgcn_readlane(__float_as_int(x), l)); }

typedef __attribute__((ext_vector_type(8))) __bf16 bf16x8;
__device__ __forceinline__ f32x4 mfma16(u32x4 a, u32x4 b, f32x4 c) {
  return __builtin_amdgcn_mfma_f32_16x16x32_bf16(__builtin_bit_cast(bf16x8, a), __builtin_bit_cast(bf16x8, b), c, 0, 0, 0);
}
__device__ __forceinline__ float sum8(const float* p) {
  const f32x4 a = *reinterpret_cast<const f32x4*>(p), b = *reinterpret_cast<const f32x4*>(p + 4);
  return ((a[0] + b[0]) + (a[1] + b[1])) + ((a[2] + b[2]) + (a[3] + b[3]));
}
__device__ __forceinline__ float ln_ri(float var) {
  return __builtin_amdgcn_rcpf(rbf(__builtin_amdgcn_sqrtf(rbf(rbf(fmaxf(var, 0.f)) + 1e-5f))));
}
__device__ __forceinline__ void ln_stats2(const float (&x0)[8], const float (&x1)[8], float& mb0, float& ri0, float& mb1, float& ri1) {
  const float k0 = rdl(x0[0], 0), k1 = rdl(x1[0], 0);
  float s0 = 0.f, s1 = 0.f, q0 = 0.f, q1 = 0.f;
#pragma unroll
  for (int k = 0; k < 8; ++k) {
    const float d0 = x0[k] - k0, d1 = x1[k] - k1;
    s0 += d0; s1 += d1; q0 = fmaf(d0, d0, q0); q1 = fmaf(d1, d1, q1);
  }
  float t = swap16_add(swap32_add(s0, s1), swap32_add(q0, q1));
  t += dpp<XOR1>(t); t += dpp<XOR2>(t); t += dpp<XOR7>(t); t += dpp<XOR8>(t);
  auto u = __builtin_amdgcn_permlane16_swap(__float_as_uint(t), __float_as_uint(t), false, false);
  const float ms = __uint_as_float(u[0]) * (1.f / 512.f), mq = __uint_as_float(u[1]) * (1.f / 512.f);
  const float ri = ln_ri(fmaf(-ms, ms, mq));
  mb0 = rbf(k0 + rdl(ms, 0)); mb1 = rbf(k1 + rdl(ms, 32));
  ri0 = rdl(ri, 0); ri1 = rdl(ri, 32);
}
template <int I> struct IC { static constexpr int v = I; };
template <int I, int N, class F> __device__ __forceinline__ void sfor(F&& f) {
  if constexpr (I < N) { if (f(IC<I>{})) sfor<I + 1, N>(f); }
}
typedef __attribute__((ext_vector_type(4))) short s16x4;
typedef __attribute__((address_space(3))) s16x4* lds_s16x4p;
__device__ __forceinline__ u32x2 tr_read(u32 lds_addr) {
  return __builtin_bit_cast(u32x2, __builtin_amdgcn_ds_read_tr16_b64_v4i16((lds_s16x4p)lds_addr));
}
__device__ __forceinline__ u32 next_tag(u32 e) { return ((e | 0x40004000u) + 1u) & ~0x40004000u; }
__device__ __forceinline__ float u2f(u32 v) { return __uint_as_float(v); }
#define LDSP(T, a) ((__attribute__((address_space(3))) T*)(a))
template <class T> __device__ __forceinline__ u32 lds_addr(T* p) { u32 a = (u32)(uintptr_t)p; PIN(a); return a; }

#define TAB_LOAD "s_add_u32 s30, s28, wg_tab@rel32@lo+4\ns_addc_u32 s31, s29, wg_tab@rel32@hi+12\nglobal_load_dwordx2 %[te], %[va], s[30:31]\n"
__global__ void __launch_bounds__(512, 2) fused_kernel(const u16* kc_, const u16* vc_, const u16* wo_, const u16* wq_, const u16* wmod_, const u16* w1_, const u16* w3_,
                                                       unsigned char* ws_, const u16* x_, const u16* cnd_, const u16* w2_, u16* out_ PRM_ARG) {
  __shared__ __attribute__((aligned(256))) Smem sm;
  const u64 a_ka = (u64)__builtin_amdgcn_kernarg_segment_ptr();
  const int bid = blockIdx.x, tid = threadIdx.x;
  u64 wa, t64; u32x2 te; int tid0;
  u32 ar[14];
  u32 tch2, sa_, sb_, va_;
  u32x4 kr[NL], vr[NL];
  asm volatile(
    "s_load_dwordx16 s[4:19], %[ka], 0x0\ns_load_dwordx8 s[20:27], %[ka], 0x40\n"
    "s_memrealtime %[t]\n"
    "v_lshlrev_b32 %[va], 3, %[bid]\n"
    "s_getpc_b64 s[28:29]\n.Lcpf_first:\n"
    TAB_LOAD
    "s_cmp_gt_u32 %[bid], 15\ns_cbranch_scc1 .Lno_cpf\n"
    "s_mov_b32 %[sa], .Lend_a-.Lcpf_first\ns_max_i32 %[sa], %[sa], .Lend_b-.Lcpf_first\ns_max_i32 %[sa], %[sa], .Lend_c-.Lcpf_first\ns_max_i32 %[sa], %[sa], .Lend_d-.Lcpf_first\ns_max_i32 %[sa], %[sa], .Lend_e-.Lcpf_first\n"
    "v_lshlrev_b32 %[va], 6, %[tid]\nv_min_u32 %[va], %[sa], %[va]\nglobal_load_dword %[pf], %[va], s[28:29]\n"
    ".Lno_cpf:\n"
    "s_and_b32 %[sa], %[bid], 15\ns_lshr_b32 %[sb], %[bid], 6\ns_lshl2_add_u32 %[sa], %[sa], %[sb]\ns_lshl_b32 %[sa], %[sa], 22\ns_xor_b32 %[sa], %[sa], 0xff0000\n"
    "s_and_b32 %[sb], %[bid], 48\nv_lshl_add_u32 %[va], %[sb], 5, %[tid]\nv_lshl_add_u32 %[va], %[va], 4, %[sa]\n"
    "v_readfirstlane_b32 %[t0], %[tid]\n"
    "s_waitcnt lgkmcnt(0)\nglobal_load_dwordx4 %[k0], %[va], s[4:5] nt\nglobal_load_dwordx4 %[v0], %[va], s[6:7] nt\n"
    "s_bitcmp1_b32 %[t0], 6\ns_cselect_b64 s[14:15], s[16:17], s[14:15]\n"
    "s_bitcmp1_b32 %[bid], 4\ns_cselect_b64 s[8:9], s[10:11], s[8:9]\ns_cselect_b64 s[12:13], s[14:15], s[12:13]\ns_bitcmp1_b32 %[bid], 5\ns_cselect_b64 %[wa], s[12:13], s[8:9]"
    : "=&{s4}"(ar[0]), "=&{s5}"(ar[1]), "=&{s6}"(ar[2]), "=&{s7}"(ar[3]), "=&{s18}"(ar[4]), "=&{s19}"(ar[5]), "=&{s20}"(ar[6]), "=&{s21}"(ar[7]), "=&{s22}"(ar[8]), "=&{s23}"(ar[9]),
      "=&{s24}"(ar[10]), "=&{s25}"(ar[11]), "=&{s26}"(ar[12]), "=&{s27}"(ar[13]), [wa] "=&s"(wa), [t0] "=&s"(tid0), [t] "=&s"(t64), [te] "=&v"(te), [sa] "=&s"(sa_), [sb] "=&s"(sb_),
      [va] "=&v"(va_), [pf] "=&v"(tch2), [k0] "=&v"(kr[0]), [v0] "=&v"(vr[0])
    : [ka] "s"(a_ka), [bid] "s"(bid), [tid] "v"(tid) : "s8", "s9", "s10", "s11", "s12", "s13", "s14", "s15", "s16", "s17", "s28", "s29", "s30", "s31", "scc", "memory");
  asm volatile("" : "+s"(ar[0]), "+s"(ar[1]), "+s"(ar[2]), "+s"(ar[3]), "+s"(ar[4]), "+s"(ar[5]), "+s"(ar[6]), "+s"(ar[7]), "+s"(ar[8]), "+s"(ar[9]), "+s"(ar[10]), "+s"(ar[11]), "+s"(ar[12]), "+s"(ar[13]));
  asm volatile("" : "+s"(wa), "+s"(t64), "+s"(tid0));
  const u32 tka = (u32)wall_clock64();
  const u32 t0 = (u32)t64, te0_ = 0;
  __builtin_assume(tid >= 0 && tid < 512 && tid0 >= 0 && tid0 < 512 && bid >= 0 && bid < 256);
  constexpr Prm P = PRM0;
  const int TEND = P.TEND;
  const int xcd = bid & 7, part = bid >> 4 & 3;
  const int lane = tid & 63, wave = tid0 >> 6;
  SB();
  const bool p3 = part == 3;
  u32 pitch = (u32)(0x005600c001000020ull >> (part * 16) & 0xffff) * 32u;
  const int sh = p3 ? 2 : 3;
  u32 wv0 = (u32)((p3 ? (wave >> 1) * 128 : wave * 64) + (part == 0 ? xcd * 512 : 0) + (lane >> sh)) * pitch + ((lane & (p3 ? 3 : 7)) ^ (lane >> (p3 ? 5 : 3))) * 16u;
  const u32 wimg = (u32)(uintptr_t)(&sm.wbuf[0][0][0]);
  u32 wsz = (part == 0 ? 4096u : 512u) * pitch, wld = wimg + (p3 ? (wave & 1) * 32768 + (wave >> 1) * 8192 : wave * 8192);
  PIN(wv0);
  asm volatile("" : "+s"(wsz), "+s"(wld), "+s"(pitch));
  auto karg = [&](int i) { return ar[2 * i] | (u64)ar[2 * i + 1] << 32; };
  const u64 a_kc = karg(0), a_vc = karg(1);
  unsigned char* ws = (unsigned char*)karg(2);
  const u16 *x = (const u16*)karg(3), *cnd = (const u16*)karg(4), *w2 = (const u16*)karg(5);
  u16* out = (u16*)karg(6);
  const rsrc_t wsr = mk_rsrc(ws, WS_END);
  asm volatile("s_waitcnt vmcnt(2)" : "+v"(te));
  const u32 te0 = __builtin_amdgcn_readfirstlane(te[0]), te1 = __builtin_amdgcn_readfirstlane(te[1]);
  const int bl0 = te0 & 127, nl = te0 >> 7 & 63, blk = te0 >> 13 & 63;
  const bool has_w = te0 >> 19 & 1;
  const rsrc_t rw = mk_rsrc((const void*)wa, wsz);
  const u32 wstep = pitch << (6 - sh), wflip = p3 ? 32u : 0u;
  u32 wso = blk << (sh + 4);
  int wleft = 8;
  auto piece = [&]() {
    dma16(rw, wv0, wso, wld);
    wso += wstep; wld += 1024;
    wv0 ^= (wleft & 1) ? wflip : 0u;
    --wleft;
  };
  SB();
  STAMP(23);
  const int b = bid >> 3 & 1, head = xcd * 4 + (bid >> 6 ^ 3);
  const int g = lane >> 4, c = lane & 15;
  const int i8 = b | bid >> 6 << 1, wi = i8 * 8 + xcd;
  const bool is_up = p3 && has_w, modc = part == 2 && wi < 16;
  const size_t base = ((size_t)(b * NH + head) * SK + (SK - WIN)) * DH;
  const rsrc_t rk = mk_rsrc((const u16*)a_kc + base, WIN * DH * 2), rv = mk_rsrc((const u16*)a_vc + base, WIN * DH * 2);
  int tau = te1 & 0xffff;
  const int tauw = te1 >> 16;
  int due = P.NF * tau, now = 0;
  auto gate = [&]() {
    while (now < due) { now = (int)(((u32)wall_clock64() - t0) << 8); if (now < due) __builtin_amdgcn_s_sleep(1); }
    due += tau;
  };
  auto until = [&](int t) { while ((int)(((u32)wall_clock64() - t0) << 8) < t); };
  const u32 pslot = G_P + ((b * 32 + head) * 4 + part) * 256;
  u32x2 ep; u32 xs0, xs1, tch1; u32x4 xr0, xr1;
  const rsrc_t rx = mk_rsrc(part == 2 ? cnd : x, (part == 1 || (part == 2 && has_w)) ? 2048u : 0u);
  auto small_ops = [&]() {
    ep = ld8<AUX_SC1>(wsr, wave == 7 ? WS_EPOCH : OOB, 0);
    xs0 = __builtin_amdgcn_raw_buffer_load_b16(rx, tid * 2, 0, 0); xs1 = __builtin_amdgcn_raw_buffer_load_b16(rx, tid * 2, 1024, 0);
    xr0 = ld16<0>(rx, part == 1 ? lane * 16 : OOB, 0); xr1 = ld16<0>(rx, part == 1 ? lane * 16 : OOB, 1024);
    tch1 = __builtin_amdgcn_raw_buffer_load_b32(mk_rsrc(w2 + (size_t)blk * 32 * 512, is_up ? 64u : 0u), tid == 384 ? 0 : OOB, 0, AUX_NT);
  };
  if (has_w) {
    if (modc) now = 1 << 30;
#pragma nounroll
    while (wleft > (int)(te0 >> 20)) { gate(); due += tauw - tau; piece(); }
    now = 0;
  }
  SB();
  u32 sot;
  {
    const u32 s1 = bl0 + lane - 1, s0 = s1 + (s1 >= 108 ? 4 : 0);
    u32 sb = s0 & 96u;
#pragma unroll
    for (int k = 0; k < 5; ++k) sb |= (s0 >> k & 1u) << (P.PM >> (4 * k) & 7);
    const u32 src = (sb >> 5 & 3u) | (u32)(b * 32 + head) << 2;
    sb ^= ((__builtin_popcount(src & P.M0) & 1) | (__builtin_popcount(src & P.M1) & 1) << 1 | (__builtin_popcount(src & P.M2) & 1) << 2 | (__builtin_popcount(src & P.M3) & 1) << 3 | (__builtin_popcount(src & P.M4) & 1) << 4) & (s1 >= 96 ? 7u : 31u);
    sot = (sb & 7u) << 10 | (u32)wave << 13 | (sb >> 3) << 16;
    sot = s1 >= 120 ? (s1 + 4) << 13 | (u32)wave << 10 : sot;
    sot = lane < nl ? sot : OOB;
    PIN(sot);
  }
  u32 tag = 0;
  u32x4 bf[8];
  auto wo_prep = [&](int s0 = 0, int s1 = 2) {
    const u32 wi_ = wimg + wave * 8192;
    const int q = (lane >> 2) & 3, pp = lane & 3;
#pragma unroll
    for (int s = s0; s < s1; ++s) {
#pragma unroll
      for (int t = 0; t < 4; ++t) {
        const int r0 = s * 32 + g * 8 + q, r1 = r0 + 4, ch = t * 2 + (pp >> 1);
        const u32x2 lo = tr_read(wi_ + r0 * 128 + ((ch ^ (r0 & 7)) * 16) + (pp & 1) * 8), hi = tr_read(wi_ + r1 * 128 + ((ch ^ (r1 & 7)) * 16) + (pp & 1) * 8);
        bf[s * 4 + t] = u32x4{lo[0], lo[1], hi[0], hi[1]};
      }
    }
  };
  auto gemv_go = [&](u32 hh) {
    sm.hb[tid] = (u16)hh; sm.hb[512 + tid] = (u16)(hh >> 16);
    WAVE_SYNC();
    const u32x4* a = (c & 11) == 0 ? reinterpret_cast<const u32x4*>(&sm.hb[(c >> 2) * 512 + wave * 64 + g * 8]) : &sm.zero[0];
    const u32x4 a0 = a[0], a1 = a[(c & 11) == 0 ? 4 : 0];
    const f32x4 z = {0.f, 0.f, 0.f, 0.f};
    f32x4 d[4];
#pragma unroll
    for (int t = 0; t < 4; ++t) d[t] = mfma16(a0, bf[t], z);
#pragma unroll
    for (int t = 0; t < 4; ++t) d[t] = mfma16(a1, bf[4 + t], d[t]);
    if (lane < 32) {
#pragma unroll
      for (int t = 0; t < 4; ++t) sm.r2[(t * 16 + c) * 20 + g * 8 + wave] = d[t][0];
    }
  };
  auto mod_stage = [&]() {
    wo_prep();
    gemv_go(pack_bf(silu_f(lo_f(xs0)), silu_f(lo_f(xs1))));
    __builtin_amdgcn_s_waitcnt(0xc07f);
    if (lane == 0) *LDSP(volatile u32, (u32)(uintptr_t)&sm.mflag[wave]) = 1u;
    if (wave == 7) {
#pragma nounroll
      for (int spin = 0; spin < SPIN_MAX; ++spin) {
        const u32 f = *LDSP(volatile u32, (u32)(uintptr_t)&sm.mflag[lane & 7]);
        if (all_ok(f != 0u)) break;
        __builtin_amdgcn_s_sleep(1);
      }
      CFENCE();
      st8_sc1(wsr, G_MOD + (blk * 64 + lane) * 8, pack_bf(sum8(&sm.r2[lane * 20]), sum8(&sm.r2[lane * 20 + 8])), tag);
    }
    STAMP(9);
  };
  if (lane < 4) sm.zero[lane] = u32x4{0u, 0u, 0u, 0u};
  if (part == 2 && has_w) {
    if (tid < 8) sm.mflag[tid] = 0u;
    LDS_BARRIER();
    if (__builtin_expect(modc, 0)) {
      small_ops();
      __builtin_amdgcn_raw_buffer_store_b128(u32x4{SENT, SENT, SENT, SENT}, wsr, tid < 320 ? G_P + wi * 5120 + tid * 16 : OOB, 0, AUX_SC1);
      asm volatile("s_waitcnt vmcnt(0)" ::: "memory");
      tag = next_tag(__builtin_amdgcn_readfirstlane(ep[0]));
      mod_stage();
      __builtin_amdgcn_s_barrier();
    }
  }
  SB();
  const int fd = wi * 8 + (lane & 7);
  const int ub = wave >> 2, ud = (wave & 3) * 128 + c * 8 + g * 2;
  u32x4 sg[3]; u32 sx2;
  auto side_loads = [&]() {
    const bool fin = part == 1 && wave == 0;
    sg[0] = ld16<AUX_SC1>(wsr, is_up ? G_MOD + (1024 + ud) * 8 : (fin ? G_MOD + (1024 + fd) * 8 : OOB), 0);
    sg[1] = ld16<AUX_SC1>(wsr, is_up ? G_MOD + (1536 + ud) * 8 : (fin ? G_MOD + (2560 + fd) * 8 : OOB), 0);
    sg[2] = ld16<AUX_SC1>(wsr, is_up ? G_MOD + (2048 + ud) * 8 : OOB, 0);
    sx2 = __builtin_amdgcn_raw_buffer_load_b32(mk_rsrc(x, (is_up || fin) ? 2048u : 0u), is_up ? ub * 1024 + ud * 2 : (g & 1) * 1024 + (fd >> 1) * 4, 0, 0);
  };
  auto side_pin = [&]() { PIN(sg[0]); PIN(sg[1]); PIN(sg[2]); PIN(sx2); };
  const u32 mlo = G_ML + (head * 2 + b) * 64 + (lane & 3) * 16;
  float fmrg = 0.f;
  bool fok = wave != 0;
  auto merge_factor = [&](const u32x4& ml) {
    const float mi = u2f(ml[0]);
    float M = fmaxf(mi, dpp<XOR1>(mi)); M = fmaxf(M, dpp<XOR2>(M));
    const float e = __expf(mi - M);
    float L = e * u2f(ml[2]);
    L += dpp<XOR1>(L); L += dpp<XOR2>(L);
    fmrg = rdl(e * __builtin_amdgcn_rcpf(L), part);
  };
  f32x2 ga = {0.f, 0.f}, xin = {0.f, 0.f}, cc1 = {0.f, 0.f};
  float Af = 0.f, Bf = 0.f;
  const int hpos = ub * 512 + ud;
  auto up_prep = [&]() {
    if (!all_ok((sg[0][1] == tag) & (sg[0][3] == tag) & (sg[1][1] == tag) & (sg[1][3] == tag) & (sg[2][1] == tag) & (sg[2][3] == tag))) gather16<3>(wsr, G_MOD + (1024 + ud) * 8, 512 * 8, tag, sg);
    ga = f32x2{ub ? hi_f(sg[0][0]) : lo_f(sg[0][0]), ub ? hi_f(sg[0][2]) : lo_f(sg[0][2])};
    cc1 = f32x2{rbf(1.f + (ub ? hi_f(sg[2][0]) : lo_f(sg[2][0]))), rbf(1.f + (ub ? hi_f(sg[2][2]) : lo_f(sg[2][2])))};
    xin = f32x2{lo_f(sx2), hi_f(sx2)};
    u32* hp = reinterpret_cast<u32*>(&sm.hb[hpos]);
    hp[512] = pack_bf(cc1[0], cc1[1]);
    hp[1024] = ub ? perm_hi(sg[1][0], sg[1][2]) : perm_lo(sg[1][0], sg[1][2]);
  };
  auto up_frags = [&]() {
    const int pp = lane & 3;
    const u32 a = wimg + (pp & 1) * 32768 + ((pp >> 1) * 32 + g * 8 + ((lane >> 2) & 3)) * 64 + (((wave >> 1) ^ ((g & 1) | (pp & 2))) * 16) + (wave & 1) * 8;
#pragma unroll
    for (int rb = 0; rb < 8; ++rb) {
      const u32x2 lo = tr_read(a + rb * 4096), hi = tr_read(a + rb * 4096 + 256);
      bf[rb] = u32x4{lo[0], lo[1], hi[0], hi[1]};
    }
  };
  const u32x4* ap = reinterpret_cast<const u32x4*>(&sm.hb[((lane >> 3) & 1) * 512 + (lane & 1) * 32 + g * 8]);
  const bool par = lane & 8;
  float l0 = 0.f, l1 = 0.f;
  u32x2 gmd[2], tbg;
  int t1 = 0;
#pragma unroll
  for (int i = NCMIN / 8; i < NL; ++i) kr[i] = u32x4{0u, 0u, 0u, 0u};
  sfor<0, NL>([&](auto ic) {
    constexpr int i = ic.v;
    if (i >= NCMIN / 8 && i >= nl) return false;
    if constexpr (i == 0) return true;
    const u32 so = __builtin_amdgcn_readlane(sot, i);
    gate();
    SB();
    kr[i] = ld16<AUX_NT>(rk, lane * 16, so);
    if constexpr (i == SMO) small_ops();
    if constexpr (i == TBL) tbg = ld8<AUX_SC1>(wsr, wave == 7 ? G_TB + xcd * 128 + (lane & 7) * 8 : OOB, 0);
    if constexpr (i == QP) { gmd[0] = ld8<AUX_SC1>(wsr, part == 1 ? G_MOD + tid * 8 : OOB, 0); gmd[1] = ld8<AUX_SC1>(wsr, part == 1 ? G_MOD + tid * 8 : OOB, 4096); }
    SB();
    SB();
    if (i >= WK) { WAIT_VM(WK); SB(); }
    if (i == 1) STAMP(25);
    if (i == 8) STAMP(12);
    if (i == 20) STAMP(21);
    if (i == 25) STAMP(22);
    if (i == TBP) {
      if (part == 3 && i8 == 0 && wave == 7) { tag = next_tag(__builtin_amdgcn_readfirstlane(ep[0])); if (lane < 8) st8_sc1(wsr, G_TB + lane * 128 + xcd * 8, t0 + (((tka - t0) * P.GA) >> 3) - P.GS, tag); }
      SB();
    }
    if (i == TBU) {
      if (wave == 7) {
        const bool ok = tbg[1] == tag;
        float lf = ok ? (float)min(max((int)(t0 - tbg[0]), -300), 300) : 0.f, nf = ok ? 1.f : 0.f;
        lf += dpp<XOR1>(lf); nf += dpp<XOR1>(nf); lf += dpp<XOR2>(lf); nf += dpp<XOR2>(nf); lf += dpp<XOR7>(lf); nf += dpp<XOR7>(nf);
        if (lane == 0) sm.bc = u32x2{tag, t0 - (u32)(int)(lf * __builtin_amdgcn_rcpf(fmaxf(nf, 1.f)))};
      }
      LDS_BARRIER();
      const u32x2 bc = sm.bc;
      tag = __builtin_amdgcn_readfirstlane(bc[0]);
      t1 = (__builtin_amdgcn_readfirstlane((int)(bc[1] - t0)) + TEND + P.X1) * 256;
      tau -= __builtin_amdgcn_readfirstlane((int)((float)(int)(t0 - bc[1]) * 256.f * __builtin_amdgcn_rcpf((float)(2 * nl - TBU - 3))));
      SB();
    }
    if (i == LNS) {
      tag = next_tag(__builtin_amdgcn_readfirstlane(ep[0]));
      if (part == 1) {
        float x0[8], x1[8], mb0, ri0, mb1, ri1;
#pragma unroll
        for (int k = 0; k < 4; ++k) { x0[2 * k] = lo_f(xr0[k]); x0[2 * k + 1] = hi_f(xr0[k]); x1[2 * k] = lo_f(xr1[k]); x1[2 * k + 1] = hi_f(xr1[k]); }
        ln_stats2(x0, x1, mb0, ri0, mb1, ri1);
        l0 = rbf(rbf(lo_f(xs0) - mb0) * ri0); l1 = rbf(rbf(lo_f(xs1) - mb1) * ri1);
      }
      PIN(l0); PIN(l1);
      SB();
    }
    if (i == WUSE) {
      PIN(tch1); PIN(tch2);
      if ((part == 1 || (part == 2 && has_w && !modc))) {
        WAIT_VM(WK); CFENCE();
        if (part == 2) mod_stage(); else wo_prep();
      }
#pragma unroll
      for (int k = 0; k < 8; ++k) PIN(bf[k]);
      SB();
    }
    if (i == QP + QD) {
      if (part == 1) {
        u32 gm[2] = {gmd[0][0], gmd[1][0]};
        if (!all_ok((gmd[0][1] == tag) & (gmd[1][1] == tag))) { STAMP(11); gather8<2>(wsr, G_MOD + tid * 8, 512 * 8, tag, gm); }
        STAMP(5);
        gemv_go(pack_bf(rbf(l0 * rbf(1.f + lo_f(gm[1]))) + lo_f(gm[0]), rbf(l1 * rbf(1.f + hi_f(gm[1]))) + hi_f(gm[0])));
      }
      SB();
    }
    if (i == QP + QD + 1) {
      if (part == 1) {
        LDS_BARRIER();
        if (wave == 0) {
          const int bb = lane >> 5, jq = lane & 31;
          st8_sc1(wsr, G_Q + (bb * 2048 + blk * 32 + jq) * 8, pack_bf(sum8(&sm.r2[2 * jq * 20 + bb * 8]), sum8(&sm.r2[(2 * jq + 1) * 20 + bb * 8])), tag);
        }
        STAMP(6);
      }
      SB();
    }
    return true;
  });
  PIN(sot);
  const u32 qoff = G_Q + ((b * 32 + head) * 64 + c * 4) * 8;
  u32x4 qg[2], fl2[3];
  u32 qq[4];
  constexpr int NB = (NL + 15) / 16;
  float xlo, sc[NB], pr[NB], m = 0.f;
  f32x4 acc = {0.f, 0.f, 0.f, 0.f};
  u32 pb[NB], pm[4], cpf;
  const u32 adst0 = lds_addr(lane < 32 ? &sm.red[wave][c * 8 + g * 4] : &sm.r2[(lane - 32) * 4]);
  u32 po, rdst, rsrc, xo;
  auto wo_addr = [&]() {
    po = G_P + (((c >> 2 & 1) * 32 + xcd * 4 + (wave >> 1)) * 4 + (c & 3)) * 256 + (wave & 1) * 128 + (c >> 3) * 64 + g * 16;
    rdst = lane < 32 ? (u32)(uintptr_t)&sm.red2[wave][0] + lane * 16 : (u32)(uintptr_t)&sm.r2[0] + (lane - 32) * 16;
    rsrc = (u32)(uintptr_t)&sm.red2[0][((wave & 1) * 16 + (lane & 15)) * 4 + (lane >> 4)];
    xo = (lane & 1) ? OOB : G_XP + xcd * 2048 + (wave & 1) * 1024 + (blk * 64 + lane) * 2;
    PIN(po); PIN(rdst); PIN(rsrc); PIN(xo);
  };
  u32 pso = wave == 0 ? pslot + lane * 4 : OOB;
  PIN(pso);
  const u32 nadd = (te0 >> 20) ? P.LW : (is_up ? P.LU : 0);
  u32 nto = tid == 0 ? G_NT + bid * 8 : OOB;
  PIN(nto);
  auto notices = [&](auto nc_, u32 off, u32x4 (&gn)[nc_.v]) -> int {
    constexpr int N = nc_.v;
#pragma nounroll
    for (int spin = 0; spin < SPIN_MAX; ++spin) {
      bool ok = true;
#pragma unroll
      for (int i = 0; i < N; ++i) ok &= (gn[i][1] == tag) & (gn[i][3] == tag);
      if (all_ok(ok)) break;
      __builtin_amdgcn_s_sleep(10);
      CFENCE();
#pragma unroll
      for (int i = 0; i < N; ++i) gn[i] = ld16<AUX_SC1>(wsr, off, i * 1024);
    }
    int mx = 0;
#pragma unroll
    for (int i = 0; i < N; ++i) mx = max(mx, max((int)(gn[i][0] - t0), (int)(gn[i][2] - t0)));
    return __builtin_amdgcn_readfirstlane((int)wave_max((float)min(mx, 1 << 20)));
  };
  const int side = nl >= SIDE + 13 ? SIDE + 8 : (nl >= SIDE + 9 ? SIDE + 4 : SIDE);
  sfor<0, NL + WV>([&](auto ic) {
    constexpr int v = ic.v;
    if (cons_end(v - 1) >= NCMIN / 8 && cons_end(v - 1) >= nl) return false;
    if constexpr (v > 0 && v < NL) {
      const u32 so = __builtin_amdgcn_readlane(sot, v);
      if constexpr (v >= 12) { if (v == nl - P.NE) tau = (tau * P.FE) >> 4; }
      if constexpr (v >= NCMIN / 8) { if (v < nl) gate(); } else gate();
      SB();
      vr[v] = ld16<AUX_NT>(rv, lane * 16, so);
      SB();
      SB();
    }
    if constexpr (v == NCMIN / 8 - 1) {
#pragma unroll
      for (int i = NCMIN / 8; i < NL; ++i) asm volatile("" : "=v"(vr[i]));
      asm volatile("" : "=v"(fl2[1]), "=v"(fl2[2]));
    }
    if constexpr (v == VQ - 4) { qg[0] = ld16<AUX_SC1>(wsr, qoff, 0); qg[1] = ld16<AUX_SC1>(wsr, qoff + 16, 0); }
    if constexpr (v == VSIDE) side_loads();
    if constexpr (v == CPF) {
      u64 pc; u32 dlt, len;
      asm volatile("s_getpc_b64 %0\n.Lcpf_here:\n\ts_mov_b32 %1, .Ltail_start-.Lcpf_here\n\ts_mov_b32 %2, .Lend_a-.Ltail_start\n\ts_max_i32 %2, %2, .Lend_b-.Ltail_start\n\ts_max_i32 %2, %2, .Lend_c-.Ltail_start\n\ts_max_i32 %2, %2, .Lend_d-.Ltail_start\n\ts_max_i32 %2, %2, .Lend_e-.Ltail_start" : "=s"(pc), "=s"(dlt), "=s"(len) :: "scc");
      pc += dlt;
      const rsrc_t rc = mk_rsrc((const void*)pc, (part == 3 && i8 == 1) ? len : 0u);
      cpf = __builtin_amdgcn_raw_buffer_load_b32(rc, tid * 64, 0, 0);
    }
    if constexpr (v == CPF + WV + 1) PIN(cpf);
    if constexpr (v == SIDE || v == SIDE + 4 || v == SIDE + 8) fl2[(v - SIDE) / 4] = ld16<AUX_SC1>(wsr, (wave == 0 && v == side) ? mlo : OOB, 0);
    SB();
    if constexpr (v < WV) { WAIT_VM(WK); SB(); } else if constexpr (v < NL && cons_end(v) < v - WV + 1) { WAIT_VM(WV); SB(); }
    if constexpr (v == VQ) {
      if (!all_ok((qg[0][1] == tag) & (qg[0][3] == tag) & (qg[1][1] == tag) & (qg[1][3] == tag))) { STAMP(20); gather16<2>(wsr, qoff, 16, tag, qg); }
      STAMP(1); WSTAMP(1);
      qq[0] = qg[0][0]; qq[1] = qg[0][2]; qq[2] = qg[1][0]; qq[3] = qg[1][2];
    }
    if constexpr (v >= VQ && v < VS) {
      constexpr int sl = v - VQ, bt = sl >> 1;
      float xs[8];
#pragma unroll
      for (int i = 0; i < 8; ++i) {
        float d = 0.f;
        if (sl * 8 + i < NL) {
#pragma unroll
          for (int k = 0; k < 4; ++k) d = dot2(kr[sl * 8 + i][k], qq[k], d);
        }
        xs[i] = d;
      }
#pragma unroll
      for (int h = 0; h < 4; ++h) xs[h] = sel_add<XOR7>(xs[h], xs[h + 4], c & 4);
#pragma unroll
      for (int h = 0; h < 2; ++h) xs[h] = sel_add<XOR2>(xs[h], xs[h + 2], c & 2);
      xs[0] = sel_add<XOR1>(xs[0], xs[1], c & 1);
      if constexpr ((sl & 1) || sl == NSL - 1) {
        const float t = (sl & 1) ? sel_add<XOR8>(xlo, xs[0], c & 8) : xs[0] + dpp<XOR8>(xs[0]);
        const float s = rbf(rbf(t) * 0.08838834764831845f);
        sc[bt] = (bt * 16 + c < nl) ? s : -__builtin_inff();
        PIN(sc[bt]);
      } else {
        xlo = xs[0];
        PIN(xlo);
      }
    }
    if constexpr (v == VS) {
      float mloc = sc[0];
#pragma unroll
      for (int bt = 1; bt < NB; ++bt) mloc = fmaxf(mloc, sc[bt]);
      m = fmaxf(wave_max(mloc), -3.0e38f);
      float ls = 0.f;
#pragma unroll
      for (int bt = 0; bt < NB; ++bt) { pr[bt] = __expf(sc[bt] - m); ls += pr[bt]; }
      const float l = wave_sum(ls);
      if (lane == 0) { sm.ml[wave][0] = m; sm.ml[wave][1] = l; }
      STAMP(2); WSTAMP(2);
    }
    sfor<cons_end(v - 1), cons_end(v)>([&](auto jc) {
      constexpr int i = jc.v;
      if (i < NCMIN / 8 || i < nl) {
        const u32 pv = __float_as_uint(bcast16(u2f(pb[i >> 4]), i & 15));
        acc = mfma16(u32x4{pv & pm[0], pv & pm[1], pv & pm[2], pv & pm[3]}, vr[i], acc);
      }
      PIN(acc);
      return true;
    });
    if constexpr (v == MLS - 1) { side_pin(); if (is_up) up_prep(); PIN(ga); PIN(xin); PIN(cc1); }
    if constexpr (v == UPREP - 1) wo_addr();
    if constexpr (v == UPREP) {
#pragma unroll
      for (int k = 0; k < 8; ++k) asm volatile("" : "=v"(bf[k]));
      if (part == 0) wo_prep(0, 1); else if (is_up) up_frags();
#pragma unroll
      for (int k = 0; k < 8; ++k) PIN(bf[k]);
    }
    if constexpr (v == UPREP + 1) {
      if (is_up) {
        const u32x4* a1 = ap + 128 + ((lane >> 1) & 1) * 128;
        f32x4 d0 = {0.f, 0.f, 0.f, 0.f}, d1 = {0.f, 0.f, 0.f, 0.f};
#pragma unroll
        for (int rb = 0; rb < 4; ++rb) { d0 = mfma16(a1[rb * 8], bf[rb], d0); d1 = mfma16(a1[(rb + 4) * 8], bf[rb + 4], d1); }
        Af = par ? d0[1] + d1[1] : d0[0] + d1[0]; Bf = par ? d0[3] + d1[3] : d0[2] + d1[2];
        Af += dpp<XOR8>(Af); Bf += dpp<XOR8>(Bf);
      }
      PIN(Af); PIN(Bf);
    }
    if constexpr (v == SIDE + WV + 1 || v == SIDE + WV + 5 || v == SIDE + WV + 9) {
      if (wave == 0 && v == side + WV + 1) {
        const u32x4 ml = fl2[(v - SIDE - WV - 1) / 4];
        fok = all_ok((ml[1] == tag) & (ml[3] == tag));
        merge_factor(ml);
      }
    }
    if (v % 6 == 4 && v >= 10 && v < 30) STAMP(15 + v / 6);
    if constexpr (v == MLS) {
      LDS_BARRIER();
      float mm[8], ll[8];
#pragma unroll
      for (int w = 0; w < 8; ++w) { const f32x2 t = *reinterpret_cast<const f32x2*>(&sm.ml[w][0]); mm[w] = t[0]; ll[w] = t[1]; }
      float Mwg = mm[0], Lwg = 0.f;
#pragma unroll
      for (int w = 1; w < 8; ++w) Mwg = fmaxf(Mwg, mm[w]);
      if (wave == 0) {
#pragma unroll
        for (int w = 0; w < 8; ++w) Lwg = fmaf(__expf(mm[w] - Mwg), ll[w], Lwg);
        if (lane == 0) st16_sc1(wsr, G_ML + ((head * 2 + b) * 4 + part) * 16, __float_as_uint(Mwg), __float_as_uint(Lwg), tag);
      }
      const float fw = __expf(m - Mwg);
#pragma unroll
      for (int bt = 0; bt < NB; ++bt) { pb[bt] = pack_bf(pr[bt] * fw, pr[bt] * fw); PIN(pb[bt]); }
#pragma unroll
      for (int k = 0; k < 4; ++k) { pm[k] = (c >> 1) == k ? ((c & 1) ? 0xffff0000u : 0x0000ffffu) : 0u; PIN(pm[k]); }
    }
    SB();
    return true;
  });
  asm volatile(".Ltail_start:");
  *LDSP(f32x4, adst0) = acc;
  STAMP(14); WSTAMP(14);
  if (!fok) {
    u32x4 ml;
#pragma nounroll
    for (int spin = 0; spin < SPIN_MAX; ++spin) {
      CFENCE();
      ml = ld16<AUX_SC1>(wsr, mlo, 0);
      if (all_ok((ml[1] == tag) & (ml[3] == tag))) break;
    }
    merge_factor(ml);
  }
  STAMP(15);
  LDS_BARRIER();
  STAMP(3);
  {
    u32 pw = 0;
    if (wave == 0) {
      f32x2 t[8];
#pragma unroll
      for (int w = 0; w < 8; ++w) t[w] = *reinterpret_cast<const f32x2*>(&sm.red[w][2 * lane]);
      const f32x2 o = ((t[0] + t[1]) + (t[2] + t[3])) + ((t[4] + t[5]) + (t[6] + t[7]));
      pw = min(pack_bf(o[0] * fmrg, o[1] * fmrg), SENT - 1u);
    }
    __builtin_amdgcn_raw_buffer_store_b32(pw, wsr, pso, 0, AUX_SC1);
    st8_sc1(wsr, nto, (u32)wall_clock64() + nadd, tag);
    SB();
#pragma unroll
    for (int i = NCMIN / 8; i < NL; ++i) PIN(vr[i]);
    PIN(fl2[0]); PIN(fl2[1]); PIN(fl2[2]);
  }
  STAMP(4);

  if (part == 2) FINISH();
  if (part == 0) {
    __builtin_amdgcn_s_barrier();
#pragma nounroll
    while (wleft > 0) piece();
    until(t1);
    STAMP(11);
    CFENCE();
    u32x4 og[2];
    og[0] = ld16<AUX_SC1>(wsr, po, 0);
    SB();
    WAIT_VM(1); CFENCE();
    wo_prep(1, 2);
#pragma unroll
    for (int k = 4; k < 8; ++k) PIN(bf[k]);
    SB();
    f32x4 d[4];
    auto wo_mm = [&]() {
      const f32x4 z = {0.f, 0.f, 0.f, 0.f};
#pragma unroll
      for (int t = 0; t < 4; ++t) { d[t] = mfma16(og[0], bf[t], z); og[1][t] = __float_as_uint(dpp<XOR8>(u2f(og[0][t]))); }
      SB();
#pragma unroll
      for (int t = 0; t < 4; ++t) d[t] = mfma16(og[1], bf[4 + t], d[t]);
    };
    auto p_bad = [&]() { return !all_ok(max(max(og[0][0], og[0][1]), max(og[0][2], og[0][3])) != SENT); };
    wo_mm();
    if (__builtin_expect(p_bad(), 0)) {
#pragma nounroll
      for (int spin = 0; spin < SPIN_MAX; ++spin) {
        __builtin_amdgcn_s_sleep(1);
        CFENCE();
        og[0] = ld16<AUX_SC1>(wsr, po, 0);
        if (!p_bad()) break;
      }
      wo_mm();
      SB();
      asm volatile(".Lend_d:");
    }
    STAMP(5); WSTAMP(5);
    {
      SB();
      f32x4 y; f32x2 h[4];
#pragma unroll
      for (int t = 0; t < 4; ++t) { h[t] = d[t].xy + d[t].zw; PIN(h[t]); }
#pragma unroll
      for (int t = 0; t < 4; ++t) { y[t] = h[t][0] + h[t][1]; PIN(y[t]); }
      *LDSP(f32x4, rdst) = y;
    }
    STAMP(16);
    LDS_BARRIER();
    STAMP(17);
    if (wave < 2) {
      f32x2 v[4];
#pragma unroll
      for (int w = 0; w < 4; ++w) v[w] = f32x2{LDSP(float, rsrc)[w * 256], LDSP(float, rsrc)[w * 256 + 128]};
      const f32x2 h = (v[0] + v[1]) + (v[2] + v[3]);
      const float y = h[0] + h[1];
      __builtin_amdgcn_raw_buffer_store_b32(min(pack_bf(y, dpp<XOR1>(y)), SENT - 1u), wsr, xo, 0, AUX_SC1);
      asm volatile(".Lend_a:");
    }
    STAMP(6);
    FINISH();
  }
  if (part == 3) {
    if (!has_w) FINISH();
    u32x4 b2[4];
    {
      __builtin_amdgcn_s_barrier();
      const rsrc_t r2 = mk_rsrc(w2 + (size_t)(blk * 32 + wave * 4) * 512, 4096u);
      const u32 l2 = (u32)(uintptr_t)(&sm.w2b[wave * 4][0]);
#pragma unroll
      for (int n = 0; n < 4; ++n) dma16(r2, (lane ^ (n * 2) ^ ((wave & 2) << 2)) * 16, n * 1024, l2 + n * 1024);
    }
    const u32 tdst = lds_addr(&sm.hb[hpos]);
    const u32 sdst = lds_addr(&sm.stat[wave][lane]);
    const u32 ssrc = lds_addr(&sm.stat[(g >> 1) * 4 + ((lane >> 1) & 3)][(lane & 1) * 16]);
    const u32 adst = lds_addr((c < 4 && (g & 1) == 0) ? &sm.actl[g >> 1][wave * 4 + c] : reinterpret_cast<u16*>(&sm.dummy[lane]));
    const u32 asrc = lds_addr(&sm.actl[lane & 1][g * 8]);
    const u32 apa = lds_addr(&sm.hb[(c >> 3) * 512 + (c & 3) * 64 + ((c >> 2) & 1) * 32 + g * 8]);
    u32 xoff = G_XP + g * 2048 + ub * 1024 + (wave & 3) * 256 + c * 16;
    u32x4 one;
#pragma unroll
    for (int k = 0; k < 4; ++k) one[k] = ((c & 2) == 0 && (c >> 2) == k) ? ((c & 1) ? 0x3f800000u : 0x00003f80u) : 0u;
    PIN(one);
    u32 ooff = G_OP + (blk * 512 + tid) * 8;
    float Afp = Af, Bfp = Bf;
    PIN(xoff); PIN(ooff); PIN(Afp); PIN(Bfp);
    PIN(ga); PIN(xin); PIN(cc1);
    u32 w2a = (u32)(uintptr_t)(&sm.w2b[0][0]) + (g * 8 + ((lane >> 2) & 3)) * 1024 + (wave ^ (g & 1)) * 128 + (lane & 15) * 8;
    PIN(w2a);
    if (wave == 1) {
      due = t1 + P.N1 * 256; gate();
      u32x4 gn[2] = {ld16<AUX_SC1>(wsr, G_NT + lane * 16, 0), ld16<AUX_SC1>(wsr, G_NT + lane * 16, 1024)};
      const int tn = notices(IC<2>{}, G_NT + lane * 16, gn) * 256;
      due = (tn > t1 + 2 * 256 ? max(tn, t1 + P.L0 * 256) + P.L1 * 256 : t1) + P.X2 * 256;
      until(due);
    }
    asm volatile("s_waitcnt vmcnt(0)" ::: "memory");
    STAMP(9);
    __builtin_amdgcn_s_barrier();
    STAMP(11);
    CFENCE();
    u32x4 xg[2] = {ld16<AUX_SC1>(wsr, xoff, 0), ld16<AUX_SC1>(wsr, xoff, 8192)};
    SB();
#pragma unroll
    for (int t = 0; t < 4; ++t) {
      const u32x2 lo = tr_read(w2a ^ (t * 32)), hi = tr_read((w2a ^ (t * 32)) + 4096);
      b2[t] = u32x4{lo[0], lo[1], hi[0], hi[1]};
    }
#pragma unroll
    for (int t = 0; t < 4; ++t) PIN(b2[t]);
    SB();
    const f32x4 z4 = {0.f, 0.f, 0.f, 0.f};
    auto xp_bad = [&]() { return !all_ok(max(max(max(xg[0][0], xg[0][1]), max(xg[0][2], xg[0][3])), max(max(xg[1][0], xg[1][1]), max(xg[1][2], xg[1][3]))) != SENT); };
    f32x4 v = mfma16(one, xg[1], mfma16(one, xg[0], z4));
    if (__builtin_expect(xp_bad(), 0)) {
#pragma nounroll
      for (int spin = 0; spin < SPIN_MAX; ++spin) {
        __builtin_amdgcn_s_sleep(1);
        CFENCE();
        xg[0] = ld16<AUX_SC1>(wsr, xoff, 0); xg[1] = ld16<AUX_SC1>(wsr, xoff, 8192);
        if (!xp_bad()) break;
      }
      v = mfma16(one, xg[1], mfma16(one, xg[0], z4));
      SB();
      asm volatile(".Lend_e:");
    }
    STAMP(5); WSTAMP(5);
    {
      const float a0 = v[0], a1 = v[1];
      const float x10 = fmaf(ga[0], a0, xin[0]), x11 = fmaf(ga[1], a1, xin[1]);
      *LDSP(u32, tdst) = pack_bf(x10 * cc1[0], x11 * cc1[1]);
      float u = swap32_add(x10 + x11, fmaf(x10, x10, x11 * x11));
      u += dpp<XOR1>(u); u += dpp<XOR2>(u); u += dpp<XOR7>(u); u += dpp<XOR8>(u);
      *LDSP(float, sdst) = u;
    }
    STAMP(16);
    LDS_BARRIER();
    STAMP(17);
    {
      const f32x4 z = {0.f, 0.f, 0.f, 0.f};
      const u32x4 af0 = LDSP(u32x4, apa)[0], af1 = LDSP(u32x4, apa)[32];
      float s = LDSP(float, ssrc)[0], q = LDSP(float, ssrc)[32];
      SB();
      f32x4 d[4];
#pragma unroll
      for (int j = 0; j < 4; ++j) d[j] = mfma16(af0, bf[j], z);
#pragma unroll
      for (int j = 0; j < 4; ++j) d[j] = mfma16(af1, bf[4 + j], d[j]);
      s += dpp<XOR1>(s); q += dpp<XOR1>(q); s += dpp<XOR2>(s); q += dpp<XOR2>(q); s += dpp<XOR7>(s); q += dpp<XOR7>(q);
      const float mean = s * (1.f / 512.f);
      const float var = fmaf(q, 1.f / 512.f, -mean * mean);
      const float ri = __builtin_amdgcn_rsqf(fmaxf(var, 0.f) + 1e-5f);
      const float cf = fmaf(-ri * mean, Afp, Bfp);
      float y = (d[0][0] + d[1][1]) + (d[2][2] + d[3][3]);
      y = __builtin_bit_cast(float, __builtin_amdgcn_update_dpp(__builtin_bit_cast(int, y), __builtin_bit_cast(int, y), XOR8, 0xa, 0xf, false));
      y = swap16_add(y, y);
      y = fmaf(y, ri, cf);
      const float up = dpp<0x104>(y);
      *LDSP(u16, adst) = (u16)pack_bf(silu_f(y) * up, 0.f);
    }
    STAMP(18);
    LDS_BARRIER();
    STAMP(10);
    {
      const u32x4 af = *LDSP(u32x4, asrc);
      const f32x4 z = {0.f, 0.f, 0.f, 0.f};
      const f32x4 d0 = mfma16(af, b2[0], z), d1 = mfma16(af, b2[1], z), d2 = mfma16(af, b2[2], z), d3 = mfma16(af, b2[3], z);
      const bool g1 = g & 1, g2 = g & 2;
      const float e0 = g1 ? d1[0] : d0[0], e1 = g1 ? d1[1] : d0[1], f0 = g1 ? d3[0] : d2[0], f1 = g1 ? d3[1] : d2[1];
      st8_sc1(wsr, ooff, pack_bf(g2 ? f0 : e0, g2 ? f1 : e1), tag);
    }
    asm volatile(".Lend_b:");
    STAMP(6);
    FINISH();
  }
  if (wave == 0) {
    const int d = fd, ul = lane >> 3;
    u32 gm[2] = {sg[0][0], sg[1][0]};
    if (!all_ok((sg[0][1] == tag) & (sg[1][1] == tag))) gather8<2>(wsr, G_MOD + (1024 + d) * 8, 1536 * 8, tag, gm);
    u32 xa0, xa1;
    {
      due = t1 + P.N2 * 256; gate();
      u32x4 gn[2] = {ld16<AUX_SC1>(wsr, G_NT + lane * 16, 0), ld16<AUX_SC1>(wsr, G_NT + lane * 16, 1024)};
      const int tn = notices(IC<2>{}, G_NT + lane * 16, gn) * 256;
      due = (tn > t1 + 2 * 256 ? max(tn, t1 + P.L0 * 256) + P.L1 * 256 : t1) + P.X3 * 256;
      until(due);
    }
#pragma nounroll
    for (int spin = 0; spin < SPIN_MAX; ++spin) {
      CFENCE();
      xa0 = __builtin_amdgcn_raw_buffer_load_b32(wsr, G_XP + ul * 2048 + (d >> 1) * 4, 0, AUX_SC1);
      xa1 = __builtin_amdgcn_raw_buffer_load_b32(wsr, G_XP + ul * 2048 + (d >> 1) * 4, 1024, AUX_SC1);
      if (all_ok(max(xa0, xa1) != SENT)) break;
      __builtin_amdgcn_s_sleep(1);
    }
    float a0 = (d & 1) ? hi_f(xa0) : lo_f(xa0), a1 = (d & 1) ? hi_f(xa1) : lo_f(xa1);
    a0 += dpp<XOR8>(a0); a1 += dpp<XOR8>(a1);
    a0 = swap16_add(a0, a0); a1 = swap16_add(a1, a1);
    a0 = swap32_add(a0, a0); a1 = swap32_add(a1, a1);
    const bool hb = g & 1;
    float xv = rbf(((d & 1) ? hi_f(sx2) : lo_f(sx2)) + rbf((hb ? hi_f(gm[0]) : lo_f(gm[0])) * rbf(hb ? a1 : a0)));
    float gv = hb ? hi_f(gm[1]) : lo_f(gm[1]);
    PIN(xv); PIN(gv);
    if (wi == 0 && lane == 0) __builtin_amdgcn_raw_buffer_store_b32(tag, wsr, WS_EPOCH, 0, AUX_SC1);
    STAMP(10);
    due = max(due + P.X4 * 256, (int)(((u32)wall_clock64() - t0) << 8) + P.GX * 256);
    const bool last = ul + 40 < NUP;
    u32 o0 = G_OP + (ul * 512 + d) * 8, o5 = last ? o0 : OOB, tg5 = last ? tag : 0u;
    const rsrc_t rout = mk_rsrc(out, 2048u);
    u32 oo = (lane & 40) == 0 ? (g * 512 + d) * 2 : OOB;
    const u32 pk = c == 0 ? 0x00003f80u : (c == 4 ? 0x3f800000u : 0u);
    u32x4 one = {pk, 0u, pk, 0u};
    PIN(o0); PIN(o5); PIN(tg5); PIN(oo); PIN(one);
    __builtin_amdgcn_raw_buffer_store_b16((u16)pack_bf(xv, 0.f), rout, oo, 0, AUX_SC1);
    until(due);
    f32x4 sv;
#pragma nounroll
    for (int spin = 0; spin < SPIN_MAX; ++spin) {
      CFENCE();
      u32x2 og[6];
#pragma unroll
      for (int k = 0; k < 5; ++k) og[k] = ld8<AUX_SC1>(wsr, o0, k * 32768);
      og[5] = ld8<AUX_SC1>(wsr, o5, 5 * 32768);
      sv = f32x4{0.f, 0.f, 0.f, 0.f};
#pragma unroll
      for (int k = 0; k < 3; ++k) sv = mfma16(one, u32x4{og[2 * k][0], og[2 * k][1], og[2 * k + 1][0], og[2 * k + 1][1]}, sv);
      const u32 bad = ((og[0][1] ^ tag) | (og[1][1] ^ tag) | (og[2][1] ^ tag)) | ((og[3][1] ^ tag) | (og[4][1] ^ tag) | (og[5][1] ^ tg5));
      PIN(sv);
      if (all_ok(bad == 0u)) break;
      __builtin_amdgcn_s_sleep(1);
    }
    STAMP(7);
    const float s = sv[0] + dpp<XOR8>(sv[0]);
    __builtin_amdgcn_raw_buffer_store_b16((u16)pack_bf(fmaf(gv, s, xv), 0.f), rout, oo, 0, AUX_SC1);
    asm volatile(".Lend_c:");
  }
  FINISH();
}

extern "C" void kernel_launch(void* const* d_in, const int* in_sizes, int n_in,
                              void* d_out, int out_size, void* d_ws, size_t ws_size,
                              hipStream_t stream) {
  fused_kernel<<<256, 512, 0, stream>>>((const u16*)d_in[1], (const u16*)d_in[2], (const u16*)d_in[4], (const u16*)d_in[3], (const u16*)d_in[10], (const u16*)d_in[6], (const u16*)d_in[7],
                                        (unsigned char*)d_ws, (const u16*)d_in[0], (const u16*)d_in[9], (const u16*)d_in[8], (u16*)d_out
    );
}
```

```cpp
#define PROBE_MUL 10
#include <hip/hip_runtime.h>
#include <stdint.h>

typedef unsigned int u32;
typedef unsigned short u16;
typedef unsigned long long u64;
typedef __attribute__((ext_vector_type(4))) unsigned u32x4;
typedef __attribute__((ext_vector_type(2))) unsigned u32x2;
typedef __attribute__((ext_vector_type(2))) __bf16 bf16x2;
typedef __attribute__((ext_vector_type(4))) float f32x4;
typedef __attribute__((ext_vector_type(2))) float f32x2;
typedef __amdgpu_buffer_rsrc_t rsrc_t;

constexpr int NH = 32, SK = 16384, DH = 128, WIN = 4096, FF = 1376;
struct Prm { int E0, ES, E1, E2, E2C, EU, TEND, DT_UP, L0, L1, X1, X2, X3, X4, NE, FE, M0, M1, M2, M3, M4, GS, GX, N1, N2, GA, WD, DT_WO, LW, LU, EW, PM, NF; };
constexpr Prm PRM0 = {32, 24, 32, 32, 56, 64, 2010, 60, 30, 25, 92, 86, 131, 88, 4, 12, 5, 8, 21, 4, 32, 30, 56, 28, 70, 12, 4, 100, 45, 15, 8, 0x24310, 2};
#define PRM_ARG
constexpr int NL = 37;
constexpr int NCMIN = 192;
constexpr int WK = 8, WV = 5;
constexpr int WUSE = 12, LNS = 7;
constexpr int QP = 14, QD = 4;
constexpr int VQ = 6;
constexpr int NSL = (NL + 7) / 8;
constexpr int VS = VQ + NSL;
constexpr int MLS = VS + 2;
constexpr int VSIDE = MLS - 5, UPREP = MLS + 8;
constexpr int SIDE = NCMIN / 8 - 4;
constexpr int CPF = 18;
constexpr int SMO = 1;
constexpr int TBP = 4, TBL = 11, TBU = 15;
constexpr int cons_end(int v) {
  const int a = v - WV + 1, c = (v - MLS) * 4;
  const int m = a < c ? a : c;
  return m < 0 ? 0 : (m > NL ? NL : m);
}
struct Tab { u32 e[512]; };
constexpr Tab make_tab(const Prm& P) {
  Tab T{};
  for (int bid = 0; bid < 256; ++bid) {
    const int xcd = bid & 7, part = bid >> 4 & 3, i8 = (bid >> 3 & 1) | bid >> 6 << 1, wi = i8 * 8 + xcd;
    const bool has_mod = wi < 48, has_up = wi >= 21;
    const int cb0 = (i8 + 5) & 7, cb1 = (i8 + 4) & 7, ms = wi - 20;
    const int mi = wi < 8 ? wi : (wi < 16 ? 8 + ((xcd + 4) & 7) : (wi < 20 ? (wi - 14) * 8 + 3 : 16 + ms / 7 * 8 + ms % 7 + (ms % 7 >= 3 ? 1 : 0)));
    const int ui = xcd * 5 + (xcd > 5 ? xcd - 5 : 0) + (i8 >= 3 ? (xcd >= 5 ? 1 : 0) + i8 - 3 : 0);
    const int wd = cb0 == 3 ? 0 : P.WD;
    const int e[4] = {P.E0 - 4 * wd + (cb0 == 3 ? P.ES : 0), P.E1 + (cb1 == 3 ? P.ES : 0), has_mod ? (wi < 16 ? P.E2C : P.E2) + ((mi & 7) == 3 ? P.ES : 0) : 0, has_up ? P.EU : 0};
    const int ew = wd ? P.EW : 0, pa = (1024 + e[0] + ew + e[1] + e[2] + e[3]) / 4;
    int c[5] = {0, 0, 0, 0, 124};
    for (int k = 0; k < 3; ++k) c[k + 1] = c[k] + (pa - e[k] - (k == 0 ? ew : 0) + 4) / 8 - 1;
    const int nl = c[part + 1] - c[part] + 1, wst = e[part] / 4;
    const bool has_w = part < 2 || (part == 2 ? has_mod : has_up);
    const int blk = !has_w ? 0 : (part == 0 ? cb0 : (part == 1 ? xcd * 8 + cb1 : (part == 2 ? mi : ui)));
    const int tau = (part == 3 && has_up ? P.TEND - P.DT_UP : (part == 0 && wd ? P.TEND - P.DT_WO : P.TEND)) * 256 / (2 * nl + wst);
    T.e[2 * bid] = (u32)(c[part] | nl << 7 | blk << 13 | (has_w ? 1 : 0) << 19 | (part == 0 ? wd : 0) << 20);
    T.e[2 * bid + 1] = (u32)(tau | (tau * wst / (part == 0 ? 8 - wd : 8)) << 16);
  }
  return T;
}
constexpr int step_range(const Prm& P, bool mx_) {
  int mx = 0, mn = 1 << 20;
  const Tab T = make_tab(P);
  for (int bid = 0; bid < 256; ++bid) { const int n = T.e[2 * bid] >> 7 & 63; mx = n > mx ? n : mx; mn = n < mn ? n : mn; }
  return mx_ ? mx : mn;
}
extern "C" __attribute__((used, visibility("protected"))) __constant__ const Tab wg_tab = make_tab(PRM0);
static_assert(step_range(PRM0, true) <= NL && step_range(PRM0, false) >= NCMIN / 8, "a wave's key range does not fit its load steps");
static_assert(cons_end(SIDE + WV + 1) <= NCMIN / 8 && cons_end(UPREP) <= NCMIN / 8 && SIDE > MLS + 3, "steps every wave runs");
constexpr int NUP = FF / 32;
constexpr int SPIN_MAX = 1 << 17;

constexpr u32 WS_EPOCH = 0;
constexpr u32 G_TB = 1024;
constexpr u32 G_NT = 2048;
constexpr u32 G_MOD = 4096;
constexpr u32 G_Q = G_MOD + 3072 * 8;
constexpr u32 G_ML = G_Q + 4096 * 8;
constexpr u32 G_P = G_ML + 256 * 16;
constexpr u32 G_XP = G_P + 256 * 256;
constexpr u32 G_OP = G_XP + 8 * 512 * 4;
constexpr u32 WS_END = G_OP + 43 * 512 * 8;
constexpr u32 SENT = 0xffffffffu;
constexpr u32 OOB = 0x40000000u;
#define WST 0

constexpr int AUX_NT = 2, AUX_SC1 = 16;

__device__ __forceinline__ float lo_f(u32 v) { return __uint_as_float(v << 16); }
__device__ __forceinline__ float hi_f(u32 v) { return __uint_as_float(v & 0xffff0000u); }
__device__ __forceinline__ float rbf(float x) { return (float)(__bf16)x; }
__device__ __forceinline__ u32 pack_bf(float lo, float hi) {
  bf16x2 r; r[0] = (__bf16)lo; r[1] = (__bf16)hi; return __builtin_bit_cast(u32, r);
}
__device__ __forceinline__ float dot2(u32 k, u32 q, float acc) {
  return __builtin_amdgcn_fdot2_f32_bf16(__builtin_bit_cast(bf16x2, k), __builtin_bit_cast(bf16x2, q), acc, false);
}
template <int CTRL> __device__ __forceinline__ float dpp(float x) {
  return __builtin_bit_cast(float, __builtin_amdgcn_mov_dpp(__builtin_bit_cast(int, x), CTRL, 0xf, 0xf, true));
}
constexpr int XOR1 = 0xB1, XOR2 = 0x4E, XOR7 = 0x141, XOR8 = 0x128;
__device__ __forceinline__ float swap32_add(float a, float b) {
  auto t = __builtin_amdgcn_permlane32_swap(__float_as_uint(a), __float_as_uint(b), false, false);
  return __uint_as_float(t[0]) + __uint_as_float(t[1]);
}
__device__ __forceinline__ float swap16_add(float a, float b) {
  auto t = __builtin_amdgcn_permlane16_swap(__float_as_uint(a), __float_as_uint(b), false, false);
  return __uint_as_float(t[0]) + __uint_as_float(t[1]);
}
__device__ __forceinline__ float wave_sum(float x) {
  x += dpp<XOR1>(x); x += dpp<XOR2>(x); x += dpp<XOR7>(x); x += dpp<XOR8>(x);
  x = swap16_add(x, x);
  return swap32_add(x, x);
}
__device__ __forceinline__ float wave_max(float x) {
  x = fmaxf(x, dpp<XOR1>(x)); x = fmaxf(x, dpp<XOR2>(x)); x = fmaxf(x, dpp<XOR7>(x)); x = fmaxf(x, dpp<XOR8>(x));
  auto s = __builtin_amdgcn_permlane16_swap(__float_as_uint(x), __float_as_uint(x), false, false);
  x = fmaxf(__uint_as_float(s[0]), __uint_as_float(s[1]));
  auto t = __builtin_amdgcn_permlane32_swap(__float_as_uint(x), __float_as_uint(x), false, false);
  return fmaxf(__uint_as_float(t[0]), __uint_as_float(t[1]));
}
template <int CTRL> __device__ __forceinline__ float sel_add(float lo, float hi, bool up) {
  const float t1 = lo + dpp<CTRL>(lo), t2 = hi + dpp<CTRL>(hi);
  return up ? t2 : t1;
}
template <int I> __device__ __forceinline__ float bc16(float x) {
  return __builtin_bit_cast(float, __builtin_amdgcn_update_dpp(0, __builtin_bit_cast(int, x), 0x150 + I, 0xf, 0xf, false));
}
__device__ __forceinline__ float bcast16(float x, int i) {
  switch (i) {
    case 0: return bc16<0>(x); case 1: return bc16<1>(x); case 2: return bc16<2>(x); case 3: return bc16<3>(x);
    case 4: return bc16<4>(x); case 5: return bc16<5>(x); case 6: return bc16<6>(x); case 7: return bc16<7>(x);
    case 8: return bc16<8>(x); case 9: return bc16<9>(x); case 10: return bc16<10>(x); case 11: return bc16<11>(x);
    case 12: return bc16<12>(x); case 13: return bc16<13>(x); case 14: return bc16<14>(x); default: return bc16<15>(x);
  }
}
__device__ __forceinline__ float silu_f(float x) { return x * __builtin_amdgcn_rcpf(1.f + __expf(-x)); }

template <int AUX> __device__ __forceinline__ u32x4 ld16(rsrc_t r, u32 voff, u32 soff) {
  return __builtin_amdgcn_raw_buffer_load_b128(r, voff, soff, AUX);
}
template <int AUX> __device__ __forceinline__ u32x2 ld8(rsrc_t r, u32 voff, u32 soff) {
  return __builtin_amdgcn_raw_buffer_load_b64(r, voff, soff, AUX);
}
__device__ __forceinline__ void st8_sc1(rsrc_t r, u32 voff, u32 v, u32 tag) {
  __builtin_amdgcn_raw_buffer_store_b64(u32x2{v, tag}, r, voff, 0, AUX_SC1);
}
__device__ __forceinline__ void st16_sc1(rsrc_t r, u32 voff, u32 v0, u32 v1, u32 tag) {
  __builtin_amdgcn_raw_buffer_store_b128(u32x4{v0, tag, v1, tag}, r, voff, 0, AUX_SC1);
}
__device__ __forceinline__ rsrc_t mk_rsrc(const void* p, u32 bytes) {
  return __builtin_amdgcn_make_buffer_rsrc(const_cast<void*>(p), 0, bytes, 0x00020000);
}
#define WAIT_VM(n) __builtin_amdgcn_s_waitcnt(((n) & 15) | (((n) >> 4) << 14) | (7 << 4) | (15 << 8))
#define SB() __builtin_amdgcn_sched_barrier(0)
#define CFENCE() asm volatile("" ::: "memory")
__device__ __forceinline__ bool all_ok(bool ok) { return __builtin_amdgcn_ballot_w64(!ok) == 0ull; }

template <int N> __device__ __forceinline__ void gather16(rsrc_t ws, u32 voff, u32 stride, u32 tag, u32x4 (&g)[N]) {
#pragma nounroll
  for (int spin = 0; spin < SPIN_MAX; ++spin) {
    CFENCE();
    bool ok = true;
#pragma unroll
    for (int i = 0; i < N; ++i) g[i] = ld16<AUX_SC1>(ws, voff + i * stride, 0);
#pragma unroll
    for (int i = 0; i < N; ++i) ok &= (g[i][1] == tag) & (g[i][3] == tag);
    if (all_ok(ok)) break;
    __builtin_amdgcn_s_sleep(8);
  }
}
template <int N> __device__ __forceinline__ void gather8(rsrc_t ws, u32 voff, u32 stride, u32 tag, u32 (&g)[N], bool act = true) {
#pragma nounroll
  for (int spin = 0; spin < SPIN_MAX; ++spin) {
    CFENCE();
    bool ok = true;
    u32x2 t[N];
#pragma unroll
    for (int i = 0; i < N; ++i) t[i] = ld8<AUX_SC1>(ws, voff + i * stride, 0);
#pragma unroll
    for (int i = 0; i < N; ++i) { ok &= (t[i][1] == tag); g[i] = t[i][0]; }
    if (all_ok(ok | !act)) break;
    __builtin_amdgcn_s_sleep(8);
  }
}

struct Smem {
  float red[8][128];
  float ml[8][2];
  float r2[64 * 20];
  u16 hb[3 * 1024];
  float stat[8][64];
  float red2[8][128];
  u32 dummy[64];
  u32x2 bc;
  u32x4 zero[4];
  u32 mflag[8];
  u32x4 wbuf[8][8][64];
  __attribute__((aligned(256))) u16 w2b[32][512];
  u16 actl[2][32];
};

#define PROBE_K 8
#define PROBE_OP 0
#define PROBE_PART -1
#define STAMP(k) do { if ((k) == PROBE_K && tid == 0) pst = wall_clock64(); } while (0)
#define WSTAMP(k) do {} while (0)
#define FINISH() do { STAMP(8); if (tid == 0) { __hip_atomic_store(reinterpret_cast<u64*>(ws + (1u << 20)) + bid * 2, pt0, __ATOMIC_RELAXED, __HIP_MEMORY_SCOPE_AGENT); \
   __hip_atomic_store(reinterpret_cast<u64*>(ws + (1u << 20)) + bid * 2 + 1, (PROBE_PART < 0 || part == PROBE_PART) ? pst : 0ull, __ATOMIC_RELAXED, __HIP_MEMORY_SCOPE_AGENT); } \
   if (bid == 8 && wave == 0) probe_end(ws, pt0, lane); return; } while (0)
#define PIN(x) asm volatile("" : "+v"(x))
#define LDS_BARRIER() do { __builtin_amdgcn_s_waitcnt(0xc07f); __builtin_amdgcn_s_barrier(); CFENCE(); } while (0)
#define WAVE_SYNC() do { __builtin_amdgcn_fence(__ATOMIC_RELEASE, "wavefront"); __builtin_amdgcn_wave_barrier(); __builtin_amdgcn_fence(__ATOMIC_ACQUIRE, "wavefront"); } while (0)

__device__ __forceinline__ void dma16(rsrc_t r, u32 voff, u32 soff, u32 lds_dst) {
  u32 keep;
  asm volatile("s_mov_b32 %0, m0\n\ts_mov_b32 m0, %4\n\ts_nop 4\n\tbuffer_load_dwordx4 %1, %2, %3 offen nt lds\n\ts_mov_b32 m0, %0"
               : "=&s"(keep) : "v"(voff), "s"(r), "s"(soff), "s"(lds_dst) : "memory");
}
__device__ __forceinline__ u32 perm_lo(u32 a, u32 b) { return __builtin_amdgcn_perm(b, a, 0x05040100u); }
__device__ __forceinline__ u32 perm_hi(u32 a, u32 b) { return __builtin_amdgcn_perm(b, a, 0x07060302u); }
__device__ __forceinline__ float rdl(float x, int l) { return __int_as_float(__builtin_amdgcn_readlane(__float_as_int(x), l)); }

typedef __attribute__((ext_vector_type(8))) __bf16 bf16x8;
__device__ __forceinline__ f32x4 mfma16(u32x4 a, u32x4 b, f32x4 c) {
  return __builtin_amdgcn_mfma_f32_16x16x32_bf16(__builtin_bit_cast(bf16x8, a), __builtin_bit_cast(bf16x8, b), c, 0, 0, 0);
}
__device__ __forceinline__ float sum8(const float* p) {
  const f32x4 a = *reinterpret_cast<const f32x4*>(p), b = *reinterpret_cast<const f32x4*>(p + 4);
  return ((a[0] + b[0]) + (a[1] + b[1])) + ((a[2] + b[2]) + (a[3] + b[3]));
}
__device__ __forceinline__ float ln_ri(float var) {
  return __builtin_amdgcn_rcpf(rbf(__builtin_amdgcn_sqrtf(rbf(rbf(fmaxf(var, 0.f)) + 1e-5f))));
}
__device__ __forceinline__ void ln_stats2(const float (&x0)[8], const float (&x1)[8], float& mb0, float& ri0, float& mb1, float& ri1) {
  const float k0 = rdl(x0[0], 0), k1 = rdl(x1[0], 0);
  float s0 = 0.f, s1 = 0.f, q0 = 0.f, q1 = 0.f;
#pragma unroll
  for (int k = 0; k < 8; ++k) {
    const float d0 = x0[k] - k0, d1 = x1[k] - k1;
    s0 += d0; s1 += d1; q0 = fmaf(d0, d0, q0); q1 = fmaf(d1, d1, q1);
  }
  float t = swap16_add(swap32_add(s0, s1), swap32_add(q0, q1));
  t += dpp<XOR1>(t); t += dpp<XOR2>(t); t += dpp<XOR7>(t); t += dpp<XOR8>(t);
  auto u = __builtin_amdgcn_permlane16_swap(__float_as_uint(t), __float_as_uint(t), false, false);
  const float ms = __uint_as_float(u[0]) * (1.f / 512.f), mq = __uint_as_float(u[1]) * (1.f / 512.f);
  const float ri = ln_ri(fmaf(-ms, ms, mq));
  mb0 = rbf(k0 + rdl(ms, 0)); mb1 = rbf(k1 + rdl(ms, 32));
  ri0 = rdl(ri, 0); ri1 = rdl(ri, 32);
}
template <int I> struct IC { static constexpr int v = I; };
template <int I, int N, class F> __device__ __forceinline__ void sfor(F&& f) {
  if constexpr (I < N) { if (f(IC<I>{})) sfor<I + 1, N>(f); }
}
typedef __attribute__((ext_vector_type(4))) short s16x4;
typedef __attribute__((address_space(3))) s16x4* lds_s16x4p;
__device__ __forceinline__ u32x2 tr_read(u32 lds_addr) {
  return __builtin_bit_cast(u32x2, __builtin_amdgcn_ds_read_tr16_b64_v4i16((lds_s16x4p)lds_addr));
}
__device__ __forceinline__ u32 next_tag(u32 e) { return ((e | 0x40004000u) + 1u) & ~0x40004000u; }
__device__ __forceinline__ float u2f(u32 v) { return __uint_as_float(v); }
#define LDSP(T, a) ((__attribute__((address_space(3))) T*)(a))
template <class T> __device__ __forceinline__ u32 lds_addr(T* p) { u32 a = (u32)(uintptr_t)p; PIN(a); return a; }

#define TAB_LOAD "s_add_u32 s30, s28, wg_tab@rel32@lo+4\ns_addc_u32 s31, s29, wg_tab@rel32@hi+12\nglobal_load_dwordx2 %[te], %[va], s[30:31]\n"
__device__ __forceinline__ void probe_end(unsigned char* ws, u64 pt0, int lane) {
  while (wall_clock64() < pt0 + 5500ull) __builtin_amdgcn_s_sleep(8);
  u64 a[4], c2[4];
  for (int i = 0; i < 4; ++i) {
    a[i] = __hip_atomic_load(reinterpret_cast<u64*>(ws + (1u << 20)) + (i * 64 + lane) * 2, __ATOMIC_RELAXED, __HIP_MEMORY_SCOPE_AGENT);
    c2[i] = __hip_atomic_load(reinterpret_cast<u64*>(ws + (1u << 20)) + (i * 64 + lane) * 2 + 1, __ATOMIC_RELAXED, __HIP_MEMORY_SCOPE_AGENT);
  }
  int mn0 = 1 << 30, mx1 = -(1 << 30), mn1 = 1 << 30, sum = 0, cnt = 0;
  for (int i = 0; i < 4; ++i) {
    const int d0 = (int)(a[i] - pt0);
    mn0 = min(mn0, d0);
    if (c2[i] != 0ull) { const int d1 = (int)(c2[i] - pt0); mx1 = max(mx1, d1); mn1 = min(mn1, d1); sum += d1; cnt += 1; }
  }
  for (int o = 1; o < 64; o <<= 1) { mn0 = min(mn0, __shfl_xor(mn0, o)); mx1 = max(mx1, __shfl_xor(mx1, o)); mn1 = min(mn1, __shfl_xor(mn1, o)); sum += __shfl_xor(sum, o); cnt += __shfl_xor(cnt, o); }
  int X = PROBE_OP == 0 ? mx1 : (PROBE_OP == 1 ? sum / max(cnt, 1) : mn1);
  if (PROBE_K == 102) X = mn0;
  X = mn0 + (X - mn0) * PROBE_MUL;
  const u64 tg = pt0 + (u64)(long long)(X + 8000);
  for (int i = 0; i < 400000 && wall_clock64() < tg; ++i) __builtin_amdgcn_s_sleep(1);
}
__global__ void __launch_bounds__(512, 2) fused_kernel(const u16* kc_, const u16* vc_, const u16* wo_, const u16* wq_, const u16* wmod_, const u16* w1_, const u16* w3_,
                                                       unsigned char* ws_, const u16* x_, const u16* cnd_, const u16* w2_, u16* out_ PRM_ARG) {
  __shared__ __attribute__((aligned(256))) Smem sm;
  const u64 a_ka = (u64)__builtin_amdgcn_kernarg_segment_ptr();
  const int bid = blockIdx.x, tid = threadIdx.x;
  u64 wa, t64; u32x2 te; int tid0;
  u32 ar[14];
  u32 tch2, sa_, sb_, va_;
  u32x4 kr[NL], vr[NL];
  asm volatile(
    "s_load_dwordx16 s[4:19], %[ka], 0x0\ns_load_dwordx8 s[20:27], %[ka], 0x40\n"
    "s_memrealtime %[t]\n"
    "v_lshlrev_b32 %[va], 3, %[bid]\n"
    "s_getpc_b64 s[28:29]\n.Lcpf_first:\n"
    TAB_LOAD
    "s_cmp_gt_u32 %[bid], 15\ns_cbranch_scc1 .Lno_cpf\n"
    "s_mov_b32 %[sa], .Lend_a-.Lcpf_first\ns_max_i32 %[sa], %[sa], .Lend_b-.Lcpf_first\ns_max_i32 %[sa], %[sa], .Lend_c-.Lcpf_first\ns_max_i32 %[sa], %[sa], .Lend_d-.Lcpf_first\ns_max_i32 %[sa], %[sa], .Lend_e-.Lcpf_first\n"
    "v_lshlrev_b32 %[va], 6, %[tid]\nv_min_u32 %[va], %[sa], %[va]\nglobal_load_dword %[pf], %[va], s[28:29]\n"
    ".Lno_cpf:\n"
    "s_and_b32 %[sa], %[bid], 15\ns_lshr_b32 %[sb], %[bid], 6\ns_lshl2_add_u32 %[sa], %[sa], %[sb]\ns_lshl_b32 %[sa], %[sa], 22\ns_xor_b32 %[sa], %[sa], 0xff0000\n"
    "s_and_b32 %[sb], %[bid], 48\nv_lshl_add_u32 %[va], %[sb], 5, %[tid]\nv_lshl_add_u32 %[va], %[va], 4, %[sa]\n"
    "v_readfirstlane_b32 %[t0], %[tid]\n"
    "s_waitcnt lgkmcnt(0)\nglobal_load_dwordx4 %[k0], %[va], s[4:5] nt\nglobal_load_dwordx4 %[v0], %[va], s[6:7] nt\n"
    "s_bitcmp1_b32 %[t0], 6\ns_cselect_b64 s[14:15], s[16:17], s[14:15]\n"
    "s_bitcmp1_b32 %[bid], 4\ns_cselect_b64 s[8:9], s[10:11], s[8:9]\ns_cselect_b64 s[12:13], s[14:15], s[12:13]\ns_bitcmp1_b32 %[bid], 5\ns_cselect_b64 %[wa], s[12:13], s[8:9]"
    : "=&{s4}"(ar[0]), "=&{s5}"(ar[1]), "=&{s6}"(ar[2]), "=&{s7}"(ar[3]), "=&{s18}"(ar[4]), "=&{s19}"(ar[5]), "=&{s20}"(ar[6]), "=&{s21}"(ar[7]), "=&{s22}"(ar[8]), "=&{s23}"(ar[9]),
      "=&{s24}"(ar[10]), "=&{s25}"(ar[11]), "=&{s26}"(ar[12]), "=&{s27}"(ar[13]), [wa] "=&s"(wa), [t0] "=&s"(tid0), [t] "=&s"(t64), [te] "=&v"(te), [sa] "=&s"(sa_), [sb] "=&s"(sb_),
      [va] "=&v"(va_), [pf] "=&v"(tch2), [k0] "=&v"(kr[0]), [v0] "=&v"(vr[0])
    : [ka] "s"(a_ka), [bid] "s"(bid), [tid] "v"(tid) : "s8", "s9", "s10", "s11", "s12", "s13", "s14", "s15", "s16", "s17", "s28", "s29", "s30", "s31", "scc", "memory");
  asm volatile("" : "+s"(ar[0]), "+s"(ar[1]), "+s"(ar[2]), "+s"(ar[3]), "+s"(ar[4]), "+s"(ar[5]), "+s"(ar[6]), "+s"(ar[7]), "+s"(ar[8]), "+s"(ar[9]), "+s"(ar[10]), "+s"(ar[11]), "+s"(ar[12]), "+s"(ar[13]));
  asm volatile("" : "+s"(wa), "+s"(t64), "+s"(tid0));
  const u32 tka = (u32)wall_clock64();
  const u64 pt0 = t64; u64 pst = (PROBE_K == 100 || PROBE_K == 102) ? pt0 : (PROBE_K == 101 ? pt0 + (u32)(tka - (u32)t64) : 0);
  const u32 t0 = (u32)t64, te0_ = 0;
  __builtin_assume(tid >= 0 && tid < 512 && tid0 >= 0 && tid0 < 512 && bid >= 0 && bid < 256);
  constexpr Prm P = PRM0;
  const int TEND = P.TEND;
  const int xcd = bid & 7, part = bid >> 4 & 3;
  const int lane = tid & 63, wave = tid0 >> 6;
  SB();
  const bool p3 = part == 3;
  u32 pitch = (u32)(0x005600c001000020ull >> (part * 16) & 0xffff) * 32u;
  const int sh = p3 ? 2 : 3;
  u32 wv0 = (u32)((p3 ? (wave >> 1) * 128 : wave * 64) + (part == 0 ? xcd * 512 : 0) + (lane >> sh)) * pitch + ((lane & (p3 ? 3 : 7)) ^ (lane >> (p3 ? 5 : 3))) * 16u;
  const u32 wimg = (u32)(uintptr_t)(&sm.wbuf[0][0][0]);
  u32 wsz = (part == 0 ? 4096u : 512u) * pitch, wld = wimg + (p3 ? (wave & 1) * 32768 + (wave >> 1) * 8192 : wave * 8192);
  PIN(wv0);
  asm volatile("" : "+s"(wsz), "+s"(wld), "+s"(pitch));
  auto karg = [&](int i) { return ar[2 * i] | (u64)ar[2 * i + 1] << 32; };
  const u64 a_kc = karg(0), a_vc = karg(1);
  unsigned char* ws = (unsigned char*)karg(2);
  const u16 *x = (const u16*)karg(3), *cnd = (const u16*)karg(4), *w2 = (const u16*)karg(5);
  u16* out = (u16*)karg(6);
  const rsrc_t wsr = mk_rsrc(ws, WS_END);
  asm volatile("s_waitcnt vmcnt(2)" : "+v"(te));
  const u32 te0 = __builtin_amdgcn_readfirstlane(te[0]), te1 = __builtin_amdgcn_readfirstlane(te[1]);
  const int bl0 = te0 & 127, nl = te0 >> 7 & 63, blk = te0 >> 13 & 63;
  const bool has_w = te0 >> 19 & 1;
  const rsrc_t rw = mk_rsrc((const void*)wa, wsz);
  const u32 wstep = pitch << (6 - sh), wflip = p3 ? 32u : 0u;
  u32 wso = blk << (sh + 4);
  int wleft = 8;
  auto piece = [&]() {
    dma16(rw, wv0, wso, wld);
    wso += wstep; wld += 1024;
    wv0 ^= (wleft & 1) ? wflip : 0u;
    --wleft;
  };
  SB();
  STAMP(23);
  const int b = bid >> 3 & 1, head = xcd * 4 + (bid >> 6 ^ 3);
  const int g = lane >> 4, c = lane & 15;
  const int i8 = b | bid >> 6 << 1, wi = i8 * 8 + xcd;
  const bool is_up = p3 && has_w, modc = part == 2 && wi < 16;
  const size_t base = ((size_t)(b * NH + head) * SK + (SK - WIN)) * DH;
  const rsrc_t rk = mk_rsrc((const u16*)a_kc + base, WIN * DH * 2), rv = mk_rsrc((const u16*)a_vc + base, WIN * DH * 2);
  int tau = te1 & 0xffff;
  const int tauw = te1 >> 16;
  int due = P.NF * tau, now = 0;
  auto gate = [&]() {
    while (now < due) { now = (int)(((u32)wall_clock64() - t0) << 8); if (now < due) __builtin_amdgcn_s_sleep(1); }
    due += tau;
  };
  auto until = [&](int t) { while ((int)(((u32)wall_clock64() - t0) << 8) < t); };
  const u32 pslot = G_P + ((b * 32 + head) * 4 + part) * 256;
  u32x2 ep; u32 xs0, xs1, tch1; u32x4 xr0, xr1;
  const rsrc_t rx = mk_rsrc(part == 2 ? cnd : x, (part == 1 || (part == 2 && has_w)) ? 2048u : 0u);
  auto small_ops = [&]() {
    ep = ld8<AUX_SC1>(wsr, wave == 7 ? WS_EPOCH : OOB, 0);
    xs0 = __builtin_amdgcn_raw_buffer_load_b16(rx, tid * 2, 0, 0); xs1 = __builtin_amdgcn_raw_buffer_load_b16(rx, tid * 2, 1024, 0);
    xr0 = ld16<0>(rx, part == 1 ? lane * 16 : OOB, 0); xr1 = ld16<0>(rx, part == 1 ? lane * 16 : OOB, 1024);
    tch1 = __builtin_amdgcn_raw_buffer_load_b32(mk_rsrc(w2 + (size_t)blk * 32 * 512, is_up ? 64u : 0u), tid == 384 ? 0 : OOB, 0, AUX_NT);
  };
  if (has_w) {
    if (modc) now = 1 << 30;
#pragma nounroll
    while (wleft > (int)(te0 >> 20)) { gate(); due += tauw - tau; piece(); }
    now = 0;
  }
  SB();
  u32 sot;
  {
    const u32 s1 = bl0 + lane - 1, s0 = s1 + (s1 >= 108 ? 4 : 0);
    u32 sb = s0 & 96u;
#pragma unroll
    for (int k = 0; k < 5; ++k) sb |= (s0 >> k & 1u) << (P.PM >> (4 * k) & 7);
    const u32 src = (sb >> 5 & 3u) | (u32)(b * 32 + head) << 2;
    sb ^= ((__builtin_popcount(src & P.M0) & 1) | (__builtin_popcount(src & P.M1) & 1) << 1 | (__builtin_popcount(src & P.M2) & 1) << 2 | (__builtin_popcount(src & P.M3) & 1) << 3 | (__builtin_popcount(src & P.M4) & 1) << 4) & (s1 >= 96 ? 7u : 31u);
    sot = (sb & 7u) << 10 | (u32)wave << 13 | (sb >> 3) << 16;
    sot = s1 >= 120 ? (s1 + 4) << 13 | (u32)wave << 10 : sot;
    sot = lane < nl ? sot : OOB;
    PIN(sot);
  }
  u32 tag = 0;
  u32x4 bf[8];
  auto wo_prep = [&](int s0 = 0, int s1 = 2) {
    const u32 wi_ = wimg + wave * 8192;
    const int q = (lane >> 2) & 3, pp = lane & 3;
#pragma unroll
    for (int s = s0; s < s1; ++s) {
#pragma unroll
      for (int t = 0; t < 4; ++t) {
        const int r0 = s * 32 + g * 8 + q, r1 = r0 + 4, ch = t * 2 + (pp >> 1);
        const u32x2 lo = tr_read(wi_ + r0 * 128 + ((ch ^ (r0 & 7)) * 16) + (pp & 1) * 8), hi = tr_read(wi_ + r1 * 128 + ((ch ^ (r1 & 7)) * 16) + (pp & 1) * 8);
        bf[s * 4 + t] = u32x4{lo[0], lo[1], hi[0], hi[1]};
      }
    }
  };
  auto gemv_go = [&](u32 hh) {
    sm.hb[tid] = (u16)hh; sm.hb[512 + tid] = (u16)(hh >> 16);
    WAVE_SYNC();
    const u32x4* a = (c & 11) == 0 ? reinterpret_cast<const u32x4*>(&sm.hb[(c >> 2) * 512 + wave * 64 + g * 8]) : &sm.zero[0];
    const u32x4 a0 = a[0], a1 = a[(c & 11) == 0 ? 4 : 0];
    const f32x4 z = {0.f, 0.f, 0.f, 0.f};
    f32x4 d[4];
#pragma unroll
    for (int t = 0; t < 4; ++t) d[t] = mfma16(a0, bf[t], z);
#pragma unroll
    for (int t = 0; t < 4; ++t) d[t] = mfma16(a1, bf[4 + t], d[t]);
    if (lane < 32) {
#pragma unroll
      for (int t = 0; t < 4; ++t) sm.r2[(t * 16 + c) * 20 + g * 8 + wave] = d[t][0];
    }
  };
  auto mod_stage = [&]() {
    wo_prep();
    gemv_go(pack_bf(silu_f(lo_f(xs0)), silu_f(lo_f(xs1))));
    __builtin_amdgcn_s_waitcnt(0xc07f);
    if (lane == 0) *LDSP(volatile u32, (u32)(uintptr_t)&sm.mflag[wave]) = 1u;
    if (wave == 7) {
#pragma nounroll
      for (int spin = 0; spin < SPIN_MAX; ++spin) {
        const u32 f = *LDSP(volatile u32, (u32)(uintptr_t)&sm.mflag[lane & 7]);
        if (all_ok(f != 0u)) break;
        __builtin_amdgcn_s_sleep(1);
      }
      CFENCE();
      st8_sc1(wsr, G_MOD + (blk * 64 + lane) * 8, pack_bf(sum8(&sm.r2[lane * 20]), sum8(&sm.r2[lane * 20 + 8])), tag);
    }
    STAMP(9);
  };
  if (lane < 4) sm.zero[lane] = u32x4{0u, 0u, 0u, 0u};
  if (part == 2 && has_w) {
    if (tid < 8) sm.mflag[tid] = 0u;
    LDS_BARRIER();
    if (__builtin_expect(modc, 0)) {
      small_ops();
      __builtin_amdgcn_raw_buffer_store_b128(u32x4{SENT, SENT, SENT, SENT}, wsr, tid < 320 ? G_P + wi * 5120 + tid * 16 : OOB, 0, AUX_SC1);
      asm volatile("s_waitcnt vmcnt(0)" ::: "memory");
      tag = next_tag(__builtin_amdgcn_readfirstlane(ep[0]));
      mod_stage();
      __builtin_amdgcn_s_barrier();
    }
  }
  SB();
  const int fd = wi * 8 + (lane & 7);
  const int ub = wave >> 2, ud = (wave & 3) * 128 + c * 8 + g * 2;
  u32x4 sg[3]; u32 sx2;
  auto side_loads = [&]() {
    const bool fin = part == 1 && wave == 0;
    sg[0] = ld16<AUX_SC1>(wsr, is_up ? G_MOD + (1024 + ud) * 8 : (fin ? G_MOD + (1024 + fd) * 8 : OOB), 0);
    sg[1] = ld16<AUX_SC1>(wsr, is_up ? G_MOD + (1536 + ud) * 8 : (fin ? G_MOD + (2560 + fd) * 8 : OOB), 0);
    sg[2] = ld16<AUX_SC1>(wsr, is_up ? G_MOD + (2048 + ud) * 8 : OOB, 0);
    sx2 = __builtin_amdgcn_raw_buffer_load_b32(mk_rsrc(x, (is_up || fin) ? 2048u : 0u), is_up ? ub * 1024 + ud * 2 : (g & 1) * 1024 + (fd >> 1) * 4, 0, 0);
  };
  auto side_pin = [&]() { PIN(sg[0]); PIN(sg[1]); PIN(sg[2]); PIN(sx2); };
  const u32 mlo = G_ML + (head * 2 + b) * 64 + (lane & 3) * 16;
  float fmrg = 0.f;
  bool fok = wave != 0;
  auto merge_factor = [&](const u32x4& ml) {
    const float mi = u2f(ml[0]);
    float M = fmaxf(mi, dpp<XOR1>(mi)); M = fmaxf(M, dpp<XOR2>(M));
    const float e = __expf(mi - M);
    float L = e * u2f(ml[2]);
    L += dpp<XOR1>(L); L += dpp<XOR2>(L);
    fmrg = rdl(e * __builtin_amdgcn_rcpf(L), part);
  };
  f32x2 ga = {0.f, 0.f}, xin = {0.f, 0.f}, cc1 = {0.f, 0.f};
  float Af = 0.f, Bf = 0.f;
  const int hpos = ub * 512 + ud;
  auto up_prep = [&]() {
    if (!all_ok((sg[0][1] == tag) & (sg[0][3] == tag) & (sg[1][1] == tag) & (sg[1][3] == tag) & (sg[2][1] == tag) & (sg[2][3] == tag))) gather16<3>(wsr, G_MOD + (1024 + ud) * 8, 512 * 8, tag, sg);
    ga = f32x2{ub ? hi_f(sg[0][0]) : lo_f(sg[0][0]), ub ? hi_f(sg[0][2]) : lo_f(sg[0][2])};
    cc1 = f32x2{rbf(1.f + (ub ? hi_f(sg[2][0]) : lo_f(sg[2][0]))), rbf(1.f + (ub ? hi_f(sg[2][2]) : lo_f(sg[2][2])))};
    xin = f32x2{lo_f(sx2), hi_f(sx2)};
    u32* hp = reinterpret_cast<u32*>(&sm.hb[hpos]);
    hp[512] = pack_bf(cc1[0], cc1[1]);
    hp[1024] = ub ? perm_hi(sg[1][0], sg[1][2]) : perm_lo(sg[1][0], sg[1][2]);
  };
  auto up_frags = [&]() {
    const int pp = lane & 3;
    const u32 a = wimg + (pp & 1) * 32768 + ((pp >> 1) * 32 + g * 8 + ((lane >> 2) & 3)) * 64 + (((wave >> 1) ^ ((g & 1) | (pp & 2))) * 16) + (wave & 1) * 8;
#pragma unroll
    for (int rb = 0; rb < 8; ++rb) {
      const u32x2 lo = tr_read(a + rb * 4096), hi = tr_read(a + rb * 4096 + 256);
      bf[rb] = u32x4{lo[0], lo[1], hi[0], hi[1]};
    }
  };
  const u32x4* ap = reinterpret_cast<const u32x4*>(&sm.hb[((lane >> 3) & 1) * 512 + (lane & 1) * 32 + g * 8]);
  const bool par = lane & 8;
  float l0 = 0.f, l1 = 0.f;
  u32x2 gmd[2], tbg;
  int t1 = 0;
#pragma unroll
  for (int i = NCMIN / 8; i < NL; ++i) kr[i] = u32x4{0u, 0u, 0u, 0u};
  sfor<0, NL>([&](auto ic) {
    constexpr int i = ic.v;
    if (i >= NCMIN / 8 && i >= nl) return false;
    if constexpr (i == 0) return true;
    const u32 so = __builtin_amdgcn_readlane(sot, i);
    gate();
    SB();
    kr[i] = ld16<AUX_NT>(rk, lane * 16, so);
    if constexpr (i == SMO) small_ops();
    if constexpr (i == TBL) tbg = ld8<AUX_SC1>(wsr, wave == 7 ? G_TB + xcd * 128 + (lane & 7) * 8 : OOB, 0);
    if constexpr (i == QP) { gmd[0] = ld8<AUX_SC1>(wsr, part == 1 ? G_MOD + tid * 8 : OOB, 0); gmd[1] = ld8<AUX_SC1>(wsr, part == 1 ? G_MOD + tid * 8 : OOB, 4096); }
    SB();
    SB();
    if (i >= WK) { WAIT_VM(WK); SB(); }
    if (i == 1) STAMP(25);
    if (i == 8) STAMP(12);
    if (i == 20) STAMP(21);
    if (i == 25) STAMP(22);
    if (i == TBP) {
      if (part == 3 && i8 == 0 && wave == 7) { tag = next_tag(__builtin_amdgcn_readfirstlane(ep[0])); if (lane < 8) st8_sc1(wsr, G_TB + lane * 128 + xcd * 8, t0 + (((tka - t0) * P.GA) >> 3) - P.GS, tag); }
      SB();
    }
    if (i == TBU) {
      if (wave == 7) {
        const bool ok = tbg[1] == tag;
        float lf = ok ? (float)min(max((int)(t0 - tbg[0]), -300), 300) : 0.f, nf = ok ? 1.f : 0.f;
        lf += dpp<XOR1>(lf); nf += dpp<XOR1>(nf); lf += dpp<XOR2>(lf); nf += dpp<XOR2>(nf); lf += dpp<XOR7>(lf); nf += dpp<XOR7>(nf);
        if (lane == 0) sm.bc = u32x2{tag, t0 - (u32)(int)(lf * __builtin_amdgcn_rcpf(fmaxf(nf, 1.f)))};
      }
      LDS_BARRIER();
      const u32x2 bc = sm.bc;
      tag = __builtin_amdgcn_readfirstlane(bc[0]);
      t1 = (__builtin_amdgcn_readfirstlane((int)(bc[1] - t0)) + TEND + P.X1) * 256;
      tau -= __builtin_amdgcn_readfirstlane((int)((float)(int)(t0 - bc[1]) * 256.f * __builtin_amdgcn_rcpf((float)(2 * nl - TBU - 3))));
      SB();
    }
    if (i == LNS) {
      tag = next_tag(__builtin_amdgcn_readfirstlane(ep[0]));
      if (part == 1) {
        float x0[8], x1[8], mb0, ri0, mb1, ri1;
#pragma unroll
        for (int k = 0; k < 4; ++k) { x0[2 * k] = lo_f(xr0[k]); x0[2 * k + 1] = hi_f(xr0[k]); x1[2 * k] = lo_f(xr1[k]); x1[2 * k + 1] = hi_f(xr1[k]); }
        ln_stats2(x0, x1, mb0, ri0, mb1, ri1);
        l0 = rbf(rbf(lo_f(xs0) - mb0) * ri0); l1 = rbf(rbf(lo_f(xs1) - mb1) * ri1);
      }
      PIN(l0); PIN(l1);
      SB();
    }
    if (i == WUSE) {
      PIN(tch1); PIN(tch2);
      if ((part == 1 || (part == 2 && has_w && !modc))) {
        WAIT_VM(WK); CFENCE();
        if (part == 2) mod_stage(); else wo_prep();
      }
#pragma unroll
      for (int k = 0; k < 8; ++k) PIN(bf[k]);
      SB();
    }
    if (i == QP + QD) {
      if (part == 1) {
        u32 gm[2] = {gmd[0][0], gmd[1][0]};
        if (!all_ok((gmd[0][1] == tag) & (gmd[1][1] == tag))) { STAMP(11); gather8<2>(wsr, G_MOD + tid * 8, 512 * 8, tag, gm); }
        STAMP(5);
        gemv_go(pack_bf(rbf(l0 * rbf(1.f + lo_f(gm[1]))) + lo_f(gm[0]), rbf(l1 * rbf(1.f + hi_f(gm[1]))) + hi_f(gm[0])));
      }
      SB();
    }
    if (i == QP + QD + 1) {
      if (part == 1) {
        LDS_BARRIER();
        if (wave == 0) {
          const int bb = lane >> 5, jq = lane & 31;
          st8_sc1(wsr, G_Q + (bb * 2048 + blk * 32 + jq) * 8, pack_bf(sum8(&sm.r2[2 * jq * 20 + bb * 8]), sum8(&sm.r2[(2 * jq + 1) * 20 + bb * 8])), tag);
        }
        STAMP(6);
      }
      SB();
    }
    return true;
  });
  PIN(sot);
  const u32 qoff = G_Q + ((b * 32 + head) * 64 + c * 4) * 8;
  u32x4 qg[2], fl2[3];
  u32 qq[4];
  constexpr int NB = (NL + 15) / 16;
  float xlo, sc[NB], pr[NB], m = 0.f;
  f32x4 acc = {0.f, 0.f, 0.f, 0.f};
  u32 pb[NB], pm[4], cpf;
  const u32 adst0 = lds_addr(lane < 32 ? &sm.red[wave][c * 8 + g * 4] : &sm.r2[(lane - 32) * 4]);
  u32 po, rdst, rsrc, xo;
  auto wo_addr = [&]() {
    po = G_P + (((c >> 2 & 1) * 32 + xcd * 4 + (wave >> 1)) * 4 + (c & 3)) * 256 + (wave & 1) * 128 + (c >> 3) * 64 + g * 16;
    rdst = lane < 32 ? (u32)(uintptr_t)&sm.red2[wave][0] + lane * 16 : (u32)(uintptr_t)&sm.r2[0] + (lane - 32) * 16;
    rsrc = (u32)(uintptr_t)&sm.red2[0][((wave & 1) * 16 + (lane & 15)) * 4 + (lane >> 4)];
    xo = (lane & 1) ? OOB : G_XP + xcd * 2048 + (wave & 1) * 1024 + (blk * 64 + lane) * 2;
    PIN(po); PIN(rdst); PIN(rsrc); PIN(xo);
  };
  u32 pso = wave == 0 ? pslot + lane * 4 : OOB;
  PIN(pso);
  const u32 nadd = (te0 >> 20) ? P.LW : (is_up ? P.LU : 0);
  u32 nto = tid == 0 ? G_NT + bid * 8 : OOB;
  PIN(nto);
  auto notices = [&](auto nc_, u32 off, u32x4 (&gn)[nc_.v]) -> int {
    constexpr int N = nc_.v;
#pragma nounroll
    for (int spin = 0; spin < SPIN_MAX; ++spin) {
      bool ok = true;
#pragma unroll
      for (int i = 0; i < N; ++i) ok &= (gn[i][1] == tag) & (gn[i][3] == tag);
      if (all_ok(ok)) break;
      __builtin_amdgcn_s_sleep(10);
      CFENCE();
#pragma unroll
      for (int i = 0; i < N; ++i) gn[i] = ld16<AUX_SC1>(wsr, off, i * 1024);
    }
    int mx = 0;
#pragma unroll
    for (int i = 0; i < N; ++i) mx = max(mx, max((int)(gn[i][0] - t0), (int)(gn[i][2] - t0)));
    return __builtin_amdgcn_readfirstlane((int)wave_max((float)min(mx, 1 << 20)));
  };
  const int side = nl >= SIDE + 13 ? SIDE + 8 : (nl >= SIDE + 9 ? SIDE + 4 : SIDE);
  sfor<0, NL + WV>([&](auto ic) {
    constexpr int v = ic.v;
    if (cons_end(v - 1) >= NCMIN / 8 && cons_end(v - 1) >= nl) return false;
    if constexpr (v > 0 && v < NL) {
      const u32 so = __builtin_amdgcn_readlane(sot, v);
      if constexpr (v >= 12) { if (v == nl - P.NE) tau = (tau * P.FE) >> 4; }
      if constexpr (v >= NCMIN / 8) { if (v < nl) gate(); } else gate();
      SB();
      vr[v] = ld16<AUX_NT>(rv, lane * 16, so);
      SB();
      SB();
    }
    if constexpr (v == NCMIN / 8 - 1) {
#pragma unroll
      for (int i = NCMIN / 8; i < NL; ++i) asm volatile("" : "=v"(vr[i]));
      asm volatile("" : "=v"(fl2[1]), "=v"(fl2[2]));
    }
    if constexpr (v == VQ - 4) { qg[0] = ld16<AUX_SC1>(wsr, qoff, 0); qg[1] = ld16<AUX_SC1>(wsr, qoff + 16, 0); }
    if constexpr (v == VSIDE) side_loads();
    if constexpr (v == CPF) {
      u64 pc; u32 dlt, len;
      asm volatile("s_getpc_b64 %0\n.Lcpf_here:\n\ts_mov_b32 %1, .Ltail_start-.Lcpf_here\n\ts_mov_b32 %2, .Lend_a-.Ltail_start\n\ts_max_i32 %2, %2, .Lend_b-.Ltail_start\n\ts_max_i32 %2, %2, .Lend_c-.Ltail_start\n\ts_max_i32 %2, %2, .Lend_d-.Ltail_start\n\ts_max_i32 %2, %2, .Lend_e-.Ltail_start" : "=s"(pc), "=s"(dlt), "=s"(len) :: "scc");
      pc += dlt;
      const rsrc_t rc = mk_rsrc((const void*)pc, (part == 3 && i8 == 1) ? len : 0u);
      cpf = __builtin_amdgcn_raw_buffer_load_b32(rc, tid * 64, 0, 0);
    }
    if constexpr (v == CPF + WV + 1) PIN(cpf);
    if constexpr (v == SIDE || v == SIDE + 4 || v == SIDE + 8) fl2[(v - SIDE) / 4] = ld16<AUX_SC1>(wsr, (wave == 0 && v == side) ? mlo : OOB, 0);
    SB();
    if constexpr (v < WV) { WAIT_VM(WK); SB(); } else if constexpr (v < NL && cons_end(v) < v - WV + 1) { WAIT_VM(WV); SB(); }
    if constexpr (v == VQ) {
      if (!all_ok((qg[0][1] == tag) & (qg[0][3] == tag) & (qg[1][1] == tag) & (qg[1][3] == tag))) { STAMP(20); gather16<2>(wsr, qoff, 16, tag, qg); }
      STAMP(1); WSTAMP(1);
      qq[0] = qg[0][0]; qq[1] = qg[0][2]; qq[2] = qg[1][0]; qq[3] = qg[1][2];
    }
    if constexpr (v >= VQ && v < VS) {
      constexpr int sl = v - VQ, bt = sl >> 1;
      float xs[8];
#pragma unroll
      for (int i = 0; i < 8; ++i) {
        float d = 0.f;
        if (sl * 8 + i < NL) {
#pragma unroll
          for (int k = 0; k < 4; ++k) d = dot2(kr[sl * 8 + i][k], qq[k], d);
        }
        xs[i] = d;
      }
#pragma unroll
      for (int h = 0; h < 4; ++h) xs[h] = sel_add<XOR7>(xs[h], xs[h + 4], c & 4);
#pragma unroll
      for (int h = 0; h < 2; ++h) xs[h] = sel_add<XOR2>(xs[h], xs[h + 2], c & 2);
      xs[0] = sel_add<XOR1>(xs[0], xs[1], c & 1);
      if constexpr ((sl & 1) || sl == NSL - 1) {
        const float t = (sl & 1) ? sel_add<XOR8>(xlo, xs[0], c & 8) : xs[0] + dpp<XOR8>(xs[0]);
        const float s = rbf(rbf(t) * 0.08838834764831845f);
        sc[bt] = (bt * 16 + c < nl) ? s : -__builtin_inff();
        PIN(sc[bt]);
      } else {
        xlo = xs[0];
        PIN(xlo);
      }
    }
    if constexpr (v == VS) {
      float mloc = sc[0];
#pragma unroll
      for (int bt = 1; bt < NB; ++bt) mloc = fmaxf(mloc, sc[bt]);
      m = fmaxf(wave_max(mloc), -3.0e38f);
      float ls = 0.f;
#pragma unroll
      for (int bt = 0; bt < NB; ++bt) { pr[bt] = __expf(sc[bt] - m); ls += pr[bt]; }
      const float l = wave_sum(ls);
      if (lane == 0) { sm.ml[wave][0] = m; sm.ml[wave][1] = l; }
      STAMP(2); WSTAMP(2);
    }
    sfor<cons_end(v - 1), cons_end(v)>([&](auto jc) {
      constexpr int i = jc.v;
      if (i < NCMIN / 8 || i < nl) {
        const u32 pv = __float_as_uint(bcast16(u2f(pb[i >> 4]), i & 15));
        acc = mfma16(u32x4{pv & pm[0], pv & pm[1], pv & pm[2], pv & pm[3]}, vr[i], acc);
      }
      PIN(acc);
      return true;
    });
    if constexpr (v == MLS - 1) { side_pin(); if (is_up) up_prep(); PIN(ga); PIN(xin); PIN(cc1); }
    if constexpr (v == UPREP - 1) wo_addr();
    if constexpr (v == UPREP) {
#pragma unroll
      for (int k = 0; k < 8; ++k) asm volatile("" : "=v"(bf[k]));
      if (part == 0) wo_prep(0, 1); else if (is_up) up_frags();
#pragma unroll
      for (int k = 0; k < 8; ++k) PIN(bf[k]);
    }
    if constexpr (v == UPREP + 1) {
      if (is_up) {
        const u32x4* a1 = ap + 128 + ((lane >> 1) & 1) * 128;
        f32x4 d0 = {0.f, 0.f, 0.f, 0.f}, d1 = {0.f, 0.f, 0.f, 0.f};
#pragma unroll
        for (int rb = 0; rb < 4; ++rb) { d0 = mfma16(a1[rb * 8], bf[rb], d0); d1 = mfma16(a1[(rb + 4) * 8], bf[rb + 4], d1); }
        Af = par ? d0[1] + d1[1] : d0[0] + d1[0]; Bf = par ? d0[3] + d1[3] : d0[2] + d1[2];
        Af += dpp<XOR8>(Af); Bf += dpp<XOR8>(Bf);
      }
      PIN(Af); PIN(Bf);
    }
    if constexpr (v == SIDE + WV + 1 || v == SIDE + WV + 5 || v == SIDE + WV + 9) {
      if (wave == 0 && v == side + WV + 1) {
        const u32x4 ml = fl2[(v - SIDE - WV - 1) / 4];
        fok = all_ok((ml[1] == tag) & (ml[3] == tag));
        merge_factor(ml);
      }
    }
    if (v % 6 == 4 && v >= 10 && v < 30) STAMP(15 + v / 6);
    if constexpr (v == MLS) {
      LDS_BARRIER();
      float mm[8], ll[8];
#pragma unroll
      for (int w = 0; w < 8; ++w) { const f32x2 t = *reinterpret_cast<const f32x2*>(&sm.ml[w][0]); mm[w] = t[0]; ll[w] = t[1]; }
      float Mwg = mm[0], Lwg = 0.f;
#pragma unroll
      for (int w = 1; w < 8; ++w) Mwg = fmaxf(Mwg, mm[w]);
      if (wave == 0) {
#pragma unroll
        for (int w = 0; w < 8; ++w) Lwg = fmaf(__expf(mm[w] - Mwg), ll[w], Lwg);
        if (lane == 0) st16_sc1(wsr, G_ML + ((head * 2 + b) * 4 + part) * 16, __float_as_uint(Mwg), __float_as_uint(Lwg), tag);
      }
      const float fw = __expf(m - Mwg);
#pragma unroll
      for (int bt = 0; bt < NB; ++bt) { pb[bt] = pack_bf(pr[bt] * fw, pr[bt] * fw); PIN(pb[bt]); }
#pragma unroll
      for (int k = 0; k < 4; ++k) { pm[k] = (c >> 1) == k ? ((c & 1) ? 0xffff0000u : 0x0000ffffu) : 0u; PIN(pm[k]); }
    }
    SB();
    return true;
  });
  asm volatile(".Ltail_start:");
  *LDSP(f32x4, adst0) = acc;
  STAMP(14); WSTAMP(14);
  if (!fok) {
    u32x4 ml;
#pragma nounroll
    for (int spin = 0; spin < SPIN_MAX; ++spin) {
      CFENCE();
      ml = ld16<AUX_SC1>(wsr, mlo, 0);
      if (all_ok((ml[1] == tag) & (ml[3] == tag))) break;
    }
    merge_factor(ml);
  }
  STAMP(15);
  LDS_BARRIER();
  STAMP(3);
  {
    u32 pw = 0;
    if (wave == 0) {
      f32x2 t[8];
#pragma unroll
      for (int w = 0; w < 8; ++w) t[w] = *reinterpret_cast<const f32x2*>(&sm.red[w][2 * lane]);
      const f32x2 o = ((t[0] + t[1]) + (t[2] + t[3])) + ((t[4] + t[5]) + (t[6] + t[7]));
      pw = min(pack_bf(o[0] * fmrg, o[1] * fmrg), SENT - 1u);
    }
    __builtin_amdgcn_raw_buffer_store_b32(pw, wsr, pso, 0, AUX_SC1);
    st8_sc1(wsr, nto, (u32)wall_clock64() + nadd, tag);
    SB();
#pragma unroll
    for (int i = NCMIN / 8; i < NL; ++i) PIN(vr[i]);
    PIN(fl2[0]); PIN(fl2[1]); PIN(fl2[2]);
  }
  STAMP(4);

  if (part == 2) FINISH();
  if (part == 0) {
    __builtin_amdgcn_s_barrier();
#pragma nounroll
    while (wleft > 0) piece();
    until(t1);
    STAMP(11);
    CFENCE();
    u32x4 og[2];
    og[0] = ld16<AUX_SC1>(wsr, po, 0);
    SB();
    WAIT_VM(1); CFENCE();
    wo_prep(1, 2);
#pragma unroll
    for (int k = 4; k < 8; ++k) PIN(bf[k]);
    SB();
    f32x4 d[4];
    auto wo_mm = [&]() {
      const f32x4 z = {0.f, 0.f, 0.f, 0.f};
#pragma unroll
      for (int t = 0; t < 4; ++t) { d[t] = mfma16(og[0], bf[t], z); og[1][t] = __float_as_uint(dpp<XOR8>(u2f(og[0][t]))); }
      SB();
#pragma unroll
      for (int t = 0; t < 4; ++t) d[t] = mfma16(og[1], bf[4 + t], d[t]);
    };
    auto p_bad = [&]() { return !all_ok(max(max(og[0][0], og[0][1]), max(og[0][2], og[0][3])) != SENT); };
    wo_mm();
    if (__builtin_expect(p_bad(), 0)) {
#pragma nounroll
      for (int spin = 0; spin < SPIN_MAX; ++spin) {
        __builtin_amdgcn_s_sleep(1);
        CFENCE();
        og[0] = ld16<AUX_SC1>(wsr, po, 0);
        if (!p_bad()) break;
      }
      wo_mm();
      SB();
      asm volatile(".Lend_d:");
    }
    STAMP(5); WSTAMP(5);
    {
      SB();
      f32x4 y; f32x2 h[4];
#pragma unroll
      for (int t = 0; t < 4; ++t) { h[t] = d[t].xy + d[t].zw; PIN(h[t]); }
#pragma unroll
      for (int t = 0; t < 4; ++t) { y[t] = h[t][0] + h[t][1]; PIN(y[t]); }
      *LDSP(f32x4, rdst) = y;
    }
    STAMP(16);
    LDS_BARRIER();
    STAMP(17);
    if (wave < 2) {
      f32x2 v[4];
#pragma unroll
      for (int w = 0; w < 4; ++w) v[w] = f32x2{LDSP(float, rsrc)[w * 256], LDSP(float, rsrc)[w * 256 + 128]};
      const f32x2 h = (v[0] + v[1]) + (v[2] + v[3]);
      const float y = h[0] + h[1];
      __builtin_amdgcn_raw_buffer_store_b32(min(pack_bf(y, dpp<XOR1>(y)), SENT - 1u), wsr, xo, 0, AUX_SC1);
      asm volatile(".Lend_a:");
    }
    STAMP(6);
    FINISH();
  }
  if (part == 3) {
    if (!has_w) FINISH();
    u32x4 b2[4];
    {
      __builtin_amdgcn_s_barrier();
      const rsrc_t r2 = mk_rsrc(w2 + (size_t)(blk * 32 + wave * 4) * 512, 4096u);
      const u32 l2 = (u32)(uintptr_t)(&sm.w2b[wave * 4][0]);
#pragma unroll
      for (int n = 0; n < 4; ++n) dma16(r2, (lane ^ (n * 2) ^ ((wave & 2) << 2)) * 16, n * 1024, l2 + n * 1024);
    }
    const u32 tdst = lds_addr(&sm.hb[hpos]);
    const u32 sdst = lds_addr(&sm.stat[wave][lane]);
    const u32 ssrc = lds_addr(&sm.stat[(g >> 1) * 4 + ((lane >> 1) & 3)][(lane & 1) * 16]);
    const u32 adst = lds_addr((c < 4 && (g & 1) == 0) ? &sm.actl[g >> 1][wave * 4 + c] : reinterpret_cast<u16*>(&sm.dummy[lane]));
    const u32 asrc = lds_addr(&sm.actl[lane & 1][g * 8]);
    const u32 apa = lds_addr(&sm.hb[(c >> 3) * 512 + (c & 3) * 64 + ((c >> 2) & 1) * 32 + g * 8]);
    u32 xoff = G_XP + g * 2048 + ub * 1024 + (wave & 3) * 256 + c * 16;
    u32x4 one;
#pragma unroll
    for (int k = 0; k < 4; ++k) one[k] = ((c & 2) == 0 && (c >> 2) == k) ? ((c & 1) ? 0x3f800000u : 0x00003f80u) : 0u;
    PIN(one);
    u32 ooff = G_OP + (blk * 512 + tid) * 8;
    float Afp = Af, Bfp = Bf;
    PIN(xoff); PIN(ooff); PIN(Afp); PIN(Bfp);
    PIN(ga); PIN(xin); PIN(cc1);
    u32 w2a = (u32)(uintptr_t)(&sm.w2b[0][0]) + (g * 8 + ((lane >> 2) & 3)) * 1024 + (wave ^ (g & 1)) * 128 + (lane & 15) * 8;
    PIN(w2a);
    if (wave == 1) {
      due = t1 + P.N1 * 256; gate();
      u32x4 gn[2] = {ld16<AUX_SC1>(wsr, G_NT + lane * 16, 0), ld16<AUX_SC1>(wsr, G_NT + lane * 16, 1024)};
      const int tn = notices(IC<2>{}, G_NT + lane * 16, gn) * 256;
      due = (tn > t1 + 2 * 256 ? max(tn, t1 + P.L0 * 256) + P.L1 * 256 : t1) + P.X2 * 256;
      until(due);
    }
    asm volatile("s_waitcnt vmcnt(0)" ::: "memory");
    STAMP(9);
    __builtin_amdgcn_s_barrier();
    STAMP(11);
    CFENCE();
    u32x4 xg[2] = {ld16<AUX_SC1>(wsr, xoff, 0), ld16<AUX_SC1>(wsr, xoff, 8192)};
    SB();
#pragma unroll
    for (int t = 0; t < 4; ++t) {
      const u32x2 lo = tr_read(w2a ^ (t * 32)), hi = tr_read((w2a ^ (t * 32)) + 4096);
      b2[t] = u32x4{lo[0], lo[1], hi[0], hi[1]};
    }
#pragma unroll
    for (int t = 0; t < 4; ++t) PIN(b2[t]);
    SB();
    const f32x4 z4 = {0.f, 0.f, 0.f, 0.f};
    auto xp_bad = [&]() { return !all_ok(max(max(max(xg[0][0], xg[0][1]), max(xg[0][2], xg[0][3])), max(max(xg[1][0], xg[1][1]), max(xg[1][2], xg[1][3]))) != SENT); };
    f32x4 v = mfma16(one, xg[1], mfma16(one, xg[0], z4));
    if (__builtin_expect(xp_bad(), 0)) {
#pragma nounroll
      for (int spin = 0; spin < SPIN_MAX; ++spin) {
        __builtin_amdgcn_s_sleep(1);
        CFENCE();
        xg[0] = ld16<AUX_SC1>(wsr, xoff, 0); xg[1] = ld16<AUX_SC1>(wsr, xoff, 8192);
        if (!xp_bad()) break;
      }
      v = mfma16(one, xg[1], mfma16(one, xg[0], z4));
      SB();
      asm volatile(".Lend_e:");
    }
    STAMP(5); WSTAMP(5);
    {
      const float a0 = v[0], a1 = v[1];
      const float x10 = fmaf(ga[0], a0, xin[0]), x11 = fmaf(ga[1], a1, xin[1]);
      *LDSP(u32, tdst) = pack_bf(x10 * cc1[0], x11 * cc1[1]);
      float u = swap32_add(x10 + x11, fmaf(x10, x10, x11 * x11));
      u += dpp<XOR1>(u); u += dpp<XOR2>(u); u += dpp<XOR7>(u); u += dpp<XOR8>(u);
      *LDSP(float, sdst) = u;
    }
    STAMP(16);
    LDS_BARRIER();
    STAMP(17);
    {
      const f32x4 z = {0.f, 0.f, 0.f, 0.f};
      const u32x4 af0 = LDSP(u32x4, apa)[0], af1 = LDSP(u32x4, apa)[32];
      float s = LDSP(float, ssrc)[0], q = LDSP(float, ssrc)[32];
      SB();
      f32x4 d[4];
#pragma unroll
      for (int j = 0; j < 4; ++j) d[j] = mfma16(af0, bf[j], z);
#pragma unroll
      for (int j = 0; j < 4; ++j) d[j] = mfma16(af1, bf[4 + j], d[j]);
      s += dpp<XOR1>(s); q += dpp<XOR1>(q); s += dpp<XOR2>(s); q += dpp<XOR2>(q); s += dpp<XOR7>(s); q += dpp<XOR7>(q);
      const float mean = s * (1.f / 512.f);
      const float var = fmaf(q, 1.f / 512.f, -mean * mean);
      const float ri = __builtin_amdgcn_rsqf(fmaxf(var, 0.f) + 1e-5f);
      const float cf = fmaf(-ri * mean, Afp, Bfp);
      float y = (d[0][0] + d[1][1]) + (d[2][2] + d[3][3]);
      y = __builtin_bit_cast(float, __builtin_amdgcn_update_dpp(__builtin_bit_cast(int, y), __builtin_bit_cast(int, y), XOR8, 0xa, 0xf, false));
      y = swap16_add(y, y);
      y = fmaf(y, ri, cf);
      const float up = dpp<0x104>(y);
      *LDSP(u16, adst) = (u16)pack_bf(silu_f(y) * up, 0.f);
    }
    STAMP(18);
    LDS_BARRIER();
    STAMP(10);
    {
      const u32x4 af = *LDSP(u32x4, asrc);
      const f32x4 z = {0.f, 0.f, 0.f, 0.f};
      const f32x4 d0 = mfma16(af, b2[0], z), d1 = mfma16(af, b2[1], z), d2 = mfma16(af, b2[2], z), d3 = mfma16(af, b2[3], z);
      const bool g1 = g & 1, g2 = g & 2;
      const float e0 = g1 ? d1[0] : d0[0], e1 = g1 ? d1[1] : d0[1], f0 = g1 ? d3[0] : d2[0], f1 = g1 ? d3[1] : d2[1];
      st8_sc1(wsr, ooff, pack_bf(g2 ? f0 : e0, g2 ? f1 : e1), tag);
    }
    asm volatile(".Lend_b:");
    STAMP(6);
    FINISH();
  }
  if (wave == 0) {
    const int d = fd, ul = lane >> 3;
    u32 gm[2] = {sg[0][0], sg[1][0]};
    if (!all_ok((sg[0][1] == tag) & (sg[1][1] == tag))) gather8<2>(wsr, G_MOD + (1024 + d) * 8, 1536 * 8, tag, gm);
    u32 xa0, xa1;
    {
      due = t1 + P.N2 * 256; gate();
      u32x4 gn[2] = {ld16<AUX_SC1>(wsr, G_NT + lane * 16, 0), ld16<AUX_SC1>(wsr, G_NT + lane * 16, 1024)};
      const int tn = notices(IC<2>{}, G_NT + lane * 16, gn) * 256;
      due = (tn > t1 + 2 * 256 ? max(tn, t1 + P.L0 * 256) + P.L1 * 256 : t1) + P.X3 * 256;
      until(due);
    }
#pragma nounroll
    for (int spin = 0; spin < SPIN_MAX; ++spin) {
      CFENCE();
      xa0 = __builtin_amdgcn_raw_buffer_load_b32(wsr, G_XP + ul * 2048 + (d >> 1) * 4, 0, AUX_SC1);
      xa1 = __builtin_amdgcn_raw_buffer_load_b32(wsr, G_XP + ul * 2048 + (d >> 1) * 4, 1024, AUX_SC1);
      if (all_ok(max(xa0, xa1) != SENT)) break;
      __builtin_amdgcn_s_sleep(1);
    }
    float a0 = (d & 1) ? hi_f(xa0) : lo_f(xa0), a1 = (d & 1) ? hi_f(xa1) : lo_f(xa1);
    a0 += dpp<XOR8>(a0); a1 += dpp<XOR8>(a1);
    a0 = swap16_add(a0, a0); a1 = swap16_add(a1, a1);
    a0 = swap32_add(a0, a0); a1 = swap32_add(a1, a1);
    const bool hb = g & 1;
    float xv = rbf(((d & 1) ? hi_f(sx2) : lo_f(sx2)) + rbf((hb ? hi_f(gm[0]) : lo_f(gm[0])) * rbf(hb ? a1 : a0)));
    float gv = hb ? hi_f(gm[1]) : lo_f(gm[1]);
    PIN(xv); PIN(gv);
    if (wi == 0 && lane == 0) __builtin_amdgcn_raw_buffer_store_b32(tag, wsr, WS_EPOCH, 0, AUX_SC1);
    STAMP(10);
    due = max(due + P.X4 * 256, (int)(((u32)wall_clock64() - t0) << 8) + P.GX * 256);
    const bool last = ul + 40 < NUP;
    u32 o0 = G_OP + (ul * 512 + d) * 8, o5 = last ? o0 : OOB, tg5 = last ? tag : 0u;
    const rsrc_t rout = mk_rsrc(out, 2048u);
    u32 oo = (lane & 40) == 0 ? (g * 512 + d) * 2 : OOB;
    const u32 pk = c == 0 ? 0x00003f80u : (c == 4 ? 0x3f800000u : 0u);
    u32x4 one = {pk, 0u, pk, 0u};
    PIN(o0); PIN(o5); PIN(tg5); PIN(oo); PIN(one);
    __builtin_amdgcn_raw_buffer_store_b16((u16)pack_bf(xv, 0.f), rout, oo, 0, AUX_SC1);
    until(due);
    f32x4 sv;
#pragma nounroll
    for (int spin = 0; spin < SPIN_MAX; ++spin) {
      CFENCE();
      u32x2 og[6];
#pragma unroll
      for (int k = 0; k < 5; ++k) og[k] = ld8<AUX_SC1>(wsr, o0, k * 32768);
      og[5] = ld8<AUX_SC1>(wsr, o5, 5 * 32768);
      sv = f32x4{0.f, 0.f, 0.f, 0.f};
#pragma unroll
      for (int k = 0; k < 3; ++k) sv = mfma16(one, u32x4{og[2 * k][0], og[2 * k][1], og[2 * k + 1][0], og[2 * k + 1][1]}, sv);
      const u32 bad = ((og[0][1] ^ tag) | (og[1][1] ^ tag) | (og[2][1] ^ tag)) | ((og[3][1] ^ tag) | (og[4][1] ^ tag) | (og[5][1] ^ tg5));
      PIN(sv);
      if (all_ok(bad == 0u)) break;
      __builtin_amdgcn_s_sleep(1);
    }
    STAMP(7);
    const float s = sv[0] + dpp<XOR8>(sv[0]);
    __builtin_amdgcn_raw_buffer_store_b16((u16)pack_bf(fmaf(gv, s, xv), 0.f), rout, oo, 0, AUX_SC1);
    asm volatile(".Lend_c:");
  }
  FINISH();
}

extern "C" void kernel_launch(void* const* d_in, const int* in_sizes, int n_in,
                              void* d_out, int out_size, void* d_ws, size_t ws_size,
                              hipStream_t stream) {
  fused_kernel<<<256, 512, 0, stream>>>((const u16*)d_in[1], (const u16*)d_in[2], (const u16*)d_in[4], (const u16*)d_in[3], (const u16*)d_in[10], (const u16*)d_in[6], (const u16*)d_in[7],
                                        (unsigned char*)d_ws, (const u16*)d_in[0], (const u16*)d_in[9], (const u16*)d_in[8], (u16*)d_out
    );
}
```

```cpp
#define PROBE_MUL 10
#include <hip/hip_runtime.h>
#include <stdint.h>

typedef unsigned int u32;
typedef unsigned short u16;
typedef unsigned long long u64;
typedef __attribute__((ext_vector_type(4))) unsigned u32x4;
typedef __attribute__((ext_vector_type(2))) unsigned u32x2;
typedef __attribute__((ext_vector_type(2))) __bf16 bf16x2;
typedef __attribute__((ext_vector_type(4))) float f32x4;
typedef __attribute__((ext_vector_type(2))) float f32x2;
typedef __amdgpu_buffer_rsrc_t rsrc_t;

constexpr int NH = 32, SK = 16384, DH = 128, WIN = 4096, FF = 1376;
struct Prm { int E0, ES, E1, E2, E2C, EU, TEND, DT_UP, L0, L1, X1, X2, X3, X4, NE, FE, M0, M1, M2, M3, M4, GS, GX, N1, N2, GA, WD, DT_WO, LW, LU, EW, PM, NF; };
constexpr Prm PRM0 = {32, 24, 32, 32, 56, 64, 2030, 60, 30, 25, 92, 86, 131, 88, 4, 12, 5, 8, 21, 4, 32, 30, 56, 28, 70, 12, 4, 100, 45, 15, 8, 0x24310, 2};
constexpr int NL = 37;
constexpr int NCMIN = 192;
constexpr int WK = 8, WV = 5;
constexpr int WUSE = 12, LNS = 7;
constexpr int QP = 14, QD = 4;
constexpr int VQ = 6;
constexpr int NSL = (NL + 7) / 8;
constexpr int VS = VQ + NSL;
constexpr int MLS = VS + 2;
constexpr int VSIDE = MLS - 5, UPREP = MLS + 8;
constexpr int SIDE = NCMIN / 8 - 4;
constexpr int CPF = 18;
constexpr int SMO = 1;
constexpr int TBP = 4, TBL = 11, TBU = 15;
constexpr int cons_end(int v) {
  const int a = v - WV + 1, c = (v - MLS) * 4;
  const int m = a < c ? a : c;
  return m < 0 ? 0 : (m > NL ? NL : m);
}
struct Tab { u32 e[512]; };
constexpr Tab make_tab(const Prm& P) {
  Tab T{};
  for (int bid = 0; bid < 256; ++bid) {
    const int xcd = bid & 7, part = bid >> 4 & 3, i8 = (bid >> 3 & 1) | bid >> 6 << 1, wi = i8 * 8 + xcd;
    const bool has_mod = wi < 48, has_up = wi >= 21;
    const int cb0 = (i8 + 5) & 7, cb1 = (i8 + 4) & 7, ms = wi - 20;
    const int mi = wi < 8 ? wi : (wi < 16 ? 8 + ((xcd + 4) & 7) : (wi < 20 ? (wi - 14) * 8 + 3 : 16 + ms / 7 * 8 + ms % 7 + (ms % 7 >= 3 ? 1 : 0)));
    const int ui = xcd * 5 + (xcd > 5 ? xcd - 5 : 0) + (i8 >= 3 ? (xcd >= 5 ? 1 : 0) + i8 - 3 : 0);
    const int wd = cb0 == 3 ? 0 : P.WD;
    const int e[4] = {P.E0 - 4 * wd + (cb0 == 3 ? P.ES : 0), P.E1 + (cb1 == 3 ? P.ES : 0), has_mod ? (wi < 16 ? P.E2C : P.E2) + ((mi & 7) == 3 ? P.ES : 0) : 0, has_up ? P.EU : 0};
    const int ew = wd ? P.EW : 0, pa = (1024 + e[0] + ew + e[1] + e[2] + e[3]) / 4;
    int c[5] = {0, 0, 0, 0, 124};
    for (int k = 0; k < 3; ++k) c[k + 1] = c[k] + (pa - e[k] - (k == 0 ? ew : 0) + 4) / 8 - 1;
    const int nl = c[part + 1] - c[part] + 1, wst = e[part] / 4;
    const bool has_w = part < 2 || (part == 2 ? has_mod : has_up);
    const int blk = !has_w ? 0 : (part == 0 ? cb0 : (part == 1 ? xcd * 8 + cb1 : (part == 2 ? mi : ui)));
    const int tau = (part == 3 && has_up ? P.TEND - P.DT_UP : (part == 0 && wd ? P.TEND - P.DT_WO : P.TEND)) * 256 / (2 * nl + wst);
    T.e[2 * bid] = (u32)(c[part] | nl << 7 | blk << 13 | (has_w ? 1 : 0) << 19 | (part == 0 ? wd : 0) << 20);
    T.e[2 * bid + 1] = (u32)(tau | (tau * wst / (part == 0 && wd < 8 ? 8 - wd : 8)) << 16);
  }
  return T;
}
constexpr int step_range(const Prm& P, bool mx_) {
  int mx = 0, mn = 1 << 20;
  const Tab T = make_tab(P);
  for (int bid = 0; bid < 256; ++bid) { const int n = T.e[2 * bid] >> 7 & 63; mx = n > mx ? n : mx; mn = n < mn ? n : mn; }
  return mx_ ? mx : mn;
}
extern "C" __attribute__((used, visibility("protected"))) __constant__ const Tab wg_tab = make_tab(PRM0);
static_assert(step_range(PRM0, true) <= NL && step_range(PRM0, false) >= NCMIN / 8, "a wave's key range does not fit its load steps");
static_assert(cons_end(SIDE + WV + 1) <= NCMIN / 8 && cons_end(UPREP) <= NCMIN / 8 && SIDE > MLS + 3, "steps every wave runs");
constexpr int NUP = FF / 32;
constexpr int SPIN_MAX = 1 << 17;

constexpr u32 WS_EPOCH = 0;
constexpr u32 G_TB = 1024;
constexpr u32 G_NT = 2048;
constexpr u32 G_MOD = 4096;
constexpr u32 G_Q = G_MOD + 3072 * 8;
constexpr u32 G_ML = G_Q + 4096 * 8;
constexpr u32 G_P = G_ML + 256 * 16;
constexpr u32 G_XP = G_P + 256 * 256;
constexpr u32 G_OP = G_XP + 8 * 512 * 4;
constexpr u32 WS_END = G_OP + 43 * 512 * 8;
constexpr u32 SENT = 0xffffffffu;
constexpr u32 OOB = 0x40000000u;
#define WST 0

constexpr int AUX_NT = 2, AUX_SC1 = 16;

__device__ __forceinline__ float lo_f(u32 v) { return __uint_as_float(v << 16); }
__device__ __forceinline__ float hi_f(u32 v) { return __uint_as_float(v & 0xffff0000u); }
__device__ __forceinline__ float rbf(float x) { return (float)(__bf16)x; }
__device__ __forceinline__ u32 pack_bf(float lo, float hi) {
  bf16x2 r; r[0] = (__bf16)lo; r[1] = (__bf16)hi; return __builtin_bit_cast(u32, r);
}
__device__ __forceinline__ float dot2(u32 k, u32 q, float acc) {
  return __builtin_amdgcn_fdot2_f32_bf16(__builtin_bit_cast(bf16x2, k), __builtin_bit_cast(bf16x2, q), acc, false);
}
template <int CTRL> __device__ __forceinline__ float dpp(float x) {
  return __builtin_bit_cast(float, __builtin_amdgcn_mov_dpp(__builtin_bit_cast(int, x), CTRL, 0xf, 0xf, true));
}
constexpr int XOR1 = 0xB1, XOR2 = 0x4E, XOR7 = 0x141, XOR8 = 0x128;
__device__ __forceinline__ float swap32_add(float a, float b) {
  auto t = __builtin_amdgcn_permlane32_swap(__float_as_uint(a), __float_as_uint(b), false, false);
  return __uint_as_float(t[0]) + __uint_as_float(t[1]);
}
__device__ __forceinline__ float swap16_add(float a, float b) {
  auto t = __builtin_amdgcn_permlane16_swap(__float_as_uint(a), __float_as_uint(b), false, false);
  return __uint_as_float(t[0]) + __uint_as_float(t[1]);
}
__device__ __forceinline__ float wave_sum(float x) {
  x += dpp<XOR1>(x); x += dpp<XOR2>(x); x += dpp<XOR7>(x); x += dpp<XOR8>(x);
  x = swap16_add(x, x);
  return swap32_add(x, x);
}
__device__ __forceinline__ float wave_max(float x) {
  x = fmaxf(x, dpp<XOR1>(x)); x = fmaxf(x, dpp<XOR2>(x)); x = fmaxf(x, dpp<XOR7>(x)); x = fmaxf(x, dpp<XOR8>(x));
  auto s = __builtin_amdgcn_permlane16_swap(__float_as_uint(x), __float_as_uint(x), false, false);
  x = fmaxf(__uint_as_float(s[0]), __uint_as_float(s[1]));
  auto t = __builtin_amdgcn_permlane32_swap(__float_as_uint(x), __float_as_uint(x), false, false);
  return fmaxf(__uint_as_float(t[0]), __uint_as_float(t[1]));
}
template <int CTRL> __device__ __forceinline__ float sel_add(float lo, float hi, bool up) {
  const float t1 = lo + dpp<CTRL>(lo), t2 = hi + dpp<CTRL>(hi);
  return up ? t2 : t1;
}
template <int I> __device__ __forceinline__ float bc16(float x) {
  return __builtin_bit_cast(float, __builtin_amdgcn_update_dpp(0, __builtin_bit_cast(int, x), 0x150 + I, 0xf, 0xf, false));
}
__device__ __forceinline__ float bcast16(float x, int i) {
  switch (i) {
    case 0: return bc16<0>(x); case 1: return bc16<1>(x); case 2: return bc16<2>(x); case 3: return bc16<3>(x);
    case 4: return bc16<4>(x); case 5: return bc16<5>(x); case 6: return bc16<6>(x); case 7: return bc16<7>(x);
    case 8: return bc16<8>(x); case 9: return bc16<9>(x); case 10: return bc16<10>(x); case 11: return bc16<11>(x);
    case 12: return bc16<12>(x); case 13: return bc16<13>(x); case 14: return bc16<14>(x); default: return bc16<15>(x);
  }
}
__device__ __forceinline__ float silu_f(float x) { return x * __builtin_amdgcn_rcpf(1.f + __expf(-x)); }

template <int AUX> __device__ __forceinline__ u32x4 ld16(rsrc_t r, u32 voff, u32 soff) {
  return __builtin_amdgcn_raw_buffer_load_b128(r, voff, soff, AUX);
}
template <int AUX> __device__ __forceinline__ u32x2 ld8(rsrc_t r, u32 voff, u32 soff) {
  return __builtin_amdgcn_raw_buffer_load_b64(r, voff, soff, AUX);
}
__device__ __forceinline__ void st8_sc1(rsrc_t r, u32 voff, u32 v, u32 tag) {
  __builtin_amdgcn_raw_buffer_store_b64(u32x2{v, tag}, r, voff, 0, AUX_SC1);
}
__device__ __forceinline__ void st16_sc1(rsrc_t r, u32 voff, u32 v0, u32 v1, u32 tag) {
  __builtin_amdgcn_raw_buffer_store_b128(u32x4{v0, tag, v1, tag}, r, voff, 0, AUX_SC1);
}
__device__ __forceinline__ rsrc_t mk_rsrc(const void* p, u32 bytes) {
  return __builtin_amdgcn_make_buffer_rsrc(const_cast<void*>(p), 0, bytes, 0x00020000);
}
#define WAIT_VM(n) __builtin_amdgcn_s_waitcnt(((n) & 15) | (((n) >> 4) << 14) | (7 << 4) | (15 << 8))
#define SB() __builtin_amdgcn_sched_barrier(0)
#define CFENCE() asm volatile("" ::: "memory")
__device__ __forceinline__ bool all_ok(bool ok) { return __builtin_amdgcn_ballot_w64(!ok) == 0ull; }

template <int N> __device__ __forceinline__ void gather16(rsrc_t ws, u32 voff, u32 stride, u32 tag, u32x4 (&g)[N]) {
#pragma nounroll
  for (int spin = 0; spin < SPIN_MAX; ++spin) {
    CFENCE();
    bool ok = true;
#pragma unroll
    for (int i = 0; i < N; ++i) g[i] = ld16<AUX_SC1>(ws, voff + i * stride, 0);
#pragma unroll
    for (int i = 0; i < N; ++i) ok &= (g[i][1] == tag) & (g[i][3] == tag);
    if (all_ok(ok)) break;
    __builtin_amdgcn_s_sleep(8);
  }
}
template <int N> __device__ __forceinline__ void gather8(rsrc_t ws, u32 voff, u32 stride, u32 tag, u32 (&g)[N], bool act = true) {
#pragma nounroll
  for (int spin = 0; spin < SPIN_MAX; ++spin) {
    CFENCE();
    bool ok = true;
    u32x2 t[N];
#pragma unroll
    for (int i = 0; i < N; ++i) t[i] = ld8<AUX_SC1>(ws, voff + i * stride, 0);
#pragma unroll
    for (int i = 0; i < N; ++i) { ok &= (t[i][1] == tag); g[i] = t[i][0]; }
    if (all_ok(ok | !act)) break;
    __builtin_amdgcn_s_sleep(8);
  }
}

struct Smem {
  float red[8][128];
  float ml[8][2];
  float r2[64 * 20];
  u16 hb[3 * 1024];
  float stat[8][64];
  float red2[8][128];
  u32 dummy[64];
  u32x2 bc;
  u32x4 zero[4];
  u32 mflag[8];
  u32x4 wbuf[8][8][64];
  __attribute__((aligned(256))) u16 w2b[32][512];
  u16 actl[2][32];
};

#define PROBE_K 12
#define PROBE_OP 1
#define PROBE_PART -1
#define STAMP(k) do { if ((k) == PROBE_K && tid == 0) pst = wall_clock64(); } while (0)
#define WSTAMP(k) do {} while (0)
#define FINISH() do { STAMP(8); if (tid == 0) { __hip_atomic_store(reinterpret_cast<u64*>(ws + (1u << 20)) + bid * 2, pt0, __ATOMIC_RELAXED, __HIP_MEMORY_SCOPE_AGENT); \
   __hip_atomic_store(reinterpret_cast<u64*>(ws + (1u << 20)) + bid * 2 + 1, (PROBE_PART < 0 || part == PROBE_PART) ? pst : 0ull, __ATOMIC_RELAXED, __HIP_MEMORY_SCOPE_AGENT); } \
   if (bid == 8 && wave == 0) probe_end(ws, pt0, lane); return; } while (0)
#define PIN(x) asm volatile("" : "+v"(x))
#define LDS_BARRIER() do { __builtin_amdgcn_s_waitcnt(0xc07f); __builtin_amdgcn_s_barrier(); CFENCE(); } while (0)
#define WAVE_SYNC() do { __builtin_amdgcn_fence(__ATOMIC_RELEASE, "wavefront"); __builtin_amdgcn_wave_barrier(); __builtin_amdgcn_fence(__ATOMIC_ACQUIRE, "wavefront"); } while (0)

__device__ __forceinline__ void dma16(rsrc_t r, u32 voff, u32 soff, u32 lds_dst) {
  u32 keep;
  asm volatile("s_mov_b32 %0, m0\n\ts_mov_b32 m0, %4\n\ts_nop 4\n\tbuffer_load_dwordx4 %1, %2, %3 offen nt lds\n\ts_mov_b32 m0, %0"
               : "=&s"(keep) : "v"(voff), "s"(r), "s"(soff), "s"(lds_dst) : "memory");
}
__device__ __forceinline__ u32 perm_lo(u32 a, u32 b) { return __builtin_amdgcn_perm(b, a, 0x05040100u); }
__device__ __forceinline__ u32 perm_hi(u32 a, u32 b) { return __builtin_amdgcn_perm(b, a, 0x07060302u); }
__device__ __forceinline__ float rdl(float x, int l) { return __int_as_float(__builtin_amdgcn_readlane(__float_as_int(x), l)); }

typedef __attribute__((ext_vector_type(8))) __bf16 bf16x8;
__device__ __forceinline__ f32x4 mfma16(u32x4 a, u32x4 b, f32x4 c) {
  return __builtin_amdgcn_mfma_f32_16x16x32_bf16(__builtin_bit_cast(bf16x8, a), __builtin_bit_cast(bf16x8, b), c, 0, 0, 0);
}
__device__ __forceinline__ float sum8(const float* p) {
  const f32x4 a = *reinterpret_cast<const f32x4*>(p), b = *reinterpret_cast<const f32x4*>(p + 4);
  return ((a[0] + b[0]) + (a[1] + b[1])) + ((a[2] + b[2]) + (a[3] + b[3]));
}
__device__ __forceinline__ float ln_ri(float var) {
  return __builtin_amdgcn_rcpf(rbf(__builtin_amdgcn_sqrtf(rbf(rbf(fmaxf(var, 0.f)) + 1e-5f))));
}
__device__ __forceinline__ void ln_stats2(const float (&x0)[8], const float (&x1)[8], float& mb0, float& ri0, float& mb1, float& ri1) {
  const float k0 = rdl(x0[0], 0), k1 = rdl(x1[0], 0);
  float s0 = 0.f, s1 = 0.f, q0 = 0.f, q1 = 0.f;
#pragma unroll
  for (int k = 0; k < 8; ++k) {
    const float d0 = x0[k] - k0, d1 = x1[k] - k1;
    s0 += d0; s1 += d1; q0 = fmaf(d0, d0, q0); q1 = fmaf(d1, d1, q1);
  }
  float t = swap16_add(swap32_add(s0, s1), swap32_add(q0, q1));
  t += dpp<XOR1>(t); t += dpp<XOR2>(t); t += dpp<XOR7>(t); t += dpp<XOR8>(t);
  auto u = __builtin_amdgcn_permlane16_swap(__float_as_uint(t), __float_as_uint(t), false, false);
  const float ms = __uint_as_float(u[0]) * (1.f / 512.f), mq = __uint_as_float(u[1]) * (1.f / 512.f);
  const float ri = ln_ri(fmaf(-ms, ms, mq));
  mb0 = rbf(k0 + rdl(ms, 0)); mb1 = rbf(k1 + rdl(ms, 32));
  ri0 = rdl(ri, 0); ri1 = rdl(ri, 32);
}
template <int I> struct IC { static constexpr int v = I; };
template <int I, int N, class F> __device__ __forceinline__ void sfor(F&& f) {
  if constexpr (I < N) { if (f(IC<I>{})) sfor<I + 1, N>(f); }
}
typedef __attribute__((ext_vector_type(4))) short s16x4;
typedef __attribute__((address_space(3))) s16x4* lds_s16x4p;
__device__ __forceinline__ u32x2 tr_read(u32 lds_addr) {
  return __builtin_bit_cast(u32x2, __builtin_amdgcn_ds_read_tr16_b64_v4i16((lds_s16x4p)lds_addr));
}
__device__ __forceinline__ u32 next_tag(u32 e) { return ((e | 0x40004000u) + 1u) & ~0x40004000u; }
__device__ __forceinline__ float u2f(u32 v) { return __uint_as_float(v); }
#define LDSP(T, a) ((__attribute__((address_space(3))) T*)(a))
template <class T> __device__ __forceinline__ u32 lds_addr(T* p) { u32 a = (u32)(uintptr_t)p; PIN(a); return a; }

#define TAB_LOAD "s_add_u32 s30, s28, wg_tab@rel32@lo+4\ns_addc_u32 s31, s29, wg_tab@rel32@hi+12\nglobal_load_dwordx2 %[te], %[va], s[30:31]\n"
__device__ __forceinline__ void probe_end(unsigned char* ws, u64 pt0, int lane) {
  while (wall_clock64() < pt0 + 5500ull) __builtin_amdgcn_s_sleep(8);
  u64 a[4], c2[4];
  for (int i = 0; i < 4; ++i) {
    a[i] = __hip_atomic_load(reinterpret_cast<u64*>(ws + (1u << 20)) + (i * 64 + lane) * 2, __ATOMIC_RELAXED, __HIP_MEMORY_SCOPE_AGENT);
    c2[i] = __hip_atomic_load(reinterpret_cast<u64*>(ws + (1u << 20)) + (i * 64 + lane) * 2 + 1, __ATOMIC_RELAXED, __HIP_MEMORY_SCOPE_AGENT);
  }
  int mn0 = 1 << 30, mx1 = -(1 << 30), mn1 = 1 << 30, sum = 0, cnt = 0;
  for (int i = 0; i < 4; ++i) {
    const int d0 = (int)(a[i] - pt0);
    mn0 = min(mn0, d0);
    if (c2[i] != 0ull) { const int d1 = (int)(c2[i] - pt0); mx1 = max(mx1, d1); mn1 = min(mn1, d1); sum += d1; cnt += 1; }
  }
  for (int o = 1; o < 64; o <<= 1) { mn0 = min(mn0, __shfl_xor(mn0, o)); mx1 = max(mx1, __shfl_xor(mx1, o)); mn1 = min(mn1, __shfl_xor(mn1, o)); sum += __shfl_xor(sum, o); cnt += __shfl_xor(cnt, o); }
  int X = PROBE_OP == 0 ? mx1 : (PROBE_OP == 1 ? sum / max(cnt, 1) : mn1);
  if (PROBE_K == 102) X = mn0;
  X = mn0 + (X - mn0) * PROBE_MUL;
  const u64 tg = pt0 + (u64)(long long)(X + 8000);
  for (int i = 0; i < 400000 && wall_clock64() < tg; ++i) __builtin_amdgcn_s_sleep(1);
}
__global__ void __launch_bounds__(512, 2) fused_kernel(const u16* kc_, const u16* vc_, const u16* wo_, const u16* wq_, const u16* wmod_, const u16* w1_, const u16* w3_,
                                                       unsigned char* ws_, const u16* x_, const u16* cnd_, const u16* w2_, u16* out_) {
  __shared__ __attribute__((aligned(256))) Smem sm;
  const u64 a_ka = (u64)__builtin_amdgcn_kernarg_segment_ptr();
  const int bid = blockIdx.x, tid = threadIdx.x;
  u64 wa, t64; u32x2 te; int tid0;
  u32 ar[14];
  u32 tch2, sa_, sb_, va_;
  u32x4 kr[NL], vr[NL];
  asm volatile(
    "s_load_dwordx16 s[4:19], %[ka], 0x0\ns_load_dwordx8 s[20:27], %[ka], 0x40\n"
    "s_memrealtime %[t]\n"
    "v_lshlrev_b32 %[va], 3, %[bid]\n"
    "s_getpc_b64 s[28:29]\n.Lcpf_first:\n"
    TAB_LOAD
    "s_cmp_gt_u32 %[bid], 15\ns_cbranch_scc1 .Lno_cpf\n"
    "s_mov_b32 %[sa], .Lend_a-.Lcpf_first\ns_max_i32 %[sa], %[sa], .Lend_b-.Lcpf_first\ns_max_i32 %[sa], %[sa], .Lend_c-.Lcpf_first\ns_max_i32 %[sa], %[sa], .Lend_d-.Lcpf_first\ns_max_i32 %[sa], %[sa], .Lend_e-.Lcpf_first\n"
    "v_lshlrev_b32 %[va], 6, %[tid]\nv_min_u32 %[va], %[sa], %[va]\nglobal_load_dword %[pf], %[va], s[28:29]\n"
    ".Lno_cpf:\n"
    "s_and_b32 %[sa], %[bid], 15\ns_lshr_b32 %[sb], %[bid], 6\ns_lshl2_add_u32 %[sa], %[sa], %[sb]\ns_lshl_b32 %[sa], %[sa], 22\ns_xor_b32 %[sa], %[sa], 0xff0000\n"
    "s_and_b32 %[sb], %[bid], 48\nv_lshl_add_u32 %[va], %[sb], 5, %[tid]\nv_lshl_add_u32 %[va], %[va], 4, %[sa]\n"
    "v_readfirstlane_b32 %[t0], %[tid]\n"
    "s_waitcnt lgkmcnt(0)\nglobal_load_dwordx4 %[k0], %[va], s[4:5] nt\nglobal_load_dwordx4 %[v0], %[va], s[6:7] nt\n"
    "s_bitcmp1_b32 %[t0], 6\ns_cselect_b64 s[14:15], s[16:17], s[14:15]\n"
    "s_bitcmp1_b32 %[bid], 4\ns_cselect_b64 s[8:9], s[10:11], s[8:9]\ns_cselect_b64 s[12:13], s[14:15], s[12:13]\ns_bitcmp1_b32 %[bid], 5\ns_cselect_b64 %[wa], s[12:13], s[8:9]"
    : "=&{s4}"(ar[0]), "=&{s5}"(ar[1]), "=&{s6}"(ar[2]), "=&{s7}"(ar[3]), "=&{s18}"(ar[4]), "=&{s19}"(ar[5]), "=&{s20}"(ar[6]), "=&{s21}"(ar[7]), "=&{s22}"(ar[8]), "=&{s23}"(ar[9]),
      "=&{s24}"(ar[10]), "=&{s25}"(ar[11]), "=&{s26}"(ar[12]), "=&{s27}"(ar[13]), [wa] "=&s"(wa), [t0] "=&s"(tid0), [t] "=&s"(t64), [te] "=&v"(te), [sa] "=&s"(sa_), [sb] "=&s"(sb_),
      [va] "=&v"(va_), [pf] "=&v"(tch2), [k0] "=&v"(kr[0]), [v0] "=&v"(vr[0])
    : [ka] "s"(a_ka), [bid] "s"(bid), [tid] "v"(tid) : "s8", "s9", "s10", "s11", "s12", "s13", "s14", "s15", "s16", "s17", "s28", "s29", "s30", "s31", "scc", "memory");
  asm volatile("" : "+s"(ar[0]), "+s"(ar[1]), "+s"(ar[2]), "+s"(ar[3]), "+s"(ar[4]), "+s"(ar[5]), "+s"(ar[6]), "+s"(ar[7]), "+s"(ar[8]), "+s"(ar[9]), "+s"(ar[10]), "+s"(ar[11]), "+s"(ar[12]), "+s"(ar[13]));
  asm volatile("" : "+s"(wa), "+s"(t64), "+s"(tid0));
  const u32 tka = (u32)wall_clock64();
  const u64 pt0 = t64; u64 pst = (PROBE_K == 100 || PROBE_K == 102) ? pt0 : (PROBE_K == 101 ? pt0 + (u32)(tka - (u32)t64) : 0);
  const u32 t0 = (u32)t64, te0_ = 0;
  __builtin_assume(tid >= 0 && tid < 512 && tid0 >= 0 && tid0 < 512 && bid >= 0 && bid < 256);
  constexpr Prm P = PRM0;
  const int TEND = P.TEND;
  const int xcd = bid & 7, part = bid >> 4 & 3;
  const int lane = tid & 63, wave = tid0 >> 6;
  SB();
  const bool p3 = part == 3;
  u32 pitch = (u32)(0x005600c001000020ull >> (part * 16) & 0xffff) * 32u;
  const int sh = p3 ? 2 : 3;
  u32 wv0 = (u32)((p3 ? (wave >> 1) * 128 : wave * 64) + (part == 0 ? xcd * 512 : 0) + (lane >> sh)) * pitch + ((lane & (p3 ? 3 : 7)) ^ (lane >> (p3 ? 5 : 3))) * 16u;
  const u32 wimg = (u32)(uintptr_t)(&sm.wbuf[0][0][0]);
  u32 wsz = (part == 0 ? 4096u : 512u) * pitch, wld = wimg + (p3 ? (wave & 1) * 32768 + (wave >> 1) * 8192 : wave * 8192);
  PIN(wv0);
  asm volatile("" : "+s"(wsz), "+s"(wld), "+s"(pitch));
  auto karg = [&](int i) { return ar[2 * i] | (u64)ar[2 * i + 1] << 32; };
  const u64 a_kc = karg(0), a_vc = karg(1);
  unsigned char* ws = (unsigned char*)karg(2);
  const u16 *x = (const u16*)karg(3), *cnd = (const u16*)karg(4), *w2 = (const u16*)karg(5);
  u16* out = (u16*)karg(6);
  const rsrc_t wsr = mk_rsrc(ws, WS_END);
  asm volatile("s_waitcnt vmcnt(2)" : "+v"(te));
  const u32 te0 = __builtin_amdgcn_readfirstlane(te[0]), te1 = __builtin_amdgcn_readfirstlane(te[1]);
  const int bl0 = te0 & 127, nl = te0 >> 7 & 63, blk = te0 >> 13 & 63;
  const bool has_w = te0 >> 19 & 1;
  const rsrc_t rw = mk_rsrc((const void*)wa, wsz);
  const u32 wstep = pitch << (6 - sh), wflip = p3 ? 32u : 0u;
  u32 wso = blk << (sh + 4);
  int wleft = 8;
  auto piece = [&]() {
    dma16(rw, wv0, wso, wld);
    wso += wstep; wld += 1024;
    wv0 ^= (wleft & 1) ? wflip : 0u;
    --wleft;
  };
  SB();
  STAMP(23);
  const int b = bid >> 3 & 1, head = xcd * 4 + (bid >> 6 ^ 3);
  const int g = lane >> 4, c = lane & 15;
  const int i8 = b | bid >> 6 << 1, wi = i8 * 8 + xcd;
  const bool is_up = p3 && has_w, modc = part == 2 && wi < 16;
  const size_t base = ((size_t)(b * NH + head) * SK + (SK - WIN)) * DH;
  const rsrc_t rk = mk_rsrc((const u16*)a_kc + base, WIN * DH * 2), rv = mk_rsrc((const u16*)a_vc + base, WIN * DH * 2);
  int tau = te1 & 0xffff;
  const int tauw = te1 >> 16;
  int due = P.NF * tau, now = 0;
  auto gate = [&]() {
    while (now < due) { now = (int)(((u32)wall_clock64() - t0) << 8); if (now < due) __builtin_amdgcn_s_sleep(1); }
    due += tau;
  };
  auto until = [&](int t) { while ((int)(((u32)wall_clock64() - t0) << 8) < t); };
  const u32 pslot = G_P + ((b * 32 + head) * 4 + part) * 256;
  u32x2 ep; u32 xs0, xs1, tch1; u32x4 xr0, xr1;
  const rsrc_t rx = mk_rsrc(part == 2 ? cnd : x, (part == 1 || (part == 2 && has_w)) ? 2048u : 0u);
  auto small_ops = [&]() {
    ep = ld8<AUX_SC1>(wsr, wave == 7 ? WS_EPOCH : OOB, 0);
    xs0 = __builtin_amdgcn_raw_buffer_load_b16(rx, tid * 2, 0, 0); xs1 = __builtin_amdgcn_raw_buffer_load_b16(rx, tid * 2, 1024, 0);
    xr0 = ld16<0>(rx, part == 1 ? lane * 16 : OOB, 0); xr1 = ld16<0>(rx, part == 1 ? lane * 16 : OOB, 1024);
    tch1 = __builtin_amdgcn_raw_buffer_load_b32(mk_rsrc(w2 + (size_t)blk * 32 * 512, is_up ? 64u : 0u), tid == 384 ? 0 : OOB, 0, AUX_NT);
  };
  if (has_w) {
    if (modc) now = 1 << 30;
#pragma nounroll
    while (wleft > (int)(te0 >> 20)) { gate(); due += tauw - tau; piece(); }
    now = 0;
  }
  SB();
  u32 sot;
  {
    const u32 s1 = bl0 + lane - 1, s0 = s1 + (s1 >= 108 ? 4 : 0);
    u32 sb = s0 & 96u;
#pragma unroll
    for (int k = 0; k < 5; ++k) sb |= (s0 >> k & 1u) << (P.PM >> (4 * k) & 7);
    const u32 src = (sb >> 5 & 3u) | (u32)(b * 32 + head) << 2;
    sb ^= ((__builtin_popcount(src & P.M0) & 1) | (__builtin_popcount(src & P.M1) & 1) << 1 | (__builtin_popcount(src & P.M2) & 1) << 2 | (__builtin_popcount(src & P.M3) & 1) << 3 | (__builtin_popcount(src & P.M4) & 1) << 4) & (s1 >= 96 ? 7u : 31u);
    sot = (sb & 7u) << 10 | (u32)wave << 13 | (sb >> 3) << 16;
    sot = s1 >= 120 ? (s1 + 4) << 13 | (u32)wave << 10 : sot;
    sot = lane < nl ? sot : OOB;
    PIN(sot);
  }
  u32 tag = 0;
  u32x4 bf[8];
  auto wo_prep = [&](int s0 = 0, int s1 = 2) {
    const u32 wi_ = wimg + wave * 8192;
    const int q = (lane >> 2) & 3, pp = lane & 3;
#pragma unroll
    for (int s = s0; s < s1; ++s) {
#pragma unroll
      for (int t = 0; t < 4; ++t) {
        const int r0 = s * 32 + g * 8 + q, r1 = r0 + 4, ch = t * 2 + (pp >> 1);
        const u32x2 lo = tr_read(wi_ + r0 * 128 + ((ch ^ (r0 & 7)) * 16) + (pp & 1) * 8), hi = tr_read(wi_ + r1 * 128 + ((ch ^ (r1 & 7)) * 16) + (pp & 1) * 8);
        bf[s * 4 + t] = u32x4{lo[0], lo[1], hi[0], hi[1]};
      }
    }
  };
  auto gemv_go = [&](u32 hh) {
    sm.hb[tid] = (u16)hh; sm.hb[512 + tid] = (u16)(hh >> 16);
    WAVE_SYNC();
    const u32x4* a = (c & 11) == 0 ? reinterpret_cast<const u32x4*>(&sm.hb[(c >> 2) * 512 + wave * 64 + g * 8]) : &sm.zero[0];
    const u32x4 a0 = a[0], a1 = a[(c & 11) == 0 ? 4 : 0];
    const f32x4 z = {0.f, 0.f, 0.f, 0.f};
    f32x4 d[4];
#pragma unroll
    for (int t = 0; t < 4; ++t) d[t] = mfma16(a0, bf[t], z);
#pragma unroll
    for (int t = 0; t < 4; ++t) d[t] = mfma16(a1, bf[4 + t], d[t]);
    if (lane < 32) {
#pragma unroll
      for (int t = 0; t < 4; ++t) sm.r2[(t * 16 + c) * 20 + g * 8 + wave] = d[t][0];
    }
  };
  auto mod_stage = [&]() {
    wo_prep();
    gemv_go(pack_bf(silu_f(lo_f(xs0)), silu_f(lo_f(xs1))));
    __builtin_amdgcn_s_waitcnt(0xc07f);
    if (lane == 0) *LDSP(volatile u32, (u32)(uintptr_t)&sm.mflag[wave]) = 1u;
    if (wave == 7) {
#pragma nounroll
      for (int spin = 0; spin < SPIN_MAX; ++spin) {
        const u32 f = *LDSP(volatile u32, (u32)(uintptr_t)&sm.mflag[lane & 7]);
        if (all_ok(f != 0u)) break;
        __builtin_amdgcn_s_sleep(1);
      }
      CFENCE();
      st8_sc1(wsr, G_MOD + (blk * 64 + lane) * 8, pack_bf(sum8(&sm.r2[lane * 20]), sum8(&sm.r2[lane * 20 + 8])), tag);
    }
    STAMP(9);
  };
  if (lane < 4) sm.zero[lane] = u32x4{0u, 0u, 0u, 0u};
  if (part == 2 && has_w) {
    if (tid < 8) sm.mflag[tid] = 0u;
    LDS_BARRIER();
    if (__builtin_expect(modc, 0)) {
      small_ops();
      __builtin_amdgcn_raw_buffer_store_b128(u32x4{SENT, SENT, SENT, SENT}, wsr, tid < 320 ? G_P + wi * 5120 + tid * 16 : OOB, 0, AUX_SC1);
      asm volatile("s_waitcnt vmcnt(0)" ::: "memory");
      tag = next_tag(__builtin_amdgcn_readfirstlane(ep[0]));
      mod_stage();
      __builtin_amdgcn_s_barrier();
    }
  }
  SB();
  const int fd = wi * 8 + (lane & 7);
  const int ub = wave >> 2, ud = (wave & 3) * 128 + c * 8 + g * 2;
  u32x4 sg[3]; u32 sx2;
  auto side_loads = [&]() {
    const bool fin = part == 1 && wave == 0;
    sg[0] = ld16<AUX_SC1>(wsr, is_up ? G_MOD + (1024 + ud) * 8 : (fin ? G_MOD + (1024 + fd) * 8 : OOB), 0);
    sg[1] = ld16<AUX_SC1>(wsr, is_up ? G_MOD + (1536 + ud) * 8 : (fin ? G_MOD + (2560 + fd) * 8 : OOB), 0);
    sg[2] = ld16<AUX_SC1>(wsr, is_up ? G_MOD + (2048 + ud) * 8 : OOB, 0);
    sx2 = __builtin_amdgcn_raw_buffer_load_b32(mk_rsrc(x, (is_up || fin) ? 2048u : 0u), is_up ? ub * 1024 + ud * 2 : (g & 1) * 1024 + (fd >> 1) * 4, 0, 0);
  };
  auto side_pin = [&]() { PIN(sg[0]); PIN(sg[1]); PIN(sg[2]); PIN(sx2); };
  const u32 mlo = G_ML + (head * 2 + b) * 64 + (lane & 3) * 16;
  float fmrg = 0.f;
  bool fok = wave != 0;
  auto merge_factor = [&](const u32x4& ml) {
    const float mi = u2f(ml[0]);
    float M = fmaxf(mi, dpp<XOR1>(mi)); M = fmaxf(M, dpp<XOR2>(M));
    const float e = __expf(mi - M);
    float L = e * u2f(ml[2]);
    L += dpp<XOR1>(L); L += dpp<XOR2>(L);
    fmrg = rdl(e * __builtin_amdgcn_rcpf(L), part);
  };
  f32x2 ga = {0.f, 0.f}, xin = {0.f, 0.f}, cc1 = {0.f, 0.f};
  float Af = 0.f, Bf = 0.f;
  const int hpos = ub * 512 + ud;
  auto up_prep = [&]() {
    if (!all_ok((sg[0][1] == tag) & (sg[0][3] == tag) & (sg[1][1] == tag) & (sg[1][3] == tag) & (sg[2][1] == tag) & (sg[2][3] == tag))) gather16<3>(wsr, G_MOD + (1024 + ud) * 8, 512 * 8, tag, sg);
    ga = f32x2{ub ? hi_f(sg[0][0]) : lo_f(sg[0][0]), ub ? hi_f(sg[0][2]) : lo_f(sg[0][2])};
    cc1 = f32x2{rbf(1.f + (ub ? hi_f(sg[2][0]) : lo_f(sg[2][0]))), rbf(1.f + (ub ? hi_f(sg[2][2]) : lo_f(sg[2][2])))};
    xin = f32x2{lo_f(sx2), hi_f(sx2)};
    u32* hp = reinterpret_cast<u32*>(&sm.hb[hpos]);
    hp[512] = pack_bf(cc1[0], cc1[1]);
    hp[1024] = ub ? perm_hi(sg[1][0], sg[1][2]) : perm_lo(sg[1][0], sg[1][2]);
  };
  auto up_frags = [&]() {
    const int pp = lane & 3;
    const u32 a = wimg + (pp & 1) * 32768 + ((pp >> 1) * 32 + g * 8 + ((lane >> 2) & 3)) * 64 + (((wave >> 1) ^ ((g & 1) | (pp & 2))) * 16) + (wave & 1) * 8;
#pragma unroll
    for (int rb = 0; rb < 8; ++rb) {
      const u32x2 lo = tr_read(a + rb * 4096), hi = tr_read(a + rb * 4096 + 256);
      bf[rb] = u32x4{lo[0], lo[1], hi[0], hi[1]};
    }
  };
  const u32x4* ap = reinterpret_cast<const u32x4*>(&sm.hb[((lane >> 3) & 1) * 512 + (lane & 1) * 32 + g * 8]);
  const bool par = lane & 8;
  float l0 = 0.f, l1 = 0.f;
  u32x2 gmd[2], tbg;
  int t1 = 0;
#pragma unroll
  for (int i = NCMIN / 8; i < NL; ++i) kr[i] = u32x4{0u, 0u, 0u, 0u};
  sfor<0, NL>([&](auto ic) {
    constexpr int i = ic.v;
    if (i >= NCMIN / 8 && i >= nl) return false;
    if constexpr (i == 0) return true;
    const u32 so = __builtin_amdgcn_readlane(sot, i);
    gate();
    SB();
    kr[i] = ld16<AUX_NT>(rk, lane * 16, so);
    if constexpr (i == SMO) small_ops();
    if constexpr (i == TBL) tbg = ld8<AUX_SC1>(wsr, wave == 7 ? G_TB + xcd * 128 + (lane & 7) * 8 : OOB, 0);
    if constexpr (i == QP) { gmd[0] = ld8<AUX_SC1>(wsr, part == 1 ? G_MOD + tid * 8 : OOB, 0); gmd[1] = ld8<AUX_SC1>(wsr, part == 1 ? G_MOD + tid * 8 : OOB, 4096); }
    SB();
    SB();
    if (i >= WK) { WAIT_VM(WK); SB(); }
    if (i == 1) STAMP(25);
    if (i == 8) STAMP(12);
    if (i == 20) STAMP(21);
    if (i == 25) STAMP(22);
    if (i == TBP) {
      if (part == 3 && i8 == 0 && wave == 7) { tag = next_tag(__builtin_amdgcn_readfirstlane(ep[0])); if (lane < 8) st8_sc1(wsr, G_TB + lane * 128 + xcd * 8, t0 + (((tka - t0) * P.GA) >> 3) - P.GS, tag); }
      SB();
    }
    if (i == TBU) {
      if (wave == 7) {
        const bool ok = tbg[1] == tag;
        float lf = ok ? (float)min(max((int)(t0 - tbg[0]), -300), 300) : 0.f, nf = ok ? 1.f : 0.f;
        lf += dpp<XOR1>(lf); nf += dpp<XOR1>(nf); lf += dpp<XOR2>(lf); nf += dpp<XOR2>(nf); lf += dpp<XOR7>(lf); nf += dpp<XOR7>(nf);
        if (lane == 0) sm.bc = u32x2{tag, t0 - (u32)(int)(lf * __builtin_amdgcn_rcpf(fmaxf(nf, 1.f)))};
      }
      LDS_BARRIER();
      const u32x2 bc = sm.bc;
      tag = __builtin_amdgcn_readfirstlane(bc[0]);
      t1 = (__builtin_amdgcn_readfirstlane((int)(bc[1] - t0)) + TEND + P.X1) * 256;
      tau -= __builtin_amdgcn_readfirstlane((int)((float)(int)(t0 - bc[1]) * 256.f * __builtin_amdgcn_rcpf((float)(2 * nl - TBU - 3))));
      SB();
    }
    if (i == LNS) {
      tag = next_tag(__builtin_amdgcn_readfirstlane(ep[0]));
      if (part == 1) {
        float x0[8], x1[8], mb0, ri0, mb1, ri1;
#pragma unroll
        for (int k = 0; k < 4; ++k) { x0[2 * k] = lo_f(xr0[k]); x0[2 * k + 1] = hi_f(xr0[k]); x1[2 * k] = lo_f(xr1[k]); x1[2 * k + 1] = hi_f(xr1[k]); }
        ln_stats2(x0, x1, mb0, ri0, mb1, ri1);
        l0 = rbf(rbf(lo_f(xs0) - mb0) * ri0); l1 = rbf(rbf(lo_f(xs1) - mb1) * ri1);
      }
      PIN(l0); PIN(l1);
      SB();
    }
    if (i == WUSE) {
      PIN(tch1); PIN(tch2);
      if ((part == 1 || (part == 2 && has_w && !modc))) {
        WAIT_VM(WK); CFENCE();
        if (part == 2) mod_stage(); else wo_prep();
      }
#pragma unroll
      for (int k = 0; k < 8; ++k) PIN(bf[k]);
      SB();
    }
    if (i == QP + QD) {
      if (part == 1) {
        u32 gm[2] = {gmd[0][0], gmd[1][0]};
        if (!all_ok((gmd[0][1] == tag) & (gmd[1][1] == tag))) { STAMP(11); gather8<2>(wsr, G_MOD + tid * 8, 512 * 8, tag, gm); }
        STAMP(5);
        gemv_go(pack_bf(rbf(l0 * rbf(1.f + lo_f(gm[1]))) + lo_f(gm[0]), rbf(l1 * rbf(1.f + hi_f(gm[1]))) + hi_f(gm[0])));
      }
      SB();
    }
    if (i == QP + QD + 1) {
      if (part == 1) {
        LDS_BARRIER();
        if (wave == 0) {
          const int bb = lane >> 5, jq = lane & 31;
          st8_sc1(wsr, G_Q + (bb * 2048 + blk * 32 + jq) * 8, pack_bf(sum8(&sm.r2[2 * jq * 20 + bb * 8]), sum8(&sm.r2[(2 * jq + 1) * 20 + bb * 8])), tag);
        }
        STAMP(6);
      }
      SB();
    }
    return true;
  });
  PIN(sot);
  const u32 qoff = G_Q + ((b * 32 + head) * 64 + c * 4) * 8;
  u32x4 qg[2], fl2[3];
  u32 qq[4];
  constexpr int NB = (NL + 15) / 16;
  float xlo, sc[NB], pr[NB], m = 0.f;
  f32x4 acc = {0.f, 0.f, 0.f, 0.f};
  u32 pb[NB], pm[4], cpf;
  const u32 adst0 = lds_addr(lane < 32 ? &sm.red[wave][c * 8 + g * 4] : &sm.r2[(lane - 32) * 4]);
  u32 po, rdst, rsrc, xo;
  auto wo_addr = [&]() {
    po = G_P + (((c >> 2 & 1) * 32 + xcd * 4 + (wave >> 1)) * 4 + (c & 3)) * 256 + (wave & 1) * 128 + (c >> 3) * 64 + g * 16;
    rdst = lane < 32 ? (u32)(uintptr_t)&sm.red2[wave][0] + lane * 16 : (u32)(uintptr_t)&sm.r2[0] + (lane - 32) * 16;
    rsrc = (u32)(uintptr_t)&sm.red2[0][((wave & 1) * 16 + (lane & 15)) * 4 + (lane >> 4)];
    xo = (lane & 1) ? OOB : G_XP + xcd * 2048 + (wave & 1) * 1024 + (blk * 64 + lane) * 2;
    PIN(po); PIN(rdst); PIN(rsrc); PIN(xo);
  };
  u32 pso = wave == 0 ? pslot + lane * 4 : OOB;
  PIN(pso);
  const u32 nadd = (te0 >> 20) ? P.LW : (is_up ? P.LU : 0);
  u32 nto = tid == 0 ? G_NT + bid * 8 : OOB;
  PIN(nto);
  auto notices = [&](auto nc_, u32 off, u32x4 (&gn)[nc_.v]) -> int {
    constexpr int N = nc_.v;
#pragma nounroll
    for (int spin = 0; spin < SPIN_MAX; ++spin) {
      bool ok = true;
#pragma unroll
      for (int i = 0; i < N; ++i) ok &= (gn[i][1] == tag) & (gn[i][3] == tag);
      if (all_ok(ok)) break;
      __builtin_amdgcn_s_sleep(10);
      CFENCE();
#pragma unroll
      for (int i = 0; i < N; ++i) gn[i] = ld16<AUX_SC1>(wsr, off, i * 1024);
    }
    int mx = 0;
#pragma unroll
    for (int i = 0; i < N; ++i) mx = max(mx, max((int)(gn[i][0] - t0), (int)(gn[i][2] - t0)));
    return __builtin_amdgcn_readfirstlane((int)wave_max((float)min(mx, 1 << 20)));
  };
  const int side = nl >= SIDE + 13 ? SIDE + 8 : (nl >= SIDE + 9 ? SIDE + 4 : SIDE);
  sfor<0, NL + WV>([&](auto ic) {
    constexpr int v = ic.v;
    if (cons_end(v - 1) >= NCMIN / 8 && cons_end(v - 1) >= nl) return false;
    if constexpr (v > 0 && v < NL) {
      const u32 so = __builtin_amdgcn_readlane(sot, v);
      if constexpr (v >= 12) { if (v == nl - P.NE) tau = (tau * P.FE) >> 4; }
      if constexpr (v >= NCMIN / 8) { if (v < nl) gate(); } else gate();
      SB();
      vr[v] = ld16<AUX_NT>(rv, lane * 16, so);
      SB();
      SB();
    }
    if constexpr (v == NCMIN / 8 - 1) {
#pragma unroll
      for (int i = NCMIN / 8; i < NL; ++i) asm volatile("" : "=v"(vr[i]));
      asm volatile("" : "=v"(fl2[1]), "=v"(fl2[2]));
    }
    if constexpr (v == VQ - 4) { qg[0] = ld16<AUX_SC1>(wsr, qoff, 0); qg[1] = ld16<AUX_SC1>(wsr, qoff + 16, 0); }
    if constexpr (v == VSIDE) side_loads();
    if constexpr (v == CPF) {
      u64 pc; u32 dlt, len;
      asm volatile("s_getpc_b64 %0\n.Lcpf_here:\n\ts_mov_b32 %1, .Ltail_start-.Lcpf_here\n\ts_mov_b32 %2, .Lend_a-.Ltail_start\n\ts_max_i32 %2, %2, .Lend_b-.Ltail_start\n\ts_max_i32 %2, %2, .Lend_c-.Ltail_start\n\ts_max_i32 %2, %2, .Lend_d-.Ltail_start\n\ts_max_i32 %2, %2, .Lend_e-.Ltail_start" : "=s"(pc), "=s"(dlt), "=s"(len) :: "scc");
      pc += dlt;
      const rsrc_t rc = mk_rsrc((const void*)pc, (part == 3 && i8 == 1) ? len : 0u);
      cpf = __builtin_amdgcn_raw_buffer_load_b32(rc, tid * 64, 0, 0);
    }
    if constexpr (v == CPF + WV + 1) PIN(cpf);
    if constexpr (v == SIDE || v == SIDE + 4 || v == SIDE + 8) fl2[(v - SIDE) / 4] = ld16<AUX_SC1>(wsr, (wave == 0 && v == side) ? mlo : OOB, 0);
    SB();
    if constexpr (v < WV) { WAIT_VM(WK); SB(); } else if constexpr (v < NL && cons_end(v) < v - WV + 1) { WAIT_VM(WV); SB(); }
    if constexpr (v == VQ) {
      if (!all_ok((qg[0][1] == tag) & (qg[0][3] == tag) & (qg[1][1] == tag) & (qg[1][3] == tag))) { STAMP(20); gather16<2>(wsr, qoff, 16, tag, qg); }
      STAMP(1); WSTAMP(1);
      qq[0] = qg[0][0]; qq[1] = qg[0][2]; qq[2] = qg[1][0]; qq[3] = qg[1][2];
    }
    if constexpr (v >= VQ && v < VS) {
      constexpr int sl = v - VQ, bt = sl >> 1;
      float xs[8];
#pragma unroll
      for (int i = 0; i < 8; ++i) {
        float d = 0.f;
        if (sl * 8 + i < NL) {
#pragma unroll
          for (int k = 0; k < 4; ++k) d = dot2(kr[sl * 8 + i][k], qq[k], d);
        }
        xs[i] = d;
      }
#pragma unroll
      for (int h = 0; h < 4; ++h) xs[h] = sel_add<XOR7>(xs[h], xs[h + 4], c & 4);
#pragma unroll
      for (int h = 0; h < 2; ++h) xs[h] = sel_add<XOR2>(xs[h], xs[h + 2], c & 2);
      xs[0] = sel_add<XOR1>(xs[0], xs[1], c & 1);
      if constexpr ((sl & 1) || sl == NSL - 1) {
        const float t = (sl & 1) ? sel_add<XOR8>(xlo, xs[0], c & 8) : xs[0] + dpp<XOR8>(xs[0]);
        const float s = rbf(rbf(t) * 0.08838834764831845f);
        sc[bt] = (bt * 16 + c < nl) ? s : -__builtin_inff();
        PIN(sc[bt]);
      } else {
        xlo = xs[0];
        PIN(xlo);
      }
    }
    if constexpr (v == VS) {
      float mloc = sc[0];
#pragma unroll
      for (int bt = 1; bt < NB; ++bt) mloc = fmaxf(mloc, sc[bt]);
      m = fmaxf(wave_max(mloc), -3.0e38f);
      float ls = 0.f;
#pragma unroll
      for (int bt = 0; bt < NB; ++bt) { pr[bt] = __expf(sc[bt] - m); ls += pr[bt]; }
      const float l = wave_sum(ls);
      if (lane == 0) { sm.ml[wave][0] = m; sm.ml[wave][1] = l; }
      STAMP(2); WSTAMP(2);
    }
    sfor<cons_end(v - 1), cons_end(v)>([&](auto jc) {
      constexpr int i = jc.v;
      if (i < NCMIN / 8 || i < nl) {
        const u32 pv = __float_as_uint(bcast16(u2f(pb[i >> 4]), i & 15));
        acc = mfma16(u32x4{pv & pm[0], pv & pm[1], pv & pm[2], pv & pm[3]}, vr[i], acc);
      }
      PIN(acc);
      return true;
    });
    if constexpr (v == MLS - 1) { side_pin(); if (is_up) up_prep(); PIN(ga); PIN(xin); PIN(cc1); }
    if constexpr (v == UPREP - 1) wo_addr();
    if constexpr (v == UPREP) {
#pragma unroll
      for (int k = 0; k < 8; ++k) asm volatile("" : "=v"(bf[k]));
      if (is_up) up_frags();
#pragma unroll
      for (int k = 0; k < 8; ++k) PIN(bf[k]);
    }
    if constexpr (v == UPREP + 1) {
      if (is_up) {
        const u32x4* a1 = ap + 128 + ((lane >> 1) & 1) * 128;
        f32x4 d0 = {0.f, 0.f, 0.f, 0.f}, d1 = {0.f, 0.f, 0.f, 0.f};
#pragma unroll
        for (int rb = 0; rb < 4; ++rb) { d0 = mfma16(a1[rb * 8], bf[rb], d0); d1 = mfma16(a1[(rb + 4) * 8], bf[rb + 4], d1); }
        Af = par ? d0[1] + d1[1] : d0[0] + d1[0]; Bf = par ? d0[3] + d1[3] : d0[2] + d1[2];
        Af += dpp<XOR8>(Af); Bf += dpp<XOR8>(Bf);
      }
      PIN(Af); PIN(Bf);
    }
    if constexpr (v == SIDE + WV + 1 || v == SIDE + WV + 5 || v == SIDE + WV + 9) {
      if (wave == 0 && v == side + WV + 1) {
        const u32x4 ml = fl2[(v - SIDE - WV - 1) / 4];
        fok = all_ok((ml[1] == tag) & (ml[3] == tag));
        merge_factor(ml);
      }
    }
    if (v % 6 == 4 && v >= 10 && v < 30) STAMP(15 + v / 6);
    if constexpr (v == MLS) {
      LDS_BARRIER();
      float mm[8], ll[8];
#pragma unroll
      for (int w = 0; w < 8; ++w) { const f32x2 t = *reinterpret_cast<const f32x2*>(&sm.ml[w][0]); mm[w] = t[0]; ll[w] = t[1]; }
      float Mwg = mm[0], Lwg = 0.f;
#pragma unroll
      for (int w = 1; w < 8; ++w) Mwg = fmaxf(Mwg, mm[w]);
      if (wave == 0) {
#pragma unroll
        for (int w = 0; w < 8; ++w) Lwg = fmaf(__expf(mm[w] - Mwg), ll[w], Lwg);
        if (lane == 0) st16_sc1(wsr, G_ML + ((head * 2 + b) * 4 + part) * 16, __float_as_uint(Mwg), __float_as_uint(Lwg), tag);
      }
      const float fw = __expf(m - Mwg);
#pragma unroll
      for (int bt = 0; bt < NB; ++bt) { pb[bt] = pack_bf(pr[bt] * fw, pr[bt] * fw); PIN(pb[bt]); }
#pragma unroll
      for (int k = 0; k < 4; ++k) { pm[k] = (c >> 1) == k ? ((c & 1) ? 0xffff0000u : 0x0000ffffu) : 0u; PIN(pm[k]); }
    }
    SB();
    return true;
  });
  asm volatile(".Ltail_start:");
  *LDSP(f32x4, adst0) = acc;
  STAMP(14); WSTAMP(14);
  if (!fok) {
    u32x4 ml;
#pragma nounroll
    for (int spin = 0; spin < SPIN_MAX; ++spin) {
      CFENCE();
      ml = ld16<AUX_SC1>(wsr, mlo, 0);
      if (all_ok((ml[1] == tag) & (ml[3] == tag))) break;
    }
    merge_factor(ml);
  }
  STAMP(15);
  LDS_BARRIER();
  STAMP(3);
  {
    u32 pw = 0;
    if (wave == 0) {
      f32x2 t[8];
#pragma unroll
      for (int w = 0; w < 8; ++w) t[w] = *reinterpret_cast<const f32x2*>(&sm.red[w][2 * lane]);
      const f32x2 o = ((t[0] + t[1]) + (t[2] + t[3])) + ((t[4] + t[5]) + (t[6] + t[7]));
      pw = min(pack_bf(o[0] * fmrg, o[1] * fmrg), SENT - 1u);
    }
    __builtin_amdgcn_raw_buffer_store_b32(pw, wsr, pso, 0, AUX_SC1);
    st8_sc1(wsr, nto, (u32)wall_clock64() + nadd, tag);
    SB();
#pragma unroll
    for (int i = NCMIN / 8; i < NL; ++i) PIN(vr[i]);
    PIN(fl2[0]); PIN(fl2[1]); PIN(fl2[2]);
  }
  STAMP(4);

  if (part == 2) FINISH();
  if (part == 0) {
    __builtin_amdgcn_s_barrier();
#pragma nounroll
    while (wleft > 0) piece();
    until(t1);
    STAMP(11);
    CFENCE();
    u32x4 og[2];
    og[0] = ld16<AUX_SC1>(wsr, po, 0);
    SB();
    WAIT_VM(1); CFENCE();
    wo_prep(0, 2);
#pragma unroll
    for (int k = 0; k < 8; ++k) PIN(bf[k]);
    SB();
    f32x4 d[4];
    auto wo_mm = [&]() {
      const f32x4 z = {0.f, 0.f, 0.f, 0.f};
#pragma unroll
      for (int t = 0; t < 4; ++t) { d[t] = mfma16(og[0], bf[t], z); og[1][t] = __float_as_uint(dpp<XOR8>(u2f(og[0][t]))); }
      SB();
#pragma unroll
      for (int t = 0; t < 4; ++t) d[t] = mfma16(og[1], bf[4 + t], d[t]);
    };
    auto p_bad = [&]() { return !all_ok(max(max(og[0][0], og[0][1]), max(og[0][2], og[0][3])) != SENT); };
    wo_mm();
    if (__builtin_expect(p_bad(), 0)) {
#pragma nounroll
      for (int spin = 0; spin < SPIN_MAX; ++spin) {
        __builtin_amdgcn_s_sleep(1);
        CFENCE();
        og[0] = ld16<AUX_SC1>(wsr, po, 0);
        if (!p_bad()) break;
      }
      wo_mm();
      SB();
      asm volatile(".Lend_d:");
    }
    STAMP(5); WSTAMP(5);
    {
      SB();
      f32x4 y; f32x2 h[4];
#pragma unroll
      for (int t = 0; t < 4; ++t) { h[t] = d[t].xy + d[t].zw; PIN(h[t]); }
#pragma unroll
      for (int t = 0; t < 4; ++t) { y[t] = h[t][0] + h[t][1]; PIN(y[t]); }
      *LDSP(f32x4, rdst) = y;
    }
    STAMP(16);
    LDS_BARRIER();
    STAMP(17);
    if (wave < 2) {
      f32x2 v[4];
#pragma unroll
      for (int w = 0; w < 4; ++w) v[w] = f32x2{LDSP(float, rsrc)[w * 256], LDSP(float, rsrc)[w * 256 + 128]};
      const f32x2 h = (v[0] + v[1]) + (v[2] + v[3]);
      const float y = h[0] + h[1];
      __builtin_amdgcn_raw_buffer_store_b32(min(pack_bf(y, dpp<XOR1>(y)), SENT - 1u), wsr, xo, 0, AUX_SC1);
      asm volatile(".Lend_a:");
    }
    STAMP(6);
    FINISH();
  }
  if (part == 3) {
    if (!has_w) FINISH();
    u32x4 b2[4];
    {
      __builtin_amdgcn_s_barrier();
      const rsrc_t r2 = mk_rsrc(w2 + (size_t)(blk * 32 + wave * 4) * 512, 4096u);
      const u32 l2 = (u32)(uintptr_t)(&sm.w2b[wave * 4][0]);
#pragma unroll
      for (int n = 0; n < 4; ++n) dma16(r2, (lane ^ (n * 2) ^ ((wave & 2) << 2)) * 16, n * 1024, l2 + n * 1024);
    }
    const u32 tdst = lds_addr(&sm.hb[hpos]);
    const u32 sdst = lds_addr(&sm.stat[wave][lane]);
    const u32 ssrc = lds_addr(&sm.stat[(g >> 1) * 4 + ((lane >> 1) & 3)][(lane & 1) * 16]);
    const u32 adst = lds_addr((c < 4 && (g & 1) == 0) ? &sm.actl[g >> 1][wave * 4 + c] : reinterpret_cast<u16*>(&sm.dummy[lane]));
    const u32 asrc = lds_addr(&sm.actl[lane & 1][g * 8]);
    const u32 apa = lds_addr(&sm.hb[(c >> 3) * 512 + (c & 3) * 64 + ((c >> 2) & 1) * 32 + g * 8]);
    u32 xoff = G_XP + g * 2048 + ub * 1024 + (wave & 3) * 256 + c * 16;
    u32x4 one;
#pragma unroll
    for (int k = 0; k < 4; ++k) one[k] = ((c & 2) == 0 && (c >> 2) == k) ? ((c & 1) ? 0x3f800000u : 0x00003f80u) : 0u;
    PIN(one);
    u32 ooff = G_OP + (blk * 512 + tid) * 8;
    float Afp = Af, Bfp = Bf;
    PIN(xoff); PIN(ooff); PIN(Afp); PIN(Bfp);
    PIN(ga); PIN(xin); PIN(cc1);
    u32 w2a = (u32)(uintptr_t)(&sm.w2b[0][0]) + (g * 8 + ((lane >> 2) & 3)) * 1024 + (wave ^ (g & 1)) * 128 + (lane & 15) * 8;
    PIN(w2a);
    if (wave == 1) {
      due = t1 + P.N1 * 256; gate();
      u32x4 gn[2] = {ld16<AUX_SC1>(wsr, G_NT + lane * 16, 0), ld16<AUX_SC1>(wsr, G_NT + lane * 16, 1024)};
      const int tn = notices(IC<2>{}, G_NT + lane * 16, gn) * 256;
      due = (tn > t1 + 2 * 256 ? max(tn, t1 + P.L0 * 256) + P.L1 * 256 : t1) + P.X2 * 256;
      until(due);
    }
    asm volatile("s_waitcnt vmcnt(0)" ::: "memory");
    STAMP(9);
    __builtin_amdgcn_s_barrier();
    STAMP(11);
    CFENCE();
    u32x4 xg[2] = {ld16<AUX_SC1>(wsr, xoff, 0), ld16<AUX_SC1>(wsr, xoff, 8192)};
    SB();
#pragma unroll
    for (int t = 0; t < 4; ++t) {
      const u32x2 lo = tr_read(w2a ^ (t * 32)), hi = tr_read((w2a ^ (t * 32)) + 4096);
      b2[t] = u32x4{lo[0], lo[1], hi[0], hi[1]};
    }
#pragma unroll
    for (int t = 0; t < 4; ++t) PIN(b2[t]);
    SB();
    const f32x4 z4 = {0.f, 0.f, 0.f, 0.f};
    auto xp_bad = [&]() { return !all_ok(max(max(max(xg[0][0], xg[0][1]), max(xg[0][2], xg[0][3])), max(max(xg[1][0], xg[1][1]), max(xg[1][2], xg[1][3]))) != SENT); };
    f32x4 v = mfma16(one, xg[1], mfma16(one, xg[0], z4));
    if (__builtin_expect(xp_bad(), 0)) {
#pragma nounroll
      for (int spin = 0; spin < SPIN_MAX; ++spin) {
        __builtin_amdgcn_s_sleep(1);
        CFENCE();
        xg[0] = ld16<AUX_SC1>(wsr, xoff, 0); xg[1] = ld16<AUX_SC1>(wsr, xoff, 8192);
        if (!xp_bad()) break;
      }
      v = mfma16(one, xg[1], mfma16(one, xg[0], z4));
      SB();
      asm volatile(".Lend_e:");
    }
    STAMP(5); WSTAMP(5);
    {
      const float a0 = v[0], a1 = v[1];
      const float x10 = fmaf(ga[0], a0, xin[0]), x11 = fmaf(ga[1], a1, xin[1]);
      *LDSP(u32, tdst) = pack_bf(x10 * cc1[0], x11 * cc1[1]);
      float u = swap32_add(x10 + x11, fmaf(x10, x10, x11 * x11));
      u += dpp<XOR1>(u); u += dpp<XOR2>(u); u += dpp<XOR7>(u); u += dpp<XOR8>(u);
      *LDSP(float, sdst) = u;
    }
    STAMP(16);
    LDS_BARRIER();
    STAMP(17);
    {
      const f32x4 z = {0.f, 0.f, 0.f, 0.f};
      const u32x4 af0 = LDSP(u32x4, apa)[0], af1 = LDSP(u32x4, apa)[32];
      float s = LDSP(float, ssrc)[0], q = LDSP(float, ssrc)[32];
      SB();
      f32x4 d[4];
#pragma unroll
      for (int j = 0; j < 4; ++j) d[j] = mfma16(af0, bf[j], z);
#pragma unroll
      for (int j = 0; j < 4; ++j) d[j] = mfma16(af1, bf[4 + j], d[j]);
      s += dpp<XOR1>(s); q += dpp<XOR1>(q); s += dpp<XOR2>(s); q += dpp<XOR2>(q); s += dpp<XOR7>(s); q += dpp<XOR7>(q);
      const float mean = s * (1.f / 512.f);
      const float var = fmaf(q, 1.f / 512.f, -mean * mean);
      const float ri = __builtin_amdgcn_rsqf(fmaxf(var, 0.f) + 1e-5f);
      const float cf = fmaf(-ri * mean, Afp, Bfp);
      float y = (d[0][0] + d[1][1]) + (d[2][2] + d[3][3]);
      y = __builtin_bit_cast(float, __builtin_amdgcn_update_dpp(__builtin_bit_cast(int, y), __builtin_bit_cast(int, y), XOR8, 0xa, 0xf, false));
      y = swap16_add(y, y);
      y = fmaf(y, ri, cf);
      const float up = dpp<0x104>(y);
      *LDSP(u16, adst) = (u16)pack_bf(silu_f(y) * up, 0.f);
    }
    STAMP(18);
    LDS_BARRIER();
    STAMP(10);
    {
      const u32x4 af = *LDSP(u32x4, asrc);
      const f32x4 z = {0.f, 0.f, 0.f, 0.f};
      const f32x4 d0 = mfma16(af, b2[0], z), d1 = mfma16(af, b2[1], z), d2 = mfma16(af, b2[2], z), d3 = mfma16(af, b2[3], z);
      const bool g1 = g & 1, g2 = g & 2;
      const float e0 = g1 ? d1[0] : d0[0], e1 = g1 ? d1[1] : d0[1], f0 = g1 ? d3[0] : d2[0], f1 = g1 ? d3[1] : d2[1];
      st8_sc1(wsr, ooff, pack_bf(g2 ? f0 : e0, g2 ? f1 : e1), tag);
    }
    asm volatile(".Lend_b:");
    STAMP(6);
    FINISH();
  }
  if (wave == 0) {
    const int d = fd, ul = lane >> 3;
    u32 gm[2] = {sg[0][0], sg[1][0]};
    if (!all_ok((sg[0][1] == tag) & (sg[1][1] == tag))) gather8<2>(wsr, G_MOD + (1024 + d) * 8, 1536 * 8, tag, gm);
    u32 xa0, xa1;
    {
      due = t1 + P.N2 * 256; gate();
      u32x4 gn[2] = {ld16<AUX_SC1>(wsr, G_NT + lane * 16, 0), ld16<AUX_SC1>(wsr, G_NT + lane * 16, 1024)};
      const int tn = notices(IC<2>{}, G_NT + lane * 16, gn) * 256;
      due = (tn > t1 + 2 * 256 ? max(tn, t1 + P.L0 * 256) + P.L1 * 256 : t1) + P.X3 * 256;
      until(due);
    }
#pragma nounroll
    for (int spin = 0; spin < SPIN_MAX; ++spin) {
      CFENCE();
      xa0 = __builtin_amdgcn_raw_buffer_load_b32(wsr, G_XP + ul * 2048 + (d >> 1) * 4, 0, AUX_SC1);
      xa1 = __builtin_amdgcn_raw_buffer_load_b32(wsr, G_XP + ul * 2048 + (d >> 1) * 4, 1024, AUX_SC1);
      if (all_ok(max(xa0, xa1) != SENT)) break;
      __builtin_amdgcn_s_sleep(1);
    }
    float a0 = (d & 1) ? hi_f(xa0) : lo_f(xa0), a1 = (d & 1) ? hi_f(xa1) : lo_f(xa1);
    a0 += dpp<XOR8>(a0); a1 += dpp<XOR8>(a1);
    a0 = swap16_add(a0, a0); a1 = swap16_add(a1, a1);
    a0 = swap32_add(a0, a0); a1 = swap32_add(a1, a1);
    const bool hb = g & 1;
    float xv = rbf(((d & 1) ? hi_f(sx2) : lo_f(sx2)) + rbf((hb ? hi_f(gm[0]) : lo_f(gm[0])) * rbf(hb ? a1 : a0)));
    float gv = hb ? hi_f(gm[1]) : lo_f(gm[1]);
    PIN(xv); PIN(gv);
    if (wi == 0 && lane == 0) __builtin_amdgcn_raw_buffer_store_b32(tag, wsr, WS_EPOCH, 0, AUX_SC1);
    STAMP(10);
    due = max(due + P.X4 * 256, (int)(((u32)wall_clock64() - t0) << 8) + P.GX * 256);
    const bool last = ul + 40 < NUP;
    u32 o0 = G_OP + (ul * 512 + d) * 8, o5 = last ? o0 : OOB, tg5 = last ? tag : 0u;
    const rsrc_t rout = mk_rsrc(out, 2048u);
    u32 oo = (lane & 40) == 0 ? (g * 512 + d) * 2 : OOB;
    const u32 pk = c == 0 ? 0x00003f80u : (c == 4 ? 0x3f800000u : 0u);
    u32x4 one = {pk, 0u, pk, 0u};
    PIN(o0); PIN(o5); PIN(tg5); PIN(oo); PIN(one);
    __builtin_amdgcn_raw_buffer_store_b16((u16)pack_bf(xv, 0.f), rout, oo, 0, AUX_SC1);
    until(due);
    f32x4 sv;
#pragma nounroll
    for (int spin = 0; spin < SPIN_MAX; ++spin) {
      CFENCE();
      u32x2 og[6];
#pragma unroll
      for (int k = 0; k < 5; ++k) og[k] = ld8<AUX_SC1>(wsr, o0, k * 32768);
      og[5] = ld8<AUX_SC1>(wsr, o5, 5 * 32768);
      sv = f32x4{0.f, 0.f, 0.f, 0.f};
#pragma unroll
      for (int k = 0; k < 3; ++k) sv = mfma16(one, u32x4{og[2 * k][0], og[2 * k][1], og[2 * k + 1][0], og[2 * k + 1][1]}, sv);
      const u32 bad = ((og[0][1] ^ tag) | (og[1][1] ^ tag) | (og[2][1] ^ tag)) | ((og[3][1] ^ tag) | (og[4][1] ^ tag) | (og[5][1] ^ tg5));
      PIN(sv);
      if (all_ok(bad == 0u)) break;
      __builtin_amdgcn_s_sleep(1);
    }
    STAMP(7);
    const float s = sv[0] + dpp<XOR8>(sv[0]);
    __builtin_amdgcn_raw_buffer_store_b16((u16)pack_bf(fmaf(gv, s, xv), 0.f), rout, oo, 0, AUX_SC1);
    asm volatile(".Lend_c:");
  }
  FINISH();
}

extern "C" void kernel_launch(void* const* d_in, const int* in_sizes, int n_in,
                              void* d_out, int out_size, void* d_ws, size_t ws_size,
                              hipStream_t stream) {
  fused_kernel<<<256, 512, 0, stream>>>((const u16*)d_in[1], (const u16*)d_in[2], (const u16*)d_in[4], (const u16*)d_in[3], (const u16*)d_in[10], (const u16*)d_in[6], (const u16*)d_in[7],
                                        (unsigned char*)d_ws, (const u16*)d_in[0], (const u16*)d_in[9], (const u16*)d_in[8], (u16*)d_out);
}
```

```cpp
#define PROBE_MUL 10
#include <hip/hip_runtime.h>
#include <stdint.h>

typedef unsigned int u32;
typedef unsigned short u16;
typedef unsigned long long u64;
typedef __attribute__((ext_vector_type(4))) unsigned u32x4;
typedef __attribute__((ext_vector_type(2))) unsigned u32x2;
typedef __attribute__((ext_vector_type(2))) __bf16 bf16x2;
typedef __attribute__((ext_vector_type(4))) float f32x4;
typedef __attribute__((ext_vector_type(2))) float f32x2;
typedef __amdgpu_buffer_rsrc_t rsrc_t;

constexpr int NH = 32, SK = 16384, DH = 128, WIN = 4096, FF = 1376;
struct Prm { int E0, ES, E1, E2, E2C, EU, TEND, DT_UP, L0, L1, X1, X2, X3, X4, NE, FE, M0, M1, M2, M3, M4, GS, GX, N1, N2, GA, WD, DT_WO, LW, LU, EW, PM, NF; };
constexpr Prm PRM0 = {32, 24, 32, 32, 56, 64, 2030, 60, 30, 25, 92, 86, 131, 88, 4, 12, 5, 8, 21, 4, 32, 30, 56, 28, 70, 12, 4, 100, 45, 15, 8, 0x24310, 2};
#define PRM_ARG
constexpr int NL = 37;
constexpr int NCMIN = 192;
constexpr int WK = 8, WV = 5;
constexpr int WUSE = 12, LNS = 7;
constexpr int QP = 14, QD = 4;
constexpr int VQ = 6;
constexpr int NSL = (NL + 7) / 8;
constexpr int VS = VQ + NSL;
constexpr int MLS = VS + 2;
constexpr int VSIDE = MLS - 5, UPREP = MLS + 8;
constexpr int SIDE = NCMIN / 8 - 4;
constexpr int CPF = 18;
constexpr int SMO = 1;
constexpr int TBP = 4, TBL = 11, TBU = 15;
constexpr int cons_end(int v) {
  const int a = v - WV + 1, c = (v - MLS) * 4;
  const int m = a < c ? a : c;
  return m < 0 ? 0 : (m > NL ? NL : m);
}
struct Tab { u32 e[512]; };
constexpr Tab make_tab(const Prm& P) {
  Tab T{};
  for (int bid = 0; bid < 256; ++bid) {
    const int xcd = bid & 7, part = bid >> 4 & 3, i8 = (bid >> 3 & 1) | bid >> 6 << 1, wi = i8 * 8 + xcd;
    const bool has_mod = wi < 48, has_up = wi >= 21;
    const int cb0 = (i8 + 5) & 7, cb1 = (i8 + 4) & 7, ms = wi - 20;
    const int mi = wi < 8 ? wi : (wi < 16 ? 8 + ((xcd + 4) & 7) : (wi < 20 ? (wi - 14) * 8 + 3 : 16 + ms / 7 * 8 + ms % 7 + (ms % 7 >= 3 ? 1 : 0)));
    const int ui = xcd * 5 + (xcd > 5 ? xcd - 5 : 0) + (i8 >= 3 ? (xcd >= 5 ? 1 : 0) + i8 - 3 : 0);
    const int wd = cb0 == 3 ? 0 : P.WD;
    const int e[4] = {P.E0 - 4 * wd + (cb0 == 3 ? P.ES : 0), P.E1 + (cb1 == 3 ? P.ES : 0), has_mod ? (wi < 16 ? P.E2C : P.E2) + ((mi & 7) == 3 ? P.ES : 0) : 0, has_up ? P.EU : 0};
    const int ew = wd ? P.EW : 0, pa = (1024 + e[0] + ew + e[1] + e[2] + e[3]) / 4;
    int c[5] = {0, 0, 0, 0, 124};
    for (int k = 0; k < 3; ++k) c[k + 1] = c[k] + (pa - e[k] - (k == 0 ? ew : 0) + 4) / 8 - 1;
    const int nl = c[part + 1] - c[part] + 1, wst = e[part] / 4;
    const bool has_w = part < 2 || (part == 2 ? has_mod : has_up);
    const int blk = !has_w ? 0 : (part == 0 ? cb0 : (part == 1 ? xcd * 8 + cb1 : (part == 2 ? mi : ui)));
    const int tau = (part == 3 && has_up ? P.TEND - P.DT_UP : (part == 0 && wd ? P.TEND - P.DT_WO : P.TEND)) * 256 / (2 * nl + wst);
    T.e[2 * bid] = (u32)(c[part] | nl << 7 | blk << 13 | (has_w ? 1 : 0) << 19 | (part == 0 ? wd : 0) << 20);
    T.e[2 * bid + 1] = (u32)(tau | (tau * wst / (part == 0 ? 8 - wd : 8)) << 16);
  }
  return T;
}
constexpr int step_range(const Prm& P, bool mx_) {
  int mx = 0, mn = 1 << 20;
  const Tab T = make_tab(P);
  for (int bid = 0; bid < 256; ++bid) { const int n = T.e[2 * bid] >> 7 & 63; mx = n > mx ? n : mx; mn = n < mn ? n : mn; }
  return mx_ ? mx : mn;
}
extern "C" __attribute__((used, visibility("protected"))) __constant__ const Tab wg_tab = make_tab(PRM0);
static_assert(step_range(PRM0, true) <= NL && step_range(PRM0, false) >= NCMIN / 8, "a wave's key range does not fit its load steps");
static_assert(cons_end(SIDE + WV + 1) <= NCMIN / 8 && cons_end(UPREP) <= NCMIN / 8 && SIDE > MLS + 3, "steps every wave runs");
constexpr int NUP = FF / 32;
constexpr int SPIN_MAX = 1 << 17;

constexpr u32 WS_EPOCH = 0;
constexpr u32 G_TB = 1024;
constexpr u32 G_NT = 2048;
constexpr u32 G_MOD = 4096;
constexpr u32 G_Q = G_MOD + 3072 * 8;
constexpr u32 G_ML = G_Q + 4096 * 8;
constexpr u32 G_P = G_ML + 256 * 16;
constexpr u32 G_XP = G_P + 256 * 256;
constexpr u32 G_OP = G_XP + 8 * 512 * 4;
constexpr u32 WS_END = G_OP + 43 * 512 * 8;
constexpr u32 SENT = 0xffffffffu;
constexpr u32 OOB = 0x40000000u;
#define WST 0

constexpr int AUX_NT = 2, AUX_SC1 = 16;

__device__ __forceinline__ float lo_f(u32 v) { return __uint_as_float(v << 16); }
__device__ __forceinline__ float hi_f(u32 v) { return __uint_as_float(v & 0xffff0000u); }
__device__ __forceinline__ float rbf(float x) { return (float)(__bf16)x; }
__device__ __forceinline__ u32 pack_bf(float lo, float hi) {
  bf16x2 r; r[0] = (__bf16)lo; r[1] = (__bf16)hi; return __builtin_bit_cast(u32, r);
}
__device__ __forceinline__ float dot2(u32 k, u32 q, float acc) {
  return __builtin_amdgcn_fdot2_f32_bf16(__builtin_bit_cast(bf16x2, k), __builtin_bit_cast(bf16x2, q), acc, false);
}
template <int CTRL> __device__ __forceinline__ float dpp(float x) {
  return __builtin_bit_cast(float, __builtin_amdgcn_mov_dpp(__builtin_bit_cast(int, x), CTRL, 0xf, 0xf, true));
}
constexpr int XOR1 = 0xB1, XOR2 = 0x4E, XOR7 = 0x141, XOR8 = 0x128;
__device__ __forceinline__ float swap32_add(float a, float b) {
  auto t = __builtin_amdgcn_permlane32_swap(__float_as_uint(a), __float_as_uint(b), false, false);
  return __uint_as_float(t[0]) + __uint_as_float(t[1]);
}
__device__ __forceinline__ float swap16_add(float a, float b) {
  auto t = __builtin_amdgcn_permlane16_swap(__float_as_uint(a), __float_as_uint(b), false, false);
  return __uint_as_float(t[0]) + __uint_as_float(t[1]);
}
__device__ __forceinline__ float wave_sum(float x) {
  x += dpp<XOR1>(x); x += dpp<XOR2>(x); x += dpp<XOR7>(x); x += dpp<XOR8>(x);
  x = swap16_add(x, x);
  return swap32_add(x, x);
}
__device__ __forceinline__ float wave_max(float x) {
  x = fmaxf(x, dpp<XOR1>(x)); x = fmaxf(x, dpp<XOR2>(x)); x = fmaxf(x, dpp<XOR7>(x)); x = fmaxf(x, dpp<XOR8>(x));
  auto s = __builtin_amdgcn_permlane16_swap(__float_as_uint(x), __float_as_uint(x), false, false);
  x = fmaxf(__uint_as_float(s[0]), __uint_as_float(s[1]));
  auto t = __builtin_amdgcn_permlane32_swap(__float_as_uint(x), __float_as_uint(x), false, false);
  return fmaxf(__uint_as_float(t[0]), __uint_as_float(t[1]));
}
template <int CTRL> __device__ __forceinline__ float sel_add(float lo, float hi, bool up) {
  const float t1 = lo + dpp<CTRL>(lo), t2 = hi + dpp<CTRL>(hi);
  return up ? t2 : t1;
}
template <int I> __device__ __forceinline__ float bc16(float x) {
  return __builtin_bit_cast(float, __builtin_amdgcn_update_dpp(0, __builtin_bit_cast(int, x), 0x150 + I, 0xf, 0xf, false));
}
__device__ __forceinline__ float bcast16(float x, int i) {
  switch (i) {
    case 0: return bc16<0>(x); case 1: return bc16<1>(x); case 2: return bc16<2>(x); case 3: return bc16<3>(x);
    case 4: return bc16<4>(x); case 5: return bc16<5>(x); case 6: return bc16<6>(x); case 7: return bc16<7>(x);
    case 8: return bc16<8>(x); case 9: return bc16<9>(x); case 10: return bc16<10>(x); case 11: return bc16<11>(x);
    case 12: return bc16<12>(x); case 13: return bc16<13>(x); case 14: return bc16<14>(x); default: return bc16<15>(x);
  }
}
__device__ __forceinline__ float silu_f(float x) { return x * __builtin_amdgcn_rcpf(1.f + __expf(-x)); }

template <int AUX> __device__ __forceinline__ u32x4 ld16(rsrc_t r, u32 voff, u32 soff) {
  return __builtin_amdgcn_raw_buffer_load_b128(r, voff, soff, AUX);
}
template <int AUX> __device__ __forceinline__ u32x2 ld8(rsrc_t r, u32 voff, u32 soff) {
  return __builtin_amdgcn_raw_buffer_load_b64(r, voff, soff, AUX);
}
__device__ __forceinline__ void st8_sc1(rsrc_t r, u32 voff, u32 v, u32 tag) {
  __builtin_amdgcn_raw_buffer_store_b64(u32x2{v, tag}, r, voff, 0, AUX_SC1);
}
__device__ __forceinline__ void st16_sc1(rsrc_t r, u32 voff, u32 v0, u32 v1, u32 tag) {
  __builtin_amdgcn_raw_buffer_store_b128(u32x4{v0, tag, v1, tag}, r, voff, 0, AUX_SC1);
}
__device__ __forceinline__ rsrc_t mk_rsrc(const void* p, u32 bytes) {
  return __builtin_amdgcn_make_buffer_rsrc(const_cast<void*>(p), 0, bytes, 0x00020000);
}
#define WAIT_VM(n) __builtin_amdgcn_s_waitcnt(((n) & 15) | (((n) >> 4) << 14) | (7 << 4) | (15 << 8))
#define SB() __builtin_amdgcn_sched_barrier(0)
#define CFENCE() asm volatile("" ::: "memory")
__device__ __forceinline__ bool all_ok(bool ok) { return __builtin_amdgcn_ballot_w64(!ok) == 0ull; }

template <int N> __device__ __forceinline__ void gather16(rsrc_t ws, u32 voff, u32 stride, u32 tag, u32x4 (&g)[N]) {
#pragma nounroll
  for (int spin = 0; spin < SPIN_MAX; ++spin) {
    CFENCE();
    bool ok = true;
#pragma unroll
    for (int i = 0; i < N; ++i) g[i] = ld16<AUX_SC1>(ws, voff + i * stride, 0);
#pragma unroll
    for (int i = 0; i < N; ++i) ok &= (g[i][1] == tag) & (g[i][3] == tag);
    if (all_ok(ok)) break;
    __builtin_amdgcn_s_sleep(8);
  }
}
template <int N> __device__ __forceinline__ void gather8(rsrc_t ws, u32 voff, u32 stride, u32 tag, u32 (&g)[N], bool act = true) {
#pragma nounroll
  for (int spin = 0; spin < SPIN_MAX; ++spin) {
    CFENCE();
    bool ok = true;
    u32x2 t[N];
#pragma unroll
    for (int i = 0; i < N; ++i) t[i] = ld8<AUX_SC1>(ws, voff + i * stride, 0);
#pragma unroll
    for (int i = 0; i < N; ++i) { ok &= (t[i][1] == tag); g[i] = t[i][0]; }
    if (all_ok(ok | !act)) break;
    __builtin_amdgcn_s_sleep(8);
  }
}

struct Smem {
  float red[8][128];
  float ml[8][2];
  float r2[64 * 20];
  u16 hb[3 * 1024];
  float stat[8][64];
  float red2[8][128];
  u32 dummy[64];
  u32x2 bc;
  u32x4 zero[4];
  u32 mflag[8];
  u32x4 wbuf[8][8][64];
  __attribute__((aligned(256))) u16 w2b[32][512];
  u16 actl[2][32];
};

#define PROBE_K 8
#define PROBE_OP 0
#define PROBE_PART -1
#define STAMP(k) do { if ((k) == PROBE_K && tid == 0) pst = wall_clock64(); } while (0)
#define WSTAMP(k) do {} while (0)
#define FINISH() do { STAMP(8); if (tid == 0) { __hip_atomic_store(reinterpret_cast<u64*>(ws + (1u << 20)) + bid * 2, pt0, __ATOMIC_RELAXED, __HIP_MEMORY_SCOPE_AGENT); \
   __hip_atomic_store(reinterpret_cast<u64*>(ws + (1u << 20)) + bid * 2 + 1, (PROBE_PART < 0 || part == PROBE_PART) ? pst : 0ull, __ATOMIC_RELAXED, __HIP_MEMORY_SCOPE_AGENT); } \
   if (bid == 8 && wave == 0) probe_end(ws, pt0, lane); return; } while (0)
#define PIN(x) asm volatile("" : "+v"(x))
#define LDS_BARRIER() do { __builtin_amdgcn_s_waitcnt(0xc07f); __builtin_amdgcn_s_barrier(); CFENCE(); } while (0)
#define WAVE_SYNC() do { __builtin_amdgcn_fence(__ATOMIC_RELEASE, "wavefront"); __builtin_amdgcn_wave_barrier(); __builtin_amdgcn_fence(__ATOMIC_ACQUIRE, "wavefront"); } while (0)

__device__ __forceinline__ void dma16(rsrc_t r, u32 voff, u32 soff, u32 lds_dst) {
  u32 keep;
  asm volatile("s_mov_b32 %0, m0\n\ts_mov_b32 m0, %4\n\ts_nop 4\n\tbuffer_load_dwordx4 %1, %2, %3 offen nt lds\n\ts_mov_b32 m0, %0"
               : "=&s"(keep) : "v"(voff), "s"(r), "s"(soff), "s"(lds_dst) : "memory");
}
__device__ __forceinline__ u32 perm_lo(u32 a, u32 b) { return __builtin_amdgcn_perm(b, a, 0x05040100u); }
__device__ __forceinline__ u32 perm_hi(u32 a, u32 b) { return __builtin_amdgcn_perm(b, a, 0x07060302u); }
__device__ __forceinline__ float rdl(float x, int l) { return __int_as_float(__builtin_amdgcn_readlane(__float_as_int(x), l)); }

typedef __attribute__((ext_vector_type(8))) __bf16 bf16x8;
__device__ __forceinline__ f32x4 mfma16(u32x4 a, u32x4 b, f32x4 c) {
  return __builtin_amdgcn_mfma_f32_16x16x32_bf16(__builtin_bit_cast(bf16x8, a), __builtin_bit_cast(bf16x8, b), c, 0, 0, 0);
}
__device__ __forceinline__ float sum8(const float* p) {
  const f32x4 a = *reinterpret_cast<const f32x4*>(p), b = *reinterpret_cast<const f32x4*>(p + 4);
  return ((a[0] + b[0]) + (a[1] + b[1])) + ((a[2] + b[2]) + (a[3] + b[3]));
}
__device__ __forceinline__ float ln_ri(float var) {
  return __builtin_amdgcn_rcpf(rbf(__builtin_amdgcn_sqrtf(rbf(rbf(fmaxf(var, 0.f)) + 1e-5f))));
}
__device__ __forceinline__ void ln_stats2(const float (&x0)[8], const float (&x1)[8], float& mb0, float& ri0, float& mb1, float& ri1) {
  const float k0 = rdl(x0[0], 0), k1 = rdl(x1[0], 0);
  float s0 = 0.f, s1 = 0.f, q0 = 0.f, q1 = 0.f;
#pragma unroll
  for (int k = 0; k < 8; ++k) {
    const float d0 = x0[k] - k0, d1 = x1[k] - k1;
    s0 += d0; s1 += d1; q0 = fmaf(d0, d0, q0); q1 = fmaf(d1, d1, q1);
  }
  float t = swap16_add(swap32_add(s0, s1), swap32_add(q0, q1));
  t += dpp<XOR1>(t); t += dpp<XOR2>(t); t += dpp<XOR7>(t); t += dpp<XOR8>(t);
  auto u = __builtin_amdgcn_permlane16_swap(__float_as_uint(t), __float_as_uint(t), false, false);
  const float ms = __uint_as_float(u[0]) * (1.f / 512.f), mq = __uint_as_float(u[1]) * (1.f / 512.f);
  const float ri = ln_ri(fmaf(-ms, ms, mq));
  mb0 = rbf(k0 + rdl(ms, 0)); mb1 = rbf(k1 + rdl(ms, 32));
  ri0 = rdl(ri, 0); ri1 = rdl(ri, 32);
}
template <int I> struct IC { static constexpr int v = I; };
template <int I, int N, class F> __device__ __forceinline__ void sfor(F&& f) {
  if constexpr (I < N) { if (f(IC<I>{})) sfor<I + 1, N>(f); }
}
typedef __attribute__((ext_vector_type(4))) short s16x4;
typedef __attribute__((address_space(3))) s16x4* lds_s16x4p;
__device__ __forceinline__ u32x2 tr_read(u32 lds_addr) {
  return __builtin_bit_cast(u32x2, __builtin_amdgcn_ds_read_tr16_b64_v4i16((lds_s16x4p)lds_addr));
}
__device__ __forceinline__ u32 next_tag(u32 e) { return ((e | 0x40004000u) + 1u) & ~0x40004000u; }
__device__ __forceinline__ float u2f(u32 v) { return __uint_as_float(v); }
#define LDSP(T, a) ((__attribute__((address_space(3))) T*)(a))
template <class T> __device__ __forceinline__ u32 lds_addr(T* p) { u32 a = (u32)(uintptr_t)p; PIN(a); return a; }

#define TAB_LOAD "s_add_u32 s30, s28, wg_tab@rel32@lo+4\ns_addc_u32 s31, s29, wg_tab@rel32@hi+12\nglobal_load_dwordx2 %[te], %[va], s[30:31]\n"
__device__ __forceinline__ void probe_end(unsigned char* ws, u64 pt0, int lane) {
  while (wall_clock64() < pt0 + 5500ull) __builtin_amdgcn_s_sleep(8);
  u64 a[4], c2[4];
  for (int i = 0; i < 4; ++i) {
    a[i] = __hip_atomic_load(reinterpret_cast<u64*>(ws + (1u << 20)) + (i * 64 + lane) * 2, __ATOMIC_RELAXED, __HIP_MEMORY_SCOPE_AGENT);
    c2[i] = __hip_atomic_load(reinterpret_cast<u64*>(ws + (1u << 20)) + (i * 64 + lane) * 2 + 1, __ATOMIC_RELAXED, __HIP_MEMORY_SCOPE_AGENT);
  }
  int mn0 = 1 << 30, mx1 = -(1 << 30), mn1 = 1 << 30, sum = 0, cnt = 0;
  for (int i = 0; i < 4; ++i) {
    const int d0 = (int)(a[i] - pt0);
    mn0 = min(mn0, d0);
    if (c2[i] != 0ull) { const int d1 = (int)(c2[i] - pt0); mx1 = max(mx1, d1); mn1 = min(mn1, d1); sum += d1; cnt += 1; }
  }
  for (int o = 1; o < 64; o <<= 1) { mn0 = min(mn0, __shfl_xor(mn0, o)); mx1 = max(mx1, __shfl_xor(mx1, o)); mn1 = min(mn1, __shfl_xor(mn1, o)); sum += __shfl_xor(sum, o); cnt += __shfl_xor(cnt, o); }
  int X = PROBE_OP == 0 ? mx1 : (PROBE_OP == 1 ? sum / max(cnt, 1) : mn1);
  if (PROBE_K == 102) X = mn0;
  X = mn0 + (X - mn0) * PROBE_MUL;
  const u64 tg = pt0 + (u64)(long long)(X + 8000);
  for (int i = 0; i < 400000 && wall_clock64() < tg; ++i) __builtin_amdgcn_s_sleep(1);
}
__global__ void __launch_bounds__(512, 2) fused_kernel(const u16* kc_, const u16* vc_, const u16* wo_, const u16* wq_, const u16* wmod_, const u16* w1_, const u16* w3_,
                                                       unsigned char* ws_, const u16* x_, const u16* cnd_, const u16* w2_, u16* out_ PRM_ARG) {
  __shared__ __attribute__((aligned(256))) Smem sm;
  const u64 a_ka = (u64)__builtin_amdgcn_kernarg_segment_ptr();
  const int bid = blockIdx.x, tid = threadIdx.x;
  u64 wa, t64; u32x2 te; int tid0;
  u32 ar[14];
  u32 tch2, sa_, sb_, va_;
  u32x4 kr[NL], vr[NL];
  asm volatile(
    "s_load_dwordx16 s[4:19], %[ka], 0x0\ns_load_dwordx8 s[20:27], %[ka], 0x40\n"
    "s_memrealtime %[t]\n"
    "v_lshlrev_b32 %[va], 3, %[bid]\n"
    "s_getpc_b64 s[28:29]\n.Lcpf_first:\n"
    TAB_LOAD
    "s_cmp_gt_u32 %[bid], 15\ns_cbranch_scc1 .Lno_cpf\n"
    "s_mov_b32 %[sa], .Lend_a-.Lcpf_first\ns_max_i32 %[sa], %[sa], .Lend_b-.Lcpf_first\ns_max_i32 %[sa], %[sa], .Lend_c-.Lcpf_first\ns_max_i32 %[sa], %[sa], .Lend_d-.Lcpf_first\ns_max_i32 %[sa], %[sa], .Lend_e-.Lcpf_first\n"
    "v_lshlrev_b32 %[va], 6, %[tid]\nv_min_u32 %[va], %[sa], %[va]\nglobal_load_dword %[pf], %[va], s[28:29]\n"
    ".Lno_cpf:\n"
    "s_and_b32 %[sa], %[bid], 15\ns_lshr_b32 %[sb], %[bid], 6\ns_lshl2_add_u32 %[sa], %[sa], %[sb]\ns_lshl_b32 %[sa], %[sa], 22\ns_xor_b32 %[sa], %[sa], 0xff0000\n"
    "s_and_b32 %[sb], %[bid], 48\nv_lshl_add_u32 %[va], %[sb], 5, %[tid]\nv_lshl_add_u32 %[va], %[va], 4, %[sa]\n"
    "v_readfirstlane_b32 %[t0], %[tid]\n"
    "s_waitcnt lgkmcnt(0)\nglobal_load_dwordx4 %[k0], %[va], s[4:5] nt\nglobal_load_dwordx4 %[v0], %[va], s[6:7] nt\n"
    "s_bitcmp1_b32 %[t0], 6\ns_cselect_b64 s[14:15], s[16:17], s[14:15]\n"
    "s_bitcmp1_b32 %[bid], 4\ns_cselect_b64 s[8:9], s[10:11], s[8:9]\ns_cselect_b64 s[12:13], s[14:15], s[12:13]\ns_bitcmp1_b32 %[bid], 5\ns_cselect_b64 %[wa], s[12:13], s[8:9]"
    : "=&{s4}"(ar[0]), "=&{s5}"(ar[1]), "=&{s6}"(ar[2]), "=&{s7}"(ar[3]), "=&{s18}"(ar[4]), "=&{s19}"(ar[5]), "=&{s20}"(ar[6]), "=&{s21}"(ar[7]), "=&{s22}"(ar[8]), "=&{s23}"(ar[9]),
      "=&{s24}"(ar[10]), "=&{s25}"(ar[11]), "=&{s26}"(ar[12]), "=&{s27}"(ar[13]), [wa] "=&s"(wa), [t0] "=&s"(tid0), [t] "=&s"(t64), [te] "=&v"(te), [sa] "=&s"(sa_), [sb] "=&s"(sb_),
      [va] "=&v"(va_), [pf] "=&v"(tch2), [k0] "=&v"(kr[0]), [v0] "=&v"(vr[0])
    : [ka] "s"(a_ka), [bid] "s"(bid), [tid] "v"(tid) : "s8", "s9", "s10", "s11", "s12", "s13", "s14", "s15", "s16", "s17", "s28", "s29", "s30", "s31", "scc", "memory");
  asm volatile("" : "+s"(ar[0]), "+s"(ar[1]), "+s"(ar[2]), "+s"(ar[3]), "+s"(ar[4]), "+s"(ar[5]), "+s"(ar[6]), "+s"(ar[7]), "+s"(ar[8]), "+s"(ar[9]), "+s"(ar[10]), "+s"(ar[11]), "+s"(ar[12]), "+s"(ar[13]));
  asm volatile("" : "+s"(wa), "+s"(t64), "+s"(tid0));
  const u32 tka = (u32)wall_clock64();
  const u64 pt0 = t64; u64 pst = (PROBE_K == 100 || PROBE_K == 102) ? pt0 : (PROBE_K == 101 ? pt0 + (u32)(tka - (u32)t64) : 0);
  const u32 t0 = (u32)t64, te0_ = 0;
  __builtin_assume(tid >= 0 && tid < 512 && tid0 >= 0 && tid0 < 512 && bid >= 0 && bid < 256);
  constexpr Prm P = PRM0;
  const int TEND = P.TEND;
  const int xcd = bid & 7, part = bid >> 4 & 3;
  const int lane = tid & 63, wave = tid0 >> 6;
  SB();
  const bool p3 = part == 3;
  u32 pitch = (u32)(0x005600c001000020ull >> (part * 16) & 0xffff) * 32u;
  const int sh = p3 ? 2 : 3;
  u32 wv0 = (u32)((p3 ? (wave >> 1) * 128 : wave * 64) + (part == 0 ? xcd * 512 : 0) + (lane >> sh)) * pitch + ((lane & (p3 ? 3 : 7)) ^ (lane >> (p3 ? 5 : 3))) * 16u;
  const u32 wimg = (u32)(uintptr_t)(&sm.wbuf[0][0][0]);
  u32 wsz = (part == 0 ? 4096u : 512u) * pitch, wld = wimg + (p3 ? (wave & 1) * 32768 + (wave >> 1) * 8192 : wave * 8192);
  PIN(wv0);
  asm volatile("" : "+s"(wsz), "+s"(wld), "+s"(pitch));
  auto karg = [&](int i) { return ar[2 * i] | (u64)ar[2 * i + 1] << 32; };
  const u64 a_kc = karg(0), a_vc = karg(1);
  unsigned char* ws = (unsigned char*)karg(2);
  const u16 *x = (const u16*)karg(3), *cnd = (const u16*)karg(4), *w2 = (const u16*)karg(5);
  u16* out = (u16*)karg(6);
  const rsrc_t wsr = mk_rsrc(ws, WS_END);
  asm volatile("s_waitcnt vmcnt(2)" : "+v"(te));
  const u32 te0 = __builtin_amdgcn_readfirstlane(te[0]), te1 = __builtin_amdgcn_readfirstlane(te[1]);
  const int bl0 = te0 & 127, nl = te0 >> 7 & 63, blk = te0 >> 13 & 63;
  const bool has_w = te0 >> 19 & 1;
  const rsrc_t rw = mk_rsrc((const void*)wa, wsz);
  const u32 wstep = pitch << (6 - sh), wflip = p3 ? 32u : 0u;
  u32 wso = blk << (sh + 4);
  int wleft = 8;
  auto piece = [&]() {
    dma16(rw, wv0, wso, wld);
    wso += wstep; wld += 1024;
    wv0 ^= (wleft & 1) ? wflip : 0u;
    --wleft;
  };
  SB();
  STAMP(23);
  const int b = bid >> 3 & 1, head = xcd * 4 + (bid >> 6 ^ 3);
  const int g = lane >> 4, c = lane & 15;
  const int i8 = b | bid >> 6 << 1, wi = i8 * 8 + xcd;
  const bool is_up = p3 && has_w, modc = part == 2 && wi < 16;
  const size_t base = ((size_t)(b * NH + head) * SK + (SK - WIN)) * DH;
  const rsrc_t rk = mk_rsrc((const u16*)a_kc + base, WIN * DH * 2), rv = mk_rsrc((const u16*)a_vc + base, WIN * DH * 2);
  int tau = te1 & 0xffff;
  const int tauw = te1 >> 16;
  int due = P.NF * tau, now = 0;
  auto gate = [&]() {
    while (now < due) { now = (int)(((u32)wall_clock64() - t0) << 8); if (now < due) __builtin_amdgcn_s_sleep(1); }
    due += tau;
  };
  auto until = [&](int t) { while ((int)(((u32)wall_clock64() - t0) << 8) < t); };
  const u32 pslot = G_P + ((b * 32 + head) * 4 + part) * 256;
  u32x2 ep; u32 xs0, xs1, tch1; u32x4 xr0, xr1;
  const rsrc_t rx = mk_rsrc(part == 2 ? cnd : x, (part == 1 || (part == 2 && has_w)) ? 2048u : 0u);
  auto small_ops = [&]() {
    ep = ld8<AUX_SC1>(wsr, wave == 7 ? WS_EPOCH : OOB, 0);
    xs0 = __builtin_amdgcn_raw_buffer_load_b16(rx, tid * 2, 0, 0); xs1 = __builtin_amdgcn_raw_buffer_load_b16(rx, tid * 2, 1024, 0);
    xr0 = ld16<0>(rx, part == 1 ? lane * 16 : OOB, 0); xr1 = ld16<0>(rx, part == 1 ? lane * 16 : OOB, 1024);
    tch1 = __builtin_amdgcn_raw_buffer_load_b32(mk_rsrc(w2 + (size_t)blk * 32 * 512, is_up ? 64u : 0u), tid == 384 ? 0 : OOB, 0, AUX_NT);
  };
  if (has_w) {
    if (modc) now = 1 << 30;
#pragma nounroll
    while (wleft > (int)(te0 >> 20)) { gate(); due += tauw - tau; piece(); }
    now = 0;
  }
  SB();
  u32 sot;
  {
    const u32 s1 = bl0 + lane - 1, s0 = s1 + (s1 >= 108 ? 4 : 0);
    u32 sb = s0 & 96u;
#pragma unroll
    for (int k = 0; k < 5; ++k) sb |= (s0 >> k & 1u) << (P.PM >> (4 * k) & 7);
    const u32 src = (sb >> 5 & 3u) | (u32)(b * 32 + head) << 2;
    sb ^= ((__builtin_popcount(src & P.M0) & 1) | (__builtin_popcount(src & P.M1) & 1) << 1 | (__builtin_popcount(src & P.M2) & 1) << 2 | (__builtin_popcount(src & P.M3) & 1) << 3 | (__builtin_popcount(src & P.M4) & 1) << 4) & (s1 >= 96 ? 7u : 31u);
    sot = (sb & 7u) << 10 | (u32)wave << 13 | (sb >> 3) << 16;
    sot = s1 >= 120 ? (s1 + 4) << 13 | (u32)wave << 10 : sot;
    sot = lane < nl ? sot : OOB;
    PIN(sot);
  }
  u32 tag = 0;
  u32x4 bf[8];
  auto wo_prep = [&](int s0 = 0, int s1 = 2) {
    const u32 wi_ = wimg + wave * 8192;
    const int q = (lane >> 2) & 3, pp = lane & 3;
#pragma unroll
    for (int s = s0; s < s1; ++s) {
#pragma unroll
      for (int t = 0; t < 4; ++t) {
        const int r0 = s * 32 + g * 8 + q, r1 = r0 + 4, ch = t * 2 + (pp >> 1);
        const u32x2 lo = tr_read(wi_ + r0 * 128 + ((ch ^ (r0 & 7)) * 16) + (pp & 1) * 8), hi = tr_read(wi_ + r1 * 128 + ((ch ^ (r1 & 7)) * 16) + (pp & 1) * 8);
        bf[s * 4 + t] = u32x4{lo[0], lo[1], hi[0], hi[1]};
      }
    }
  };
  auto gemv_go = [&](u32 hh) {
    sm.hb[tid] = (u16)hh; sm.hb[512 + tid] = (u16)(hh >> 16);
    WAVE_SYNC();
    const u32x4* a = (c & 11) == 0 ? reinterpret_cast<const u32x4*>(&sm.hb[(c >> 2) * 512 + wave * 64 + g * 8]) : &sm.zero[0];
    const u32x4 a0 = a[0], a1 = a[(c & 11) == 0 ? 4 : 0];
    const f32x4 z = {0.f, 0.f, 0.f, 0.f};
    f32x4 d[4];
#pragma unroll
    for (int t = 0; t < 4; ++t) d[t] = mfma16(a0, bf[t], z);
#pragma unroll
    for (int t = 0; t < 4; ++t) d[t] = mfma16(a1, bf[4 + t], d[t]);
    if (lane < 32) {
#pragma unroll
      for (int t = 0; t < 4; ++t) sm.r2[(t * 16 + c) * 20 + g * 8 + wave] = d[t][0];
    }
  };
  auto mod_stage = [&]() {
    wo_prep();
    gemv_go(pack_bf(silu_f(lo_f(xs0)), silu_f(lo_f(xs1))));
    __builtin_amdgcn_s_waitcnt(0xc07f);
    if (lane == 0) *LDSP(volatile u32, (u32)(uintptr_t)&sm.mflag[wave]) = 1u;
    if (wave == 7) {
#pragma nounroll
      for (int spin = 0; spin < SPIN_MAX; ++spin) {
        const u32 f = *LDSP(volatile u32, (u32)(uintptr_t)&sm.mflag[lane & 7]);
        if (all_ok(f != 0u)) break;
        __builtin_amdgcn_s_sleep(1);
      }
      CFENCE();
      st8_sc1(wsr, G_MOD + (blk * 64 + lane) * 8, pack_bf(sum8(&sm.r2[lane * 20]), sum8(&sm.r2[lane * 20 + 8])), tag);
    }
    STAMP(9);
  };
  if (lane < 4) sm.zero[lane] = u32x4{0u, 0u, 0u, 0u};
  if (part == 2 && has_w) {
    if (tid < 8) sm.mflag[tid] = 0u;
    LDS_BARRIER();
    if (__builtin_expect(modc, 0)) {
      small_ops();
      __builtin_amdgcn_raw_buffer_store_b128(u32x4{SENT, SENT, SENT, SENT}, wsr, tid < 320 ? G_P + wi * 5120 + tid * 16 : OOB, 0, AUX_SC1);
      asm volatile("s_waitcnt vmcnt(0)" ::: "memory");
      tag = next_tag(__builtin_amdgcn_readfirstlane(ep[0]));
      mod_stage();
      __builtin_amdgcn_s_barrier();
    }
  }
  SB();
  const int fd = wi * 8 + (lane & 7);
  const int ub = wave >> 2, ud = (wave & 3) * 128 + c * 8 + g * 2;
  u32x4 sg[3]; u32 sx2;
  auto side_loads = [&]() {
    const bool fin = part == 1 && wave == 0;
    sg[0] = ld16<AUX_SC1>(wsr, is_up ? G_MOD + (1024 + ud) * 8 : (fin ? G_MOD + (1024 + fd) * 8 : OOB), 0);
    sg[1] = ld16<AUX_SC1>(wsr, is_up ? G_MOD + (1536 + ud) * 8 : (fin ? G_MOD + (2560 + fd) * 8 : OOB), 0);
    sg[2] = ld16<AUX_SC1>(wsr, is_up ? G_MOD + (2048 + ud) * 8 : OOB, 0);
    sx2 = __builtin_amdgcn_raw_buffer_load_b32(mk_rsrc(x, (is_up || fin) ? 2048u : 0u), is_up ? ub * 1024 + ud * 2 : (g & 1) * 1024 + (fd >> 1) * 4, 0, 0);
  };
  auto side_pin = [&]() { PIN(sg[0]); PIN(sg[1]); PIN(sg[2]); PIN(sx2); };
  const u32 mlo = G_ML + (head * 2 + b) * 64 + (lane & 3) * 16;
  float fmrg = 0.f;
  bool fok = wave != 0;
  auto merge_factor = [&](const u32x4& ml) {
    const float mi = u2f(ml[0]);
    float M = fmaxf(mi, dpp<XOR1>(mi)); M = fmaxf(M, dpp<XOR2>(M));
    const float e = __expf(mi - M);
    float L = e * u2f(ml[2]);
    L += dpp<XOR1>(L); L += dpp<XOR2>(L);
    fmrg = rdl(e * __builtin_amdgcn_rcpf(L), part);
  };
  f32x2 ga = {0.f, 0.f}, xin = {0.f, 0.f}, cc1 = {0.f, 0.f};
  float Af = 0.f, Bf = 0.f;
  const int hpos = ub * 512 + ud;
  auto up_prep = [&]() {
    if (!all_ok((sg[0][1] == tag) & (sg[0][3] == tag) & (sg[1][1] == tag) & (sg[1][3] == tag) & (sg[2][1] == tag) & (sg[2][3] == tag))) gather16<3>(wsr, G_MOD + (1024 + ud) * 8, 512 * 8, tag, sg);
    ga = f32x2{ub ? hi_f(sg[0][0]) : lo_f(sg[0][0]), ub ? hi_f(sg[0][2]) : lo_f(sg[0][2])};
    cc1 = f32x2{rbf(1.f + (ub ? hi_f(sg[2][0]) : lo_f(sg[2][0]))), rbf(1.f + (ub ? hi_f(sg[2][2]) : lo_f(sg[2][2])))};
    xin = f32x2{lo_f(sx2), hi_f(sx2)};
    u32* hp = reinterpret_cast<u32*>(&sm.hb[hpos]);
    hp[512] = pack_bf(cc1[0], cc1[1]);
    hp[1024] = ub ? perm_hi(sg[1][0], sg[1][2]) : perm_lo(sg[1][0], sg[1][2]);
  };
  auto up_frags = [&]() {
    const int pp = lane & 3;
    const u32 a = wimg + (pp & 1) * 32768 + ((pp >> 1) * 32 + g * 8 + ((lane >> 2) & 3)) * 64 + (((wave >> 1) ^ ((g & 1) | (pp & 2))) * 16) + (wave & 1) * 8;
#pragma unroll
    for (int rb = 0; rb < 8; ++rb) {
      const u32x2 lo = tr_read(a + rb * 4096), hi = tr_read(a + rb * 4096 + 256);
      bf[rb] = u32x4{lo[0], lo[1], hi[0], hi[1]};
    }
  };
  const u32x4* ap = reinterpret_cast<const u32x4*>(&sm.hb[((lane >> 3) & 1) * 512 + (lane & 1) * 32 + g * 8]);
  const bool par = lane & 8;
  float l0 = 0.f, l1 = 0.f;
  u32x2 gmd[2], tbg;
  int t1 = 0;
#pragma unroll
  for (int i = NCMIN / 8; i < NL; ++i) kr[i] = u32x4{0u, 0u, 0u, 0u};
  sfor<0, NL>([&](auto ic) {
    constexpr int i = ic.v;
    if (i >= NCMIN / 8 && i >= nl) return false;
    if constexpr (i == 0) return true;
    const u32 so = __builtin_amdgcn_readlane(sot, i);
    gate();
    SB();
    kr[i] = ld16<AUX_NT>(rk, lane * 16, so);
    if constexpr (i == SMO) small_ops();
    if constexpr (i == TBL) tbg = ld8<AUX_SC1>(wsr, wave == 7 ? G_TB + xcd * 128 + (lane & 7) * 8 : OOB, 0);
    if constexpr (i == QP) { gmd[0] = ld8<AUX_SC1>(wsr, part == 1 ? G_MOD + tid * 8 : OOB, 0); gmd[1] = ld8<AUX_SC1>(wsr, part == 1 ? G_MOD + tid * 8 : OOB, 4096); }
    SB();
    SB();
    if (i >= WK) { WAIT_VM(WK); SB(); }
    if (i == 1) STAMP(25);
    if (i == 8) STAMP(12);
    if (i == 20) STAMP(21);
    if (i == 25) STAMP(22);
    if (i == TBP) {
      if (part == 3 && i8 == 0 && wave == 7) { tag = next_tag(__builtin_amdgcn_readfirstlane(ep[0])); if (lane < 8) st8_sc1(wsr, G_TB + lane * 128 + xcd * 8, t0 + (((tka - t0) * P.GA) >> 3) - P.GS, tag); }
      SB();
    }
    if (i == TBU) {
      if (wave == 7) {
        const bool ok = tbg[1] == tag;
        float lf = ok ? (float)min(max((int)(t0 - tbg[0]), -300), 300) : 0.f, nf = ok ? 1.f : 0.f;
        lf += dpp<XOR1>(lf); nf += dpp<XOR1>(nf); lf += dpp<XOR2>(lf); nf += dpp<XOR2>(nf); lf += dpp<XOR7>(lf); nf += dpp<XOR7>(nf);
        if (lane == 0) sm.bc = u32x2{tag, t0 - (u32)(int)(lf * __builtin_amdgcn_rcpf(fmaxf(nf, 1.f)))};
      }
      LDS_BARRIER();
      const u32x2 bc = sm.bc;
      tag = __builtin_amdgcn_readfirstlane(bc[0]);
      t1 = (__builtin_amdgcn_readfirstlane((int)(bc[1] - t0)) + TEND + P.X1) * 256;
      tau -= __builtin_amdgcn_readfirstlane((int)((float)(int)(t0 - bc[1]) * 256.f * __builtin_amdgcn_rcpf((float)(2 * nl - TBU - 3))));
      SB();
    }
    if (i == LNS) {
      tag = next_tag(__builtin_amdgcn_readfirstlane(ep[0]));
      if (part == 1) {
        float x0[8], x1[8], mb0, ri0, mb1, ri1;
#pragma unroll
        for (int k = 0; k < 4; ++k) { x0[2 * k] = lo_f(xr0[k]); x0[2 * k + 1] = hi_f(xr0[k]); x1[2 * k] = lo_f(xr1[k]); x1[2 * k + 1] = hi_f(xr1[k]); }
        ln_stats2(x0, x1, mb0, ri0, mb1, ri1);
        l0 = rbf(rbf(lo_f(xs0) - mb0) * ri0); l1 = rbf(rbf(lo_f(xs1) - mb1) * ri1);
      }
      PIN(l0); PIN(l1);
      SB();
    }
    if (i == WUSE) {
      PIN(tch1); PIN(tch2);
      if ((part == 1 || (part == 2 && has_w && !modc))) {
        WAIT_VM(WK); CFENCE();
        if (part == 2) mod_stage(); else wo_prep();
      }
#pragma unroll
      for (int k = 0; k < 8; ++k) PIN(bf[k]);
      SB();
    }
    if (i == QP + QD) {
      if (part == 1) {
        u32 gm[2] = {gmd[0][0], gmd[1][0]};
        if (!all_ok((gmd[0][1] == tag) & (gmd[1][1] == tag))) { STAMP(11); gather8<2>(wsr, G_MOD + tid * 8, 512 * 8, tag, gm); }
        STAMP(5);
        gemv_go(pack_bf(rbf(l0 * rbf(1.f + lo_f(gm[1]))) + lo_f(gm[0]), rbf(l1 * rbf(1.f + hi_f(gm[1]))) + hi_f(gm[0])));
      }
      SB();
    }
    if (i == QP + QD + 1) {
      if (part == 1) {
        LDS_BARRIER();
        if (wave == 0) {
          const int bb = lane >> 5, jq = lane & 31;
          st8_sc1(wsr, G_Q + (bb * 2048 + blk * 32 + jq) * 8, pack_bf(sum8(&sm.r2[2 * jq * 20 + bb * 8]), sum8(&sm.r2[(2 * jq + 1) * 20 + bb * 8])), tag);
        }
        STAMP(6);
      }
      SB();
    }
    return true;
  });
  PIN(sot);
  const u32 qoff = G_Q + ((b * 32 + head) * 64 + c * 4) * 8;
  u32x4 qg[2], fl2[3];
  u32 qq[4];
  constexpr int NB = (NL + 15) / 16;
  float xlo, sc[NB], pr[NB], m = 0.f;
  f32x4 acc = {0.f, 0.f, 0.f, 0.f};
  u32 pb[NB], pm[4], cpf;
  const u32 adst0 = lds_addr(lane < 32 ? &sm.red[wave][c * 8 + g * 4] : &sm.r2[(lane - 32) * 4]);
  u32 po, rdst, rsrc, xo;
  auto wo_addr = [&]() {
    po = G_P + (((c >> 2 & 1) * 32 + xcd * 4 + (wave >> 1)) * 4 + (c & 3)) * 256 + (wave & 1) * 128 + (c >> 3) * 64 + g * 16;
    rdst = lane < 32 ? (u32)(uintptr_t)&sm.red2[wave][0] + lane * 16 : (u32)(uintptr_t)&sm.r2[0] + (lane - 32) * 16;
    rsrc = (u32)(uintptr_t)&sm.red2[0][((wave & 1) * 16 + (lane & 15)) * 4 + (lane >> 4)];
    xo = (lane & 1) ? OOB : G_XP + xcd * 2048 + (wave & 1) * 1024 + (blk * 64 + lane) * 2;
    PIN(po); PIN(rdst); PIN(rsrc); PIN(xo);
  };
  u32 pso = wave == 0 ? pslot + lane * 4 : OOB;
  PIN(pso);
  const u32 nadd = (te0 >> 20) ? P.LW : (is_up ? P.LU : 0);
  u32 nto = tid == 0 ? G_NT + bid * 8 : OOB;
  PIN(nto);
  auto notices = [&](auto nc_, u32 off, u32x4 (&gn)[nc_.v]) -> int {
    constexpr int N = nc_.v;
#pragma nounroll
    for (int spin = 0; spin < SPIN_MAX; ++spin) {
      bool ok = true;
#pragma unroll
      for (int i = 0; i < N; ++i) ok &= (gn[i][1] == tag) & (gn[i][3] == tag);
      if (all_ok(ok)) break;
      __builtin_amdgcn_s_sleep(10);
      CFENCE();
#pragma unroll
      for (int i = 0; i < N; ++i) gn[i] = ld16<AUX_SC1>(wsr, off, i * 1024);
    }
    int mx = 0;
#pragma unroll
    for (int i = 0; i < N; ++i) mx = max(mx, max((int)(gn[i][0] - t0), (int)(gn[i][2] - t0)));
    return __builtin_amdgcn_readfirstlane((int)wave_max((float)min(mx, 1 << 20)));
  };
  const int side = nl >= SIDE + 13 ? SIDE + 8 : (nl >= SIDE + 9 ? SIDE + 4 : SIDE);
  sfor<0, NL + WV>([&](auto ic) {
    constexpr int v = ic.v;
    if (cons_end(v - 1) >= NCMIN / 8 && cons_end(v - 1) >= nl) return false;
    if constexpr (v > 0 && v < NL) {
      const u32 so = __builtin_amdgcn_readlane(sot, v);
      if constexpr (v >= 12) { if (v == nl - P.NE) tau = (tau * P.FE) >> 4; }
      if constexpr (v >= NCMIN / 8) { if (v < nl) gate(); } else gate();
      SB();
      vr[v] = ld16<AUX_NT>(rv, lane * 16, so);
      SB();
      SB();
    }
    if constexpr (v == NCMIN / 8 - 1) {
#pragma unroll
      for (int i = NCMIN / 8; i < NL; ++i) asm volatile("" : "=v"(vr[i]));
      asm volatile("" : "=v"(fl2[1]), "=v"(fl2[2]));
    }
    if constexpr (v == VQ - 4) { qg[0] = ld16<AUX_SC1>(wsr, qoff, 0); qg[1] = ld16<AUX_SC1>(wsr, qoff + 16, 0); }
    if constexpr (v == VSIDE) side_loads();
    if constexpr (v == CPF) {
      u64 pc; u32 dlt, len;
      asm volatile("s_getpc_b64 %0\n.Lcpf_here:\n\ts_mov_b32 %1, .Ltail_start-.Lcpf_here\n\ts_mov_b32 %2, .Lend_a-.Ltail_start\n\ts_max_i32 %2, %2, .Lend_b-.Ltail_start\n\ts_max_i32 %2, %2, .Lend_c-.Ltail_start\n\ts_max_i32 %2, %2, .Lend_d-.Ltail_start\n\ts_max_i32 %2, %2, .Lend_e-.Ltail_start" : "=s"(pc), "=s"(dlt), "=s"(len) :: "scc");
      pc += dlt;
      const rsrc_t rc = mk_rsrc((const void*)pc, (part == 3 && i8 == 1) ? len : 0u);
      cpf = __builtin_amdgcn_raw_buffer_load_b32(rc, tid * 64, 0, 0);
    }
    if constexpr (v == CPF + WV + 1) PIN(cpf);
    if constexpr (v == SIDE || v == SIDE + 4 || v == SIDE + 8) fl2[(v - SIDE) / 4] = ld16<AUX_SC1>(wsr, (wave == 0 && v == side) ? mlo : OOB, 0);
    SB();
    if constexpr (v < WV) { WAIT_VM(WK); SB(); } else if constexpr (v < NL && cons_end(v) < v - WV + 1) { WAIT_VM(WV); SB(); }
    if constexpr (v == VQ) {
      if (!all_ok((qg[0][1] == tag) & (qg[0][3] == tag) & (qg[1][1] == tag) & (qg[1][3] == tag))) { STAMP(20); gather16<2>(wsr, qoff, 16, tag, qg); }
      STAMP(1); WSTAMP(1);
      qq[0] = qg[0][0]; qq[1] = qg[0][2]; qq[2] = qg[1][0]; qq[3] = qg[1][2];
    }
    if constexpr (v >= VQ && v < VS) {
      constexpr int sl = v - VQ, bt = sl >> 1;
      float xs[8];
#pragma unroll
      for (int i = 0; i < 8; ++i) {
        float d = 0.f;
        if (sl * 8 + i < NL) {
#pragma unroll
          for (int k = 0; k < 4; ++k) d = dot2(kr[sl * 8 + i][k], qq[k], d);
        }
        xs[i] = d;
      }
#pragma unroll
      for (int h = 0; h < 4; ++h) xs[h] = sel_add<XOR7>(xs[h], xs[h + 4], c & 4);
#pragma unroll
      for (int h = 0; h < 2; ++h) xs[h] = sel_add<XOR2>(xs[h], xs[h + 2], c & 2);
      xs[0] = sel_add<XOR1>(xs[0], xs[1], c & 1);
      if constexpr ((sl & 1) || sl == NSL - 1) {
        const float t = (sl & 1) ? sel_add<XOR8>(xlo, xs[0], c & 8) : xs[0] + dpp<XOR8>(xs[0]);
        const float s = rbf(rbf(t) * 0.08838834764831845f);
        sc[bt] = (bt * 16 + c < nl) ? s : -__builtin_inff();
        PIN(sc[bt]);
      } else {
        xlo = xs[0];
        PIN(xlo);
      }
    }
    if constexpr (v == VS) {
      float mloc = sc[0];
#pragma unroll
      for (int bt = 1; bt < NB; ++bt) mloc = fmaxf(mloc, sc[bt]);
      m = fmaxf(wave_max(mloc), -3.0e38f);
      float ls = 0.f;
#pragma unroll
      for (int bt = 0; bt < NB; ++bt) { pr[bt] = __expf(sc[bt] - m); ls += pr[bt]; }
      const float l = wave_sum(ls);
      if (lane == 0) { sm.ml[wave][0] = m; sm.ml[wave][1] = l; }
      STAMP(2); WSTAMP(2);
    }
    sfor<cons_end(v - 1), cons_end(v)>([&](auto jc) {
      constexpr int i = jc.v;
      if (i < NCMIN / 8 || i < nl) {
        const u32 pv = __float_as_uint(bcast16(u2f(pb[i >> 4]), i & 15));
        acc = mfma16(u32x4{pv & pm[0], pv & pm[1], pv & pm[2], pv & pm[3]}, vr[i], acc);
      }
      PIN(acc);
      return true;
    });
    if constexpr (v == MLS - 1) { side_pin(); if (is_up) up_prep(); PIN(ga); PIN(xin); PIN(cc1); }
    if constexpr (v == UPREP - 1) wo_addr();
    if constexpr (v == UPREP) {
#pragma unroll
      for (int k = 0; k < 8; ++k) asm volatile("" : "=v"(bf[k]));
      if (part == 0) wo_prep(0, 1); else if (is_up) up_frags();
#pragma unroll
      for (int k = 0; k < 8; ++k) PIN(bf[k]);
    }
    if constexpr (v == UPREP + 1) {
      if (is_up) {
        const u32x4* a1 = ap + 128 + ((lane >> 1) & 1) * 128;
        f32x4 d0 = {0.f, 0.f, 0.f, 0.f}, d1 = {0.f, 0.f, 0.f, 0.f};
#pragma unroll
        for (int rb = 0; rb < 4; ++rb) { d0 = mfma16(a1[rb * 8], bf[rb], d0); d1 = mfma16(a1[(rb + 4) * 8], bf[rb + 4], d1); }
        Af = par ? d0[1] + d1[1] : d0[0] + d1[0]; Bf = par ? d0[3] + d1[3] : d0[2] + d1[2];
        Af += dpp<XOR8>(Af); Bf += dpp<XOR8>(Bf);
      }
      PIN(Af); PIN(Bf);
    }
    if constexpr (v == SIDE + WV + 1 || v == SIDE + WV + 5 || v == SIDE + WV + 9) {
      if (wave == 0 && v == side + WV + 1) {
        const u32x4 ml = fl2[(v - SIDE - WV - 1) / 4];
        fok = all_ok((ml[1] == tag) & (ml[3] == tag));
        merge_factor(ml);
      }
    }
    if (v % 6 == 4 && v >= 10 && v < 30) STAMP(15 + v / 6);
    if constexpr (v == MLS) {
      LDS_BARRIER();
      float mm[8], ll[8];
#pragma unroll
      for (int w = 0; w < 8; ++w) { const f32x2 t = *reinterpret_cast<const f32x2*>(&sm.ml[w][0]); mm[w] = t[0]; ll[w] = t[1]; }
      float Mwg = mm[0], Lwg = 0.f;
#pragma unroll
      for (int w = 1; w < 8; ++w) Mwg = fmaxf(Mwg, mm[w]);
      if (wave == 0) {
#pragma unroll
        for (int w = 0; w < 8; ++w) Lwg = fmaf(__expf(mm[w] - Mwg), ll[w], Lwg);
        if (lane == 0) st16_sc1(wsr, G_ML + ((head * 2 + b) * 4 + part) * 16, __float_as_uint(Mwg), __float_as_uint(Lwg), tag);
      }
      const float fw = __expf(m - Mwg);
#pragma unroll
      for (int bt = 0; bt < NB; ++bt) { pb[bt] = pack_bf(pr[bt] * fw, pr[bt] * fw); PIN(pb[bt]); }
#pragma unroll
      for (int k = 0; k < 4; ++k) { pm[k] = (c >> 1) == k ? ((c & 1) ? 0xffff0000u : 0x0000ffffu) : 0u; PIN(pm[k]); }
    }
    SB();
    return true;
  });
  asm volatile(".Ltail_start:");
  *LDSP(f32x4, adst0) = acc;
  STAMP(14); WSTAMP(14);
  if (!fok) {
    u32x4 ml;
#pragma nounroll
    for (int spin = 0; spin < SPIN_MAX; ++spin) {
      CFENCE();
      ml = ld16<AUX_SC1>(wsr, mlo, 0);
      if (all_ok((ml[1] == tag) & (ml[3] == tag))) break;
    }
    merge_factor(ml);
  }
  STAMP(15);
  LDS_BARRIER();
  STAMP(3);
  {
    u32 pw = 0;
    if (wave == 0) {
      f32x2 t[8];
#pragma unroll
      for (int w = 0; w < 8; ++w) t[w] = *reinterpret_cast<const f32x2*>(&sm.red[w][2 * lane]);
      const f32x2 o = ((t[0] + t[1]) + (t[2] + t[3])) + ((t[4] + t[5]) + (t[6] + t[7]));
      pw = min(pack_bf(o[0] * fmrg, o[1] * fmrg), SENT - 1u);
    }
    __builtin_amdgcn_raw_buffer_store_b32(pw, wsr, pso, 0, AUX_SC1);
    st8_sc1(wsr, nto, (u32)wall_clock64() + nadd, tag);
    SB();
#pragma unroll
    for (int i = NCMIN / 8; i < NL; ++i) PIN(vr[i]);
    PIN(fl2[0]); PIN(fl2[1]); PIN(fl2[2]);
  }
  STAMP(4);

  if (part == 2) FINISH();
  if (part == 0) {
    __builtin_amdgcn_s_barrier();
#pragma nounroll
    while (wleft > 0) piece();
    until(t1);
    STAMP(11);
    CFENCE();
    u32x4 og[2];
    og[0] = ld16<AUX_SC1>(wsr, po, 0);
    SB();
    WAIT_VM(1); CFENCE();
    wo_prep(1, 2);
#pragma unroll
    for (int k = 4; k < 8; ++k) PIN(bf[k]);
    SB();
    f32x4 d[4];
    auto wo_mm = [&]() {
      const f32x4 z = {0.f, 0.f, 0.f, 0.f};
#pragma unroll
      for (int t = 0; t < 4; ++t) { d[t] = mfma16(og[0], bf[t], z); og[1][t] = __float_as_uint(dpp<XOR8>(u2f(og[0][t]))); }
      SB();
#pragma unroll
      for (int t = 0; t < 4; ++t) d[t] = mfma16(og[1], bf[4 + t], d[t]);
    };
    auto p_bad = [&]() { return !all_ok(max(max(og[0][0], og[0][1]), max(og[0][2], og[0][3])) != SENT); };
    wo_mm();
    if (__builtin_expect(p_bad(), 0)) {
#pragma nounroll
      for (int spin = 0; spin < SPIN_MAX; ++spin) {
        __builtin_amdgcn_s_sleep(1);
        CFENCE();
        og[0] = ld16<AUX_SC1>(wsr, po, 0);
        if (!p_bad()) break;
      }
      wo_mm();
      SB();
      asm volatile(".Lend_d:");
    }
    STAMP(5); WSTAMP(5);
    {
      SB();
      f32x4 y; f32x2 h[4];
#pragma unroll
      for (int t = 0; t < 4; ++t) { h[t] = d[t].xy + d[t].zw; PIN(h[t]); }
#pragma unroll
      for (int t = 0; t < 4; ++t) { y[t] = h[t][0] + h[t][1]; PIN(y[t]); }
      *LDSP(f32x4, rdst) = y;
    }
    STAMP(16);
    LDS_BARRIER();
    STAMP(17);
    if (wave < 2) {
      f32x2 v[4];
#pragma unroll
      for (int w = 0; w < 4; ++w) v[w] = f32x2{LDSP(float, rsrc)[w * 256], LDSP(float, rsrc)[w * 256 + 128]};
      const f32x2 h = (v[0] + v[1]) + (v[2] + v[3]);
      const float y = h[0] + h[1];
      __builtin_amdgcn_raw_buffer_store_b32(min(pack_bf(y, dpp<XOR1>(y)), SENT - 1u), wsr, xo, 0, AUX_SC1);
      asm volatile(".Lend_a:");
    }
    STAMP(6);
    FINISH();
  }
  if (part == 3) {
    if (!has_w) FINISH();
    u32x4 b2[4];
    {
      __builtin_amdgcn_s_barrier();
      const rsrc_t r2 = mk_rsrc(w2 + (size_t)(blk * 32 + wave * 4) * 512, 4096u);
      const u32 l2 = (u32)(uintptr_t)(&sm.w2b[wave * 4][0]);
#pragma unroll
      for (int n = 0; n < 4; ++n) dma16(r2, (lane ^ (n * 2) ^ ((wave & 2) << 2)) * 16, n * 1024, l2 + n * 1024);
    }
    const u32 tdst = lds_addr(&sm.hb[hpos]);
    const u32 sdst = lds_addr(&sm.stat[wave][lane]);
    const u32 ssrc = lds_addr(&sm.stat[(g >> 1) * 4 + ((lane >> 1) & 3)][(lane & 1) * 16]);
    const u32 adst = lds_addr((c < 4 && (g & 1) == 0) ? &sm.actl[g >> 1][wave * 4 + c] : reinterpret_cast<u16*>(&sm.dummy[lane]));
    const u32 asrc = lds_addr(&sm.actl[lane & 1][g * 8]);
    const u32 apa = lds_addr(&sm.hb[(c >> 3) * 512 + (c & 3) * 64 + ((c >> 2) & 1) * 32 + g * 8]);
    u32 xoff = G_XP + g * 2048 + ub * 1024 + (wave & 3) * 256 + c * 16;
    u32x4 one;
#pragma unroll
    for (int k = 0; k < 4; ++k) one[k] = ((c & 2) == 0 && (c >> 2) == k) ? ((c & 1) ? 0x3f800000u : 0x00003f80u) : 0u;
    PIN(one);
    u32 ooff = G_OP + (blk * 512 + tid) * 8;
    float Afp = Af, Bfp = Bf;
    PIN(xoff); PIN(ooff); PIN(Afp); PIN(Bfp);
    PIN(ga); PIN(xin); PIN(cc1);
    u32 w2a = (u32)(uintptr_t)(&sm.w2b[0][0]) + (g * 8 + ((lane >> 2) & 3)) * 1024 + (wave ^ (g & 1)) * 128 + (lane & 15) * 8;
    PIN(w2a);
    if (wave == 1) {
      due = t1 + P.N1 * 256; gate();
      u32x4 gn[2] = {ld16<AUX_SC1>(wsr, G_NT + lane * 16, 0), ld16<AUX_SC1>(wsr, G_NT + lane * 16, 1024)};
      const int tn = notices(IC<2>{}, G_NT + lane * 16, gn) * 256;
      due = (tn > t1 + 2 * 256 ? max(tn, t1 + P.L0 * 256) + P.L1 * 256 : t1) + P.X2 * 256;
      until(due);
    }
    asm volatile("s_waitcnt vmcnt(0)" ::: "memory");
    STAMP(9);
    __builtin_amdgcn_s_barrier();
    STAMP(11);
    CFENCE();
    u32x4 xg[2] = {ld16<AUX_SC1>(wsr, xoff, 0), ld16<AUX_SC1>(wsr, xoff, 8192)};
    SB();
#pragma unroll
    for (int t = 0; t < 4; ++t) {
      const u32x2 lo = tr_read(w2a ^ (t * 32)), hi = tr_read((w2a ^ (t * 32)) + 4096);
      b2[t] = u32x4{lo[0], lo[1], hi[0], hi[1]};
    }
#pragma unroll
    for (int t = 0; t < 4; ++t) PIN(b2[t]);
    SB();
    const f32x4 z4 = {0.f, 0.f, 0.f, 0.f};
    auto xp_bad = [&]() { return !all_ok(max(max(max(xg[0][0], xg[0][1]), max(xg[0][2], xg[0][3])), max(max(xg[1][0], xg[1][1]), max(xg[1][2], xg[1][3]))) != SENT); };
    f32x4 v = mfma16(one, xg[1], mfma16(one, xg[0], z4));
    if (__builtin_expect(xp_bad(), 0)) {
#pragma nounroll
      for (int spin = 0; spin < SPIN_MAX; ++spin) {
        __builtin_amdgcn_s_sleep(1);
        CFENCE();
        xg[0] = ld16<AUX_SC1>(wsr, xoff, 0); xg[1] = ld16<AUX_SC1>(wsr, xoff, 8192);
        if (!xp_bad()) break;
      }
      v = mfma16(one, xg[1], mfma16(one, xg[0], z4));
      SB();
      asm volatile(".Lend_e:");
    }
    STAMP(5); WSTAMP(5);
    {
      const float a0 = v[0], a1 = v[1];
      const float x10 = fmaf(ga[0], a0, xin[0]), x11 = fmaf(ga[1], a1, xin[1]);
      *LDSP(u32, tdst) = pack_bf(x10 * cc1[0], x11 * cc1[1]);
      float u = swap32_add(x10 + x11, fmaf(x10, x10, x11 * x11));
      u += dpp<XOR1>(u); u += dpp<XOR2>(u); u += dpp<XOR7>(u); u += dpp<XOR8>(u);
      *LDSP(float, sdst) = u;
    }
    STAMP(16);
    LDS_BARRIER();
    STAMP(17);
    {
      const f32x4 z = {0.f, 0.f, 0.f, 0.f};
      const u32x4 af0 = LDSP(u32x4, apa)[0], af1 = LDSP(u32x4, apa)[32];
      float s = LDSP(float, ssrc)[0], q = LDSP(float, ssrc)[32];
      SB();
      f32x4 d[4];
#pragma unroll
      for (int j = 0; j < 4; ++j) d[j] = mfma16(af0, bf[j], z);
#pragma unroll
      for (int j = 0; j < 4; ++j) d[j] = mfma16(af1, bf[4 + j], d[j]);
      s += dpp<XOR1>(s); q += dpp<XOR1>(q); s += dpp<XOR2>(s); q += dpp<XOR2>(q); s += dpp<XOR7>(s); q += dpp<XOR7>(q);
      const float mean = s * (1.f / 512.f);
      const float var = fmaf(q, 1.f / 512.f, -mean * mean);
      const float ri = __builtin_amdgcn_rsqf(fmaxf(var, 0.f) + 1e-5f);
      const float cf = fmaf(-ri * mean, Afp, Bfp);
      float y = (d[0][0] + d[1][1]) + (d[2][2] + d[3][3]);
      y = __builtin_bit_cast(float, __builtin_amdgcn_update_dpp(__builtin_bit_cast(int, y), __builtin_bit_cast(int, y), XOR8, 0xa, 0xf, false));
      y = swap16_add(y, y);
      y = fmaf(y, ri, cf);
      const float up = dpp<0x104>(y);
      *LDSP(u16, adst) = (u16)pack_bf(silu_f(y) * up, 0.f);
    }
    STAMP(18);
    LDS_BARRIER();
    STAMP(10);
    {
      const u32x4 af = *LDSP(u32x4, asrc);
      const f32x4 z = {0.f, 0.f, 0.f, 0.f};
      const f32x4 d0 = mfma16(af, b2[0], z), d1 = mfma16(af, b2[1], z), d2 = mfma16(af, b2[2], z), d3 = mfma16(af, b2[3], z);
      const bool g1 = g & 1, g2 = g & 2;
      const float e0 = g1 ? d1[0] : d0[0], e1 = g1 ? d1[1] : d0[1], f0 = g1 ? d3[0] : d2[0], f1 = g1 ? d3[1] : d2[1];
      st8_sc1(wsr, ooff, pack_bf(g2 ? f0 : e0, g2 ? f1 : e1), tag);
    }
    asm volatile(".Lend_b:");
    STAMP(6);
    FINISH();
  }
  if (wave == 0) {
    const int d = fd, ul = lane >> 3;
    u32 gm[2] = {sg[0][0], sg[1][0]};
    if (!all_ok((sg[0][1] == tag) & (sg[1][1] == tag))) gather8<2>(wsr, G_MOD + (1024 + d) * 8, 1536 * 8, tag, gm);
    u32 xa0, xa1;
    {
      due = t1 + P.N2 * 256; gate();
      u32x4 gn[2] = {ld16<AUX_SC1>(wsr, G_NT + lane * 16, 0), ld16<AUX_SC1>(wsr, G_NT + lane * 16, 1024)};
      const int tn = notices(IC<2>{}, G_NT + lane * 16, gn) * 256;
      due = (tn > t1 + 2 * 256 ? max(tn, t1 + P.L0 * 256) + P.L1 * 256 : t1) + P.X3 * 256;
      until(due);
    }
#pragma nounroll
    for (int spin = 0; spin < SPIN_MAX; ++spin) {
      CFENCE();
      xa0 = __builtin_amdgcn_raw_buffer_load_b32(wsr, G_XP + ul * 2048 + (d >> 1) * 4, 0, AUX_SC1);
      xa1 = __builtin_amdgcn_raw_buffer_load_b32(wsr, G_XP + ul * 2048 + (d >> 1) * 4, 1024, AUX_SC1);
      if (all_ok(max(xa0, xa1) != SENT)) break;
      __builtin_amdgcn_s_sleep(1);
    }
    float a0 = (d & 1) ? hi_f(xa0) : lo_f(xa0), a1 = (d & 1) ? hi_f(xa1) : lo_f(xa1);
    a0 += dpp<XOR8>(a0); a1 += dpp<XOR8>(a1);
    a0 = swap16_add(a0, a0); a1 = swap16_add(a1, a1);
    a0 = swap32_add(a0, a0); a1 = swap32_add(a1, a1);
    const bool hb = g & 1;
    float xv = rbf(((d & 1) ? hi_f(sx2) : lo_f(sx2)) + rbf((hb ? hi_f(gm[0]) : lo_f(gm[0])) * rbf(hb ? a1 : a0)));
    float gv = hb ? hi_f(gm[1]) : lo_f(gm[1]);
    PIN(xv); PIN(gv);
    if (wi == 0 && lane == 0) __builtin_amdgcn_raw_buffer_store_b32(tag, wsr, WS_EPOCH, 0, AUX_SC1);
    STAMP(10);
    due = max(due + P.X4 * 256, (int)(((u32)wall_clock64() - t0) << 8) + P.GX * 256);
    const bool last = ul + 40 < NUP;
    u32 o0 = G_OP + (ul * 512 + d) * 8, o5 = last ? o0 : OOB, tg5 = last ? tag : 0u;
    const rsrc_t rout = mk_rsrc(out, 2048u);
    u32 oo = (lane & 40) == 0 ? (g * 512 + d) * 2 : OOB;
    const u32 pk = c == 0 ? 0x00003f80u : (c == 4 ? 0x3f800000u : 0u);
    u32x4 one = {pk, 0u, pk, 0u};
    PIN(o0); PIN(o5); PIN(tg5); PIN(oo); PIN(one);
    __builtin_amdgcn_raw_buffer_store_b16((u16)pack_bf(xv, 0.f), rout, oo, 0, AUX_SC1);
    until(due);
    f32x4 sv;
#pragma nounroll
    for (int spin = 0; spin < SPIN_MAX; ++spin) {
      CFENCE();
      u32x2 og[6];
#pragma unroll
      for (int k = 0; k < 5; ++k) og[k] = ld8<AUX_SC1>(wsr, o0, k * 32768);
      og[5] = ld8<AUX_SC1>(wsr, o5, 5 * 32768);
      sv = f32x4{0.f, 0.f, 0.f, 0.f};
#pragma unroll
      for (int k = 0; k < 3; ++k) sv = mfma16(one, u32x4{og[2 * k][0], og[2 * k][1], og[2 * k + 1][0], og[2 * k + 1][1]}, sv);
      const u32 bad = ((og[0][1] ^ tag) | (og[1][1] ^ tag) | (og[2][1] ^ tag)) | ((og[3][1] ^ tag) | (og[4][1] ^ tag) | (og[5][1] ^ tg5));
      PIN(sv);
      if (all_ok(bad == 0u)) break;
      __builtin_amdgcn_s_sleep(1);
    }
    STAMP(7);
    const float s = sv[0] + dpp<XOR8>(sv[0]);
    __builtin_amdgcn_raw_buffer_store_b16((u16)pack_bf(fmaf(gv, s, xv), 0.f), rout, oo, 0, AUX_SC1);
    asm volatile(".Lend_c:");
  }
  FINISH();
}

extern "C" void kernel_launch(void* const* d_in, const int* in_sizes, int n_in,
                              void* d_out, int out_size, void* d_ws, size_t ws_size,
                              hipStream_t stream) {
  fused_kernel<<<256, 512, 0, stream>>>((const u16*)d_in[1], (const u16*)d_in[2], (const u16*)d_in[4], (const u16*)d_in[3], (const u16*)d_in[10], (const u16*)d_in[6], (const u16*)d_in[7],
                                        (unsigned char*)d_ws, (const u16*)d_in[0], (const u16*)d_in[9], (const u16*)d_in[8], (u16*)d_out
    );
}
```

```cpp
#define PROBE_MUL 10
#include <hip/hip_runtime.h>
#include <stdint.h>

typedef unsigned int u32;
typedef unsigned short u16;
typedef unsigned long long u64;
typedef __attribute__((ext_vector_type(4))) unsigned u32x4;
typedef __attribute__((ext_vector_type(2))) unsigned u32x2;
typedef __attribute__((ext_vector_type(2))) __bf16 bf16x2;
typedef __attribute__((ext_vector_type(4))) float f32x4;
typedef __attribute__((ext_vector_type(2))) float f32x2;
typedef __amdgpu_buffer_rsrc_t rsrc_t;

constexpr int NH = 32, SK = 16384, DH = 128, WIN = 4096, FF = 1376;
struct Prm { int E0, ES, E1, E2, E2C, EU, TEND, DT_UP, L0, L1, X1, X2, X3, X4, NE, FE, M0, M1, M2, M3, M4, GS, GX, N1, N2, GA, WD, DT_WO, LW, LU, EW, PM; };
constexpr Prm PRM0 = {32, 24, 32, 32, 56, 64, 2030, 60, 30, 25, 92, 86, 131, 88, 4, 12, 5, 8, 21, 4, 32, 30, 56, 28, 70, 12, 4, 100, 45, 15, 8, 0x24310};
#define PRM_ARG
constexpr int NL = 37;
constexpr int NCMIN = 192;
constexpr int WK = 8, WV = 5;
constexpr int WUSE = 12, LNS = 6;
constexpr int QP = 14, QD = 4;
constexpr int VQ = 6;
constexpr int NSL = (NL + 7) / 8;
constexpr int VS = VQ + NSL;
constexpr int MLS = VS + 2;
constexpr int VSIDE = MLS - 5, UPREP = MLS + 8;
constexpr int SIDE = NCMIN / 8 - 4;
constexpr int CPF = 18;
constexpr int TBP = 3, TBL = 10, TBU = 15;
constexpr int cons_end(int v) {
  const int a = v - WV + 1, c = (v - MLS) * 4;
  const int m = a < c ? a : c;
  return m < 0 ? 0 : (m > NL ? NL : m);
}
struct Tab { u32 e[512]; };
constexpr Tab make_tab(const Prm& P) {
  Tab T{};
  for (int bid = 0; bid < 256; ++bid) {
    const int xcd = bid & 7, idx = bid >> 3, part = idx & 3, i8 = idx >> 2, wi = i8 * 8 + xcd;
    const bool has_mod = wi < 48, has_up = wi >= 21;
    const int cb0 = (i8 + 5) & 7, cb1 = (i8 + 4) & 7, ms = wi - 20;
    const int mi = wi < 8 ? wi : (wi < 16 ? 8 + ((xcd + 4) & 7) : (wi < 20 ? (wi - 14) * 8 + 3 : 16 + ms / 7 * 8 + ms % 7 + (ms % 7 >= 3 ? 1 : 0)));
    const int ui = xcd * 5 + (xcd > 5 ? xcd - 5 : 0) + (i8 >= 3 ? (xcd >= 5 ? 1 : 0) + i8 - 3 : 0);
    const int wd = cb0 == 3 ? 0 : P.WD;
    const int e[4] = {P.E0 - 4 * wd + (cb0 == 3 ? P.ES : 0), P.E1 + (cb1 == 3 ? P.ES : 0), has_mod ? (wi < 16 ? P.E2C : P.E2) + ((mi & 7) == 3 ? P.ES : 0) : 0, has_up ? P.EU : 0};
    const int ew = wd ? P.EW : 0, pa = (1024 + e[0] + ew + e[1] + e[2] + e[3]) / 4;
    int c[5] = {0, 0, 0, 0, 128};
    for (int k = 0; k < 3; ++k) c[k + 1] = c[k] + (pa - e[k] - (k == 0 ? ew : 0) + 4) / 8;
    const int nl = c[part + 1] - c[part], wst = e[part] / 4;
    const bool has_w = part < 2 || (part == 2 ? has_mod : has_up);
    const int blk = !has_w ? 0 : (part == 0 ? cb0 : (part == 1 ? xcd * 8 + cb1 : (part == 2 ? mi : ui)));
    const int tau = (part == 3 && has_up ? P.TEND - P.DT_UP : (part == 0 && wd ? P.TEND - P.DT_WO : P.TEND)) * 256 / (2 * nl + wst);
    T.e[2 * bid] = (u32)(c[part] | nl << 7 | blk << 13 | (has_w ? 1 : 0) << 19 | (part == 0 ? wd : 0) << 20);
    T.e[2 * bid + 1] = (u32)(tau | (tau * wst / (part == 0 ? 8 - wd : 8)) << 16);
  }
  return T;
}
constexpr int step_range(const Prm& P, bool mx_) {
  int mx = 0, mn = 1 << 20;
  const Tab T = make_tab(P);
  for (int bid = 0; bid < 256; ++bid) { const int n = T.e[2 * bid] >> 7 & 63; mx = n > mx ? n : mx; mn = n < mn ? n : mn; }
  return mx_ ? mx : mn;
}
__constant__ const Tab TAB = make_tab(PRM0);
static_assert(step_range(PRM0, true) <= NL && step_range(PRM0, false) >= NCMIN / 8, "a wave's key range does not fit its load steps");
static_assert(cons_end(SIDE + WV + 1) <= NCMIN / 8 && cons_end(UPREP) <= NCMIN / 8 && SIDE > MLS + 3, "steps every wave runs");
constexpr int NUP = FF / 32;
constexpr int SPIN_MAX = 1 << 17;

constexpr u32 WS_EPOCH = 0;
constexpr u32 G_TB = 1024;
constexpr u32 G_NT = 2048;
constexpr u32 G_MOD = 4096;
constexpr u32 G_Q = G_MOD + 3072 * 8;
constexpr u32 G_ML = G_Q + 4096 * 8;
constexpr u32 G_P = G_ML + 256 * 16;
constexpr u32 G_XP = G_P + 256 * 256;
constexpr u32 G_OP = G_XP + 8 * 512 * 4;
constexpr u32 WS_END = G_OP + 43 * 512 * 8;
constexpr u32 SENT = 0xffffffffu;
constexpr u32 OOB = 0x40000000u;
#define WST 0

constexpr int AUX_NT = 2, AUX_SC1 = 16;

__device__ __forceinline__ float lo_f(u32 v) { return __uint_as_float(v << 16); }
__device__ __forceinline__ float hi_f(u32 v) { return __uint_as_float(v & 0xffff0000u); }
__device__ __forceinline__ float rbf(float x) { return (float)(__bf16)x; }
__device__ __forceinline__ u32 pack_bf(float lo, float hi) {
  bf16x2 r; r[0] = (__bf16)lo; r[1] = (__bf16)hi; return __builtin_bit_cast(u32, r);
}
__device__ __forceinline__ float dot2(u32 k, u32 q, float acc) {
  return __builtin_amdgcn_fdot2_f32_bf16(__builtin_bit_cast(bf16x2, k), __builtin_bit_cast(bf16x2, q), acc, false);
}
template <int CTRL> __device__ __forceinline__ float dpp(float x) {
  return __builtin_bit_cast(float, __builtin_amdgcn_mov_dpp(__builtin_bit_cast(int, x), CTRL, 0xf, 0xf, true));
}
constexpr int XOR1 = 0xB1, XOR2 = 0x4E, XOR7 = 0x141, XOR8 = 0x128;
__device__ __forceinline__ float swap32_add(float a, float b) {
  auto t = __builtin_amdgcn_permlane32_swap(__float_as_uint(a), __float_as_uint(b), false, false);
  return __uint_as_float(t[0]) + __uint_as_float(t[1]);
}
__device__ __forceinline__ float swap16_add(float a, float b) {
  auto t = __builtin_amdgcn_permlane16_swap(__float_as_uint(a), __float_as_uint(b), false, false);
  return __uint_as_float(t[0]) + __uint_as_float(t[1]);
}
__device__ __forceinline__ float wave_sum(float x) {
  x += dpp<XOR1>(x); x += dpp<XOR2>(x); x += dpp<XOR7>(x); x += dpp<XOR8>(x);
  x = swap16_add(x, x);
  return swap32_add(x, x);
}
__device__ __forceinline__ float wave_max(float x) {
  x = fmaxf(x, dpp<XOR1>(x)); x = fmaxf(x, dpp<XOR2>(x)); x = fmaxf(x, dpp<XOR7>(x)); x = fmaxf(x, dpp<XOR8>(x));
  auto s = __builtin_amdgcn_permlane16_swap(__float_as_uint(x), __float_as_uint(x), false, false);
  x = fmaxf(__uint_as_float(s[0]), __uint_as_float(s[1]));
  auto t = __builtin_amdgcn_permlane32_swap(__float_as_uint(x), __float_as_uint(x), false, false);
  return fmaxf(__uint_as_float(t[0]), __uint_as_float(t[1]));
}
template <int CTRL> __device__ __forceinline__ float sel_add(float lo, float hi, bool up) {
  const float t1 = lo + dpp<CTRL>(lo), t2 = hi + dpp<CTRL>(hi);
  return up ? t2 : t1;
}
template <int I> __device__ __forceinline__ float bc16(float x) {
  return __builtin_bit_cast(float, __builtin_amdgcn_update_dpp(0, __builtin_bit_cast(int, x), 0x150 + I, 0xf, 0xf, false));
}
__device__ __forceinline__ float bcast16(float x, int i) {
  switch (i) {
    case 0: return bc16<0>(x); case 1: return bc16<1>(x); case 2: return bc16<2>(x); case 3: return bc16<3>(x);
    case 4: return bc16<4>(x); case 5: return bc16<5>(x); case 6: return bc16<6>(x); case 7: return bc16<7>(x);
    case 8: return bc16<8>(x); case 9: return bc16<9>(x); case 10: return bc16<10>(x); case 11: return bc16<11>(x);
    case 12: return bc16<12>(x); case 13: return bc16<13>(x); case 14: return bc16<14>(x); default: return bc16<15>(x);
  }
}
__device__ __forceinline__ float silu_f(float x) { return x * __builtin_amdgcn_rcpf(1.f + __expf(-x)); }

template <int AUX> __device__ __forceinline__ u32x4 ld16(rsrc_t r, u32 voff, u32 soff) {
  return __builtin_amdgcn_raw_buffer_load_b128(r, voff, soff, AUX);
}
template <int AUX> __device__ __forceinline__ u32x2 ld8(rsrc_t r, u32 voff, u32 soff) {
  return __builtin_amdgcn_raw_buffer_load_b64(r, voff, soff, AUX);
}
__device__ __forceinline__ void st8_sc1(rsrc_t r, u32 voff, u32 v, u32 tag) {
  __builtin_amdgcn_raw_buffer_store_b64(u32x2{v, tag}, r, voff, 0, AUX_SC1);
}
__device__ __forceinline__ void st16_sc1(rsrc_t r, u32 voff, u32 v0, u32 v1, u32 tag) {
  __builtin_amdgcn_raw_buffer_store_b128(u32x4{v0, tag, v1, tag}, r, voff, 0, AUX_SC1);
}
__device__ __forceinline__ rsrc_t mk_rsrc(const void* p, u32 bytes) {
  return __builtin_amdgcn_make_buffer_rsrc(const_cast<void*>(p), 0, bytes, 0x00020000);
}
#define WAIT_VM(n) __builtin_amdgcn_s_waitcnt(((n) & 15) | (((n) >> 4) << 14) | (7 << 4) | (15 << 8))
#define SB() __builtin_amdgcn_sched_barrier(0)
#define CFENCE() asm volatile("" ::: "memory")
__device__ __forceinline__ bool all_ok(bool ok) { return __builtin_amdgcn_ballot_w64(!ok) == 0ull; }

template <int N> __device__ __forceinline__ void gather16(rsrc_t ws, u32 voff, u32 stride, u32 tag, u32x4 (&g)[N]) {
#pragma nounroll
  for (int spin = 0; spin < SPIN_MAX; ++spin) {
    CFENCE();
    bool ok = true;
#pragma unroll
    for (int i = 0; i < N; ++i) g[i] = ld16<AUX_SC1>(ws, voff + i * stride, 0);
#pragma unroll
    for (int i = 0; i < N; ++i) ok &= (g[i][1] == tag) & (g[i][3] == tag);
    if (all_ok(ok)) break;
    __builtin_amdgcn_s_sleep(8);
  }
}
template <int N> __device__ __forceinline__ void gather8(rsrc_t ws, u32 voff, u32 stride, u32 tag, u32 (&g)[N], bool act = true) {
#pragma nounroll
  for (int spin = 0; spin < SPIN_MAX; ++spin) {
    CFENCE();
    bool ok = true;
    u32x2 t[N];
#pragma unroll
    for (int i = 0; i < N; ++i) t[i] = ld8<AUX_SC1>(ws, voff + i * stride, 0);
#pragma unroll
    for (int i = 0; i < N; ++i) { ok &= (t[i][1] == tag); g[i] = t[i][0]; }
    if (all_ok(ok | !act)) break;
    __builtin_amdgcn_s_sleep(8);
  }
}

struct Smem {
  float red[8][128];
  float ml[8][2];
  float r2[64 * 20];
  u16 hb[3 * 1024];
  float stat[8][64];
  float red2[8][128];
  u32 dummy[64];
  u32x2 bc;
  u32x4 zero[4];
  u32 mflag[8];
  u32x4 wbuf[8][8][64];
  __attribute__((aligned(256))) u16 w2b[32][512];
  u16 actl[2][32];
};

#define PROBE_K 8
#define PROBE_OP 0
#define PROBE_PART -1
#define STAMP(k) do { if ((k) == PROBE_K && tid == 0) pst = wall_clock64(); } while (0)
#define WSTAMP(k) do {} while (0)
#define FINISH() do { STAMP(8); if (tid == 0) { __hip_atomic_store(reinterpret_cast<u64*>(ws + (1u << 20)) + bid * 2, pt0, __ATOMIC_RELAXED, __HIP_MEMORY_SCOPE_AGENT); \
   __hip_atomic_store(reinterpret_cast<u64*>(ws + (1u << 20)) + bid * 2 + 1, (PROBE_PART < 0 || part == PROBE_PART) ? pst : 0ull, __ATOMIC_RELAXED, __HIP_MEMORY_SCOPE_AGENT); } \
   if (bid == 8 && wave == 0) probe_end(ws, pt0, lane); return; } while (0)
#define PIN(x) asm volatile("" : "+v"(x))
#define LDS_BARRIER() do { __builtin_amdgcn_s_waitcnt(0xc07f); __builtin_amdgcn_s_barrier(); CFENCE(); } while (0)
#define WAVE_SYNC() do { __builtin_amdgcn_fence(__ATOMIC_RELEASE, "wavefront"); __builtin_amdgcn_wave_barrier(); __builtin_amdgcn_fence(__ATOMIC_ACQUIRE, "wavefront"); } while (0)

__device__ __forceinline__ void dma16(rsrc_t r, u32 voff, u32 soff, u32 lds_dst) {
  u32 keep;
  asm volatile("s_mov_b32 %0, m0\n\ts_mov_b32 m0, %4\n\ts_nop 4\n\tbuffer_load_dwordx4 %1, %2, %3 offen nt lds\n\ts_mov_b32 m0, %0"
               : "=&s"(keep) : "v"(voff), "s"(r), "s"(soff), "s"(lds_dst) : "memory");
}
__device__ __forceinline__ u32 perm_lo(u32 a, u32 b) { return __builtin_amdgcn_perm(b, a, 0x05040100u); }
__device__ __forceinline__ u32 perm_hi(u32 a, u32 b) { return __builtin_amdgcn_perm(b, a, 0x07060302u); }
__device__ __forceinline__ float rdl(float x, int l) { return __int_as_float(__builtin_amdgcn_readlane(__float_as_int(x), l)); }

typedef __attribute__((ext_vector_type(8))) __bf16 bf16x8;
__device__ __forceinline__ f32x4 mfma16(u32x4 a, u32x4 b, f32x4 c) {
  return __builtin_amdgcn_mfma_f32_16x16x32_bf16(__builtin_bit_cast(bf16x8, a), __builtin_bit_cast(bf16x8, b), c, 0, 0, 0);
}
__device__ __forceinline__ float sum8(const float* p) {
  const f32x4 a = *reinterpret_cast<const f32x4*>(p), b = *reinterpret_cast<const f32x4*>(p + 4);
  return ((a[0] + b[0]) + (a[1] + b[1])) + ((a[2] + b[2]) + (a[3] + b[3]));
}
__device__ __forceinline__ float ln_ri(float var) {
  return __builtin_amdgcn_rcpf(rbf(__builtin_amdgcn_sqrtf(rbf(rbf(fmaxf(var, 0.f)) + 1e-5f))));
}
__device__ __forceinline__ void ln_stats2(const float (&x0)[8], const float (&x1)[8], float& mb0, float& ri0, float& mb1, float& ri1) {
  const float k0 = rdl(x0[0], 0), k1 = rdl(x1[0], 0);
  float s0 = 0.f, s1 = 0.f, q0 = 0.f, q1 = 0.f;
#pragma unroll
  for (int k = 0; k < 8; ++k) {
    const float d0 = x0[k] - k0, d1 = x1[k] - k1;
    s0 += d0; s1 += d1; q0 = fmaf(d0, d0, q0); q1 = fmaf(d1, d1, q1);
  }
  float t = swap16_add(swap32_add(s0, s1), swap32_add(q0, q1));
  t += dpp<XOR1>(t); t += dpp<XOR2>(t); t += dpp<XOR7>(t); t += dpp<XOR8>(t);
  auto u = __builtin_amdgcn_permlane16_swap(__float_as_uint(t), __float_as_uint(t), false, false);
  const float ms = __uint_as_float(u[0]) * (1.f / 512.f), mq = __uint_as_float(u[1]) * (1.f / 512.f);
  const float ri = ln_ri(fmaf(-ms, ms, mq));
  mb0 = rbf(k0 + rdl(ms, 0)); mb1 = rbf(k1 + rdl(ms, 32));
  ri0 = rdl(ri, 0); ri1 = rdl(ri, 32);
}
template <int I> struct IC { static constexpr int v = I; };
template <int I, int N, class F> __device__ __forceinline__ void sfor(F&& f) {
  if constexpr (I < N) { if (f(IC<I>{})) sfor<I + 1, N>(f); }
}
typedef __attribute__((ext_vector_type(4))) short s16x4;
typedef __attribute__((address_space(3))) s16x4* lds_s16x4p;
__device__ __forceinline__ u32x2 tr_read(u32 lds_addr) {
  return __builtin_bit_cast(u32x2, __builtin_amdgcn_ds_read_tr16_b64_v4i16((lds_s16x4p)lds_addr));
}
__device__ __forceinline__ u32 next_tag(u32 e) { return ((e | 0x40004000u) + 1u) & ~0x40004000u; }
__device__ __forceinline__ float u2f(u32 v) { return __uint_as_float(v); }
#define LDSP(T, a) ((__attribute__((address_space(3))) T*)(a))
template <class T> __device__ __forceinline__ u32 lds_addr(T* p) { u32 a = (u32)(uintptr_t)p; PIN(a); return a; }

__device__ __forceinline__ void probe_end(unsigned char* ws, u64 pt0, int lane) {
  while (wall_clock64() < pt0 + 5500ull) __builtin_amdgcn_s_sleep(8);
  u64 a[4], c2[4];
  for (int i = 0; i < 4; ++i) {
    a[i] = __hip_atomic_load(reinterpret_cast<u64*>(ws + (1u << 20)) + (i * 64 + lane) * 2, __ATOMIC_RELAXED, __HIP_MEMORY_SCOPE_AGENT);
    c2[i] = __hip_atomic_load(reinterpret_cast<u64*>(ws + (1u << 20)) + (i * 64 + lane) * 2 + 1, __ATOMIC_RELAXED, __HIP_MEMORY_SCOPE_AGENT);
  }
  int mn0 = 1 << 30, mx1 = -(1 << 30), mn1 = 1 << 30, sum = 0, cnt = 0;
  for (int i = 0; i < 4; ++i) {
    const int d0 = (int)(a[i] - pt0);
    mn0 = min(mn0, d0);
    if (c2[i] != 0ull) { const int d1 = (int)(c2[i] - pt0); mx1 = max(mx1, d1); mn1 = min(mn1, d1); sum += d1; cnt += 1; }
  }
  for (int o = 1; o < 64; o <<= 1) { mn0 = min(mn0, __shfl_xor(mn0, o)); mx1 = max(mx1, __shfl_xor(mx1, o)); mn1 = min(mn1, __shfl_xor(mn1, o)); sum += __shfl_xor(sum, o); cnt += __shfl_xor(cnt, o); }
  int X = PROBE_OP == 0 ? mx1 : (PROBE_OP == 1 ? sum / max(cnt, 1) : mn1);
  if (PROBE_K == 102) X = mn0;
  X = mn0 + (X - mn0) * PROBE_MUL;
  const u64 tg = pt0 + (u64)(long long)(X + 8000);
  for (int i = 0; i < 400000 && wall_clock64() < tg; ++i) __builtin_amdgcn_s_sleep(1);
}
__global__ void __launch_bounds__(512, 2) fused_kernel(const u16* __restrict__ x, const u16* __restrict__ kc, const u16* __restrict__ vc,
                                                       const u16* __restrict__ wq, const u16* __restrict__ wo, const u16* __restrict__ w1,
                                                       const u16* __restrict__ w3, const u16* __restrict__ w2, const u16* __restrict__ cnd,
                                                       const u16* __restrict__ wmod, unsigned char* ws, u16* __restrict__ out PRM_ARG) {
  __shared__ __attribute__((aligned(256))) Smem sm;
  constexpr Prm P = PRM0;
  const int TEND = P.TEND;
  const u64 pt0 = wall_clock64(); u64 pst = (PROBE_K == 100 || PROBE_K == 102) ? pt0 : 0;
  const u32 t0 = (u32)pt0;
  const int bid = blockIdx.x, xcd = bid & 7, idx = bid >> 3, part = idx & 3;
  const int tid = threadIdx.x, lane = tid & 63, wave = __builtin_amdgcn_readfirstlane(tid >> 6);
  u32 tch2 = 0;
  if ((idx | 4) == 7) {
    u64 pc; u32 len;
    asm volatile("s_getpc_b64 %0\n.Lcpf_first:\n\ts_mov_b32 %1, .Lend_a-.Lcpf_first\n\ts_max_i32 %1, %1, .Lend_b-.Lcpf_first\n\ts_max_i32 %1, %1, .Lend_c-.Lcpf_first\n\ts_max_i32 %1, %1, .Lend_d-.Lcpf_first\n\ts_max_i32 %1, %1, .Lend_e-.Lcpf_first" : "=s"(pc), "=s"(len) :: "scc");
    tch2 = __builtin_amdgcn_raw_buffer_load_b32(mk_rsrc((const void*)pc, len), tid * 64, 0, 0);
  }
  SB();
  const bool p3 = part == 3;
  typedef __attribute__((address_space(4))) const u64* kap_t;
  const u64 wa = ((kap_t)__builtin_amdgcn_kernarg_segment_ptr())[((0x5934 >> (part * 4)) & 15) + (p3 ? wave & 1 : 0)];
  const u32 te0 = TAB.e[2 * bid], te1 = TAB.e[2 * bid + 1];
  u32 pitch = (u32)(0x005600c001000020ull >> (part * 16) & 0xffff) * 32u;
  const int sh = p3 ? 2 : 3;
  u32 wv0 = (u32)((p3 ? (wave >> 1) * 128 : wave * 64) + (part == 0 ? xcd * 512 : 0) + (lane >> sh)) * pitch + ((lane & (p3 ? 3 : 7)) ^ (lane >> (p3 ? 5 : 3))) * 16u;
  const u32 wimg = (u32)(uintptr_t)(&sm.wbuf[0][0][0]);
  u32 wsz = (part == 0 ? 4096u : 512u) * pitch, wld = wimg + (p3 ? (wave & 1) * 32768 + (wave >> 1) * 8192 : wave * 8192);
  asm volatile("" : "+v"(wv0), "+s"(wsz), "+s"(wld), "+s"(pitch));
  asm volatile("" :: "s"(x), "s"(kc), "s"(vc), "s"(w2), "s"(cnd), "s"(ws), "s"(out), "s"(wa), "s"(te0), "s"(te1));
  const int bl0 = te0 & 127, nl = te0 >> 7 & 63, blk = te0 >> 13 & 63;
  const bool has_w = te0 >> 19 & 1;
  const rsrc_t rw = mk_rsrc((const void*)wa, wsz);
  const u32 wstep = pitch << (6 - sh), wflip = p3 ? 32u : 0u;
  u32 wso = blk << (sh + 4);
  int wleft = 8;
  auto piece = [&]() {
    dma16(rw, wv0, wso, wld);
    wso += wstep; wld += 1024;
    wv0 ^= (wleft & 1) ? wflip : 0u;
    --wleft;
  };
  const u32 tka = (u32)wall_clock64();
  if (has_w) piece();
  SB();
  STAMP(23);
  const int b = idx >> 2 & 1, head = xcd * 4 + (((idx >> 3) & 1) << 1 | idx >> 4);
  const rsrc_t wsr = mk_rsrc(ws, WS_END);
  const int g = lane >> 4, c = lane & 15;
  const int i8 = idx >> 2, wi = i8 * 8 + xcd;
  const bool is_up = p3 && has_w, modc = part == 2 && wi < 16;
  const size_t base = ((size_t)(b * NH + head) * SK + (SK - WIN)) * DH;
  const rsrc_t rk = mk_rsrc(kc + base, WIN * DH * 2), rv = mk_rsrc(vc + base, WIN * DH * 2);
  u32 sot;
  {
    const u32 s0 = bl0 + lane;
    u32 sb = s0 & 96u;
#pragma unroll
    for (int k = 0; k < 5; ++k) sb |= (s0 >> k & 1u) << (P.PM >> (4 * k) & 7);
    const u32 src = (sb >> 5 & 3u) | (u32)(b * 32 + head) << 2;
    sb ^= (__builtin_popcount(src & P.M0) & 1) | (__builtin_popcount(src & P.M1) & 1) << 1 | (__builtin_popcount(src & P.M2) & 1) << 2 | (__builtin_popcount(src & P.M3) & 1) << 3 | (__builtin_popcount(src & P.M4) & 1) << 4;
    sot = (sb & 7u) << 10 | (u32)wave << 13 | (sb >> 3) << 16;
    sot = lane < nl ? sot : OOB;
    PIN(sot);
  }
  u32x4 kr[NL];
  int tau = te1 & 0xffff;
  const int tauw = te1 >> 16;
  int due = 0, now = 0;
  auto gate = [&]() {
    while (now < due) { now = (int)(((u32)wall_clock64() - t0) << 8); if (now < due) __builtin_amdgcn_s_sleep(1); }
    due += tau;
  };
  auto until = [&](int t) { while ((int)(((u32)wall_clock64() - t0) << 8) < t); };
  const u32 pslot = G_P + ((b * 32 + head) * 4 + part) * 256;
  const rsrc_t rx = mk_rsrc(part == 2 ? cnd : x, (part == 1 || (part == 2 && has_w)) ? 2048u : 0u);
  u32x2 ep; u32 xs0, xs1, tch1; u32x4 xr0, xr1;
  auto small_ops = [&]() {
    ep = ld8<AUX_SC1>(wsr, wave == 7 ? WS_EPOCH : OOB, 0);
    xs0 = __builtin_amdgcn_raw_buffer_load_b16(rx, tid * 2, 0, 0); xs1 = __builtin_amdgcn_raw_buffer_load_b16(rx, tid * 2, 1024, 0);
    xr0 = ld16<0>(rx, part == 1 ? lane * 16 : OOB, 0); xr1 = ld16<0>(rx, part == 1 ? lane * 16 : OOB, 1024);
    tch1 = __builtin_amdgcn_raw_buffer_load_b32(mk_rsrc(w2 + (size_t)blk * 32 * 512, is_up ? 64u : 0u), tid == 384 ? 0 : OOB, 0, AUX_NT);
  };
  if (modc) __builtin_amdgcn_raw_buffer_store_b128(u32x4{SENT, SENT, SENT, SENT}, wsr, tid < 320 ? G_P + wi * 5120 + tid * 16 : OOB, 0, AUX_SC1);
  if (has_w) {
    if (modc) now = 1 << 30;
#pragma nounroll
    while (wleft > (int)(te0 >> 20)) { due += tauw; gate(); due -= tau; piece(); }
    due += tauw; now = 0;
  }
  SB();
  u32 tag = 0;
  u32x4 bf[8];
  auto wo_prep = [&](int s0 = 0, int s1 = 2) {
    const u32 wi_ = wimg + wave * 8192;
    const int q = (lane >> 2) & 3, pp = lane & 3;
#pragma unroll
    for (int s = s0; s < s1; ++s) {
#pragma unroll
      for (int t = 0; t < 4; ++t) {
        const int r0 = s * 32 + g * 8 + q, r1 = r0 + 4, ch = t * 2 + (pp >> 1);
        const u32x2 lo = tr_read(wi_ + r0 * 128 + ((ch ^ (r0 & 7)) * 16) + (pp & 1) * 8), hi = tr_read(wi_ + r1 * 128 + ((ch ^ (r1 & 7)) * 16) + (pp & 1) * 8);
        bf[s * 4 + t] = u32x4{lo[0], lo[1], hi[0], hi[1]};
      }
    }
  };
  auto gemv_go = [&](u32 hh) {
    sm.hb[tid] = (u16)hh; sm.hb[512 + tid] = (u16)(hh >> 16);
    WAVE_SYNC();
    const u32x4* a = (c & 11) == 0 ? reinterpret_cast<const u32x4*>(&sm.hb[(c >> 2) * 512 + wave * 64 + g * 8]) : &sm.zero[0];
    const u32x4 a0 = a[0], a1 = a[(c & 11) == 0 ? 4 : 0];
    const f32x4 z = {0.f, 0.f, 0.f, 0.f};
    f32x4 d[4];
#pragma unroll
    for (int t = 0; t < 4; ++t) d[t] = mfma16(a0, bf[t], z);
#pragma unroll
    for (int t = 0; t < 4; ++t) d[t] = mfma16(a1, bf[4 + t], d[t]);
    if (lane < 32) {
#pragma unroll
      for (int t = 0; t < 4; ++t) sm.r2[(t * 16 + c) * 20 + g * 8 + wave] = d[t][0];
    }
  };
  auto mod_stage = [&]() {
    wo_prep();
    gemv_go(pack_bf(silu_f(lo_f(xs0)), silu_f(lo_f(xs1))));
    __builtin_amdgcn_s_waitcnt(0xc07f);
    if (lane == 0) *LDSP(volatile u32, (u32)(uintptr_t)&sm.mflag[wave]) = 1u;
    if (wave == 7) {
#pragma nounroll
      for (int spin = 0; spin < SPIN_MAX; ++spin) {
        const u32 f = *LDSP(volatile u32, (u32)(uintptr_t)&sm.mflag[lane & 7]);
        if (all_ok(f != 0u)) break;
        __builtin_amdgcn_s_sleep(1);
      }
      CFENCE();
      st8_sc1(wsr, G_MOD + (blk * 64 + lane) * 8, pack_bf(sum8(&sm.r2[lane * 20]), sum8(&sm.r2[lane * 20 + 8])), tag);
    }
    STAMP(9);
  };
  if (lane < 4) sm.zero[lane] = u32x4{0u, 0u, 0u, 0u};
  if (part == 2 && has_w) {
    if (tid < 8) sm.mflag[tid] = 0u;
    LDS_BARRIER();
    if (__builtin_expect(modc, 0)) {
      small_ops();
      asm volatile("s_waitcnt vmcnt(0)" ::: "memory");
      tag = next_tag(__builtin_amdgcn_readfirstlane(ep[0]));
      mod_stage();
      __builtin_amdgcn_s_barrier();
    }
  }
  SB();
  const int fd = wi * 8 + (lane & 7);
  const int ub = wave >> 2, ud = (wave & 3) * 128 + c * 8 + g * 2;
  u32x4 sg[3]; u32 sx2;
  auto side_loads = [&]() {
    const bool fin = part == 1 && wave == 0;
    sg[0] = ld16<AUX_SC1>(wsr, is_up ? G_MOD + (1024 + ud) * 8 : (fin ? G_MOD + (1024 + fd) * 8 : OOB), 0);
    sg[1] = ld16<AUX_SC1>(wsr, is_up ? G_MOD + (1536 + ud) * 8 : (fin ? G_MOD + (2560 + fd) * 8 : OOB), 0);
    sg[2] = ld16<AUX_SC1>(wsr, is_up ? G_MOD + (2048 + ud) * 8 : OOB, 0);
    sx2 = __builtin_amdgcn_raw_buffer_load_b32(mk_rsrc(x, (is_up || fin) ? 2048u : 0u), is_up ? ub * 1024 + ud * 2 : (g & 1) * 1024 + (fd >> 1) * 4, 0, 0);
  };
  auto side_pin = [&]() { PIN(sg[0]); PIN(sg[1]); PIN(sg[2]); PIN(sx2); };
  const u32 mlo = G_ML + (head * 2 + b) * 64 + (lane & 3) * 16;
  float fmrg = 0.f;
  bool fok = wave != 0;
  auto merge_factor = [&](const u32x4& ml) {
    const float mi = u2f(ml[0]);
    float M = fmaxf(mi, dpp<XOR1>(mi)); M = fmaxf(M, dpp<XOR2>(M));
    const float e = __expf(mi - M);
    float L = e * u2f(ml[2]);
    L += dpp<XOR1>(L); L += dpp<XOR2>(L);
    fmrg = rdl(e * __builtin_amdgcn_rcpf(L), part);
  };
  f32x2 ga = {0.f, 0.f}, xin = {0.f, 0.f}, cc1 = {0.f, 0.f};
  float Af = 0.f, Bf = 0.f;
  const int hpos = ub * 512 + ud;
  auto up_prep = [&]() {
    if (!all_ok((sg[0][1] == tag) & (sg[0][3] == tag) & (sg[1][1] == tag) & (sg[1][3] == tag) & (sg[2][1] == tag) & (sg[2][3] == tag))) gather16<3>(wsr, G_MOD + (1024 + ud) * 8, 512 * 8, tag, sg);
    ga = f32x2{ub ? hi_f(sg[0][0]) : lo_f(sg[0][0]), ub ? hi_f(sg[0][2]) : lo_f(sg[0][2])};
    cc1 = f32x2{rbf(1.f + (ub ? hi_f(sg[2][0]) : lo_f(sg[2][0]))), rbf(1.f + (ub ? hi_f(sg[2][2]) : lo_f(sg[2][2])))};
    xin = f32x2{lo_f(sx2), hi_f(sx2)};
    u32* hp = reinterpret_cast<u32*>(&sm.hb[hpos]);
    hp[512] = pack_bf(cc1[0], cc1[1]);
    hp[1024] = ub ? perm_hi(sg[1][0], sg[1][2]) : perm_lo(sg[1][0], sg[1][2]);
  };
  auto up_frags = [&]() {
    const int pp = lane & 3;
    const u32 a = wimg + (pp & 1) * 32768 + ((pp >> 1) * 32 + g * 8 + ((lane >> 2) & 3)) * 64 + (((wave >> 1) ^ ((g & 1) | (pp & 2))) * 16) + (wave & 1) * 8;
#pragma unroll
    for (int rb = 0; rb < 8; ++rb) {
      const u32x2 lo = tr_read(a + rb * 4096), hi = tr_read(a + rb * 4096 + 256);
      bf[rb] = u32x4{lo[0], lo[1], hi[0], hi[1]};
    }
  };
  const u32x4* ap = reinterpret_cast<const u32x4*>(&sm.hb[((lane >> 3) & 1) * 512 + (lane & 1) * 32 + g * 8]);
  const bool par = lane & 8;
  float l0 = 0.f, l1 = 0.f;
  u32x2 gmd[2], tbg;
  int t1 = 0;
#pragma unroll
  for (int i = NCMIN / 8; i < NL; ++i) kr[i] = u32x4{0u, 0u, 0u, 0u};
  sfor<0, NL>([&](auto ic) {
    constexpr int i = ic.v;
    if (i >= NCMIN / 8 && i >= nl) return false;
    const u32 so = __builtin_amdgcn_readlane(sot, i);
    gate();
    SB();
    kr[i] = ld16<AUX_NT>(rk, lane * 16, so);
    if constexpr (i == 0) small_ops();
    if constexpr (i == TBL) tbg = ld8<AUX_SC1>(wsr, wave == 7 ? G_TB + xcd * 128 + (lane & 7) * 8 : OOB, 0);
    if constexpr (i == QP) { gmd[0] = ld8<AUX_SC1>(wsr, part == 1 ? G_MOD + tid * 8 : OOB, 0); gmd[1] = ld8<AUX_SC1>(wsr, part == 1 ? G_MOD + tid * 8 : OOB, 4096); }
    SB();
    SB();
    if (i >= WK) { WAIT_VM(WK); SB(); }
    if (i == 0) STAMP(25);
    if (i == 8) STAMP(12);
    if (i == 20) STAMP(21);
    if (i == 25) STAMP(22);
    if (i == TBP) {
      if (part == 3 && i8 == 0 && wave == 7) { tag = next_tag(__builtin_amdgcn_readfirstlane(ep[0])); if (lane < 8) st8_sc1(wsr, G_TB + lane * 128 + xcd * 8, t0 + (((tka - t0) * P.GA) >> 3) - P.GS, tag); }
      SB();
    }
    if (i == TBU) {
      if (wave == 7) {
        const bool ok = tbg[1] == tag;
        float lf = ok ? (float)min(max((int)(t0 - tbg[0]), -300), 300) : 0.f, nf = ok ? 1.f : 0.f;
        lf += dpp<XOR1>(lf); nf += dpp<XOR1>(nf); lf += dpp<XOR2>(lf); nf += dpp<XOR2>(nf); lf += dpp<XOR7>(lf); nf += dpp<XOR7>(nf);
        if (lane == 0) sm.bc = u32x2{tag, t0 - (u32)(int)(lf * __builtin_amdgcn_rcpf(fmaxf(nf, 1.f)))};
      }
      LDS_BARRIER();
      const u32x2 bc = sm.bc;
      tag = __builtin_amdgcn_readfirstlane(bc[0]);
      t1 = (__builtin_amdgcn_readfirstlane((int)(bc[1] - t0)) + TEND + P.X1) * 256;
      tau -= __builtin_amdgcn_readfirstlane((int)((float)(int)(t0 - bc[1]) * 256.f * __builtin_amdgcn_rcpf((float)(2 * nl - TBU - 2))));
      SB();
    }
    if (i == LNS) {
      tag = next_tag(__builtin_amdgcn_readfirstlane(ep[0]));
      if (part == 1) {
        float x0[8], x1[8], mb0, ri0, mb1, ri1;
#pragma unroll
        for (int k = 0; k < 4; ++k) { x0[2 * k] = lo_f(xr0[k]); x0[2 * k + 1] = hi_f(xr0[k]); x1[2 * k] = lo_f(xr1[k]); x1[2 * k + 1] = hi_f(xr1[k]); }
        ln_stats2(x0, x1, mb0, ri0, mb1, ri1);
        l0 = rbf(rbf(lo_f(xs0) - mb0) * ri0); l1 = rbf(rbf(lo_f(xs1) - mb1) * ri1);
      }
      PIN(l0); PIN(l1);
      SB();
    }
    if (i == WUSE) {
      PIN(tch1); PIN(tch2);
      if ((part == 1 || (part == 2 && has_w && !modc))) {
        WAIT_VM(WK); CFENCE();
        if (part == 2) mod_stage(); else wo_prep();
      }
#pragma unroll
      for (int k = 0; k < 8; ++k) PIN(bf[k]);
      SB();
    }
    if (i == QP + QD) {
      if (part == 1) {
        u32 gm[2] = {gmd[0][0], gmd[1][0]};
        if (!all_ok((gmd[0][1] == tag) & (gmd[1][1] == tag))) { STAMP(11); gather8<2>(wsr, G_MOD + tid * 8, 512 * 8, tag, gm); }
        STAMP(5);
        gemv_go(pack_bf(rbf(l0 * rbf(1.f + lo_f(gm[1]))) + lo_f(gm[0]), rbf(l1 * rbf(1.f + hi_f(gm[1]))) + hi_f(gm[0])));
      }
      SB();
    }
    if (i == QP + QD + 1) {
      if (part == 1) {
        LDS_BARRIER();
        if (wave == 0) {
          const int bb = lane >> 5, jq = lane & 31;
          st8_sc1(wsr, G_Q + (bb * 2048 + blk * 32 + jq) * 8, pack_bf(sum8(&sm.r2[2 * jq * 20 + bb * 8]), sum8(&sm.r2[(2 * jq + 1) * 20 + bb * 8])), tag);
        }
        STAMP(6);
      }
      SB();
    }
    return true;
  });
  PIN(sot);
  const u32 qoff = G_Q + ((b * 32 + head) * 64 + c * 4) * 8;
  u32x4 qg[2], vr[NL], fl2[3];
  u32 qq[4];
  constexpr int NB = (NL + 15) / 16;
  float xlo, sc[NB], pr[NB], m = 0.f;
  f32x4 acc = {0.f, 0.f, 0.f, 0.f};
  u32 pb[NB], pm[4], cpf;
  const u32 adst0 = lds_addr(lane < 32 ? &sm.red[wave][c * 8 + g * 4] : &sm.r2[(lane - 32) * 4]);
  u32 po, rdst, rsrc, xo;
  auto wo_addr = [&]() {
    po = G_P + (((c >> 2 & 1) * 32 + xcd * 4 + (wave >> 1)) * 4 + (c & 3)) * 256 + (wave & 1) * 128 + (c >> 3) * 64 + g * 16;
    rdst = lane < 32 ? (u32)(uintptr_t)&sm.red2[wave][0] + lane * 16 : (u32)(uintptr_t)&sm.r2[0] + (lane - 32) * 16;
    rsrc = (u32)(uintptr_t)&sm.red2[0][((wave & 1) * 16 + (lane & 15)) * 4 + (lane >> 4)];
    xo = (lane & 1) ? OOB : G_XP + xcd * 2048 + (wave & 1) * 1024 + (blk * 64 + lane) * 2;
    PIN(po); PIN(rdst); PIN(rsrc); PIN(xo);
  };
  u32 pso = wave == 0 ? pslot + lane * 4 : OOB;
  PIN(pso);
  const u32 nadd = (te0 >> 20) ? P.LW : (is_up ? P.LU : 0);
  u32 nto = tid == 0 ? G_NT + bid * 8 : OOB;
  PIN(nto);
  auto notices = [&](auto nc_, u32 off, u32x4 (&gn)[nc_.v]) -> int {
    constexpr int N = nc_.v;
#pragma nounroll
    for (int spin = 0; spin < SPIN_MAX; ++spin) {
      bool ok = true;
#pragma unroll
      for (int i = 0; i < N; ++i) ok &= (gn[i][1] == tag) & (gn[i][3] == tag);
      if (all_ok(ok)) break;
      __builtin_amdgcn_s_sleep(10);
      CFENCE();
#pragma unroll
      for (int i = 0; i < N; ++i) gn[i] = ld16<AUX_SC1>(wsr, off, i * 1024);
    }
    int mx = 0;
#pragma unroll
    for (int i = 0; i < N; ++i) mx = max(mx, max((int)(gn[i][0] - t0), (int)(gn[i][2] - t0)));
    return __builtin_amdgcn_readfirstlane((int)wave_max((float)min(mx, 1 << 20)));
  };
  const int side = nl >= SIDE + 13 ? SIDE + 8 : (nl >= SIDE + 9 ? SIDE + 4 : SIDE);
  sfor<0, NL + WV>([&](auto ic) {
    constexpr int v = ic.v;
    if (cons_end(v - 1) >= NCMIN / 8 && cons_end(v - 1) >= nl) return false;
    if constexpr (v < NL) {
      const u32 so = __builtin_amdgcn_readlane(sot, v);
      if constexpr (v >= 12) { if (v == nl - P.NE) tau = (tau * P.FE) >> 4; }
      if constexpr (v >= NCMIN / 8) { if (v < nl) gate(); } else gate();
      SB();
      vr[v] = ld16<AUX_NT>(rv, lane * 16, so);
      SB();
      SB();
    }
    if constexpr (v == NCMIN / 8 - 1) {
#pragma unroll
      for (int i = NCMIN / 8; i < NL; ++i) asm volatile("" : "=v"(vr[i]));
      asm volatile("" : "=v"(fl2[1]), "=v"(fl2[2]));
    }
    if constexpr (v == VQ - 4) { qg[0] = ld16<AUX_SC1>(wsr, qoff, 0); qg[1] = ld16<AUX_SC1>(wsr, qoff + 16, 0); }
    if constexpr (v == VSIDE) side_loads();
    if constexpr (v == CPF) {
      u64 pc; u32 dlt, len;
      asm volatile("s_getpc_b64 %0\n.Lcpf_here:\n\ts_mov_b32 %1, .Ltail_start-.Lcpf_here\n\ts_mov_b32 %2, .Lend_a-.Ltail_start\n\ts_max_i32 %2, %2, .Lend_b-.Ltail_start\n\ts_max_i32 %2, %2, .Lend_c-.Ltail_start\n\ts_max_i32 %2, %2, .Lend_d-.Ltail_start\n\ts_max_i32 %2, %2, .Lend_e-.Ltail_start" : "=s"(pc), "=s"(dlt), "=s"(len) :: "scc");
      pc += dlt;
      const rsrc_t rc = mk_rsrc((const void*)pc, (part == 3 && i8 == 1) ? len : 0u);
      cpf = __builtin_amdgcn_raw_buffer_load_b32(rc, tid * 64, 0, 0);
    }
    if constexpr (v == CPF + WV + 1) PIN(cpf);
    if constexpr (v == SIDE || v == SIDE + 4 || v == SIDE + 8) fl2[(v - SIDE) / 4] = ld16<AUX_SC1>(wsr, (wave == 0 && v == side) ? mlo : OOB, 0);
    SB();
    if constexpr (v < WV) { WAIT_VM(WK); SB(); } else if constexpr (v < NL && cons_end(v) < v - WV + 1) { WAIT_VM(WV); SB(); }
    if constexpr (v == VQ) {
      if (!all_ok((qg[0][1] == tag) & (qg[0][3] == tag) & (qg[1][1] == tag) & (qg[1][3] == tag))) { STAMP(20); gather16<2>(wsr, qoff, 16, tag, qg); }
      STAMP(1); WSTAMP(1);
      qq[0] = qg[0][0]; qq[1] = qg[0][2]; qq[2] = qg[1][0]; qq[3] = qg[1][2];
    }
    if constexpr (v >= VQ && v < VS) {
      constexpr int sl = v - VQ, bt = sl >> 1;
      float xs[8];
#pragma unroll
      for (int i = 0; i < 8; ++i) {
        float d = 0.f;
        if (sl * 8 + i < NL) {
#pragma unroll
          for (int k = 0; k < 4; ++k) d = dot2(kr[sl * 8 + i][k], qq[k], d);
        }
        xs[i] = d;
      }
#pragma unroll
      for (int h = 0; h < 4; ++h) xs[h] = sel_add<XOR7>(xs[h], xs[h + 4], c & 4);
#pragma unroll
      for (int h = 0; h < 2; ++h) xs[h] = sel_add<XOR2>(xs[h], xs[h + 2], c & 2);
      xs[0] = sel_add<XOR1>(xs[0], xs[1], c & 1);
      if constexpr ((sl & 1) || sl == NSL - 1) {
        const float t = (sl & 1) ? sel_add<XOR8>(xlo, xs[0], c & 8) : xs[0] + dpp<XOR8>(xs[0]);
        const float s = rbf(rbf(t) * 0.08838834764831845f);
        sc[bt] = (bt * 16 + c < nl) ? s : -__builtin_inff();
        PIN(sc[bt]);
      } else {
        xlo = xs[0];
        PIN(xlo);
      }
    }
    if constexpr (v == VS) {
      float mloc = sc[0];
#pragma unroll
      for (int bt = 1; bt < NB; ++bt) mloc = fmaxf(mloc, sc[bt]);
      m = fmaxf(wave_max(mloc), -3.0e38f);
      float ls = 0.f;
#pragma unroll
      for (int bt = 0; bt < NB; ++bt) { pr[bt] = __expf(sc[bt] - m); ls += pr[bt]; }
      const float l = wave_sum(ls);
      if (lane == 0) { sm.ml[wave][0] = m; sm.ml[wave][1] = l; }
      STAMP(2); WSTAMP(2);
    }
    sfor<cons_end(v - 1), cons_end(v)>([&](auto jc) {
      constexpr int i = jc.v;
      if (i < NCMIN / 8 || i < nl) {
        const u32 pv = __float_as_uint(bcast16(u2f(pb[i >> 4]), i & 15));
        acc = mfma16(u32x4{pv & pm[0], pv & pm[1], pv & pm[2], pv & pm[3]}, vr[i], acc);
      }
      PIN(acc);
      return true;
    });
    if constexpr (v == MLS - 1) { side_pin(); if (is_up) up_prep(); PIN(ga); PIN(xin); PIN(cc1); }
    if constexpr (v == UPREP - 1) wo_addr();
    if constexpr (v == UPREP) {
#pragma unroll
      for (int k = 0; k < 8; ++k) asm volatile("" : "=v"(bf[k]));
      if (part == 0) wo_prep(0, 1); else if (is_up) up_frags();
#pragma unroll
      for (int k = 0; k < 8; ++k) PIN(bf[k]);
    }
    if constexpr (v == UPREP + 1) {
      if (is_up) {
        const u32x4* a1 = ap + 128 + ((lane >> 1) & 1) * 128;
        f32x4 d0 = {0.f, 0.f, 0.f, 0.f}, d1 = {0.f, 0.f, 0.f, 0.f};
#pragma unroll
        for (int rb = 0; rb < 4; ++rb) { d0 = mfma16(a1[rb * 8], bf[rb], d0); d1 = mfma16(a1[(rb + 4) * 8], bf[rb + 4], d1); }
        Af = par ? d0[1] + d1[1] : d0[0] + d1[0]; Bf = par ? d0[3] + d1[3] : d0[2] + d1[2];
        Af += dpp<XOR8>(Af); Bf += dpp<XOR8>(Bf);
      }
      PIN(Af); PIN(Bf);
    }
    if constexpr (v == SIDE + WV + 1 || v == SIDE + WV + 5 || v == SIDE + WV + 9) {
      if (wave == 0 && v == side + WV + 1) {
        const u32x4 ml = fl2[(v - SIDE - WV - 1) / 4];
        fok = all_ok((ml[1] == tag) & (ml[3] == tag));
        merge_factor(ml);
      }
    }
    if (v % 6 == 4 && v >= 10 && v < 30) STAMP(15 + v / 6);
    if constexpr (v == MLS) {
      LDS_BARRIER();
      float mm[8], ll[8];
#pragma unroll
      for (int w = 0; w < 8; ++w) { const f32x2 t = *reinterpret_cast<const f32x2*>(&sm.ml[w][0]); mm[w] = t[0]; ll[w] = t[1]; }
      float Mwg = mm[0], Lwg = 0.f;
#pragma unroll
      for (int w = 1; w < 8; ++w) Mwg = fmaxf(Mwg, mm[w]);
      if (wave == 0) {
#pragma unroll
        for (int w = 0; w < 8; ++w) Lwg = fmaf(__expf(mm[w] - Mwg), ll[w], Lwg);
        if (lane == 0) st16_sc1(wsr, G_ML + ((head * 2 + b) * 4 + part) * 16, __float_as_uint(Mwg), __float_as_uint(Lwg), tag);
      }
      const float fw = __expf(m - Mwg);
#pragma unroll
      for (int bt = 0; bt < NB; ++bt) { pb[bt] = pack_bf(pr[bt] * fw, pr[bt] * fw); PIN(pb[bt]); }
#pragma unroll
      for (int k = 0; k < 4; ++k) { pm[k] = (c >> 1) == k ? ((c & 1) ? 0xffff0000u : 0x0000ffffu) : 0u; PIN(pm[k]); }
    }
    SB();
    return true;
  });
  asm volatile(".Ltail_start:");
  *LDSP(f32x4, adst0) = acc;
  STAMP(14); WSTAMP(14);
  if (!fok) {
    u32x4 ml;
#pragma nounroll
    for (int spin = 0; spin < SPIN_MAX; ++spin) {
      CFENCE();
      ml = ld16<AUX_SC1>(wsr, mlo, 0);
      if (all_ok((ml[1] == tag) & (ml[3] == tag))) break;
    }
    merge_factor(ml);
  }
  STAMP(15);
  LDS_BARRIER();
  STAMP(3);
  {
    u32 pw = 0;
    if (wave == 0) {
      f32x2 t[8];
#pragma unroll
      for (int w = 0; w < 8; ++w) t[w] = *reinterpret_cast<const f32x2*>(&sm.red[w][2 * lane]);
      const f32x2 o = ((t[0] + t[1]) + (t[2] + t[3])) + ((t[4] + t[5]) + (t[6] + t[7]));
      pw = min(pack_bf(o[0] * fmrg, o[1] * fmrg), SENT - 1u);
    }
    __builtin_amdgcn_raw_buffer_store_b32(pw, wsr, pso, 0, AUX_SC1);
    st8_sc1(wsr, nto, (u32)wall_clock64() + nadd, tag);
    SB();
#pragma unroll
    for (int i = NCMIN / 8; i < NL; ++i) PIN(vr[i]);
    PIN(fl2[0]); PIN(fl2[1]); PIN(fl2[2]);
  }
  STAMP(4);

  if (part == 2) FINISH();
  if (part == 0) {
    __builtin_amdgcn_s_barrier();
#pragma nounroll
    while (wleft > 0) piece();
    until(t1);
    STAMP(11);
    CFENCE();
    u32x4 og[2];
    og[0] = ld16<AUX_SC1>(wsr, po, 0);
    SB();
    WAIT_VM(1); CFENCE();
    wo_prep(1, 2);
#pragma unroll
    for (int k = 4; k < 8; ++k) PIN(bf[k]);
    SB();
    f32x4 d[4];
    auto wo_mm = [&]() {
      const f32x4 z = {0.f, 0.f, 0.f, 0.f};
#pragma unroll
      for (int t = 0; t < 4; ++t) { d[t] = mfma16(og[0], bf[t], z); og[1][t] = __float_as_uint(dpp<XOR8>(u2f(og[0][t]))); }
      SB();
#pragma unroll
      for (int t = 0; t < 4; ++t) d[t] = mfma16(og[1], bf[4 + t], d[t]);
    };
    auto p_bad = [&]() { return !all_ok(max(max(og[0][0], og[0][1]), max(og[0][2], og[0][3])) != SENT); };
    wo_mm();
    if (__builtin_expect(p_bad(), 0)) {
#pragma nounroll
      for (int spin = 0; spin < SPIN_MAX; ++spin) {
        __builtin_amdgcn_s_sleep(1);
        CFENCE();
        og[0] = ld16<AUX_SC1>(wsr, po, 0);
        if (!p_bad()) break;
      }
      wo_mm();
      SB();
      asm volatile(".Lend_d:");
    }
    STAMP(5); WSTAMP(5);
    {
      SB();
      f32x4 y; f32x2 h[4];
#pragma unroll
      for (int t = 0; t < 4; ++t) { h[t] = d[t].xy + d[t].zw; PIN(h[t]); }
#pragma unroll
      for (int t = 0; t < 4; ++t) { y[t] = h[t][0] + h[t][1]; PIN(y[t]); }
      *LDSP(f32x4, rdst) = y;
    }
    STAMP(16);
    LDS_BARRIER();
    STAMP(17);
    if (wave < 2) {
      f32x2 v[4];
#pragma unroll
      for (int w = 0; w < 4; ++w) v[w] = f32x2{LDSP(float, rsrc)[w * 256], LDSP(float, rsrc)[w * 256 + 128]};
      const f32x2 h = (v[0] + v[1]) + (v[2] + v[3]);
      const float y = h[0] + h[1];
      __builtin_amdgcn_raw_buffer_store_b32(min(pack_bf(y, dpp<XOR1>(y)), SENT - 1u), wsr, xo, 0, AUX_SC1);
      asm volatile(".Lend_a:");
    }
    STAMP(6);
    FINISH();
  }
  if (part == 3) {
    if (!has_w) FINISH();
    u32x4 b2[4];
    {
      __builtin_amdgcn_s_barrier();
      const rsrc_t r2 = mk_rsrc(w2 + (size_t)(blk * 32 + wave * 4) * 512, 4096u);
      const u32 l2 = (u32)(uintptr_t)(&sm.w2b[wave * 4][0]);
#pragma unroll
      for (int n = 0; n < 4; ++n) dma16(r2, (lane ^ (n * 2) ^ ((wave & 2) << 2)) * 16, n * 1024, l2 + n * 1024);
    }
    const u32 tdst = lds_addr(&sm.hb[hpos]);
    const u32 sdst = lds_addr(&sm.stat[wave][lane]);
    const u32 ssrc = lds_addr(&sm.stat[(g >> 1) * 4 + ((lane >> 1) & 3)][(lane & 1) * 16]);
    const u32 adst = lds_addr((c < 4 && (g & 1) == 0) ? &sm.actl[g >> 1][wave * 4 + c] : reinterpret_cast<u16*>(&sm.dummy[lane]));
    const u32 asrc = lds_addr(&sm.actl[lane & 1][g * 8]);
    const u32 apa = lds_addr(&sm.hb[(c >> 3) * 512 + (c & 3) * 64 + ((c >> 2) & 1) * 32 + g * 8]);
    u32 xoff = G_XP + g * 2048 + ub * 1024 + (wave & 3) * 256 + c * 16;
    u32x4 one;
#pragma unroll
    for (int k = 0; k < 4; ++k) one[k] = ((c & 2) == 0 && (c >> 2) == k) ? ((c & 1) ? 0x3f800000u : 0x00003f80u) : 0u;
    PIN(one);
    u32 ooff = G_OP + (blk * 512 + tid) * 8;
    float Afp = Af, Bfp = Bf;
    PIN(xoff); PIN(ooff); PIN(Afp); PIN(Bfp);
    PIN(ga); PIN(xin); PIN(cc1);
    u32 w2a = (u32)(uintptr_t)(&sm.w2b[0][0]) + (g * 8 + ((lane >> 2) & 3)) * 1024 + (wave ^ (g & 1)) * 128 + (lane & 15) * 8;
    PIN(w2a);
    if (wave == 1) {
      due = t1 + P.N1 * 256; gate();
      u32x4 gn[2] = {ld16<AUX_SC1>(wsr, G_NT + lane * 16, 0), ld16<AUX_SC1>(wsr, G_NT + lane * 16, 1024)};
      const int tn = notices(IC<2>{}, G_NT + lane * 16, gn) * 256;
      due = (tn > t1 + 2 * 256 ? max(tn, t1 + P.L0 * 256) + P.L1 * 256 : t1) + P.X2 * 256;
      until(due);
    }
    asm volatile("s_waitcnt vmcnt(0)" ::: "memory");
    STAMP(9);
    __builtin_amdgcn_s_barrier();
    STAMP(11);
    CFENCE();
    u32x4 xg[2] = {ld16<AUX_SC1>(wsr, xoff, 0), ld16<AUX_SC1>(wsr, xoff, 8192)};
    SB();
#pragma unroll
    for (int t = 0; t < 4; ++t) {
      const u32x2 lo = tr_read(w2a ^ (t * 32)), hi = tr_read((w2a ^ (t * 32)) + 4096);
      b2[t] = u32x4{lo[0], lo[1], hi[0], hi[1]};
    }
#pragma unroll
    for (int t = 0; t < 4; ++t) PIN(b2[t]);
    SB();
    const f32x4 z4 = {0.f, 0.f, 0.f, 0.f};
    auto xp_bad = [&]() { return !all_ok(max(max(max(xg[0][0], xg[0][1]), max(xg[0][2], xg[0][3])), max(max(xg[1][0], xg[1][1]), max(xg[1][2], xg[1][3]))) != SENT); };
    f32x4 v = mfma16(one, xg[1], mfma16(one, xg[0], z4));
    if (__builtin_expect(xp_bad(), 0)) {
#pragma nounroll
      for (int spin = 0; spin < SPIN_MAX; ++spin) {
        __builtin_amdgcn_s_sleep(1);
        CFENCE();
        xg[0] = ld16<AUX_SC1>(wsr, xoff, 0); xg[1] = ld16<AUX_SC1>(wsr, xoff, 8192);
        if (!xp_bad()) break;
      }
      v = mfma16(one, xg[1], mfma16(one, xg[0], z4));
      SB();
      asm volatile(".Lend_e:");
    }
    STAMP(5); WSTAMP(5);
    {
      const float a0 = v[0], a1 = v[1];
      const float x10 = fmaf(ga[0], a0, xin[0]), x11 = fmaf(ga[1], a1, xin[1]);
      *LDSP(u32, tdst) = pack_bf(x10 * cc1[0], x11 * cc1[1]);
      float u = swap32_add(x10 + x11, fmaf(x10, x10, x11 * x11));
      u += dpp<XOR1>(u); u += dpp<XOR2>(u); u += dpp<XOR7>(u); u += dpp<XOR8>(u);
      *LDSP(float, sdst) = u;
    }
    STAMP(16);
    LDS_BARRIER();
    STAMP(17);
    {
      const f32x4 z = {0.f, 0.f, 0.f, 0.f};
      const u32x4 af0 = LDSP(u32x4, apa)[0], af1 = LDSP(u32x4, apa)[32];
      float s = LDSP(float, ssrc)[0], q = LDSP(float, ssrc)[32];
      SB();
      f32x4 d[4];
#pragma unroll
      for (int j = 0; j < 4; ++j) d[j] = mfma16(af0, bf[j], z);
#pragma unroll
      for (int j = 0; j < 4; ++j) d[j] = mfma16(af1, bf[4 + j], d[j]);
      s += dpp<XOR1>(s); q += dpp<XOR1>(q); s += dpp<XOR2>(s); q += dpp<XOR2>(q); s += dpp<XOR7>(s); q += dpp<XOR7>(q);
      const float mean = s * (1.f / 512.f);
      const float var = fmaf(q, 1.f / 512.f, -mean * mean);
      const float ri = __builtin_amdgcn_rsqf(fmaxf(var, 0.f) + 1e-5f);
      const float cf = fmaf(-ri * mean, Afp, Bfp);
      float y = (d[0][0] + d[1][1]) + (d[2][2] + d[3][3]);
      y = __builtin_bit_cast(float, __builtin_amdgcn_update_dpp(__builtin_bit_cast(int, y), __builtin_bit_cast(int, y), XOR8, 0xa, 0xf, false));
      y = swap16_add(y, y);
      y = fmaf(y, ri, cf);
      const float up = dpp<0x104>(y);
      *LDSP(u16, adst) = (u16)pack_bf(silu_f(y) * up, 0.f);
    }
    STAMP(18);
    LDS_BARRIER();
    STAMP(10);
    {
      const u32x4 af = *LDSP(u32x4, asrc);
      const f32x4 z = {0.f, 0.f, 0.f, 0.f};
      const f32x4 d0 = mfma16(af, b2[0], z), d1 = mfma16(af, b2[1], z), d2 = mfma16(af, b2[2], z), d3 = mfma16(af, b2[3], z);
      const bool g1 = g & 1, g2 = g & 2;
      const float e0 = g1 ? d1[0] : d0[0], e1 = g1 ? d1[1] : d0[1], f0 = g1 ? d3[0] : d2[0], f1 = g1 ? d3[1] : d2[1];
      st8_sc1(wsr, ooff, pack_bf(g2 ? f0 : e0, g2 ? f1 : e1), tag);
    }
    asm volatile(".Lend_b:");
    STAMP(6);
    FINISH();
  }
  if (wave == 0) {
    const int d = fd, ul = lane >> 3;
    u32 gm[2] = {sg[0][0], sg[1][0]};
    if (!all_ok((sg[0][1] == tag) & (sg[1][1] == tag))) gather8<2>(wsr, G_MOD + (1024 + d) * 8, 1536 * 8, tag, gm);
    u32 xa0, xa1;
    {
      due = t1 + P.N2 * 256; gate();
      u32x4 gn[2] = {ld16<AUX_SC1>(wsr, G_NT + lane * 16, 0), ld16<AUX_SC1>(wsr, G_NT + lane * 16, 1024)};
      const int tn = notices(IC<2>{}, G_NT + lane * 16, gn) * 256;
      due = (tn > t1 + 2 * 256 ? max(tn, t1 + P.L0 * 256) + P.L1 * 256 : t1) + P.X3 * 256;
      until(due);
    }
#pragma nounroll
    for (int spin = 0; spin < SPIN_MAX; ++spin) {
      CFENCE();
      xa0 = __builtin_amdgcn_raw_buffer_load_b32(wsr, G_XP + ul * 2048 + (d >> 1) * 4, 0, AUX_SC1);
      xa1 = __builtin_amdgcn_raw_buffer_load_b32(wsr, G_XP + ul * 2048 + (d >> 1) * 4, 1024, AUX_SC1);
      if (all_ok(max(xa0, xa1) != SENT)) break;
      __builtin_amdgcn_s_sleep(1);
    }
    float a0 = (d & 1) ? hi_f(xa0) : lo_f(xa0), a1 = (d & 1) ? hi_f(xa1) : lo_f(xa1);
    a0 += dpp<XOR8>(a0); a1 += dpp<XOR8>(a1);
    a0 = swap16_add(a0, a0); a1 = swap16_add(a1, a1);
    a0 = swap32_add(a0, a0); a1 = swap32_add(a1, a1);
    const bool hb = g & 1;
    float xv = rbf(((d & 1) ? hi_f(sx2) : lo_f(sx2)) + rbf((hb ? hi_f(gm[0]) : lo_f(gm[0])) * rbf(hb ? a1 : a0)));
    float gv = hb ? hi_f(gm[1]) : lo_f(gm[1]);
    PIN(xv); PIN(gv);
    if (wi == 0 && lane == 0) __builtin_amdgcn_raw_buffer_store_b32(tag, wsr, WS_EPOCH, 0, AUX_SC1);
    STAMP(10);
    due = max(due + P.X4 * 256, (int)(((u32)wall_clock64() - t0) << 8) + P.GX * 256);
    const bool last = ul + 40 < NUP;
    u32 o0 = G_OP + (ul * 512 + d) * 8, o5 = last ? o0 : OOB, tg5 = last ? tag : 0u;
    const rsrc_t rout = mk_rsrc(out, 2048u);
    u32 oo = (lane & 40) == 0 ? (g * 512 + d) * 2 : OOB;
    const u32 pk = c == 0 ? 0x00003f80u : (c == 4 ? 0x3f800000u : 0u);
    u32x4 one = {pk, 0u, pk, 0u};
    PIN(o0); PIN(o5); PIN(tg5); PIN(oo); PIN(one);
    __builtin_amdgcn_raw_buffer_store_b16((u16)pack_bf(xv, 0.f), rout, oo, 0, AUX_SC1);
    until(due);
    f32x4 sv;
#pragma nounroll
    for (int spin = 0; spin < SPIN_MAX; ++spin) {
      CFENCE();
      u32x2 og[6];
#pragma unroll
      for (int k = 0; k < 5; ++k) og[k] = ld8<AUX_SC1>(wsr, o0, k * 32768);
      og[5] = ld8<AUX_SC1>(wsr, o5, 5 * 32768);
      sv = f32x4{0.f, 0.f, 0.f, 0.f};
#pragma unroll
      for (int k = 0; k < 3; ++k) sv = mfma16(one, u32x4{og[2 * k][0], og[2 * k][1], og[2 * k + 1][0], og[2 * k + 1][1]}, sv);
      const u32 bad = ((og[0][1] ^ tag) | (og[1][1] ^ tag) | (og[2][1] ^ tag)) | ((og[3][1] ^ tag) | (og[4][1] ^ tag) | (og[5][1] ^ tg5));
      PIN(sv);
      if (all_ok(bad == 0u)) break;
      __builtin_amdgcn_s_sleep(1);
    }
    STAMP(7);
    const float s = sv[0] + dpp<XOR8>(sv[0]);
    __builtin_amdgcn_raw_buffer_store_b16((u16)pack_bf(fmaf(gv, s, xv), 0.f), rout, oo, 0, AUX_SC1);
    asm volatile(".Lend_c:");
  }
  FINISH();
}

extern "C" void kernel_launch(void* const* d_in, const int* in_sizes, int n_in,
                              void* d_out, int out_size, void* d_ws, size_t ws_size,
                              hipStream_t stream) {
  fused_kernel<<<256, 512, 0, stream>>>((const u16*)d_in[0], (const u16*)d_in[1], (const u16*)d_in[2], (const u16*)d_in[3], (const u16*)d_in[4],
                                        (const u16*)d_in[6], (const u16*)d_in[7], (const u16*)d_in[8], (const u16*)d_in[9], (const u16*)d_in[10],
                                        (unsigned char*)d_ws, (u16*)d_out
    );
}
```

```cpp
#define PROBE_MUL 10
#include <hip/hip_runtime.h>
#include <stdint.h>

typedef unsigned int u32;
typedef unsigned short u16;
typedef unsigned long long u64;
typedef __attribute__((ext_vector_type(4))) unsigned u32x4;
typedef __attribute__((ext_vector_type(2))) unsigned u32x2;
typedef __attribute__((ext_vector_type(2))) __bf16 bf16x2;
typedef __attribute__((ext_vector_type(4))) float f32x4;
typedef __attribute__((ext_vector_type(2))) float f32x2;
typedef __amdgpu_buffer_rsrc_t rsrc_t;

constexpr int NH = 32, SK = 16384, DH = 128, WIN = 4096, FF = 1376;
struct Prm { int E0, ES, E1, E2, E2C, EU, TEND, DT_UP, L0, L1, X1, X2, X3, X4, NE, FE, M0, M1, M2, M3, M4, GS, GX, N1, N2, GA, WD, DT_WO, LW, LU, EW, PM, NF; };
constexpr Prm PRM0 = {32, 24, 32, 32, 56, 64, 2010, 60, 30, 25, 92, 86, 131, 88, 4, 12, 5, 8, 21, 4, 32, 0, 56, 28, 70, 0, 4, 100, 45, 15, 8, 0x24310, 1};
constexpr int NL = 37;
constexpr int NCMIN = 192;
constexpr int WK = 8, WV = 5;
constexpr int WUSE = 12, LNS = 7;
constexpr int QP = 14, QD = 4;
constexpr int VQ = 6;
constexpr int NSL = (NL + 7) / 8;
constexpr int VS = VQ + NSL;
constexpr int MLS = VS + 2;
constexpr int VSIDE = MLS - 5, UPREP = MLS + 8;
constexpr int SIDE = NCMIN / 8 - 4;
constexpr int CPF = 18;
constexpr int SMO = 1;
constexpr int TBP = 4, TBL = 11, TBU = 15;
constexpr int cons_end(int v) {
  const int a = v - WV + 1, c = (v - MLS) * 4;
  const int m = a < c ? a : c;
  return m < 0 ? 0 : (m > NL ? NL : m);
}
struct Tab { u32 e[512]; };
constexpr Tab make_tab(const Prm& P) {
  Tab T{};
  for (int bid = 0; bid < 256; ++bid) {
    const int xcd = bid & 7, part = bid >> 4 & 3, i8 = (bid >> 3 & 1) | bid >> 6 << 1, wi = i8 * 8 + xcd;
    const bool has_mod = wi < 48, has_up = wi >= 21;
    const int cb0 = (i8 + 5) & 7, cb1 = (i8 + 4) & 7, ms = wi - 20;
    const int mi = wi < 8 ? wi : (wi < 16 ? 8 + ((xcd + 4) & 7) : (wi < 20 ? (wi - 14) * 8 + 3 : 16 + ms / 7 * 8 + ms % 7 + (ms % 7 >= 3 ? 1 : 0)));
    const int ui = xcd * 5 + (xcd > 5 ? xcd - 5 : 0) + (i8 >= 3 ? (xcd >= 5 ? 1 : 0) + i8 - 3 : 0);
    const int wd = cb0 == 3 ? 0 : P.WD;
    const int e[4] = {P.E0 - 4 * wd + (cb0 == 3 ? P.ES : 0), P.E1 + (cb1 == 3 ? P.ES : 0), has_mod ? (wi < 16 ? P.E2C : P.E2) + ((mi & 7) == 3 ? P.ES : 0) : 0, has_up ? P.EU : 0};
    const int ew = wd ? P.EW : 0, pa = (1024 + e[0] + ew + e[1] + e[2] + e[3]) / 4;
    int c[5] = {0, 0, 0, 0, 124};
    for (int k = 0; k < 3; ++k) c[k + 1] = c[k] + (pa - e[k] - (k == 0 ? ew : 0) + 4) / 8 - 1;
    const int nl = c[part + 1] - c[part] + 1, wst = e[part] / 4;
    const bool has_w = part < 2 || (part == 2 ? has_mod : has_up);
    const int blk = !has_w ? 0 : (part == 0 ? cb0 : (part == 1 ? xcd * 8 + cb1 : (part == 2 ? mi : ui)));
    const int tau = (part == 3 && has_up ? P.TEND - P.DT_UP : (part == 0 && wd ? P.TEND - P.DT_WO : P.TEND)) * 1024 / (4 * (2 * nl + wst - 2) + P.NF);
    T.e[2 * bid] = (u32)(c[part] | nl << 7 | blk << 13 | (has_w ? 1 : 0) << 19 | (part == 0 ? wd : 0) << 20);
    T.e[2 * bid + 1] = (u32)(tau | (tau * wst / (part == 0 && wd < 8 ? 8 - wd : 8)) << 16);
  }
  return T;
}
constexpr int step_range(const Prm& P, bool mx_) {
  int mx = 0, mn = 1 << 20;
  const Tab T = make_tab(P);
  for (int bid = 0; bid < 256; ++bid) { const int n = T.e[2 * bid] >> 7 & 63; mx = n > mx ? n : mx; mn = n < mn ? n : mn; }
  return mx_ ? mx : mn;
}
extern "C" __attribute__((used, visibility("protected"))) __constant__ const Tab wg_tab = make_tab(PRM0);
static_assert(step_range(PRM0, true) <= NL && step_range(PRM0, false) >= NCMIN / 8, "a wave's key range does not fit its load steps");
static_assert(cons_end(SIDE + WV + 1) <= NCMIN / 8 && cons_end(UPREP) <= NCMIN / 8 && SIDE > MLS + 3, "steps every wave runs");
constexpr int NUP = FF / 32;
constexpr int SPIN_MAX = 1 << 17;

constexpr u32 WS_EPOCH = 0;
constexpr u32 G_TB = 1024;
constexpr u32 G_NT = 2048;
constexpr u32 G_MOD = 4096;
constexpr u32 G_Q = G_MOD + 3072 * 8;
constexpr u32 G_ML = G_Q + 4096 * 8;
constexpr u32 G_P = G_ML + 256 * 16;
constexpr u32 G_XP = G_P + 256 * 256;
constexpr u32 G_OP = G_XP + 8 * 512 * 4;
constexpr u32 WS_END = G_OP + 43 * 512 * 8;
constexpr u32 SENT = 0xffffffffu;
constexpr u32 OOB = 0x40000000u;
#define WST 0

constexpr int AUX_NT = 2, AUX_SC1 = 16;

__device__ __forceinline__ float lo_f(u32 v) { return __uint_as_float(v << 16); }
__device__ __forceinline__ float hi_f(u32 v) { return __uint_as_float(v & 0xffff0000u); }
__device__ __forceinline__ float rbf(float x) { return (float)(__bf16)x; }
__device__ __forceinline__ u32 pack_bf(float lo, float hi) {
  bf16x2 r; r[0] = (__bf16)lo; r[1] = (__bf16)hi; return __builtin_bit_cast(u32, r);
}
__device__ __forceinline__ float dot2(u32 k, u32 q, float acc) {
  return __builtin_amdgcn_fdot2_f32_bf16(__builtin_bit_cast(bf16x2, k), __builtin_bit_cast(bf16x2, q), acc, false);
}
template <int CTRL> __device__ __forceinline__ float dpp(float x) {
  return __builtin_bit_cast(float, __builtin_amdgcn_mov_dpp(__builtin_bit_cast(int, x), CTRL, 0xf, 0xf, true));
}
constexpr int XOR1 = 0xB1, XOR2 = 0x4E, XOR7 = 0x141, XOR8 = 0x128;
__device__ __forceinline__ float swap32_add(float a, float b) {
  auto t = __builtin_amdgcn_permlane32_swap(__float_as_uint(a), __float_as_uint(b), false, false);
  return __uint_as_float(t[0]) + __uint_as_float(t[1]);
}
__device__ __forceinline__ float swap16_add(float a, float b) {
  auto t = __builtin_amdgcn_permlane16_swap(__float_as_uint(a), __float_as_uint(b), false, false);
  return __uint_as_float(t[0]) + __uint_as_float(t[1]);
}
__device__ __forceinline__ float wave_sum(float x) {
  x += dpp<XOR1>(x); x += dpp<XOR2>(x); x += dpp<XOR7>(x); x += dpp<XOR8>(x);
  x = swap16_add(x, x);
  return swap32_add(x, x);
}
__device__ __forceinline__ float wave_max(float x) {
  x = fmaxf(x, dpp<XOR1>(x)); x = fmaxf(x, dpp<XOR2>(x)); x = fmaxf(x, dpp<XOR7>(x)); x = fmaxf(x, dpp<XOR8>(x));
  auto s = __builtin_amdgcn_permlane16_swap(__float_as_uint(x), __float_as_uint(x), false, false);
  x = fmaxf(__uint_as_float(s[0]), __uint_as_float(s[1]));
  auto t = __builtin_amdgcn_permlane32_swap(__float_as_uint(x), __float_as_uint(x), false, false);
  return fmaxf(__uint_as_float(t[0]), __uint_as_float(t[1]));
}
template <int CTRL> __device__ __forceinline__ float sel_add(float lo, float hi, bool up) {
  const float t1 = lo + dpp<CTRL>(lo), t2 = hi + dpp<CTRL>(hi);
  return up ? t2 : t1;
}
template <int I> __device__ __forceinline__ float bc16(float x) {
  return __builtin_bit_cast(float, __builtin_amdgcn_update_dpp(0, __builtin_bit_cast(int, x), 0x150 + I, 0xf, 0xf, false));
}
__device__ __forceinline__ float bcast16(float x, int i) {
  switch (i) {
    case 0: return bc16<0>(x); case 1: return bc16<1>(x); case 2: return bc16<2>(x); case 3: return bc16<3>(x);
    case 4: return bc16<4>(x); case 5: return bc16<5>(x); case 6: return bc16<6>(x); case 7: return bc16<7>(x);
    case 8: return bc16<8>(x); case 9: return bc16<9>(x); case 10: return bc16<10>(x); case 11: return bc16<11>(x);
    case 12: return bc16<12>(x); case 13: return bc16<13>(x); case 14: return bc16<14>(x); default: return bc16<15>(x);
  }
}
__device__ __forceinline__ float silu_f(float x) { return x * __builtin_amdgcn_rcpf(1.f + __expf(-x)); }

template <int AUX> __device__ __forceinline__ u32x4 ld16(rsrc_t r, u32 voff, u32 soff) {
  return __builtin_amdgcn_raw_buffer_load_b128(r, voff, soff, AUX);
}
template <int AUX> __device__ __forceinline__ u32x2 ld8(rsrc_t r, u32 voff, u32 soff) {
  return __builtin_amdgcn_raw_buffer_load_b64(r, voff, soff, AUX);
}
__device__ __forceinline__ void st8_sc1(rsrc_t r, u32 voff, u32 v, u32 tag) {
  __builtin_amdgcn_raw_buffer_store_b64(u32x2{v, tag}, r, voff, 0, AUX_SC1);
}
__device__ __forceinline__ void st16_sc1(rsrc_t r, u32 voff, u32 v0, u32 v1, u32 tag) {
  __builtin_amdgcn_raw_buffer_store_b128(u32x4{v0, tag, v1, tag}, r, voff, 0, AUX_SC1);
}
__device__ __forceinline__ rsrc_t mk_rsrc(const void* p, u32 bytes) {
  return __builtin_amdgcn_make_buffer_rsrc(const_cast<void*>(p), 0, bytes, 0x00020000);
}
#define WAIT_VM(n) __builtin_amdgcn_s_waitcnt(((n) & 15) | (((n) >> 4) << 14) | (7 << 4) | (15 << 8))
#define SB() __builtin_amdgcn_sched_barrier(0)
#define CFENCE() asm volatile("" ::: "memory")
__device__ __forceinline__ bool all_ok(bool ok) { return __builtin_amdgcn_ballot_w64(!ok) == 0ull; }

template <int N> __device__ __forceinline__ void gather16(rsrc_t ws, u32 voff, u32 stride, u32 tag, u32x4 (&g)[N]) {
#pragma nounroll
  for (int spin = 0; spin < SPIN_MAX; ++spin) {
    CFENCE();
    bool ok = true;
#pragma unroll
    for (int i = 0; i < N; ++i) g[i] = ld16<AUX_SC1>(ws, voff + i * stride, 0);
#pragma unroll
    for (int i = 0; i < N; ++i) ok &= (g[i][1] == tag) & (g[i][3] == tag);
    if (all_ok(ok)) break;
    __builtin_amdgcn_s_sleep(8);
  }
}
template <int N> __device__ __forceinline__ void gather8(rsrc_t ws, u32 voff, u32 stride, u32 tag, u32 (&g)[N], bool act = true) {
#pragma nounroll
  for (int spin = 0; spin < SPIN_MAX; ++spin) {
    CFENCE();
    bool ok = true;
    u32x2 t[N];
#pragma unroll
    for (int i = 0; i < N; ++i) t[i] = ld8<AUX_SC1>(ws, voff + i * stride, 0);
#pragma unroll
    for (int i = 0; i < N; ++i) { ok &= (t[i][1] == tag); g[i] = t[i][0]; }
    if (all_ok(ok | !act)) break;
    __builtin_amdgcn_s_sleep(8);
  }
}

struct Smem {
  float red[8][128];
  float ml[8][2];
  float r2[64 * 20];
  u16 hb[3 * 1024];
  float stat[8][64];
  float red2[8][128];
  u32 dummy[64];
  u32x2 bc;
  u32x4 zero[4];
  u32 mflag[8];
  u32x4 wbuf[8][8][64];
  __attribute__((aligned(256))) u16 w2b[32][512];
  u16 actl[2][32];
};

#define PROBE_K 8
#define PROBE_OP 0
#define PROBE_PART -1
#define STAMP(k) do { if ((k) == PROBE_K && tid == 0) pst = wall_clock64(); } while (0)
#define WSTAMP(k) do {} while (0)
#define FINISH() do { STAMP(8); if (tid == 0) { __hip_atomic_store(reinterpret_cast<u64*>(ws + (1u << 20)) + bid * 2, pt0, __ATOMIC_RELAXED, __HIP_MEMORY_SCOPE_AGENT); \
   __hip_atomic_store(reinterpret_cast<u64*>(ws + (1u << 20)) + bid * 2 + 1, (PROBE_PART < 0 || part == PROBE_PART) ? pst : 0ull, __ATOMIC_RELAXED, __HIP_MEMORY_SCOPE_AGENT); } \
   if (bid == 8 && wave == 0) probe_end(ws, pt0, lane); return; } while (0)
#define PIN(x) asm volatile("" : "+v"(x))
#define LDS_BARRIER() do { __builtin_amdgcn_s_waitcnt(0xc07f); __builtin_amdgcn_s_barrier(); CFENCE(); } while (0)
#define WAVE_SYNC() do { __builtin_amdgcn_fence(__ATOMIC_RELEASE, "wavefront"); __builtin_amdgcn_wave_barrier(); __builtin_amdgcn_fence(__ATOMIC_ACQUIRE, "wavefront"); } while (0)

__device__ __forceinline__ void dma16(rsrc_t r, u32 voff, u32 soff, u32 lds_dst) {
  u32 keep;
  asm volatile("s_mov_b32 %0, m0\n\ts_mov_b32 m0, %4\n\ts_nop 4\n\tbuffer_load_dwordx4 %1, %2, %3 offen nt lds\n\ts_mov_b32 m0, %0"
               : "=&s"(keep) : "v"(voff), "s"(r), "s"(soff), "s"(lds_dst) : "memory");
}
__device__ __forceinline__ u32 perm_lo(u32 a, u32 b) { return __builtin_amdgcn_perm(b, a, 0x05040100u); }
__device__ __forceinline__ u32 perm_hi(u32 a, u32 b) { return __builtin_amdgcn_perm(b, a, 0x07060302u); }
__device__ __forceinline__ float rdl(float x, int l) { return __int_as_float(__builtin_amdgcn_readlane(__float_as_int(x), l)); }

typedef __attribute__((ext_vector_type(8))) __bf16 bf16x8;
__device__ __forceinline__ f32x4 mfma16(u32x4 a, u32x4 b, f32x4 c) {
  return __builtin_amdgcn_mfma_f32_16x16x32_bf16(__builtin_bit_cast(bf16x8, a), __builtin_bit_cast(bf16x8, b), c, 0, 0, 0);
}
__device__ __forceinline__ float sum8(const float* p) {
  const f32x4 a = *reinterpret_cast<const f32x4*>(p), b = *reinterpret_cast<const f32x4*>(p + 4);
  return ((a[0] + b[0]) + (a[1] + b[1])) + ((a[2] + b[2]) + (a[3] + b[3]));
}
__device__ __forceinline__ float ln_ri(float var) {
  return __builtin_amdgcn_rcpf(rbf(__builtin_amdgcn_sqrtf(rbf(rbf(fmaxf(var, 0.f)) + 1e-5f))));
}
__device__ __forceinline__ void ln_stats2(const float (&x0)[8], const float (&x1)[8], float& mb0, float& ri0, float& mb1, float& ri1) {
  const float k0 = rdl(x0[0], 0), k1 = rdl(x1[0], 0);
  float s0 = 0.f, s1 = 0.f, q0 = 0.f, q1 = 0.f;
#pragma unroll
  for (int k = 0; k < 8; ++k) {
    const float d0 = x0[k] - k0, d1 = x1[k] - k1;
    s0 += d0; s1 += d1; q0 = fmaf(d0, d0, q0); q1 = fmaf(d1, d1, q1);
  }
  float t = swap16_add(swap32_add(s0, s1), swap32_add(q0, q1));
  t += dpp<XOR1>(t); t += dpp<XOR2>(t); t += dpp<XOR7>(t); t += dpp<XOR8>(t);
  auto u = __builtin_amdgcn_permlane16_swap(__float_as_uint(t), __float_as_uint(t), false, false);
  const float ms = __uint_as_float(u[0]) * (1.f / 512.f), mq = __uint_as_float(u[1]) * (1.f / 512.f);
  const float ri = ln_ri(fmaf(-ms, ms, mq));
  mb0 = rbf(k0 + rdl(ms, 0)); mb1 = rbf(k1 + rdl(ms, 32));
  ri0 = rdl(ri, 0); ri1 = rdl(ri, 32);
}
template <int I> struct IC { static constexpr int v = I; };
template <int I, int N, class F> __device__ __forceinline__ void sfor(F&& f) {
  if constexpr (I < N) { if (f(IC<I>{})) sfor<I + 1, N>(f); }
}
typedef __attribute__((ext_vector_type(4))) short s16x4;
typedef __attribute__((address_space(3))) s16x4* lds_s16x4p;
__device__ __forceinline__ u32x2 tr_read(u32 lds_addr) {
  return __builtin_bit_cast(u32x2, __builtin_amdgcn_ds_read_tr16_b64_v4i16((lds_s16x4p)lds_addr));
}
__device__ __forceinline__ u32 next_tag(u32 e) { return ((e | 0x40004000u) + 1u) & ~0x40004000u; }
__device__ __forceinline__ float u2f(u32 v) { return __uint_as_float(v); }
#define LDSP(T, a) ((__attribute__((address_space(3))) T*)(a))
template <class T> __device__ __forceinline__ u32 lds_addr(T* p) { u32 a = (u32)(uintptr_t)p; PIN(a); return a; }

#define TAB_LOAD "s_add_u32 s30, s28, wg_tab@rel32@lo+4\ns_addc_u32 s31, s29, wg_tab@rel32@hi+12\nglobal_load_dwordx2 %[te], %[va], s[30:31]\n"
__device__ __forceinline__ void probe_end(unsigned char* ws, u64 pt0, int lane) {
  while (wall_clock64() < pt0 + 5500ull) __builtin_amdgcn_s_sleep(8);
  u64 a[4], c2[4];
  for (int i = 0; i < 4; ++i) {
    a[i] = __hip_atomic_load(reinterpret_cast<u64*>(ws + (1u << 20)) + (i * 64 + lane) * 2, __ATOMIC_RELAXED, __HIP_MEMORY_SCOPE_AGENT);
    c2[i] = __hip_atomic_load(reinterpret_cast<u64*>(ws + (1u << 20)) + (i * 64 + lane) * 2 + 1, __ATOMIC_RELAXED, __HIP_MEMORY_SCOPE_AGENT);
  }
  int mn0 = 1 << 30, mx1 = -(1 << 30), mn1 = 1 << 30, sum = 0, cnt = 0;
  for (int i = 0; i < 4; ++i) {
    const int d0 = (int)(a[i] - pt0);
    mn0 = min(mn0, d0);
    if (c2[i] != 0ull) { const int d1 = (int)(c2[i] - pt0); mx1 = max(mx1, d1); mn1 = min(mn1, d1); sum += d1; cnt += 1; }
  }
  for (int o = 1; o < 64; o <<= 1) { mn0 = min(mn0, __shfl_xor(mn0, o)); mx1 = max(mx1, __shfl_xor(mx1, o)); mn1 = min(mn1, __shfl_xor(mn1, o)); sum += __shfl_xor(sum, o); cnt += __shfl_xor(cnt, o); }
  int X = PROBE_OP == 0 ? mx1 : (PROBE_OP == 1 ? sum / max(cnt, 1) : mn1);
  if (PROBE_K == 102) X = mn0;
  X = mn0 + (X - mn0) * PROBE_MUL;
  const u64 tg = pt0 + (u64)(long long)(X + 8000);
  for (int i = 0; i < 400000 && wall_clock64() < tg; ++i) __builtin_amdgcn_s_sleep(1);
}
__global__ void __launch_bounds__(512, 2) fused_kernel(const u16* kc_, const u16* vc_, const u16* wo_, const u16* wq_, const u16* wmod_, const u16* w1_, const u16* w3_,
                                                       unsigned char* ws_, const u16* x_, const u16* cnd_, const u16* w2_, u16* out_) {
  __shared__ __attribute__((aligned(256))) Smem sm;
  const u64 a_ka = (u64)__builtin_amdgcn_kernarg_segment_ptr();
  const int bid = blockIdx.x, tid = threadIdx.x;
  u64 wa, t64; u32x2 te; int tid0;
  u32 ar[14];
  u32 tch2, sa_, sb_, va_;
  u32x4 kr[NL], vr[NL];
  asm volatile(
    "s_load_dwordx16 s[4:19], %[ka], 0x0\ns_load_dwordx8 s[20:27], %[ka], 0x40\n"
    "s_memrealtime %[t]\n"
    "v_lshlrev_b32 %[va], 3, %[bid]\n"
    "s_getpc_b64 s[28:29]\n.Lcpf_first:\n"
    TAB_LOAD
    "s_cmp_gt_u32 %[bid], 15\ns_cbranch_scc1 .Lno_cpf\n"
    "s_mov_b32 %[sa], .Lend_a-.Lcpf_first\ns_max_i32 %[sa], %[sa], .Lend_b-.Lcpf_first\ns_max_i32 %[sa], %[sa], .Lend_c-.Lcpf_first\ns_max_i32 %[sa], %[sa], .Lend_d-.Lcpf_first\ns_max_i32 %[sa], %[sa], .Lend_e-.Lcpf_first\n"
    "v_lshlrev_b32 %[va], 6, %[tid]\nv_min_u32 %[va], %[sa], %[va]\nglobal_load_dword %[pf], %[va], s[28:29]\n"
    ".Lno_cpf:\n"
    "s_and_b32 %[sa], %[bid], 15\ns_lshr_b32 %[sb], %[bid], 6\ns_lshl2_add_u32 %[sa], %[sa], %[sb]\ns_lshl_b32 %[sa], %[sa], 22\ns_xor_b32 %[sa], %[sa], 0xff0000\n"
    "s_and_b32 %[sb], %[bid], 48\nv_lshl_add_u32 %[va], %[sb], 5, %[tid]\nv_lshl_add_u32 %[va], %[va], 4, %[sa]\n"
    "v_readfirstlane_b32 %[t0], %[tid]\n"
    "s_waitcnt lgkmcnt(0)\nglobal_load_dwordx4 %[k0], %[va], s[4:5] nt\nglobal_load_dwordx4 %[v0], %[va], s[6:7] nt\n"
    "s_bitcmp1_b32 %[t0], 6\ns_cselect_b64 s[14:15], s[16:17], s[14:15]\n"
    "s_bitcmp1_b32 %[bid], 4\ns_cselect_b64 s[8:9], s[10:11], s[8:9]\ns_cselect_b64 s[12:13], s[14:15], s[12:13]\ns_bitcmp1_b32 %[bid], 5\ns_cselect_b64 %[wa], s[12:13], s[8:9]"
    : "=&{s4}"(ar[0]), "=&{s5}"(ar[1]), "=&{s6}"(ar[2]), "=&{s7}"(ar[3]), "=&{s18}"(ar[4]), "=&{s19}"(ar[5]), "=&{s20}"(ar[6]), "=&{s21}"(ar[7]), "=&{s22}"(ar[8]), "=&{s23}"(ar[9]),
      "=&{s24}"(ar[10]), "=&{s25}"(ar[11]), "=&{s26}"(ar[12]), "=&{s27}"(ar[13]), [wa] "=&s"(wa), [t0] "=&s"(tid0), [t] "=&s"(t64), [te] "=&v"(te), [sa] "=&s"(sa_), [sb] "=&s"(sb_),
      [va] "=&v"(va_), [pf] "=&v"(tch2), [k0] "=&v"(kr[0]), [v0] "=&v"(vr[0])
    : [ka] "s"(a_ka), [bid] "s"(bid), [tid] "v"(tid) : "s8", "s9", "s10", "s11", "s12", "s13", "s14", "s15", "s16", "s17", "s28", "s29", "s30", "s31", "scc", "memory");
  asm volatile("" : "+s"(ar[0]), "+s"(ar[1]), "+s"(ar[2]), "+s"(ar[3]), "+s"(ar[4]), "+s"(ar[5]), "+s"(ar[6]), "+s"(ar[7]), "+s"(ar[8]), "+s"(ar[9]), "+s"(ar[10]), "+s"(ar[11]), "+s"(ar[12]), "+s"(ar[13]));
  asm volatile("" : "+s"(wa), "+s"(t64), "+s"(tid0));
  const u32 tka = (u32)wall_clock64();
  const u64 pt0 = t64; u64 pst = (PROBE_K == 100 || PROBE_K == 102) ? pt0 : (PROBE_K == 101 ? pt0 + (u32)(tka - (u32)t64) : 0);
  const u32 t0 = tka;
  __builtin_assume(tid >= 0 && tid < 512 && tid0 >= 0 && tid0 < 512 && bid >= 0 && bid < 256);
  constexpr Prm P = PRM0;
  const int TEND = P.TEND;
  const int xcd = bid & 7, part = bid >> 4 & 3;
  const int lane = tid & 63, wave = tid0 >> 6;
  SB();
  const bool p3 = part == 3;
  u32 pitch = (u32)(0x005600c001000020ull >> (part * 16) & 0xffff) * 32u;
  const int sh = p3 ? 2 : 3;
  u32 wv0 = (u32)((p3 ? (wave >> 1) * 128 : wave * 64) + (part == 0 ? xcd * 512 : 0) + (lane >> sh)) * pitch + ((lane & (p3 ? 3 : 7)) ^ (lane >> (p3 ? 5 : 3))) * 16u;
  const u32 wimg = (u32)(uintptr_t)(&sm.wbuf[0][0][0]);
  u32 wsz = (part == 0 ? 4096u : 512u) * pitch, wld = wimg + (p3 ? (wave & 1) * 32768 + (wave >> 1) * 8192 : wave * 8192);
  PIN(wv0);
  asm volatile("" : "+s"(wsz), "+s"(wld), "+s"(pitch));
  auto karg = [&](int i) { return ar[2 * i] | (u64)ar[2 * i + 1] << 32; };
  const u64 a_kc = karg(0), a_vc = karg(1);
  unsigned char* ws = (unsigned char*)karg(2);
  const u16 *x = (const u16*)karg(3), *cnd = (const u16*)karg(4), *w2 = (const u16*)karg(5);
  u16* out = (u16*)karg(6);
  const rsrc_t wsr = mk_rsrc(ws, WS_END);
  asm volatile("s_waitcnt vmcnt(2)" : "+v"(te));
  const u32 te0 = __builtin_amdgcn_readfirstlane(te[0]), te1 = __builtin_amdgcn_readfirstlane(te[1]);
  const int bl0 = te0 & 127, nl = te0 >> 7 & 63, blk = te0 >> 13 & 63;
  const bool has_w = te0 >> 19 & 1;
  const rsrc_t rw = mk_rsrc((const void*)wa, wsz);
  const u32 wstep = pitch << (6 - sh), wflip = p3 ? 32u : 0u;
  u32 wso = blk << (sh + 4);
  int wleft = 8;
  auto piece = [&]() {
    dma16(rw, wv0, wso, wld);
    wso += wstep; wld += 1024;
    wv0 ^= (wleft & 1) ? wflip : 0u;
    --wleft;
  };
  SB();
  STAMP(23);
  const int b = bid >> 3 & 1, head = xcd * 4 + (bid >> 6 ^ 3);
  const int g = lane >> 4, c = lane & 15;
  const int i8 = b | bid >> 6 << 1, wi = i8 * 8 + xcd;
  const bool is_up = p3 && has_w, modc = part == 2 && wi < 16;
  const size_t base = ((size_t)(b * NH + head) * SK + (SK - WIN)) * DH;
  const rsrc_t rk = mk_rsrc((const u16*)a_kc + base, WIN * DH * 2), rv = mk_rsrc((const u16*)a_vc + base, WIN * DH * 2);
  int tau = te1 & 0xffff;
  const int tauw = te1 >> 16;
  int due = P.NF * tau >> 2, now = 0;
  auto gate = [&]() {
    while (now < due) { now = (int)(((u32)wall_clock64() - t0) << 8); if (now < due) __builtin_amdgcn_s_sleep(1); }
    due += tau;
  };
  auto until = [&](int t) { while ((int)(((u32)wall_clock64() - t0) << 8) < t); };
  const u32 pslot = G_P + ((b * 32 + head) * 4 + part) * 256;
  u32x2 ep; u32 xs0, xs1, tch1; u32x4 xr0, xr1;
  const rsrc_t rx = mk_rsrc(part == 2 ? cnd : x, (part == 1 || (part == 2 && has_w)) ? 2048u : 0u);
  auto small_ops = [&]() {
    ep = ld8<AUX_SC1>(wsr, wave == 7 ? WS_EPOCH : OOB, 0);
    xs0 = __builtin_amdgcn_raw_buffer_load_b16(rx, tid * 2, 0, 0); xs1 = __builtin_amdgcn_raw_buffer_load_b16(rx, tid * 2, 1024, 0);
    xr0 = ld16<0>(rx, part == 1 ? lane * 16 : OOB, 0); xr1 = ld16<0>(rx, part == 1 ? lane * 16 : OOB, 1024);
    tch1 = __builtin_amdgcn_raw_buffer_load_b32(mk_rsrc(w2 + (size_t)blk * 32 * 512, is_up ? 64u : 0u), tid == 384 ? 0 : OOB, 0, AUX_NT);
  };
  if (has_w) {
    if (modc) now = 1 << 30;
#pragma nounroll
    while (wleft > (int)(te0 >> 20)) { gate(); due += tauw - tau; piece(); }
    now = 0;
  }
  SB();
  u32 sot;
  {
    const u32 s1 = bl0 + lane - 1, s0 = s1 + (s1 >= 108 ? 4 : 0);
    u32 sb = s0 & 96u;
#pragma unroll
    for (int k = 0; k < 5; ++k) sb |= (s0 >> k & 1u) << (P.PM >> (4 * k) & 7);
    const u32 src = (sb >> 5 & 3u) | (u32)(b * 32 + head) << 2;
    sb ^= ((__builtin_popcount(src & P.M0) & 1) | (__builtin_popcount(src & P.M1) & 1) << 1 | (__builtin_popcount(src & P.M2) & 1) << 2 | (__builtin_popcount(src & P.M3) & 1) << 3 | (__builtin_popcount(src & P.M4) & 1) << 4) & (s1 >= 96 ? 7u : 31u);
    sot = (sb & 7u) << 10 | (u32)wave << 13 | (sb >> 3) << 16;
    sot = s1 >= 120 ? (s1 + 4) << 13 | (u32)wave << 10 : sot;
    sot = lane < nl ? sot : OOB;
    PIN(sot);
  }
  u32 tag = 0;
  u32x4 bf[8];
  auto wo_prep = [&](int s0 = 0, int s1 = 2) {
    const u32 wi_ = wimg + wave * 8192;
    const int q = (lane >> 2) & 3, pp = lane & 3;
#pragma unroll
    for (int s = s0; s < s1; ++s) {
#pragma unroll
      for (int t = 0; t < 4; ++t) {
        const int r0 = s * 32 + g * 8 + q, r1 = r0 + 4, ch = t * 2 + (pp >> 1);
        const u32x2 lo = tr_read(wi_ + r0 * 128 + ((ch ^ (r0 & 7)) * 16) + (pp & 1) * 8), hi = tr_read(wi_ + r1 * 128 + ((ch ^ (r1 & 7)) * 16) + (pp & 1) * 8);
        bf[s * 4 + t] = u32x4{lo[0], lo[1], hi[0], hi[1]};
      }
    }
  };
  auto gemv_go = [&](u32 hh) {
    sm.hb[tid] = (u16)hh; sm.hb[512 + tid] = (u16)(hh >> 16);
    WAVE_SYNC();
    const u32x4* a = (c & 11) == 0 ? reinterpret_cast<const u32x4*>(&sm.hb[(c >> 2) * 512 + wave * 64 + g * 8]) : &sm.zero[0];
    const u32x4 a0 = a[0], a1 = a[(c & 11) == 0 ? 4 : 0];
    const f32x4 z = {0.f, 0.f, 0.f, 0.f};
    f32x4 d[4];
#pragma unroll
    for (int t = 0; t < 4; ++t) d[t] = mfma16(a0, bf[t], z);
#pragma unroll
    for (int t = 0; t < 4; ++t) d[t] = mfma16(a1, bf[4 + t], d[t]);
    if (lane < 32) {
#pragma unroll
      for (int t = 0; t < 4; ++t) sm.r2[(t * 16 + c) * 20 + g * 8 + wave] = d[t][0];
    }
  };
  auto mod_stage = [&]() {
    wo_prep();
    gemv_go(pack_bf(silu_f(lo_f(xs0)), silu_f(lo_f(xs1))));
    __builtin_amdgcn_s_waitcnt(0xc07f);
    if (lane == 0) *LDSP(volatile u32, (u32)(uintptr_t)&sm.mflag[wave]) = 1u;
    if (wave == 7) {
#pragma nounroll
      for (int spin = 0; spin < SPIN_MAX; ++spin) {
        const u32 f = *LDSP(volatile u32, (u32)(uintptr_t)&sm.mflag[lane & 7]);
        if (all_ok(f != 0u)) break;
        __builtin_amdgcn_s_sleep(1);
      }
      CFENCE();
      st8_sc1(wsr, G_MOD + (blk * 64 + lane) * 8, pack_bf(sum8(&sm.r2[lane * 20]), sum8(&sm.r2[lane * 20 + 8])), tag);
    }
    STAMP(9);
  };
  if (lane < 4) sm.zero[lane] = u32x4{0u, 0u, 0u, 0u};
  if (part == 2 && has_w) {
    if (tid < 8) sm.mflag[tid] = 0u;
    LDS_BARRIER();
    if (__builtin_expect(modc, 0)) {
      small_ops();
      __builtin_amdgcn_raw_buffer_store_b128(u32x4{SENT, SENT, SENT, SENT}, wsr, tid < 320 ? G_P + wi * 5120 + tid * 16 : OOB, 0, AUX_SC1);
      asm volatile("s_waitcnt vmcnt(0)" ::: "memory");
      tag = next_tag(__builtin_amdgcn_readfirstlane(ep[0]));
      mod_stage();
      __builtin_amdgcn_s_barrier();
    }
  }
  SB();
  const int fd = wi * 8 + (lane & 7);
  const int ub = wave >> 2, ud = (wave & 3) * 128 + c * 8 + g * 2;
  u32x4 sg[3]; u32 sx2;
  auto side_loads = [&]() {
    const bool fin = part == 1 && wave == 0;
    sg[0] = ld16<AUX_SC1>(wsr, is_up ? G_MOD + (1024 + ud) * 8 : (fin ? G_MOD + (1024 + fd) * 8 : OOB), 0);
    sg[1] = ld16<AUX_SC1>(wsr, is_up ? G_MOD + (1536 + ud) * 8 : (fin ? G_MOD + (2560 + fd) * 8 : OOB), 0);
    sg[2] = ld16<AUX_SC1>(wsr, is_up ? G_MOD + (2048 + ud) * 8 : OOB, 0);
    sx2 = __builtin_amdgcn_raw_buffer_load_b32(mk_rsrc(x, (is_up || fin) ? 2048u : 0u), is_up ? ub * 1024 + ud * 2 : (g & 1) * 1024 + (fd >> 1) * 4, 0, 0);
  };
  auto side_pin = [&]() { PIN(sg[0]); PIN(sg[1]); PIN(sg[2]); PIN(sx2); };
  const u32 mlo = G_ML + (head * 2 + b) * 64 + (lane & 3) * 16;
  float fmrg = 0.f;
  bool fok = wave != 0;
  auto merge_factor = [&](const u32x4& ml) {
    const float mi = u2f(ml[0]);
    float M = fmaxf(mi, dpp<XOR1>(mi)); M = fmaxf(M, dpp<XOR2>(M));
    const float e = __expf(mi - M);
    float L = e * u2f(ml[2]);
    L += dpp<XOR1>(L); L += dpp<XOR2>(L);
    fmrg = rdl(e * __builtin_amdgcn_rcpf(L), part);
  };
  f32x2 ga = {0.f, 0.f}, xin = {0.f, 0.f}, cc1 = {0.f, 0.f};
  float Af = 0.f, Bf = 0.f;
  const int hpos = ub * 512 + ud;
  auto up_prep = [&]() {
    if (!all_ok((sg[0][1] == tag) & (sg[0][3] == tag) & (sg[1][1] == tag) & (sg[1][3] == tag) & (sg[2][1] == tag) & (sg[2][3] == tag))) gather16<3>(wsr, G_MOD + (1024 + ud) * 8, 512 * 8, tag, sg);
    ga = f32x2{ub ? hi_f(sg[0][0]) : lo_f(sg[0][0]), ub ? hi_f(sg[0][2]) : lo_f(sg[0][2])};
    cc1 = f32x2{rbf(1.f + (ub ? hi_f(sg[2][0]) : lo_f(sg[2][0]))), rbf(1.f + (ub ? hi_f(sg[2][2]) : lo_f(sg[2][2])))};
    xin = f32x2{lo_f(sx2), hi_f(sx2)};
    u32* hp = reinterpret_cast<u32*>(&sm.hb[hpos]);
    hp[512] = pack_bf(cc1[0], cc1[1]);
    hp[1024] = ub ? perm_hi(sg[1][0], sg[1][2]) : perm_lo(sg[1][0], sg[1][2]);
  };
  auto up_frags = [&]() {
    const int pp = lane & 3;
    const u32 a = wimg + (pp & 1) * 32768 + ((pp >> 1) * 32 + g * 8 + ((lane >> 2) & 3)) * 64 + (((wave >> 1) ^ ((g & 1) | (pp & 2))) * 16) + (wave & 1) * 8;
#pragma unroll
    for (int rb = 0; rb < 8; ++rb) {
      const u32x2 lo = tr_read(a + rb * 4096), hi = tr_read(a + rb * 4096 + 256);
      bf[rb] = u32x4{lo[0], lo[1], hi[0], hi[1]};
    }
  };
  const u32x4* ap = reinterpret_cast<const u32x4*>(&sm.hb[((lane >> 3) & 1) * 512 + (lane & 1) * 32 + g * 8]);
  const bool par = lane & 8;
  float l0 = 0.f, l1 = 0.f;
  u32x2 gmd[2], tbg;
  int t1 = 0;
#pragma unroll
  for (int i = NCMIN / 8; i < NL; ++i) kr[i] = u32x4{0u, 0u, 0u, 0u};
  sfor<0, NL>([&](auto ic) {
    constexpr int i = ic.v;
    if (i >= NCMIN / 8 && i >= nl) return false;
    if constexpr (i == 0) return true;
    const u32 so = __builtin_amdgcn_readlane(sot, i);
    gate();
    SB();
    kr[i] = ld16<AUX_NT>(rk, lane * 16, so);
    if constexpr (i == SMO) small_ops();
    if constexpr (i == TBL) tbg = ld8<AUX_SC1>(wsr, wave == 7 ? G_TB + xcd * 128 + (lane & 7) * 8 : OOB, 0);
    if constexpr (i == QP) { gmd[0] = ld8<AUX_SC1>(wsr, part == 1 ? G_MOD + tid * 8 : OOB, 0); gmd[1] = ld8<AUX_SC1>(wsr, part == 1 ? G_MOD + tid * 8 : OOB, 4096); }
    SB();
    SB();
    if (i >= WK) { WAIT_VM(WK); SB(); }
    if (i == 1) STAMP(25);
    if (i == 8) STAMP(12);
    if (i == 20) STAMP(21);
    if (i == 25) STAMP(22);
    if (i == TBP) {
      if (part == 3 && i8 == 0 && wave == 7) { tag = next_tag(__builtin_amdgcn_readfirstlane(ep[0])); if (lane < 8) st8_sc1(wsr, G_TB + lane * 128 + xcd * 8, t0 + (((tka - (u32)t64) * P.GA) >> 3) - P.GS, tag); }
      SB();
    }
    if (i == TBU) {
      if (wave == 7) {
        const bool ok = tbg[1] == tag;
        float lf = ok ? (float)min(max((int)(t0 - tbg[0]), -300), 300) : 0.f, nf = ok ? 1.f : 0.f;
        lf += dpp<XOR1>(lf); nf += dpp<XOR1>(nf); lf += dpp<XOR2>(lf); nf += dpp<XOR2>(nf); lf += dpp<XOR7>(lf); nf += dpp<XOR7>(nf);
        if (lane == 0) sm.bc = u32x2{tag, t0 - (u32)(int)(lf * __builtin_amdgcn_rcpf(fmaxf(nf, 1.f)))};
      }
      LDS_BARRIER();
      const u32x2 bc = sm.bc;
      tag = __builtin_amdgcn_readfirstlane(bc[0]);
      t1 = (__builtin_amdgcn_readfirstlane((int)(bc[1] - t0)) + TEND + P.X1) * 256;
      tau -= __builtin_amdgcn_readfirstlane((int)((float)(int)(t0 - bc[1]) * 256.f * __builtin_amdgcn_rcpf((float)(2 * nl - TBU - 3))));
      SB();
    }
    if (i == LNS) {
      tag = next_tag(__builtin_amdgcn_readfirstlane(ep[0]));
      if (part == 1) {
        float x0[8], x1[8], mb0, ri0, mb1, ri1;
#pragma unroll
        for (int k = 0; k < 4; ++k) { x0[2 * k] = lo_f(xr0[k]); x0[2 * k + 1] = hi_f(xr0[k]); x1[2 * k] = lo_f(xr1[k]); x1[2 * k + 1] = hi_f(xr1[k]); }
        ln_stats2(x0, x1, mb0, ri0, mb1, ri1);
        l0 = rbf(rbf(lo_f(xs0) - mb0) * ri0); l1 = rbf(rbf(lo_f(xs1) - mb1) * ri1);
      }
      PIN(l0); PIN(l1);
      SB();
    }
    if (i == WUSE) {
      PIN(tch1); PIN(tch2);
      if ((part == 1 || (part == 2 && has_w && !modc))) {
        WAIT_VM(WK); CFENCE();
        if (part == 2) mod_stage(); else wo_prep();
      }
#pragma unroll
      for (int k = 0; k < 8; ++k) PIN(bf[k]);
      SB();
    }
    if (i == QP + QD) {
      if (part == 1) {
        u32 gm[2] = {gmd[0][0], gmd[1][0]};
        if (!all_ok((gmd[0][1] == tag) & (gmd[1][1] == tag))) { STAMP(11); gather8<2>(wsr, G_MOD + tid * 8, 512 * 8, tag, gm); }
        STAMP(5);
        gemv_go(pack_bf(rbf(l0 * rbf(1.f + lo_f(gm[1]))) + lo_f(gm[0]), rbf(l1 * rbf(1.f + hi_f(gm[1]))) + hi_f(gm[0])));
      }
      SB();
    }
    if (i == QP + QD + 1) {
      if (part == 1) {
        LDS_BARRIER();
        if (wave == 0) {
          const int bb = lane >> 5, jq = lane & 31;
          st8_sc1(wsr, G_Q + (bb * 2048 + blk * 32 + jq) * 8, pack_bf(sum8(&sm.r2[2 * jq * 20 + bb * 8]), sum8(&sm.r2[(2 * jq + 1) * 20 + bb * 8])), tag);
        }
        STAMP(6);
      }
      SB();
    }
    return true;
  });
  PIN(sot);
  const u32 qoff = G_Q + ((b * 32 + head) * 64 + c * 4) * 8;
  u32x4 qg[2], fl2[3];
  u32 qq[4];
  constexpr int NB = (NL + 15) / 16;
  float xlo, sc[NB], pr[NB], m = 0.f;
  f32x4 acc = {0.f, 0.f, 0.f, 0.f};
  u32 pb[NB], pm[4], cpf;
  const u32 adst0 = lds_addr(lane < 32 ? &sm.red[wave][c * 8 + g * 4] : &sm.r2[(lane - 32) * 4]);
  u32 po, rdst, rsrc, xo;
  auto wo_addr = [&]() {
    po = G_P + (((c >> 2 & 1) * 32 + xcd * 4 + (wave >> 1)) * 4 + (c & 3)) * 256 + (wave & 1) * 128 + (c >> 3) * 64 + g * 16;
    rdst = lane < 32 ? (u32)(uintptr_t)&sm.red2[wave][0] + lane * 16 : (u32)(uintptr_t)&sm.r2[0] + (lane - 32) * 16;
    rsrc = (u32)(uintptr_t)&sm.red2[0][((wave & 1) * 16 + (lane & 15)) * 4 + (lane >> 4)];
    xo = (lane & 1) ? OOB : G_XP + xcd * 2048 + (wave & 1) * 1024 + (blk * 64 + lane) * 2;
    PIN(po); PIN(rdst); PIN(rsrc); PIN(xo);
  };
  u32 pso = wave == 0 ? pslot + lane * 4 : OOB;
  PIN(pso);
  const u32 nadd = (te0 >> 20) ? P.LW : (is_up ? P.LU : 0);
  u32 nto = tid == 0 ? G_NT + bid * 8 : OOB;
  PIN(nto);
  auto notices = [&](auto nc_, u32 off, u32x4 (&gn)[nc_.v]) -> int {
    constexpr int N = nc_.v;
#pragma nounroll
    for (int spin = 0; spin < SPIN_MAX; ++spin) {
      bool ok = true;
#pragma unroll
      for (int i = 0; i < N; ++i) ok &= (gn[i][1] == tag) & (gn[i][3] == tag);
      if (all_ok(ok)) break;
      __builtin_amdgcn_s_sleep(10);
      CFENCE();
#pragma unroll
      for (int i = 0; i < N; ++i) gn[i] = ld16<AUX_SC1>(wsr, off, i * 1024);
    }
    int mx = 0;
#pragma unroll
    for (int i = 0; i < N; ++i) mx = max(mx, max((int)(gn[i][0] - t0), (int)(gn[i][2] - t0)));
    return __builtin_amdgcn_readfirstlane((int)wave_max((float)min(mx, 1 << 20)));
  };
  const int side = nl >= SIDE + 13 ? SIDE + 8 : (nl >= SIDE + 9 ? SIDE + 4 : SIDE);
  sfor<0, NL + WV>([&](auto ic) {
    constexpr int v = ic.v;
    if (cons_end(v - 1) >= NCMIN / 8 && cons_end(v - 1) >= nl) return false;
    if constexpr (v > 0 && v < NL) {
      const u32 so = __builtin_amdgcn_readlane(sot, v);
      if constexpr (v >= 12) { if (v == nl - P.NE) tau = (tau * P.FE) >> 4; }
      if constexpr (v >= NCMIN / 8) { if (v < nl) gate(); } else gate();
      SB();
      vr[v] = ld16<AUX_NT>(rv, lane * 16, so);
      SB();
      SB();
    }
    if constexpr (v == NCMIN / 8 - 1) {
#pragma unroll
      for (int i = NCMIN / 8; i < NL; ++i) asm volatile("" : "=v"(vr[i]));
      asm volatile("" : "=v"(fl2[1]), "=v"(fl2[2]));
    }
    if constexpr (v == VQ - 4) { qg[0] = ld16<AUX_SC1>(wsr, qoff, 0); qg[1] = ld16<AUX_SC1>(wsr, qoff + 16, 0); }
    if constexpr (v == VSIDE) side_loads();
    if constexpr (v == CPF) {
      u64 pc; u32 dlt, len;
      asm volatile("s_getpc_b64 %0\n.Lcpf_here:\n\ts_mov_b32 %1, .Ltail_start-.Lcpf_here\n\ts_mov_b32 %2, .Lend_a-.Ltail_start\n\ts_max_i32 %2, %2, .Lend_b-.Ltail_start\n\ts_max_i32 %2, %2, .Lend_c-.Ltail_start\n\ts_max_i32 %2, %2, .Lend_d-.Ltail_start\n\ts_max_i32 %2, %2, .Lend_e-.Ltail_start" : "=s"(pc), "=s"(dlt), "=s"(len) :: "scc");
      pc += dlt;
      const rsrc_t rc = mk_rsrc((const void*)pc, (part == 3 && i8 == 1) ? len : 0u);
      cpf = __builtin_amdgcn_raw_buffer_load_b32(rc, tid * 64, 0, 0);
    }
    if constexpr (v == CPF + WV + 1) PIN(cpf);
    if constexpr (v == SIDE || v == SIDE + 4 || v == SIDE + 8) fl2[(v - SIDE) / 4] = ld16<AUX_SC1>(wsr, (wave == 0 && v == side) ? mlo : OOB, 0);
    SB();
    if constexpr (v < WV) { WAIT_VM(WK); SB(); } else if constexpr (v < NL && cons_end(v) < v - WV + 1) { WAIT_VM(WV); SB(); }
    if constexpr (v == VQ) {
      if (!all_ok((qg[0][1] == tag) & (qg[0][3] == tag) & (qg[1][1] == tag) & (qg[1][3] == tag))) { STAMP(20); gather16<2>(wsr, qoff, 16, tag, qg); }
      STAMP(1); WSTAMP(1);
      qq[0] = qg[0][0]; qq[1] = qg[0][2]; qq[2] = qg[1][0]; qq[3] = qg[1][2];
    }
    if constexpr (v >= VQ && v < VS) {
      constexpr int sl = v - VQ, bt = sl >> 1;
      float xs[8];
#pragma unroll
      for (int i = 0; i < 8; ++i) {
        float d = 0.f;
        if (sl * 8 + i < NL) {
#pragma unroll
          for (int k = 0; k < 4; ++k) d = dot2(kr[sl * 8 + i][k], qq[k], d);
        }
        xs[i] = d;
      }
#pragma unroll
      for (int h = 0; h < 4; ++h) xs[h] = sel_add<XOR7>(xs[h], xs[h + 4], c & 4);
#pragma unroll
      for (int h = 0; h < 2; ++h) xs[h] = sel_add<XOR2>(xs[h], xs[h + 2], c & 2);
      xs[0] = sel_add<XOR1>(xs[0], xs[1], c & 1);
      if constexpr ((sl & 1) || sl == NSL - 1) {
        const float t = (sl & 1) ? sel_add<XOR8>(xlo, xs[0], c & 8) : xs[0] + dpp<XOR8>(xs[0]);
        const float s = rbf(rbf(t) * 0.08838834764831845f);
        sc[bt] = (bt * 16 + c < nl) ? s : -__builtin_inff();
        PIN(sc[bt]);
      } else {
        xlo = xs[0];
        PIN(xlo);
      }
    }
    if constexpr (v == VS) {
      float mloc = sc[0];
#pragma unroll
      for (int bt = 1; bt < NB; ++bt) mloc = fmaxf(mloc, sc[bt]);
      m = fmaxf(wave_max(mloc), -3.0e38f);
      float ls = 0.f;
#pragma unroll
      for (int bt = 0; bt < NB; ++bt) { pr[bt] = __expf(sc[bt] - m); ls += pr[bt]; }
      const float l = wave_sum(ls);
      if (lane == 0) { sm.ml[wave][0] = m; sm.ml[wave][1] = l; }
      STAMP(2); WSTAMP(2);
    }
    sfor<cons_end(v - 1), cons_end(v)>([&](auto jc) {
      constexpr int i = jc.v;
      if (i < NCMIN / 8 || i < nl) {
        const u32 pv = __float_as_uint(bcast16(u2f(pb[i >> 4]), i & 15));
        acc = mfma16(u32x4{pv & pm[0], pv & pm[1], pv & pm[2], pv & pm[3]}, vr[i], acc);
      }
      PIN(acc);
      return true;
    });
    if constexpr (v == MLS - 1) { side_pin(); if (is_up) up_prep(); PIN(ga); PIN(xin); PIN(cc1); }
    if constexpr (v == UPREP - 1) wo_addr();
    if constexpr (v == UPREP) {
#pragma unroll
      for (int k = 0; k < 8; ++k) asm volatile("" : "=v"(bf[k]));
      if (is_up) up_frags();
#pragma unroll
      for (int k = 0; k < 8; ++k) PIN(bf[k]);
    }
    if constexpr (v == UPREP + 1) {
      if (is_up) {
        const u32x4* a1 = ap + 128 + ((lane >> 1) & 1) * 128;
        f32x4 d0 = {0.f, 0.f, 0.f, 0.f}, d1 = {0.f, 0.f, 0.f, 0.f};
#pragma unroll
        for (int rb = 0; rb < 4; ++rb) { d0 = mfma16(a1[rb * 8], bf[rb], d0); d1 = mfma16(a1[(rb + 4) * 8], bf[rb + 4], d1); }
        Af = par ? d0[1] + d1[1] : d0[0] + d1[0]; Bf = par ? d0[3] + d1[3] : d0[2] + d1[2];
        Af += dpp<XOR8>(Af); Bf += dpp<XOR8>(Bf);
      }
      PIN(Af); PIN(Bf);
    }
    if constexpr (v == SIDE + WV + 1 || v == SIDE + WV + 5 || v == SIDE + WV + 9) {
      if (wave == 0 && v == side + WV + 1) {
        const u32x4 ml = fl2[(v - SIDE - WV - 1) / 4];
        fok = all_ok((ml[1] == tag) & (ml[3] == tag));
        merge_factor(ml);
      }
    }
    if (v % 6 == 4 && v >= 10 && v < 30) STAMP(15 + v / 6);
    if constexpr (v == MLS) {
      LDS_BARRIER();
      float mm[8], ll[8];
#pragma unroll
      for (int w = 0; w < 8; ++w) { const f32x2 t = *reinterpret_cast<const f32x2*>(&sm.ml[w][0]); mm[w] = t[0]; ll[w] = t[1]; }
      float Mwg = mm[0], Lwg = 0.f;
#pragma unroll
      for (int w = 1; w < 8; ++w) Mwg = fmaxf(Mwg, mm[w]);
      if (wave == 0) {
#pragma unroll
        for (int w = 0; w < 8; ++w) Lwg = fmaf(__expf(mm[w] - Mwg), ll[w], Lwg);
        if (lane == 0) st16_sc1(wsr, G_ML + ((head * 2 + b) * 4 + part) * 16, __float_as_uint(Mwg), __float_as_uint(Lwg), tag);
      }
      const float fw = __expf(m - Mwg);
#pragma unroll
      for (int bt = 0; bt < NB; ++bt) { pb[bt] = pack_bf(pr[bt] * fw, pr[bt] * fw); PIN(pb[bt]); }
#pragma unroll
      for (int k = 0; k < 4; ++k) { pm[k] = (c >> 1) == k ? ((c & 1) ? 0xffff0000u : 0x0000ffffu) : 0u; PIN(pm[k]); }
    }
    SB();
    return true;
  });
  asm volatile(".Ltail_start:");
  *LDSP(f32x4, adst0) = acc;
  STAMP(14); WSTAMP(14);
  if (!fok) {
    u32x4 ml;
#pragma nounroll
    for (int spin = 0; spin < SPIN_MAX; ++spin) {
      CFENCE();
      ml = ld16<AUX_SC1>(wsr, mlo, 0);
      if (all_ok((ml[1] == tag) & (ml[3] == tag))) break;
    }
    merge_factor(ml);
  }
  STAMP(15);
  LDS_BARRIER();
  STAMP(3);
  {
    u32 pw = 0;
    if (wave == 0) {
      f32x2 t[8];
#pragma unroll
      for (int w = 0; w < 8; ++w) t[w] = *reinterpret_cast<const f32x2*>(&sm.red[w][2 * lane]);
      const f32x2 o = ((t[0] + t[1]) + (t[2] + t[3])) + ((t[4] + t[5]) + (t[6] + t[7]));
      pw = min(pack_bf(o[0] * fmrg, o[1] * fmrg), SENT - 1u);
    }
    __builtin_amdgcn_raw_buffer_store_b32(pw, wsr, pso, 0, AUX_SC1);
    st8_sc1(wsr, nto, (u32)wall_clock64() + nadd, tag);
    SB();
#pragma unroll
    for (int i = NCMIN / 8; i < NL; ++i) PIN(vr[i]);
    PIN(fl2[0]); PIN(fl2[1]); PIN(fl2[2]);
  }
  STAMP(4);

  if (part == 2) FINISH();
  if (part == 0) {
    __builtin_amdgcn_s_barrier();
#pragma nounroll
    while (wleft > 0) piece();
    until(t1);
    STAMP(11);
    CFENCE();
    u32x4 og[2];
    og[0] = ld16<AUX_SC1>(wsr, po, 0);
    SB();
    WAIT_VM(1); CFENCE();
    wo_prep(0, 2);
#pragma unroll
    for (int k = 0; k < 8; ++k) PIN(bf[k]);
    SB();
    f32x4 d[4];
    auto wo_mm = [&]() {
      const f32x4 z = {0.f, 0.f, 0.f, 0.f};
#pragma unroll
      for (int t = 0; t < 4; ++t) { d[t] = mfma16(og[0], bf[t], z); og[1][t] = __float_as_uint(dpp<XOR8>(u2f(og[0][t]))); }
      SB();
#pragma unroll
      for (int t = 0; t < 4; ++t) d[t] = mfma16(og[1], bf[4 + t], d[t]);
    };
    auto p_bad = [&]() { return !all_ok(max(max(og[0][0], og[0][1]), max(og[0][2], og[0][3])) != SENT); };
    wo_mm();
    if (__builtin_expect(p_bad(), 0)) {
#pragma nounroll
      for (int spin = 0; spin < SPIN_MAX; ++spin) {
        __builtin_amdgcn_s_sleep(1);
        CFENCE();
        og[0] = ld16<AUX_SC1>(wsr, po, 0);
        if (!p_bad()) break;
      }
      wo_mm();
      SB();
      asm volatile(".Lend_d:");
    }
    STAMP(5); WSTAMP(5);
    {
      SB();
      f32x4 y; f32x2 h[4];
#pragma unroll
      for (int t = 0; t < 4; ++t) { h[t] = d[t].xy + d[t].zw; PIN(h[t]); }
#pragma unroll
      for (int t = 0; t < 4; ++t) { y[t] = h[t][0] + h[t][1]; PIN(y[t]); }
      *LDSP(f32x4, rdst) = y;
    }
    STAMP(16);
    LDS_BARRIER();
    STAMP(17);
    if (wave < 2) {
      f32x2 v[4];
#pragma unroll
      for (int w = 0; w < 4; ++w) v[w] = f32x2{LDSP(float, rsrc)[w * 256], LDSP(float, rsrc)[w * 256 + 128]};
      const f32x2 h = (v[0] + v[1]) + (v[2] + v[3]);
      const float y = h[0] + h[1];
      __builtin_amdgcn_raw_buffer_store_b32(min(pack_bf(y, dpp<XOR1>(y)), SENT - 1u), wsr, xo, 0, AUX_SC1);
      asm volatile(".Lend_a:");
    }
    STAMP(6);
    FINISH();
  }
  if (part == 3) {
    if (!has_w) FINISH();
    u32x4 b2[4];
    {
      __builtin_amdgcn_s_barrier();
      const rsrc_t r2 = mk_rsrc(w2 + (size_t)(blk * 32 + wave * 4) * 512, 4096u);
      const u32 l2 = (u32)(uintptr_t)(&sm.w2b[wave * 4][0]);
#pragma unroll
      for (int n = 0; n < 4; ++n) dma16(r2, (lane ^ (n * 2) ^ ((wave & 2) << 2)) * 16, n * 1024, l2 + n * 1024);
    }
    const u32 tdst = lds_addr(&sm.hb[hpos]);
    const u32 sdst = lds_addr(&sm.stat[wave][lane]);
    const u32 ssrc = lds_addr(&sm.stat[(g >> 1) * 4 + ((lane >> 1) & 3)][(lane & 1) * 16]);
    const u32 adst = lds_addr((c < 4 && (g & 1) == 0) ? &sm.actl[g >> 1][wave * 4 + c] : reinterpret_cast<u16*>(&sm.dummy[lane]));
    const u32 asrc = lds_addr(&sm.actl[lane & 1][g * 8]);
    const u32 apa = lds_addr(&sm.hb[(c >> 3) * 512 + (c & 3) * 64 + ((c >> 2) & 1) * 32 + g * 8]);
    u32 xoff = G_XP + g * 2048 + ub * 1024 + (wave & 3) * 256 + c * 16;
    u32x4 one;
#pragma unroll
    for (int k = 0; k < 4; ++k) one[k] = ((c & 2) == 0 && (c >> 2) == k) ? ((c & 1) ? 0x3f800000u : 0x00003f80u) : 0u;
    PIN(one);
    u32 ooff = G_OP + (blk * 512 + tid) * 8;
    float Afp = Af, Bfp = Bf;
    PIN(xoff); PIN(ooff); PIN(Afp); PIN(Bfp);
    PIN(ga); PIN(xin); PIN(cc1);
    u32 w2a = (u32)(uintptr_t)(&sm.w2b[0][0]) + (g * 8 + ((lane >> 2) & 3)) * 1024 + (wave ^ (g & 1)) * 128 + (lane & 15) * 8;
    PIN(w2a);
    if (wave == 1) {
      due = t1 + P.N1 * 256; gate();
      u32x4 gn[2] = {ld16<AUX_SC1>(wsr, G_NT + lane * 16, 0), ld16<AUX_SC1>(wsr, G_NT + lane * 16, 1024)};
      const int tn = notices(IC<2>{}, G_NT + lane * 16, gn) * 256;
      due = (tn > t1 + 2 * 256 ? max(tn, t1 + P.L0 * 256) + P.L1 * 256 : t1) + P.X2 * 256;
      until(due);
    }
    asm volatile("s_waitcnt vmcnt(0)" ::: "memory");
    STAMP(9);
    __builtin_amdgcn_s_barrier();
    STAMP(11);
    CFENCE();
    u32x4 xg[2] = {ld16<AUX_SC1>(wsr, xoff, 0), ld16<AUX_SC1>(wsr, xoff, 8192)};
    SB();
#pragma unroll
    for (int t = 0; t < 4; ++t) {
      const u32x2 lo = tr_read(w2a ^ (t * 32)), hi = tr_read((w2a ^ (t * 32)) + 4096);
      b2[t] = u32x4{lo[0], lo[1], hi[0], hi[1]};
    }
#pragma unroll
    for (int t = 0; t < 4; ++t) PIN(b2[t]);
    SB();
    const f32x4 z4 = {0.f, 0.f, 0.f, 0.f};
    auto xp_bad = [&]() { return !all_ok(max(max(max(xg[0][0], xg[0][1]), max(xg[0][2], xg[0][3])), max(max(xg[1][0], xg[1][1]), max(xg[1][2], xg[1][3]))) != SENT); };
    f32x4 v = mfma16(one, xg[1], mfma16(one, xg[0], z4));
    if (__builtin_expect(xp_bad(), 0)) {
#pragma nounroll
      for (int spin = 0; spin < SPIN_MAX; ++spin) {
        __builtin_amdgcn_s_sleep(1);
        CFENCE();
        xg[0] = ld16<AUX_SC1>(wsr, xoff, 0); xg[1] = ld16<AUX_SC1>(wsr, xoff, 8192);
        if (!xp_bad()) break;
      }
      v = mfma16(one, xg[1], mfma16(one, xg[0], z4));
      SB();
      asm volatile(".Lend_e:");
    }
    STAMP(5); WSTAMP(5);
    {
      const float a0 = v[0], a1 = v[1];
      const float x10 = fmaf(ga[0], a0, xin[0]), x11 = fmaf(ga[1], a1, xin[1]);
      *LDSP(u32, tdst) = pack_bf(x10 * cc1[0], x11 * cc1[1]);
      float u = swap32_add(x10 + x11, fmaf(x10, x10, x11 * x11));
      u += dpp<XOR1>(u); u += dpp<XOR2>(u); u += dpp<XOR7>(u); u += dpp<XOR8>(u);
      *LDSP(float, sdst) = u;
    }
    STAMP(16);
    LDS_BARRIER();
    STAMP(17);
    {
      const f32x4 z = {0.f, 0.f, 0.f, 0.f};
      const u32x4 af0 = LDSP(u32x4, apa)[0], af1 = LDSP(u32x4, apa)[32];
      float s = LDSP(float, ssrc)[0], q = LDSP(float, ssrc)[32];
      SB();
      f32x4 d[4];
#pragma unroll
      for (int j = 0; j < 4; ++j) d[j] = mfma16(af0, bf[j], z);
#pragma unroll
      for (int j = 0; j < 4; ++j) d[j] = mfma16(af1, bf[4 + j], d[j]);
      s += dpp<XOR1>(s); q += dpp<XOR1>(q); s += dpp<XOR2>(s); q += dpp<XOR2>(q); s += dpp<XOR7>(s); q += dpp<XOR7>(q);
      const float mean = s * (1.f / 512.f);
      const float var = fmaf(q, 1.f / 512.f, -mean * mean);
      const float ri = __builtin_amdgcn_rsqf(fmaxf(var, 0.f) + 1e-5f);
      const float cf = fmaf(-ri * mean, Afp, Bfp);
      float y = (d[0][0] + d[1][1]) + (d[2][2] + d[3][3]);
      y = __builtin_bit_cast(float, __builtin_amdgcn_update_dpp(__builtin_bit_cast(int, y), __builtin_bit_cast(int, y), XOR8, 0xa, 0xf, false));
      y = swap16_add(y, y);
      y = fmaf(y, ri, cf);
      const float up = dpp<0x104>(y);
      *LDSP(u16, adst) = (u16)pack_bf(silu_f(y) * up, 0.f);
    }
    STAMP(18);
    LDS_BARRIER();
    STAMP(10);
    {
      const u32x4 af = *LDSP(u32x4, asrc);
      const f32x4 z = {0.f, 0.f, 0.f, 0.f};
      const f32x4 d0 = mfma16(af, b2[0], z), d1 = mfma16(af, b2[1], z), d2 = mfma16(af, b2[2], z), d3 = mfma16(af, b2[3], z);
      const bool g1 = g & 1, g2 = g & 2;
      const float e0 = g1 ? d1[0] : d0[0], e1 = g1 ? d1[1] : d0[1], f0 = g1 ? d3[0] : d2[0], f1 = g1 ? d3[1] : d2[1];
      st8_sc1(wsr, ooff, pack_bf(g2 ? f0 : e0, g2 ? f1 : e1), tag);
    }
    asm volatile(".Lend_b:");
    STAMP(6);
    FINISH();
  }
  if (wave == 0) {
    const int d = fd, ul = lane >> 3;
    u32 gm[2] = {sg[0][0], sg[1][0]};
    if (!all_ok((sg[0][1] == tag) & (sg[1][1] == tag))) gather8<2>(wsr, G_MOD + (1024 + d) * 8, 1536 * 8, tag, gm);
    u32 xa0, xa1;
    {
      due = t1 + P.N2 * 256; gate();
      u32x4 gn[2] = {ld16<AUX_SC1>(wsr, G_NT + lane * 16, 0), ld16<AUX_SC1>(wsr, G_NT + lane * 16, 1024)};
      const int tn = notices(IC<2>{}, G_NT + lane * 16, gn) * 256;
      due = (tn > t1 + 2 * 256 ? max(tn, t1 + P.L0 * 256) + P.L1 * 256 : t1) + P.X3 * 256;
      until(due);
    }
#pragma nounroll
    for (int spin = 0; spin < SPIN_MAX; ++spin) {
      CFENCE();
      xa0 = __builtin_amdgcn_raw_buffer_load_b32(wsr, G_XP + ul * 2048 + (d >> 1) * 4, 0, AUX_SC1);
      xa1 = __builtin_amdgcn_raw_buffer_load_b32(wsr, G_XP + ul * 2048 + (d >> 1) * 4, 1024, AUX_SC1);
      if (all_ok(max(xa0, xa1) != SENT)) break;
      __builtin_amdgcn_s_sleep(1);
    }
    float a0 = (d & 1) ? hi_f(xa0) : lo_f(xa0), a1 = (d & 1) ? hi_f(xa1) : lo_f(xa1);
    a0 += dpp<XOR8>(a0); a1 += dpp<XOR8>(a1);
    a0 = swap16_add(a0, a0); a1 = swap16_add(a1, a1);
    a0 = swap32_add(a0, a0); a1 = swap32_add(a1, a1);
    const bool hb = g & 1;
    float xv = rbf(((d & 1) ? hi_f(sx2) : lo_f(sx2)) + rbf((hb ? hi_f(gm[0]) : lo_f(gm[0])) * rbf(hb ? a1 : a0)));
    float gv = hb ? hi_f(gm[1]) : lo_f(gm[1]);
    PIN(xv); PIN(gv);
    if (wi == 0 && lane == 0) __builtin_amdgcn_raw_buffer_store_b32(tag, wsr, WS_EPOCH, 0, AUX_SC1);
    STAMP(10);
    due = max(due + P.X4 * 256, (int)(((u32)wall_clock64() - t0) << 8) + P.GX * 256);
    const bool last = ul + 40 < NUP;
    u32 o0 = G_OP + (ul * 512 + d) * 8, o5 = last ? o0 : OOB, tg5 = last ? tag : 0u;
    const rsrc_t rout = mk_rsrc(out, 2048u);
    u32 oo = (lane & 40) == 0 ? (g * 512 + d) * 2 : OOB;
    const u32 pk = c == 0 ? 0x00003f80u : (c == 4 ? 0x3f800000u : 0u);
    u32x4 one = {pk, 0u, pk, 0u};
    PIN(o0); PIN(o5); PIN(tg5); PIN(oo); PIN(one);
    __builtin_amdgcn_raw_buffer_store_b16((u16)pack_bf(xv, 0.f), rout, oo, 0, AUX_SC1);
    until(due);
    f32x4 sv;
#pragma nounroll
    for (int spin = 0; spin < SPIN_MAX; ++spin) {
      CFENCE();
      u32x2 og[6];
#pragma unroll
      for (int k = 0; k < 5; ++k) og[k] = ld8<AUX_SC1>(wsr, o0, k * 32768);
      og[5] = ld8<AUX_SC1>(wsr, o5, 5 * 32768);
      sv = f32x4{0.f, 0.f, 0.f, 0.f};
#pragma unroll
      for (int k = 0; k < 3; ++k) sv = mfma16(one, u32x4{og[2 * k][0], og[2 * k][1], og[2 * k + 1][0], og[2 * k + 1][1]}, sv);
      const u32 bad = ((og[0][1] ^ tag) | (og[1][1] ^ tag) | (og[2][1] ^ tag)) | ((og[3][1] ^ tag) | (og[4][1] ^ tag) | (og[5][1] ^ tg5));
      PIN(sv);
      if (all_ok(bad == 0u)) break;
      __builtin_amdgcn_s_sleep(1);
    }
    STAMP(7);
    const float s = sv[0] + dpp<XOR8>(sv[0]);
    __builtin_amdgcn_raw_buffer_store_b16((u16)pack_bf(fmaf(gv, s, xv), 0.f), rout, oo, 0, AUX_SC1);
    asm volatile(".Lend_c:");
  }
  FINISH();
}

extern "C" void kernel_launch(void* const* d_in, const int* in_sizes, int n_in,
                              void* d_out, int out_size, void* d_ws, size_t ws_size,
                              hipStream_t stream) {
  fused_kernel<<<256, 512, 0, stream>>>((const u16*)d_in[1], (const u16*)d_in[2], (const u16*)d_in[4], (const u16*)d_in[3], (const u16*)d_in[10], (const u16*)d_in[6], (const u16*)d_in[7],
                                        (unsigned char*)d_ws, (const u16*)d_in[0], (const u16*)d_in[9], (const u16*)d_in[8], (u16*)d_out);
}
```

```cpp
#define PROBE_MUL 10
#include <hip/hip_runtime.h>
#include <stdint.h>

typedef unsigned int u32;
typedef unsigned short u16;
typedef unsigned long long u64;
typedef __attribute__((ext_vector_type(4))) unsigned u32x4;
typedef __attribute__((ext_vector_type(2))) unsigned u32x2;
typedef __attribute__((ext_vector_type(2))) __bf16 bf16x2;
typedef __attribute__((ext_vector_type(4))) float f32x4;
typedef __attribute__((ext_vector_type(2))) float f32x2;
typedef __amdgpu_buffer_rsrc_t rsrc_t;

constexpr int NH = 32, SK = 16384, DH = 128, WIN = 4096, FF = 1376;
struct Prm { int E0, ES, E1, E2, E2C, EU, TEND, DT_UP, L0, L1, X1, X2, X3, X4, NE, FE, M0, M1, M2, M3, M4, GS, GX, N1, N2, GA, WD, DT_WO, LW, LU, EW, PM, NF; };
constexpr Prm PRM0 = {32, 24, 32, 32, 56, 64, 2010, 60, 30, 25, 92, 86, 131, 88, 4, 12, 5, 8, 21, 4, 32, 0, 56, 28, 70, 0, 4, 100, 45, 15, 8, 0x24310, 1};
constexpr int NL = 37;
constexpr int NCMIN = 192;
constexpr int WK = 8, WV = 5;
constexpr int WUSE = 12, LNS = 7;
constexpr int QP = 14, QD = 4;
constexpr int VQ = 6;
constexpr int NSL = (NL + 7) / 8;
constexpr int VS = VQ + NSL;
constexpr int MLS = VS + 2;
constexpr int VSIDE = MLS - 5, UPREP = MLS + 8;
constexpr int SIDE = NCMIN / 8 - 4;
constexpr int CPF = 18;
constexpr int SMO = 1;
constexpr int TBP = 4, TBL = 11, TBU = 15;
constexpr int cons_end(int v) {
  const int a = v - WV + 1, c = (v - MLS) * 4;
  const int m = a < c ? a : c;
  return m < 0 ? 0 : (m > NL ? NL : m);
}
struct Tab { u32 e[512]; };
constexpr Tab make_tab(const Prm& P) {
  Tab T{};
  for (int bid = 0; bid < 256; ++bid) {
    const int xcd = bid & 7, part = bid >> 4 & 3, i8 = (bid >> 3 & 1) | bid >> 6 << 1, wi = i8 * 8 + xcd;
    const bool has_mod = wi < 48, has_up = wi >= 21;
    const int cb0 = (i8 + 5) & 7, cb1 = (i8 + 4) & 7, ms = wi - 20;
    const int mi = wi < 8 ? wi : (wi < 16 ? 8 + ((xcd + 4) & 7) : (wi < 20 ? (wi - 14) * 8 + 3 : 16 + ms / 7 * 8 + ms % 7 + (ms % 7 >= 3 ? 1 : 0)));
    const int ui = xcd * 5 + (xcd > 5 ? xcd - 5 : 0) + (i8 >= 3 ? (xcd >= 5 ? 1 : 0) + i8 - 3 : 0);
    const int wd = cb0 == 3 ? 0 : P.WD;
    const int e[4] = {P.E0 - 4 * wd + (cb0 == 3 ? P.ES : 0), P.E1 + (cb1 == 3 ? P.ES : 0), has_mod ? (wi < 16 ? P.E2C : P.E2) + ((mi & 7) == 3 ? P.ES : 0) : 0, has_up ? P.EU : 0};
    const int ew = wd ? P.EW : 0, pa = (1024 + e[0] + ew + e[1] + e[2] + e[3]) / 4;
    int c[5] = {0, 0, 0, 0, 124};
    for (int k = 0; k < 3; ++k) c[k + 1] = c[k] + (pa - e[k] - (k == 0 ? ew : 0) + 4) / 8 - 1;
    const int nl = c[part + 1] - c[part] + 1, wst = e[part] / 4;
    const bool has_w = part < 2 || (part == 2 ? has_mod : has_up);
    const int blk = !has_w ? 0 : (part == 0 ? cb0 : (part == 1 ? xcd * 8 + cb1 : (part == 2 ? mi : ui)));
    const int tau = (part == 3 && has_up ? P.TEND - P.DT_UP : (part == 0 && wd ? P.TEND - P.DT_WO : P.TEND)) * 1024 / (4 * (2 * nl + wst - 2) + P.NF);
    T.e[2 * bid] = (u32)(c[part] | nl << 7 | blk << 13 | (has_w ? 1 : 0) << 19 | (part == 0 ? wd : 0) << 20);
    T.e[2 * bid + 1] = (u32)(tau | (tau * wst / (part == 0 && wd < 8 ? 8 - wd : 8)) << 16);
  }
  return T;
}
constexpr int step_range(const Prm& P, bool mx_) {
  int mx = 0, mn = 1 << 20;
  const Tab T = make_tab(P);
  for (int bid = 0; bid < 256; ++bid) { const int n = T.e[2 * bid] >> 7 & 63; mx = n > mx ? n : mx; mn = n < mn ? n : mn; }
  return mx_ ? mx : mn;
}
extern "C" __attribute__((used, visibility("protected"))) __constant__ const Tab wg_tab = make_tab(PRM0);
static_assert(step_range(PRM0, true) <= NL && step_range(PRM0, false) >= NCMIN / 8, "a wave's key range does not fit its load steps");
static_assert(cons_end(SIDE + WV + 1) <= NCMIN / 8 && cons_end(UPREP) <= NCMIN / 8 && SIDE > MLS + 3, "steps every wave runs");
constexpr int NUP = FF / 32;
constexpr int SPIN_MAX = 1 << 17;

constexpr u32 WS_EPOCH = 0;
constexpr u32 G_TB = 1024;
constexpr u32 G_NT = 2048;
constexpr u32 G_MOD = 4096;
constexpr u32 G_Q = G_MOD + 3072 * 8;
constexpr u32 G_ML = G_Q + 4096 * 8;
constexpr u32 G_P = G_ML + 256 * 16;
constexpr u32 G_XP = G_P + 256 * 256;
constexpr u32 G_OP = G_XP + 8 * 512 * 4;
constexpr u32 WS_END = G_OP + 43 * 512 * 8;
constexpr u32 SENT = 0xffffffffu;
constexpr u32 OOB = 0x40000000u;
#define WST 0

constexpr int AUX_NT = 2, AUX_SC1 = 16;

__device__ __forceinline__ float lo_f(u32 v) { return __uint_as_float(v << 16); }
__device__ __forceinline__ float hi_f(u32 v) { return __uint_as_float(v & 0xffff0000u); }
__device__ __forceinline__ float rbf(float x) { return (float)(__bf16)x; }
__device__ __forceinline__ u32 pack_bf(float lo, float hi) {
  bf16x2 r; r[0] = (__bf16)lo; r[1] = (__bf16)hi; return __builtin_bit_cast(u32, r);
}
__device__ __forceinline__ float dot2(u32 k, u32 q, float acc) {
  return __builtin_amdgcn_fdot2_f32_bf16(__builtin_bit_cast(bf16x2, k), __builtin_bit_cast(bf16x2, q), acc, false);
}
template <int CTRL> __device__ __forceinline__ float dpp(float x) {
  return __builtin_bit_cast(float, __builtin_amdgcn_mov_dpp(__builtin_bit_cast(int, x), CTRL, 0xf, 0xf, true));
}
constexpr int XOR1 = 0xB1, XOR2 = 0x4E, XOR7 = 0x141, XOR8 = 0x128;
__device__ __forceinline__ float swap32_add(float a, float b) {
  auto t = __builtin_amdgcn_permlane32_swap(__float_as_uint(a), __float_as_uint(b), false, false);
  return __uint_as_float(t[0]) + __uint_as_float(t[1]);
}
__device__ __forceinline__ float swap16_add(float a, float b) {
  auto t = __builtin_amdgcn_permlane16_swap(__float_as_uint(a), __float_as_uint(b), false, false);
  return __uint_as_float(t[0]) + __uint_as_float(t[1]);
}
__device__ __forceinline__ float wave_sum(float x) {
  x += dpp<XOR1>(x); x += dpp<XOR2>(x); x += dpp<XOR7>(x); x += dpp<XOR8>(x);
  x = swap16_add(x, x);
  return swap32_add(x, x);
}
__device__ __forceinline__ float wave_max(float x) {
  x = fmaxf(x, dpp<XOR1>(x)); x = fmaxf(x, dpp<XOR2>(x)); x = fmaxf(x, dpp<XOR7>(x)); x = fmaxf(x, dpp<XOR8>(x));
  auto s = __builtin_amdgcn_permlane16_swap(__float_as_uint(x), __float_as_uint(x), false, false);
  x = fmaxf(__uint_as_float(s[0]), __uint_as_float(s[1]));
  auto t = __builtin_amdgcn_permlane32_swap(__float_as_uint(x), __float_as_uint(x), false, false);
  return fmaxf(__uint_as_float(t[0]), __uint_as_float(t[1]));
}
template <int CTRL> __device__ __forceinline__ float sel_add(float lo, float hi, bool up) {
  const float t1 = lo + dpp<CTRL>(lo), t2 = hi + dpp<CTRL>(hi);
  return up ? t2 : t1;
}
template <int I> __device__ __forceinline__ float bc16(float x) {
  return __builtin_bit_cast(float, __builtin_amdgcn_update_dpp(0, __builtin_bit_cast(int, x), 0x150 + I, 0xf, 0xf, false));
}
__device__ __forceinline__ float bcast16(float x, int i) {
  switch (i) {
    case 0: return bc16<0>(x); case 1: return bc16<1>(x); case 2: return bc16<2>(x); case 3: return bc16<3>(x);
    case 4: return bc16<4>(x); case 5: return bc16<5>(x); case 6: return bc16<6>(x); case 7: return bc16<7>(x);
    case 8: return bc16<8>(x); case 9: return bc16<9>(x); case 10: return bc16<10>(x); case 11: return bc16<11>(x);
    case 12: return bc16<12>(x); case 13: return bc16<13>(x); case 14: return bc16<14>(x); default: return bc16<15>(x);
  }
}
__device__ __forceinline__ float silu_f(float x) { return x * __builtin_amdgcn_rcpf(1.f + __expf(-x)); }

template <int AUX> __device__ __forceinline__ u32x4 ld16(rsrc_t r, u32 voff, u32 soff) {
  return __builtin_amdgcn_raw_buffer_load_b128(r, voff, soff, AUX);
}
template <int AUX> __device__ __forceinline__ u32x2 ld8(rsrc_t r, u32 voff, u32 soff) {
  return __builtin_amdgcn_raw_buffer_load_b64(r, voff, soff, AUX);
}
__device__ __forceinline__ void st8_sc1(rsrc_t r, u32 voff, u32 v, u32 tag) {
  __builtin_amdgcn_raw_buffer_store_b64(u32x2{v, tag}, r, voff, 0, AUX_SC1);
}
__device__ __forceinline__ void st16_sc1(rsrc_t r, u32 voff, u32 v0, u32 v1, u32 tag) {
  __builtin_amdgcn_raw_buffer_store_b128(u32x4{v0, tag, v1, tag}, r, voff, 0, AUX_SC1);
}
__device__ __forceinline__ rsrc_t mk_rsrc(const void* p, u32 bytes) {
  return __builtin_amdgcn_make_buffer_rsrc(const_cast<void*>(p), 0, bytes, 0x00020000);
}
#define WAIT_VM(n) __builtin_amdgcn_s_waitcnt(((n) & 15) | (((n) >> 4) << 14) | (7 << 4) | (15 << 8))
#define SB() __builtin_amdgcn_sched_barrier(0)
#define CFENCE() asm volatile("" ::: "memory")
__device__ __forceinline__ bool all_ok(bool ok) { return __builtin_amdgcn_ballot_w64(!ok) == 0ull; }

template <int N> __device__ __forceinline__ void gather16(rsrc_t ws, u32 voff, u32 stride, u32 tag, u32x4 (&g)[N]) {
#pragma nounroll
  for (int spin = 0; spin < SPIN_MAX; ++spin) {
    CFENCE();
    bool ok = true;
#pragma unroll
    for (int i = 0; i < N; ++i) g[i] = ld16<AUX_SC1>(ws, voff + i * stride, 0);
#pragma unroll
    for (int i = 0; i < N; ++i) ok &= (g[i][1] == tag) & (g[i][3] == tag);
    if (all_ok(ok)) break;
    __builtin_amdgcn_s_sleep(8);
  }
}
template <int N> __device__ __forceinline__ void gather8(rsrc_t ws, u32 voff, u32 stride, u32 tag, u32 (&g)[N], bool act = true) {
#pragma nounroll
  for (int spin = 0; spin < SPIN_MAX; ++spin) {
    CFENCE();
    bool ok = true;
    u32x2 t[N];
#pragma unroll
    for (int i = 0; i < N; ++i) t[i] = ld8<AUX_SC1>(ws, voff + i * stride, 0);
#pragma unroll
    for (int i = 0; i < N; ++i) { ok &= (t[i][1] == tag); g[i] = t[i][0]; }
    if (all_ok(ok | !act)) break;
    __builtin_amdgcn_s_sleep(8);
  }
}

struct Smem {
  float red[8][128];
  float ml[8][2];
  float r2[64 * 20];
  u16 hb[3 * 1024];
  float stat[8][64];
  float red2[8][128];
  u32 dummy[64];
  u32x2 bc;
  u32x4 zero[4];
  u32 mflag[8];
  u32x4 wbuf[8][8][64];
  __attribute__((aligned(256))) u16 w2b[32][512];
  u16 actl[2][32];
};

#define PROBE_K 11
#define PROBE_OP 1
#define PROBE_PART 0
#define STAMP(k) do { if ((k) == PROBE_K && tid == 0) pst = wall_clock64(); } while (0)
#define WSTAMP(k) do {} while (0)
#define FINISH() do { STAMP(8); if (tid == 0) { __hip_atomic_store(reinterpret_cast<u64*>(ws + (1u << 20)) + bid * 2, pt0 + (u32)(t0 - (u32)pt0) + (t1 >> 8), __ATOMIC_RELAXED, __HIP_MEMORY_SCOPE_AGENT); \
   __hip_atomic_store(reinterpret_cast<u64*>(ws + (1u << 20)) + bid * 2 + 1, (PROBE_PART < 0 || part == PROBE_PART) ? pst : 0ull, __ATOMIC_RELAXED, __HIP_MEMORY_SCOPE_AGENT); } \
   if (bid == 8 && wave == 0) probe_end(ws, pt0, lane); return; } while (0)
#define PIN(x) asm volatile("" : "+v"(x))
#define LDS_BARRIER() do { __builtin_amdgcn_s_waitcnt(0xc07f); __builtin_amdgcn_s_barrier(); CFENCE(); } while (0)
#define WAVE_SYNC() do { __builtin_amdgcn_fence(__ATOMIC_RELEASE, "wavefront"); __builtin_amdgcn_wave_barrier(); __builtin_amdgcn_fence(__ATOMIC_ACQUIRE, "wavefront"); } while (0)

__device__ __forceinline__ void dma16(rsrc_t r, u32 voff, u32 soff, u32 lds_dst) {
  u32 keep;
  asm volatile("s_mov_b32 %0, m0\n\ts_mov_b32 m0, %4\n\ts_nop 4\n\tbuffer_load_dwordx4 %1, %2, %3 offen nt lds\n\ts_mov_b32 m0, %0"
               : "=&s"(keep) : "v"(voff), "s"(r), "s"(soff), "s"(lds_dst) : "memory");
}
__device__ __forceinline__ u32 perm_lo(u32 a, u32 b) { return __builtin_amdgcn_perm(b, a, 0x05040100u); }
__device__ __forceinline__ u32 perm_hi(u32 a, u32 b) { return __builtin_amdgcn_perm(b, a, 0x07060302u); }
__device__ __forceinline__ float rdl(float x, int l) { return __int_as_float(__builtin_amdgcn_readlane(__float_as_int(x), l)); }

typedef __attribute__((ext_vector_type(8))) __bf16 bf16x8;
__device__ __forceinline__ f32x4 mfma16(u32x4 a, u32x4 b, f32x4 c) {
  return __builtin_amdgcn_mfma_f32_16x16x32_bf16(__builtin_bit_cast(bf16x8, a), __builtin_bit_cast(bf16x8, b), c, 0, 0, 0);
}
__device__ __forceinline__ float sum8(const float* p) {
  const f32x4 a = *reinterpret_cast<const f32x4*>(p), b = *reinterpret_cast<const f32x4*>(p + 4);
  return ((a[0] + b[0]) + (a[1] + b[1])) + ((a[2] + b[2]) + (a[3] + b[3]));
}
__device__ __forceinline__ float ln_ri(float var) {
  return __builtin_amdgcn_rcpf(rbf(__builtin_amdgcn_sqrtf(rbf(rbf(fmaxf(var, 0.f)) + 1e-5f))));
}
__device__ __forceinline__ void ln_stats2(const float (&x0)[8], const float (&x1)[8], float& mb0, float& ri0, float& mb1, float& ri1) {
  const float k0 = rdl(x0[0], 0), k1 = rdl(x1[0], 0);
  float s0 = 0.f, s1 = 0.f, q0 = 0.f, q1 = 0.f;
#pragma unroll
  for (int k = 0; k < 8; ++k) {
    const float d0 = x0[k] - k0, d1 = x1[k] - k1;
    s0 += d0; s1 += d1; q0 = fmaf(d0, d0, q0); q1 = fmaf(d1, d1, q1);
  }
  float t = swap16_add(swap32_add(s0, s1), swap32_add(q0, q1));
  t += dpp<XOR1>(t); t += dpp<XOR2>(t); t += dpp<XOR7>(t); t += dpp<XOR8>(t);
  auto u = __builtin_amdgcn_permlane16_swap(__float_as_uint(t), __float_as_uint(t), false, false);
  const float ms = __uint_as_float(u[0]) * (1.f / 512.f), mq = __uint_as_float(u[1]) * (1.f / 512.f);
  const float ri = ln_ri(fmaf(-ms, ms, mq));
  mb0 = rbf(k0 + rdl(ms, 0)); mb1 = rbf(k1 + rdl(ms, 32));
  ri0 = rdl(ri, 0); ri1 = rdl(ri, 32);
}
template <int I> struct IC { static constexpr int v = I; };
template <int I, int N, class F> __device__ __forceinline__ void sfor(F&& f) {
  if constexpr (I < N) { if (f(IC<I>{})) sfor<I + 1, N>(f); }
}
typedef __attribute__((ext_vector_type(4))) short s16x4;
typedef __attribute__((address_space(3))) s16x4* lds_s16x4p;
__device__ __forceinline__ u32x2 tr_read(u32 lds_addr) {
  return __builtin_bit_cast(u32x2, __builtin_amdgcn_ds_read_tr16_b64_v4i16((lds_s16x4p)lds_addr));
}
__device__ __forceinline__ u32 next_tag(u32 e) { return ((e | 0x40004000u) + 1u) & ~0x40004000u; }
__device__ __forceinline__ float u2f(u32 v) { return __uint_as_float(v); }
#define LDSP(T, a) ((__attribute__((address_space(3))) T*)(a))
template <class T> __device__ __forceinline__ u32 lds_addr(T* p) { u32 a = (u32)(uintptr_t)p; PIN(a); return a; }

#define TAB_LOAD "s_add_u32 s30, s28, wg_tab@rel32@lo+4\ns_addc_u32 s31, s29, wg_tab@rel32@hi+12\nglobal_load_dwordx2 %[te], %[va], s[30:31]\n"
__device__ __forceinline__ void probe_end(unsigned char* ws, u64 pt0, int lane) {
  while (wall_clock64() < pt0 + 5500ull) __builtin_amdgcn_s_sleep(8);
  u64 a[4], c2[4];
  for (int i = 0; i < 4; ++i) {
    a[i] = __hip_atomic_load(reinterpret_cast<u64*>(ws + (1u << 20)) + (i * 64 + lane) * 2, __ATOMIC_RELAXED, __HIP_MEMORY_SCOPE_AGENT);
    c2[i] = __hip_atomic_load(reinterpret_cast<u64*>(ws + (1u << 20)) + (i * 64 + lane) * 2 + 1, __ATOMIC_RELAXED, __HIP_MEMORY_SCOPE_AGENT);
  }
  int mn0 = 1 << 30, mx1 = -(1 << 30), mn1 = 1 << 30, sum = 0, cnt = 0;
  for (int i = 0; i < 4; ++i) {
    const int d0 = (int)(a[i] - pt0);
    mn0 = min(mn0, d0);
    if (c2[i] != 0ull) { const int d1 = (int)(c2[i] - pt0); mx1 = max(mx1, d1); mn1 = min(mn1, d1); sum += d1; cnt += 1; }
  }
  for (int o = 1; o < 64; o <<= 1) { mn0 = min(mn0, __shfl_xor(mn0, o)); mx1 = max(mx1, __shfl_xor(mx1, o)); mn1 = min(mn1, __shfl_xor(mn1, o)); sum += __shfl_xor(sum, o); cnt += __shfl_xor(cnt, o); }
  int X = PROBE_OP == 0 ? mx1 : (PROBE_OP == 1 ? sum / max(cnt, 1) : mn1);
  if (PROBE_K == 102) X = mn0;
  X = mn0 + (X - mn0) * PROBE_MUL;
  const u64 tg = pt0 + (u64)(long long)(X + 8000);
  for (int i = 0; i < 400000 && wall_clock64() < tg; ++i) __builtin_amdgcn_s_sleep(1);
}
__global__ void __launch_bounds__(512, 2) fused_kernel(const u16* kc_, const u16* vc_, const u16* wo_, const u16* wq_, const u16* wmod_, const u16* w1_, const u16* w3_,
                                                       unsigned char* ws_, const u16* x_, const u16* cnd_, const u16* w2_, u16* out_) {
  __shared__ __attribute__((aligned(256))) Smem sm;
  const u64 a_ka = (u64)__builtin_amdgcn_kernarg_segment_ptr();
  const int bid = blockIdx.x, tid = threadIdx.x;
  u64 wa, t64; u32x2 te; int tid0;
  u32 ar[14];
  u32 tch2, sa_, sb_, va_;
  u32x4 kr[NL], vr[NL];
  asm volatile(
    "s_load_dwordx16 s[4:19], %[ka], 0x0\ns_load_dwordx8 s[20:27], %[ka], 0x40\n"
    "s_memrealtime %[t]\n"
    "v_lshlrev_b32 %[va], 3, %[bid]\n"
    "s_getpc_b64 s[28:29]\n.Lcpf_first:\n"
    TAB_LOAD
    "s_cmp_gt_u32 %[bid], 15\ns_cbranch_scc1 .Lno_cpf\n"
    "s_mov_b32 %[sa], .Lend_a-.Lcpf_first\ns_max_i32 %[sa], %[sa], .Lend_b-.Lcpf_first\ns_max_i32 %[sa], %[sa], .Lend_c-.Lcpf_first\ns_max_i32 %[sa], %[sa], .Lend_d-.Lcpf_first\ns_max_i32 %[sa], %[sa], .Lend_e-.Lcpf_first\n"
    "v_lshlrev_b32 %[va], 6, %[tid]\nv_min_u32 %[va], %[sa], %[va]\nglobal_load_dword %[pf], %[va], s[28:29]\n"
    ".Lno_cpf:\n"
    "s_and_b32 %[sa], %[bid], 15\ns_lshr_b32 %[sb], %[bid], 6\ns_lshl2_add_u32 %[sa], %[sa], %[sb]\ns_lshl_b32 %[sa], %[sa], 22\ns_xor_b32 %[sa], %[sa], 0xff0000\n"
    "s_and_b32 %[sb], %[bid], 48\nv_lshl_add_u32 %[va], %[sb], 5, %[tid]\nv_lshl_add_u32 %[va], %[va], 4, %[sa]\n"
    "v_readfirstlane_b32 %[t0], %[tid]\n"
    "s_waitcnt lgkmcnt(0)\nglobal_load_dwordx4 %[k0], %[va], s[4:5] nt\nglobal_load_dwordx4 %[v0], %[va], s[6:7] nt\n"
    "s_bitcmp1_b32 %[t0], 6\ns_cselect_b64 s[14:15], s[16:17], s[14:15]\n"
    "s_bitcmp1_b32 %[bid], 4\ns_cselect_b64 s[8:9], s[10:11], s[8:9]\ns_cselect_b64 s[12:13], s[14:15], s[12:13]\ns_bitcmp1_b32 %[bid], 5\ns_cselect_b64 %[wa], s[12:13], s[8:9]"
    : "=&{s4}"(ar[0]), "=&{s5}"(ar[1]), "=&{s6}"(ar[2]), "=&{s7}"(ar[3]), "=&{s18}"(ar[4]), "=&{s19}"(ar[5]), "=&{s20}"(ar[6]), "=&{s21}"(ar[7]), "=&{s22}"(ar[8]), "=&{s23}"(ar[9]),
      "=&{s24}"(ar[10]), "=&{s25}"(ar[11]), "=&{s26}"(ar[12]), "=&{s27}"(ar[13]), [wa] "=&s"(wa), [t0] "=&s"(tid0), [t] "=&s"(t64), [te] "=&v"(te), [sa] "=&s"(sa_), [sb] "=&s"(sb_),
      [va] "=&v"(va_), [pf] "=&v"(tch2), [k0] "=&v"(kr[0]), [v0] "=&v"(vr[0])
    : [ka] "s"(a_ka), [bid] "s"(bid), [tid] "v"(tid) : "s8", "s9", "s10", "s11", "s12", "s13", "s14", "s15", "s16", "s17", "s28", "s29", "s30", "s31", "scc", "memory");
  asm volatile("" : "+s"(ar[0]), "+s"(ar[1]), "+s"(ar[2]), "+s"(ar[3]), "+s"(ar[4]), "+s"(ar[5]), "+s"(ar[6]), "+s"(ar[7]), "+s"(ar[8]), "+s"(ar[9]), "+s"(ar[10]), "+s"(ar[11]), "+s"(ar[12]), "+s"(ar[13]));
  asm volatile("" : "+s"(wa), "+s"(t64), "+s"(tid0));
  const u32 tka = (u32)wall_clock64();
  const u64 pt0 = t64; u64 pst = (PROBE_K == 100 || PROBE_K == 102) ? pt0 : (PROBE_K == 101 ? pt0 + (u32)(tka - (u32)t64) : 0);
  const u32 t0 = tka;
  __builtin_assume(tid >= 0 && tid < 512 && tid0 >= 0 && tid0 < 512 && bid >= 0 && bid < 256);
  constexpr Prm P = PRM0;
  const int TEND = P.TEND;
  const int xcd = bid & 7, part = bid >> 4 & 3;
  const int lane = tid & 63, wave = tid0 >> 6;
  SB();
  const bool p3 = part == 3;
  u32 pitch = (u32)(0x005600c001000020ull >> (part * 16) & 0xffff) * 32u;
  const int sh = p3 ? 2 : 3;
  u32 wv0 = (u32)((p3 ? (wave >> 1) * 128 : wave * 64) + (part == 0 ? xcd * 512 : 0) + (lane >> sh)) * pitch + ((lane & (p3 ? 3 : 7)) ^ (lane >> (p3 ? 5 : 3))) * 16u;
  const u32 wimg = (u32)(uintptr_t)(&sm.wbuf[0][0][0]);
  u32 wsz = (part == 0 ? 4096u : 512u) * pitch, wld = wimg + (p3 ? (wave & 1) * 32768 + (wave >> 1) * 8192 : wave * 8192);
  PIN(wv0);
  asm volatile("" : "+s"(wsz), "+s"(wld), "+s"(pitch));
  auto karg = [&](int i) { return ar[2 * i] | (u64)ar[2 * i + 1] << 32; };
  const u64 a_kc = karg(0), a_vc = karg(1);
  unsigned char* ws = (unsigned char*)karg(2);
  const u16 *x = (const u16*)karg(3), *cnd = (const u16*)karg(4), *w2 = (const u16*)karg(5);
  u16* out = (u16*)karg(6);
  const rsrc_t wsr = mk_rsrc(ws, WS_END);
  asm volatile("s_waitcnt vmcnt(2)" : "+v"(te));
  const u32 te0 = __builtin_amdgcn_readfirstlane(te[0]), te1 = __builtin_amdgcn_readfirstlane(te[1]);
  const int bl0 = te0 & 127, nl = te0 >> 7 & 63, blk = te0 >> 13 & 63;
  const bool has_w = te0 >> 19 & 1;
  const rsrc_t rw = mk_rsrc((const void*)wa, wsz);
  const u32 wstep = pitch << (6 - sh), wflip = p3 ? 32u : 0u;
  u32 wso = blk << (sh + 4);
  int wleft = 8;
  auto piece = [&]() {
    dma16(rw, wv0, wso, wld);
    wso += wstep; wld += 1024;
    wv0 ^= (wleft & 1) ? wflip : 0u;
    --wleft;
  };
  SB();
  STAMP(23);
  const int b = bid >> 3 & 1, head = xcd * 4 + (bid >> 6 ^ 3);
  const int g = lane >> 4, c = lane & 15;
  const int i8 = b | bid >> 6 << 1, wi = i8 * 8 + xcd;
  const bool is_up = p3 && has_w, modc = part == 2 && wi < 16;
  const size_t base = ((size_t)(b * NH + head) * SK + (SK - WIN)) * DH;
  const rsrc_t rk = mk_rsrc((const u16*)a_kc + base, WIN * DH * 2), rv = mk_rsrc((const u16*)a_vc + base, WIN * DH * 2);
  int tau = te1 & 0xffff;
  const int tauw = te1 >> 16;
  int due = P.NF * tau >> 2, now = 0;
  auto gate = [&]() {
    while (now < due) { now = (int)(((u32)wall_clock64() - t0) << 8); if (now < due) __builtin_amdgcn_s_sleep(1); }
    due += tau;
  };
  auto until = [&](int t) { while ((int)(((u32)wall_clock64() - t0) << 8) < t); };
  const u32 pslot = G_P + ((b * 32 + head) * 4 + part) * 256;
  u32x2 ep; u32 xs0, xs1, tch1; u32x4 xr0, xr1;
  const rsrc_t rx = mk_rsrc(part == 2 ? cnd : x, (part == 1 || (part == 2 && has_w)) ? 2048u : 0u);
  auto small_ops = [&]() {
    ep = ld8<AUX_SC1>(wsr, wave == 7 ? WS_EPOCH : OOB, 0);
    xs0 = __builtin_amdgcn_raw_buffer_load_b16(rx, tid * 2, 0, 0); xs1 = __builtin_amdgcn_raw_buffer_load_b16(rx, tid * 2, 1024, 0);
    xr0 = ld16<0>(rx, part == 1 ? lane * 16 : OOB, 0); xr1 = ld16<0>(rx, part == 1 ? lane * 16 : OOB, 1024);
    tch1 = __builtin_amdgcn_raw_buffer_load_b32(mk_rsrc(w2 + (size_t)blk * 32 * 512, is_up ? 64u : 0u), tid == 384 ? 0 : OOB, 0, AUX_NT);
  };
  if (has_w) {
    if (modc) now = 1 << 30;
#pragma nounroll
    while (wleft > (int)(te0 >> 20)) { gate(); due += tauw - tau; piece(); }
    now = 0;
  }
  SB();
  u32 sot;
  {
    const u32 s1 = bl0 + lane - 1, s0 = s1 + (s1 >= 108 ? 4 : 0);
    u32 sb = s0 & 96u;
#pragma unroll
    for (int k = 0; k < 5; ++k) sb |= (s0 >> k & 1u) << (P.PM >> (4 * k) & 7);
    const u32 src = (sb >> 5 & 3u) | (u32)(b * 32 + head) << 2;
    sb ^= ((__builtin_popcount(src & P.M0) & 1) | (__builtin_popcount(src & P.M1) & 1) << 1 | (__builtin_popcount(src & P.M2) & 1) << 2 | (__builtin_popcount(src & P.M3) & 1) << 3 | (__builtin_popcount(src & P.M4) & 1) << 4) & (s1 >= 96 ? 7u : 31u);
    sot = (sb & 7u) << 10 | (u32)wave << 13 | (sb >> 3) << 16;
    sot = s1 >= 120 ? (s1 + 4) << 13 | (u32)wave << 10 : sot;
    sot = lane < nl ? sot : OOB;
    PIN(sot);
  }
  u32 tag = 0;
  u32x4 bf[8];
  auto wo_prep = [&](int s0 = 0, int s1 = 2) {
    const u32 wi_ = wimg + wave * 8192;
    const int q = (lane >> 2) & 3, pp = lane & 3;
#pragma unroll
    for (int s = s0; s < s1; ++s) {
#pragma unroll
      for (int t = 0; t < 4; ++t) {
        const int r0 = s * 32 + g * 8 + q, r1 = r0 + 4, ch = t * 2 + (pp >> 1);
        const u32x2 lo = tr_read(wi_ + r0 * 128 + ((ch ^ (r0 & 7)) * 16) + (pp & 1) * 8), hi = tr_read(wi_ + r1 * 128 + ((ch ^ (r1 & 7)) * 16) + (pp & 1) * 8);
        bf[s * 4 + t] = u32x4{lo[0], lo[1], hi[0], hi[1]};
      }
    }
  };
  auto gemv_go = [&](u32 hh) {
    sm.hb[tid] = (u16)hh; sm.hb[512 + tid] = (u16)(hh >> 16);
    WAVE_SYNC();
    const u32x4* a = (c & 11) == 0 ? reinterpret_cast<const u32x4*>(&sm.hb[(c >> 2) * 512 + wave * 64 + g * 8]) : &sm.zero[0];
    const u32x4 a0 = a[0], a1 = a[(c & 11) == 0 ? 4 : 0];
    const f32x4 z = {0.f, 0.f, 0.f, 0.f};
    f32x4 d[4];
#pragma unroll
    for (int t = 0; t < 4; ++t) d[t] = mfma16(a0, bf[t], z);
#pragma unroll
    for (int t = 0; t < 4; ++t) d[t] = mfma16(a1, bf[4 + t], d[t]);
    if (lane < 32) {
#pragma unroll
      for (int t = 0; t < 4; ++t) sm.r2[(t * 16 + c) * 20 + g * 8 + wave] = d[t][0];
    }
  };
  auto mod_stage = [&]() {
    wo_prep();
    gemv_go(pack_bf(silu_f(lo_f(xs0)), silu_f(lo_f(xs1))));
    __builtin_amdgcn_s_waitcnt(0xc07f);
    if (lane == 0) *LDSP(volatile u32, (u32)(uintptr_t)&sm.mflag[wave]) = 1u;
    if (wave == 7) {
#pragma nounroll
      for (int spin = 0; spin < SPIN_MAX; ++spin) {
        const u32 f = *LDSP(volatile u32, (u32)(uintptr_t)&sm.mflag[lane & 7]);
        if (all_ok(f != 0u)) break;
        __builtin_amdgcn_s_sleep(1);
      }
      CFENCE();
      st8_sc1(wsr, G_MOD + (blk * 64 + lane) * 8, pack_bf(sum8(&sm.r2[lane * 20]), sum8(&sm.r2[lane * 20 + 8])), tag);
    }
    STAMP(9);
  };
  if (lane < 4) sm.zero[lane] = u32x4{0u, 0u, 0u, 0u};
  if (part == 2 && has_w) {
    if (tid < 8) sm.mflag[tid] = 0u;
    LDS_BARRIER();
    if (__builtin_expect(modc, 0)) {
      small_ops();
      __builtin_amdgcn_raw_buffer_store_b128(u32x4{SENT, SENT, SENT, SENT}, wsr, tid < 320 ? G_P + wi * 5120 + tid * 16 : OOB, 0, AUX_SC1);
      asm volatile("s_waitcnt vmcnt(0)" ::: "memory");
      tag = next_tag(__builtin_amdgcn_readfirstlane(ep[0]));
      mod_stage();
      __builtin_amdgcn_s_barrier();
    }
  }
  SB();
  const int fd = wi * 8 + (lane & 7);
  const int ub = wave >> 2, ud = (wave & 3) * 128 + c * 8 + g * 2;
  u32x4 sg[3]; u32 sx2;
  auto side_loads = [&]() {
    const bool fin = part == 1 && wave == 0;
    sg[0] = ld16<AUX_SC1>(wsr, is_up ? G_MOD + (1024 + ud) * 8 : (fin ? G_MOD + (1024 + fd) * 8 : OOB), 0);
    sg[1] = ld16<AUX_SC1>(wsr, is_up ? G_MOD + (1536 + ud) * 8 : (fin ? G_MOD + (2560 + fd) * 8 : OOB), 0);
    sg[2] = ld16<AUX_SC1>(wsr, is_up ? G_MOD + (2048 + ud) * 8 : OOB, 0);
    sx2 = __builtin_amdgcn_raw_buffer_load_b32(mk_rsrc(x, (is_up || fin) ? 2048u : 0u), is_up ? ub * 1024 + ud * 2 : (g & 1) * 1024 + (fd >> 1) * 4, 0, 0);
  };
  auto side_pin = [&]() { PIN(sg[0]); PIN(sg[1]); PIN(sg[2]); PIN(sx2); };
  const u32 mlo = G_ML + (head * 2 + b) * 64 + (lane & 3) * 16;
  float fmrg = 0.f;
  bool fok = wave != 0;
  auto merge_factor = [&](const u32x4& ml) {
    const float mi = u2f(ml[0]);
    float M = fmaxf(mi, dpp<XOR1>(mi)); M = fmaxf(M, dpp<XOR2>(M));
    const float e = __expf(mi - M);
    float L = e * u2f(ml[2]);
    L += dpp<XOR1>(L); L += dpp<XOR2>(L);
    fmrg = rdl(e * __builtin_amdgcn_rcpf(L), part);
  };
  f32x2 ga = {0.f, 0.f}, xin = {0.f, 0.f}, cc1 = {0.f, 0.f};
  float Af = 0.f, Bf = 0.f;
  const int hpos = ub * 512 + ud;
  auto up_prep = [&]() {
    if (!all_ok((sg[0][1] == tag) & (sg[0][3] == tag) & (sg[1][1] == tag) & (sg[1][3] == tag) & (sg[2][1] == tag) & (sg[2][3] == tag))) gather16<3>(wsr, G_MOD + (1024 + ud) * 8, 512 * 8, tag, sg);
    ga = f32x2{ub ? hi_f(sg[0][0]) : lo_f(sg[0][0]), ub ? hi_f(sg[0][2]) : lo_f(sg[0][2])};
    cc1 = f32x2{rbf(1.f + (ub ? hi_f(sg[2][0]) : lo_f(sg[2][0]))), rbf(1.f + (ub ? hi_f(sg[2][2]) : lo_f(sg[2][2])))};
    xin = f32x2{lo_f(sx2), hi_f(sx2)};
    u32* hp = reinterpret_cast<u32*>(&sm.hb[hpos]);
    hp[512] = pack_bf(cc1[0], cc1[1]);
    hp[1024] = ub ? perm_hi(sg[1][0], sg[1][2]) : perm_lo(sg[1][0], sg[1][2]);
  };
  auto up_frags = [&]() {
    const int pp = lane & 3;
    const u32 a = wimg + (pp & 1) * 32768 + ((pp >> 1) * 32 + g * 8 + ((lane >> 2) & 3)) * 64 + (((wave >> 1) ^ ((g & 1) | (pp & 2))) * 16) + (wave & 1) * 8;
#pragma unroll
    for (int rb = 0; rb < 8; ++rb) {
      const u32x2 lo = tr_read(a + rb * 4096), hi = tr_read(a + rb * 4096 + 256);
      bf[rb] = u32x4{lo[0], lo[1], hi[0], hi[1]};
    }
  };
  const u32x4* ap = reinterpret_cast<const u32x4*>(&sm.hb[((lane >> 3) & 1) * 512 + (lane & 1) * 32 + g * 8]);
  const bool par = lane & 8;
  float l0 = 0.f, l1 = 0.f;
  u32x2 gmd[2], tbg;
  int t1 = 0;
#pragma unroll
  for (int i = NCMIN / 8; i < NL; ++i) kr[i] = u32x4{0u, 0u, 0u, 0u};
  sfor<0, NL>([&](auto ic) {
    constexpr int i = ic.v;
    if (i >= NCMIN / 8 && i >= nl) return false;
    if constexpr (i == 0) return true;
    const u32 so = __builtin_amdgcn_readlane(sot, i);
    gate();
    SB();
    kr[i] = ld16<AUX_NT>(rk, lane * 16, so);
    if constexpr (i == SMO) small_ops();
    if constexpr (i == TBL) tbg = ld8<AUX_SC1>(wsr, wave == 7 ? G_TB + xcd * 128 + (lane & 7) * 8 : OOB, 0);
    if constexpr (i == QP) { gmd[0] = ld8<AUX_SC1>(wsr, part == 1 ? G_MOD + tid * 8 : OOB, 0); gmd[1] = ld8<AUX_SC1>(wsr, part == 1 ? G_MOD + tid * 8 : OOB, 4096); }
    SB();
    SB();
    if (i >= WK) { WAIT_VM(WK); SB(); }
    if (i == 1) STAMP(25);
    if (i == 8) STAMP(12);
    if (i == 20) STAMP(21);
    if (i == 25) STAMP(22);
    if (i == TBP) {
      if (part == 3 && i8 == 0 && wave == 7) { tag = next_tag(__builtin_amdgcn_readfirstlane(ep[0])); if (lane < 8) st8_sc1(wsr, G_TB + lane * 128 + xcd * 8, t0 + (((tka - (u32)t64) * P.GA) >> 3) - P.GS, tag); }
      SB();
    }
    if (i == TBU) {
      if (wave == 7) {
        const bool ok = tbg[1] == tag;
        float lf = ok ? (float)min(max((int)(t0 - tbg[0]), -300), 300) : 0.f, nf = ok ? 1.f : 0.f;
        lf += dpp<XOR1>(lf); nf += dpp<XOR1>(nf); lf += dpp<XOR2>(lf); nf += dpp<XOR2>(nf); lf += dpp<XOR7>(lf); nf += dpp<XOR7>(nf);
        if (lane == 0) sm.bc = u32x2{tag, t0 - (u32)(int)(lf * __builtin_amdgcn_rcpf(fmaxf(nf, 1.f)))};
      }
      LDS_BARRIER();
      const u32x2 bc = sm.bc;
      tag = __builtin_amdgcn_readfirstlane(bc[0]);
      t1 = (__builtin_amdgcn_readfirstlane((int)(bc[1] - t0)) + TEND + P.X1) * 256;
      tau -= __builtin_amdgcn_readfirstlane((int)((float)(int)(t0 - bc[1]) * 256.f * __builtin_amdgcn_rcpf((float)(2 * nl - TBU - 3))));
      SB();
    }
    if (i == LNS) {
      tag = next_tag(__builtin_amdgcn_readfirstlane(ep[0]));
      if (part == 1) {
        float x0[8], x1[8], mb0, ri0, mb1, ri1;
#pragma unroll
        for (int k = 0; k < 4; ++k) { x0[2 * k] = lo_f(xr0[k]); x0[2 * k + 1] = hi_f(xr0[k]); x1[2 * k] = lo_f(xr1[k]); x1[2 * k + 1] = hi_f(xr1[k]); }
        ln_stats2(x0, x1, mb0, ri0, mb1, ri1);
        l0 = rbf(rbf(lo_f(xs0) - mb0) * ri0); l1 = rbf(rbf(lo_f(xs1) - mb1) * ri1);
      }
      PIN(l0); PIN(l1);
      SB();
    }
    if (i == WUSE) {
      PIN(tch1); PIN(tch2);
      if ((part == 1 || (part == 2 && has_w && !modc))) {
        WAIT_VM(WK); CFENCE();
        if (part == 2) mod_stage(); else wo_prep();
      }
#pragma unroll
      for (int k = 0; k < 8; ++k) PIN(bf[k]);
      SB();
    }
    if (i == QP + QD) {
      if (part == 1) {
        u32 gm[2] = {gmd[0][0], gmd[1][0]};
        if (!all_ok((gmd[0][1] == tag) & (gmd[1][1] == tag))) { STAMP(11); gather8<2>(wsr, G_MOD + tid * 8, 512 * 8, tag, gm); }
        STAMP(5);
        gemv_go(pack_bf(rbf(l0 * rbf(1.f + lo_f(gm[1]))) + lo_f(gm[0]), rbf(l1 * rbf(1.f + hi_f(gm[1]))) + hi_f(gm[0])));
      }
      SB();
    }
    if (i == QP + QD + 1) {
      if (part == 1) {
        LDS_BARRIER();
        if (wave == 0) {
          const int bb = lane >> 5, jq = lane & 31;
          st8_sc1(wsr, G_Q + (bb * 2048 + blk * 32 + jq) * 8, pack_bf(sum8(&sm.r2[2 * jq * 20 + bb * 8]), sum8(&sm.r2[(2 * jq + 1) * 20 + bb * 8])), tag);
        }
        STAMP(6);
      }
      SB();
    }
    return true;
  });
  PIN(sot);
  const u32 qoff = G_Q + ((b * 32 + head) * 64 + c * 4) * 8;
  u32x4 qg[2], fl2[3];
  u32 qq[4];
  constexpr int NB = (NL + 15) / 16;
  float xlo, sc[NB], pr[NB], m = 0.f;
  f32x4 acc = {0.f, 0.f, 0.f, 0.f};
  u32 pb[NB], pm[4], cpf;
  const u32 adst0 = lds_addr(lane < 32 ? &sm.red[wave][c * 8 + g * 4] : &sm.r2[(lane - 32) * 4]);
  u32 po, rdst, rsrc, xo;
  auto wo_addr = [&]() {
    po = G_P + (((c >> 2 & 1) * 32 + xcd * 4 + (wave >> 1)) * 4 + (c & 3)) * 256 + (wave & 1) * 128 + (c >> 3) * 64 + g * 16;
    rdst = lane < 32 ? (u32)(uintptr_t)&sm.red2[wave][0] + lane * 16 : (u32)(uintptr_t)&sm.r2[0] + (lane - 32) * 16;
    rsrc = (u32)(uintptr_t)&sm.red2[0][((wave & 1) * 16 + (lane & 15)) * 4 + (lane >> 4)];
    xo = (lane & 1) ? OOB : G_XP + xcd * 2048 + (wave & 1) * 1024 + (blk * 64 + lane) * 2;
    PIN(po); PIN(rdst); PIN(rsrc); PIN(xo);
  };
  u32 pso = wave == 0 ? pslot + lane * 4 : OOB;
  PIN(pso);
  const u32 nadd = (te0 >> 20) ? P.LW : (is_up ? P.LU : 0);
  u32 nto = tid == 0 ? G_NT + bid * 8 : OOB;
  PIN(nto);
  auto notices = [&](auto nc_, u32 off, u32x4 (&gn)[nc_.v]) -> int {
    constexpr int N = nc_.v;
#pragma nounroll
    for (int spin = 0; spin < SPIN_MAX; ++spin) {
      bool ok = true;
#pragma unroll
      for (int i = 0; i < N; ++i) ok &= (gn[i][1] == tag) & (gn[i][3] == tag);
      if (all_ok(ok)) break;
      __builtin_amdgcn_s_sleep(10);
      CFENCE();
#pragma unroll
      for (int i = 0; i < N; ++i) gn[i] = ld16<AUX_SC1>(wsr, off, i * 1024);
    }
    int mx = 0;
#pragma unroll
    for (int i = 0; i < N; ++i) mx = max(mx, max((int)(gn[i][0] - t0), (int)(gn[i][2] - t0)));
    return __builtin_amdgcn_readfirstlane((int)wave_max((float)min(mx, 1 << 20)));
  };
  const int side = nl >= SIDE + 13 ? SIDE + 8 : (nl >= SIDE + 9 ? SIDE + 4 : SIDE);
  sfor<0, NL + WV>([&](auto ic) {
    constexpr int v = ic.v;
    if (cons_end(v - 1) >= NCMIN / 8 && cons_end(v - 1) >= nl) return false;
    if constexpr (v > 0 && v < NL) {
      const u32 so = __builtin_amdgcn_readlane(sot, v);
      if constexpr (v >= 12) { if (v == nl - P.NE) tau = (tau * P.FE) >> 4; }
      if constexpr (v >= NCMIN / 8) { if (v < nl) gate(); } else gate();
      SB();
      vr[v] = ld16<AUX_NT>(rv, lane * 16, so);
      SB();
      SB();
    }
    if constexpr (v == NCMIN / 8 - 1) {
#pragma unroll
      for (int i = NCMIN / 8; i < NL; ++i) asm volatile("" : "=v"(vr[i]));
      asm volatile("" : "=v"(fl2[1]), "=v"(fl2[2]));
    }
    if constexpr (v == VQ - 4) { qg[0] = ld16<AUX_SC1>(wsr, qoff, 0); qg[1] = ld16<AUX_SC1>(wsr, qoff + 16, 0); }
    if constexpr (v == VSIDE) side_loads();
    if constexpr (v == CPF) {
      u64 pc; u32 dlt, len;
      asm volatile("s_getpc_b64 %0\n.Lcpf_here:\n\ts_mov_b32 %1, .Ltail_start-.Lcpf_here\n\ts_mov_b32 %2, .Lend_a-.Ltail_start\n\ts_max_i32 %2, %2, .Lend_b-.Ltail_start\n\ts_max_i32 %2, %2, .Lend_c-.Ltail_start\n\ts_max_i32 %2, %2, .Lend_d-.Ltail_start\n\ts_max_i32 %2, %2, .Lend_e-.Ltail_start" : "=s"(pc), "=s"(dlt), "=s"(len) :: "scc");
      pc += dlt;
      const rsrc_t rc = mk_rsrc((const void*)pc, (part == 3 && i8 == 1) ? len : 0u);
      cpf = __builtin_amdgcn_raw_buffer_load_b32(rc, tid * 64, 0, 0);
    }
    if constexpr (v == CPF + WV + 1) PIN(cpf);
    if constexpr (v == SIDE || v == SIDE + 4 || v == SIDE + 8) fl2[(v - SIDE) / 4] = ld16<AUX_SC1>(wsr, (wave == 0 && v == side) ? mlo : OOB, 0);
    SB();
    if constexpr (v < WV) { WAIT_VM(WK); SB(); } else if constexpr (v < NL && cons_end(v) < v - WV + 1) { WAIT_VM(WV); SB(); }
    if constexpr (v == VQ) {
      if (!all_ok((qg[0][1] == tag) & (qg[0][3] == tag) & (qg[1][1] == tag) & (qg[1][3] == tag))) { STAMP(20); gather16<2>(wsr, qoff, 16, tag, qg); }
      STAMP(1); WSTAMP(1);
      qq[0] = qg[0][0]; qq[1] = qg[0][2]; qq[2] = qg[1][0]; qq[3] = qg[1][2];
    }
    if constexpr (v >= VQ && v < VS) {
      constexpr int sl = v - VQ, bt = sl >> 1;
      float xs[8];
#pragma unroll
      for (int i = 0; i < 8; ++i) {
        float d = 0.f;
        if (sl * 8 + i < NL) {
#pragma unroll
          for (int k = 0; k < 4; ++k) d = dot2(kr[sl * 8 + i][k], qq[k], d);
        }
        xs[i] = d;
      }
#pragma unroll
      for (int h = 0; h < 4; ++h) xs[h] = sel_add<XOR7>(xs[h], xs[h + 4], c & 4);
#pragma unroll
      for (int h = 0; h < 2; ++h) xs[h] = sel_add<XOR2>(xs[h], xs[h + 2], c & 2);
      xs[0] = sel_add<XOR1>(xs[0], xs[1], c & 1);
      if constexpr ((sl & 1) || sl == NSL - 1) {
        const float t = (sl & 1) ? sel_add<XOR8>(xlo, xs[0], c & 8) : xs[0] + dpp<XOR8>(xs[0]);
        const float s = rbf(rbf(t) * 0.08838834764831845f);
        sc[bt] = (bt * 16 + c < nl) ? s : -__builtin_inff();
        PIN(sc[bt]);
      } else {
        xlo = xs[0];
        PIN(xlo);
      }
    }
    if constexpr (v == VS) {
      float mloc = sc[0];
#pragma unroll
      for (int bt = 1; bt < NB; ++bt) mloc = fmaxf(mloc, sc[bt]);
      m = fmaxf(wave_max(mloc), -3.0e38f);
      float ls = 0.f;
#pragma unroll
      for (int bt = 0; bt < NB; ++bt) { pr[bt] = __expf(sc[bt] - m); ls += pr[bt]; }
      const float l = wave_sum(ls);
      if (lane == 0) { sm.ml[wave][0] = m; sm.ml[wave][1] = l; }
      STAMP(2); WSTAMP(2);
    }
    sfor<cons_end(v - 1), cons_end(v)>([&](auto jc) {
      constexpr int i = jc.v;
      if (i < NCMIN / 8 || i < nl) {
        const u32 pv = __float_as_uint(bcast16(u2f(pb[i >> 4]), i & 15));
        acc = mfma16(u32x4{pv & pm[0], pv & pm[1], pv & pm[2], pv & pm[3]}, vr[i], acc);
      }
      PIN(acc);
      return true;
    });
    if constexpr (v == MLS - 1) { side_pin(); if (is_up) up_prep(); PIN(ga); PIN(xin); PIN(cc1); }
    if constexpr (v == UPREP - 1) wo_addr();
    if constexpr (v == UPREP) {
#pragma unroll
      for (int k = 0; k < 8; ++k) asm volatile("" : "=v"(bf[k]));
      if (is_up) up_frags();
#pragma unroll
      for (int k = 0; k < 8; ++k) PIN(bf[k]);
    }
    if constexpr (v == UPREP + 1) {
      if (is_up) {
        const u32x4* a1 = ap + 128 + ((lane >> 1) & 1) * 128;
        f32x4 d0 = {0.f, 0.f, 0.f, 0.f}, d1 = {0.f, 0.f, 0.f, 0.f};
#pragma unroll
        for (int rb = 0; rb < 4; ++rb) { d0 = mfma16(a1[rb * 8], bf[rb], d0); d1 = mfma16(a1[(rb + 4) * 8], bf[rb + 4], d1); }
        Af = par ? d0[1] + d1[1] : d0[0] + d1[0]; Bf = par ? d0[3] + d1[3] : d0[2] + d1[2];
        Af += dpp<XOR8>(Af); Bf += dpp<XOR8>(Bf);
      }
      PIN(Af); PIN(Bf);
    }
    if constexpr (v == SIDE + WV + 1 || v == SIDE + WV + 5 || v == SIDE + WV + 9) {
      if (wave == 0 && v == side + WV + 1) {
        const u32x4 ml = fl2[(v - SIDE - WV - 1) / 4];
        fok = all_ok((ml[1] == tag) & (ml[3] == tag));
        merge_factor(ml);
      }
    }
    if (v % 6 == 4 && v >= 10 && v < 30) STAMP(15 + v / 6);
    if constexpr (v == MLS) {
      LDS_BARRIER();
      float mm[8], ll[8];
#pragma unroll
      for (int w = 0; w < 8; ++w) { const f32x2 t = *reinterpret_cast<const f32x2*>(&sm.ml[w][0]); mm[w] = t[0]; ll[w] = t[1]; }
      float Mwg = mm[0], Lwg = 0.f;
#pragma unroll
      for (int w = 1; w < 8; ++w) Mwg = fmaxf(Mwg, mm[w]);
      if (wave == 0) {
#pragma unroll
        for (int w = 0; w < 8; ++w) Lwg = fmaf(__expf(mm[w] - Mwg), ll[w], Lwg);
        if (lane == 0) st16_sc1(wsr, G_ML + ((head * 2 + b) * 4 + part) * 16, __float_as_uint(Mwg), __float_as_uint(Lwg), tag);
      }
      const float fw = __expf(m - Mwg);
#pragma unroll
      for (int bt = 0; bt < NB; ++bt) { pb[bt] = pack_bf(pr[bt] * fw, pr[bt] * fw); PIN(pb[bt]); }
#pragma unroll
      for (int k = 0; k < 4; ++k) { pm[k] = (c >> 1) == k ? ((c & 1) ? 0xffff0000u : 0x0000ffffu) : 0u; PIN(pm[k]); }
    }
    SB();
    return true;
  });
  asm volatile(".Ltail_start:");
  *LDSP(f32x4, adst0) = acc;
  STAMP(14); WSTAMP(14);
  if (!fok) {
    u32x4 ml;
#pragma nounroll
    for (int spin = 0; spin < SPIN_MAX; ++spin) {
      CFENCE();
      ml = ld16<AUX_SC1>(wsr, mlo, 0);
      if (all_ok((ml[1] == tag) & (ml[3] == tag))) break;
    }
    merge_factor(ml);
  }
  STAMP(15);
  LDS_BARRIER();
  STAMP(3);
  {
    u32 pw = 0;
    if (wave == 0) {
      f32x2 t[8];
#pragma unroll
      for (int w = 0; w < 8; ++w) t[w] = *reinterpret_cast<const f32x2*>(&sm.red[w][2 * lane]);
      const f32x2 o = ((t[0] + t[1]) + (t[2] + t[3])) + ((t[4] + t[5]) + (t[6] + t[7]));
      pw = min(pack_bf(o[0] * fmrg, o[1] * fmrg), SENT - 1u);
    }
    __builtin_amdgcn_raw_buffer_store_b32(pw, wsr, pso, 0, AUX_SC1);
    st8_sc1(wsr, nto, (u32)wall_clock64() + nadd, tag);
    SB();
#pragma unroll
    for (int i = NCMIN / 8; i < NL; ++i) PIN(vr[i]);
    PIN(fl2[0]); PIN(fl2[1]); PIN(fl2[2]);
  }
  STAMP(4);

  if (part == 2) FINISH();
  if (part == 0) {
    __builtin_amdgcn_s_barrier();
#pragma nounroll
    while (wleft > 0) piece();
    until(t1);
    STAMP(11);
    CFENCE();
    u32x4 og[2];
    og[0] = ld16<AUX_SC1>(wsr, po, 0);
    SB();
    WAIT_VM(1); CFENCE();
    wo_prep(0, 2);
#pragma unroll
    for (int k = 0; k < 8; ++k) PIN(bf[k]);
    SB();
    f32x4 d[4];
    auto wo_mm = [&]() {
      const f32x4 z = {0.f, 0.f, 0.f, 0.f};
#pragma unroll
      for (int t = 0; t < 4; ++t) { d[t] = mfma16(og[0], bf[t], z); og[1][t] = __float_as_uint(dpp<XOR8>(u2f(og[0][t]))); }
      SB();
#pragma unroll
      for (int t = 0; t < 4; ++t) d[t] = mfma16(og[1], bf[4 + t], d[t]);
    };
    auto p_bad = [&]() { return !all_ok(max(max(og[0][0], og[0][1]), max(og[0][2], og[0][3])) != SENT); };
    wo_mm();
    if (__builtin_expect(p_bad(), 0)) {
#pragma nounroll
      for (int spin = 0; spin < SPIN_MAX; ++spin) {
        __builtin_amdgcn_s_sleep(1);
        CFENCE();
        og[0] = ld16<AUX_SC1>(wsr, po, 0);
        if (!p_bad()) break;
      }
      wo_mm();
      SB();
      asm volatile(".Lend_d:");
    }
    STAMP(5); WSTAMP(5);
    {
      SB();
      f32x4 y; f32x2 h[4];
#pragma unroll
      for (int t = 0; t < 4; ++t) { h[t] = d[t].xy + d[t].zw; PIN(h[t]); }
#pragma unroll
      for (int t = 0; t < 4; ++t) { y[t] = h[t][0] + h[t][1]; PIN(y[t]); }
      *LDSP(f32x4, rdst) = y;
    }
    STAMP(16);
    LDS_BARRIER();
    STAMP(17);
    if (wave < 2) {
      f32x2 v[4];
#pragma unroll
      for (int w = 0; w < 4; ++w) v[w] = f32x2{LDSP(float, rsrc)[w * 256], LDSP(float, rsrc)[w * 256 + 128]};
      const f32x2 h = (v[0] + v[1]) + (v[2] + v[3]);
      const float y = h[0] + h[1];
      __builtin_amdgcn_raw_buffer_store_b32(min(pack_bf(y, dpp<XOR1>(y)), SENT - 1u), wsr, xo, 0, AUX_SC1);
      asm volatile(".Lend_a:");
    }
    STAMP(6);
    FINISH();
  }
  if (part == 3) {
    if (!has_w) FINISH();
    u32x4 b2[4];
    {
      __builtin_amdgcn_s_barrier();
      const rsrc_t r2 = mk_rsrc(w2 + (size_t)(blk * 32 + wave * 4) * 512, 4096u);
      const u32 l2 = (u32)(uintptr_t)(&sm.w2b[wave * 4][0]);
#pragma unroll
      for (int n = 0; n < 4; ++n) dma16(r2, (lane ^ (n * 2) ^ ((wave & 2) << 2)) * 16, n * 1024, l2 + n * 1024);
    }
    const u32 tdst = lds_addr(&sm.hb[hpos]);
    const u32 sdst = lds_addr(&sm.stat[wave][lane]);
    const u32 ssrc = lds_addr(&sm.stat[(g >> 1) * 4 + ((lane >> 1) & 3)][(lane & 1) * 16]);
    const u32 adst = lds_addr((c < 4 && (g & 1) == 0) ? &sm.actl[g >> 1][wave * 4 + c] : reinterpret_cast<u16*>(&sm.dummy[lane]));
    const u32 asrc = lds_addr(&sm.actl[lane & 1][g * 8]);
    const u32 apa = lds_addr(&sm.hb[(c >> 3) * 512 + (c & 3) * 64 + ((c >> 2) & 1) * 32 + g * 8]);
    u32 xoff = G_XP + g * 2048 + ub * 1024 + (wave & 3) * 256 + c * 16;
    u32x4 one;
#pragma unroll
    for (int k = 0; k < 4; ++k) one[k] = ((c & 2) == 0 && (c >> 2) == k) ? ((c & 1) ? 0x3f800000u : 0x00003f80u) : 0u;
    PIN(one);
    u32 ooff = G_OP + (blk * 512 + tid) * 8;
    float Afp = Af, Bfp = Bf;
    PIN(xoff); PIN(ooff); PIN(Afp); PIN(Bfp);
    PIN(ga); PIN(xin); PIN(cc1);
    u32 w2a = (u32)(uintptr_t)(&sm.w2b[0][0]) + (g * 8 + ((lane >> 2) & 3)) * 1024 + (wave ^ (g & 1)) * 128 + (lane & 15) * 8;
    PIN(w2a);
    if (wave == 1) {
      due = t1 + P.N1 * 256; gate();
      u32x4 gn[2] = {ld16<AUX_SC1>(wsr, G_NT + lane * 16, 0), ld16<AUX_SC1>(wsr, G_NT + lane * 16, 1024)};
      const int tn = notices(IC<2>{}, G_NT + lane * 16, gn) * 256;
      due = (tn > t1 + 2 * 256 ? max(tn, t1 + P.L0 * 256) + P.L1 * 256 : t1) + P.X2 * 256;
      until(due);
    }
    asm volatile("s_waitcnt vmcnt(0)" ::: "memory");
    STAMP(9);
    __builtin_amdgcn_s_barrier();
    STAMP(11);
    CFENCE();
    u32x4 xg[2] = {ld16<AUX_SC1>(wsr, xoff, 0), ld16<AUX_SC1>(wsr, xoff, 8192)};
    SB();
#pragma unroll
    for (int t = 0; t < 4; ++t) {
      const u32x2 lo = tr_read(w2a ^ (t * 32)), hi = tr_read((w2a ^ (t * 32)) + 4096);
      b2[t] = u32x4{lo[0], lo[1], hi[0], hi[1]};
    }
#pragma unroll
    for (int t = 0; t < 4; ++t) PIN(b2[t]);
    SB();
    const f32x4 z4 = {0.f, 0.f, 0.f, 0.f};
    auto xp_bad = [&]() { return !all_ok(max(max(max(xg[0][0], xg[0][1]), max(xg[0][2], xg[0][3])), max(max(xg[1][0], xg[1][1]), max(xg[1][2], xg[1][3]))) != SENT); };
    f32x4 v = mfma16(one, xg[1], mfma16(one, xg[0], z4));
    if (__builtin_expect(xp_bad(), 0)) {
#pragma nounroll
      for (int spin = 0; spin < SPIN_MAX; ++spin) {
        __builtin_amdgcn_s_sleep(1);
        CFENCE();
        xg[0] = ld16<AUX_SC1>(wsr, xoff, 0); xg[1] = ld16<AUX_SC1>(wsr, xoff, 8192);
        if (!xp_bad()) break;
      }
      v = mfma16(one, xg[1], mfma16(one, xg[0], z4));
      SB();
      asm volatile(".Lend_e:");
    }
    STAMP(5); WSTAMP(5);
    {
      const float a0 = v[0], a1 = v[1];
      const float x10 = fmaf(ga[0], a0, xin[0]), x11 = fmaf(ga[1], a1, xin[1]);
      *LDSP(u32, tdst) = pack_bf(x10 * cc1[0], x11 * cc1[1]);
      float u = swap32_add(x10 + x11, fmaf(x10, x10, x11 * x11));
      u += dpp<XOR1>(u); u += dpp<XOR2>(u); u += dpp<XOR7>(u); u += dpp<XOR8>(u);
      *LDSP(float, sdst) = u;
    }
    STAMP(16);
    LDS_BARRIER();
    STAMP(17);
    {
      const f32x4 z = {0.f, 0.f, 0.f, 0.f};
      const u32x4 af0 = LDSP(u32x4, apa)[0], af1 = LDSP(u32x4, apa)[32];
      float s = LDSP(float, ssrc)[0], q = LDSP(float, ssrc)[32];
      SB();
      f32x4 d[4];
#pragma unroll
      for (int j = 0; j < 4; ++j) d[j] = mfma16(af0, bf[j], z);
#pragma unroll
      for (int j = 0; j < 4; ++j) d[j] = mfma16(af1, bf[4 + j], d[j]);
      s += dpp<XOR1>(s); q += dpp<XOR1>(q); s += dpp<XOR2>(s); q += dpp<XOR2>(q); s += dpp<XOR7>(s); q += dpp<XOR7>(q);
      const float mean = s * (1.f / 512.f);
      const float var = fmaf(q, 1.f / 512.f, -mean * mean);
      const float ri = __builtin_amdgcn_rsqf(fmaxf(var, 0.f) + 1e-5f);
      const float cf = fmaf(-ri * mean, Afp, Bfp);
      float y = (d[0][0] + d[1][1]) + (d[2][2] + d[3][3]);
      y = __builtin_bit_cast(float, __builtin_amdgcn_update_dpp(__builtin_bit_cast(int, y), __builtin_bit_cast(int, y), XOR8, 0xa, 0xf, false));
      y = swap16_add(y, y);
      y = fmaf(y, ri, cf);
      const float up = dpp<0x104>(y);
      *LDSP(u16, adst) = (u16)pack_bf(silu_f(y) * up, 0.f);
    }
    STAMP(18);
    LDS_BARRIER();
    STAMP(10);
    {
      const u32x4 af = *LDSP(u32x4, asrc);
      const f32x4 z = {0.f, 0.f, 0.f, 0.f};
      const f32x4 d0 = mfma16(af, b2[0], z), d1 = mfma16(af, b2[1], z), d2 = mfma16(af, b2[2], z), d3 = mfma16(af, b2[3], z);
      const bool g1 = g & 1, g2 = g & 2;
      const float e0 = g1 ? d1[0] : d0[0], e1 = g1 ? d1[1] : d0[1], f0 = g1 ? d3[0] : d2[0], f1 = g1 ? d3[1] : d2[1];
      st8_sc1(wsr, ooff, pack_bf(g2 ? f0 : e0, g2 ? f1 : e1), tag);
    }
    asm volatile(".Lend_b:");
    STAMP(6);
    FINISH();
  }
  if (wave == 0) {
    const int d = fd, ul = lane >> 3;
    u32 gm[2] = {sg[0][0], sg[1][0]};
    if (!all_ok((sg[0][1] == tag) & (sg[1][1] == tag))) gather8<2>(wsr, G_MOD + (1024 + d) * 8, 1536 * 8, tag, gm);
    u32 xa0, xa1;
    {
      due = t1 + P.N2 * 256; gate();
      u32x4 gn[2] = {ld16<AUX_SC1>(wsr, G_NT + lane * 16, 0), ld16<AUX_SC1>(wsr, G_NT + lane * 16, 1024)};
      const int tn = notices(IC<2>{}, G_NT + lane * 16, gn) * 256;
      due = (tn > t1 + 2 * 256 ? max(tn, t1 + P.L0 * 256) + P.L1 * 256 : t1) + P.X3 * 256;
      until(due);
    }
#pragma nounroll
    for (int spin = 0; spin < SPIN_MAX; ++spin) {
      CFENCE();
      xa0 = __builtin_amdgcn_raw_buffer_load_b32(wsr, G_XP + ul * 2048 + (d >> 1) * 4, 0, AUX_SC1);
      xa1 = __builtin_amdgcn_raw_buffer_load_b32(wsr, G_XP + ul * 2048 + (d >> 1) * 4, 1024, AUX_SC1);
      if (all_ok(max(xa0, xa1) != SENT)) break;
      __builtin_amdgcn_s_sleep(1);
    }
    float a0 = (d & 1) ? hi_f(xa0) : lo_f(xa0), a1 = (d & 1) ? hi_f(xa1) : lo_f(xa1);
    a0 += dpp<XOR8>(a0); a1 += dpp<XOR8>(a1);
    a0 = swap16_add(a0, a0); a1 = swap16_add(a1, a1);
    a0 = swap32_add(a0, a0); a1 = swap32_add(a1, a1);
    const bool hb = g & 1;
    float xv = rbf(((d & 1) ? hi_f(sx2) : lo_f(sx2)) + rbf((hb ? hi_f(gm[0]) : lo_f(gm[0])) * rbf(hb ? a1 : a0)));
    float gv = hb ? hi_f(gm[1]) : lo_f(gm[1]);
    PIN(xv); PIN(gv);
    if (wi == 0 && lane == 0) __builtin_amdgcn_raw_buffer_store_b32(tag, wsr, WS_EPOCH, 0, AUX_SC1);
    STAMP(10);
    due = max(due + P.X4 * 256, (int)(((u32)wall_clock64() - t0) << 8) + P.GX * 256);
    const bool last = ul + 40 < NUP;
    u32 o0 = G_OP + (ul * 512 + d) * 8, o5 = last ? o0 : OOB, tg5 = last ? tag : 0u;
    const rsrc_t rout = mk_rsrc(out, 2048u);
    u32 oo = (lane & 40) == 0 ? (g * 512 + d) * 2 : OOB;
    const u32 pk = c == 0 ? 0x00003f80u : (c == 4 ? 0x3f800000u : 0u);
    u32x4 one = {pk, 0u, pk, 0u};
    PIN(o0); PIN(o5); PIN(tg5); PIN(oo); PIN(one);
    __builtin_amdgcn_raw_buffer_store_b16((u16)pack_bf(xv, 0.f), rout, oo, 0, AUX_SC1);
    until(due);
    f32x4 sv;
#pragma nounroll
    for (int spin = 0; spin < SPIN_MAX; ++spin) {
      CFENCE();
      u32x2 og[6];
#pragma unroll
      for (int k = 0; k < 5; ++k) og[k] = ld8<AUX_SC1>(wsr, o0, k * 32768);
      og[5] = ld8<AUX_SC1>(wsr, o5, 5 * 32768);
      sv = f32x4{0.f, 0.f, 0.f, 0.f};
#pragma unroll
      for (int k = 0; k < 3; ++k) sv = mfma16(one, u32x4{og[2 * k][0], og[2 * k][1], og[2 * k + 1][0], og[2 * k + 1][1]}, sv);
      const u32 bad = ((og[0][1] ^ tag) | (og[1][1] ^ tag) | (og[2][1] ^ tag)) | ((og[3][1] ^ tag) | (og[4][1] ^ tag) | (og[5][1] ^ tg5));
      PIN(sv);
      if (all_ok(bad == 0u)) break;
      __builtin_amdgcn_s_sleep(1);
    }
    STAMP(7);
    const float s = sv[0] + dpp<XOR8>(sv[0]);
    __builtin_amdgcn_raw_buffer_store_b16((u16)pack_bf(fmaf(gv, s, xv), 0.f), rout, oo, 0, AUX_SC1);
    asm volatile(".Lend_c:");
  }
  FINISH();
}

extern "C" void kernel_launch(void* const* d_in, const int* in_sizes, int n_in,
                              void* d_out, int out_size, void* d_ws, size_t ws_size,
                              hipStream_t stream) {
  fused_kernel<<<256, 512, 0, stream>>>((const u16*)d_in[1], (const u16*)d_in[2], (const u16*)d_in[4], (const u16*)d_in[3], (const u16*)d_in[10], (const u16*)d_in[6], (const u16*)d_in[7],
                                        (unsigned char*)d_ws, (const u16*)d_in[0], (const u16*)d_in[9], (const u16*)d_in[8], (u16*)d_out);
}
```

```cpp
#include <hip/hip_runtime.h>
#include <stdint.h>

typedef unsigned int u32;
typedef unsigned short u16;
typedef unsigned long long u64;
typedef __attribute__((ext_vector_type(4))) unsigned u32x4;
typedef __attribute__((ext_vector_type(2))) unsigned u32x2;
typedef __attribute__((ext_vector_type(2))) __bf16 bf16x2;
typedef __attribute__((ext_vector_type(4))) float f32x4;
typedef __attribute__((ext_vector_type(2))) float f32x2;
typedef __amdgpu_buffer_rsrc_t rsrc_t;

constexpr int NH = 32, SK = 16384, DH = 128, WIN = 4096, FF = 1376;
struct Prm { int E0, ES, E1, E2, E2C, EU, TEND, DT_UP, L0, L1, X1, X2, X3, X4, NE, FE, M0, M1, M2, M3, M4, GS, GX, N1, N2, GA, WD, DT_WO, LW, LU, EW, PM, NF; };
constexpr Prm PRM0 = {32, 24, 32, 32, 56, 64, 2010, 60, 30, 25, 92, 86, 131, 88, 4, 12, 5, 8, 21, 4, 32, 0, 56, 28, 70, 0, 4, 100, 45, 15, 8, 0x24310, 1};
constexpr int NL = 37;
constexpr int NCMIN = 192;
constexpr int WK = 8, WV = 5;
constexpr int WUSE = 12, LNS = 7;
constexpr int QP = 14, QD = 4;
constexpr int VQ = 6;
constexpr int NSL = (NL + 7) / 8;
constexpr int VS = VQ + NSL;
constexpr int MLS = VS + 2;
constexpr int VSIDE = MLS - 5, UPREP = MLS + 8;
constexpr int SIDE = NCMIN / 8 - 4;
constexpr int CPF = 18;
constexpr int SMO = 1;
constexpr int TBP = 4, TBL = 11, TBU = 15;
constexpr int cons_end(int v) {
  const int a = v - WV + 1, c = (v - MLS) * 4;
  const int m = a < c ? a : c;
  return m < 0 ? 0 : (m > NL ? NL : m);
}
struct Tab { u32 e[512]; };
constexpr Tab make_tab(const Prm& P) {
  Tab T{};
  for (int bid = 0; bid < 256; ++bid) {
    const int xcd = bid & 7, part = bid >> 4 & 3, i8 = (bid >> 3 & 1) | bid >> 6 << 1, wi = i8 * 8 + xcd;
    const bool has_mod = wi < 48, has_up = wi >= 21;
    const int cb0 = (i8 + 5) & 7, cb1 = (i8 + 4) & 7, ms = wi - 20;
    const int mi = wi < 8 ? wi : (wi < 16 ? 8 + ((xcd + 4) & 7) : (wi < 20 ? (wi - 14) * 8 + 3 : 16 + ms / 7 * 8 + ms % 7 + (ms % 7 >= 3 ? 1 : 0)));
    const int ui = xcd * 5 + (xcd > 5 ? xcd - 5 : 0) + (i8 >= 3 ? (xcd >= 5 ? 1 : 0) + i8 - 3 : 0);
    const int wd = cb0 == 3 ? 0 : P.WD;
    const int e[4] = {P.E0 - 4 * wd + (cb0 == 3 ? P.ES : 0), P.E1 + (cb1 == 3 ? P.ES : 0), has_mod ? (wi < 16 ? P.E2C : P.E2) + ((mi & 7) == 3 ? P.ES : 0) : 0, has_up ? P.EU : 0};
    const int ew = wd ? P.EW : 0, pa = (1024 + e[0] + ew + e[1] + e[2] + e[3]) / 4;
    int c[5] = {0, 0, 0, 0, 124};
    for (int k = 0; k < 3; ++k) c[k + 1] = c[k] + (pa - e[k] - (k == 0 ? ew : 0) + 4) / 8 - 1;
    const int nl = c[part + 1] - c[part] + 1, wst = e[part] / 4;
    const bool has_w = part < 2 || (part == 2 ? has_mod : has_up);
    const int blk = !has_w ? 0 : (part == 0 ? cb0 : (part == 1 ? xcd * 8 + cb1 : (part == 2 ? mi : ui)));
    const int tau = (part == 3 && has_up ? P.TEND - P.DT_UP : (part == 0 && wd ? P.TEND - P.DT_WO : P.TEND)) * 1024 / (4 * (2 * nl + wst - 2) + P.NF);
    T.e[2 * bid] = (u32)(c[part] | nl << 7 | blk << 13 | (has_w ? 1 : 0) << 19 | (part == 0 ? wd : 0) << 20);
    T.e[2 * bid + 1] = (u32)(tau | (tau * wst / (part == 0 && wd < 8 ? 8 - wd : 8)) << 16);
  }
  return T;
}
constexpr int step_range(const Prm& P, bool mx_) {
  int mx = 0, mn = 1 << 20;
  const Tab T = make_tab(P);
  for (int bid = 0; bid < 256; ++bid) { const int n = T.e[2 * bid] >> 7 & 63; mx = n > mx ? n : mx; mn = n < mn ? n : mn; }
  return mx_ ? mx : mn;
}
extern "C" __attribute__((used, visibility("protected"))) __constant__ const Tab wg_tab = make_tab(PRM0);
static_assert(step_range(PRM0, true) <= NL && step_range(PRM0, false) >= NCMIN / 8, "a wave's key range does not fit its load steps");
static_assert(cons_end(SIDE + WV + 1) <= NCMIN / 8 && cons_end(UPREP) <= NCMIN / 8 && SIDE > MLS + 3, "steps every wave runs");
constexpr int NUP = FF / 32;
constexpr int SPIN_MAX = 1 << 17;

constexpr u32 WS_EPOCH = 0;
constexpr u32 G_TB = 1024;
constexpr u32 G_NT = 2048;
constexpr u32 G_MOD = 4096;
constexpr u32 G_Q = G_MOD + 3072 * 8;
constexpr u32 G_ML = G_Q + 4096 * 8;
constexpr u32 G_P = G_ML + 256 * 16;
constexpr u32 G_XP = G_P + 256 * 256;
constexpr u32 G_OP = G_XP + 8 * 512 * 4;
constexpr u32 WS_END = G_OP + 43 * 512 * 8;
constexpr u32 SENT = 0xffffffffu;
constexpr u32 OOB = 0x40000000u;
#define WST 0

constexpr int AUX_NT = 2, AUX_SC1 = 16;

__device__ __forceinline__ float lo_f(u32 v) { return __uint_as_float(v << 16); }
__device__ __forceinline__ float hi_f(u32 v) { return __uint_as_float(v & 0xffff0000u); }
__device__ __forceinline__ float rbf(float x) { return (float)(__bf16)x; }
__device__ __forceinline__ u32 pack_bf(float lo, float hi) {
  bf16x2 r; r[0] = (__bf16)lo; r[1] = (__bf16)hi; return __builtin_bit_cast(u32, r);
}
__device__ __forceinline__ float dot2(u32 k, u32 q, float acc) {
  return __builtin_amdgcn_fdot2_f32_bf16(__builtin_bit_cast(bf16x2, k), __builtin_bit_cast(bf16x2, q), acc, false);
}
template <int CTRL> __device__ __forceinline__ float dpp(float x) {
  return __builtin_bit_cast(float, __builtin_amdgcn_mov_dpp(__builtin_bit_cast(int, x), CTRL, 0xf, 0xf, true));
}
constexpr int XOR1 = 0xB1, XOR2 = 0x4E, XOR7 = 0x141, XOR8 = 0x128;
__device__ __forceinline__ float swap32_add(float a, float b) {
  auto t = __builtin_amdgcn_permlane32_swap(__float_as_uint(a), __float_as_uint(b), false, false);
  return __uint_as_float(t[0]) + __uint_as_float(t[1]);
}
__device__ __forceinline__ float swap16_add(float a, float b) {
  auto t = __builtin_amdgcn_permlane16_swap(__float_as_uint(a), __float_as_uint(b), false, false);
  return __uint_as_float(t[0]) + __uint_as_float(t[1]);
}
__device__ __forceinline__ float wave_sum(float x) {
  x += dpp<XOR1>(x); x += dpp<XOR2>(x); x += dpp<XOR7>(x); x += dpp<XOR8>(x);
  x = swap16_add(x, x);
  return swap32_add(x, x);
}
__device__ __forceinline__ float wave_max(float x) {
  x = fmaxf(x, dpp<XOR1>(x)); x = fmaxf(x, dpp<XOR2>(x)); x = fmaxf(x, dpp<XOR7>(x)); x = fmaxf(x, dpp<XOR8>(x));
  auto s = __builtin_amdgcn_permlane16_swap(__float_as_uint(x), __float_as_uint(x), false, false);
  x = fmaxf(__uint_as_float(s[0]), __uint_as_float(s[1]));
  auto t = __builtin_amdgcn_permlane32_swap(__float_as_uint(x), __float_as_uint(x), false, false);
  return fmaxf(__uint_as_float(t[0]), __uint_as_float(t[1]));
}
template <int CTRL> __device__ __forceinline__ float sel_add(float lo, float hi, bool up) {
  const float t1 = lo + dpp<CTRL>(lo), t2 = hi + dpp<CTRL>(hi);
  return up ? t2 : t1;
}
template <int I> __device__ __forceinline__ float bc16(float x) {
  return __builtin_bit_cast(float, __builtin_amdgcn_update_dpp(0, __builtin_bit_cast(int, x), 0x150 + I, 0xf, 0xf, false));
}
__device__ __forceinline__ float bcast16(float x, int i) {
  switch (i) {
    case 0: return bc16<0>(x); case 1: return bc16<1>(x); case 2: return bc16<2>(x); case 3: return bc16<3>(x);
    case 4: return bc16<4>(x); case 5: return bc16<5>(x); case 6: return bc16<6>(x); case 7: return bc16<7>(x);
    case 8: return bc16<8>(x); case 9: return bc16<9>(x); case 10: return bc16<10>(x); case 11: return bc16<11>(x);
    case 12: return bc16<12>(x); case 13: return bc16<13>(x); case 14: return bc16<14>(x); default: return bc16<15>(x);
  }
}
__device__ __forceinline__ float silu_f(float x) { return x * __builtin_amdgcn_rcpf(1.f + __expf(-x)); }

template <int AUX> __device__ __forceinline__ u32x4 ld16(rsrc_t r, u32 voff, u32 soff) {
  return __builtin_amdgcn_raw_buffer_load_b128(r, voff, soff, AUX);
}
template <int AUX> __device__ __forceinline__ u32x2 ld8(rsrc_t r, u32 voff, u32 soff) {
  return __builtin_amdgcn_raw_buffer_load_b64(r, voff, soff, AUX);
}
__device__ __forceinline__ void st8_sc1(rsrc_t r, u32 voff, u32 v, u32 tag) {
  __builtin_amdgcn_raw_buffer_store_b64(u32x2{v, tag}, r, voff, 0, AUX_SC1);
}
__device__ __forceinline__ void st16_sc1(rsrc_t r, u32 voff, u32 v0, u32 v1, u32 tag) {
  __builtin_amdgcn_raw_buffer_store_b128(u32x4{v0, tag, v1, tag}, r, voff, 0, AUX_SC1);
}
__device__ __forceinline__ rsrc_t mk_rsrc(const void* p, u32 bytes) {
  return __builtin_amdgcn_make_buffer_rsrc(const_cast<void*>(p), 0, bytes, 0x00020000);
}
#define WAIT_VM(n) __builtin_amdgcn_s_waitcnt(((n) & 15) | (((n) >> 4) << 14) | (7 << 4) | (15 << 8))
#define SB() __builtin_amdgcn_sched_barrier(0)
#define CFENCE() asm volatile("" ::: "memory")
__device__ __forceinline__ bool all_ok(bool ok) { return __builtin_amdgcn_ballot_w64(!ok) == 0ull; }

template <int N> __device__ __forceinline__ void gather16(rsrc_t ws, u32 voff, u32 stride, u32 tag, u32x4 (&g)[N]) {
#pragma nounroll
  for (int spin = 0; spin < SPIN_MAX; ++spin) {
    CFENCE();
    bool ok = true;
#pragma unroll
    for (int i = 0; i < N; ++i) g[i] = ld16<AUX_SC1>(ws, voff + i * stride, 0);
#pragma unroll
    for (int i = 0; i < N; ++i) ok &= (g[i][1] == tag) & (g[i][3] == tag);
    if (all_ok(ok)) break;
    __builtin_amdgcn_s_sleep(8);
  }
}
template <int N> __device__ __forceinline__ void gather8(rsrc_t ws, u32 voff, u32 stride, u32 tag, u32 (&g)[N], bool act = true) {
#pragma nounroll
  for (int spin = 0; spin < SPIN_MAX; ++spin) {
    CFENCE();
    bool ok = true;
    u32x2 t[N];
#pragma unroll
    for (int i = 0; i < N; ++i) t[i] = ld8<AUX_SC1>(ws, voff + i * stride, 0);
#pragma unroll
    for (int i = 0; i < N; ++i) { ok &= (t[i][1] == tag); g[i] = t[i][0]; }
    if (all_ok(ok | !act)) break;
    __builtin_amdgcn_s_sleep(8);
  }
}

struct Smem {
  float red[8][128];
  float ml[8][2];
  float r2[64 * 20];
  u16 hb[3 * 1024];
  float stat[8][64];
  float red2[8][128];
  u32 dummy[64];
  u32x2 bc;
  u32x4 zero[4];
  u32 mflag[8];
  u32x4 wbuf[8][8][64];
  __attribute__((aligned(256))) u16 w2b[32][512];
  u16 actl[2][32];
};

#define STAMP(k) do {} while (0)
#define WSTAMP(k) do {} while (0)
#define FINISH() return
#define PIN(x) asm volatile("" : "+v"(x))
#define LDS_BARRIER() do { __builtin_amdgcn_s_waitcnt(0xc07f); __builtin_amdgcn_s_barrier(); CFENCE(); } while (0)
#define WAVE_SYNC() do { __builtin_amdgcn_fence(__ATOMIC_RELEASE, "wavefront"); __builtin_amdgcn_wave_barrier(); __builtin_amdgcn_fence(__ATOMIC_ACQUIRE, "wavefront"); } while (0)

__device__ __forceinline__ void dma16(rsrc_t r, u32 voff, u32 soff, u32 lds_dst) {
  u32 keep;
  asm volatile("s_mov_b32 %0, m0\n\ts_mov_b32 m0, %4\n\ts_nop 4\n\tbuffer_load_dwordx4 %1, %2, %3 offen nt lds\n\ts_mov_b32 m0, %0"
               : "=&s"(keep) : "v"(voff), "s"(r), "s"(soff), "s"(lds_dst) : "memory");
}
__device__ __forceinline__ u32 perm_lo(u32 a, u32 b) { return __builtin_amdgcn_perm(b, a, 0x05040100u); }
__device__ __forceinline__ u32 perm_hi(u32 a, u32 b) { return __builtin_amdgcn_perm(b, a, 0x07060302u); }
__device__ __forceinline__ float rdl(float x, int l) { return __int_as_float(__builtin_amdgcn_readlane(__float_as_int(x), l)); }

typedef __attribute__((ext_vector_type(8))) __bf16 bf16x8;
__device__ __forceinline__ f32x4 mfma16(u32x4 a, u32x4 b, f32x4 c) {
  return __builtin_amdgcn_mfma_f32_16x16x32_bf16(__builtin_bit_cast(bf16x8, a), __builtin_bit_cast(bf16x8, b), c, 0, 0, 0);
}
__device__ __forceinline__ float sum8(const float* p) {
  const f32x4 a = *reinterpret_cast<const f32x4*>(p), b = *reinterpret_cast<const f32x4*>(p + 4);
  return ((a[0] + b[0]) + (a[1] + b[1])) + ((a[2] + b[2]) + (a[3] + b[3]));
}
__device__ __forceinline__ float ln_ri(float var) {
  return __builtin_amdgcn_rcpf(rbf(__builtin_amdgcn_sqrtf(rbf(rbf(fmaxf(var, 0.f)) + 1e-5f))));
}
__device__ __forceinline__ void ln_stats2(const float (&x0)[8], const float (&x1)[8], float& mb0, float& ri0, float& mb1, float& ri1) {
  const float k0 = rdl(x0[0], 0), k1 = rdl(x1[0], 0);
  float s0 = 0.f, s1 = 0.f, q0 = 0.f, q1 = 0.f;
#pragma unroll
  for (int k = 0; k < 8; ++k) {
    const float d0 = x0[k] - k0, d1 = x1[k] - k1;
    s0 += d0; s1 += d1; q0 = fmaf(d0, d0, q0); q1 = fmaf(d1, d1, q1);
  }
  float t = swap16_add(swap32_add(s0, s1), swap32_add(q0, q1));
  t += dpp<XOR1>(t); t += dpp<XOR2>(t); t += dpp<XOR7>(t); t += dpp<XOR8>(t);
  auto u = __builtin_amdgcn_permlane16_swap(__float_as_uint(t), __float_as_uint(t), false, false);
  const float ms = __uint_as_float(u[0]) * (1.f / 512.f), mq = __uint_as_float(u[1]) * (1.f / 512.f);
  const float ri = ln_ri(fmaf(-ms, ms, mq));
  mb0 = rbf(k0 + rdl(ms, 0)); mb1 = rbf(k1 + rdl(ms, 32));
  ri0 = rdl(ri, 0); ri1 = rdl(ri, 32);
}
template <int I> struct IC { static constexpr int v = I; };
template <int I, int N, class F> __device__ __forceinline__ void sfor(F&& f) {
  if constexpr (I < N) { if (f(IC<I>{})) sfor<I + 1, N>(f); }
}
typedef __attribute__((ext_vector_type(4))) short s16x4;
typedef __attribute__((address_space(3))) s16x4* lds_s16x4p;
__device__ __forceinline__ u32x2 tr_read(u32 lds_addr) {
  return __builtin_bit_cast(u32x2, __builtin_amdgcn_ds_read_tr16_b64_v4i16((lds_s16x4p)lds_addr));
}
__device__ __forceinline__ u32 next_tag(u32 e) { return ((e | 0x40004000u) + 1u) & ~0x40004000u; }
__device__ __forceinline__ float u2f(u32 v) { return __uint_as_float(v); }
#define LDSP(T, a) ((__attribute__((address_space(3))) T*)(a))
template <class T> __device__ __forceinline__ u32 lds_addr(T* p) { u32 a = (u32)(uintptr_t)p; PIN(a); return a; }

#define TAB_LOAD "s_add_u32 s30, s28, wg_tab@rel32@lo+4\ns_addc_u32 s31, s29, wg_tab@rel32@hi+12\nglobal_load_dwordx2 %[te], %[va], s[30:31]\n"
__global__ void __launch_bounds__(512, 2) fused_kernel(const u16* kc_, const u16* vc_, const u16* wo_, const u16* wq_, const u16* wmod_, const u16* w1_, const u16* w3_,
                                                       unsigned char* ws_, const u16* x_, const u16* cnd_, const u16* w2_, u16* out_) {
  __shared__ __attribute__((aligned(256))) Smem sm;
  const u64 a_ka = (u64)__builtin_amdgcn_kernarg_segment_ptr();
  const int bid = blockIdx.x, tid = threadIdx.x;
  u64 wa, t64; u32x2 te; int tid0;
  u32 ar[14];
  u32 tch2, sa_, sb_, va_;
  u32x4 kr[NL], vr[NL];
  asm volatile(
    "s_load_dwordx16 s[4:19], %[ka], 0x0\ns_load_dwordx8 s[20:27], %[ka], 0x40\n"
    "s_memrealtime %[t]\n"
    "v_lshlrev_b32 %[va], 3, %[bid]\n"
    "s_getpc_b64 s[28:29]\n.Lcpf_first:\n"
    TAB_LOAD
    "s_cmp_gt_u32 %[bid], 15\ns_cbranch_scc1 .Lno_cpf\n"
    "s_mov_b32 %[sa], .Lend_a-.Lcpf_first\ns_max_i32 %[sa], %[sa], .Lend_b-.Lcpf_first\ns_max_i32 %[sa], %[sa], .Lend_c-.Lcpf_first\ns_max_i32 %[sa], %[sa], .Lend_d-.Lcpf_first\ns_max_i32 %[sa], %[sa], .Lend_e-.Lcpf_first\n"
    "v_lshlrev_b32 %[va], 6, %[tid]\nv_min_u32 %[va], %[sa], %[va]\nglobal_load_dword %[pf], %[va], s[28:29]\n"
    ".Lno_cpf:\n"
    "s_and_b32 %[sa], %[bid], 15\ns_lshr_b32 %[sb], %[bid], 6\ns_lshl2_add_u32 %[sa], %[sa], %[sb]\ns_lshl_b32 %[sa], %[sa], 22\ns_xor_b32 %[sa], %[sa], 0xff0000\n"
    "s_and_b32 %[sb], %[bid], 48\nv_lshl_add_u32 %[va], %[sb], 5, %[tid]\nv_lshl_add_u32 %[va], %[va], 4, %[sa]\n"
    "v_readfirstlane_b32 %[t0], %[tid]\n"
    "s_waitcnt lgkmcnt(0)\nglobal_load_dwordx4 %[k0], %[va], s[4:5] nt\nglobal_load_dwordx4 %[v0], %[va], s[6:7] nt\n"
    "s_bitcmp1_b32 %[t0], 6\ns_cselect_b64 s[14:15], s[16:17], s[14:15]\n"
    "s_bitcmp1_b32 %[bid], 4\ns_cselect_b64 s[8:9], s[10:11], s[8:9]\ns_cselect_b64 s[12:13], s[14:15], s[12:13]\ns_bitcmp1_b32 %[bid], 5\ns_cselect_b64 %[wa], s[12:13], s[8:9]"
    : "=&{s4}"(ar[0]), "=&{s5}"(ar[1]), "=&{s6}"(ar[2]), "=&{s7}"(ar[3]), "=&{s18}"(ar[4]), "=&{s19}"(ar[5]), "=&{s20}"(ar[6]), "=&{s21}"(ar[7]), "=&{s22}"(ar[8]), "=&{s23}"(ar[9]),
      "=&{s24}"(ar[10]), "=&{s25}"(ar[11]), "=&{s26}"(ar[12]), "=&{s27}"(ar[13]), [wa] "=&s"(wa), [t0] "=&s"(tid0), [t] "=&s"(t64), [te] "=&v"(te), [sa] "=&s"(sa_), [sb] "=&s"(sb_),
      [va] "=&v"(va_), [pf] "=&v"(tch2), [k0] "=&v"(kr[0]), [v0] "=&v"(vr[0])
    : [ka] "s"(a_ka), [bid] "s"(bid), [tid] "v"(tid) : "s8", "s9", "s10", "s11", "s12", "s13", "s14", "s15", "s16", "s17", "s28", "s29", "s30", "s31", "scc", "memory");
  asm volatile("" : "+s"(ar[0]), "+s"(ar[1]), "+s"(ar[2]), "+s"(ar[3]), "+s"(ar[4]), "+s"(ar[5]), "+s"(ar[6]), "+s"(ar[7]), "+s"(ar[8]), "+s"(ar[9]), "+s"(ar[10]), "+s"(ar[11]), "+s"(ar[12]), "+s"(ar[13]));
  asm volatile("" : "+s"(wa), "+s"(t64), "+s"(tid0));
  const u32 tka = (u32)wall_clock64();
  const u32 t0 = tka;
  __builtin_assume(tid >= 0 && tid < 512 && tid0 >= 0 && tid0 < 512 && bid >= 0 && bid < 256);
  constexpr Prm P = PRM0;
  const int TEND = P.TEND;
  const int xcd = bid & 7, part = bid >> 4 & 3;
  const int lane = tid & 63, wave = tid0 >> 6;
  SB();
  const bool p3 = part == 3;
  u32 pitch = (u32)(0x005600c001000020ull >> (part * 16) & 0xffff) * 32u;
  const int sh = p3 ? 2 : 3;
  u32 wv0 = (u32)((p3 ? (wave >> 1) * 128 : wave * 64) + (part == 0 ? xcd * 512 : 0) + (lane >> sh)) * pitch + ((lane & (p3 ? 3 : 7)) ^ (lane >> (p3 ? 5 : 3))) * 16u;
  const u32 wimg = (u32)(uintptr_t)(&sm.wbuf[0][0][0]);
  u32 wsz = (part == 0 ? 4096u : 512u) * pitch, wld = wimg + (p3 ? (wave & 1) * 32768 + (wave >> 1) * 8192 : wave * 8192);
  PIN(wv0);
  asm volatile("" : "+s"(wsz), "+s"(wld), "+s"(pitch));
  auto karg = [&](int i) { return ar[2 * i] | (u64)ar[2 * i + 1] << 32; };
  const u64 a_kc = karg(0), a_vc = karg(1);
  unsigned char* ws = (unsigned char*)karg(2);
  const u16 *x = (const u16*)karg(3), *cnd = (const u16*)karg(4), *w2 = (const u16*)karg(5);
  u16* out = (u16*)karg(6);
  const rsrc_t wsr = mk_rsrc(ws, WS_END);
  asm volatile("s_waitcnt vmcnt(2)" : "+v"(te));
  const u32 te0 = __builtin_amdgcn_readfirstlane(te[0]), te1 = __builtin_amdgcn_readfirstlane(te[1]);
  const int bl0 = te0 & 127, nl = te0 >> 7 & 63, blk = te0 >> 13 & 63;
  const bool has_w = te0 >> 19 & 1;
  const rsrc_t rw = mk_rsrc((const void*)wa, wsz);
  const u32 wstep = pitch << (6 - sh), wflip = p3 ? 32u : 0u;
  u32 wso = blk << (sh + 4);
  int wleft = 8;
  auto piece = [&]() {
    dma16(rw, wv0, wso, wld);
    wso += wstep; wld += 1024;
    wv0 ^= (wleft & 1) ? wflip : 0u;
    --wleft;
  };
  SB();
  STAMP(23);
  const int b = bid >> 3 & 1, head = xcd * 4 + (bid >> 6 ^ 3);
  const int g = lane >> 4, c = lane & 15;
  const int i8 = b | bid >> 6 << 1, wi = i8 * 8 + xcd;
  const bool is_up = p3 && has_w, modc = part == 2 && wi < 16;
  const size_t base = ((size_t)(b * NH + head) * SK + (SK - WIN)) * DH;
  const rsrc_t rk = mk_rsrc((const u16*)a_kc + base, WIN * DH * 2), rv = mk_rsrc((const u16*)a_vc + base, WIN * DH * 2);
  int tau = te1 & 0xffff;
  const int tauw = te1 >> 16;
  int due = P.NF * tau >> 2, now = 0;
  auto gate = [&]() {
    while (now < due) { now = (int)(((u32)wall_clock64() - t0) << 8); if (now < due) __builtin_amdgcn_s_sleep(1); }
    due += tau;
  };
  auto until = [&](int t) { while ((int)(((u32)wall_clock64() - t0) << 8) < t); };
  const u32 pslot = G_P + ((b * 32 + head) * 4 + part) * 256;
  u32x2 ep; u32 xs0, xs1, tch1; u32x4 xr0, xr1;
  const rsrc_t rx = mk_rsrc(part == 2 ? cnd : x, (part == 1 || (part == 2 && has_w)) ? 2048u : 0u);
  auto small_ops = [&]() {
    ep = ld8<AUX_SC1>(wsr, wave == 7 ? WS_EPOCH : OOB, 0);
    xs0 = __builtin_amdgcn_raw_buffer_load_b16(rx, tid * 2, 0, 0); xs1 = __builtin_amdgcn_raw_buffer_load_b16(rx, tid * 2, 1024, 0);
    xr0 = ld16<0>(rx, part == 1 ? lane * 16 : OOB, 0); xr1 = ld16<0>(rx, part == 1 ? lane * 16 : OOB, 1024);
    tch1 = __builtin_amdgcn_raw_buffer_load_b32(mk_rsrc(w2 + (size_t)blk * 32 * 512, is_up ? 64u : 0u), tid == 384 ? 0 : OOB, 0, AUX_NT);
  };
  if (has_w) {
    if (modc) now = 1 << 30;
#pragma nounroll
    while (wleft > (int)(te0 >> 20)) { gate(); due += tauw - tau; piece(); }
    now = 0;
  }
  SB();
  u32 sot;
  {
    const u32 s1 = bl0 + lane - 1, s0 = s1 + (s1 >= 108 ? 4 : 0);
    u32 sb = s0 & 96u;
#pragma unroll
    for (int k = 0; k < 5; ++k) sb |= (s0 >> k & 1u) << (P.PM >> (4 * k) & 7);
    const u32 src = (sb >> 5 & 3u) | (u32)(b * 32 + head) << 2;
    sb ^= ((__builtin_popcount(src & P.M0) & 1) | (__builtin_popcount(src & P.M1) & 1) << 1 | (__builtin_popcount(src & P.M2) & 1) << 2 | (__builtin_popcount(src & P.M3) & 1) << 3 | (__builtin_popcount(src & P.M4) & 1) << 4) & (s1 >= 96 ? 7u : 31u);
    sot = (sb & 7u) << 10 | (u32)wave << 13 | (sb >> 3) << 16;
    sot = s1 >= 120 ? (s1 + 4) << 13 | (u32)wave << 10 : sot;
    sot = lane < nl ? sot : OOB;
    PIN(sot);
  }
  u32 tag = 0;
  u32x4 bf[8];
  auto wo_prep = [&](int s0 = 0, int s1 = 2) {
    const u32 wi_ = wimg + wave * 8192;
    const int q = (lane >> 2) & 3, pp = lane & 3;
#pragma unroll
    for (int s = s0; s < s1; ++s) {
#pragma unroll
      for (int t = 0; t < 4; ++t) {
        const int r0 = s * 32 + g * 8 + q, r1 = r0 + 4, ch = t * 2 + (pp >> 1);
        const u32x2 lo = tr_read(wi_ + r0 * 128 + ((ch ^ (r0 & 7)) * 16) + (pp & 1) * 8), hi = tr_read(wi_ + r1 * 128 + ((ch ^ (r1 & 7)) * 16) + (pp & 1) * 8);
        bf[s * 4 + t] = u32x4{lo[0], lo[1], hi[0], hi[1]};
      }
    }
  };
  auto gemv_go = [&](u32 hh) {
    sm.hb[tid] = (u16)hh; sm.hb[512 + tid] = (u16)(hh >> 16);
    WAVE_SYNC();
    const u32x4* a = (c & 11) == 0 ? reinterpret_cast<const u32x4*>(&sm.hb[(c >> 2) * 512 + wave * 64 + g * 8]) : &sm.zero[0];
    const u32x4 a0 = a[0], a1 = a[(c & 11) == 0 ? 4 : 0];
    const f32x4 z = {0.f, 0.f, 0.f, 0.f};
    f32x4 d[4];
#pragma unroll
    for (int t = 0; t < 4; ++t) d[t] = mfma16(a0, bf[t], z);
#pragma unroll
    for (int t = 0; t < 4; ++t) d[t] = mfma16(a1, bf[4 + t], d[t]);
    if (lane < 32) {
#pragma unroll
      for (int t = 0; t < 4; ++t) sm.r2[(t * 16 + c) * 20 + g * 8 + wave] = d[t][0];
    }
  };
  auto mod_stage = [&]() {
    wo_prep();
    gemv_go(pack_bf(silu_f(lo_f(xs0)), silu_f(lo_f(xs1))));
    __builtin_amdgcn_s_waitcnt(0xc07f);
    if (lane == 0) *LDSP(volatile u32, (u32)(uintptr_t)&sm.mflag[wave]) = 1u;
    if (wave == 7) {
#pragma nounroll
      for (int spin = 0; spin < SPIN_MAX; ++spin) {
        const u32 f = *LDSP(volatile u32, (u32)(uintptr_t)&sm.mflag[lane & 7]);
        if (all_ok(f != 0u)) break;
        __builtin_amdgcn_s_sleep(1);
      }
      CFENCE();
      st8_sc1(wsr, G_MOD + (blk * 64 + lane) * 8, pack_bf(sum8(&sm.r2[lane * 20]), sum8(&sm.r2[lane * 20 + 8])), tag);
    }
    STAMP(9);
  };
  if (lane < 4) sm.zero[lane] = u32x4{0u, 0u, 0u, 0u};
  if (part == 2 && has_w) {
    if (tid < 8) sm.mflag[tid] = 0u;
    LDS_BARRIER();
    if (__builtin_expect(modc, 0)) {
      small_ops();
      __builtin_amdgcn_raw_buffer_store_b128(u32x4{SENT, SENT, SENT, SENT}, wsr, tid < 320 ? G_P + wi * 5120 + tid * 16 : OOB, 0, AUX_SC1);
      asm volatile("s_waitcnt vmcnt(0)" ::: "memory");
      tag = next_tag(__builtin_amdgcn_readfirstlane(ep[0]));
      mod_stage();
      __builtin_amdgcn_s_barrier();
    }
  }
  SB();
  const int fd = wi * 8 + (lane & 7);
  const int ub = wave >> 2, ud = (wave & 3) * 128 + c * 8 + g * 2;
  u32x4 sg[3]; u32 sx2;
  auto side_loads = [&]() {
    const bool fin = part == 1 && wave == 0;
    sg[0] = ld16<AUX_SC1>(wsr, is_up ? G_MOD + (1024 + ud) * 8 : (fin ? G_MOD + (1024 + fd) * 8 : OOB), 0);
    sg[1] = ld16<AUX_SC1>(wsr, is_up ? G_MOD + (1536 + ud) * 8 : (fin ? G_MOD + (2560 + fd) * 8 : OOB), 0);
    sg[2] = ld16<AUX_SC1>(wsr, is_up ? G_MOD + (2048 + ud) * 8 : OOB, 0);
    sx2 = __builtin_amdgcn_raw_buffer_load_b32(mk_rsrc(x, (is_up || fin) ? 2048u : 0u), is_up ? ub * 1024 + ud * 2 : (g & 1) * 1024 + (fd >> 1) * 4, 0, 0);
  };
  auto side_pin = [&]() { PIN(sg[0]); PIN(sg[1]); PIN(sg[2]); PIN(sx2); };
  const u32 mlo = G_ML + (head * 2 + b) * 64 + (lane & 3) * 16;
  float fmrg = 0.f;
  bool fok = wave != 0;
  auto merge_factor = [&](const u32x4& ml) {
    const float mi = u2f(ml[0]);
    float M = fmaxf(mi, dpp<XOR1>(mi)); M = fmaxf(M, dpp<XOR2>(M));
    const float e = __expf(mi - M);
    float L = e * u2f(ml[2]);
    L += dpp<XOR1>(L); L += dpp<XOR2>(L);
    fmrg = rdl(e * __builtin_amdgcn_rcpf(L), part);
  };
  f32x2 ga = {0.f, 0.f}, xin = {0.f, 0.f}, cc1 = {0.f, 0.f};
  float Af = 0.f, Bf = 0.f;
  const int hpos = ub * 512 + ud;
  auto up_prep = [&]() {
    if (!all_ok((sg[0][1] == tag) & (sg[0][3] == tag) & (sg[1][1] == tag) & (sg[1][3] == tag) & (sg[2][1] == tag) & (sg[2][3] == tag))) gather16<3>(wsr, G_MOD + (1024 + ud) * 8, 512 * 8, tag, sg);
    ga = f32x2{ub ? hi_f(sg[0][0]) : lo_f(sg[0][0]), ub ? hi_f(sg[0][2]) : lo_f(sg[0][2])};
    cc1 = f32x2{rbf(1.f + (ub ? hi_f(sg[2][0]) : lo_f(sg[2][0]))), rbf(1.f + (ub ? hi_f(sg[2][2]) : lo_f(sg[2][2])))};
    xin = f32x2{lo_f(sx2), hi_f(sx2)};
    u32* hp = reinterpret_cast<u32*>(&sm.hb[hpos]);
    hp[512] = pack_bf(cc1[0], cc1[1]);
    hp[1024] = ub ? perm_hi(sg[1][0], sg[1][2]) : perm_lo(sg[1][0], sg[1][2]);
  };
  auto up_frags = [&]() {
    const int pp = lane & 3;
    const u32 a = wimg + (pp & 1) * 32768 + ((pp >> 1) * 32 + g * 8 + ((lane >> 2) & 3)) * 64 + (((wave >> 1) ^ ((g & 1) | (pp & 2))) * 16) + (wave & 1) * 8;
#pragma unroll
    for (int rb = 0; rb < 8; ++rb) {
      const u32x2 lo = tr_read(a + rb * 4096), hi = tr_read(a + rb * 4096 + 256);
      bf[rb] = u32x4{lo[0], lo[1], hi[0], hi[1]};
    }
  };
  const u32x4* ap = reinterpret_cast<const u32x4*>(&sm.hb[((lane >> 3) & 1) * 512 + (lane & 1) * 32 + g * 8]);
  const bool par = lane & 8;
  float l0 = 0.f, l1 = 0.f;
  u32x2 gmd[2], tbg;
  int t1 = 0;
#pragma unroll
  for (int i = NCMIN / 8; i < NL; ++i) kr[i] = u32x4{0u, 0u, 0u, 0u};
  sfor<0, NL>([&](auto ic) {
    constexpr int i = ic.v;
    if (i >= NCMIN / 8 && i >= nl) return false;
    if constexpr (i == 0) return true;
    const u32 so = __builtin_amdgcn_readlane(sot, i);
    gate();
    SB();
    kr[i] = ld16<AUX_NT>(rk, lane * 16, so);
    if constexpr (i == SMO) small_ops();
    if constexpr (i == TBL) tbg = ld8<AUX_SC1>(wsr, wave == 7 ? G_TB + xcd * 128 + (lane & 7) * 8 : OOB, 0);
    if constexpr (i == QP) { gmd[0] = ld8<AUX_SC1>(wsr, part == 1 ? G_MOD + tid * 8 : OOB, 0); gmd[1] = ld8<AUX_SC1>(wsr, part == 1 ? G_MOD + tid * 8 : OOB, 4096); }
    SB();
    SB();
    if (i >= WK) { WAIT_VM(WK); SB(); }
    if (i == 1) STAMP(25);
    if (i == 8) STAMP(12);
    if (i == 20) STAMP(21);
    if (i == 25) STAMP(22);
    if (i == TBP) {
      if (part == 3 && i8 == 0 && wave == 7) { tag = next_tag(__builtin_amdgcn_readfirstlane(ep[0])); if (lane < 8) st8_sc1(wsr, G_TB + lane * 128 + xcd * 8, t0 + (((tka - (u32)t64) * P.GA) >> 3) - P.GS, tag); }
      SB();
    }
    if (i == TBU) {
      if (wave == 7) {
        const bool ok = tbg[1] == tag;
        float lf = ok ? (float)min(max((int)(t0 - tbg[0]), -300), 300) : 0.f, nf = ok ? 1.f : 0.f;
        lf += dpp<XOR1>(lf); nf += dpp<XOR1>(nf); lf += dpp<XOR2>(lf); nf += dpp<XOR2>(nf); lf += dpp<XOR7>(lf); nf += dpp<XOR7>(nf);
        if (lane == 0) sm.bc = u32x2{tag, t0 - (u32)(int)(lf * __builtin_amdgcn_rcpf(fmaxf(nf, 1.f)))};
      }
      LDS_BARRIER();
      const u32x2 bc = sm.bc;
      tag = __builtin_amdgcn_readfirstlane(bc[0]);
      t1 = (__builtin_amdgcn_readfirstlane((int)(bc[1] - t0)) + TEND + P.X1) * 256;
      tau -= __builtin_amdgcn_readfirstlane((int)((float)(int)(t0 - bc[1]) * 256.f * __builtin_amdgcn_rcpf((float)(2 * nl - TBU - 3))));
      SB();
    }
    if (i == LNS) {
      tag = next_tag(__builtin_amdgcn_readfirstlane(ep[0]));
      if (part == 1) {
        float x0[8], x1[8], mb0, ri0, mb1, ri1;
#pragma unroll
        for (int k = 0; k < 4; ++k) { x0[2 * k] = lo_f(xr0[k]); x0[2 * k + 1] = hi_f(xr0[k]); x1[2 * k] = lo_f(xr1[k]); x1[2 * k + 1] = hi_f(xr1[k]); }
        ln_stats2(x0, x1, mb0, ri0, mb1, ri1);
        l0 = rbf(rbf(lo_f(xs0) - mb0) * ri0); l1 = rbf(rbf(lo_f(xs1) - mb1) * ri1);
      }
      PIN(l0); PIN(l1);
      SB();
    }
    if (i == WUSE) {
      PIN(tch1); PIN(tch2);
      if ((part == 1 || (part == 2 && has_w && !modc))) {
        WAIT_VM(WK); CFENCE();
        if (part == 2) mod_stage(); else wo_prep();
      }
#pragma unroll
      for (int k = 0; k < 8; ++k) PIN(bf[k]);
      SB();
    }
    if (i == QP + QD) {
      if (part == 1) {
        u32 gm[2] = {gmd[0][0], gmd[1][0]};
        if (!all_ok((gmd[0][1] == tag) & (gmd[1][1] == tag))) { STAMP(11); gather8<2>(wsr, G_MOD + tid * 8, 512 * 8, tag, gm); }
        STAMP(5);
        gemv_go(pack_bf(rbf(l0 * rbf(1.f + lo_f(gm[1]))) + lo_f(gm[0]), rbf(l1 * rbf(1.f + hi_f(gm[1]))) + hi_f(gm[0])));
      }
      SB();
    }
    if (i == QP + QD + 1) {
      if (part == 1) {
        LDS_BARRIER();
        if (wave == 0) {
          const int bb = lane >> 5, jq = lane & 31;
          st8_sc1(wsr, G_Q + (bb * 2048 + blk * 32 + jq) * 8, pack_bf(sum8(&sm.r2[2 * jq * 20 + bb * 8]), sum8(&sm.r2[(2 * jq + 1) * 20 + bb * 8])), tag);
        }
        STAMP(6);
      }
      SB();
    }
    return true;
  });
  PIN(sot);
  const u32 qoff = G_Q + ((b * 32 + head) * 64 + c * 4) * 8;
  u32x4 qg[2], fl2[3];
  u32 qq[4];
  constexpr int NB = (NL + 15) / 16;
  float xlo, sc[NB], pr[NB], m = 0.f;
  f32x4 acc = {0.f, 0.f, 0.f, 0.f};
  u32 pb[NB], pm[4], cpf;
  const u32 adst0 = lds_addr(lane < 32 ? &sm.red[wave][c * 8 + g * 4] : &sm.r2[(lane - 32) * 4]);
  u32 po, rdst, rsrc, xo;
  auto wo_addr = [&]() {
    po = G_P + (((c >> 2 & 1) * 32 + xcd * 4 + (wave >> 1)) * 4 + (c & 3)) * 256 + (wave & 1) * 128 + (c >> 3) * 64 + g * 16;
    rdst = lane < 32 ? (u32)(uintptr_t)&sm.red2[wave][0] + lane * 16 : (u32)(uintptr_t)&sm.r2[0] + (lane - 32) * 16;
    rsrc = (u32)(uintptr_t)&sm.red2[0][((wave & 1) * 16 + (lane & 15)) * 4 + (lane >> 4)];
    xo = (lane & 1) ? OOB : G_XP + xcd * 2048 + (wave & 1) * 1024 + (blk * 64 + lane) * 2;
    PIN(po); PIN(rdst); PIN(rsrc); PIN(xo);
  };
  u32 pso = wave == 0 ? pslot + lane * 4 : OOB;
  PIN(pso);
  const u32 nadd = (te0 >> 20) ? P.LW : (is_up ? P.LU : 0);
  u32 nto = tid == 0 ? G_NT + bid * 8 : OOB;
  PIN(nto);
  auto notices = [&](auto nc_, u32 off, u32x4 (&gn)[nc_.v]) -> int {
    constexpr int N = nc_.v;
#pragma nounroll
    for (int spin = 0; spin < SPIN_MAX; ++spin) {
      bool ok = true;
#pragma unroll
      for (int i = 0; i < N; ++i) ok &= (gn[i][1] == tag) & (gn[i][3] == tag);
      if (all_ok(ok)) break;
      __builtin_amdgcn_s_sleep(10);
      CFENCE();
#pragma unroll
      for (int i = 0; i < N; ++i) gn[i] = ld16<AUX_SC1>(wsr, off, i * 1024);
    }
    int mx = 0;
#pragma unroll
    for (int i = 0; i < N; ++i) mx = max(mx, max((int)(gn[i][0] - t0), (int)(gn[i][2] - t0)));
    return __builtin_amdgcn_readfirstlane((int)wave_max((float)min(mx, 1 << 20)));
  };
  const int side = nl >= SIDE + 13 ? SIDE + 8 : (nl >= SIDE + 9 ? SIDE + 4 : SIDE);
  sfor<0, NL + WV>([&](auto ic) {
    constexpr int v = ic.v;
    if (cons_end(v - 1) >= NCMIN / 8 && cons_end(v - 1) >= nl) return false;
    if constexpr (v > 0 && v < NL) {
      const u32 so = __builtin_amdgcn_readlane(sot, v);
      if constexpr (v >= 12) { if (v == nl - P.NE) tau = (tau * P.FE) >> 4; }
      if constexpr (v >= NCMIN / 8) { if (v < nl) gate(); } else gate();
      SB();
      vr[v] = ld16<AUX_NT>(rv, lane * 16, so);
      SB();
      SB();
    }
    if constexpr (v == NCMIN / 8 - 1) {
#pragma unroll
      for (int i = NCMIN / 8; i < NL; ++i) asm volatile("" : "=v"(vr[i]));
      asm volatile("" : "=v"(fl2[1]), "=v"(fl2[2]));
    }
    if constexpr (v == VQ - 4) { qg[0] = ld16<AUX_SC1>(wsr, qoff, 0); qg[1] = ld16<AUX_SC1>(wsr, qoff + 16, 0); }
    if constexpr (v == VSIDE) side_loads();
    if constexpr (v == CPF) {
      u64 pc; u32 dlt, len;
      asm volatile("s_getpc_b64 %0\n.Lcpf_here:\n\ts_mov_b32 %1, .Ltail_start-.Lcpf_here\n\ts_mov_b32 %2, .Lend_a-.Ltail_start\n\ts_max_i32 %2, %2, .Lend_b-.Ltail_start\n\ts_max_i32 %2, %2, .Lend_c-.Ltail_start\n\ts_max_i32 %2, %2, .Lend_d-.Ltail_start\n\ts_max_i32 %2, %2, .Lend_e-.Ltail_start" : "=s"(pc), "=s"(dlt), "=s"(len) :: "scc");
      pc += dlt;
      const rsrc_t rc = mk_rsrc((const void*)pc, (part == 3 && i8 == 1) ? len : 0u);
      cpf = __builtin_amdgcn_raw_buffer_load_b32(rc, tid * 64, 0, 0);
    }
    if constexpr (v == CPF + WV + 1) PIN(cpf);
    if constexpr (v == SIDE || v == SIDE + 4 || v == SIDE + 8) fl2[(v - SIDE) / 4] = ld16<AUX_SC1>(wsr, (wave == 0 && v == side) ? mlo : OOB, 0);
    SB();
    if constexpr (v < WV) { WAIT_VM(WK); SB(); } else if constexpr (v < NL && cons_end(v) < v - WV + 1) { WAIT_VM(WV); SB(); }
    if constexpr (v == VQ) {
      if (!all_ok((qg[0][1] == tag) & (qg[0][3] == tag) & (qg[1][1] == tag) & (qg[1][3] == tag))) { STAMP(20); gather16<2>(wsr, qoff, 16, tag, qg); }
      STAMP(1); WSTAMP(1);
      qq[0] = qg[0][0]; qq[1] = qg[0][2]; qq[2] = qg[1][0]; qq[3] = qg[1][2];
    }
    if constexpr (v >= VQ && v < VS) {
      constexpr int sl = v - VQ, bt = sl >> 1;
      float xs[8];
#pragma unroll
      for (int i = 0; i < 8; ++i) {
        float d = 0.f;
        if (sl * 8 + i < NL) {
#pragma unroll
          for (int k = 0; k < 4; ++k) d = dot2(kr[sl * 8 + i][k], qq[k], d);
        }
        xs[i] = d;
      }
#pragma unroll
      for (int h = 0; h < 4; ++h) xs[h] = sel_add<XOR7>(xs[h], xs[h + 4], c & 4);
#pragma unroll
      for (int h = 0; h < 2; ++h) xs[h] = sel_add<XOR2>(xs[h], xs[h + 2], c & 2);
      xs[0] = sel_add<XOR1>(xs[0], xs[1], c & 1);
      if constexpr ((sl & 1) || sl == NSL - 1) {
        const float t = (sl & 1) ? sel_add<XOR8>(xlo, xs[0], c & 8) : xs[0] + dpp<XOR8>(xs[0]);
        const float s = rbf(rbf(t) * 0.08838834764831845f);
        sc[bt] = (bt * 16 + c < nl) ? s : -__builtin_inff();
        PIN(sc[bt]);
      } else {
        xlo = xs[0];
        PIN(xlo);
      }
    }
    if constexpr (v == VS) {
      float mloc = sc[0];
#pragma unroll
      for (int bt = 1; bt < NB; ++bt) mloc = fmaxf(mloc, sc[bt]);
      m = fmaxf(wave_max(mloc), -3.0e38f);
      float ls = 0.f;
#pragma unroll
      for (int bt = 0; bt < NB; ++bt) { pr[bt] = __expf(sc[bt] - m); ls += pr[bt]; }
      const float l = wave_sum(ls);
      if (lane == 0) { sm.ml[wave][0] = m; sm.ml[wave][1] = l; }
      STAMP(2); WSTAMP(2);
    }
    sfor<cons_end(v - 1), cons_end(v)>([&](auto jc) {
      constexpr int i = jc.v;
      if (i < NCMIN / 8 || i < nl) {
        const u32 pv = __float_as_uint(bcast16(u2f(pb[i >> 4]), i & 15));
        acc = mfma16(u32x4{pv & pm[0], pv & pm[1], pv & pm[2], pv & pm[3]}, vr[i], acc);
      }
      PIN(acc);
      return true;
    });
    if constexpr (v == MLS - 1) { side_pin(); if (is_up) up_prep(); PIN(ga); PIN(xin); PIN(cc1); }
    if constexpr (v == UPREP - 1) wo_addr();
    if constexpr (v == UPREP) {
#pragma unroll
      for (int k = 0; k < 8; ++k) asm volatile("" : "=v"(bf[k]));
      if (is_up) up_frags();
#pragma unroll
      for (int k = 0; k < 8; ++k) PIN(bf[k]);
    }
    if constexpr (v == UPREP + 1) {
      if (is_up) {
        const u32x4* a1 = ap + 128 + ((lane >> 1) & 1) * 128;
        f32x4 d0 = {0.f, 0.f, 0.f, 0.f}, d1 = {0.f, 0.f, 0.f, 0.f};
#pragma unroll
        for (int rb = 0; rb < 4; ++rb) { d0 = mfma16(a1[rb * 8], bf[rb], d0); d1 = mfma16(a1[(rb + 4) * 8], bf[rb + 4], d1); }
        Af = par ? d0[1] + d1[1] : d0[0] + d1[0]; Bf = par ? d0[3] + d1[3] : d0[2] + d1[2];
        Af += dpp<XOR8>(Af); Bf += dpp<XOR8>(Bf);
      }
      PIN(Af); PIN(Bf);
    }
    if constexpr (v == SIDE + WV + 1 || v == SIDE + WV + 5 || v == SIDE + WV + 9) {
      if (wave == 0 && v == side + WV + 1) {
        const u32x4 ml = fl2[(v - SIDE - WV - 1) / 4];
        fok = all_ok((ml[1] == tag) & (ml[3] == tag));
        merge_factor(ml);
      }
    }
    if (v % 6 == 4 && v >= 10 && v < 30) STAMP(15 + v / 6);
    if constexpr (v == MLS) {
      LDS_BARRIER();
      float mm[8], ll[8];
#pragma unroll
      for (int w = 0; w < 8; ++w) { const f32x2 t = *reinterpret_cast<const f32x2*>(&sm.ml[w][0]); mm[w] = t[0]; ll[w] = t[1]; }
      float Mwg = mm[0], Lwg = 0.f;
#pragma unroll
      for (int w = 1; w < 8; ++w) Mwg = fmaxf(Mwg, mm[w]);
      if (wave == 0) {
#pragma unroll
        for (int w = 0; w < 8; ++w) Lwg = fmaf(__expf(mm[w] - Mwg), ll[w], Lwg);
        if (lane == 0) st16_sc1(wsr, G_ML + ((head * 2 + b) * 4 + part) * 16, __float_as_uint(Mwg), __float_as_uint(Lwg), tag);
      }
      const float fw = __expf(m - Mwg);
#pragma unroll
      for (int bt = 0; bt < NB; ++bt) { pb[bt] = pack_bf(pr[bt] * fw, pr[bt] * fw); PIN(pb[bt]); }
#pragma unroll
      for (int k = 0; k < 4; ++k) { pm[k] = (c >> 1) == k ? ((c & 1) ? 0xffff0000u : 0x0000ffffu) : 0u; PIN(pm[k]); }
    }
    SB();
    return true;
  });
  asm volatile(".Ltail_start:");
  *LDSP(f32x4, adst0) = acc;
  STAMP(14); WSTAMP(14);
  if (!fok) {
    u32x4 ml;
#pragma nounroll
    for (int spin = 0; spin < SPIN_MAX; ++spin) {
      CFENCE();
      ml = ld16<AUX_SC1>(wsr, mlo, 0);
      if (all_ok((ml[1] == tag) & (ml[3] == tag))) break;
    }
    merge_factor(ml);
  }
  STAMP(15);
  LDS_BARRIER();
  STAMP(3);
  {
    u32 pw = 0;
    if (wave == 0) {
      f32x2 t[8];
#pragma unroll
      for (int w = 0; w < 8; ++w) t[w] = *reinterpret_cast<const f32x2*>(&sm.red[w][2 * lane]);
      const f32x2 o = ((t[0] + t[1]) + (t[2] + t[3])) + ((t[4] + t[5]) + (t[6] + t[7]));
      pw = min(pack_bf(o[0] * fmrg, o[1] * fmrg), SENT - 1u);
    }
    __builtin_amdgcn_raw_buffer_store_b32(pw, wsr, pso, 0, AUX_SC1);
    st8_sc1(wsr, nto, (u32)wall_clock64() + nadd, tag);
    SB();
#pragma unroll
    for (int i = NCMIN / 8; i < NL; ++i) PIN(vr[i]);
    PIN(fl2[0]); PIN(fl2[1]); PIN(fl2[2]);
  }
  STAMP(4);

  if (part == 2) FINISH();
  if (part == 0) {
    __builtin_amdgcn_s_barrier();
#pragma nounroll
    while (wleft > 0) piece();
    until(t1);
    STAMP(11);
    CFENCE();
    u32x4 og[2];
    og[0] = ld16<AUX_SC1>(wsr, po, 0);
    SB();
    WAIT_VM(1); CFENCE();
    wo_prep(0, 2);
#pragma unroll
    for (int k = 0; k < 8; ++k) PIN(bf[k]);
    SB();
    f32x4 d[4];
    auto wo_mm = [&]() {
      const f32x4 z = {0.f, 0.f, 0.f, 0.f};
#pragma unroll
      for (int t = 0; t < 4; ++t) { d[t] = mfma16(og[0], bf[t], z); og[1][t] = __float_as_uint(dpp<XOR8>(u2f(og[0][t]))); }
      SB();
#pragma unroll
      for (int t = 0; t < 4; ++t) d[t] = mfma16(og[1], bf[4 + t], d[t]);
    };
    auto p_bad = [&]() { return !all_ok(max(max(og[0][0], og[0][1]), max(og[0][2], og[0][3])) != SENT); };
    wo_mm();
    if (__builtin_expect(p_bad(), 0)) {
#pragma nounroll
      for (int spin = 0; spin < SPIN_MAX; ++spin) {
        __builtin_amdgcn_s_sleep(1);
        CFENCE();
        og[0] = ld16<AUX_SC1>(wsr, po, 0);
        if (!p_bad()) break;
      }
      wo_mm();
      SB();
      asm volatile(".Lend_d:");
    }
    STAMP(5); WSTAMP(5);
    {
      SB();
      f32x4 y; f32x2 h[4];
#pragma unroll
      for (int t = 0; t < 4; ++t) { h[t] = d[t].xy + d[t].zw; PIN(h[t]); }
#pragma unroll
      for (int t = 0; t < 4; ++t) { y[t] = h[t][0] + h[t][1]; PIN(y[t]); }
      *LDSP(f32x4, rdst) = y;
    }
    STAMP(16);
    LDS_BARRIER();
    STAMP(17);
    if (wave < 2) {
      f32x2 v[4];
#pragma unroll
      for (int w = 0; w < 4; ++w) v[w] = f32x2{LDSP(float, rsrc)[w * 256], LDSP(float, rsrc)[w * 256 + 128]};
      const f32x2 h = (v[0] + v[1]) + (v[2] + v[3]);
      const float y = h[0] + h[1];
      __builtin_amdgcn_raw_buffer_store_b32(min(pack_bf(y, dpp<XOR1>(y)), SENT - 1u), wsr, xo, 0, AUX_SC1);
      asm volatile(".Lend_a:");
    }
    STAMP(6);
    FINISH();
  }
  if (part == 3) {
    if (!has_w) FINISH();
    u32x4 b2[4];
    {
      __builtin_amdgcn_s_barrier();
      const rsrc_t r2 = mk_rsrc(w2 + (size_t)(blk * 32 + wave * 4) * 512, 4096u);
      const u32 l2 = (u32)(uintptr_t)(&sm.w2b[wave * 4][0]);
#pragma unroll
      for (int n = 0; n < 4; ++n) dma16(r2, (lane ^ (n * 2) ^ ((wave & 2) << 2)) * 16, n * 1024, l2 + n * 1024);
    }
    const u32 tdst = lds_addr(&sm.hb[hpos]);
    const u32 sdst = lds_addr(&sm.stat[wave][lane]);
    const u32 ssrc = lds_addr(&sm.stat[(g >> 1) * 4 + ((lane >> 1) & 3)][(lane & 1) * 16]);
    const u32 adst = lds_addr((c < 4 && (g & 1) == 0) ? &sm.actl[g >> 1][wave * 4 + c] : reinterpret_cast<u16*>(&sm.dummy[lane]));
    const u32 asrc = lds_addr(&sm.actl[lane & 1][g * 8]);
    const u32 apa = lds_addr(&sm.hb[(c >> 3) * 512 + (c & 3) * 64 + ((c >> 2) & 1) * 32 + g * 8]);
    u32 xoff = G_XP + g * 2048 + ub * 1024 + (wave & 3) * 256 + c * 16;
    u32x4 one;
#pragma unroll
    for (int k = 0; k < 4; ++k) one[k] = ((c & 2) == 0 && (c >> 2) == k) ? ((c & 1) ? 0x3f800000u : 0x00003f80u) : 0u;
    PIN(one);
    u32 ooff = G_OP + (blk * 512 + tid) * 8;
    float Afp = Af, Bfp = Bf;
    PIN(xoff); PIN(ooff); PIN(Afp); PIN(Bfp);
    PIN(ga); PIN(xin); PIN(cc1);
    u32 w2a = (u32)(uintptr_t)(&sm.w2b[0][0]) + (g * 8 + ((lane >> 2) & 3)) * 1024 + (wave ^ (g & 1)) * 128 + (lane & 15) * 8;
    PIN(w2a);
    if (wave == 1) {
      due = t1 + P.N1 * 256; gate();
      u32x4 gn[2] = {ld16<AUX_SC1>(wsr, G_NT + lane * 16, 0), ld16<AUX_SC1>(wsr, G_NT + lane * 16, 1024)};
      const int tn = notices(IC<2>{}, G_NT + lane * 16, gn) * 256;
      due = (tn > t1 + 2 * 256 ? max(tn, t1 + P.L0 * 256) + P.L1 * 256 : t1) + P.X2 * 256;
      until(due);
    }
    asm volatile("s_waitcnt vmcnt(0)" ::: "memory");
    STAMP(9);
    __builtin_amdgcn_s_barrier();
    STAMP(11);
    CFENCE();
    u32x4 xg[2] = {ld16<AUX_SC1>(wsr, xoff, 0), ld16<AUX_SC1>(wsr, xoff, 8192)};
    SB();
#pragma unroll
    for (int t = 0; t < 4; ++t) {
      const u32x2 lo = tr_read(w2a ^ (t * 32)), hi = tr_read((w2a ^ (t * 32)) + 4096);
      b2[t] = u32x4{lo[0], lo[1], hi[0], hi[1]};
    }
#pragma unroll
    for (int t = 0; t < 4; ++t) PIN(b2[t]);
    SB();
    const f32x4 z4 = {0.f, 0.f, 0.f, 0.f};
    auto xp_bad = [&]() { return !all_ok(max(max(max(xg[0][0], xg[0][1]), max(xg[0][2], xg[0][3])), max(max(xg[1][0], xg[1][1]), max(xg[1][2], xg[1][3]))) != SENT); };
    f32x4 v = mfma16(one, xg[1], mfma16(one, xg[0], z4));
    if (__builtin_expect(xp_bad(), 0)) {
#pragma nounroll
      for (int spin = 0; spin < SPIN_MAX; ++spin) {
        __builtin_amdgcn_s_sleep(1);
        CFENCE();
        xg[0] = ld16<AUX_SC1>(wsr, xoff, 0); xg[1] = ld16<AUX_SC1>(wsr, xoff, 8192);
        if (!xp_bad()) break;
      }
      v = mfma16(one, xg[1], mfma16(one, xg[0], z4));
      SB();
      asm volatile(".Lend_e:");
    }
    STAMP(5); WSTAMP(5);
    {
      const float a0 = v[0], a1 = v[1];
      const float x10 = fmaf(ga[0], a0, xin[0]), x11 = fmaf(ga[1], a1, xin[1]);
      *LDSP(u32, tdst) = pack_bf(x10 * cc1[0], x11 * cc1[1]);
      float u = swap32_add(x10 + x11, fmaf(x10, x10, x11 * x11));
      u += dpp<XOR1>(u); u += dpp<XOR2>(u); u += dpp<XOR7>(u); u += dpp<XOR8>(u);
      *LDSP(float, sdst) = u;
    }
    STAMP(16);
    LDS_BARRIER();
    STAMP(17);
    {
      const f32x4 z = {0.f, 0.f, 0.f, 0.f};
      const u32x4 af0 = LDSP(u32x4, apa)[0], af1 = LDSP(u32x4, apa)[32];
      float s = LDSP(float, ssrc)[0], q = LDSP(float, ssrc)[32];
      SB();
      f32x4 d[4];
#pragma unroll
      for (int j = 0; j < 4; ++j) d[j] = mfma16(af0, bf[j], z);
#pragma unroll
      for (int j = 0; j < 4; ++j) d[j] = mfma16(af1, bf[4 + j], d[j]);
      s += dpp<XOR1>(s); q += dpp<XOR1>(q); s += dpp<XOR2>(s); q += dpp<XOR2>(q); s += dpp<XOR7>(s); q += dpp<XOR7>(q);
      const float mean = s * (1.f / 512.f);
      const float var = fmaf(q, 1.f / 512.f, -mean * mean);
      const float ri = __builtin_amdgcn_rsqf(fmaxf(var, 0.f) + 1e-5f);
      const float cf = fmaf(-ri * mean, Afp, Bfp);
      float y = (d[0][0] + d[1][1]) + (d[2][2] + d[3][3]);
      y = __builtin_bit_cast(float, __builtin_amdgcn_update_dpp(__builtin_bit_cast(int, y), __builtin_bit_cast(int, y), XOR8, 0xa, 0xf, false));
      y = swap16_add(y, y);
      y = fmaf(y, ri, cf);
      const float up = dpp<0x104>(y);
      *LDSP(u16, adst) = (u16)pack_bf(silu_f(y) * up, 0.f);
    }
    STAMP(18);
    LDS_BARRIER();
    STAMP(10);
    {
      const u32x4 af = *LDSP(u32x4, asrc);
      const f32x4 z = {0.f, 0.f, 0.f, 0.f};
      const f32x4 d0 = mfma16(af, b2[0], z), d1 = mfma16(af, b2[1], z), d2 = mfma16(af, b2[2], z), d3 = mfma16(af, b2[3], z);
      const bool g1 = g & 1, g2 = g & 2;
      const float e0 = g1 ? d1[0] : d0[0], e1 = g1 ? d1[1] : d0[1], f0 = g1 ? d3[0] : d2[0], f1 = g1 ? d3[1] : d2[1];
      st8_sc1(wsr, ooff, pack_bf(g2 ? f0 : e0, g2 ? f1 : e1), tag);
    }
    asm volatile(".Lend_b:");
    STAMP(6);
    FINISH();
  }
  if (wave == 0) {
    const int d = fd, ul = lane >> 3;
    u32 gm[2] = {sg[0][0], sg[1][0]};
    if (!all_ok((sg[0][1] == tag) & (sg[1][1] == tag))) gather8<2>(wsr, G_MOD + (1024 + d) * 8, 1536 * 8, tag, gm);
    u32 xa0, xa1;
    {
      due = t1 + P.N2 * 256; gate();
      u32x4 gn[2] = {ld16<AUX_SC1>(wsr, G_NT + lane * 16, 0), ld16<AUX_SC1>(wsr, G_NT + lane * 16, 1024)};
      const int tn = notices(IC<2>{}, G_NT + lane * 16, gn) * 256;
      due = (tn > t1 + 2 * 256 ? max(tn, t1 + P.L0 * 256) + P.L1 * 256 : t1) + P.X3 * 256;
      until(due);
    }
#pragma nounroll
    for (int spin = 0; spin < SPIN_MAX; ++spin) {
      CFENCE();
      xa0 = __builtin_amdgcn_raw_buffer_load_b32(wsr, G_XP + ul * 2048 + (d >> 1) * 4, 0, AUX_SC1);
      xa1 = __builtin_amdgcn_raw_buffer_load_b32(wsr, G_XP + ul * 2048 + (d >> 1) * 4, 1024, AUX_SC1);
      if (all_ok(max(xa0, xa1) != SENT)) break;
      __builtin_amdgcn_s_sleep(1);
    }
    float a0 = (d & 1) ? hi_f(xa0) : lo_f(xa0), a1 = (d & 1) ? hi_f(xa1) : lo_f(xa1);
    a0 += dpp<XOR8>(a0); a1 += dpp<XOR8>(a1);
    a0 = swap16_add(a0, a0); a1 = swap16_add(a1, a1);
    a0 = swap32_add(a0, a0); a1 = swap32_add(a1, a1);
    const bool hb = g & 1;
    float xv = rbf(((d & 1) ? hi_f(sx2) : lo_f(sx2)) + rbf((hb ? hi_f(gm[0]) : lo_f(gm[0])) * rbf(hb ? a1 : a0)));
    float gv = hb ? hi_f(gm[1]) : lo_f(gm[1]);
    PIN(xv); PIN(gv);
    if (wi == 0 && lane == 0) __builtin_amdgcn_raw_buffer_store_b32(tag, wsr, WS_EPOCH, 0, AUX_SC1);
    STAMP(10);
    due = max(due + P.X4 * 256, (int)(((u32)wall_clock64() - t0) << 8) + P.GX * 256);
    const bool last = ul + 40 < NUP;
    u32 o0 = G_OP + (ul * 512 + d) * 8, o5 = last ? o0 : OOB, tg5 = last ? tag : 0u;
    const rsrc_t rout = mk_rsrc(out, 2048u);
    u32 oo = (lane & 40) == 0 ? (g * 512 + d) * 2 : OOB;
    const u32 pk = c == 0 ? 0x00003f80u : (c == 4 ? 0x3f800000u : 0u);
    u32x4 one = {pk, 0u, pk, 0u};
    PIN(o0); PIN(o5); PIN(tg5); PIN(oo); PIN(one);
    __builtin_amdgcn_raw_buffer_store_b16((u16)pack_bf(xv, 0.f), rout, oo, 0, AUX_SC1);
    until(due);
    f32x4 sv;
#pragma nounroll
    for (int spin = 0; spin < SPIN_MAX; ++spin) {
      CFENCE();
      u32x2 og[6];
#pragma unroll
      for (int k = 0; k < 5; ++k) og[k] = ld8<AUX_SC1>(wsr, o0, k * 32768);
      og[5] = ld8<AUX_SC1>(wsr, o5, 5 * 32768);
      sv = f32x4{0.f, 0.f, 0.f, 0.f};
#pragma unroll
      for (int k = 0; k < 3; ++k) sv = mfma16(one, u32x4{og[2 * k][0], og[2 * k][1], og[2 * k + 1][0], og[2 * k + 1][1]}, sv);
      const u32 bad = ((og[0][1] ^ tag) | (og[1][1] ^ tag) | (og[2][1] ^ tag)) | ((og[3][1] ^ tag) | (og[4][1] ^ tag) | (og[5][1] ^ tg5));
      PIN(sv);
      if (all_ok(bad == 0u)) break;
      __builtin_amdgcn_s_sleep(1);
    }
    STAMP(7);
    const float s = sv[0] + dpp<XOR8>(sv[0]);
    __builtin_amdgcn_raw_buffer_store_b16((u16)pack_bf(fmaf(gv, s, xv), 0.f), rout, oo, 0, AUX_SC1);
    asm volatile(".Lend_c:");
  }
  FINISH();
}

extern "C" void kernel_launch(void* const* d_in, const int* in_sizes, int n_in,
                              void* d_out, int out_size, void* d_ws, size_t ws_size,
                              hipStream_t stream) {
  fused_kernel<<<256, 512, 0, stream>>>((const u16*)d_in[1], (const u16*)d_in[2], (const u16*)d_in[4], (const u16*)d_in[3], (const u16*)d_in[10], (const u16*)d_in[6], (const u16*)d_in[7],
                                        (unsigned char*)d_ws, (const u16*)d_in[0], (const u16*)d_in[9], (const u16*)d_in[8], (u16*)d_out);
}
```

```cpp
#define PROBE_MUL 10
#include <hip/hip_runtime.h>
#include <stdint.h>

typedef unsigned int u32;
typedef unsigned short u16;
typedef unsigned long long u64;
typedef __attribute__((ext_vector_type(4))) unsigned u32x4;
typedef __attribute__((ext_vector_type(2))) unsigned u32x2;
typedef __attribute__((ext_vector_type(2))) __bf16 bf16x2;
typedef __attribute__((ext_vector_type(4))) float f32x4;
typedef __attribute__((ext_vector_type(2))) float f32x2;
typedef __amdgpu_buffer_rsrc_t rsrc_t;

constexpr int NH = 32, SK = 16384, DH = 128, WIN = 4096, FF = 1376;
struct Prm { int E0, ES, E1, E2, E2C, EU, TEND, DT_UP, L0, L1, X1, X2, X3, X4, NE, FE, M0, M1, M2, M3, M4, GS, GX, N1, N2, GA, WD, DT_WO, LW, LU, EW, PM, NF; };
constexpr Prm PRM0 = {32, 24, 32, 32, 56, 64, 2010, 60, 30, 25, 92, 86, 131, 88, 4, 12, 5, 8, 21, 4, 32, 0, 56, 28, 70, 0, 4, 100, 45, 15, 8, 0x24310, 1};
constexpr int NL = 37;
constexpr int NCMIN = 192;
constexpr int WK = 8, WV = 5;
constexpr int WUSE = 12, LNS = 7;
constexpr int QP = 14, QD = 4;
constexpr int VQ = 6;
constexpr int NSL = (NL + 7) / 8;
constexpr int VS = VQ + NSL;
constexpr int MLS = VS + 2;
constexpr int VSIDE = MLS - 5, UPREP = MLS + 8;
constexpr int SIDE = NCMIN / 8 - 4;
constexpr int CPF = 18;
constexpr int SMO = 1;
constexpr int TBP = 4, TBL = 11, TBU = 15;
constexpr int cons_end(int v) {
  const int a = v - WV + 1, c = (v - MLS) * 4;
  const int m = a < c ? a : c;
  return m < 0 ? 0 : (m > NL ? NL : m);
}
struct Tab { u32 e[512]; };
constexpr Tab make_tab(const Prm& P) {
  Tab T{};
  for (int bid = 0; bid < 256; ++bid) {
    const int xcd = bid & 7, part = bid >> 4 & 3, i8 = (bid >> 3 & 1) | bid >> 6 << 1, wi = i8 * 8 + xcd;
    const bool has_mod = wi < 48, has_up = wi >= 21;
    const int cb0 = (i8 + 5) & 7, cb1 = (i8 + 4) & 7, ms = wi - 20;
    const int mi = wi < 8 ? wi : (wi < 16 ? 8 + ((xcd + 4) & 7) : (wi < 20 ? (wi - 14) * 8 + 3 : 16 + ms / 7 * 8 + ms % 7 + (ms % 7 >= 3 ? 1 : 0)));
    const int ui = xcd * 5 + (xcd > 5 ? xcd - 5 : 0) + (i8 >= 3 ? (xcd >= 5 ? 1 : 0) + i8 - 3 : 0);
    const int wd = cb0 == 3 ? 0 : P.WD;
    const int e[4] = {P.E0 - 4 * wd + (cb0 == 3 ? P.ES : 0), P.E1 + (cb1 == 3 ? P.ES : 0), has_mod ? (wi < 16 ? P.E2C : P.E2) + ((mi & 7) == 3 ? P.ES : 0) : 0, has_up ? P.EU : 0};
    const int ew = wd ? P.EW : 0, pa = (1024 + e[0] + ew + e[1] + e[2] + e[3]) / 4;
    int c[5] = {0, 0, 0, 0, 124};
    for (int k = 0; k < 3; ++k) c[k + 1] = c[k] + (pa - e[k] - (k == 0 ? ew : 0) + 4) / 8 - 1;
    const int nl = c[part + 1] - c[part] + 1, wst = e[part] / 4;
    const bool has_w = part < 2 || (part == 2 ? has_mod : has_up);
    const int blk = !has_w ? 0 : (part == 0 ? cb0 : (part == 1 ? xcd * 8 + cb1 : (part == 2 ? mi : ui)));
    const int tau = (part == 3 && has_up ? P.TEND - P.DT_UP : (part == 0 && wd ? P.TEND - P.DT_WO : P.TEND)) * 1024 / (4 * (2 * nl + wst - 2) + P.NF);
    T.e[2 * bid] = (u32)(c[part] | nl << 7 | blk << 13 | (has_w ? 1 : 0) << 19 | (part == 0 ? wd : 0) << 20);
    T.e[2 * bid + 1] = (u32)(tau | (tau * wst / (part == 0 && wd < 8 ? 8 - wd : 8)) << 16);
  }
  return T;
}
constexpr int step_range(const Prm& P, bool mx_) {
  int mx = 0, mn = 1 << 20;
  const Tab T = make_tab(P);
  for (int bid = 0; bid < 256; ++bid) { const int n = T.e[2 * bid] >> 7 & 63; mx = n > mx ? n : mx; mn = n < mn ? n : mn; }
  return mx_ ? mx : mn;
}
extern "C" __attribute__((used, visibility("protected"))) __constant__ const Tab wg_tab = make_tab(PRM0);
static_assert(step_range(PRM0, true) <= NL && step_range(PRM0, false) >= NCMIN / 8, "a wave's key range does not fit its load steps");
static_assert(cons_end(SIDE + WV + 1) <= NCMIN / 8 && cons_end(UPREP) <= NCMIN / 8 && SIDE > MLS + 3, "steps every wave runs");
constexpr int NUP = FF / 32;
constexpr int SPIN_MAX = 1 << 17;

constexpr u32 WS_EPOCH = 0;
constexpr u32 G_TB = 1024;
constexpr u32 G_NT = 2048;
constexpr u32 G_MOD = 4096;
constexpr u32 G_Q = G_MOD + 3072 * 8;
constexpr u32 G_ML = G_Q + 4096 * 8;
constexpr u32 G_P = G_ML + 256 * 16;
constexpr u32 G_XP = G_P + 256 * 256;
constexpr u32 G_OP = G_XP + 8 * 512 * 4;
constexpr u32 WS_END = G_OP + 43 * 512 * 8;
constexpr u32 SENT = 0xffffffffu;
constexpr u32 OOB = 0x40000000u;
#define WST 0

constexpr int AUX_NT = 2, AUX_SC1 = 16;

__device__ __forceinline__ float lo_f(u32 v) { return __uint_as_float(v << 16); }
__device__ __forceinline__ float hi_f(u32 v) { return __uint_as_float(v & 0xffff0000u); }
__device__ __forceinline__ float rbf(float x) { return (float)(__bf16)x; }
__device__ __forceinline__ u32 pack_bf(float lo, float hi) {
  bf16x2 r; r[0] = (__bf16)lo; r[1] = (__bf16)hi; return __builtin_bit_cast(u32, r);
}
__device__ __forceinline__ float dot2(u32 k, u32 q, float acc) {
  return __builtin_amdgcn_fdot2_f32_bf16(__builtin_bit_cast(bf16x2, k), __builtin_bit_cast(bf16x2, q), acc, false);
}
template <int CTRL> __device__ __forceinline__ float dpp(float x) {
  return __builtin_bit_cast(float, __builtin_amdgcn_mov_dpp(__builtin_bit_cast(int, x), CTRL, 0xf, 0xf, true));
}
constexpr int XOR1 = 0xB1, XOR2 = 0x4E, XOR7 = 0x141, XOR8 = 0x128;
__device__ __forceinline__ float swap32_add(float a, float b) {
  auto t = __builtin_amdgcn_permlane32_swap(__float_as_uint(a), __float_as_uint(b), false, false);
  return __uint_as_float(t[0]) + __uint_as_float(t[1]);
}
__device__ __forceinline__ float swap16_add(float a, float b) {
  auto t = __builtin_amdgcn_permlane16_swap(__float_as_uint(a), __float_as_uint(b), false, false);
  return __uint_as_float(t[0]) + __uint_as_float(t[1]);
}
__device__ __forceinline__ float wave_sum(float x) {
  x += dpp<XOR1>(x); x += dpp<XOR2>(x); x += dpp<XOR7>(x); x += dpp<XOR8>(x);
  x = swap16_add(x, x);
  return swap32_add(x, x);
}
__device__ __forceinline__ float wave_max(float x) {
  x = fmaxf(x, dpp<XOR1>(x)); x = fmaxf(x, dpp<XOR2>(x)); x = fmaxf(x, dpp<XOR7>(x)); x = fmaxf(x, dpp<XOR8>(x));
  auto s = __builtin_amdgcn_permlane16_swap(__float_as_uint(x), __float_as_uint(x), false, false);
  x = fmaxf(__uint_as_float(s[0]), __uint_as_float(s[1]));
  auto t = __builtin_amdgcn_permlane32_swap(__float_as_uint(x), __float_as_uint(x), false, false);
  return fmaxf(__uint_as_float(t[0]), __uint_as_float(t[1]));
}
template <int CTRL> __device__ __forceinline__ float sel_add(float lo, float hi, bool up) {
  const float t1 = lo + dpp<CTRL>(lo), t2 = hi + dpp<CTRL>(hi);
  return up ? t2 : t1;
}
template <int I> __device__ __forceinline__ float bc16(float x) {
  return __builtin_bit_cast(float, __builtin_amdgcn_update_dpp(0, __builtin_bit_cast(int, x), 0x150 + I, 0xf, 0xf, false));
}
__device__ __forceinline__ float bcast16(float x, int i) {
  switch (i) {
    case 0: return bc16<0>(x); case 1: return bc16<1>(x); case 2: return bc16<2>(x); case 3: return bc16<3>(x);
    case 4: return bc16<4>(x); case 5: return bc16<5>(x); case 6: return bc16<6>(x); case 7: return bc16<7>(x);
    case 8: return bc16<8>(x); case 9: return bc16<9>(x); case 10: return bc16<10>(x); case 11: return bc16<11>(x);
    case 12: return bc16<12>(x); case 13: return bc16<13>(x); case 14: return bc16<14>(x); default: return bc16<15>(x);
  }
}
__device__ __forceinline__ float silu_f(float x) { return x * __builtin_amdgcn_rcpf(1.f + __expf(-x)); }

template <int AUX> __device__ __forceinline__ u32x4 ld16(rsrc_t r, u32 voff, u32 soff) {
  return __builtin_amdgcn_raw_buffer_load_b128(r, voff, soff, AUX);
}
template <int AUX> __device__ __forceinline__ u32x2 ld8(rsrc_t r, u32 voff, u32 soff) {
  return __builtin_amdgcn_raw_buffer_load_b64(r, voff, soff, AUX);
}
__device__ __forceinline__ void st8_sc1(rsrc_t r, u32 voff, u32 v, u32 tag) {
  __builtin_amdgcn_raw_buffer_store_b64(u32x2{v, tag}, r, voff, 0, AUX_SC1);
}
__device__ __forceinline__ void st16_sc1(rsrc_t r, u32 voff, u32 v0, u32 v1, u32 tag) {
  __builtin_amdgcn_raw_buffer_store_b128(u32x4{v0, tag, v1, tag}, r, voff, 0, AUX_SC1);
}
__device__ __forceinline__ rsrc_t mk_rsrc(const void* p, u32 bytes) {
  return __builtin_amdgcn_make_buffer_rsrc(const_cast<void*>(p), 0, bytes, 0x00020000);
}
#define WAIT_VM(n) __builtin_amdgcn_s_waitcnt(((n) & 15) | (((n) >> 4) << 14) | (7 << 4) | (15 << 8))
#define SB() __builtin_amdgcn_sched_barrier(0)
#define CFENCE() asm volatile("" ::: "memory")
__device__ __forceinline__ bool all_ok(bool ok) { return __builtin_amdgcn_ballot_w64(!ok) == 0ull; }

template <int N> __device__ __forceinline__ void gather16(rsrc_t ws, u32 voff, u32 stride, u32 tag, u32x4 (&g)[N]) {
#pragma nounroll
  for (int spin = 0; spin < SPIN_MAX; ++spin) {
    CFENCE();
    bool ok = true;
#pragma unroll
    for (int i = 0; i < N; ++i) g[i] = ld16<AUX_SC1>(ws, voff + i * stride, 0);
#pragma unroll
    for (int i = 0; i < N; ++i) ok &= (g[i][1] == tag) & (g[i][3] == tag);
    if (all_ok(ok)) break;
    __builtin_amdgcn_s_sleep(8);
  }
}
template <int N> __device__ __forceinline__ void gather8(rsrc_t ws, u32 voff, u32 stride, u32 tag, u32 (&g)[N], bool act = true) {
#pragma nounroll
  for (int spin = 0; spin < SPIN_MAX; ++spin) {
    CFENCE();
    bool ok = true;
    u32x2 t[N];
#pragma unroll
    for (int i = 0; i < N; ++i) t[i] = ld8<AUX_SC1>(ws, voff + i * stride, 0);
#pragma unroll
    for (int i = 0; i < N; ++i) { ok &= (t[i][1] == tag); g[i] = t[i][0]; }
    if (all_ok(ok | !act)) break;
    __builtin_amdgcn_s_sleep(8);
  }
}

struct Smem {
  float red[8][128];
  float ml[8][2];
  float r2[64 * 20];
  u16 hb[3 * 1024];
  float stat[8][64];
  float red2[8][128];
  u32 dummy[64];
  u32x2 bc;
  u32x4 zero[4];
  u32 mflag[8];
  u32x4 wbuf[8][8][64];
  __attribute__((aligned(256))) u16 w2b[32][512];
  u16 actl[2][32];
};

#define PROBE_K 103
#define PROBE_OP 1
#define PROBE_PART -1
#define STAMP(k) do { if ((k) == PROBE_K && tid == 0) pst = wall_clock64(); } while (0)
#define WSTAMP(k) do {} while (0)
#define FINISH() do { STAMP(8); if (tid == 0) { __hip_atomic_store(reinterpret_cast<u64*>(ws + (1u << 20)) + bid * 2, pt0, __ATOMIC_RELAXED, __HIP_MEMORY_SCOPE_AGENT); \
   __hip_atomic_store(reinterpret_cast<u64*>(ws + (1u << 20)) + bid * 2 + 1, (PROBE_PART < 0 || part == PROBE_PART) ? pst : 0ull, __ATOMIC_RELAXED, __HIP_MEMORY_SCOPE_AGENT); } \
   if (bid == 8 && wave == 0) probe_end(ws, pt0, lane); return; } while (0)
#define PIN(x) asm volatile("" : "+v"(x))
#define LDS_BARRIER() do { __builtin_amdgcn_s_waitcnt(0xc07f); __builtin_amdgcn_s_barrier(); CFENCE(); } while (0)
#define WAVE_SYNC() do { __builtin_amdgcn_fence(__ATOMIC_RELEASE, "wavefront"); __builtin_amdgcn_wave_barrier(); __builtin_amdgcn_fence(__ATOMIC_ACQUIRE, "wavefront"); } while (0)

__device__ __forceinline__ void dma16(rsrc_t r, u32 voff, u32 soff, u32 lds_dst) {
  u32 keep;
  asm volatile("s_mov_b32 %0, m0\n\ts_mov_b32 m0, %4\n\ts_nop 4\n\tbuffer_load_dwordx4 %1, %2, %3 offen nt lds\n\ts_mov_b32 m0, %0"
               : "=&s"(keep) : "v"(voff), "s"(r), "s"(soff), "s"(lds_dst) : "memory");
}
__device__ __forceinline__ u32 perm_lo(u32 a, u32 b) { return __builtin_amdgcn_perm(b, a, 0x05040100u); }
__device__ __forceinline__ u32 perm_hi(u32 a, u32 b) { return __builtin_amdgcn_perm(b, a, 0x07060302u); }
__device__ __forceinline__ float rdl(float x, int l) { return __int_as_float(__builtin_amdgcn_readlane(__float_as_int(x), l)); }

typedef __attribute__((ext_vector_type(8))) __bf16 bf16x8;
__device__ __forceinline__ f32x4 mfma16(u32x4 a, u32x4 b, f32x4 c) {
  return __builtin_amdgcn_mfma_f32_16x16x32_bf16(__builtin_bit_cast(bf16x8, a), __builtin_bit_cast(bf16x8, b), c, 0, 0, 0);
}
__device__ __forceinline__ float sum8(const float* p) {
  const f32x4 a = *reinterpret_cast<const f32x4*>(p), b = *reinterpret_cast<const f32x4*>(p + 4);
  return ((a[0] + b[0]) + (a[1] + b[1])) + ((a[2] + b[2]) + (a[3] + b[3]));
}
__device__ __forceinline__ float ln_ri(float var) {
  return __builtin_amdgcn_rcpf(rbf(__builtin_amdgcn_sqrtf(rbf(rbf(fmaxf(var, 0.f)) + 1e-5f))));
}
__device__ __forceinline__ void ln_stats2(const float (&x0)[8], const float (&x1)[8], float& mb0, float& ri0, float& mb1, float& ri1) {
  const float k0 = rdl(x0[0], 0), k1 = rdl(x1[0], 0);
  float s0 = 0.f, s1 = 0.f, q0 = 0.f, q1 = 0.f;
#pragma unroll
  for (int k = 0; k < 8; ++k) {
    const float d0 = x0[k] - k0, d1 = x1[k] - k1;
    s0 += d0; s1 += d1; q0 = fmaf(d0, d0, q0); q1 = fmaf(d1, d1, q1);
  }
  float t = swap16_add(swap32_add(s0, s1), swap32_add(q0, q1));
  t += dpp<XOR1>(t); t += dpp<XOR2>(t); t += dpp<XOR7>(t); t += dpp<XOR8>(t);
  auto u = __builtin_amdgcn_permlane16_swap(__float_as_uint(t), __float_as_uint(t), false, false);
  const float ms = __uint_as_float(u[0]) * (1.f / 512.f), mq = __uint_as_float(u[1]) * (1.f / 512.f);
  const float ri = ln_ri(fmaf(-ms, ms, mq));
  mb0 = rbf(k0 + rdl(ms, 0)); mb1 = rbf(k1 + rdl(ms, 32));
  ri0 = rdl(ri, 0); ri1 = rdl(ri, 32);
}
template <int I> struct IC { static constexpr int v = I; };
template <int I, int N, class F> __device__ __forceinline__ void sfor(F&& f) {
  if constexpr (I < N) { if (f(IC<I>{})) sfor<I + 1, N>(f); }
}
typedef __attribute__((ext_vector_type(4))) short s16x4;
typedef __attribute__((address_space(3))) s16x4* lds_s16x4p;
__device__ __forceinline__ u32x2 tr_read(u32 lds_addr) {
  return __builtin_bit_cast(u32x2, __builtin_amdgcn_ds_read_tr16_b64_v4i16((lds_s16x4p)lds_addr));
}
__device__ __forceinline__ u32 next_tag(u32 e) { return ((e | 0x40004000u) + 1u) & ~0x40004000u; }
__device__ __forceinline__ float u2f(u32 v) { return __uint_as_float(v); }
#define LDSP(T, a) ((__attribute__((address_space(3))) T*)(a))
template <class T> __device__ __forceinline__ u32 lds_addr(T* p) { u32 a = (u32)(uintptr_t)p; PIN(a); return a; }

#define TAB_LOAD "s_add_u32 s30, s28, wg_tab@rel32@lo+4\ns_addc_u32 s31, s29, wg_tab@rel32@hi+12\nglobal_load_dwordx2 %[te], %[va], s[30:31]\n"
__device__ __forceinline__ void probe_end(unsigned char* ws, u64 pt0, int lane) {
  while (wall_clock64() < pt0 + 5500ull) __builtin_amdgcn_s_sleep(8);
  u64 a[4], c2[4];
  for (int i = 0; i < 4; ++i) {
    a[i] = __hip_atomic_load(reinterpret_cast<u64*>(ws + (1u << 20)) + (i * 64 + lane) * 2, __ATOMIC_RELAXED, __HIP_MEMORY_SCOPE_AGENT);
    c2[i] = __hip_atomic_load(reinterpret_cast<u64*>(ws + (1u << 20)) + (i * 64 + lane) * 2 + 1, __ATOMIC_RELAXED, __HIP_MEMORY_SCOPE_AGENT);
  }
  int mn0 = 1 << 30, mx1 = -(1 << 30), mn1 = 1 << 30, sum = 0, cnt = 0;
  for (int i = 0; i < 4; ++i) {
    const int d0 = (int)(a[i] - pt0);
    mn0 = min(mn0, d0);
    if (c2[i] != 0ull) { const int d1 = (int)(c2[i] - pt0); mx1 = max(mx1, d1); mn1 = min(mn1, d1); sum += d1; cnt += 1; }
  }
  for (int o = 1; o < 64; o <<= 1) { mn0 = min(mn0, __shfl_xor(mn0, o)); mx1 = max(mx1, __shfl_xor(mx1, o)); mn1 = min(mn1, __shfl_xor(mn1, o)); sum += __shfl_xor(sum, o); cnt += __shfl_xor(cnt, o); }
  int X = PROBE_OP == 0 ? mx1 : (PROBE_OP == 1 ? sum / max(cnt, 1) : mn1);
  if (PROBE_K == 102) X = mn0;
  X = mn0 + (X - mn0) * PROBE_MUL;
  const u64 tg = pt0 + (u64)(long long)(X + 8000);
  for (int i = 0; i < 400000 && wall_clock64() < tg; ++i) __builtin_amdgcn_s_sleep(1);
}
__global__ void __launch_bounds__(512, 2) fused_kernel(const u16* kc_, const u16* vc_, const u16* wo_, const u16* wq_, const u16* wmod_, const u16* w1_, const u16* w3_,
                                                       unsigned char* ws_, const u16* x_, const u16* cnd_, const u16* w2_, u16* out_) {
  __shared__ __attribute__((aligned(256))) Smem sm;
  const u64 a_ka = (u64)__builtin_amdgcn_kernarg_segment_ptr();
  const int bid = blockIdx.x, tid = threadIdx.x;
  u64 wa, t64; u32x2 te; int tid0;
  u32 ar[14];
  u32 tch2, sa_, sb_, va_;
  u32x4 kr[NL], vr[NL];
  asm volatile(
    "s_load_dwordx16 s[4:19], %[ka], 0x0\ns_load_dwordx8 s[20:27], %[ka], 0x40\n"
    "s_memrealtime %[t]\n"
    "v_mov_b32 %[va], 0\nglobal_load_dwordx2 %[te], %[va], %[ka]\ns_waitcnt vmcnt(0)\ns_memrealtime s[36:37]\nv_lshlrev_b32 %[va], 3, %[bid]\n"
    "s_getpc_b64 s[28:29]\n.Lcpf_first:\n"
    TAB_LOAD
    "s_cmp_gt_u32 %[bid], 15\ns_cbranch_scc1 .Lno_cpf\n"
    "s_mov_b32 %[sa], .Lend_a-.Lcpf_first\ns_max_i32 %[sa], %[sa], .Lend_b-.Lcpf_first\ns_max_i32 %[sa], %[sa], .Lend_c-.Lcpf_first\ns_max_i32 %[sa], %[sa], .Lend_d-.Lcpf_first\ns_max_i32 %[sa], %[sa], .Lend_e-.Lcpf_first\n"
    "v_lshlrev_b32 %[va], 6, %[tid]\nv_min_u32 %[va], %[sa], %[va]\nglobal_load_dword %[pf], %[va], s[28:29]\n"
    ".Lno_cpf:\n"
    "s_and_b32 %[sa], %[bid], 15\ns_lshr_b32 %[sb], %[bid], 6\ns_lshl2_add_u32 %[sa], %[sa], %[sb]\ns_lshl_b32 %[sa], %[sa], 22\ns_xor_b32 %[sa], %[sa], 0xff0000\n"
    "s_and_b32 %[sb], %[bid], 48\nv_lshl_add_u32 %[va], %[sb], 5, %[tid]\nv_lshl_add_u32 %[va], %[va], 4, %[sa]\n"
    "v_readfirstlane_b32 %[t0], %[tid]\n"
    "s_waitcnt lgkmcnt(0)\ns_mov_b32 %[sb], s36\nglobal_load_dwordx4 %[k0], %[va], s[4:5] nt\nglobal_load_dwordx4 %[v0], %[va], s[6:7] nt\n"
    "s_bitcmp1_b32 %[t0], 6\ns_cselect_b64 s[14:15], s[16:17], s[14:15]\n"
    "s_bitcmp1_b32 %[bid], 4\ns_cselect_b64 s[8:9], s[10:11], s[8:9]\ns_cselect_b64 s[12:13], s[14:15], s[12:13]\ns_bitcmp1_b32 %[bid], 5\ns_cselect_b64 %[wa], s[12:13], s[8:9]"
    : "=&{s4}"(ar[0]), "=&{s5}"(ar[1]), "=&{s6}"(ar[2]), "=&{s7}"(ar[3]), "=&{s18}"(ar[4]), "=&{s19}"(ar[5]), "=&{s20}"(ar[6]), "=&{s21}"(ar[7]), "=&{s22}"(ar[8]), "=&{s23}"(ar[9]),
      "=&{s24}"(ar[10]), "=&{s25}"(ar[11]), "=&{s26}"(ar[12]), "=&{s27}"(ar[13]), [wa] "=&s"(wa), [t0] "=&s"(tid0), [t] "=&s"(t64), [te] "=&v"(te), [sa] "=&s"(sa_), [sb] "=&s"(sb_),
      [va] "=&v"(va_), [pf] "=&v"(tch2), [k0] "=&v"(kr[0]), [v0] "=&v"(vr[0])
    : [ka] "s"(a_ka), [bid] "s"(bid), [tid] "v"(tid) : "s8", "s9", "s10", "s11", "s12", "s13", "s14", "s15", "s16", "s17", "s28", "s29", "s30", "s31", "s36", "s37", "scc", "memory");
  asm volatile("" : "+s"(ar[0]), "+s"(ar[1]), "+s"(ar[2]), "+s"(ar[3]), "+s"(ar[4]), "+s"(ar[5]), "+s"(ar[6]), "+s"(ar[7]), "+s"(ar[8]), "+s"(ar[9]), "+s"(ar[10]), "+s"(ar[11]), "+s"(ar[12]), "+s"(ar[13]));
  asm volatile("" : "+s"(wa), "+s"(t64), "+s"(tid0));
  const u32 tka = (u32)wall_clock64();
  const u64 pt0 = t64; u64 pst = (PROBE_K == 100 || PROBE_K == 102) ? pt0 : (PROBE_K == 101 ? pt0 + (u32)(tka - (u32)t64) : 0);
  const u32 t0 = tka; const u32 tkb = sb_; if (PROBE_K == 103) pst = pt0 + (u32)(tkb - (u32)t64);
  __builtin_assume(tid >= 0 && tid < 512 && tid0 >= 0 && tid0 < 512 && bid >= 0 && bid < 256);
  constexpr Prm P = PRM0;
  const int TEND = P.TEND;
  const int xcd = bid & 7, part = bid >> 4 & 3;
  const int lane = tid & 63, wave = tid0 >> 6;
  SB();
  const bool p3 = part == 3;
  u32 pitch = (u32)(0x005600c001000020ull >> (part * 16) & 0xffff) * 32u;
  const int sh = p3 ? 2 : 3;
  u32 wv0 = (u32)((p3 ? (wave >> 1) * 128 : wave * 64) + (part == 0 ? xcd * 512 : 0) + (lane >> sh)) * pitch + ((lane & (p3 ? 3 : 7)) ^ (lane >> (p3 ? 5 : 3))) * 16u;
  const u32 wimg = (u32)(uintptr_t)(&sm.wbuf[0][0][0]);
  u32 wsz = (part == 0 ? 4096u : 512u) * pitch, wld = wimg + (p3 ? (wave & 1) * 32768 + (wave >> 1) * 8192 : wave * 8192);
  PIN(wv0);
  asm volatile("" : "+s"(wsz), "+s"(wld), "+s"(pitch));
  auto karg = [&](int i) { return ar[2 * i] | (u64)ar[2 * i + 1] << 32; };
  const u64 a_kc = karg(0), a_vc = karg(1);
  unsigned char* ws = (unsigned char*)karg(2);
  const u16 *x = (const u16*)karg(3), *cnd = (const u16*)karg(4), *w2 = (const u16*)karg(5);
  u16* out = (u16*)karg(6);
  const rsrc_t wsr = mk_rsrc(ws, WS_END);
  asm volatile("s_waitcnt vmcnt(2)" : "+v"(te));
  const u32 te0 = __builtin_amdgcn_readfirstlane(te[0]), te1 = __builtin_amdgcn_readfirstlane(te[1]);
  const int bl0 = te0 & 127, nl = te0 >> 7 & 63, blk = te0 >> 13 & 63;
  const bool has_w = te0 >> 19 & 1;
  const rsrc_t rw = mk_rsrc((const void*)wa, wsz);
  const u32 wstep = pitch << (6 - sh), wflip = p3 ? 32u : 0u;
  u32 wso = blk << (sh + 4);
  int wleft = 8;
  auto piece = [&]() {
    dma16(rw, wv0, wso, wld);
    wso += wstep; wld += 1024;
    wv0 ^= (wleft & 1) ? wflip : 0u;
    --wleft;
  };
  SB();
  STAMP(23);
  const int b = bid >> 3 & 1, head = xcd * 4 + (bid >> 6 ^ 3);
  const int g = lane >> 4, c = lane & 15;
  const int i8 = b | bid >> 6 << 1, wi = i8 * 8 + xcd;
  const bool is_up = p3 && has_w, modc = part == 2 && wi < 16;
  const size_t base = ((size_t)(b * NH + head) * SK + (SK - WIN)) * DH;
  const rsrc_t rk = mk_rsrc((const u16*)a_kc + base, WIN * DH * 2), rv = mk_rsrc((const u16*)a_vc + base, WIN * DH * 2);
  int tau = te1 & 0xffff;
  const int tauw = te1 >> 16;
  int due = P.NF * tau >> 2, now = 0;
  auto gate = [&]() {
    while (now < due) { now = (int)(((u32)wall_clock64() - t0) << 8); if (now < due) __builtin_amdgcn_s_sleep(1); }
    due += tau;
  };
  auto until = [&](int t) { while ((int)(((u32)wall_clock64() - t0) << 8) < t); };
  const u32 pslot = G_P + ((b * 32 + head) * 4 + part) * 256;
  u32x2 ep; u32 xs0, xs1, tch1; u32x4 xr0, xr1;
  const rsrc_t rx = mk_rsrc(part == 2 ? cnd : x, (part == 1 || (part == 2 && has_w)) ? 2048u : 0u);
  auto small_ops = [&]() {
    ep = ld8<AUX_SC1>(wsr, wave == 7 ? WS_EPOCH : OOB, 0);
    xs0 = __builtin_amdgcn_raw_buffer_load_b16(rx, tid * 2, 0, 0); xs1 = __builtin_amdgcn_raw_buffer_load_b16(rx, tid * 2, 1024, 0);
    xr0 = ld16<0>(rx, part == 1 ? lane * 16 : OOB, 0); xr1 = ld16<0>(rx, part == 1 ? lane * 16 : OOB, 1024);
    tch1 = __builtin_amdgcn_raw_buffer_load_b32(mk_rsrc(w2 + (size_t)blk * 32 * 512, is_up ? 64u : 0u), tid == 384 ? 0 : OOB, 0, AUX_NT);
  };
  if (has_w) {
    if (modc) now = 1 << 30;
#pragma nounroll
    while (wleft > (int)(te0 >> 20)) { gate(); due += tauw - tau; piece(); }
    now = 0;
  }
  SB();
  u32 sot;
  {
    const u32 s1 = bl0 + lane - 1, s0 = s1 + (s1 >= 108 ? 4 : 0);
    u32 sb = s0 & 96u;
#pragma unroll
    for (int k = 0; k < 5; ++k) sb |= (s0 >> k & 1u) << (P.PM >> (4 * k) & 7);
    const u32 src = (sb >> 5 & 3u) | (u32)(b * 32 + head) << 2;
    sb ^= ((__builtin_popcount(src & P.M0) & 1) | (__builtin_popcount(src & P.M1) & 1) << 1 | (__builtin_popcount(src & P.M2) & 1) << 2 | (__builtin_popcount(src & P.M3) & 1) << 3 | (__builtin_popcount(src & P.M4) & 1) << 4) & (s1 >= 96 ? 7u : 31u);
    sot = (sb & 7u) << 10 | (u32)wave << 13 | (sb >> 3) << 16;
    sot = s1 >= 120 ? (s1 + 4) << 13 | (u32)wave << 10 : sot;
    sot = lane < nl ? sot : OOB;
    PIN(sot);
  }
  u32 tag = 0;
  u32x4 bf[8];
  auto wo_prep = [&](int s0 = 0, int s1 = 2) {
    const u32 wi_ = wimg + wave * 8192;
    const int q = (lane >> 2) & 3, pp = lane & 3;
#pragma unroll
    for (int s = s0; s < s1; ++s) {
#pragma unroll
      for (int t = 0; t < 4; ++t) {
        const int r0 = s * 32 + g * 8 + q, r1 = r0 + 4, ch = t * 2 + (pp >> 1);
        const u32x2 lo = tr_read(wi_ + r0 * 128 + ((ch ^ (r0 & 7)) * 16) + (pp & 1) * 8), hi = tr_read(wi_ + r1 * 128 + ((ch ^ (r1 & 7)) * 16) + (pp & 1) * 8);
        bf[s * 4 + t] = u32x4{lo[0], lo[1], hi[0], hi[1]};
      }
    }
  };
  auto gemv_go = [&](u32 hh) {
    sm.hb[tid] = (u16)hh; sm.hb[512 + tid] = (u16)(hh >> 16);
    WAVE_SYNC();
    const u32x4* a = (c & 11) == 0 ? reinterpret_cast<const u32x4*>(&sm.hb[(c >> 2) * 512 + wave * 64 + g * 8]) : &sm.zero[0];
    const u32x4 a0 = a[0], a1 = a[(c & 11) == 0 ? 4 : 0];
    const f32x4 z = {0.f, 0.f, 0.f, 0.f};
    f32x4 d[4];
#pragma unroll
    for (int t = 0; t < 4; ++t) d[t] = mfma16(a0, bf[t], z);
#pragma unroll
    for (int t = 0; t < 4; ++t) d[t] = mfma16(a1, bf[4 + t], d[t]);
    if (lane < 32) {
#pragma unroll
      for (int t = 0; t < 4; ++t) sm.r2[(t * 16 + c) * 20 + g * 8 + wave] = d[t][0];
    }
  };
  auto mod_stage = [&]() {
    wo_prep();
    gemv_go(pack_bf(silu_f(lo_f(xs0)), silu_f(lo_f(xs1))));
    __builtin_amdgcn_s_waitcnt(0xc07f);
    if (lane == 0) *LDSP(volatile u32, (u32)(uintptr_t)&sm.mflag[wave]) = 1u;
    if (wave == 7) {
#pragma nounroll
      for (int spin = 0; spin < SPIN_MAX; ++spin) {
        const u32 f = *LDSP(volatile u32, (u32)(uintptr_t)&sm.mflag[lane & 7]);
        if (all_ok(f != 0u)) break;
        __builtin_amdgcn_s_sleep(1);
      }
      CFENCE();
      st8_sc1(wsr, G_MOD + (blk * 64 + lane) * 8, pack_bf(sum8(&sm.r2[lane * 20]), sum8(&sm.r2[lane * 20 + 8])), tag);
    }
    STAMP(9);
  };
  if (lane < 4) sm.zero[lane] = u32x4{0u, 0u, 0u, 0u};
  if (part == 2 && has_w) {
    if (tid < 8) sm.mflag[tid] = 0u;
    LDS_BARRIER();
    if (__builtin_expect(modc, 0)) {
      small_ops();
      __builtin_amdgcn_raw_buffer_store_b128(u32x4{SENT, SENT, SENT, SENT}, wsr, tid < 320 ? G_P + wi * 5120 + tid * 16 : OOB, 0, AUX_SC1);
      asm volatile("s_waitcnt vmcnt(0)" ::: "memory");
      tag = next_tag(__builtin_amdgcn_readfirstlane(ep[0]));
      mod_stage();
      __builtin_amdgcn_s_barrier();
    }
  }
  SB();
  const int fd = wi * 8 + (lane & 7);
  const int ub = wave >> 2, ud = (wave & 3) * 128 + c * 8 + g * 2;
  u32x4 sg[3]; u32 sx2;
  auto side_loads = [&]() {
    const bool fin = part == 1 && wave == 0;
    sg[0] = ld16<AUX_SC1>(wsr, is_up ? G_MOD + (1024 + ud) * 8 : (fin ? G_MOD + (1024 + fd) * 8 : OOB), 0);
    sg[1] = ld16<AUX_SC1>(wsr, is_up ? G_MOD + (1536 + ud) * 8 : (fin ? G_MOD + (2560 + fd) * 8 : OOB), 0);
    sg[2] = ld16<AUX_SC1>(wsr, is_up ? G_MOD + (2048 + ud) * 8 : OOB, 0);
    sx2 = __builtin_amdgcn_raw_buffer_load_b32(mk_rsrc(x, (is_up || fin) ? 2048u : 0u), is_up ? ub * 1024 + ud * 2 : (g & 1) * 1024 + (fd >> 1) * 4, 0, 0);
  };
  auto side_pin = [&]() { PIN(sg[0]); PIN(sg[1]); PIN(sg[2]); PIN(sx2); };
  const u32 mlo = G_ML + (head * 2 + b) * 64 + (lane & 3) * 16;
  float fmrg = 0.f;
  bool fok = wave != 0;
  auto merge_factor = [&](const u32x4& ml) {
    const float mi = u2f(ml[0]);
    float M = fmaxf(mi, dpp<XOR1>(mi)); M = fmaxf(M, dpp<XOR2>(M));
    const float e = __expf(mi - M);
    float L = e * u2f(ml[2]);
    L += dpp<XOR1>(L); L += dpp<XOR2>(L);
    fmrg = rdl(e * __builtin_amdgcn_rcpf(L), part);
  };
  f32x2 ga = {0.f, 0.f}, xin = {0.f, 0.f}, cc1 = {0.f, 0.f};
  float Af = 0.f, Bf = 0.f;
  const int hpos = ub * 512 + ud;
  auto up_prep = [&]() {
    if (!all_ok((sg[0][1] == tag) & (sg[0][3] == tag) & (sg[1][1] == tag) & (sg[1][3] == tag) & (sg[2][1] == tag) & (sg[2][3] == tag))) gather16<3>(wsr, G_MOD + (1024 + ud) * 8, 512 * 8, tag, sg);
    ga = f32x2{ub ? hi_f(sg[0][0]) : lo_f(sg[0][0]), ub ? hi_f(sg[0][2]) : lo_f(sg[0][2])};
    cc1 = f32x2{rbf(1.f + (ub ? hi_f(sg[2][0]) : lo_f(sg[2][0]))), rbf(1.f + (ub ? hi_f(sg[2][2]) : lo_f(sg[2][2])))};
    xin = f32x2{lo_f(sx2), hi_f(sx2)};
    u32* hp = reinterpret_cast<u32*>(&sm.hb[hpos]);
    hp[512] = pack_bf(cc1[0], cc1[1]);
    hp[1024] = ub ? perm_hi(sg[1][0], sg[1][2]) : perm_lo(sg[1][0], sg[1][2]);
  };
  auto up_frags = [&]() {
    const int pp = lane & 3;
    const u32 a = wimg + (pp & 1) * 32768 + ((pp >> 1) * 32 + g * 8 + ((lane >> 2) & 3)) * 64 + (((wave >> 1) ^ ((g & 1) | (pp & 2))) * 16) + (wave & 1) * 8;
#pragma unroll
    for (int rb = 0; rb < 8; ++rb) {
      const u32x2 lo = tr_read(a + rb * 4096), hi = tr_read(a + rb * 4096 + 256);
      bf[rb] = u32x4{lo[0], lo[1], hi[0], hi[1]};
    }
  };
  const u32x4* ap = reinterpret_cast<const u32x4*>(&sm.hb[((lane >> 3) & 1) * 512 + (lane & 1) * 32 + g * 8]);
  const bool par = lane & 8;
  float l0 = 0.f, l1 = 0.f;
  u32x2 gmd[2], tbg;
  int t1 = 0;
#pragma unroll
  for (int i = NCMIN / 8; i < NL; ++i) kr[i] = u32x4{0u, 0u, 0u, 0u};
  sfor<0, NL>([&](auto ic) {
    constexpr int i = ic.v;
    if (i >= NCMIN / 8 && i >= nl) return false;
    if constexpr (i == 0) return true;
    const u32 so = __builtin_amdgcn_readlane(sot, i);
    gate();
    SB();
    kr[i] = ld16<AUX_NT>(rk, lane * 16, so);
    if constexpr (i == SMO) small_ops();
    if constexpr (i == TBL) tbg = ld8<AUX_SC1>(wsr, wave == 7 ? G_TB + xcd * 128 + (lane & 7) * 8 : OOB, 0);
    if constexpr (i == QP) { gmd[0] = ld8<AUX_SC1>(wsr, part == 1 ? G_MOD + tid * 8 : OOB, 0); gmd[1] = ld8<AUX_SC1>(wsr, part == 1 ? G_MOD + tid * 8 : OOB, 4096); }
    SB();
    SB();
    if (i >= WK) { WAIT_VM(WK); SB(); }
    if (i == 1) STAMP(25);
    if (i == 8) STAMP(12);
    if (i == 20) STAMP(21);
    if (i == 25) STAMP(22);
    if (i == TBP) {
      if (part == 3 && i8 == 0 && wave == 7) { tag = next_tag(__builtin_amdgcn_readfirstlane(ep[0])); if (lane < 8) st8_sc1(wsr, G_TB + lane * 128 + xcd * 8, t0 + (((tka - (u32)t64) * P.GA) >> 3) - P.GS, tag); }
      SB();
    }
    if (i == TBU) {
      if (wave == 7) {
        const bool ok = tbg[1] == tag;
        float lf = ok ? (float)min(max((int)(t0 - tbg[0]), -300), 300) : 0.f, nf = ok ? 1.f : 0.f;
        lf += dpp<XOR1>(lf); nf += dpp<XOR1>(nf); lf += dpp<XOR2>(lf); nf += dpp<XOR2>(nf); lf += dpp<XOR7>(lf); nf += dpp<XOR7>(nf);
        if (lane == 0) sm.bc = u32x2{tag, t0 - (u32)(int)(lf * __builtin_amdgcn_rcpf(fmaxf(nf, 1.f)))};
      }
      LDS_BARRIER();
      const u32x2 bc = sm.bc;
      tag = __builtin_amdgcn_readfirstlane(bc[0]);
      t1 = (__builtin_amdgcn_readfirstlane((int)(bc[1] - t0)) + TEND + P.X1) * 256;
      tau -= __builtin_amdgcn_readfirstlane((int)((float)(int)(t0 - bc[1]) * 256.f * __builtin_amdgcn_rcpf((float)(2 * nl - TBU - 3))));
      SB();
    }
    if (i == LNS) {
      tag = next_tag(__builtin_amdgcn_readfirstlane(ep[0]));
      if (part == 1) {
        float x0[8], x1[8], mb0, ri0, mb1, ri1;
#pragma unroll
        for (int k = 0; k < 4; ++k) { x0[2 * k] = lo_f(xr0[k]); x0[2 * k + 1] = hi_f(xr0[k]); x1[2 * k] = lo_f(xr1[k]); x1[2 * k + 1] = hi_f(xr1[k]); }
        ln_stats2(x0, x1, mb0, ri0, mb1, ri1);
        l0 = rbf(rbf(lo_f(xs0) - mb0) * ri0); l1 = rbf(rbf(lo_f(xs1) - mb1) * ri1);
      }
      PIN(l0); PIN(l1);
      SB();
    }
    if (i == WUSE) {
      PIN(tch1); PIN(tch2);
      if ((part == 1 || (part == 2 && has_w && !modc))) {
        WAIT_VM(WK); CFENCE();
        if (part == 2) mod_stage(); else wo_prep();
      }
#pragma unroll
      for (int k = 0; k < 8; ++k) PIN(bf[k]);
      SB();
    }
    if (i == QP + QD) {
      if (part == 1) {
        u32 gm[2] = {gmd[0][0], gmd[1][0]};
        if (!all_ok((gmd[0][1] == tag) & (gmd[1][1] == tag))) { STAMP(11); gather8<2>(wsr, G_MOD + tid * 8, 512 * 8, tag, gm); }
        STAMP(5);
        gemv_go(pack_bf(rbf(l0 * rbf(1.f + lo_f(gm[1]))) + lo_f(gm[0]), rbf(l1 * rbf(1.f + hi_f(gm[1]))) + hi_f(gm[0])));
      }
      SB();
    }
    if (i == QP + QD + 1) {
      if (part == 1) {
        LDS_BARRIER();
        if (wave == 0) {
          const int bb = lane >> 5, jq = lane & 31;
          st8_sc1(wsr, G_Q + (bb * 2048 + blk * 32 + jq) * 8, pack_bf(sum8(&sm.r2[2 * jq * 20 + bb * 8]), sum8(&sm.r2[(2 * jq + 1) * 20 + bb * 8])), tag);
        }
        STAMP(6);
      }
      SB();
    }
    return true;
  });
  PIN(sot);
  const u32 qoff = G_Q + ((b * 32 + head) * 64 + c * 4) * 8;
  u32x4 qg[2], fl2[3];
  u32 qq[4];
  constexpr int NB = (NL + 15) / 16;
  float xlo, sc[NB], pr[NB], m = 0.f;
  f32x4 acc = {0.f, 0.f, 0.f, 0.f};
  u32 pb[NB], pm[4], cpf;
  const u32 adst0 = lds_addr(lane < 32 ? &sm.red[wave][c * 8 + g * 4] : &sm.r2[(lane - 32) * 4]);
  u32 po, rdst, rsrc, xo;
  auto wo_addr = [&]() {
    po = G_P + (((c >> 2 & 1) * 32 + xcd * 4 + (wave >> 1)) * 4 + (c & 3)) * 256 + (wave & 1) * 128 + (c >> 3) * 64 + g * 16;
    rdst = lane < 32 ? (u32)(uintptr_t)&sm.red2[wave][0] + lane * 16 : (u32)(uintptr_t)&sm.r2[0] + (lane - 32) * 16;
    rsrc = (u32)(uintptr_t)&sm.red2[0][((wave & 1) * 16 + (lane & 15)) * 4 + (lane >> 4)];
    xo = (lane & 1) ? OOB : G_XP + xcd * 2048 + (wave & 1) * 1024 + (blk * 64 + lane) * 2;
    PIN(po); PIN(rdst); PIN(rsrc); PIN(xo);
  };
  u32 pso = wave == 0 ? pslot + lane * 4 : OOB;
  PIN(pso);
  const u32 nadd = (te0 >> 20) ? P.LW : (is_up ? P.LU : 0);
  u32 nto = tid == 0 ? G_NT + bid * 8 : OOB;
  PIN(nto);
  auto notices = [&](auto nc_, u32 off, u32x4 (&gn)[nc_.v]) -> int {
    constexpr int N = nc_.v;
#pragma nounroll
    for (int spin = 0; spin < SPIN_MAX; ++spin) {
      bool ok = true;
#pragma unroll
      for (int i = 0; i < N; ++i) ok &= (gn[i][1] == tag) & (gn[i][3] == tag);
      if (all_ok(ok)) break;
      __builtin_amdgcn_s_sleep(10);
      CFENCE();
#pragma unroll
      for (int i = 0; i < N; ++i) gn[i] = ld16<AUX_SC1>(wsr, off, i * 1024);
    }
    int mx = 0;
#pragma unroll
    for (int i = 0; i < N; ++i) mx = max(mx, max((int)(gn[i][0] - t0), (int)(gn[i][2] - t0)));
    return __builtin_amdgcn_readfirstlane((int)wave_max((float)min(mx, 1 << 20)));
  };
  const int side = nl >= SIDE + 13 ? SIDE + 8 : (nl >= SIDE + 9 ? SIDE + 4 : SIDE);
  sfor<0, NL + WV>([&](auto ic) {
    constexpr int v = ic.v;
    if (cons_end(v - 1) >= NCMIN / 8 && cons_end(v - 1) >= nl) return false;
    if constexpr (v > 0 && v < NL) {
      const u32 so = __builtin_amdgcn_readlane(sot, v);
      if constexpr (v >= 12) { if (v == nl - P.NE) tau = (tau * P.FE) >> 4; }
      if constexpr (v >= NCMIN / 8) { if (v < nl) gate(); } else gate();
      SB();
      vr[v] = ld16<AUX_NT>(rv, lane * 16, so);
      SB();
      SB();
    }
    if constexpr (v == NCMIN / 8 - 1) {
#pragma unroll
      for (int i = NCMIN / 8; i < NL; ++i) asm volatile("" : "=v"(vr[i]));
      asm volatile("" : "=v"(fl2[1]), "=v"(fl2[2]));
    }
    if constexpr (v == VQ - 4) { qg[0] = ld16<AUX_SC1>(wsr, qoff, 0); qg[1] = ld16<AUX_SC1>(wsr, qoff + 16, 0); }
    if constexpr (v == VSIDE) side_loads();
    if constexpr (v == CPF) {
      u64 pc; u32 dlt, len;
      asm volatile("s_getpc_b64 %0\n.Lcpf_here:\n\ts_mov_b32 %1, .Ltail_start-.Lcpf_here\n\ts_mov_b32 %2, .Lend_a-.Ltail_start\n\ts_max_i32 %2, %2, .Lend_b-.Ltail_start\n\ts_max_i32 %2, %2, .Lend_c-.Ltail_start\n\ts_max_i32 %2, %2, .Lend_d-.Ltail_start\n\ts_max_i32 %2, %2, .Lend_e-.Ltail_start" : "=s"(pc), "=s"(dlt), "=s"(len) :: "scc");
      pc += dlt;
      const rsrc_t rc = mk_rsrc((const void*)pc, (part == 3 && i8 == 1) ? len : 0u);
      cpf = __builtin_amdgcn_raw_buffer_load_b32(rc, tid * 64, 0, 0);
    }
    if constexpr (v == CPF + WV + 1) PIN(cpf);
    if constexpr (v == SIDE || v == SIDE + 4 || v == SIDE + 8) fl2[(v - SIDE) / 4] = ld16<AUX_SC1>(wsr, (wave == 0 && v == side) ? mlo : OOB, 0);
    SB();
    if constexpr (v < WV) { WAIT_VM(WK); SB(); } else if constexpr (v < NL && cons_end(v) < v - WV + 1) { WAIT_VM(WV); SB(); }
    if constexpr (v == VQ) {
      if (!all_ok((qg[0][1] == tag) & (qg[0][3] == tag) & (qg[1][1] == tag) & (qg[1][3] == tag))) { STAMP(20); gather16<2>(wsr, qoff, 16, tag, qg); }
      STAMP(1); WSTAMP(1);
      qq[0] = qg[0][0]; qq[1] = qg[0][2]; qq[2] = qg[1][0]; qq[3] = qg[1][2];
    }
    if constexpr (v >= VQ && v < VS) {
      constexpr int sl = v - VQ, bt = sl >> 1;
      float xs[8];
#pragma unroll
      for (int i = 0; i < 8; ++i) {
        float d = 0.f;
        if (sl * 8 + i < NL) {
#pragma unroll
          for (int k = 0; k < 4; ++k) d = dot2(kr[sl * 8 + i][k], qq[k], d);
        }
        xs[i] = d;
      }
#pragma unroll
      for (int h = 0; h < 4; ++h) xs[h] = sel_add<XOR7>(xs[h], xs[h + 4], c & 4);
#pragma unroll
      for (int h = 0; h < 2; ++h) xs[h] = sel_add<XOR2>(xs[h], xs[h + 2], c & 2);
      xs[0] = sel_add<XOR1>(xs[0], xs[1], c & 1);
      if constexpr ((sl & 1) || sl == NSL - 1) {
        const float t = (sl & 1) ? sel_add<XOR8>(xlo, xs[0], c & 8) : xs[0] + dpp<XOR8>(xs[0]);
        const float s = rbf(rbf(t) * 0.08838834764831845f);
        sc[bt] = (bt * 16 + c < nl) ? s : -__builtin_inff();
        PIN(sc[bt]);
      } else {
        xlo = xs[0];
        PIN(xlo);
      }
    }
    if constexpr (v == VS) {
      float mloc = sc[0];
#pragma unroll
      for (int bt = 1; bt < NB; ++bt) mloc = fmaxf(mloc, sc[bt]);
      m = fmaxf(wave_max(mloc), -3.0e38f);
      float ls = 0.f;
#pragma unroll
      for (int bt = 0; bt < NB; ++bt) { pr[bt] = __expf(sc[bt] - m); ls += pr[bt]; }
      const float l = wave_sum(ls);
      if (lane == 0) { sm.ml[wave][0] = m; sm.ml[wave][1] = l; }
      STAMP(2); WSTAMP(2);
    }
    sfor<cons_end(v - 1), cons_end(v)>([&](auto jc) {
      constexpr int i = jc.v;
      if (i < NCMIN / 8 || i < nl) {
        const u32 pv = __float_as_uint(bcast16(u2f(pb[i >> 4]), i & 15));
        acc = mfma16(u32x4{pv & pm[0], pv & pm[1], pv & pm[2], pv & pm[3]}, vr[i], acc);
      }
      PIN(acc);
      return true;
    });
    if constexpr (v == MLS - 1) { side_pin(); if (is_up) up_prep(); PIN(ga); PIN(xin); PIN(cc1); }
    if constexpr (v == UPREP - 1) wo_addr();
    if constexpr (v == UPREP) {
#pragma unroll
      for (int k = 0; k < 8; ++k) asm volatile("" : "=v"(bf[k]));
      if (is_up) up_frags();
#pragma unroll
      for (int k = 0; k < 8; ++k) PIN(bf[k]);
    }
    if constexpr (v == UPREP + 1) {
      if (is_up) {
        const u32x4* a1 = ap + 128 + ((lane >> 1) & 1) * 128;
        f32x4 d0 = {0.f, 0.f, 0.f, 0.f}, d1 = {0.f, 0.f, 0.f, 0.f};
#pragma unroll
        for (int rb = 0; rb < 4; ++rb) { d0 = mfma16(a1[rb * 8], bf[rb], d0); d1 = mfma16(a1[(rb + 4) * 8], bf[rb + 4], d1); }
        Af = par ? d0[1] + d1[1] : d0[0] + d1[0]; Bf = par ? d0[3] + d1[3] : d0[2] + d1[2];
        Af += dpp<XOR8>(Af); Bf += dpp<XOR8>(Bf);
      }
      PIN(Af); PIN(Bf);
    }
    if constexpr (v == SIDE + WV + 1 || v == SIDE + WV + 5 || v == SIDE + WV + 9) {
      if (wave == 0 && v == side + WV + 1) {
        const u32x4 ml = fl2[(v - SIDE - WV - 1) / 4];
        fok = all_ok((ml[1] == tag) & (ml[3] == tag));
        merge_factor(ml);
      }
    }
    if (v % 6 == 4 && v >= 10 && v < 30) STAMP(15 + v / 6);
    if constexpr (v == MLS) {
      LDS_BARRIER();
      float mm[8], ll[8];
#pragma unroll
      for (int w = 0; w < 8; ++w) { const f32x2 t = *reinterpret_cast<const f32x2*>(&sm.ml[w][0]); mm[w] = t[0]; ll[w] = t[1]; }
      float Mwg = mm[0], Lwg = 0.f;
#pragma unroll
      for (int w = 1; w < 8; ++w) Mwg = fmaxf(Mwg, mm[w]);
      if (wave == 0) {
#pragma unroll
        for (int w = 0; w < 8; ++w) Lwg = fmaf(__expf(mm[w] - Mwg), ll[w], Lwg);
        if (lane == 0) st16_sc1(wsr, G_ML + ((head * 2 + b) * 4 + part) * 16, __float_as_uint(Mwg), __float_as_uint(Lwg), tag);
      }
      const float fw = __expf(m - Mwg);
#pragma unroll
      for (int bt = 0; bt < NB; ++bt) { pb[bt] = pack_bf(pr[bt] * fw, pr[bt] * fw); PIN(pb[bt]); }
#pragma unroll
      for (int k = 0; k < 4; ++k) { pm[k] = (c >> 1) == k ? ((c & 1) ? 0xffff0000u : 0x0000ffffu) : 0u; PIN(pm[k]); }
    }
    SB();
    return true;
  });
  asm volatile(".Ltail_start:");
  *LDSP(f32x4, adst0) = acc;
  STAMP(14); WSTAMP(14);
  if (!fok) {
    u32x4 ml;
#pragma nounroll
    for (int spin = 0; spin < SPIN_MAX; ++spin) {
      CFENCE();
      ml = ld16<AUX_SC1>(wsr, mlo, 0);
      if (all_ok((ml[1] == tag) & (ml[3] == tag))) break;
    }
    merge_factor(ml);
  }
  STAMP(15);
  LDS_BARRIER();
  STAMP(3);
  {
    u32 pw = 0;
    if (wave == 0) {
      f32x2 t[8];
#pragma unroll
      for (int w = 0; w < 8; ++w) t[w] = *reinterpret_cast<const f32x2*>(&sm.red[w][2 * lane]);
      const f32x2 o = ((t[0] + t[1]) + (t[2] + t[3])) + ((t[4] + t[5]) + (t[6] + t[7]));
      pw = min(pack_bf(o[0] * fmrg, o[1] * fmrg), SENT - 1u);
    }
    __builtin_amdgcn_raw_buffer_store_b32(pw, wsr, pso, 0, AUX_SC1);
    st8_sc1(wsr, nto, (u32)wall_clock64() + nadd, tag);
    SB();
#pragma unroll
    for (int i = NCMIN / 8; i < NL; ++i) PIN(vr[i]);
    PIN(fl2[0]); PIN(fl2[1]); PIN(fl2[2]);
  }
  STAMP(4);

  if (part == 2) FINISH();
  if (part == 0) {
    __builtin_amdgcn_s_barrier();
#pragma nounroll
    while (wleft > 0) piece();
    until(t1);
    STAMP(11);
    CFENCE();
    u32x4 og[2];
    og[0] = ld16<AUX_SC1>(wsr, po, 0);
    SB();
    WAIT_VM(1); CFENCE();
    wo_prep(0, 2);
#pragma unroll
    for (int k = 0; k < 8; ++k) PIN(bf[k]);
    SB();
    f32x4 d[4];
    auto wo_mm = [&]() {
      const f32x4 z = {0.f, 0.f, 0.f, 0.f};
#pragma unroll
      for (int t = 0; t < 4; ++t) { d[t] = mfma16(og[0], bf[t], z); og[1][t] = __float_as_uint(dpp<XOR8>(u2f(og[0][t]))); }
      SB();
#pragma unroll
      for (int t = 0; t < 4; ++t) d[t] = mfma16(og[1], bf[4 + t], d[t]);
    };
    auto p_bad = [&]() { return !all_ok(max(max(og[0][0], og[0][1]), max(og[0][2], og[0][3])) != SENT); };
    wo_mm();
    if (__builtin_expect(p_bad(), 0)) {
#pragma nounroll
      for (int spin = 0; spin < SPIN_MAX; ++spin) {
        __builtin_amdgcn_s_sleep(1);
        CFENCE();
        og[0] = ld16<AUX_SC1>(wsr, po, 0);
        if (!p_bad()) break;
      }
      wo_mm();
      SB();
      asm volatile(".Lend_d:");
    }
    STAMP(5); WSTAMP(5);
    {
      SB();
      f32x4 y; f32x2 h[4];
#pragma unroll
      for (int t = 0; t < 4; ++t) { h[t] = d[t].xy + d[t].zw; PIN(h[t]); }
#pragma unroll
      for (int t = 0; t < 4; ++t) { y[t] = h[t][0] + h[t][1]; PIN(y[t]); }
      *LDSP(f32x4, rdst) = y;
    }
    STAMP(16);
    LDS_BARRIER();
    STAMP(17);
    if (wave < 2) {
      f32x2 v[4];
#pragma unroll
      for (int w = 0; w < 4; ++w) v[w] = f32x2{LDSP(float, rsrc)[w * 256], LDSP(float, rsrc)[w * 256 + 128]};
      const f32x2 h = (v[0] + v[1]) + (v[2] + v[3]);
      const float y = h[0] + h[1];
      __builtin_amdgcn_raw_buffer_store_b32(min(pack_bf(y, dpp<XOR1>(y)), SENT - 1u), wsr, xo, 0, AUX_SC1);
      asm volatile(".Lend_a:");
    }
    STAMP(6);
    FINISH();
  }
  if (part == 3) {
    if (!has_w) FINISH();
    u32x4 b2[4];
    {
      __builtin_amdgcn_s_barrier();
      const rsrc_t r2 = mk_rsrc(w2 + (size_t)(blk * 32 + wave * 4) * 512, 4096u);
      const u32 l2 = (u32)(uintptr_t)(&sm.w2b[wave * 4][0]);
#pragma unroll
      for (int n = 0; n < 4; ++n) dma16(r2, (lane ^ (n * 2) ^ ((wave & 2) << 2)) * 16, n * 1024, l2 + n * 1024);
    }
    const u32 tdst = lds_addr(&sm.hb[hpos]);
    const u32 sdst = lds_addr(&sm.stat[wave][lane]);
    const u32 ssrc = lds_addr(&sm.stat[(g >> 1) * 4 + ((lane >> 1) & 3)][(lane & 1) * 16]);
    const u32 adst = lds_addr((c < 4 && (g & 1) == 0) ? &sm.actl[g >> 1][wave * 4 + c] : reinterpret_cast<u16*>(&sm.dummy[lane]));
    const u32 asrc = lds_addr(&sm.actl[lane & 1][g * 8]);
    const u32 apa = lds_addr(&sm.hb[(c >> 3) * 512 + (c & 3) * 64 + ((c >> 2) & 1) * 32 + g * 8]);
    u32 xoff = G_XP + g * 2048 + ub * 1024 + (wave & 3) * 256 + c * 16;
    u32x4 one;
#pragma unroll
    for (int k = 0; k < 4; ++k) one[k] = ((c & 2) == 0 && (c >> 2) == k) ? ((c & 1) ? 0x3f800000u : 0x00003f80u) : 0u;
    PIN(one);
    u32 ooff = G_OP + (blk * 512 + tid) * 8;
    float Afp = Af, Bfp = Bf;
    PIN(xoff); PIN(ooff); PIN(Afp); PIN(Bfp);
    PIN(ga); PIN(xin); PIN(cc1);
    u32 w2a = (u32)(uintptr_t)(&sm.w2b[0][0]) + (g * 8 + ((lane >> 2) & 3)) * 1024 + (wave ^ (g & 1)) * 128 + (lane & 15) * 8;
    PIN(w2a);
    if (wave == 1) {
      due = t1 + P.N1 * 256; gate();
      u32x4 gn[2] = {ld16<AUX_SC1>(wsr, G_NT + lane * 16, 0), ld16<AUX_SC1>(wsr, G_NT + lane * 16, 1024)};
      const int tn = notices(IC<2>{}, G_NT + lane * 16, gn) * 256;
      due = (tn > t1 + 2 * 256 ? max(tn, t1 + P.L0 * 256) + P.L1 * 256 : t1) + P.X2 * 256;
      until(due);
    }
    asm volatile("s_waitcnt vmcnt(0)" ::: "memory");
    STAMP(9);
    __builtin_amdgcn_s_barrier();
    STAMP(11);
    CFENCE();
    u32x4 xg[2] = {ld16<AUX_SC1>(wsr, xoff, 0), ld16<AUX_SC1>(wsr, xoff, 8192)};
    SB();
#pragma unroll
    for (int t = 0; t < 4; ++t) {
      const u32x2 lo = tr_read(w2a ^ (t * 32)), hi = tr_read((w2a ^ (t * 32)) + 4096);
      b2[t] = u32x4{lo[0], lo[1], hi[0], hi[1]};
    }
#pragma unroll
    for (int t = 0; t < 4; ++t) PIN(b2[t]);
    SB();
    const f32x4 z4 = {0.f, 0.f, 0.f, 0.f};
    auto xp_bad = [&]() { return !all_ok(max(max(max(xg[0][0], xg[0][1]), max(xg[0][2], xg[0][3])), max(max(xg[1][0], xg[1][1]), max(xg[1][2], xg[1][3]))) != SENT); };
    f32x4 v = mfma16(one, xg[1], mfma16(one, xg[0], z4));
    if (__builtin_expect(xp_bad(), 0)) {
#pragma nounroll
      for (int spin = 0; spin < SPIN_MAX; ++spin) {
        __builtin_amdgcn_s_sleep(1);
        CFENCE();
        xg[0] = ld16<AUX_SC1>(wsr, xoff, 0); xg[1] = ld16<AUX_SC1>(wsr, xoff, 8192);
        if (!xp_bad()) break;
      }
      v = mfma16(one, xg[1], mfma16(one, xg[0], z4));
      SB();
      asm volatile(".Lend_e:");
    }
    STAMP(5); WSTAMP(5);
    {
      const float a0 = v[0], a1 = v[1];
      const float x10 = fmaf(ga[0], a0, xin[0]), x11 = fmaf(ga[1], a1, xin[1]);
      *LDSP(u32, tdst) = pack_bf(x10 * cc1[0], x11 * cc1[1]);
      float u = swap32_add(x10 + x11, fmaf(x10, x10, x11 * x11));
      u += dpp<XOR1>(u); u += dpp<XOR2>(u); u += dpp<XOR7>(u); u += dpp<XOR8>(u);
      *LDSP(float, sdst) = u;
    }
    STAMP(16);
    LDS_BARRIER();
    STAMP(17);
    {
      const f32x4 z = {0.f, 0.f, 0.f, 0.f};
      const u32x4 af0 = LDSP(u32x4, apa)[0], af1 = LDSP(u32x4, apa)[32];
      float s = LDSP(float, ssrc)[0], q = LDSP(float, ssrc)[32];
      SB();
      f32x4 d[4];
#pragma unroll
      for (int j = 0; j < 4; ++j) d[j] = mfma16(af0, bf[j], z);
#pragma unroll
      for (int j = 0; j < 4; ++j) d[j] = mfma16(af1, bf[4 + j], d[j]);
      s += dpp<XOR1>(s); q += dpp<XOR1>(q); s += dpp<XOR2>(s); q += dpp<XOR2>(q); s += dpp<XOR7>(s); q += dpp<XOR7>(q);
      const float mean = s * (1.f / 512.f);
      const float var = fmaf(q, 1.f / 512.f, -mean * mean);
      const float ri = __builtin_amdgcn_rsqf(fmaxf(var, 0.f) + 1e-5f);
      const float cf = fmaf(-ri * mean, Afp, Bfp);
      float y = (d[0][0] + d[1][1]) + (d[2][2] + d[3][3]);
      y = __builtin_bit_cast(float, __builtin_amdgcn_update_dpp(__builtin_bit_cast(int, y), __builtin_bit_cast(int, y), XOR8, 0xa, 0xf, false));
      y = swap16_add(y, y);
      y = fmaf(y, ri, cf);
      const float up = dpp<0x104>(y);
      *LDSP(u16, adst) = (u16)pack_bf(silu_f(y) * up, 0.f);
    }
    STAMP(18);
    LDS_BARRIER();
    STAMP(10);
    {
      const u32x4 af = *LDSP(u32x4, asrc);
      const f32x4 z = {0.f, 0.f, 0.f, 0.f};
      const f32x4 d0 = mfma16(af, b2[0], z), d1 = mfma16(af, b2[1], z), d2 = mfma16(af, b2[2], z), d3 = mfma16(af, b2[3], z);
      const bool g1 = g & 1, g2 = g & 2;
      const float e0 = g1 ? d1[0] : d0[0], e1 = g1 ? d1[1] : d0[1], f0 = g1 ? d3[0] : d2[0], f1 = g1 ? d3[1] : d2[1];
      st8_sc1(wsr, ooff, pack_bf(g2 ? f0 : e0, g2 ? f1 : e1), tag);
    }
    asm volatile(".Lend_b:");
    STAMP(6);
    FINISH();
  }
  if (wave == 0) {
    const int d = fd, ul = lane >> 3;
    u32 gm[2] = {sg[0][0], sg[1][0]};
    if (!all_ok((sg[0][1] == tag) & (sg[1][1] == tag))) gather8<2>(wsr, G_MOD + (1024 + d) * 8, 1536 * 8, tag, gm);
    u32 xa0, xa1;
    {
      due = t1 + P.N2 * 256; gate();
      u32x4 gn[2] = {ld16<AUX_SC1>(wsr, G_NT + lane * 16, 0), ld16<AUX_SC1>(wsr, G_NT + lane * 16, 1024)};
      const int tn = notices(IC<2>{}, G_NT + lane * 16, gn) * 256;
      due = (tn > t1 + 2 * 256 ? max(tn, t1 + P.L0 * 256) + P.L1 * 256 : t1) + P.X3 * 256;
      until(due);
    }
#pragma nounroll
    for (int spin = 0; spin < SPIN_MAX; ++spin) {
      CFENCE();
      xa0 = __builtin_amdgcn_raw_buffer_load_b32(wsr, G_XP + ul * 2048 + (d >> 1) * 4, 0, AUX_SC1);
      xa1 = __builtin_amdgcn_raw_buffer_load_b32(wsr, G_XP + ul * 2048 + (d >> 1) * 4, 1024, AUX_SC1);
      if (all_ok(max(xa0, xa1) != SENT)) break;
      __builtin_amdgcn_s_sleep(1);
    }
    float a0 = (d & 1) ? hi_f(xa0) : lo_f(xa0), a1 = (d & 1) ? hi_f(xa1) : lo_f(xa1);
    a0 += dpp<XOR8>(a0); a1 += dpp<XOR8>(a1);
    a0 = swap16_add(a0, a0); a1 = swap16_add(a1, a1);
    a0 = swap32_add(a0, a0); a1 = swap32_add(a1, a1);
    const bool hb = g & 1;
    float xv = rbf(((d & 1) ? hi_f(sx2) : lo_f(sx2)) + rbf((hb ? hi_f(gm[0]) : lo_f(gm[0])) * rbf(hb ? a1 : a0)));
    float gv = hb ? hi_f(gm[1]) : lo_f(gm[1]);
    PIN(xv); PIN(gv);
    if (wi == 0 && lane == 0) __builtin_amdgcn_raw_buffer_store_b32(tag, wsr, WS_EPOCH, 0, AUX_SC1);
    STAMP(10);
    due = max(due + P.X4 * 256, (int)(((u32)wall_clock64() - t0) << 8) + P.GX * 256);
    const bool last = ul + 40 < NUP;
    u32 o0 = G_OP + (ul * 512 + d) * 8, o5 = last ? o0 : OOB, tg5 = last ? tag : 0u;
    const rsrc_t rout = mk_rsrc(out, 2048u);
    u32 oo = (lane & 40) == 0 ? (g * 512 + d) * 2 : OOB;
    const u32 pk = c == 0 ? 0x00003f80u : (c == 4 ? 0x3f800000u : 0u);
    u32x4 one = {pk, 0u, pk, 0u};
    PIN(o0); PIN(o5); PIN(tg5); PIN(oo); PIN(one);
    __builtin_amdgcn_raw_buffer_store_b16((u16)pack_bf(xv, 0.f), rout, oo, 0, AUX_SC1);
    until(due);
    f32x4 sv;
#pragma nounroll
    for (int spin = 0; spin < SPIN_MAX; ++spin) {
      CFENCE();
      u32x2 og[6];
#pragma unroll
      for (int k = 0; k < 5; ++k) og[k] = ld8<AUX_SC1>(wsr, o0, k * 32768);
      og[5] = ld8<AUX_SC1>(wsr, o5, 5 * 32768);
      sv = f32x4{0.f, 0.f, 0.f, 0.f};
#pragma unroll
      for (int k = 0; k < 3; ++k) sv = mfma16(one, u32x4{og[2 * k][0], og[2 * k][1], og[2 * k + 1][0], og[2 * k + 1][1]}, sv);
      const u32 bad = ((og[0][1] ^ tag) | (og[1][1] ^ tag) | (og[2][1] ^ tag)) | ((og[3][1] ^ tag) | (og[4][1] ^ tag) | (og[5][1] ^ tg5));
      PIN(sv);
      if (all_ok(bad == 0u)) break;
      __builtin_amdgcn_s_sleep(1);
    }
    STAMP(7);
    const float s = sv[0] + dpp<XOR8>(sv[0]);
    __builtin_amdgcn_raw_buffer_store_b16((u16)pack_bf(fmaf(gv, s, xv), 0.f), rout, oo, 0, AUX_SC1);
    asm volatile(".Lend_c:");
  }
  FINISH();
}

extern "C" void kernel_launch(void* const* d_in, const int* in_sizes, int n_in,
                              void* d_out, int out_size, void* d_ws, size_t ws_size,
                              hipStream_t stream) {
  fused_kernel<<<256, 512, 0, stream>>>((const u16*)d_in[1], (const u16*)d_in[2], (const u16*)d_in[4], (const u16*)d_in[3], (const u16*)d_in[10], (const u16*)d_in[6], (const u16*)d_in[7],
                                        (unsigned char*)d_ws, (const u16*)d_in[0], (const u16*)d_in[9], (const u16*)d_in[8], (u16*)d_out);
}
```

```cpp
#define PROBE_MUL 10
#include <hip/hip_runtime.h>
#include <stdint.h>

typedef unsigned int u32;
typedef unsigned short u16;
typedef unsigned long long u64;
typedef __attribute__((ext_vector_type(4))) unsigned u32x4;
typedef __attribute__((ext_vector_type(2))) unsigned u32x2;
typedef __attribute__((ext_vector_type(2))) __bf16 bf16x2;
typedef __attribute__((ext_vector_type(4))) float f32x4;
typedef __attribute__((ext_vector_type(2))) float f32x2;
typedef __amdgpu_buffer_rsrc_t rsrc_t;

constexpr int NH = 32, SK = 16384, DH = 128, WIN = 4096, FF = 1376;
struct Prm { int E0, ES, E1, E2, E2C, EU, TEND, DT_UP, L0, L1, X1, X2, X3, X4, NE, FE, M0, M1, M2, M3, M4, GS, GX, N1, N2, GA, WD, DT_WO, LW, LU, EW, PM, NF; };
constexpr Prm PRM0 = {32, 24, 32, 32, 56, 64, 2020, 60, 30, 25, 120, 86, 131, 88, 4, 12, 5, 8, 21, 4, 32, 0, 56, 28, 70, 0, 4, 100, 45, 15, 8, 0x24310, 1};
constexpr int NL = 37;
constexpr int SK0 = 21, SK1 = 32;
constexpr int NCMIN = (NL - SK1 + SK0) * 8;
constexpr int WK = 8, WV = 5;
constexpr int WUSE = 12, LNS = 7;
constexpr int QP = 14, QD = 4;
constexpr int VQ = 6;
constexpr int NSL = (NL + 7) / 8;
constexpr int VS = VQ + NSL;
constexpr int MLS = VS + 2;
constexpr int VSIDE = MLS - 5, UPREP = MLS + 6;
constexpr int CPF = 17;
constexpr int SMO = 1;
constexpr int TBP = 4, TBL = 11, TBU = 15;
constexpr int cons_end(int v) {
  const int a = v - WV + 1, c = (v - MLS) * 4;
  const int m = a < c ? a : c;
  return m < 0 ? 0 : (m > NL ? NL : m);
}
struct Tab { u32 e[512]; };
constexpr Tab make_tab(const Prm& P) {
  Tab T{};
  for (int bid = 0; bid < 256; ++bid) {
    const int xcd = bid & 7, part = bid >> 4 & 3, i8 = (bid >> 3 & 1) | bid >> 6 << 1, wi = i8 * 8 + xcd;
    const bool has_mod = wi < 48, has_up = wi >= 21;
    const int cb0 = (i8 + 5) & 7, cb1 = (i8 + 4) & 7, ms = wi - 20;
    const int mi = wi < 8 ? wi : (wi < 16 ? 8 + ((xcd + 4) & 7) : (wi < 20 ? (wi - 14) * 8 + 3 : 16 + ms / 7 * 8 + ms % 7 + (ms % 7 >= 3 ? 1 : 0)));
    const int ui = xcd * 5 + (xcd > 5 ? xcd - 5 : 0) + (i8 >= 3 ? (xcd >= 5 ? 1 : 0) + i8 - 3 : 0);
    const int wd = cb0 == 3 ? 0 : P.WD;
    const int e[4] = {P.E0 - 4 * wd + (cb0 == 3 ? P.ES : 0), P.E1 + (cb1 == 3 ? P.ES : 0), has_mod ? (wi < 16 ? P.E2C : P.E2) + ((mi & 7) == 3 ? P.ES : 0) : 0, has_up ? P.EU : 0};
    const int ew = wd ? P.EW : 0, pa = (1024 + e[0] + ew + e[1] + e[2] + e[3]) / 4;
    int c[5] = {0, 0, 0, 0, 124};
    for (int k = 0; k < 3; ++k) c[k + 1] = c[k] + (pa - e[k] - (k == 0 ? ew : 0) + 4) / 8 - 1;
    const int nl = c[part + 1] - c[part] + 1, wst = e[part] / 4;
    const bool has_w = part < 2 || (part == 2 ? has_mod : has_up);
    const int blk = !has_w ? 0 : (part == 0 ? cb0 : (part == 1 ? xcd * 8 + cb1 : (part == 2 ? mi : ui)));
    const int tau = (part == 3 && has_up ? P.TEND - P.DT_UP : (part == 0 && wd ? P.TEND - P.DT_WO : P.TEND)) * 1024 / (4 * (2 * nl + wst - 2) + P.NF);
    T.e[2 * bid] = (u32)(c[part] | nl << 7 | blk << 13 | (has_w ? 1 : 0) << 19 | (part == 0 ? wd : 0) << 20);
    T.e[2 * bid + 1] = (u32)(tau | (tau * wst / (part == 0 && wd < 8 ? 8 - wd : 8)) << 16);
  }
  return T;
}
constexpr int step_range(const Prm& P, bool mx_) {
  int mx = 0, mn = 1 << 20;
  const Tab T = make_tab(P);
  for (int bid = 0; bid < 256; ++bid) { const int n = T.e[2 * bid] >> 7 & 63; mx = n > mx ? n : mx; mn = n < mn ? n : mn; }
  return mx_ ? mx : mn;
}
extern "C" __attribute__((used, visibility("protected"))) __constant__ const Tab wg_tab = make_tab(PRM0);
static_assert(step_range(PRM0, true) <= NL && step_range(PRM0, false) >= NCMIN / 8, "a wave's key range does not fit its load steps");
static_assert(UPREP + 1 < SK0 && QP + QD + 1 < SK0 && cons_end(SK0 - 1) == SK0 - WV && NL - SK1 == WV, "steps every wave runs");
constexpr int NUP = FF / 32;
constexpr int SPIN_MAX = 1 << 17;

constexpr u32 WS_EPOCH = 0;
constexpr u32 G_TB = 1024;
constexpr u32 G_NT = 2048;
constexpr u32 G_MOD = 4096;
constexpr u32 G_Q = G_MOD + 3072 * 8;
constexpr u32 G_ML = G_Q + 4096 * 8;
constexpr u32 G_P = G_ML + 256 * 16;
constexpr u32 G_XP = G_P + 256 * 256;
constexpr u32 G_OP = G_XP + 8 * 512 * 4;
constexpr u32 WS_END = G_OP + 43 * 512 * 8;
constexpr u32 SENT = 0xffffffffu;
constexpr u32 OOB = 0x40000000u;
#define WST 0

constexpr int AUX_NT = 2, AUX_SC1 = 16;

__device__ __forceinline__ float lo_f(u32 v) { return __uint_as_float(v << 16); }
__device__ __forceinline__ float hi_f(u32 v) { return __uint_as_float(v & 0xffff0000u); }
__device__ __forceinline__ float rbf(float x) { return (float)(__bf16)x; }
__device__ __forceinline__ u32 pack_bf(float lo, float hi) {
  bf16x2 r; r[0] = (__bf16)lo; r[1] = (__bf16)hi; return __builtin_bit_cast(u32, r);
}
__device__ __forceinline__ float dot2(u32 k, u32 q, float acc) {
  return __builtin_amdgcn_fdot2_f32_bf16(__builtin_bit_cast(bf16x2, k), __builtin_bit_cast(bf16x2, q), acc, false);
}
template <int CTRL> __device__ __forceinline__ float dpp(float x) {
  return __builtin_bit_cast(float, __builtin_amdgcn_mov_dpp(__builtin_bit_cast(int, x), CTRL, 0xf, 0xf, true));
}
constexpr int XOR1 = 0xB1, XOR2 = 0x4E, XOR7 = 0x141, XOR8 = 0x128;
__device__ __forceinline__ float swap32_add(float a, float b) {
  auto t = __builtin_amdgcn_permlane32_swap(__float_as_uint(a), __float_as_uint(b), false, false);
  return __uint_as_float(t[0]) + __uint_as_float(t[1]);
}
__device__ __forceinline__ float swap16_add(float a, float b) {
  auto t = __builtin_amdgcn_permlane16_swap(__float_as_uint(a), __float_as_uint(b), false, false);
  return __uint_as_float(t[0]) + __uint_as_float(t[1]);
}
__device__ __forceinline__ float wave_sum(float x) {
  x += dpp<XOR1>(x); x += dpp<XOR2>(x); x += dpp<XOR7>(x); x += dpp<XOR8>(x);
  x = swap16_add(x, x);
  return swap32_add(x, x);
}
__device__ __forceinline__ float wave_max(float x) {
  x = fmaxf(x, dpp<XOR1>(x)); x = fmaxf(x, dpp<XOR2>(x)); x = fmaxf(x, dpp<XOR7>(x)); x = fmaxf(x, dpp<XOR8>(x));
  auto s = __builtin_amdgcn_permlane16_swap(__float_as_uint(x), __float_as_uint(x), false, false);
  x = fmaxf(__uint_as_float(s[0]), __uint_as_float(s[1]));
  auto t = __builtin_amdgcn_permlane32_swap(__float_as_uint(x), __float_as_uint(x), false, false);
  return fmaxf(__uint_as_float(t[0]), __uint_as_float(t[1]));
}
template <int CTRL> __device__ __forceinline__ float sel_add(float lo, float hi, bool up) {
  const float t1 = lo + dpp<CTRL>(lo), t2 = hi + dpp<CTRL>(hi);
  return up ? t2 : t1;
}
template <int I> __device__ __forceinline__ float bc16(float x) {
  return __builtin_bit_cast(float, __builtin_amdgcn_update_dpp(0, __builtin_bit_cast(int, x), 0x150 + I, 0xf, 0xf, false));
}
__device__ __forceinline__ float bcast16(float x, int i) {
  switch (i) {
    case 0: return bc16<0>(x); case 1: return bc16<1>(x); case 2: return bc16<2>(x); case 3: return bc16<3>(x);
    case 4: return bc16<4>(x); case 5: return bc16<5>(x); case 6: return bc16<6>(x); case 7: return bc16<7>(x);
    case 8: return bc16<8>(x); case 9: return bc16<9>(x); case 10: return bc16<10>(x); case 11: return bc16<11>(x);
    case 12: return bc16<12>(x); case 13: return bc16<13>(x); case 14: return bc16<14>(x); default: return bc16<15>(x);
  }
}
__device__ __forceinline__ float silu_f(float x) { return x * __builtin_amdgcn_rcpf(1.f + __expf(-x)); }

template <int AUX> __device__ __forceinline__ u32x4 ld16(rsrc_t r, u32 voff, u32 soff) {
  return __builtin_amdgcn_raw_buffer_load_b128(r, voff, soff, AUX);
}
template <int AUX> __device__ __forceinline__ u32x2 ld8(rsrc_t r, u32 voff, u32 soff) {
  return __builtin_amdgcn_raw_buffer_load_b64(r, voff, soff, AUX);
}
__device__ __forceinline__ void st8_sc1(rsrc_t r, u32 voff, u32 v, u32 tag) {
  __builtin_amdgcn_raw_buffer_store_b64(u32x2{v, tag}, r, voff, 0, AUX_SC1);
}
__device__ __forceinline__ void st16_sc1(rsrc_t r, u32 voff, u32 v0, u32 v1, u32 tag) {
  __builtin_amdgcn_raw_buffer_store_b128(u32x4{v0, tag, v1, tag}, r, voff, 0, AUX_SC1);
}
__device__ __forceinline__ rsrc_t mk_rsrc(const void* p, u32 bytes) {
  return __builtin_amdgcn_make_buffer_rsrc(const_cast<void*>(p), 0, bytes, 0x00020000);
}
#define WAIT_VM(n) __builtin_amdgcn_s_waitcnt(((n) & 15) | (((n) >> 4) << 14) | (7 << 4) | (15 << 8))
#define SB() __builtin_amdgcn_sched_barrier(0)
#define CFENCE() asm volatile("" ::: "memory")
__device__ __forceinline__ bool all_ok(bool ok) { return __builtin_amdgcn_ballot_w64(!ok) == 0ull; }

template <int N> __device__ __forceinline__ void gather16(rsrc_t ws, u32 voff, u32 stride, u32 tag, u32x4 (&g)[N]) {
#pragma nounroll
  for (int spin = 0; spin < SPIN_MAX; ++spin) {
    CFENCE();
    bool ok = true;
#pragma unroll
    for (int i = 0; i < N; ++i) g[i] = ld16<AUX_SC1>(ws, voff + i * stride, 0);
#pragma unroll
    for (int i = 0; i < N; ++i) ok &= (g[i][1] == tag) & (g[i][3] == tag);
    if (all_ok(ok)) break;
    __builtin_amdgcn_s_sleep(8);
  }
}
template <int N> __device__ __forceinline__ void gather8(rsrc_t ws, u32 voff, u32 stride, u32 tag, u32 (&g)[N], bool act = true) {
#pragma nounroll
  for (int spin = 0; spin < SPIN_MAX; ++spin) {
    CFENCE();
    bool ok = true;
    u32x2 t[N];
#pragma unroll
    for (int i = 0; i < N; ++i) t[i] = ld8<AUX_SC1>(ws, voff + i * stride, 0);
#pragma unroll
    for (int i = 0; i < N; ++i) { ok &= (t[i][1] == tag); g[i] = t[i][0]; }
    if (all_ok(ok | !act)) break;
    __builtin_amdgcn_s_sleep(8);
  }
}

struct Smem {
  float red[8][128];
  float ml[8][2];
  float r2[64 * 20];
  u16 hb[3 * 1024];
  float stat[8][64];
  float red2[8][128];
  u32 dummy[64];
  u32x2 bc;
  u32x4 zero[4];
  float pl[4][8];
  u32 mflag[8];
  u32x4 wbuf[8][8][64];
  __attribute__((aligned(256))) u16 w2b[32][512];
  u16 actl[2][32];
};

#define PROBE_K 4
#define PROBE_OP 0
#define PROBE_PART -1
#define STAMP(k) do { if ((k) == PROBE_K && tid == 0) pst = wall_clock64(); } while (0)
#define WSTAMP(k) do {} while (0)
#define FINISH() do { STAMP(8); if (tid == 0) { __hip_atomic_store(reinterpret_cast<u64*>(ws + (1u << 20)) + bid * 2, pt0 + (u32)(t0 - (u32)pt0) + (t1 >> 8), __ATOMIC_RELAXED, __HIP_MEMORY_SCOPE_AGENT); \
   __hip_atomic_store(reinterpret_cast<u64*>(ws + (1u << 20)) + bid * 2 + 1, (PROBE_PART < 0 || part == PROBE_PART) ? pst : 0ull, __ATOMIC_RELAXED, __HIP_MEMORY_SCOPE_AGENT); } \
   if (bid == 8 && wave == 0) probe_end(ws, pt0, lane); return; } while (0)
#define PIN(x) asm volatile("" : "+v"(x))
#define LDS_BARRIER() do { __builtin_amdgcn_s_waitcnt(0xc07f); __builtin_amdgcn_s_barrier(); CFENCE(); } while (0)
#define WAVE_SYNC() do { __builtin_amdgcn_fence(__ATOMIC_RELEASE, "wavefront"); __builtin_amdgcn_wave_barrier(); __builtin_amdgcn_fence(__ATOMIC_ACQUIRE, "wavefront"); } while (0)

__device__ __forceinline__ void dma16(rsrc_t r, u32 voff, u32 soff, u32 lds_dst) {
  u32 keep;
  asm volatile("s_mov_b32 %0, m0\n\ts_mov_b32 m0, %4\n\ts_nop 4\n\tbuffer_load_dwordx4 %1, %2, %3 offen nt lds\n\ts_mov_b32 m0, %0"
               : "=&s"(keep) : "v"(voff), "s"(r), "s"(soff), "s"(lds_dst) : "memory");
}
__device__ __forceinline__ void ld16_into(u32x4& d, rsrc_t r, u32 voff, u32 soff) {
  asm volatile("s_nop 4\n\tbuffer_load_dwordx4 %0, %1, %2, %3 offen nt" : "+v"(d) : "v"(voff), "s"(r), "s"(soff) : "memory");
}
__device__ __forceinline__ u32 perm_lo(u32 a, u32 b) { return __builtin_amdgcn_perm(b, a, 0x05040100u); }
__device__ __forceinline__ u32 perm_hi(u32 a, u32 b) { return __builtin_amdgcn_perm(b, a, 0x07060302u); }
__device__ __forceinline__ float rdl(float x, int l) { return __int_as_float(__builtin_amdgcn_readlane(__float_as_int(x), l)); }

typedef __attribute__((ext_vector_type(8))) __bf16 bf16x8;
__device__ __forceinline__ f32x4 mfma16(u32x4 a, u32x4 b, f32x4 c) {
  return __builtin_amdgcn_mfma_f32_16x16x32_bf16(__builtin_bit_cast(bf16x8, a), __builtin_bit_cast(bf16x8, b), c, 0, 0, 0);
}
__device__ __forceinline__ float sum8(const float* p) {
  const f32x4 a = *reinterpret_cast<const f32x4*>(p), b = *reinterpret_cast<const f32x4*>(p + 4);
  return ((a[0] + b[0]) + (a[1] + b[1])) + ((a[2] + b[2]) + (a[3] + b[3]));
}
__device__ __forceinline__ float ln_ri(float var) {
  return __builtin_amdgcn_rcpf(rbf(__builtin_amdgcn_sqrtf(rbf(rbf(fmaxf(var, 0.f)) + 1e-5f))));
}
__device__ __forceinline__ void ln_stats2(const float (&x0)[8], const float (&x1)[8], float& mb0, float& ri0, float& mb1, float& ri1) {
  const float k0 = rdl(x0[0], 0), k1 = rdl(x1[0], 0);
  float s0 = 0.f, s1 = 0.f, q0 = 0.f, q1 = 0.f;
#pragma unroll
  for (int k = 0; k < 8; ++k) {
    const float d0 = x0[k] - k0, d1 = x1[k] - k1;
    s0 += d0; s1 += d1; q0 = fmaf(d0, d0, q0); q1 = fmaf(d1, d1, q1);
  }
  float t = swap16_add(swap32_add(s0, s1), swap32_add(q0, q1));
  t += dpp<XOR1>(t); t += dpp<XOR2>(t); t += dpp<XOR7>(t); t += dpp<XOR8>(t);
  auto u = __builtin_amdgcn_permlane16_swap(__float_as_uint(t), __float_as_uint(t), false, false);
  const float ms = __uint_as_float(u[0]) * (1.f / 512.f), mq = __uint_as_float(u[1]) * (1.f / 512.f);
  const float ri = ln_ri(fmaf(-ms, ms, mq));
  mb0 = rbf(k0 + rdl(ms, 0)); mb1 = rbf(k1 + rdl(ms, 32));
  ri0 = rdl(ri, 0); ri1 = rdl(ri, 32);
}
template <int I> struct IC { static constexpr int v = I; };
template <int I, int N, class F> __device__ __forceinline__ void sfor(F&& f) {
  if constexpr (I < N) { if (f(IC<I>{})) sfor<I + 1, N>(f); }
}
typedef __attribute__((ext_vector_type(4))) short s16x4;
typedef __attribute__((address_space(3))) s16x4* lds_s16x4p;
__device__ __forceinline__ u32x2 tr_read(u32 lds_addr) {
  return __builtin_bit_cast(u32x2, __builtin_amdgcn_ds_read_tr16_b64_v4i16((lds_s16x4p)lds_addr));
}
__device__ __forceinline__ u32 next_tag(u32 e) { return ((e | 0x40004000u) + 1u) & ~0x40004000u; }
__device__ __forceinline__ float u2f(u32 v) { return __uint_as_float(v); }
#define LDSP(T, a) ((__attribute__((address_space(3))) T*)(a))
template <class T> __device__ __forceinline__ u32 lds_addr(T* p) { u32 a = (u32)(uintptr_t)p; PIN(a); return a; }

#define TAB_LOAD "s_add_u32 s30, s28, wg_tab@rel32@lo+4\ns_addc_u32 s31, s29, wg_tab@rel32@hi+12\nglobal_load_dwordx2 %[te], %[va], s[30:31]\n"
__device__ __forceinline__ void probe_end(unsigned char* ws, u64 pt0, int lane) {
  while (wall_clock64() < pt0 + 5500ull) __builtin_amdgcn_s_sleep(8);
  u64 a[4], c2[4];
  for (int i = 0; i < 4; ++i) {
    a[i] = __hip_atomic_load(reinterpret_cast<u64*>(ws + (1u << 20)) + (i * 64 + lane) * 2, __ATOMIC_RELAXED, __HIP_MEMORY_SCOPE_AGENT);
    c2[i] = __hip_atomic_load(reinterpret_cast<u64*>(ws + (1u << 20)) + (i * 64 + lane) * 2 + 1, __ATOMIC_RELAXED, __HIP_MEMORY_SCOPE_AGENT);
  }
  int mn0 = 1 << 30, mx1 = -(1 << 30), mn1 = 1 << 30, sum = 0, cnt = 0;
  for (int i = 0; i < 4; ++i) {
    const int d0 = (int)(a[i] - pt0);
    mn0 = min(mn0, d0);
    if (c2[i] != 0ull) { const int d1 = (int)(c2[i] - pt0); mx1 = max(mx1, d1); mn1 = min(mn1, d1); sum += d1; cnt += 1; }
  }
  for (int o = 1; o < 64; o <<= 1) { mn0 = min(mn0, __shfl_xor(mn0, o)); mx1 = max(mx1, __shfl_xor(mx1, o)); mn1 = min(mn1, __shfl_xor(mn1, o)); sum += __shfl_xor(sum, o); cnt += __shfl_xor(cnt, o); }
  int X = PROBE_OP == 0 ? mx1 : (PROBE_OP == 1 ? sum / max(cnt, 1) : mn1);
  if (PROBE_K == 102) X = mn0;
  X = mn0 + (X - mn0) * PROBE_MUL;
  const u64 tg = pt0 + (u64)(long long)(X + 8000);
  for (int i = 0; i < 400000 && wall_clock64() < tg; ++i) __builtin_amdgcn_s_sleep(1);
}
__global__ void __launch_bounds__(512, 2) fused_kernel(const u16* kc_, const u16* vc_, const u16* wo_, const u16* wq_, const u16* wmod_, const u16* w1_, const u16* w3_,
                                                       unsigned char* ws_, const u16* x_, const u16* cnd_, const u16* w2_, u16* out_) {
  __shared__ __attribute__((aligned(256))) Smem sm;
  const u64 a_ka = (u64)__builtin_amdgcn_kernarg_segment_ptr();
  const int bid = blockIdx.x, tid = threadIdx.x;
  u64 wa, t64; u32x2 te; int tid0;
  u32 ar[14];
  u32 tch2, sa_, sb_, va_;
  u32x4 kr[NL], vr[NL];
  asm volatile(
    "s_load_dwordx16 s[4:19], %[ka], 0x0\ns_load_dwordx8 s[20:27], %[ka], 0x40\n"
    "s_memrealtime %[t]\n"
    "v_lshlrev_b32 %[va], 3, %[bid]\n"
    "s_getpc_b64 s[28:29]\n.Lcpf_first:\n"
    TAB_LOAD
    "s_cmp_gt_u32 %[bid], 15\ns_cbranch_scc1 .Lno_cpf\n"
    "s_mov_b32 %[sa], .Lend_a-.Lcpf_first\ns_max_i32 %[sa], %[sa], .Lend_b-.Lcpf_first\ns_max_i32 %[sa], %[sa], .Lend_c-.Lcpf_first\ns_max_i32 %[sa], %[sa], .Lend_d-.Lcpf_first\ns_max_i32 %[sa], %[sa], .Lend_e-.Lcpf_first\n"
    "v_lshlrev_b32 %[va], 6, %[tid]\nv_min_u32 %[va], %[sa], %[va]\nglobal_load_dword %[pf], %[va], s[28:29]\n"
    ".Lno_cpf:\n"
    "s_and_b32 %[sa], %[bid], 15\ns_lshr_b32 %[sb], %[bid], 6\ns_lshl2_add_u32 %[sa], %[sa], %[sb]\ns_lshl_b32 %[sa], %[sa], 22\ns_xor_b32 %[sa], %[sa], 0xff0000\n"
    "s_and_b32 %[sb], %[bid], 48\nv_lshl_add_u32 %[va], %[sb], 5, %[tid]\nv_lshl_add_u32 %[va], %[va], 4, %[sa]\n"
    "v_readfirstlane_b32 %[t0], %[tid]\n"
    "s_waitcnt lgkmcnt(0)\nglobal_load_dwordx4 %[k0], %[va], s[4:5] nt\nglobal_load_dwordx4 %[v0], %[va], s[6:7] nt\n"
    "s_bitcmp1_b32 %[t0], 6\ns_cselect_b64 s[14:15], s[16:17], s[14:15]\n"
    "s_bitcmp1_b32 %[bid], 4\ns_cselect_b64 s[8:9], s[10:11], s[8:9]\ns_cselect_b64 s[12:13], s[14:15], s[12:13]\ns_bitcmp1_b32 %[bid], 5\ns_cselect_b64 %[wa], s[12:13], s[8:9]"
    : "=&{s4}"(ar[0]), "=&{s5}"(ar[1]), "=&{s6}"(ar[2]), "=&{s7}"(ar[3]), "=&{s18}"(ar[4]), "=&{s19}"(ar[5]), "=&{s20}"(ar[6]), "=&{s21}"(ar[7]), "=&{s22}"(ar[8]), "=&{s23}"(ar[9]),
      "=&{s24}"(ar[10]), "=&{s25}"(ar[11]), "=&{s26}"(ar[12]), "=&{s27}"(ar[13]), [wa] "=&s"(wa), [t0] "=&s"(tid0), [t] "=&s"(t64), [te] "=&v"(te), [sa] "=&s"(sa_), [sb] "=&s"(sb_),
      [va] "=&v"(va_), [pf] "=&v"(tch2), [k0] "=&v"(kr[0]), [v0] "=&v"(vr[0])
    : [ka] "s"(a_ka), [bid] "s"(bid), [tid] "v"(tid) : "s8", "s9", "s10", "s11", "s12", "s13", "s14", "s15", "s16", "s17", "s28", "s29", "s30", "s31", "scc", "memory");
  asm volatile("" : "+s"(ar[0]), "+s"(ar[1]), "+s"(ar[2]), "+s"(ar[3]), "+s"(ar[4]), "+s"(ar[5]), "+s"(ar[6]), "+s"(ar[7]), "+s"(ar[8]), "+s"(ar[9]), "+s"(ar[10]), "+s"(ar[11]), "+s"(ar[12]), "+s"(ar[13]));
  asm volatile("" : "+s"(wa), "+s"(t64), "+s"(tid0));
  const u32 tka = (u32)wall_clock64();
  const u64 pt0 = t64; u64 pst = (PROBE_K == 100 || PROBE_K == 102) ? pt0 : (PROBE_K == 101 ? pt0 + (u32)(tka - (u32)t64) : 0);
  const u32 t0 = tka;
  __builtin_assume(tid >= 0 && tid < 512 && tid0 >= 0 && tid0 < 512 && bid >= 0 && bid < 256);
  constexpr Prm P = PRM0;
  const int TEND = P.TEND;
  const int xcd = bid & 7, part = bid >> 4 & 3;
  const int lane = tid & 63, wave = tid0 >> 6;
  SB();
  const bool p3 = part == 3;
  u32 pitch = (u32)(0x005600c001000020ull >> (part * 16) & 0xffff) * 32u;
  const int sh = p3 ? 2 : 3;
  u32 wv0 = (u32)((p3 ? (wave >> 1) * 128 : wave * 64) + (part == 0 ? xcd * 512 : 0) + (lane >> sh)) * pitch + ((lane & (p3 ? 3 : 7)) ^ (lane >> (p3 ? 5 : 3))) * 16u;
  const u32 wimg = (u32)(uintptr_t)(&sm.wbuf[0][0][0]);
  u32 wsz = (part == 0 ? 4096u : 512u) * pitch, wld = wimg + (p3 ? (wave & 1) * 32768 + (wave >> 1) * 8192 : wave * 8192);
  PIN(wv0);
  asm volatile("" : "+s"(wsz), "+s"(wld), "+s"(pitch));
  auto karg = [&](int i) { return ar[2 * i] | (u64)ar[2 * i + 1] << 32; };
  const u64 a_kc = karg(0), a_vc = karg(1);
  unsigned char* ws = (unsigned char*)karg(2);
  const u16 *x = (const u16*)karg(3), *cnd = (const u16*)karg(4), *w2 = (const u16*)karg(5);
  u16* out = (u16*)karg(6);
  const rsrc_t wsr = mk_rsrc(ws, WS_END);
  asm volatile("s_waitcnt vmcnt(2)" : "+v"(te));
  const u32 te0 = __builtin_amdgcn_readfirstlane(te[0]), te1 = __builtin_amdgcn_readfirstlane(te[1]);
  const int bl0 = te0 & 127, nl = te0 >> 7 & 63, blk = te0 >> 13 & 63;
  const bool has_w = te0 >> 19 & 1;
  const rsrc_t rw = mk_rsrc((const void*)wa, wsz);
  const u32 wstep = pitch << (6 - sh), wflip = p3 ? 32u : 0u;
  u32 wso = blk << (sh + 4);
  int wleft = 8;
  auto piece = [&]() {
    dma16(rw, wv0, wso, wld);
    wso += wstep; wld += 1024;
    wv0 ^= (wleft & 1) ? wflip : 0u;
    --wleft;
  };
  SB();
  STAMP(23);
  const int b = bid >> 3 & 1, head = xcd * 4 + (bid >> 6 ^ 3);
  const int g = lane >> 4, c = lane & 15;
  const int i8 = b | bid >> 6 << 1, wi = i8 * 8 + xcd;
  const bool is_up = p3 && has_w, modc = part == 2 && wi < 16;
  const size_t base = ((size_t)(b * NH + head) * SK + (SK - WIN)) * DH;
  const rsrc_t rk = mk_rsrc((const u16*)a_kc + base, WIN * DH * 2), rv = mk_rsrc((const u16*)a_vc + base, WIN * DH * 2);
  int tau = te1 & 0xffff;
  const int tauw = te1 >> 16;
  int due = P.NF * tau >> 2, now = 0;
  auto gate = [&]() {
    while (now < due) { now = (int)(((u32)wall_clock64() - t0) << 8); if (now < due) __builtin_amdgcn_s_sleep(1); }
    due += tau;
  };
  auto until = [&](int t) { while ((int)(((u32)wall_clock64() - t0) << 8) < t); };
  const u32 pslot = G_P + ((b * 32 + head) * 4 + part) * 256;
  u32x2 ep; u32 xs0, xs1, tch1; u32x4 xr0, xr1;
  const rsrc_t rx = mk_rsrc(part == 2 ? cnd : x, (part == 1 || (part == 2 && has_w)) ? 2048u : 0u);
  auto small_ops = [&]() {
    ep = ld8<AUX_SC1>(wsr, wave == 7 ? WS_EPOCH : OOB, 0);
    xs0 = __builtin_amdgcn_raw_buffer_load_b16(rx, tid * 2, 0, 0); xs1 = __builtin_amdgcn_raw_buffer_load_b16(rx, tid * 2, 1024, 0);
    xr0 = ld16<0>(rx, part == 1 ? lane * 16 : OOB, 0); xr1 = ld16<0>(rx, part == 1 ? lane * 16 : OOB, 1024);
    tch1 = __builtin_amdgcn_raw_buffer_load_b32(mk_rsrc(w2 + (size_t)blk * 32 * 512, is_up ? 64u : 0u), tid == 384 ? 0 : OOB, 0, AUX_NT);
  };
  if (has_w) {
    if (modc) now = 1 << 30;
#pragma nounroll
    while (wleft > (int)(te0 >> 20)) { gate(); due += tauw - tau; piece(); }
    now = 0;
  }
  SB();
  u32 sot;
  {
    const u32 s1 = bl0 + lane - 1 - (lane >= SK0 ? NL - nl : 0), s0 = s1 + (s1 >= 108 ? 4 : 0);
    u32 sb = s0 & 96u;
#pragma unroll
    for (int k = 0; k < 5; ++k) sb |= (s0 >> k & 1u) << (P.PM >> (4 * k) & 7);
    const u32 src = (sb >> 5 & 3u) | (u32)(b * 32 + head) << 2;
    sb ^= ((__builtin_popcount(src & P.M0) & 1) | (__builtin_popcount(src & P.M1) & 1) << 1 | (__builtin_popcount(src & P.M2) & 1) << 2 | (__builtin_popcount(src & P.M3) & 1) << 3 | (__builtin_popcount(src & P.M4) & 1) << 4) & (s1 >= 96 ? 7u : 31u);
    sot = (sb & 7u) << 10 | (u32)wave << 13 | (sb >> 3) << 16;
    sot = s1 >= 120 ? (s1 + 4) << 13 | (u32)wave << 10 : sot;
    PIN(sot);
  }
  u32 tag = 0;
  u32x4 bf[8];
  auto wo_prep = [&](int s0 = 0, int s1 = 2) {
    const u32 wi_ = wimg + wave * 8192;
    const int q = (lane >> 2) & 3, pp = lane & 3;
#pragma unroll
    for (int s = s0; s < s1; ++s) {
#pragma unroll
      for (int t = 0; t < 4; ++t) {
        const int r0 = s * 32 + g * 8 + q, r1 = r0 + 4, ch = t * 2 + (pp >> 1);
        const u32x2 lo = tr_read(wi_ + r0 * 128 + ((ch ^ (r0 & 7)) * 16) + (pp & 1) * 8), hi = tr_read(wi_ + r1 * 128 + ((ch ^ (r1 & 7)) * 16) + (pp & 1) * 8);
        bf[s * 4 + t] = u32x4{lo[0], lo[1], hi[0], hi[1]};
      }
    }
  };
  auto gemv_go = [&](u32 hh) {
    sm.hb[tid] = (u16)hh; sm.hb[512 + tid] = (u16)(hh >> 16);
    WAVE_SYNC();
    const u32x4* a = (c & 11) == 0 ? reinterpret_cast<const u32x4*>(&sm.hb[(c >> 2) * 512 + wave * 64 + g * 8]) : &sm.zero[0];
    const u32x4 a0 = a[0], a1 = a[(c & 11) == 0 ? 4 : 0];
    const f32x4 z = {0.f, 0.f, 0.f, 0.f};
    f32x4 d[4];
#pragma unroll
    for (int t = 0; t < 4; ++t) d[t] = mfma16(a0, bf[t], z);
#pragma unroll
    for (int t = 0; t < 4; ++t) d[t] = mfma16(a1, bf[4 + t], d[t]);
    if (lane < 32) {
#pragma unroll
      for (int t = 0; t < 4; ++t) sm.r2[(t * 16 + c) * 20 + g * 8 + wave] = d[t][0];
    }
  };
  auto mod_stage = [&]() {
    wo_prep();
    gemv_go(pack_bf(silu_f(lo_f(xs0)), silu_f(lo_f(xs1))));
    __builtin_amdgcn_s_waitcnt(0xc07f);
    if (lane == 0) *LDSP(volatile u32, (u32)(uintptr_t)&sm.mflag[wave]) = 1u;
    if (wave == 7) {
#pragma nounroll
      for (int spin = 0; spin < SPIN_MAX; ++spin) {
        const u32 f = *LDSP(volatile u32, (u32)(uintptr_t)&sm.mflag[lane & 7]);
        if (all_ok(f != 0u)) break;
        __builtin_amdgcn_s_sleep(1);
      }
      CFENCE();
      st8_sc1(wsr, G_MOD + (blk * 64 + lane) * 8, pack_bf(sum8(&sm.r2[lane * 20]), sum8(&sm.r2[lane * 20 + 8])), tag);
    }
    STAMP(9);
  };
  if (lane < 4) sm.zero[lane] = u32x4{0u, 0u, 0u, 0u};
  if (part == 2 && has_w) {
    if (tid < 8) sm.mflag[tid] = 0u;
    LDS_BARRIER();
    if (__builtin_expect(modc, 0)) {
      small_ops();
      __builtin_amdgcn_raw_buffer_store_b128(u32x4{SENT, SENT, SENT, SENT}, wsr, tid < 320 ? G_P + wi * 5120 + tid * 16 : OOB, 0, AUX_SC1);
      asm volatile("s_waitcnt vmcnt(0)" ::: "memory");
      tag = next_tag(__builtin_amdgcn_readfirstlane(ep[0]));
      mod_stage();
      __builtin_amdgcn_s_barrier();
    }
  }
  SB();
  const int fd = wi * 8 + (lane & 7);
  const int ub = wave >> 2, ud = (wave & 3) * 128 + c * 8 + g * 2;
  u32x4 sg[3]; u32 sx2;
  auto side_loads = [&]() {
    const bool fin = part == 1 && wave == 0;
    sg[0] = ld16<AUX_SC1>(wsr, is_up ? G_MOD + (1024 + ud) * 8 : (fin ? G_MOD + (1024 + fd) * 8 : OOB), 0);
    sg[1] = ld16<AUX_SC1>(wsr, is_up ? G_MOD + (1536 + ud) * 8 : (fin ? G_MOD + (2560 + fd) * 8 : OOB), 0);
    sg[2] = ld16<AUX_SC1>(wsr, is_up ? G_MOD + (2048 + ud) * 8 : OOB, 0);
    sx2 = __builtin_amdgcn_raw_buffer_load_b32(mk_rsrc(x, (is_up || fin) ? 2048u : 0u), is_up ? ub * 1024 + ud * 2 : (g & 1) * 1024 + (fd >> 1) * 4, 0, 0);
  };
  auto side_pin = [&]() { PIN(sg[0]); PIN(sg[1]); PIN(sg[2]); PIN(sx2); };
  const u32 mlo = G_ML + (head * 2 + b) * 64 + (lane & 3) * 16;
  float fmrg = 0.f;
  auto merge_factor = [&](const u32x4& ml) {
    const float mi = u2f(ml[0]);
    float M = fmaxf(mi, dpp<XOR1>(mi)); M = fmaxf(M, dpp<XOR2>(M));
    const float e = __expf(mi - M);
    float L = e * u2f(ml[2]);
    L += dpp<XOR1>(L); L += dpp<XOR2>(L);
    fmrg = rdl(e * __builtin_amdgcn_rcpf(L), part);
  };
  f32x2 ga = {0.f, 0.f}, xin = {0.f, 0.f}, cc1 = {0.f, 0.f};
  float Af = 0.f, Bf = 0.f;
  const int hpos = ub * 512 + ud;
  auto up_prep = [&]() {
    if (!all_ok((sg[0][1] == tag) & (sg[0][3] == tag) & (sg[1][1] == tag) & (sg[1][3] == tag) & (sg[2][1] == tag) & (sg[2][3] == tag))) gather16<3>(wsr, G_MOD + (1024 + ud) * 8, 512 * 8, tag, sg);
    ga = f32x2{ub ? hi_f(sg[0][0]) : lo_f(sg[0][0]), ub ? hi_f(sg[0][2]) : lo_f(sg[0][2])};
    cc1 = f32x2{rbf(1.f + (ub ? hi_f(sg[2][0]) : lo_f(sg[2][0]))), rbf(1.f + (ub ? hi_f(sg[2][2]) : lo_f(sg[2][2])))};
    xin = f32x2{lo_f(sx2), hi_f(sx2)};
    u32* hp = reinterpret_cast<u32*>(&sm.hb[hpos]);
    hp[512] = pack_bf(cc1[0], cc1[1]);
    hp[1024] = ub ? perm_hi(sg[1][0], sg[1][2]) : perm_lo(sg[1][0], sg[1][2]);
  };
  auto up_frags = [&]() {
    const int pp = lane & 3;
    const u32 a = wimg + (pp & 1) * 32768 + ((pp >> 1) * 32 + g * 8 + ((lane >> 2) & 3)) * 64 + (((wave >> 1) ^ ((g & 1) | (pp & 2))) * 16) + (wave & 1) * 8;
#pragma unroll
    for (int rb = 0; rb < 8; ++rb) {
      const u32x2 lo = tr_read(a + rb * 4096), hi = tr_read(a + rb * 4096 + 256);
      bf[rb] = u32x4{lo[0], lo[1], hi[0], hi[1]};
    }
  };
  const u32x4* ap = reinterpret_cast<const u32x4*>(&sm.hb[((lane >> 3) & 1) * 512 + (lane & 1) * 32 + g * 8]);
  const bool par = lane & 8;
  float l0 = 0.f, l1 = 0.f;
  u32x2 gmd[2], tbg;
  int t1 = 0;
#pragma unroll
  for (int i = SK0; i < SK1; ++i) kr[i] = u32x4{0u, 0u, 0u, 0u};
  const int first = SK0 + NL - nl;
  auto left_out = [&](int i) { int f = first; asm volatile("" : "+s"(f)); return i < f; };
  sfor<1, NL>([&](auto ic) {
    constexpr int i = ic.v;
    if constexpr (i >= SK0 && i < SK1) { if (left_out(i)) return true; }
    const u32 so = __builtin_amdgcn_readlane(sot, i);
    gate();
    SB();
    if constexpr (i >= SK0 && i < SK1) ld16_into(kr[i], rk, lane * 16, so); else kr[i] = ld16<AUX_NT>(rk, lane * 16, so);
    if constexpr (i == SMO) small_ops();
    if constexpr (i == TBL) tbg = ld8<AUX_SC1>(wsr, wave == 7 ? G_TB + xcd * 128 + (lane & 7) * 8 : OOB, 0);
    if constexpr (i == QP) { gmd[0] = ld8<AUX_SC1>(wsr, part == 1 ? G_MOD + tid * 8 : OOB, 0); gmd[1] = ld8<AUX_SC1>(wsr, part == 1 ? G_MOD + tid * 8 : OOB, 4096); }
    SB();
    SB();
    if (i >= WK) { WAIT_VM(WK); SB(); }
    if (i == 1) STAMP(25);
    if (i == 8) STAMP(12);
    if (i == 20) STAMP(21);
    if (i == 32) STAMP(22);
    if (i == TBP) {
      if (part == 3 && i8 == 0 && wave == 7) { tag = next_tag(__builtin_amdgcn_readfirstlane(ep[0])); if (lane < 8) st8_sc1(wsr, G_TB + lane * 128 + xcd * 8, t0 + (((tka - (u32)t64) * P.GA) >> 3) - P.GS, tag); }
      SB();
    }
    if (i == TBU) {
      if (wave == 7) {
        const bool ok = tbg[1] == tag;
        float lf = ok ? (float)min(max((int)(t0 - tbg[0]), -300), 300) : 0.f, nf = ok ? 1.f : 0.f;
        lf += dpp<XOR1>(lf); nf += dpp<XOR1>(nf); lf += dpp<XOR2>(lf); nf += dpp<XOR2>(nf); lf += dpp<XOR7>(lf); nf += dpp<XOR7>(nf);
        if (lane == 0) sm.bc = u32x2{tag, t0 - (u32)(int)(lf * __builtin_amdgcn_rcpf(fmaxf(nf, 1.f)))};
      }
      LDS_BARRIER();
      const u32x2 bc = sm.bc;
      tag = __builtin_amdgcn_readfirstlane(bc[0]);
      t1 = (__builtin_amdgcn_readfirstlane((int)(bc[1] - t0)) + TEND + P.X1) * 256;
      tau -= __builtin_amdgcn_readfirstlane((int)((float)(int)(t0 - bc[1]) * 256.f * __builtin_amdgcn_rcpf((float)(2 * nl - TBU - 3))));
      SB();
    }
    if (i == LNS) {
      tag = next_tag(__builtin_amdgcn_readfirstlane(ep[0]));
      if (part == 1) {
        float x0[8], x1[8], mb0, ri0, mb1, ri1;
#pragma unroll
        for (int k = 0; k < 4; ++k) { x0[2 * k] = lo_f(xr0[k]); x0[2 * k + 1] = hi_f(xr0[k]); x1[2 * k] = lo_f(xr1[k]); x1[2 * k + 1] = hi_f(xr1[k]); }
        ln_stats2(x0, x1, mb0, ri0, mb1, ri1);
        l0 = rbf(rbf(lo_f(xs0) - mb0) * ri0); l1 = rbf(rbf(lo_f(xs1) - mb1) * ri1);
      }
      PIN(l0); PIN(l1);
      SB();
    }
    if (i == WUSE) {
      PIN(tch1); PIN(tch2);
      if ((part == 1 || (part == 2 && has_w && !modc))) {
        WAIT_VM(WK); CFENCE();
        if (part == 2) mod_stage(); else wo_prep();
      }
#pragma unroll
      for (int k = 0; k < 8; ++k) PIN(bf[k]);
      SB();
    }
    if (i == QP + QD) {
      if (part == 1) {
        u32 gm[2] = {gmd[0][0], gmd[1][0]};
        if (!all_ok((gmd[0][1] == tag) & (gmd[1][1] == tag))) { STAMP(11); gather8<2>(wsr, G_MOD + tid * 8, 512 * 8, tag, gm); }
        STAMP(5);
        gemv_go(pack_bf(rbf(l0 * rbf(1.f + lo_f(gm[1]))) + lo_f(gm[0]), rbf(l1 * rbf(1.f + hi_f(gm[1]))) + hi_f(gm[0])));
      }
      SB();
    }
    if (i == QP + QD + 1) {
      if (part == 1) {
        LDS_BARRIER();
        if (wave == 0) {
          const int bb = lane >> 5, jq = lane & 31;
          st8_sc1(wsr, G_Q + (bb * 2048 + blk * 32 + jq) * 8, pack_bf(sum8(&sm.r2[2 * jq * 20 + bb * 8]), sum8(&sm.r2[(2 * jq + 1) * 20 + bb * 8])), tag);
        }
        STAMP(6);
      }
      SB();
    }
    return true;
  });
  PIN(sot);
  const u32 qoff = G_Q + ((b * 32 + head) * 64 + c * 4) * 8;
  u32x4 qg[2], mlg;
  u32 qq[4];
  constexpr int NB = (NL + 15) / 16;
  float xlo, sc[NB], pr[NB], m = 0.f;
  f32x4 acc = {0.f, 0.f, 0.f, 0.f};
  u32 pb[NB], pm[4], pl8[8], cpf;
  const u32 adst0 = lds_addr(lane < 32 ? &sm.red[wave][c * 8 + g * 4] : &sm.r2[(lane - 32) * 4]);
  u32 po, rdst, rsrc, xo;
  auto wo_addr = [&]() {
    po = G_P + (((c >> 2 & 1) * 32 + xcd * 4 + (wave >> 1)) * 4 + (c & 3)) * 256 + (wave & 1) * 128 + (c >> 3) * 64 + g * 16;
    rdst = lane < 32 ? (u32)(uintptr_t)&sm.red2[wave][0] + lane * 16 : (u32)(uintptr_t)&sm.r2[0] + (lane - 32) * 16;
    rsrc = (u32)(uintptr_t)&sm.red2[0][((wave & 1) * 16 + (lane & 15)) * 4 + (lane >> 4)];
    xo = (lane & 1) ? OOB : G_XP + xcd * 2048 + (wave & 1) * 1024 + (blk * 64 + lane) * 2;
    PIN(po); PIN(rdst); PIN(rsrc); PIN(xo);
  };
  u32 pso = tid < 32 ? pslot + c * 16 + g * 8 : OOB;
  PIN(pso);
  const u32 nadd = (te0 >> 20) ? P.LW : (is_up ? P.LU : 0);
  u32 nto = tid == 0 ? G_NT + bid * 8 : OOB;
  PIN(nto);
  auto notices = [&](auto nc_, u32 off, u32x4 (&gn)[nc_.v]) -> int {
    constexpr int N = nc_.v;
#pragma nounroll
    for (int spin = 0; spin < SPIN_MAX; ++spin) {
      bool ok = true;
#pragma unroll
      for (int i = 0; i < N; ++i) ok &= (gn[i][1] == tag) & (gn[i][3] == tag);
      if (all_ok(ok)) break;
      __builtin_amdgcn_s_sleep(10);
      CFENCE();
#pragma unroll
      for (int i = 0; i < N; ++i) gn[i] = ld16<AUX_SC1>(wsr, off, i * 1024);
    }
    int mx = 0;
#pragma unroll
    for (int i = 0; i < N; ++i) mx = max(mx, max((int)(gn[i][0] - t0), (int)(gn[i][2] - t0)));
    return __builtin_amdgcn_readfirstlane((int)wave_max((float)min(mx, 1 << 20)));
  };
  auto pv_row = [&](auto jc) {
    constexpr int i = jc.v;
    const u32 pv = __float_as_uint(bcast16(u2f(pb[i >> 4]), i & 15));
    acc = mfma16(u32x4{pv & pm[0], pv & pm[1], pv & pm[2], pv & pm[3]}, vr[i], acc);
    PIN(acc);
    return true;
  };
  sfor<0, NL - 1>([&](auto ic) {
    constexpr int v = ic.v;
    if constexpr (v >= SK0 && v < SK1) { if (left_out(v)) return true; }
    if constexpr (v > 0) {
      const u32 so = __builtin_amdgcn_readlane(sot, v);
      if constexpr (v >= SK1) { if (v == NL - P.NE) tau = (tau * P.FE) >> 4; }
      gate();
      SB();
      if constexpr (v >= SK0 && v < SK1) ld16_into(vr[v], rv, lane * 16, so); else vr[v] = ld16<AUX_NT>(rv, lane * 16, so);
      SB();
      SB();
    }
    if constexpr (v == SK0 - 1) {
#pragma unroll
      for (int i = SK0; i < SK1; ++i) { vr[i] = u32x4{0u, 0u, 0u, 0u}; PIN(vr[i]); }
    }
    if constexpr (v == VQ - 4) { qg[0] = ld16<AUX_SC1>(wsr, qoff, 0); qg[1] = ld16<AUX_SC1>(wsr, qoff + 16, 0); }
    if constexpr (v == VSIDE) side_loads();
    if constexpr (v == CPF) {
      u64 pc; u32 dlt, len;
      asm volatile("s_getpc_b64 %0\n.Lcpf_here:\n\ts_mov_b32 %1, .Ltail_start-.Lcpf_here\n\ts_mov_b32 %2, .Lend_a-.Ltail_start\n\ts_max_i32 %2, %2, .Lend_b-.Ltail_start\n\ts_max_i32 %2, %2, .Lend_c-.Ltail_start\n\ts_max_i32 %2, %2, .Lend_d-.Ltail_start\n\ts_max_i32 %2, %2, .Lend_e-.Ltail_start" : "=s"(pc), "=s"(dlt), "=s"(len) :: "scc");
      pc += dlt;
      const rsrc_t rc = mk_rsrc((const void*)pc, (part == 3 && i8 == 1) ? len : 0u);
      cpf = __builtin_amdgcn_raw_buffer_load_b32(rc, tid * 64, 0, 0);
    }
    if constexpr (v == SK1 + 2) PIN(cpf);
    if constexpr (v == SK1) mlg = ld16<AUX_SC1>(wsr, wave == 0 ? mlo : OOB, 0);
    SB();
    if constexpr (v < WV) { WAIT_VM(WK); SB(); } else if constexpr (v >= SK0 || cons_end(v) < v - WV + 1) { WAIT_VM(v >= SK1 ? WV + 1 : WV); SB(); }
    if constexpr (v == VQ) {
      if (!all_ok((qg[0][1] == tag) & (qg[0][3] == tag) & (qg[1][1] == tag) & (qg[1][3] == tag))) { STAMP(20); gather16<2>(wsr, qoff, 16, tag, qg); }
      STAMP(1); WSTAMP(1);
      qq[0] = qg[0][0]; qq[1] = qg[0][2]; qq[2] = qg[1][0]; qq[3] = qg[1][2];
    }
    if constexpr (v >= VQ && v < VS) {
      constexpr int sl = v - VQ, bt = sl >> 1;
      float xs[8];
#pragma unroll
      for (int i = 0; i < 8; ++i) {
        float d = 0.f;
        if (sl * 8 + i < NL) {
#pragma unroll
          for (int k = 0; k < 4; ++k) d = dot2(kr[sl * 8 + i][k], qq[k], d);
        }
        xs[i] = d;
      }
#pragma unroll
      for (int h = 0; h < 4; ++h) xs[h] = sel_add<XOR7>(xs[h], xs[h + 4], c & 4);
#pragma unroll
      for (int h = 0; h < 2; ++h) xs[h] = sel_add<XOR2>(xs[h], xs[h + 2], c & 2);
      xs[0] = sel_add<XOR1>(xs[0], xs[1], c & 1);
      if constexpr ((sl & 1) || sl == NSL - 1) {
        const float t = (sl & 1) ? sel_add<XOR8>(xlo, xs[0], c & 8) : xs[0] + dpp<XOR8>(xs[0]);
        const float s = rbf(rbf(t) * 0.08838834764831845f);
        sc[bt] = (bt * 16 + c < SK0 || (bt * 16 + c >= first && bt * 16 + c < NL)) ? s : -__builtin_inff();
        PIN(sc[bt]);
      } else {
        xlo = xs[0];
        PIN(xlo);
      }
    }
    if constexpr (v == VS) {
      float mloc = sc[0];
#pragma unroll
      for (int bt = 1; bt < NB; ++bt) mloc = fmaxf(mloc, sc[bt]);
      m = fmaxf(wave_max(mloc), -3.0e38f);
      float ls = 0.f;
#pragma unroll
      for (int bt = 0; bt < NB; ++bt) { pr[bt] = __expf(sc[bt] - m); ls += pr[bt]; }
      const float l = wave_sum(ls);
      if (lane == 0) { sm.ml[wave][0] = m; sm.ml[wave][1] = l; }
      if (c == ((NL - 1) & 15)) sm.pl[g][wave] = pr[NB - 1];
      STAMP(2); WSTAMP(2);
    }
    if constexpr (v >= SK1) pv_row(IC<v - SK1 + SK0 - WV>{});
    if constexpr (v < SK0 || v >= SK0 + WV) sfor<cons_end(v - 1), cons_end(v)>(pv_row);
    if constexpr (v == SK1 + 1) {
      float p[8];
#pragma unroll
      for (int w = 0; w < 8; ++w) p[w] = sm.pl[g][w] * __expf(sm.ml[w][0] - m);
#pragma unroll
      for (int w = 0; w < 8; ++w) pl8[w] = pack_bf(p[w], p[w]);
#pragma unroll
      for (int w = 0; w < 8; ++w) PIN(pl8[w]);
    }
    if constexpr (v == MLS - 1) { side_pin(); if (is_up) up_prep(); PIN(ga); PIN(xin); PIN(cc1); }
    if constexpr (v == UPREP - 1) wo_addr();
    if constexpr (v == UPREP) {
#pragma unroll
      for (int k = 0; k < 8; ++k) asm volatile("" : "=v"(bf[k]));
      if (part == 0) wo_prep(0, 1); else if (is_up) up_frags();
#pragma unroll
      for (int k = 0; k < 8; ++k) PIN(bf[k]);
    }
    if constexpr (v == UPREP + 1) {
      if (is_up) {
        const u32x4* a1 = ap + 128 + ((lane >> 1) & 1) * 128;
        f32x4 d0 = {0.f, 0.f, 0.f, 0.f}, d1 = {0.f, 0.f, 0.f, 0.f};
#pragma unroll
        for (int rb = 0; rb < 4; ++rb) { d0 = mfma16(a1[rb * 8], bf[rb], d0); d1 = mfma16(a1[(rb + 4) * 8], bf[rb + 4], d1); }
        Af = par ? d0[1] + d1[1] : d0[0] + d1[0]; Bf = par ? d0[3] + d1[3] : d0[2] + d1[2];
        Af += dpp<XOR8>(Af); Bf += dpp<XOR8>(Bf);
      }
      PIN(Af); PIN(Bf);
    }
    if (v == 10 || v == 16) STAMP(15 + v / 6);
    if (v == 20) STAMP(18);
    if (v == 32) STAMP(19);
    if constexpr (v == MLS) {
      LDS_BARRIER();
      float mm[8], ll[8];
#pragma unroll
      for (int w = 0; w < 8; ++w) { const f32x2 t = *reinterpret_cast<const f32x2*>(&sm.ml[w][0]); mm[w] = t[0]; ll[w] = t[1]; }
      float Mwg = mm[0], Lwg = 0.f;
#pragma unroll
      for (int w = 1; w < 8; ++w) Mwg = fmaxf(Mwg, mm[w]);
      if (wave == 0) {
#pragma unroll
        for (int w = 0; w < 8; ++w) Lwg = fmaf(__expf(mm[w] - Mwg), ll[w], Lwg);
        if (lane == 0) st16_sc1(wsr, G_ML + ((head * 2 + b) * 4 + part) * 16, __float_as_uint(Mwg), __float_as_uint(Lwg), tag);
      }
      const float fw = __expf(m - Mwg);
      m = Mwg;
#pragma unroll
      for (int bt = 0; bt < NB; ++bt) { pb[bt] = pack_bf(pr[bt] * fw, pr[bt] * fw); PIN(pb[bt]); }
#pragma unroll
      for (int k = 0; k < 4; ++k) { pm[k] = (c >> 1) == k ? ((c & 1) ? 0xffff0000u : 0x0000ffffu) : 0u; PIN(pm[k]); }
    }
    SB();
    return true;
  });
  asm volatile(".Ltail_start:");
  if (wave != 0) {
    pv_row(IC<SK0 - 1>{});
    WAIT_VM(WV); SB();
    sfor<NL - 1 - WV, NL - 1>(pv_row);
    *LDSP(f32x4, adst0) = acc;
    WSTAMP(14);
    LDS_BARRIER();
  } else {
    u32x4 xr[8], am[8];
#pragma unroll
    for (int w = 0; w < 8; ++w) am[w] = u32x4{pl8[w] & pm[0], pl8[w] & pm[1], pl8[w] & pm[2], pl8[w] & pm[3]};
#pragma unroll
    for (int w = 0; w < 8; ++w) PIN(am[w]);
    {
      const u32 so = __builtin_amdgcn_readlane(sot, NL - 1), st = p3 ? 1024u : 8192u;
      gate();
      SB();
#pragma unroll
      for (int w = 0; w < 8; ++w) xr[w] = ld16<AUX_NT>(rv, lane * 16, so + w * st);
      SB();
    }
    u32 rsum = lds_addr(&sm.red[0][c * 8 + (g & 1) * 4]);
    pv_row(IC<SK0 - 1>{});
    WAIT_VM(WV + 8); SB();
    sfor<NL - 1 - WV, NL - 3>(pv_row);
    if (__builtin_expect(!all_ok((mlg[1] == tag) & (mlg[3] == tag)), 0)) {
#pragma nounroll
      for (int spin = 0; spin < SPIN_MAX; ++spin) {
        CFENCE();
        mlg = ld16<AUX_SC1>(wsr, mlo, 0);
        if (all_ok((mlg[1] == tag) & (mlg[3] == tag))) break;
      }
    }
    merge_factor(mlg);
    sfor<NL - 3, NL - 1>(pv_row);
    STAMP(14); WSTAMP(14);
    LDS_BARRIER();
    STAMP(15);
    f32x4 t[8];
#pragma unroll
    for (int w = 1; w < 8; ++w) t[w] = LDSP(f32x4, rsum)[w * 32];
    acc = ((acc + t[1]) + (t[2] + t[3])) + ((t[4] + t[5]) + (t[6] + t[7]));
    PIN(acc);
    STAMP(3);
    SB();
#pragma unroll
    for (int w = 0; w < 8; ++w) acc = mfma16(am[w], xr[w], acc);
  }
  WAIT_VM(0);
  acc *= fmrg;
  __builtin_amdgcn_raw_buffer_store_b64(u32x2{min(pack_bf(acc[0], acc[1]), SENT - 1u), min(pack_bf(acc[2], acc[3]), SENT - 1u)}, wsr, pso, 0, AUX_SC1);
  st8_sc1(wsr, nto, (u32)wall_clock64() + nadd, tag);
  SB();
  STAMP(4);

  if (part == 2) FINISH();
  if (part == 0) {
    __builtin_amdgcn_s_barrier();
#pragma nounroll
    while (wleft > 0) piece();
    until(t1);
    STAMP(11);
    CFENCE();
    u32x4 og[2];
    og[0] = ld16<AUX_SC1>(wsr, po, 0);
    SB();
    WAIT_VM(1); CFENCE();
    wo_prep(1, 2);
#pragma unroll
    for (int k = 4; k < 8; ++k) PIN(bf[k]);
    SB();
    f32x4 d[4];
    auto wo_mm = [&]() {
      const f32x4 z = {0.f, 0.f, 0.f, 0.f};
#pragma unroll
      for (int t = 0; t < 4; ++t) { d[t] = mfma16(og[0], bf[t], z); og[1][t] = __float_as_uint(dpp<XOR8>(u2f(og[0][t]))); }
      SB();
#pragma unroll
      for (int t = 0; t < 4; ++t) d[t] = mfma16(og[1], bf[4 + t], d[t]);
    };
    auto p_bad = [&]() { return !all_ok(max(max(og[0][0], og[0][1]), max(og[0][2], og[0][3])) != SENT); };
    wo_mm();
    if (__builtin_expect(p_bad(), 0)) {
#pragma nounroll
      for (int spin = 0; spin < SPIN_MAX; ++spin) {
        __builtin_amdgcn_s_sleep(1);
        CFENCE();
        og[0] = ld16<AUX_SC1>(wsr, po, 0);
        if (!p_bad()) break;
      }
      wo_mm();
      SB();
      asm volatile(".Lend_d:");
    }
    STAMP(5); WSTAMP(5);
    {
      SB();
      f32x4 y; f32x2 h[4];
#pragma unroll
      for (int t = 0; t < 4; ++t) { h[t] = d[t].xy + d[t].zw; PIN(h[t]); }
#pragma unroll
      for (int t = 0; t < 4; ++t) { y[t] = h[t][0] + h[t][1]; PIN(y[t]); }
      *LDSP(f32x4, rdst) = y;
    }
    STAMP(16);
    LDS_BARRIER();
    STAMP(17);
    if (wave < 2) {
      f32x2 v[4];
#pragma unroll
      for (int w = 0; w < 4; ++w) v[w] = f32x2{LDSP(float, rsrc)[w * 256], LDSP(float, rsrc)[w * 256 + 128]};
      const f32x2 h = (v[0] + v[1]) + (v[2] + v[3]);
      const float y = h[0] + h[1];
      __builtin_amdgcn_raw_buffer_store_b32(min(pack_bf(y, dpp<XOR1>(y)), SENT - 1u), wsr, xo, 0, AUX_SC1);
      asm volatile(".Lend_a:");
    }
    STAMP(6);
    FINISH();
  }
  if (part == 3) {
    if (!has_w) FINISH();
    u32x4 b2[4];
    {
      __builtin_amdgcn_s_barrier();
      const rsrc_t r2 = mk_rsrc(w2 + (size_t)(blk * 32 + wave * 4) * 512, 4096u);
      const u32 l2 = (u32)(uintptr_t)(&sm.w2b[wave * 4][0]);
#pragma unroll
      for (int n = 0; n < 4; ++n) dma16(r2, (lane ^ (n * 2) ^ ((wave & 2) << 2)) * 16, n * 1024, l2 + n * 1024);
    }
    const u32 tdst = lds_addr(&sm.hb[hpos]);
    const u32 sdst = lds_addr(&sm.stat[wave][lane]);
    const u32 ssrc = lds_addr(&sm.stat[(g >> 1) * 4 + ((lane >> 1) & 3)][(lane & 1) * 16]);
    const u32 adst = lds_addr((c < 4 && (g & 1) == 0) ? &sm.actl[g >> 1][wave * 4 + c] : reinterpret_cast<u16*>(&sm.dummy[lane]));
    const u32 asrc = lds_addr(&sm.actl[lane & 1][g * 8]);
    const u32 apa = lds_addr(&sm.hb[(c >> 3) * 512 + (c & 3) * 64 + ((c >> 2) & 1) * 32 + g * 8]);
    u32 xoff = G_XP + g * 2048 + ub * 1024 + (wave & 3) * 256 + c * 16;
    u32x4 one;
#pragma unroll
    for (int k = 0; k < 4; ++k) one[k] = ((c & 2) == 0 && (c >> 2) == k) ? ((c & 1) ? 0x3f800000u : 0x00003f80u) : 0u;
    PIN(one);
    u32 ooff = G_OP + (blk * 512 + tid) * 8;
    float Afp = Af, Bfp = Bf;
    PIN(xoff); PIN(ooff); PIN(Afp); PIN(Bfp);
    PIN(ga); PIN(xin); PIN(cc1);
    u32 w2a = (u32)(uintptr_t)(&sm.w2b[0][0]) + (g * 8 + ((lane >> 2) & 3)) * 1024 + (wave ^ (g & 1)) * 128 + (lane & 15) * 8;
    PIN(w2a);
    if (wave == 1) {
      due = t1 + P.N1 * 256; gate();
      u32x4 gn[2] = {ld16<AUX_SC1>(wsr, G_NT + lane * 16, 0), ld16<AUX_SC1>(wsr, G_NT + lane * 16, 1024)};
      const int tn = notices(IC<2>{}, G_NT + lane * 16, gn) * 256;
      due = (tn > t1 + 2 * 256 ? max(tn, t1 + P.L0 * 256) + P.L1 * 256 : t1) + P.X2 * 256;
      until(due);
    }
    asm volatile("s_waitcnt vmcnt(0)" ::: "memory");
    STAMP(9);
    __builtin_amdgcn_s_barrier();
    STAMP(11);
    CFENCE();
    u32x4 xg[2] = {ld16<AUX_SC1>(wsr, xoff, 0), ld16<AUX_SC1>(wsr, xoff, 8192)};
    SB();
#pragma unroll
    for (int t = 0; t < 4; ++t) {
      const u32x2 lo = tr_read(w2a ^ (t * 32)), hi = tr_read((w2a ^ (t * 32)) + 4096);
      b2[t] = u32x4{lo[0], lo[1], hi[0], hi[1]};
    }
#pragma unroll
    for (int t = 0; t < 4; ++t) PIN(b2[t]);
    SB();
    const f32x4 z4 = {0.f, 0.f, 0.f, 0.f};
    auto xp_bad = [&]() { return !all_ok(max(max(max(xg[0][0], xg[0][1]), max(xg[0][2], xg[0][3])), max(max(xg[1][0], xg[1][1]), max(xg[1][2], xg[1][3]))) != SENT); };
    f32x4 v = mfma16(one, xg[1], mfma16(one, xg[0], z4));
    if (__builtin_expect(xp_bad(), 0)) {
#pragma nounroll
      for (int spin = 0; spin < SPIN_MAX; ++spin) {
        __builtin_amdgcn_s_sleep(1);
        CFENCE();
        xg[0] = ld16<AUX_SC1>(wsr, xoff, 0); xg[1] = ld16<AUX_SC1>(wsr, xoff, 8192);
        if (!xp_bad()) break;
      }
      v = mfma16(one, xg[1], mfma16(one, xg[0], z4));
      SB();
      asm volatile(".Lend_e:");
    }
    STAMP(5); WSTAMP(5);
    {
      const float a0 = v[0], a1 = v[1];
      const float x10 = fmaf(ga[0], a0, xin[0]), x11 = fmaf(ga[1], a1, xin[1]);
      *LDSP(u32, tdst) = pack_bf(x10 * cc1[0], x11 * cc1[1]);
      float u = swap32_add(x10 + x11, fmaf(x10, x10, x11 * x11));
      u += dpp<XOR1>(u); u += dpp<XOR2>(u); u += dpp<XOR7>(u); u += dpp<XOR8>(u);
      *LDSP(float, sdst) = u;
    }
    STAMP(16);
    LDS_BARRIER();
    STAMP(17);
    {
      const f32x4 z = {0.f, 0.f, 0.f, 0.f};
      const u32x4 af0 = LDSP(u32x4, apa)[0], af1 = LDSP(u32x4, apa)[32];
      float s = LDSP(float, ssrc)[0], q = LDSP(float, ssrc)[32];
      SB();
      f32x4 d[4];
#pragma unroll
      for (int j = 0; j < 4; ++j) d[j] = mfma16(af0, bf[j], z);
#pragma unroll
      for (int j = 0; j < 4; ++j) d[j] = mfma16(af1, bf[4 + j], d[j]);
      s += dpp<XOR1>(s); q += dpp<XOR1>(q); s += dpp<XOR2>(s); q += dpp<XOR2>(q); s += dpp<XOR7>(s); q += dpp<XOR7>(q);
      const float mean = s * (1.f / 512.f);
      const float var = fmaf(q, 1.f / 512.f, -mean * mean);
      const float ri = __builtin_amdgcn_rsqf(fmaxf(var, 0.f) + 1e-5f);
      const float cf = fmaf(-ri * mean, Afp, Bfp);
      float y = (d[0][0] + d[1][1]) + (d[2][2] + d[3][3]);
      y = __builtin_bit_cast(float, __builtin_amdgcn_update_dpp(__builtin_bit_cast(int, y), __builtin_bit_cast(int, y), XOR8, 0xa, 0xf, false));
      y = swap16_add(y, y);
      y = fmaf(y, ri, cf);
      const float up = dpp<0x104>(y);
      *LDSP(u16, adst) = (u16)pack_bf(silu_f(y) * up, 0.f);
    }
    STAMP(18);
    LDS_BARRIER();
    STAMP(10);
    {
      const u32x4 af = *LDSP(u32x4, asrc);
      const f32x4 z = {0.f, 0.f, 0.f, 0.f};
      const f32x4 d0 = mfma16(af, b2[0], z), d1 = mfma16(af, b2[1], z), d2 = mfma16(af, b2[2], z), d3 = mfma16(af, b2[3], z);
      const bool g1 = g & 1, g2 = g & 2;
      const float e0 = g1 ? d1[0] : d0[0], e1 = g1 ? d1[1] : d0[1], f0 = g1 ? d3[0] : d2[0], f1 = g1 ? d3[1] : d2[1];
      st8_sc1(wsr, ooff, pack_bf(g2 ? f0 : e0, g2 ? f1 : e1), tag);
    }
    asm volatile(".Lend_b:");
    STAMP(6);
    FINISH();
  }
  if (wave == 0) {
    const int d = fd, ul = lane >> 3;
    u32 gm[2] = {sg[0][0], sg[1][0]};
    if (!all_ok((sg[0][1] == tag) & (sg[1][1] == tag))) gather8<2>(wsr, G_MOD + (1024 + d) * 8, 1536 * 8, tag, gm);
    u32 xa0, xa1;
    {
      due = t1 + P.N2 * 256; gate();
      u32x4 gn[2] = {ld16<AUX_SC1>(wsr, G_NT + lane * 16, 0), ld16<AUX_SC1>(wsr, G_NT + lane * 16, 1024)};
      const int tn = notices(IC<2>{}, G_NT + lane * 16, gn) * 256;
      due = (tn > t1 + 2 * 256 ? max(tn, t1 + P.L0 * 256) + P.L1 * 256 : t1) + P.X3 * 256;
      until(due);
    }
#pragma nounroll
    for (int spin = 0; spin < SPIN_MAX; ++spin) {
      CFENCE();
      xa0 = __builtin_amdgcn_raw_buffer_load_b32(wsr, G_XP + ul * 2048 + (d >> 1) * 4, 0, AUX_SC1);
      xa1 = __builtin_amdgcn_raw_buffer_load_b32(wsr, G_XP + ul * 2048 + (d >> 1) * 4, 1024, AUX_SC1);
      if (all_ok(max(xa0, xa1) != SENT)) break;
      __builtin_amdgcn_s_sleep(1);
    }
    float a0 = (d & 1) ? hi_f(xa0) : lo_f(xa0), a1 = (d & 1) ? hi_f(xa1) : lo_f(xa1);
    a0 += dpp<XOR8>(a0); a1 += dpp<XOR8>(a1);
    a0 = swap16_add(a0, a0); a1 = swap16_add(a1, a1);
    a0 = swap32_add(a0, a0); a1 = swap32_add(a1, a1);
    const bool hb = g & 1;
    float xv = rbf(((d & 1) ? hi_f(sx2) : lo_f(sx2)) + rbf((hb ? hi_f(gm[0]) : lo_f(gm[0])) * rbf(hb ? a1 : a0)));
    float gv = hb ? hi_f(gm[1]) : lo_f(gm[1]);
    PIN(xv); PIN(gv);
    if (wi == 0 && lane == 0) __builtin_amdgcn_raw_buffer_store_b32(tag, wsr, WS_EPOCH, 0, AUX_SC1);
    STAMP(10);
    due = max(due + P.X4 * 256, (int)(((u32)wall_clock64() - t0) << 8) + P.GX * 256);
    const bool last = ul + 40 < NUP;
    u32 o0 = G_OP + (ul * 512 + d) * 8, o5 = last ? o0 : OOB, tg5 = last ? tag : 0u;
    const rsrc_t rout = mk_rsrc(out, 2048u);
    u32 oo = (lane & 40) == 0 ? (g * 512 + d) * 2 : OOB;
    const u32 pk = c == 0 ? 0x00003f80u : (c == 4 ? 0x3f800000u : 0u);
    u32x4 one = {pk, 0u, pk, 0u};
    PIN(o0); PIN(o5); PIN(tg5); PIN(oo); PIN(one);
    __builtin_amdgcn_raw_buffer_store_b16((u16)pack_bf(xv, 0.f), rout, oo, 0, AUX_SC1);
    until(due);
    f32x4 sv;
#pragma nounroll
    for (int spin = 0; spin < SPIN_MAX; ++spin) {
      CFENCE();
      u32x2 og[6];
#pragma unroll
      for (int k = 0; k < 5; ++k) og[k] = ld8<AUX_SC1>(wsr, o0, k * 32768);
      og[5] = ld8<AUX_SC1>(wsr, o5, 5 * 32768);
      sv = f32x4{0.f, 0.f, 0.f, 0.f};
#pragma unroll
      for (int k = 0; k < 3; ++k) sv = mfma16(one, u32x4{og[2 * k][0], og[2 * k][1], og[2 * k + 1][0], og[2 * k + 1][1]}, sv);
      const u32 bad = ((og[0][1] ^ tag) | (og[1][1] ^ tag) | (og[2][1] ^ tag)) | ((og[3][1] ^ tag) | (og[4][1] ^ tag) | (og[5][1] ^ tg5));
      PIN(sv);
      if (all_ok(bad == 0u)) break;
      __builtin_amdgcn_s_sleep(1);
    }
    STAMP(7);
    const float s = sv[0] + dpp<XOR8>(sv[0]);
    __builtin_amdgcn_raw_buffer_store_b16((u16)pack_bf(fmaf(gv, s, xv), 0.f), rout, oo, 0, AUX_SC1);
    asm volatile(".Lend_c:");
  }
  FINISH();
}

extern "C" void kernel_launch(void* const* d_in, const int* in_sizes, int n_in,
                              void* d_out, int out_size, void* d_ws, size_t ws_size,
                              hipStream_t stream) {
  fused_kernel<<<256, 512, 0, stream>>>((const u16*)d_in[1], (const u16*)d_in[2], (const u16*)d_in[4], (const u16*)d_in[3], (const u16*)d_in[10], (const u16*)d_in[6], (const u16*)d_in[7],
                                        (unsigned char*)d_ws, (const u16*)d_in[0], (const u16*)d_in[9], (const u16*)d_in[8], (u16*)d_out);
}
```

```cpp
#define PROBE_MUL 10
#include <hip/hip_runtime.h>
#include <stdint.h>

typedef unsigned int u32;
typedef unsigned short u16;
typedef unsigned long long u64;
typedef __attribute__((ext_vector_type(4))) unsigned u32x4;
typedef __attribute__((ext_vector_type(2))) unsigned u32x2;
typedef __attribute__((ext_vector_type(2))) __bf16 bf16x2;
typedef __attribute__((ext_vector_type(4))) float f32x4;
typedef __attribute__((ext_vector_type(2))) float f32x2;
typedef __amdgpu_buffer_rsrc_t rsrc_t;

constexpr int NH = 32, SK = 16384, DH = 128, WIN = 4096, FF = 1376;
struct Prm { int E0, ES, E1, E2, E2C, EU, TEND, DT_UP, L0, L1, X1, X2, X3, X4, NE, FE, M0, M1, M2, M3, M4, GS, GX, N1, N2, GA, WD, DT_WO, LW, LU, EW, PM, NF, NR, C0, W[9]; };
constexpr Prm PRM0 = {32, 24, 32, 32, 56, 64, 2050, 60, 30, 25, 100, 76, 116, 76, 3, 8, 5, 8, 21, 4, 32, 0, 40, 20, 60, 0, 4, 100, 45, 15, 0, 0x24310, 1, 14, 5, 0x77777777, 0x77777777, 0x77777777, 0x77777777, 0x77777777, 0x44444477, 0x44444444, 0x44444444, 0x44444444};
constexpr int NL = 37;
constexpr int SK0 = 21, SK1 = 32;
constexpr int NCMIN = (NL - SK1 + SK0) * 8;
constexpr int WK = 8, WV = 5;
constexpr int WUSE = 12, LNS = 7;
constexpr int QP = 14, QD = 4;
constexpr int VQ = 6;
constexpr int NSL = (NL + 7) / 8;
constexpr int VS = VQ + NSL;
constexpr int MLS = VS + 2;
constexpr int VSIDE = MLS - 5, UPREP = MLS + 6;
constexpr int CPF = 17;
constexpr int SMO = 1;
constexpr int PRB = 8;
constexpr int TBP = 4, TBL = 11, TBU = 15;
constexpr int window(int s, int w) {
  int n = w;
  for (int u = s - w + 1; u <= s; ++u) n += u == SMO ? 6 : (u == TBL || u == NL + CPF || u == NL + SK1 ? 1 : (u == QP || u == NL + VQ - 4 ? 2 : (u == NL + VSIDE ? 5 : 0)));
  return n;
}
constexpr int cons_end(int v) {
  const int a = v - WV + 1, c = (v - MLS) * 4;
  const int m = a < c ? a : c;
  return m < 0 ? 0 : (m > NL ? NL : m);
}
struct Tab { u32 e[512]; };
constexpr Tab make_tab(const Prm& P) {
  Tab T{};
  for (int bid = 0; bid < 256; ++bid) {
    const int xcd = bid & 7, part = bid >> 4 & 3, i8 = (bid >> 3 & 1) | bid >> 6 << 1, wi = i8 * 8 + xcd;
    const bool has_mod = wi < 48, has_up = wi >= 21;
    const int cb0 = (i8 + P.C0) & 7, cb1 = (i8 + 4) & 7, ms = wi - 20;
    const int mi = wi < 8 ? wi : (wi < 16 ? 8 + ((xcd + 4) & 7) : (wi < 20 ? (wi - 14) * 8 + 3 : 16 + ms / 7 * 8 + ms % 7 + (ms % 7 >= 3 ? 1 : 0)));
    const int ui = xcd * 5 + (xcd > 5 ? xcd - 5 : 0) + (i8 >= 3 ? (xcd >= 5 ? 1 : 0) + i8 - 3 : 0);
    const int wd = cb0 == 3 ? 0 : P.WD;
    const int e[4] = {P.E0 - 4 * wd + (cb0 == 3 ? P.ES : 0), P.E1 + (cb1 == 3 ? P.ES : 0), has_mod ? (wi < 16 ? P.E2C : P.E2) + ((mi & 7) == 3 ? P.ES : 0) : 0, has_up ? P.EU : 0};
    const int span[4] = {wd ? P.TEND - P.DT_WO : P.TEND, P.TEND, P.TEND, has_up ? P.TEND - P.DT_UP : P.TEND};
    const int mg[4] = {wd ? P.EW : 0, 0, 0, has_up ? P.GA : 0};
    int n[4] = {NCMIN / 8, NCMIN / 8, NCMIN / 8, NCMIN / 8};
    for (int left = 128 - NCMIN / 2; left > 0; --left) {
      int best = -1;
      for (int k = 3; k >= 0; --k)
        if (n[k] < NL && (best < 0 || (8 * n[k] + e[k] + mg[k]) * span[best] < (8 * n[best] + e[best] + mg[best]) * span[k])) best = k;
      ++n[best];
    }
    const int c[4] = {0, n[0] - 1, n[0] + n[1] - 2, n[0] + n[1] + n[2] - 3};
    const int nl = n[part], wst = e[part] / 4;
    const bool has_w = part < 2 || (part == 2 ? has_mod : has_up);
    const int blk = !has_w ? 0 : (part == 0 ? cb0 : (part == 1 ? xcd * 8 + cb1 : (part == 2 ? mi : ui)));
    const int tau = span[part] * 1024 / (4 * (2 * nl + wst - 2) + P.NF);
    T.e[2 * bid] = (u32)(c[part] | nl << 7 | blk << 13 | (has_w ? 1 : 0) << 19 | (part == 0 ? wd : 0) << 20);
    T.e[2 * bid + 1] = (u32)(tau | (tau * wst / (part == 0 && wd < 8 ? 8 - wd : 8)) << 16);
  }
  return T;
}
constexpr int step_range(const Prm& P, bool mx_) {
  int mx = 0, mn = 1 << 20;
  const Tab T = make_tab(P);
  for (int bid = 0; bid < 256; ++bid) { const int n = T.e[2 * bid] >> 7 & 63; mx = n > mx ? n : mx; mn = n < mn ? n : mn; }
  return mx_ ? mx : mn;
}
extern "C" __attribute__((used, visibility("protected"))) __constant__ const Tab wg_tab = make_tab(PRM0);
static_assert(step_range(PRM0, true) <= NL && step_range(PRM0, false) >= NCMIN / 8, "a wave's key range does not fit its load steps");
static_assert(UPREP + 1 < SK0 && QP + QD + 1 < SK0 && cons_end(SK0 - 1) == SK0 - WV && NL - SK1 == WV, "steps every wave runs");
constexpr int NUP = FF / 32;
constexpr int SPIN_MAX = 1 << 17;

constexpr u32 WS_EPOCH = 0;
constexpr u32 G_TB = 1024;
constexpr u32 G_NT = 2048;
constexpr u32 G_MOD = 4096;
constexpr u32 G_Q = G_MOD + 3072 * 8;
constexpr u32 G_ML = G_Q + 4096 * 8;
constexpr u32 G_U = G_ML + 256 * 16;
constexpr u32 G_P = 0;
constexpr u32 G_XP = G_P + 256 * 256;
constexpr u32 G_OP = G_XP + 8 * 512 * 4;
constexpr u32 G_PB = G_OP + 44 * 512 * 8;
constexpr u32 G_VT = G_U + 2 * (G_PB + 8192);
constexpr u32 WS_END = G_VT + 1024;
__device__ __forceinline__ u32 dup(u32 o) { return G_U + ((o >> 13) << 14) + (o & 8191u); }
constexpr u32 SENT = 0xffffffffu;
constexpr u32 OOB = 0x40000000u;
#define WST 0

constexpr int AUX_NT = 2, AUX_SC1 = 16;

__device__ __forceinline__ float lo_f(u32 v) { return __uint_as_float(v << 16); }
__device__ __forceinline__ float hi_f(u32 v) { return __uint_as_float(v & 0xffff0000u); }
__device__ __forceinline__ float rbf(float x) { return (float)(__bf16)x; }
__device__ __forceinline__ u32 pack_bf(float lo, float hi) {
  bf16x2 r; r[0] = (__bf16)lo; r[1] = (__bf16)hi; return __builtin_bit_cast(u32, r);
}
__device__ __forceinline__ float dot2(u32 k, u32 q, float acc) {
  return __builtin_amdgcn_fdot2_f32_bf16(__builtin_bit_cast(bf16x2, k), __builtin_bit_cast(bf16x2, q), acc, false);
}
template <int CTRL> __device__ __forceinline__ float dpp(float x) {
  return __builtin_bit_cast(float, __builtin_amdgcn_mov_dpp(__builtin_bit_cast(int, x), CTRL, 0xf, 0xf, true));
}
constexpr int XOR1 = 0xB1, XOR2 = 0x4E, XOR7 = 0x141, XOR8 = 0x128;
__device__ __forceinline__ float swap32_add(float a, float b) {
  auto t = __builtin_amdgcn_permlane32_swap(__float_as_uint(a), __float_as_uint(b), false, false);
  return __uint_as_float(t[0]) + __uint_as_float(t[1]);
}
__device__ __forceinline__ float swap16_add(float a, float b) {
  auto t = __builtin_amdgcn_permlane16_swap(__float_as_uint(a), __float_as_uint(b), false, false);
  return __uint_as_float(t[0]) + __uint_as_float(t[1]);
}
__device__ __forceinline__ float wave_sum(float x) {
  x += dpp<XOR1>(x); x += dpp<XOR2>(x); x += dpp<XOR7>(x); x += dpp<XOR8>(x);
  x = swap16_add(x, x);
  return swap32_add(x, x);
}
__device__ __forceinline__ float wave_max(float x) {
  x = fmaxf(x, dpp<XOR1>(x)); x = fmaxf(x, dpp<XOR2>(x)); x = fmaxf(x, dpp<XOR7>(x)); x = fmaxf(x, dpp<XOR8>(x));
  auto s = __builtin_amdgcn_permlane16_swap(__float_as_uint(x), __float_as_uint(x), false, false);
  x = fmaxf(__uint_as_float(s[0]), __uint_as_float(s[1]));
  auto t = __builtin_amdgcn_permlane32_swap(__float_as_uint(x), __float_as_uint(x), false, false);
  return fmaxf(__uint_as_float(t[0]), __uint_as_float(t[1]));
}
template <int CTRL> __device__ __forceinline__ float sel_add(float lo, float hi, bool up) {
  const float t1 = lo + dpp<CTRL>(lo), t2 = hi + dpp<CTRL>(hi);
  return up ? t2 : t1;
}
template <int I> __device__ __forceinline__ float bc16(float x) {
  return __builtin_bit_cast(float, __builtin_amdgcn_update_dpp(0, __builtin_bit_cast(int, x), 0x150 + I, 0xf, 0xf, false));
}
__device__ __forceinline__ float bcast16(float x, int i) {
  switch (i) {
    case 0: return bc16<0>(x); case 1: return bc16<1>(x); case 2: return bc16<2>(x); case 3: return bc16<3>(x);
    case 4: return bc16<4>(x); case 5: return bc16<5>(x); case 6: return bc16<6>(x); case 7: return bc16<7>(x);
    case 8: return bc16<8>(x); case 9: return bc16<9>(x); case 10: return bc16<10>(x); case 11: return bc16<11>(x);
    case 12: return bc16<12>(x); case 13: return bc16<13>(x); case 14: return bc16<14>(x); default: return bc16<15>(x);
  }
}
__device__ __forceinline__ float silu_f(float x) { return x * __builtin_amdgcn_rcpf(1.f + __expf(-x)); }

template <int AUX> __device__ __forceinline__ u32x4 ld16(rsrc_t r, u32 voff, u32 soff) {
  return __builtin_amdgcn_raw_buffer_load_b128(r, voff, soff, AUX);
}
template <int AUX> __device__ __forceinline__ u32x2 ld8(rsrc_t r, u32 voff, u32 soff) {
  return __builtin_amdgcn_raw_buffer_load_b64(r, voff, soff, AUX);
}
__device__ __forceinline__ void st8_sc1(rsrc_t r, u32 voff, u32 v, u32 tag) {
  __builtin_amdgcn_raw_buffer_store_b64(u32x2{v, tag}, r, voff, 0, AUX_SC1);
}
__device__ __forceinline__ void st16_sc1(rsrc_t r, u32 voff, u32 v0, u32 v1, u32 tag) {
  __builtin_amdgcn_raw_buffer_store_b128(u32x4{v0, tag, v1, tag}, r, voff, 0, AUX_SC1);
}
__device__ __forceinline__ rsrc_t mk_rsrc(const void* p, u32 bytes) {
  return __builtin_amdgcn_make_buffer_rsrc(const_cast<void*>(p), 0, bytes, 0x00020000);
}
#define WAIT_VM(n) __builtin_amdgcn_s_waitcnt(((n) & 15) | (((n) >> 4) << 14) | (7 << 4) | (15 << 8))
#define SB() __builtin_amdgcn_sched_barrier(0)
#define CFENCE() asm volatile("" ::: "memory")
__device__ __forceinline__ bool all_ok(bool ok) { return __builtin_amdgcn_ballot_w64(!ok) == 0ull; }

template <int N> __device__ __forceinline__ void gather16(rsrc_t ws, u32 voff, u32 stride, u32 tag, u32x4 (&g)[N]) {
#pragma nounroll
  for (int spin = 0; spin < SPIN_MAX; ++spin) {
    CFENCE();
    bool ok = true;
#pragma unroll
    for (int i = 0; i < N; ++i) g[i] = ld16<AUX_SC1>(ws, voff + i * stride, 0);
#pragma unroll
    for (int i = 0; i < N; ++i) ok &= (g[i][1] == tag) & (g[i][3] == tag);
    if (all_ok(ok)) break;
    __builtin_amdgcn_s_sleep(8);
  }
}
template <int N> __device__ __forceinline__ void gather8(rsrc_t ws, u32 voff, u32 stride, u32 tag, u32 (&g)[N], bool act = true) {
#pragma nounroll
  for (int spin = 0; spin < SPIN_MAX; ++spin) {
    CFENCE();
    bool ok = true;
    u32x2 t[N];
#pragma unroll
    for (int i = 0; i < N; ++i) t[i] = ld8<AUX_SC1>(ws, voff + i * stride, 0);
#pragma unroll
    for (int i = 0; i < N; ++i) { ok &= (t[i][1] == tag); g[i] = t[i][0]; }
    if (all_ok(ok | !act)) break;
    __builtin_amdgcn_s_sleep(8);
  }
}

struct Smem {
  float red[8][128];
  float ml[8][2];
  float r2[64 * 20];
  u16 hb[3 * 1024];
  float stat[8][64];
  float red2[8][128];
  u32 dummy[64];
  u32x2 bc;
  u32x4 zero[4];
  float pl[4][8];
  u32 mflag[8];
  u32x4 wbuf[8][8][64];
  __attribute__((aligned(256))) u16 w2b[32][512];
  u16 actl[2][32];
};

#define PROBE_K 8
#define PROBE_OP 0
#define PROBE_PART -1
#define STAMP(k) do { if ((k) == PROBE_K && tid == 0) pst = wall_clock64(); } while (0)
#define WSTAMP(k) do {} while (0)
#define FINISH() do { STAMP(8); if (tid == 0) { __hip_atomic_store(reinterpret_cast<u64*>(ws + (1u << 20)) + bid * 2, pt0 + (u32)(t0 - (u32)pt0) + (t1 >> 8), __ATOMIC_RELAXED, __HIP_MEMORY_SCOPE_AGENT); \
   __hip_atomic_store(reinterpret_cast<u64*>(ws + (1u << 20)) + bid * 2 + 1, (PROBE_PART < 0 || part == PROBE_PART) ? pst : 0ull, __ATOMIC_RELAXED, __HIP_MEMORY_SCOPE_AGENT); } \
   if (bid == 8 && wave == 0) probe_end(ws, pt0, lane); return; } while (0)
#define PIN(x) asm volatile("" : "+v"(x))
#define LDS_BARRIER() do { __builtin_amdgcn_s_waitcnt(0xc07f); __builtin_amdgcn_s_barrier(); CFENCE(); } while (0)
#define WAVE_SYNC() do { __builtin_amdgcn_fence(__ATOMIC_RELEASE, "wavefront"); __builtin_amdgcn_wave_barrier(); __builtin_amdgcn_fence(__ATOMIC_ACQUIRE, "wavefront"); } while (0)

__device__ __forceinline__ void dma16(rsrc_t r, u32 voff, u32 soff, u32 lds_dst) {
  u32 keep;
  asm volatile("s_mov_b32 %0, m0\n\ts_mov_b32 m0, %4\n\ts_nop 4\n\tbuffer_load_dwordx4 %1, %2, %3 offen nt lds\n\ts_mov_b32 m0, %0"
               : "=&s"(keep) : "v"(voff), "s"(r), "s"(soff), "s"(lds_dst) : "memory");
}
__device__ __forceinline__ void ld16_into(u32x4& d, rsrc_t r, u32 voff, u32 soff) {
  asm volatile("s_nop 4\n\tbuffer_load_dwordx4 %0, %1, %2, %3 offen nt" : "+v"(d) : "v"(voff), "s"(r), "s"(soff) : "memory");
}
__device__ __forceinline__ u32 perm_lo(u32 a, u32 b) { return __builtin_amdgcn_perm(b, a, 0x05040100u); }
__device__ __forceinline__ u32 perm_hi(u32 a, u32 b) { return __builtin_amdgcn_perm(b, a, 0x07060302u); }
__device__ __forceinline__ float rdl(float x, int l) { return __int_as_float(__builtin_amdgcn_readlane(__float_as_int(x), l)); }

typedef __attribute__((ext_vector_type(8))) __bf16 bf16x8;
__device__ __forceinline__ f32x4 mfma16(u32x4 a, u32x4 b, f32x4 c) {
  return __builtin_amdgcn_mfma_f32_16x16x32_bf16(__builtin_bit_cast(bf16x8, a), __builtin_bit_cast(bf16x8, b), c, 0, 0, 0);
}
__device__ __forceinline__ float sum8(const float* p) {
  const f32x4 a = *reinterpret_cast<const f32x4*>(p), b = *reinterpret_cast<const f32x4*>(p + 4);
  return ((a[0] + b[0]) + (a[1] + b[1])) + ((a[2] + b[2]) + (a[3] + b[3]));
}
__device__ __forceinline__ float ln_ri(float var) {
  return __builtin_amdgcn_rcpf(rbf(__builtin_amdgcn_sqrtf(rbf(rbf(fmaxf(var, 0.f)) + 1e-5f))));
}
__device__ __forceinline__ void ln_stats2(const float (&x0)[8], const float (&x1)[8], float& mb0, float& ri0, float& mb1, float& ri1) {
  const float k0 = rdl(x0[0], 0), k1 = rdl(x1[0], 0);
  float s0 = 0.f, s1 = 0.f, q0 = 0.f, q1 = 0.f;
#pragma unroll
  for (int k = 0; k < 8; ++k) {
    const float d0 = x0[k] - k0, d1 = x1[k] - k1;
    s0 += d0; s1 += d1; q0 = fmaf(d0, d0, q0); q1 = fmaf(d1, d1, q1);
  }
  float t = swap16_add(swap32_add(s0, s1), swap32_add(q0, q1));
  t += dpp<XOR1>(t); t += dpp<XOR2>(t); t += dpp<XOR7>(t); t += dpp<XOR8>(t);
  auto u = __builtin_amdgcn_permlane16_swap(__float_as_uint(t), __float_as_uint(t), false, false);
  const float ms = __uint_as_float(u[0]) * (1.f / 512.f), mq = __uint_as_float(u[1]) * (1.f / 512.f);
  const float ri = ln_ri(fmaf(-ms, ms, mq));
  mb0 = rbf(k0 + rdl(ms, 0)); mb1 = rbf(k1 + rdl(ms, 32));
  ri0 = rdl(ri, 0); ri1 = rdl(ri, 32);
}
template <int I> struct IC { static constexpr int v = I; };
template <int I, int N, class F> __device__ __forceinline__ void sfor(F&& f) {
  if constexpr (I < N) { if (f(IC<I>{})) sfor<I + 1, N>(f); }
}
typedef __attribute__((ext_vector_type(4))) short s16x4;
typedef __attribute__((address_space(3))) s16x4* lds_s16x4p;
__device__ __forceinline__ u32x2 tr_read(u32 lds_addr) {
  return __builtin_bit_cast(u32x2, __builtin_amdgcn_ds_read_tr16_b64_v4i16((lds_s16x4p)lds_addr));
}
__device__ __forceinline__ u32 next_tag(u32 e) { return ((e | 0x40004000u) + 1u) & ~0x40004000u; }
__device__ __forceinline__ float u2f(u32 v) { return __uint_as_float(v); }
#define LDSP(T, a) ((__attribute__((address_space(3))) T*)(a))
template <class T> __device__ __forceinline__ u32 lds_addr(T* p) { u32 a = (u32)(uintptr_t)p; PIN(a); return a; }

#define TAB_LOAD "s_add_u32 s30, s28, wg_tab@rel32@lo+4\ns_addc_u32 s31, s29, wg_tab@rel32@hi+12\nglobal_load_dwordx2 %[te], %[va], s[30:31]\n"
__device__ __forceinline__ void probe_end(unsigned char* ws, u64 pt0, int lane) {
  while (wall_clock64() < pt0 + 5500ull) __builtin_amdgcn_s_sleep(8);
  u64 a[4], c2[4];
  for (int i = 0; i < 4; ++i) {
    a[i] = __hip_atomic_load(reinterpret_cast<u64*>(ws + (1u << 20)) + (i * 64 + lane) * 2, __ATOMIC_RELAXED, __HIP_MEMORY_SCOPE_AGENT);
    c2[i] = __hip_atomic_load(reinterpret_cast<u64*>(ws + (1u << 20)) + (i * 64 + lane) * 2 + 1, __ATOMIC_RELAXED, __HIP_MEMORY_SCOPE_AGENT);
  }
  int mn0 = 1 << 30, mx1 = -(1 << 30), mn1 = 1 << 30, sum = 0, cnt = 0;
  for (int i = 0; i < 4; ++i) {
    const int d0 = (int)(a[i] - pt0);
    mn0 = min(mn0, d0);
    if (c2[i] != 0ull) { const int d1 = (int)(c2[i] - pt0); mx1 = max(mx1, d1); mn1 = min(mn1, d1); sum += d1; cnt += 1; }
  }
  for (int o = 1; o < 64; o <<= 1) { mn0 = min(mn0, __shfl_xor(mn0, o)); mx1 = max(mx1, __shfl_xor(mx1, o)); mn1 = min(mn1, __shfl_xor(mn1, o)); sum += __shfl_xor(sum, o); cnt += __shfl_xor(cnt, o); }
  int X = PROBE_OP == 0 ? mx1 : (PROBE_OP == 1 ? sum / max(cnt, 1) : mn1);
  if (PROBE_K == 102) X = mn0;
  X = mn0 + (X - mn0) * PROBE_MUL;
  const u64 tg = pt0 + (u64)(long long)(X + 8000);
  for (int i = 0; i < 400000 && wall_clock64() < tg; ++i) __builtin_amdgcn_s_sleep(1);
}
__global__ void __launch_bounds__(512, 2) fused_kernel(const u16* kc_, const u16* vc_, const u16* wo_, const u16* wq_, const u16* wmod_, const u16* w1_, const u16* w3_,
                                                       unsigned char* ws_, const u16* x_, const u16* cnd_, const u16* w2_, u16* out_) {
  __shared__ __attribute__((aligned(256))) Smem sm;
  const u64 a_ka = (u64)__builtin_amdgcn_kernarg_segment_ptr();
  const int bid = blockIdx.x, tid = threadIdx.x;
  u64 wa, t64; u32x2 te; int tid0;
  u32 ar[14];
  u32 tch2, sa_, sb_, va_;
  u32x4 kr[NL], vr[NL];
  asm volatile(
    "s_load_dwordx16 s[4:19], %[ka], 0x0\ns_load_dwordx8 s[20:27], %[ka], 0x40\n"
    "s_memrealtime %[t]\n"
    "v_lshlrev_b32 %[va], 3, %[bid]\n"
    "s_getpc_b64 s[28:29]\n.Lcpf_first:\n"
    TAB_LOAD
    "s_cmp_gt_u32 %[bid], 15\ns_cbranch_scc1 .Lno_cpf\n"
    "s_mov_b32 %[sa], .Lend_a-.Lcpf_first\ns_max_i32 %[sa], %[sa], .Lend_b-.Lcpf_first\ns_max_i32 %[sa], %[sa], .Lend_c-.Lcpf_first\ns_max_i32 %[sa], %[sa], .Lend_d-.Lcpf_first\ns_max_i32 %[sa], %[sa], .Lend_e-.Lcpf_first\n"
    "v_lshlrev_b32 %[va], 6, %[tid]\nv_min_u32 %[va], %[sa], %[va]\nglobal_load_dword %[pf], %[va], s[28:29]\n"
    ".Lno_cpf:\n"
    "s_and_b32 %[sa], %[bid], 15\ns_lshr_b32 %[sb], %[bid], 6\ns_lshl2_add_u32 %[sa], %[sa], %[sb]\ns_lshl_b32 %[sa], %[sa], 22\ns_xor_b32 %[sa], %[sa], 0xff0000\n"
    "s_and_b32 %[sb], %[bid], 48\nv_lshl_add_u32 %[va], %[sb], 5, %[tid]\nv_lshl_add_u32 %[va], %[va], 4, %[sa]\n"
    "v_readfirstlane_b32 %[t0], %[tid]\n"
    "s_waitcnt lgkmcnt(0)\nglobal_load_dwordx4 %[k0], %[va], s[4:5] nt\nglobal_load_dwordx4 %[v0], %[va], s[6:7] nt\n"
    "s_bitcmp1_b32 %[t0], 6\ns_cselect_b64 s[14:15], s[16:17], s[14:15]\n"
    "s_bitcmp1_b32 %[bid], 4\ns_cselect_b64 s[8:9], s[10:11], s[8:9]\ns_cselect_b64 s[12:13], s[14:15], s[12:13]\ns_bitcmp1_b32 %[bid], 5\ns_cselect_b64 %[wa], s[12:13], s[8:9]"
    : "=&{s4}"(ar[0]), "=&{s5}"(ar[1]), "=&{s6}"(ar[2]), "=&{s7}"(ar[3]), "=&{s18}"(ar[4]), "=&{s19}"(ar[5]), "=&{s20}"(ar[6]), "=&{s21}"(ar[7]), "=&{s22}"(ar[8]), "=&{s23}"(ar[9]),
      "=&{s24}"(ar[10]), "=&{s25}"(ar[11]), "=&{s26}"(ar[12]), "=&{s27}"(ar[13]), [wa] "=&s"(wa), [t0] "=&s"(tid0), [t] "=&s"(t64), [te] "=&v"(te), [sa] "=&s"(sa_), [sb] "=&s"(sb_),
      [va] "=&v"(va_), [pf] "=&v"(tch2), [k0] "=&v"(kr[0]), [v0] "=&v"(vr[0])
    : [ka] "s"(a_ka), [bid] "s"(bid), [tid] "v"(tid) : "s8", "s9", "s10", "s11", "s12", "s13", "s14", "s15", "s16", "s17", "s28", "s29", "s30", "s31", "scc", "memory");
  asm volatile("" : "+s"(ar[0]), "+s"(ar[1]), "+s"(ar[2]), "+s"(ar[3]), "+s"(ar[4]), "+s"(ar[5]), "+s"(ar[6]), "+s"(ar[7]), "+s"(ar[8]), "+s"(ar[9]), "+s"(ar[10]), "+s"(ar[11]), "+s"(ar[12]), "+s"(ar[13]));
  asm volatile("" : "+s"(wa), "+s"(t64), "+s"(tid0));
  const u32 tka = (u32)wall_clock64();
  const u64 pt0 = t64; u64 pst = (PROBE_K == 100 || PROBE_K == 102) ? pt0 : (PROBE_K == 101 ? pt0 + (u32)(tka - (u32)t64) : 0);
  const u32 t0 = tka;
  __builtin_assume(tid >= 0 && tid < 512 && tid0 >= 0 && tid0 < 512 && bid >= 0 && bid < 256);
  constexpr Prm P = PRM0;
  const int TEND = P.TEND;
  const int xcd = bid & 7, part = bid >> 4 & 3;
  const int lane = tid & 63, wave = tid0 >> 6;
  SB();
  const bool p3 = part == 3;
  u32 pitch = (u32)(0x005600c001000020ull >> (part * 16) & 0xffff) * 32u;
  const int sh = p3 ? 2 : 3;
  u32 wv0 = (u32)((p3 ? (wave >> 1) * 128 : wave * 64) + (part == 0 ? xcd * 512 : 0) + (lane >> sh)) * pitch + ((lane & (p3 ? 3 : 7)) ^ (lane >> (p3 ? 5 : 3))) * 16u;
  const u32 wimg = (u32)(uintptr_t)(&sm.wbuf[0][0][0]);
  u32 wsz = (part == 0 ? 4096u : 512u) * pitch, wld = wimg + (p3 ? (wave & 1) * 32768 + (wave >> 1) * 8192 : wave * 8192);
  PIN(wv0);
  asm volatile("" : "+s"(wsz), "+s"(wld), "+s"(pitch));
  auto karg = [&](int i) { return ar[2 * i] | (u64)ar[2 * i + 1] << 32; };
  const u64 a_kc = karg(0), a_vc = karg(1);
  unsigned char* ws = (unsigned char*)karg(2);
  const u16 *x = (const u16*)karg(3), *cnd = (const u16*)karg(4), *w2 = (const u16*)karg(5);
  u16* out = (u16*)karg(6);
  const rsrc_t wsr = mk_rsrc(ws, WS_END);
  asm volatile("s_waitcnt vmcnt(2)" : "+v"(te));
  const u32 te0 = __builtin_amdgcn_readfirstlane(te[0]), te1 = __builtin_amdgcn_readfirstlane(te[1]);
  const int bl0 = te0 & 127, nl = te0 >> 7 & 63, blk = te0 >> 13 & 63;
  const bool has_w = te0 >> 19 & 1;
  const rsrc_t rw = mk_rsrc((const void*)wa, wsz);
  const u32 wstep = pitch << (6 - sh), wflip = p3 ? 32u : 0u;
  u32 wso = blk << (sh + 4);
  int wleft = 8;
  auto piece = [&]() {
    dma16(rw, wv0, wso, wld);
    wso += wstep; wld += 1024;
    wv0 ^= (wleft & 1) ? wflip : 0u;
    --wleft;
  };
  SB();
  STAMP(23);
  const int b = bid >> 3 & 1, head = xcd * 4 + (bid >> 6 ^ 3);
  const int g = lane >> 4, c = lane & 15;
  const int i8 = b | bid >> 6 << 1, wi = i8 * 8 + xcd;
  const bool is_up = p3 && has_w, modc = part == 2 && wi < 16;
  const size_t base = ((size_t)(b * NH + head) * SK + (SK - WIN)) * DH;
  const rsrc_t rk = mk_rsrc((const u16*)a_kc + base, WIN * DH * 2), rv = mk_rsrc((const u16*)a_vc + base, WIN * DH * 2);
  int tau = te1 & 0xffff;
  const int tauw = te1 >> 16;
  int due = P.NF * tau >> 2, now = 0;
  auto gate = [&]() {
    while (now < due) { now = (int)(((u32)wall_clock64() - t0) << 8); if (now < due) __builtin_amdgcn_s_sleep(1); }
    due += tau;
  };
  auto until = [&](int t) { while ((int)(((u32)wall_clock64() - t0) << 8) < t); };
  const u32 pslot = dup(G_P + ((b * 32 + head) * 4 + part) * 256);
  const u32 die = __builtin_amdgcn_s_getreg(20 | (2 << 6));
  const bool voter = p3 && i8 == 1;
  const u32 pbo = dup(G_PB) + (xcd * 8 + wave) * 128;
  u32 near = 0;
  u32x2 ep; u32 xs0, xs1, tch1; u32x4 xr0, xr1;
  const rsrc_t rx = mk_rsrc(part == 2 ? cnd : x, (part == 1 || (part == 2 && has_w)) ? 2048u : 0u);
  auto small_ops = [&]() {
    ep = ld8<AUX_SC1>(wsr, wave == 7 ? WS_EPOCH : OOB, 0);
    xs0 = __builtin_amdgcn_raw_buffer_load_b16(rx, tid * 2, 0, 0); xs1 = __builtin_amdgcn_raw_buffer_load_b16(rx, tid * 2, 1024, 0);
    xr0 = ld16<0>(rx, part == 1 ? lane * 16 : OOB, 0); xr1 = ld16<0>(rx, part == 1 ? lane * 16 : OOB, 1024);
    tch1 = __builtin_amdgcn_raw_buffer_load_b32(mk_rsrc(w2 + (size_t)blk * 32 * 512, is_up ? 64u : 0u), tid == 384 ? 0 : OOB, 0, AUX_NT);
  };
  if (has_w) {
    if (modc) now = 1 << 30;
#pragma nounroll
    while (wleft > (int)(te0 >> 20)) { gate(); due += tauw - tau; piece(); }
    now = 0;
  }
  SB();
  u32 sot;
  {
    const u32 s1 = bl0 + lane - 1 - (lane >= SK0 ? NL - nl : 0), s0 = s1 + (s1 >= 108 ? 4 : 0);
    u32 sb = s0 & 96u;
#pragma unroll
    for (int k = 0; k < 5; ++k) sb |= (s0 >> k & 1u) << (P.PM >> (4 * k) & 7);
    const u32 src = (sb >> 5 & 3u) | (u32)(b * 32 + head) << 2;
    sb ^= ((__builtin_popcount(src & P.M0) & 1) | (__builtin_popcount(src & P.M1) & 1) << 1 | (__builtin_popcount(src & P.M2) & 1) << 2 | (__builtin_popcount(src & P.M3) & 1) << 3 | (__builtin_popcount(src & P.M4) & 1) << 4) & (s1 >= 96 ? 7u : 31u);
    sot = (sb & 7u) << 10 | (u32)wave << 13 | (sb >> 3) << 16;
    sot = s1 >= 120 ? (s1 + 4) << 13 | (u32)wave << 10 : sot;
    PIN(sot);
  }
  u32 tag = 0;
  u32x4 bf[8];
  auto wo_prep = [&](int s0 = 0, int s1 = 2) {
    const u32 wi_ = wimg + wave * 8192;
    const int q = (lane >> 2) & 3, pp = lane & 3;
#pragma unroll
    for (int s = s0; s < s1; ++s) {
#pragma unroll
      for (int t = 0; t < 4; ++t) {
        const int r0 = s * 32 + g * 8 + q, r1 = r0 + 4, ch = t * 2 + (pp >> 1);
        const u32x2 lo = tr_read(wi_ + r0 * 128 + ((ch ^ (r0 & 7)) * 16) + (pp & 1) * 8), hi = tr_read(wi_ + r1 * 128 + ((ch ^ (r1 & 7)) * 16) + (pp & 1) * 8);
        bf[s * 4 + t] = u32x4{lo[0], lo[1], hi[0], hi[1]};
      }
    }
  };
  auto gemv_go = [&](u32 hh) {
    sm.hb[tid] = (u16)hh; sm.hb[512 + tid] = (u16)(hh >> 16);
    WAVE_SYNC();
    const u32x4* a = (c & 11) == 0 ? reinterpret_cast<const u32x4*>(&sm.hb[(c >> 2) * 512 + wave * 64 + g * 8]) : &sm.zero[0];
    const u32x4 a0 = a[0], a1 = a[(c & 11) == 0 ? 4 : 0];
    const f32x4 z = {0.f, 0.f, 0.f, 0.f};
    f32x4 d[4];
#pragma unroll
    for (int t = 0; t < 4; ++t) d[t] = mfma16(a0, bf[t], z);
#pragma unroll
    for (int t = 0; t < 4; ++t) d[t] = mfma16(a1, bf[4 + t], d[t]);
    if (lane < 32) {
#pragma unroll
      for (int t = 0; t < 4; ++t) sm.r2[(t * 16 + c) * 20 + g * 8 + wave] = d[t][0];
    }
  };
  auto mod_stage = [&]() {
    wo_prep();
    gemv_go(pack_bf(silu_f(lo_f(xs0)), silu_f(lo_f(xs1))));
    __builtin_amdgcn_s_waitcnt(0xc07f);
    if (lane == 0) *LDSP(volatile u32, (u32)(uintptr_t)&sm.mflag[wave]) = 1u;
    if (wave == 7) {
#pragma nounroll
      for (int spin = 0; spin < SPIN_MAX; ++spin) {
        const u32 f = *LDSP(volatile u32, (u32)(uintptr_t)&sm.mflag[lane & 7]);
        if (all_ok(f != 0u)) break;
        __builtin_amdgcn_s_sleep(1);
      }
      CFENCE();
      st8_sc1(wsr, G_MOD + (blk * 64 + lane) * 8, pack_bf(sum8(&sm.r2[lane * 20]), sum8(&sm.r2[lane * 20 + 8])), tag);
    }
    STAMP(9);
  };
  if (lane < 4) sm.zero[lane] = u32x4{0u, 0u, 0u, 0u};
  if (part == 2 && has_w) {
    if (tid < 8) sm.mflag[tid] = 0u;
    LDS_BARRIER();
    if (__builtin_expect(modc, 0)) {
      small_ops();
      __builtin_amdgcn_raw_buffer_store_b128(u32x4{SENT, SENT, SENT, SENT}, wsr, G_U + wi * 10240 + tid * 16, 0, AUX_SC1);
      __builtin_amdgcn_raw_buffer_store_b128(u32x4{SENT, SENT, SENT, SENT}, wsr, tid < 128 ? G_U + wi * 10240 + tid * 16 : OOB, 8192, AUX_SC1);
      asm volatile("s_waitcnt vmcnt(0)" ::: "memory");
      tag = next_tag(__builtin_amdgcn_readfirstlane(ep[0]));
      mod_stage();
      __builtin_amdgcn_s_barrier();
    }
  }
  SB();
  const int fd = wi * 8 + (lane & 7);
  const int ub = wave >> 2, ud = (wave & 3) * 128 + c * 8 + g * 2;
  u32x4 sg[3]; u32 sx2;
  auto side_loads = [&]() {
    const bool fin = part == 1 && wave == 0;
    sg[0] = ld16<AUX_SC1>(wsr, is_up ? G_MOD + (1024 + ud) * 8 : (fin ? G_MOD + (1024 + fd) * 8 : OOB), 0);
    sg[1] = ld16<AUX_SC1>(wsr, is_up ? G_MOD + (1536 + ud) * 8 : (fin ? G_MOD + (2560 + fd) * 8 : OOB), 0);
    sg[2] = ld16<AUX_SC1>(wsr, is_up ? G_MOD + (2048 + ud) * 8 : OOB, 0);
    sx2 = __builtin_amdgcn_raw_buffer_load_b32(mk_rsrc(x, (is_up || fin) ? 2048u : 0u), is_up ? ub * 1024 + ud * 2 : (g & 1) * 1024 + (fd >> 1) * 4, 0, 0);
  };
  auto side_pin = [&]() { PIN(sg[0]); PIN(sg[1]); PIN(sg[2]); PIN(sx2); };
  const u32 mlo = G_ML + (head * 2 + b) * 64 + (lane & 3) * 16;
  float fmrg = 0.f;
  auto merge_factor = [&](const u32x4& ml) {
    const float mi = u2f(ml[0]);
    float M = fmaxf(mi, dpp<XOR1>(mi)); M = fmaxf(M, dpp<XOR2>(M));
    const float e = __expf(mi - M);
    float L = e * u2f(ml[2]);
    L += dpp<XOR1>(L); L += dpp<XOR2>(L);
    fmrg = rdl(e * __builtin_amdgcn_rcpf(L), part);
  };
  f32x2 ga = {0.f, 0.f}, xin = {0.f, 0.f}, cc1 = {0.f, 0.f};
  float Af = 0.f, Bf = 0.f;
  const int hpos = ub * 512 + ud;
  auto up_prep = [&]() {
    if (!all_ok((sg[0][1] == tag) & (sg[0][3] == tag) & (sg[1][1] == tag) & (sg[1][3] == tag) & (sg[2][1] == tag) & (sg[2][3] == tag))) gather16<3>(wsr, G_MOD + (1024 + ud) * 8, 512 * 8, tag, sg);
    ga = f32x2{ub ? hi_f(sg[0][0]) : lo_f(sg[0][0]), ub ? hi_f(sg[0][2]) : lo_f(sg[0][2])};
    cc1 = f32x2{rbf(1.f + (ub ? hi_f(sg[2][0]) : lo_f(sg[2][0]))), rbf(1.f + (ub ? hi_f(sg[2][2]) : lo_f(sg[2][2])))};
    xin = f32x2{lo_f(sx2), hi_f(sx2)};
    u32* hp = reinterpret_cast<u32*>(&sm.hb[hpos]);
    hp[512] = pack_bf(cc1[0], cc1[1]);
    hp[1024] = ub ? perm_hi(sg[1][0], sg[1][2]) : perm_lo(sg[1][0], sg[1][2]);
  };
  auto up_frags = [&]() {
    const int pp = lane & 3;
    const u32 a = wimg + (pp & 1) * 32768 + ((pp >> 1) * 32 + g * 8 + ((lane >> 2) & 3)) * 64 + (((wave >> 1) ^ ((g & 1) | (pp & 2))) * 16) + (wave & 1) * 8;
#pragma unroll
    for (int rb = 0; rb < 8; ++rb) {
      const u32x2 lo = tr_read(a + rb * 4096), hi = tr_read(a + rb * 4096 + 256);
      bf[rb] = u32x4{lo[0], lo[1], hi[0], hi[1]};
    }
  };
  const u32x4* ap = reinterpret_cast<const u32x4*>(&sm.hb[((lane >> 3) & 1) * 512 + (lane & 1) * 32 + g * 8]);
  const bool par = lane & 8;
  auto blog = [&](int) {};
  auto limit = [&](auto sc) {
    constexpr int s = sc.v;
    WAIT_VM(window(s, 1 + (PRM0.W[s >> 3] >> (4 * (s & 7)) & 7)));
    SB();
  };
  float l0 = 0.f, l1 = 0.f;
  u32x2 gmd[2], tbg;
  int t1 = 0;
#pragma unroll
  for (int i = SK0; i < SK1; ++i) kr[i] = u32x4{0u, 0u, 0u, 0u};
  const int first = SK0 + NL - nl;
  auto left_out = [&](int i) { int f = first; asm volatile("" : "+s"(f)); return i < f; };
  sfor<1, NL>([&](auto ic) {
    constexpr int i = ic.v;
    if constexpr (i >= SK0 && i < SK1) { if (left_out(i)) return true; }
    const u32 so = __builtin_amdgcn_readlane(sot, i);
    gate();
    blog(i);
    SB();
    if constexpr (i >= SK0 && i < SK1) ld16_into(kr[i], rk, lane * 16, so); else kr[i] = ld16<AUX_NT>(rk, lane * 16, so);
    if constexpr (i == SMO) small_ops();
    if constexpr (i == SMO + 1) { if (voter) __builtin_amdgcn_raw_buffer_store_b32(0u, wsr, pbo + (lane >> 5) * 8192 + (lane & 31) * 4, 0, AUX_SC1); }
    if constexpr (i == TBL) tbg = ld8<AUX_SC1>(wsr, wave == 7 ? G_TB + xcd * 128 + (lane & 7) * 8 : OOB, 0);
    if constexpr (i == QP) { gmd[0] = ld8<AUX_SC1>(wsr, part == 1 ? G_MOD + tid * 8 : OOB, 0); gmd[1] = ld8<AUX_SC1>(wsr, part == 1 ? G_MOD + tid * 8 : OOB, 4096); }
    SB();
    SB();
    if (i >= 4) limit(ic);
    if (i == 1) STAMP(25);
    if (i == 8) STAMP(12);
    if (i == 20) STAMP(21);
    if (i == 32) STAMP(22);
    if (i == TBP) {
      if (part == 3 && i8 == 0 && wave == 7) { tag = next_tag(__builtin_amdgcn_readfirstlane(ep[0])); if (lane < 8) st8_sc1(wsr, G_TB + lane * 128 + xcd * 8, t0 - P.GS, tag); }
      SB();
    }
    if (i == PRB) {
      if (voter) {
        u64 ta, tb, tc; u32 dm;
        const u32 pa = pbo + (wave & 1) * 8192;
        asm volatile("s_memtime %0\n\ts_waitcnt lgkmcnt(0)\n\ts_load_dword %3, %4, %5\n\ts_waitcnt lgkmcnt(0)\n\ts_memtime %1\n\ts_waitcnt lgkmcnt(0)\n\t"
                     "s_load_dword %3, %4, %6\n\ts_waitcnt lgkmcnt(0)\n\ts_memtime %2\n\ts_waitcnt lgkmcnt(0)"
                     : "=&s"(ta), "=&s"(tb), "=&s"(tc), "=&s"(dm) : "s"(ws), "s"(pa), "s"(pa ^ 8192u));
        const int d = min(max((int)(2 * (u32)tb - (u32)ta - (u32)tc), -500), 500);
        if (lane == 0) sm.mflag[wave] = ((wave ^ die) & 1) ? -d : d;
        STAMP(26);
      }
      SB();
    }
    if (i == TBU) {
      if (wave == 7) {
        const bool ok = tbg[1] == tag;
        float lf = ok ? (float)min(max((int)(t0 - tbg[0]), -300), 300) : 0.f, nf = ok ? 1.f : 0.f;
        lf += dpp<XOR1>(lf); nf += dpp<XOR1>(nf); lf += dpp<XOR2>(lf); nf += dpp<XOR2>(nf); lf += dpp<XOR7>(lf); nf += dpp<XOR7>(nf);
        if (lane == 0) sm.bc = u32x2{tag, t0 - (u32)(int)(lf * __builtin_amdgcn_rcpf(fmaxf(nf, 1.f)))};
      }
      LDS_BARRIER();
      const u32x2 bc = sm.bc;
      tag = __builtin_amdgcn_readfirstlane(bc[0]);
      t1 = (__builtin_amdgcn_readfirstlane((int)(bc[1] - t0)) + TEND + P.X1) * 256;
      tau -= __builtin_amdgcn_readfirstlane((int)((float)(int)(t0 - bc[1]) * 256.f * __builtin_amdgcn_rcpf((float)(2 * nl - TBU - 3))));
      if (voter && wave == 7) {
        float vt = (float)(int)sm.mflag[lane & 7];
        vt += dpp<XOR1>(vt); vt += dpp<XOR2>(vt); vt += dpp<XOR7>(vt);
        if (lane < 8) st8_sc1(wsr, G_VT + lane * 128 + xcd * 8, __float_as_uint(vt), tag);
      }
      SB();
    }
    if (i == LNS) {
      tag = next_tag(__builtin_amdgcn_readfirstlane(ep[0]));
      if (part == 1) {
        float x0[8], x1[8], mb0, ri0, mb1, ri1;
#pragma unroll
        for (int k = 0; k < 4; ++k) { x0[2 * k] = lo_f(xr0[k]); x0[2 * k + 1] = hi_f(xr0[k]); x1[2 * k] = lo_f(xr1[k]); x1[2 * k + 1] = hi_f(xr1[k]); }
        ln_stats2(x0, x1, mb0, ri0, mb1, ri1);
        l0 = rbf(rbf(lo_f(xs0) - mb0) * ri0); l1 = rbf(rbf(lo_f(xs1) - mb1) * ri1);
      }
      PIN(l0); PIN(l1);
      SB();
    }
    if (i == WUSE) {
      PIN(tch1); PIN(tch2);
      if ((part == 1 || (part == 2 && has_w && !modc))) {
        WAIT_VM(WK); CFENCE();
        if (part == 2) mod_stage(); else wo_prep();
      }
#pragma unroll
      for (int k = 0; k < 8; ++k) PIN(bf[k]);
      SB();
    }
    if (i == QP + QD) {
      if (part == 1) {
        u32 gm[2] = {gmd[0][0], gmd[1][0]};
        if (!all_ok((gmd[0][1] == tag) & (gmd[1][1] == tag))) { STAMP(11); gather8<2>(wsr, G_MOD + tid * 8, 512 * 8, tag, gm); }
        STAMP(5);
        gemv_go(pack_bf(rbf(l0 * rbf(1.f + lo_f(gm[1]))) + lo_f(gm[0]), rbf(l1 * rbf(1.f + hi_f(gm[1]))) + hi_f(gm[0])));
      }
      SB();
    }
    if (i == QP + QD + 1) {
      if (part == 1) {
        LDS_BARRIER();
        if (wave == 0) {
          const int bb = lane >> 5, jq = lane & 31;
          st8_sc1(wsr, G_Q + (bb * 2048 + blk * 32 + jq) * 8, pack_bf(sum8(&sm.r2[2 * jq * 20 + bb * 8]), sum8(&sm.r2[(2 * jq + 1) * 20 + bb * 8])), tag);
        }
        STAMP(6);
      }
      SB();
    }
    return true;
  });
  PIN(sot);
  const u32 qoff = G_Q + ((b * 32 + head) * 64 + c * 4) * 8;
  u32x4 qg[2], mlg;
  u32 qq[4];
  constexpr int NB = (NL + 15) / 16;
  float xlo, sc[NB], pr[NB], m = 0.f;
  f32x4 acc = {0.f, 0.f, 0.f, 0.f};
  u32 pb[NB], pm[4], pl8[8], cpf;
  const u32 adst0 = lds_addr(lane < 32 ? &sm.red[wave][c * 8 + g * 4] : &sm.r2[(lane - 32) * 4]);
  u32 po, rdst, rsrc, xo;
  auto wo_addr = [&]() {
    po = dup(G_P + (((c >> 2 & 1) * 32 + xcd * 4 + (wave >> 1)) * 4 + (c & 3)) * 256 + (wave & 1) * 128 + (c >> 3) * 64 + g * 16) + ((P.NR & 2) ? near : (xcd & 2) * 4096);
    rdst = lane < 32 ? (u32)(uintptr_t)&sm.red2[wave][0] + lane * 16 : (u32)(uintptr_t)&sm.r2[0] + (lane - 32) * 16;
    rsrc = (u32)(uintptr_t)&sm.red2[0][((wave & 1) * 16 + (lane & 15)) * 4 + (lane >> 4)];
    xo = dup(G_XP + xcd * 2048 + (wave & 1) * 1024 + (blk * 64 + (lane & ~1)) * 2) + (lane & 1) * 8192;
    PIN(po); PIN(rdst); PIN(rsrc); PIN(xo);
  };
  u32 pso = wave == 0 ? pslot + c * 16 + (g & 1) * 8 + (g >> 1) * 8192 : OOB;
  PIN(pso);
  const u32 nadd = (te0 >> 20) ? P.LW : (is_up ? P.LU : 0);
  u32 nto = tid == 0 ? G_NT + bid * 8 : OOB;
  PIN(nto);
  auto notices = [&](auto nc_, u32 off, u32x4 (&gn)[nc_.v]) -> int {
    constexpr int N = nc_.v;
#pragma nounroll
    for (int spin = 0; spin < SPIN_MAX; ++spin) {
      bool ok = true;
#pragma unroll
      for (int i = 0; i < N; ++i) ok &= (gn[i][1] == tag) & (gn[i][3] == tag);
      if (all_ok(ok)) break;
      __builtin_amdgcn_s_sleep(10);
      CFENCE();
#pragma unroll
      for (int i = 0; i < N; ++i) gn[i] = ld16<AUX_SC1>(wsr, off, i * 1024);
    }
    int mx = 0;
#pragma unroll
    for (int i = 0; i < N; ++i) mx = max(mx, max((int)(gn[i][0] - t0), (int)(gn[i][2] - t0)));
    return __builtin_amdgcn_readfirstlane((int)wave_max((float)min(mx, 1 << 20)));
  };
  auto pv_row = [&](auto jc) {
    constexpr int i = jc.v;
    const u32 pv = __float_as_uint(bcast16(u2f(pb[i >> 4]), i & 15));
    acc = mfma16(u32x4{pv & pm[0], pv & pm[1], pv & pm[2], pv & pm[3]}, vr[i], acc);
    PIN(acc);
    return true;
  };
  sfor<0, NL - 1>([&](auto ic) {
    constexpr int v = ic.v;
    if constexpr (v >= SK0 && v < SK1) { if (left_out(v)) return true; }
    if constexpr (v > 0) {
      const u32 so = __builtin_amdgcn_readlane(sot, v);
      if constexpr (v >= SK1) { if (v == NL - P.NE) tau = (tau * P.FE) >> 4; }
      gate();
      blog(NL + v - 1);
      SB();
      if constexpr (v >= SK0 && v < SK1) ld16_into(vr[v], rv, lane * 16, so); else vr[v] = ld16<AUX_NT>(rv, lane * 16, so);
      SB();
      SB();
    }
    if constexpr (v == SK0 - 1) {
#pragma unroll
      for (int i = SK0; i < SK1; ++i) { vr[i] = u32x4{0u, 0u, 0u, 0u}; PIN(vr[i]); }
    }
    if constexpr (v == VQ - 4) { qg[0] = ld16<AUX_SC1>(wsr, qoff, 0); qg[1] = ld16<AUX_SC1>(wsr, qoff + 16, 0); }
    if constexpr (v == VSIDE) { side_loads(); tbg = ld8<AUX_SC1>(wsr, wave == 7 ? G_VT + xcd * 128 + (lane & 7) * 8 : OOB, 0); }
    if constexpr (v == CPF) {
      u64 pc; u32 dlt, len;
      asm volatile("s_getpc_b64 %0\n.Lcpf_here:\n\ts_mov_b32 %1, .Ltail_start-.Lcpf_here\n\ts_mov_b32 %2, .Lend_a-.Ltail_start\n\ts_max_i32 %2, %2, .Lend_b-.Ltail_start\n\ts_max_i32 %2, %2, .Lend_c-.Ltail_start\n\ts_max_i32 %2, %2, .Lend_d-.Ltail_start\n\ts_max_i32 %2, %2, .Lend_e-.Ltail_start" : "=s"(pc), "=s"(dlt), "=s"(len) :: "scc");
      pc += dlt;
      const rsrc_t rc = mk_rsrc((const void*)pc, (part == 3 && i8 == 1) ? len : 0u);
      cpf = __builtin_amdgcn_raw_buffer_load_b32(rc, tid * 64, 0, 0);
    }
    if constexpr (v == SK1 + 2) PIN(cpf);
    if constexpr (v == SK1) mlg = ld16<AUX_SC1>(wsr, wave == 0 ? mlo : OOB, 0);
    SB();
    if constexpr (NL + v < 72) limit(IC<NL + v>{});
    if constexpr (v >= SK0 + WV) { WAIT_VM(v < SK1 ? WV : WV + 1); SB(); }
    if constexpr (v == VQ) {
      if (!all_ok((qg[0][1] == tag) & (qg[0][3] == tag) & (qg[1][1] == tag) & (qg[1][3] == tag))) { STAMP(20); gather16<2>(wsr, qoff, 16, tag, qg); }
      STAMP(1); WSTAMP(1);
      qq[0] = qg[0][0]; qq[1] = qg[0][2]; qq[2] = qg[1][0]; qq[3] = qg[1][2];
    }
    if constexpr (v >= VQ && v < VS) {
      constexpr int sl = v - VQ, bt = sl >> 1;
      float xs[8];
#pragma unroll
      for (int i = 0; i < 8; ++i) {
        float d = 0.f;
        if (sl * 8 + i < NL) {
#pragma unroll
          for (int k = 0; k < 4; ++k) d = dot2(kr[sl * 8 + i][k], qq[k], d);
        }
        xs[i] = d;
      }
#pragma unroll
      for (int h = 0; h < 4; ++h) xs[h] = sel_add<XOR7>(xs[h], xs[h + 4], c & 4);
#pragma unroll
      for (int h = 0; h < 2; ++h) xs[h] = sel_add<XOR2>(xs[h], xs[h + 2], c & 2);
      xs[0] = sel_add<XOR1>(xs[0], xs[1], c & 1);
      if constexpr ((sl & 1) || sl == NSL - 1) {
        const float t = (sl & 1) ? sel_add<XOR8>(xlo, xs[0], c & 8) : xs[0] + dpp<XOR8>(xs[0]);
        const float s = rbf(rbf(t) * 0.08838834764831845f);
        sc[bt] = (bt * 16 + c < SK0 || (bt * 16 + c >= first && bt * 16 + c < NL)) ? s : -__builtin_inff();
        PIN(sc[bt]);
      } else {
        xlo = xs[0];
        PIN(xlo);
      }
    }
    if constexpr (v == VS) {
      float mloc = sc[0];
#pragma unroll
      for (int bt = 1; bt < NB; ++bt) mloc = fmaxf(mloc, sc[bt]);
      m = fmaxf(wave_max(mloc), -3.0e38f);
      float ls = 0.f;
#pragma unroll
      for (int bt = 0; bt < NB; ++bt) { pr[bt] = __expf(sc[bt] - m); ls += pr[bt]; }
      const float l = wave_sum(ls);
      if (lane == 0) { sm.ml[wave][0] = m; sm.ml[wave][1] = l; }
      if (c == ((NL - 1) & 15)) sm.pl[g][wave] = pr[NB - 1];
      STAMP(2); WSTAMP(2);
    }
    if constexpr (v >= SK1) pv_row(IC<v - SK1 + SK0 - WV>{});
    if constexpr (v < SK0 || v >= SK0 + WV) sfor<cons_end(v - 1), cons_end(v)>(pv_row);
    if constexpr (v == SK1 + 1) {
      float p[8];
#pragma unroll
      for (int w = 0; w < 8; ++w) p[w] = sm.pl[g][w] * __expf(sm.ml[w][0] - m);
#pragma unroll
      for (int w = 0; w < 8; ++w) pl8[w] = pack_bf(p[w], p[w]);
#pragma unroll
      for (int w = 0; w < 8; ++w) PIN(pl8[w]);
    }
    if constexpr (v == MLS - 1) { side_pin(); if (is_up) up_prep(); PIN(ga); PIN(xin); PIN(cc1); }
    if constexpr (v == UPREP - 1) wo_addr();
    if constexpr (v == UPREP) {
#pragma unroll
      for (int k = 0; k < 8; ++k) asm volatile("" : "=v"(bf[k]));
      if (part == 0) wo_prep(0, 1); else if (is_up) up_frags();
#pragma unroll
      for (int k = 0; k < 8; ++k) PIN(bf[k]);
    }
    if constexpr (v == UPREP + 1) {
      if (is_up) {
        const u32x4* a1 = ap + 128 + ((lane >> 1) & 1) * 128;
        f32x4 d0 = {0.f, 0.f, 0.f, 0.f}, d1 = {0.f, 0.f, 0.f, 0.f};
#pragma unroll
        for (int rb = 0; rb < 4; ++rb) { d0 = mfma16(a1[rb * 8], bf[rb], d0); d1 = mfma16(a1[(rb + 4) * 8], bf[rb + 4], d1); }
        Af = par ? d0[1] + d1[1] : d0[0] + d1[0]; Bf = par ? d0[3] + d1[3] : d0[2] + d1[2];
        Af += dpp<XOR8>(Af); Bf += dpp<XOR8>(Bf);
      }
      PIN(Af); PIN(Bf);
    }
    if (v == 10 || v == 16) STAMP(15 + v / 6);
    if (v == 20) STAMP(18);
    if (v == 32) STAMP(19);
    if constexpr (v == MLS) {
      if (wave == 7) {
        float vt = tbg[1] == tag ? u2f(tbg[0]) : 0.f;
        vt += dpp<XOR1>(vt); vt += dpp<XOR2>(vt); vt += dpp<XOR7>(vt);
        if (lane == 0) sm.bc[0] = __float_as_uint(vt);
      }
      LDS_BARRIER();
      near = ((__builtin_amdgcn_readfirstlane(sm.bc[0]) >> 31 ^ die ^ P.NR ^ 1) & 1) * 8192u;
      float mm[8], ll[8];
#pragma unroll
      for (int w = 0; w < 8; ++w) { const f32x2 t = *reinterpret_cast<const f32x2*>(&sm.ml[w][0]); mm[w] = t[0]; ll[w] = t[1]; }
      float Mwg = mm[0], Lwg = 0.f;
#pragma unroll
      for (int w = 1; w < 8; ++w) Mwg = fmaxf(Mwg, mm[w]);
      if (wave == 0) {
#pragma unroll
        for (int w = 0; w < 8; ++w) Lwg = fmaf(__expf(mm[w] - Mwg), ll[w], Lwg);
        if (lane == 0) st16_sc1(wsr, G_ML + ((head * 2 + b) * 4 + part) * 16, __float_as_uint(Mwg), __float_as_uint(Lwg), tag);
      }
      const float fw = __expf(m - Mwg);
      m = Mwg;
#pragma unroll
      for (int bt = 0; bt < NB; ++bt) { pb[bt] = pack_bf(pr[bt] * fw, pr[bt] * fw); PIN(pb[bt]); }
#pragma unroll
      for (int k = 0; k < 4; ++k) { pm[k] = (c >> 1 & 3) == k ? ((c & 1) ? 0xffff0000u : 0x0000ffffu) : 0u; PIN(pm[k]); }
    }
    SB();
    return true;
  });
  asm volatile(".Ltail_start:");
  if (wave != 0) {
    pv_row(IC<SK0 - 1>{});
    WAIT_VM(WV); SB();
    sfor<NL - 1 - WV, NL - 1>(pv_row);
    *LDSP(f32x4, adst0) = acc;
    WSTAMP(14);
    LDS_BARRIER();
  } else {
    u32x4 xr[8], am[8];
#pragma unroll
    for (int w = 0; w < 8; ++w) am[w] = u32x4{pl8[w] & pm[0], pl8[w] & pm[1], pl8[w] & pm[2], pl8[w] & pm[3]};
#pragma unroll
    for (int w = 0; w < 8; ++w) PIN(am[w]);
    {
      const u32 so = __builtin_amdgcn_readlane(sot, NL - 1), st = p3 ? 1024u : 8192u;
      gate();
      SB();
#pragma unroll
      for (int w = 0; w < 8; ++w) xr[w] = ld16<AUX_NT>(rv, lane * 16, so + w * st);
      SB();
    }
    u32 rsum = lds_addr(&sm.red[0][c * 8 + (g & 1) * 4]);
    pv_row(IC<SK0 - 1>{});
    WAIT_VM(WV + 8); SB();
    sfor<NL - 1 - WV, NL - 3>(pv_row);
    if (__builtin_expect(!all_ok((mlg[1] == tag) & (mlg[3] == tag)), 0)) {
#pragma nounroll
      for (int spin = 0; spin < SPIN_MAX; ++spin) {
        CFENCE();
        mlg = ld16<AUX_SC1>(wsr, mlo, 0);
        if (all_ok((mlg[1] == tag) & (mlg[3] == tag))) break;
      }
    }
    merge_factor(mlg);
    sfor<NL - 3, NL - 1>(pv_row);
    STAMP(14); WSTAMP(14);
    LDS_BARRIER();
    STAMP(15);
    f32x4 t[8];
#pragma unroll
    for (int w = 1; w < 8; ++w) t[w] = LDSP(f32x4, rsum)[w * 32];
    acc = ((acc + t[1]) + (t[2] + t[3])) + ((t[4] + t[5]) + (t[6] + t[7]));
    PIN(acc);
    STAMP(3);
    SB();
#pragma unroll
    for (int w = 0; w < 8; ++w) acc = mfma16(am[w], xr[w], acc);
  }
  WAIT_VM(0);
  acc *= fmrg;
  __builtin_amdgcn_raw_buffer_store_b64(u32x2{min(pack_bf(acc[0], acc[1]), SENT - 1u), min(pack_bf(acc[2], acc[3]), SENT - 1u)}, wsr, pso, 0, AUX_SC1);
  st8_sc1(wsr, nto, (u32)wall_clock64() + nadd, tag);
  SB();
  STAMP(4);

  if (part == 2) FINISH();
  if (part == 0) {
    __builtin_amdgcn_s_barrier();
#pragma nounroll
    while (wleft > 0) piece();
    until(t1);
    STAMP(11);
    CFENCE();
    u32x4 og[2];
    og[0] = ld16<AUX_SC1>(wsr, po, 0);
    SB();
    WAIT_VM(1); CFENCE();
    wo_prep(1, 2);
#pragma unroll
    for (int k = 4; k < 8; ++k) PIN(bf[k]);
    SB();
    f32x4 d[4];
    auto wo_mm = [&]() {
      const f32x4 z = {0.f, 0.f, 0.f, 0.f};
#pragma unroll
      for (int t = 0; t < 4; ++t) { d[t] = mfma16(og[0], bf[t], z); og[1][t] = __float_as_uint(dpp<XOR8>(u2f(og[0][t]))); }
      SB();
#pragma unroll
      for (int t = 0; t < 4; ++t) d[t] = mfma16(og[1], bf[4 + t], d[t]);
    };
    auto p_bad = [&]() { return !all_ok(max(max(og[0][0], og[0][1]), max(og[0][2], og[0][3])) != SENT); };
    wo_mm();
    if (__builtin_expect(p_bad(), 0)) {
#pragma nounroll
      for (int spin = 0; spin < SPIN_MAX; ++spin) {
        __builtin_amdgcn_s_sleep(1);
        CFENCE();
        og[0] = ld16<AUX_SC1>(wsr, po, 0);
        if (!p_bad()) break;
      }
      wo_mm();
      SB();
      asm volatile(".Lend_d:");
    }
    STAMP(5); WSTAMP(5);
    {
      SB();
      f32x4 y; f32x2 h[4];
#pragma unroll
      for (int t = 0; t < 4; ++t) { h[t] = d[t].xy + d[t].zw; PIN(h[t]); }
#pragma unroll
      for (int t = 0; t < 4; ++t) { y[t] = h[t][0] + h[t][1]; PIN(y[t]); }
      *LDSP(f32x4, rdst) = y;
    }
    STAMP(16);
    LDS_BARRIER();
    STAMP(17);
    if (wave < 2) {
      f32x2 v[4];
#pragma unroll
      for (int w = 0; w < 4; ++w) v[w] = f32x2{LDSP(float, rsrc)[w * 256], LDSP(float, rsrc)[w * 256 + 128]};
      const f32x2 h = (v[0] + v[1]) + (v[2] + v[3]);
      const float y = h[0] + h[1];
      __builtin_amdgcn_raw_buffer_store_b32(min(pack_bf(dpp<0xA0>(y), dpp<0xF5>(y)), SENT - 1u), wsr, xo, 0, AUX_SC1);
      asm volatile(".Lend_a:");
    }
    STAMP(6);
    FINISH();
  }
  if (part == 3) {
    if (!has_w) FINISH();
    u32x4 b2[4];
    {
      __builtin_amdgcn_s_barrier();
      const rsrc_t r2 = mk_rsrc(w2 + (size_t)(blk * 32 + wave * 4) * 512, 4096u);
      const u32 l2 = (u32)(uintptr_t)(&sm.w2b[wave * 4][0]);
#pragma unroll
      for (int n = 0; n < 4; ++n) dma16(r2, (lane ^ (n * 2) ^ ((wave & 2) << 2)) * 16, n * 1024, l2 + n * 1024);
    }
    const u32 tdst = lds_addr(&sm.hb[hpos]);
    const u32 sdst = lds_addr(&sm.stat[wave][lane]);
    const u32 ssrc = lds_addr(&sm.stat[(g >> 1) * 4 + ((lane >> 1) & 3)][(lane & 1) * 16]);
    const u32 adst = lds_addr((c < 4 && (g & 1) == 0) ? &sm.actl[g >> 1][wave * 4 + c] : reinterpret_cast<u16*>(&sm.dummy[lane]));
    const u32 asrc = lds_addr(&sm.actl[lane & 1][g * 8]);
    const u32 apa = lds_addr(&sm.hb[(c >> 3) * 512 + (c & 3) * 64 + ((c >> 2) & 1) * 32 + g * 8]);
    const u32 xs2 = (P.NR & 4) ? 16384 : 16384 + 8192;
    u32 xoff = dup(G_XP) + ((P.NR & 4) ? near : 0) + g * 2048 + ub * 1024 + (wave & 3) * 256 + c * 16;
    u32x4 one;
#pragma unroll
    for (int k = 0; k < 4; ++k) one[k] = ((c & 2) == 0 && (c >> 2) == k) ? ((c & 1) ? 0x3f800000u : 0x00003f80u) : 0u;
    PIN(one);
    u32 ooff = dup(G_OP + (blk * 512 + tid) * 8);
    float Afp = Af, Bfp = Bf;
    PIN(xoff); PIN(ooff); PIN(Afp); PIN(Bfp);
    PIN(ga); PIN(xin); PIN(cc1);
    u32 w2a = (u32)(uintptr_t)(&sm.w2b[0][0]) + (g * 8 + ((lane >> 2) & 3)) * 1024 + (wave ^ (g & 1)) * 128 + (lane & 15) * 8;
    PIN(w2a);
    if (wave == 1) {
      due = t1 + P.N1 * 256; gate();
      u32x4 gn[2] = {ld16<AUX_SC1>(wsr, G_NT + lane * 16, 0), ld16<AUX_SC1>(wsr, G_NT + lane * 16, 1024)};
      const int tn = notices(IC<2>{}, G_NT + lane * 16, gn) * 256;
      due = (tn > t1 + 2 * 256 ? max(tn, t1 + P.L0 * 256) + P.L1 * 256 : t1) + P.X2 * 256;
      until(due);
    }
    asm volatile("s_waitcnt vmcnt(0)" ::: "memory");
    STAMP(9);
    __builtin_amdgcn_s_barrier();
    STAMP(11);
    CFENCE();
    u32x4 xg[2] = {ld16<AUX_SC1>(wsr, xoff, 0), ld16<AUX_SC1>(wsr, xoff, xs2)};
    SB();
#pragma unroll
    for (int t = 0; t < 4; ++t) {
      const u32x2 lo = tr_read(w2a ^ (t * 32)), hi = tr_read((w2a ^ (t * 32)) + 4096);
      b2[t] = u32x4{lo[0], lo[1], hi[0], hi[1]};
    }
#pragma unroll
    for (int t = 0; t < 4; ++t) PIN(b2[t]);
    SB();
    const f32x4 z4 = {0.f, 0.f, 0.f, 0.f};
    auto xp_bad = [&]() { return !all_ok(max(max(max(xg[0][0], xg[0][1]), max(xg[0][2], xg[0][3])), max(max(xg[1][0], xg[1][1]), max(xg[1][2], xg[1][3]))) != SENT); };
    f32x4 v = mfma16(one, xg[1], mfma16(one, xg[0], z4));
    if (__builtin_expect(xp_bad(), 0)) {
#pragma nounroll
      for (int spin = 0; spin < SPIN_MAX; ++spin) {
        __builtin_amdgcn_s_sleep(1);
        CFENCE();
        xg[0] = ld16<AUX_SC1>(wsr, xoff, 0); xg[1] = ld16<AUX_SC1>(wsr, xoff, xs2);
        if (!xp_bad()) break;
      }
      v = mfma16(one, xg[1], mfma16(one, xg[0], z4));
      SB();
      asm volatile(".Lend_e:");
    }
    STAMP(5); WSTAMP(5);
    {
      const float a0 = v[0], a1 = v[1];
      const float x10 = fmaf(ga[0], a0, xin[0]), x11 = fmaf(ga[1], a1, xin[1]);
      *LDSP(u32, tdst) = pack_bf(x10 * cc1[0], x11 * cc1[1]);
      float u = swap32_add(x10 + x11, fmaf(x10, x10, x11 * x11));
      u += dpp<XOR1>(u); u += dpp<XOR2>(u); u += dpp<XOR7>(u); u += dpp<XOR8>(u);
      *LDSP(float, sdst) = u;
    }
    STAMP(16);
    LDS_BARRIER();
    STAMP(17);
    {
      const f32x4 z = {0.f, 0.f, 0.f, 0.f};
      const u32x4 af0 = LDSP(u32x4, apa)[0], af1 = LDSP(u32x4, apa)[32];
      float s = LDSP(float, ssrc)[0], q = LDSP(float, ssrc)[32];
      SB();
      f32x4 d[4];
#pragma unroll
      for (int j = 0; j < 4; ++j) d[j] = mfma16(af0, bf[j], z);
#pragma unroll
      for (int j = 0; j < 4; ++j) d[j] = mfma16(af1, bf[4 + j], d[j]);
      s += dpp<XOR1>(s); q += dpp<XOR1>(q); s += dpp<XOR2>(s); q += dpp<XOR2>(q); s += dpp<XOR7>(s); q += dpp<XOR7>(q);
      const float mean = s * (1.f / 512.f);
      const float var = fmaf(q, 1.f / 512.f, -mean * mean);
      const float ri = __builtin_amdgcn_rsqf(fmaxf(var, 0.f) + 1e-5f);
      const float cf = fmaf(-ri * mean, Afp, Bfp);
      float y = (d[0][0] + d[1][1]) + (d[2][2] + d[3][3]);
      y = __builtin_bit_cast(float, __builtin_amdgcn_update_dpp(__builtin_bit_cast(int, y), __builtin_bit_cast(int, y), XOR8, 0xa, 0xf, false));
      y = swap16_add(y, y);
      y = fmaf(y, ri, cf);
      const float up = dpp<0x104>(y);
      *LDSP(u16, adst) = (u16)pack_bf(silu_f(y) * up, 0.f);
    }
    STAMP(18);
    LDS_BARRIER();
    STAMP(10);
    {
      const u32x4 af = *LDSP(u32x4, asrc);
      const f32x4 z = {0.f, 0.f, 0.f, 0.f};
      const f32x4 d0 = mfma16(af, b2[0], z), d1 = mfma16(af, b2[1], z), d2 = mfma16(af, b2[2], z), d3 = mfma16(af, b2[3], z);
      const bool g1 = g & 1, g2 = g & 2;
      const float e0 = g1 ? d1[0] : d0[0], e1 = g1 ? d1[1] : d0[1], f0 = g1 ? d3[0] : d2[0], f1 = g1 ? d3[1] : d2[1];
      const u32x2 ov = {pack_bf(g2 ? f0 : e0, g2 ? f1 : e1), tag};
      __builtin_amdgcn_raw_buffer_store_b64(ov, wsr, ooff, near ^ 8192u, AUX_SC1);
      __builtin_amdgcn_raw_buffer_store_b64(ov, wsr, ooff, near, AUX_SC1);
    }
    asm volatile(".Lend_b:");
    STAMP(6);
    FINISH();
  }
  if (wave == 0) {
    const int d = fd, ul = lane >> 3;
    u32 gm[2] = {sg[0][0], sg[1][0]};
    if (!all_ok((sg[0][1] == tag) & (sg[1][1] == tag))) gather8<2>(wsr, G_MOD + (1024 + d) * 8, 1536 * 8, tag, gm);
    u32 xa0, xa1;
    {
      due = t1 + P.N2 * 256; gate();
      u32x4 gn[2] = {ld16<AUX_SC1>(wsr, G_NT + lane * 16, 0), ld16<AUX_SC1>(wsr, G_NT + lane * 16, 1024)};
      const int tn = notices(IC<2>{}, G_NT + lane * 16, gn) * 256;
      due = (tn > t1 + 2 * 256 ? max(tn, t1 + P.L0 * 256) + P.L1 * 256 : t1) + P.X3 * 256;
      until(due);
    }
#pragma nounroll
    for (int spin = 0; spin < SPIN_MAX; ++spin) {
      CFENCE();
      xa0 = __builtin_amdgcn_raw_buffer_load_b32(wsr, dup(G_XP + ul * 2048) + near + (d >> 1) * 4, 0, AUX_SC1);
      xa1 = __builtin_amdgcn_raw_buffer_load_b32(wsr, dup(G_XP + ul * 2048) + near + (d >> 1) * 4, 1024, AUX_SC1);
      if (all_ok(max(xa0, xa1) != SENT)) break;
      __builtin_amdgcn_s_sleep(1);
    }
    float a0 = (d & 1) ? hi_f(xa0) : lo_f(xa0), a1 = (d & 1) ? hi_f(xa1) : lo_f(xa1);
    a0 += dpp<XOR8>(a0); a1 += dpp<XOR8>(a1);
    a0 = swap16_add(a0, a0); a1 = swap16_add(a1, a1);
    a0 = swap32_add(a0, a0); a1 = swap32_add(a1, a1);
    const bool hb = g & 1;
    float xv = rbf(((d & 1) ? hi_f(sx2) : lo_f(sx2)) + rbf((hb ? hi_f(gm[0]) : lo_f(gm[0])) * rbf(hb ? a1 : a0)));
    float gv = hb ? hi_f(gm[1]) : lo_f(gm[1]);
    PIN(xv); PIN(gv);
    if (wi == 0 && lane == 0) __builtin_amdgcn_raw_buffer_store_b32(tag, wsr, WS_EPOCH, 0, AUX_SC1);
    STAMP(10);
    due = max(due + P.X4 * 256, (int)(((u32)wall_clock64() - t0) << 8) + P.GX * 256);
    const bool last = ul + 40 < NUP;
    u32 o0 = dup(G_OP + (ul * 512 + d) * 8) + ((P.NR & 8) ? near : (ul & 2) * 4096), o5 = last ? o0 : OOB, tg5 = last ? tag : 0u;
    const rsrc_t rout = mk_rsrc(out, 2048u);
    u32 oo = (lane & 40) == 0 ? (g * 512 + d) * 2 : OOB;
    const u32 pk = c == 0 ? 0x00003f80u : (c == 4 ? 0x3f800000u : 0u);
    u32x4 one = {pk, 0u, pk, 0u};
    PIN(o0); PIN(o5); PIN(tg5); PIN(oo); PIN(one);
    __builtin_amdgcn_raw_buffer_store_b16((u16)pack_bf(xv, 0.f), rout, oo, 0, AUX_SC1);
    until(due);
    f32x4 sv;
#pragma nounroll
    for (int spin = 0; spin < SPIN_MAX; ++spin) {
      CFENCE();
      u32x2 og[6];
#pragma unroll
      for (int k = 0; k < 5; ++k) og[k] = ld8<AUX_SC1>(wsr, o0, k * 65536);
      og[5] = ld8<AUX_SC1>(wsr, o5, 5 * 65536);
      sv = f32x4{0.f, 0.f, 0.f, 0.f};
#pragma unroll
      for (int k = 0; k < 3; ++k) sv = mfma16(one, u32x4{og[2 * k][0], og[2 * k][1], og[2 * k + 1][0], og[2 * k + 1][1]}, sv);
      const u32 bad = ((og[0][1] ^ tag) | (og[1][1] ^ tag) | (og[2][1] ^ tag)) | ((og[3][1] ^ tag) | (og[4][1] ^ tag) | (og[5][1] ^ tg5));
      PIN(sv);
      if (all_ok(bad == 0u)) break;
      __builtin_amdgcn_s_sleep(1);
    }
    STAMP(7);
    const float s = sv[0] + dpp<XOR8>(sv[0]);
    __builtin_amdgcn_raw_buffer_store_b16((u16)pack_bf(fmaf(gv, s, xv), 0.f), rout, oo, 0, AUX_SC1);
    asm volatile(".Lend_c:");
  }
  FINISH();
}

extern "C" void kernel_launch(void* const* d_in, const int* in_sizes, int n_in,
                              void* d_out, int out_size, void* d_ws, size_t ws_size,
                              hipStream_t stream) {
  fused_kernel<<<256, 512, 0, stream>>>((const u16*)d_in[1], (const u16*)d_in[2], (const u16*)d_in[4], (const u16*)d_in[3], (const u16*)d_in[10], (const u16*)d_in[6], (const u16*)d_in[7],
                                        (unsigned char*)d_ws, (const u16*)d_in[0], (const u16*)d_in[9], (const u16*)d_in[8], (u16*)d_out);
}
```

```cpp
#include <hip/hip_runtime.h>
#include <stdint.h>

typedef unsigned int u32;
typedef unsigned short u16;
typedef unsigned long long u64;
typedef __attribute__((ext_vector_type(4))) unsigned u32x4;
typedef __attribute__((ext_vector_type(2))) unsigned u32x2;
typedef __attribute__((ext_vector_type(2))) __bf16 bf16x2;
typedef __attribute__((ext_vector_type(4))) float f32x4;
typedef __attribute__((ext_vector_type(2))) float f32x2;
typedef __amdgpu_buffer_rsrc_t rsrc_t;

constexpr int NH = 32, SK = 16384, DH = 128, WIN = 4096, FF = 1376;
struct Prm { int E0, ES, E1, E2, E2C, EU, TEND, DT_UP, L0, L1, X1, X2, X3, X4, NE, FE, M0, M1, M2, M3, M4, GS, GX, N1, N2, GA, WD, DT_WO, LW, LU, EW, PM, NF, NR, C0, A0, AG, AC, TS, TW, TK, TG, W[9]; };
constexpr Prm PRM0 = {32, 20, 32, 32, 54, 60, 1975, 60, 30, 25, 106, 78, 118, 75, 3, 8, 0x251fb20c, 0x2d45f3ac, 0xff083f7, 0x2b125d6a, 0x396af96d, 0, 40, 20, 60, 0, 4, 100, 45, 15, 0, 0x15c, 15, 14, 5, 75, 0, 100, 510, 236, 60, 20, 0x77777777, 0x77777777, 0x77777777, 0x77777777, 0x77777777, 0x44444477, 0x44444444, 0x44444444, 0x44444444};
constexpr int NL = 37;
constexpr int SK0 = 21, SK1 = 32;
constexpr int NCMIN = (NL - SK1 + SK0) * 8;
constexpr int WK = 8, WV = 5;
constexpr int WUSE = 12, LNS = 7;
constexpr int QP = 14, QD = 4;
constexpr int VQ = 6;
constexpr int NSL = (NL + 7) / 8;
constexpr int VS = VQ + NSL;
constexpr int MLS = VS + 2;
constexpr int VSIDE = MLS - 5, UPREP = MLS + 6;
constexpr int CPF = 17;
constexpr int SMO = 1;
constexpr int RP0 = 24;
constexpr int PRB = 8;
constexpr int TBP = 4, TBL = 11, TBU = 15;
constexpr int window(int s, int w) {
  int n = w;
  for (int u = s - w + 1; u <= s; ++u) n += u == SMO ? 6 : (u == TBL || u == NL + CPF || u == NL + SK1 ? 1 : (u == QP || u == NL + VQ - 4 ? 2 : (u == NL + VSIDE ? 5 : 0)));
  return n;
}
constexpr int cons_end(int v) {
  const int a = v - WV + 1, c = (v - MLS) * 4;
  const int m = a < c ? a : c;
  return m < 0 ? 0 : (m > NL ? NL : m);
}
constexpr u32 col(const Prm& P, int j) { const int m[6] = {P.M0, P.M1, P.M2, P.M3, P.M4, P.PM}; return (u32)(m[j / 3] >> (j % 3 * 10) & 1023) << 10; }
constexpr u32 first_col(const Prm& P, int k) {
  if (k < 4) return col(P, 12 + k) | 1u << (24 + k);
  if (k < 6) return col(P, k - 4);
  if (k < 8) return col(P, 4 + k) | 1u << (16 + k);
  if (k < 11) return col(P, k - 1);
  return (col(P, 2) ^ col(P, 3) ^ col(P, 4) ^ col(P, 5) ^ col(P, 6) ^ col(P, 10) ^ col(P, 11)) | 0xf00000u;
}
constexpr bool one_to_one(const Prm& P) {
  u32 c[10] = {};
  for (int j = 0; j < 10; ++j) c[j] = col(P, j);
  for (int j = 0; j < 10; ++j) {
    if (c[j] == 0) return false;
    const u32 low = c[j] & (0u - c[j]);
    for (int k = j + 1; k < 10; ++k) if (c[k] & low) c[k] ^= c[j];
  }
  return true;
}
static_assert(one_to_one(PRM0), "every KB of the window exactly once");
struct Tab { u32 e[512]; };
constexpr Tab make_tab(const Prm& P) {
  Tab T{};
  for (int bid = 0; bid < 256; ++bid) {
    const int xcd = bid & 7, part = bid >> 4 & 3, i8 = (bid >> 3 & 1) | bid >> 6 << 1, wi = i8 * 8 + xcd;
    const bool has_mod = wi < 48, has_up = wi >= 21;
    const int cb0 = (i8 + P.C0) & 7, cb1 = (i8 + 4) & 7, ms = wi - 20;
    const int mi = wi < 8 ? wi : (wi < 16 ? 8 + ((xcd + 4) & 7) : (wi < 20 ? (wi - 14) * 8 + 3 : 16 + ms / 7 * 8 + ms % 7 + (ms % 7 >= 3 ? 1 : 0)));
    const int ui = xcd * 5 + (xcd > 5 ? xcd - 5 : 0) + (i8 >= 3 ? (xcd >= 5 ? 1 : 0) + i8 - 3 : 0);
    const int wd = cb0 == 3 ? 0 : P.WD;
    const int e[4] = {P.E0 - 4 * wd + (cb0 == 3 ? P.ES : 0), P.E1 + (cb1 == 3 ? P.ES : 0), has_mod ? (wi < 16 ? P.E2C : P.E2) + ((mi & 7) == 3 ? P.ES : 0) : 0, has_up ? P.EU : 0};
    int tw[4] = {}, win[4] = {}, span[4] = {};
    for (int k = 0; k < 4; ++k) {
      const int np = k == 0 ? 8 - wd : 8;
      tw[k] = np ? e[k] * P.TW / (16 * np) : 0;
      span[k] = P.TEND - (P.NF + tw[k] * np / 2 > P.TK ? P.NF + tw[k] * np / 2 : P.TK) - P.TG - (k == 0 && wd ? P.DT_WO : (k == 3 && has_up ? P.DT_UP : 0));
      win[k] = span[k] - (k == 0 && wd ? P.EW : (k == 3 && has_up ? P.GA : 0));
    }
    int n[4] = {NCMIN / 8, NCMIN / 8, NCMIN / 8, NCMIN / 8};
    for (int left = 128 - NCMIN / 2; left > 0; --left) {
      int best = -1;
      for (int k = 3; k >= 0; --k)
        if (n[k] < NL && (best < 0 || n[k] * win[best] < n[best] * win[k])) best = k;
      ++n[best];
    }
    const int c[4] = {0, n[0] - 1, n[0] + n[1] - 2, n[0] + n[1] + n[2] - 3};
    const int nl = n[part];
    const bool has_w = part < 2 || (part == 2 ? has_mod : has_up);
    const int blk = !has_w ? 0 : (part == 0 ? cb0 : (part == 1 ? xcd * 8 + cb1 : (part == 2 ? mi : ui)));
    T.e[2 * bid] = (u32)(c[part] | nl << 7 | blk << 13 | (has_w ? 1 : 0) << 19 | (part == 0 ? wd : 0) << 20) | (u32)tw[part] << 24;
    T.e[2 * bid + 1] = (u32)(span[part] * P.TS / (4 * (nl - 1)) | span[part] * (1024 - P.TS) / (4 * (nl - 1)) << 16);
  }
  return T;
}
constexpr int step_range(const Prm& P, bool mx_) {
  int mx = 0, mn = 1 << 20;
  const Tab T = make_tab(P);
  for (int bid = 0; bid < 256; ++bid) { const int n = T.e[2 * bid] >> 7 & 63; mx = n > mx ? n : mx; mn = n < mn ? n : mn; }
  return mx_ ? mx : mn;
}
extern "C" __attribute__((used, visibility("protected"))) __constant__ const Tab wg_tab = make_tab(PRM0);
static_assert(step_range(PRM0, true) <= NL && step_range(PRM0, false) >= NCMIN / 8, "a wave's key range does not fit its load steps");
static_assert(UPREP + 1 < SK0 && QP + QD + 1 < SK0 && cons_end(SK0 - 1) == SK0 - WV && NL - SK1 == WV, "steps every wave runs");
constexpr int NUP = FF / 32;
constexpr int SPIN_MAX = 1 << 17;

constexpr u32 WS_EPOCH = 0;
constexpr u32 G_TB = 1024;
constexpr u32 G_NT = 2048;
constexpr u32 G_MOD = 4096;
constexpr u32 G_Q = G_MOD + 3072 * 8;
constexpr u32 G_ML = G_Q + 4096 * 8;
constexpr u32 G_U = G_ML + 256 * 16;
constexpr u32 G_P = 0;
constexpr u32 G_XP = G_P + 256 * 256;
constexpr u32 G_OP = G_XP + 8 * 512 * 4;
constexpr u32 G_PB = G_OP + 44 * 512 * 8;
constexpr u32 G_VT = G_U + 2 * (G_PB + 8192);
constexpr u32 WS_END = G_VT + 1024;
__device__ __forceinline__ u32 dup(u32 o) { return G_U + ((o >> 13) << 14) + (o & 8191u); }
constexpr u32 SENT = 0xffffffffu;
constexpr u32 OOB = 0x40000000u;
#define WST 0

constexpr int AUX_NT = 2, AUX_SC1 = 16;

__device__ __forceinline__ float lo_f(u32 v) { return __uint_as_float(v << 16); }
__device__ __forceinline__ float hi_f(u32 v) { return __uint_as_float(v & 0xffff0000u); }
__device__ __forceinline__ float rbf(float x) { return (float)(__bf16)x; }
__device__ __forceinline__ u32 pack_bf(float lo, float hi) {
  bf16x2 r; r[0] = (__bf16)lo; r[1] = (__bf16)hi; return __builtin_bit_cast(u32, r);
}
__device__ __forceinline__ float dot2(u32 k, u32 q, float acc) {
  return __builtin_amdgcn_fdot2_f32_bf16(__builtin_bit_cast(bf16x2, k), __builtin_bit_cast(bf16x2, q), acc, false);
}
template <int CTRL> __device__ __forceinline__ float dpp(float x) {
  return __builtin_bit_cast(float, __builtin_amdgcn_mov_dpp(__builtin_bit_cast(int, x), CTRL, 0xf, 0xf, true));
}
constexpr int XOR1 = 0xB1, XOR2 = 0x4E, XOR7 = 0x141, XOR8 = 0x128;
__device__ __forceinline__ float swap32_add(float a, float b) {
  auto t = __builtin_amdgcn_permlane32_swap(__float_as_uint(a), __float_as_uint(b), false, false);
  return __uint_as_float(t[0]) + __uint_as_float(t[1]);
}
__device__ __forceinline__ float swap16_add(float a, float b) {
  auto t = __builtin_amdgcn_permlane16_swap(__float_as_uint(a), __float_as_uint(b), false, false);
  return __uint_as_float(t[0]) + __uint_as_float(t[1]);
}
__device__ __forceinline__ float wave_sum(float x) {
  x += dpp<XOR1>(x); x += dpp<XOR2>(x); x += dpp<XOR7>(x); x += dpp<XOR8>(x);
  x = swap16_add(x, x);
  return swap32_add(x, x);
}
__device__ __forceinline__ float wave_max(float x) {
  x = fmaxf(x, dpp<XOR1>(x)); x = fmaxf(x, dpp<XOR2>(x)); x = fmaxf(x, dpp<XOR7>(x)); x = fmaxf(x, dpp<XOR8>(x));
  auto s = __builtin_amdgcn_permlane16_swap(__float_as_uint(x), __float_as_uint(x), false, false);
  x = fmaxf(__uint_as_float(s[0]), __uint_as_float(s[1]));
  auto t = __builtin_amdgcn_permlane32_swap(__float_as_uint(x), __float_as_uint(x), false, false);
  return fmaxf(__uint_as_float(t[0]), __uint_as_float(t[1]));
}
template <int CTRL> __device__ __forceinline__ float sel_add(float lo, float hi, bool up) {
  const float t1 = lo + dpp<CTRL>(lo), t2 = hi + dpp<CTRL>(hi);
  return up ? t2 : t1;
}
template <int I> __device__ __forceinline__ float bc16(float x) {
  return __builtin_bit_cast(float, __builtin_amdgcn_update_dpp(0, __builtin_bit_cast(int, x), 0x150 + I, 0xf, 0xf, false));
}
__device__ __forceinline__ float bcast16(float x, int i) {
  switch (i) {
    case 0: return bc16<0>(x); case 1: return bc16<1>(x); case 2: return bc16<2>(x); case 3: return bc16<3>(x);
    case 4: return bc16<4>(x); case 5: return bc16<5>(x); case 6: return bc16<6>(x); case 7: return bc16<7>(x);
    case 8: return bc16<8>(x); case 9: return bc16<9>(x); case 10: return bc16<10>(x); case 11: return bc16<11>(x);
    case 12: return bc16<12>(x); case 13: return bc16<13>(x); case 14: return bc16<14>(x); default: return bc16<15>(x);
  }
}
__device__ __forceinline__ float silu_f(float x) { return x * __builtin_amdgcn_rcpf(1.f + __expf(-x)); }

template <int AUX> __device__ __forceinline__ u32x4 ld16(rsrc_t r, u32 voff, u32 soff) {
  return __builtin_amdgcn_raw_buffer_load_b128(r, voff, soff, AUX);
}
template <int AUX> __device__ __forceinline__ u32x2 ld8(rsrc_t r, u32 voff, u32 soff) {
  return __builtin_amdgcn_raw_buffer_load_b64(r, voff, soff, AUX);
}
__device__ __forceinline__ void st8_sc1(rsrc_t r, u32 voff, u32 v, u32 tag) {
  __builtin_amdgcn_raw_buffer_store_b64(u32x2{v, tag}, r, voff, 0, AUX_SC1);
}
__device__ __forceinline__ void st16_sc1(rsrc_t r, u32 voff, u32 v0, u32 v1, u32 tag) {
  __builtin_amdgcn_raw_buffer_store_b128(u32x4{v0, tag, v1, tag}, r, voff, 0, AUX_SC1);
}
__device__ __forceinline__ rsrc_t mk_rsrc(const void* p, u32 bytes) {
  return __builtin_amdgcn_make_buffer_rsrc(const_cast<void*>(p), 0, bytes, 0x00020000);
}
#define WAIT_VM(n) __builtin_amdgcn_s_waitcnt(((n) & 15) | (((n) >> 4) << 14) | (7 << 4) | (15 << 8))
#define SB() __builtin_amdgcn_sched_barrier(0)
#define CFENCE() asm volatile("" ::: "memory")
__device__ __forceinline__ bool all_ok(bool ok) { return __builtin_amdgcn_ballot_w64(!ok) == 0ull; }

template <int N> __device__ __forceinline__ void gather16(rsrc_t ws, u32 voff, u32 stride, u32 tag, u32x4 (&g)[N]) {
#pragma nounroll
  for (int spin = 0; spin < SPIN_MAX; ++spin) {
    CFENCE();
    bool ok = true;
#pragma unroll
    for (int i = 0; i < N; ++i) g[i] = ld16<AUX_SC1>(ws, voff + i * stride, 0);
#pragma unroll
    for (int i = 0; i < N; ++i) ok &= (g[i][1] == tag) & (g[i][3] == tag);
    if (all_ok(ok)) break;
    __builtin_amdgcn_s_sleep(8);
  }
}
template <int N> __device__ __forceinline__ void gather8(rsrc_t ws, u32 voff, u32 stride, u32 tag, u32 (&g)[N], bool act = true) {
#pragma nounroll
  for (int spin = 0; spin < SPIN_MAX; ++spin) {
    CFENCE();
    bool ok = true;
    u32x2 t[N];
#pragma unroll
    for (int i = 0; i < N; ++i) t[i] = ld8<AUX_SC1>(ws, voff + i * stride, 0);
#pragma unroll
    for (int i = 0; i < N; ++i) { ok &= (t[i][1] == tag); g[i] = t[i][0]; }
    if (all_ok(ok | !act)) break;
    __builtin_amdgcn_s_sleep(8);
  }
}

struct Smem {
  float red[8][128];
  float ml[8][2];
  float r2[64 * 20];
  u16 hb[3 * 1024];
  float stat[8][64];
  float red2[8][128];
  u32 dummy[64];
  u32x2 bc;
  u32x4 zero[4];
  float pl[4][8];
  u32 mflag[8];
  u32x4 wbuf[8][8][64];
  __attribute__((aligned(256))) u16 w2b[32][512];
  u16 actl[2][32];
};

#define STAMP(k) do {} while (0)
#define WSTAMP(k) do {} while (0)
#define FINISH() return
#define PIN(x) asm volatile("" : "+v"(x))
#define LDS_BARRIER() do { __builtin_amdgcn_s_waitcnt(0xc07f); __builtin_amdgcn_s_barrier(); CFENCE(); } while (0)
#define WAVE_SYNC() do { __builtin_amdgcn_fence(__ATOMIC_RELEASE, "wavefront"); __builtin_amdgcn_wave_barrier(); __builtin_amdgcn_fence(__ATOMIC_ACQUIRE, "wavefront"); } while (0)

__device__ __forceinline__ void dma16(rsrc_t r, u32 voff, u32 soff, u32 lds_dst) {
  u32 keep;
  asm volatile("s_mov_b32 %0, m0\n\ts_mov_b32 m0, %4\n\ts_nop 4\n\tbuffer_load_dwordx4 %1, %2, %3 offen nt lds\n\ts_mov_b32 m0, %0"
               : "=&s"(keep) : "v"(voff), "s"(r), "s"(soff), "s"(lds_dst) : "memory");
}
__device__ __forceinline__ void ld16_into(u32x4& d, rsrc_t r, u32 voff, u32 soff) {
  asm volatile("s_nop 4\n\tbuffer_load_dwordx4 %0, %1, %2, %3 offen nt" : "+v"(d) : "v"(voff), "s"(r), "s"(soff) : "memory");
}
__device__ __forceinline__ u32 perm_lo(u32 a, u32 b) { return __builtin_amdgcn_perm(b, a, 0x05040100u); }
__device__ __forceinline__ u32 perm_hi(u32 a, u32 b) { return __builtin_amdgcn_perm(b, a, 0x07060302u); }
__device__ __forceinline__ float rdl(float x, int l) { return __int_as_float(__builtin_amdgcn_readlane(__float_as_int(x), l)); }

typedef __attribute__((ext_vector_type(8))) __bf16 bf16x8;
__device__ __forceinline__ f32x4 mfma16(u32x4 a, u32x4 b, f32x4 c) {
  return __builtin_amdgcn_mfma_f32_16x16x32_bf16(__builtin_bit_cast(bf16x8, a), __builtin_bit_cast(bf16x8, b), c, 0, 0, 0);
}
__device__ __forceinline__ float sum8(const float* p) {
  const f32x4 a = *reinterpret_cast<const f32x4*>(p), b = *reinterpret_cast<const f32x4*>(p + 4);
  return ((a[0] + b[0]) + (a[1] + b[1])) + ((a[2] + b[2]) + (a[3] + b[3]));
}
__device__ __forceinline__ float ln_ri(float var) {
  return __builtin_amdgcn_rcpf(rbf(__builtin_amdgcn_sqrtf(rbf(rbf(fmaxf(var, 0.f)) + 1e-5f))));
}
__device__ __forceinline__ void ln_stats2(const float (&x0)[8], const float (&x1)[8], float& mb0, float& ri0, float& mb1, float& ri1) {
  const float k0 = rdl(x0[0], 0), k1 = rdl(x1[0], 0);
  float s0 = 0.f, s1 = 0.f, q0 = 0.f, q1 = 0.f;
#pragma unroll
  for (int k = 0; k < 8; ++k) {
    const float d0 = x0[k] - k0, d1 = x1[k] - k1;
    s0 += d0; s1 += d1; q0 = fmaf(d0, d0, q0); q1 = fmaf(d1, d1, q1);
  }
  float t = swap16_add(swap32_add(s0, s1), swap32_add(q0, q1));
  t += dpp<XOR1>(t); t += dpp<XOR2>(t); t += dpp<XOR7>(t); t += dpp<XOR8>(t);
  auto u = __builtin_amdgcn_permlane16_swap(__float_as_uint(t), __float_as_uint(t), false, false);
  const float ms = __uint_as_float(u[0]) * (1.f / 512.f), mq = __uint_as_float(u[1]) * (1.f / 512.f);
  const float ri = ln_ri(fmaf(-ms, ms, mq));
  mb0 = rbf(k0 + rdl(ms, 0)); mb1 = rbf(k1 + rdl(ms, 32));
  ri0 = rdl(ri, 0); ri1 = rdl(ri, 32);
}
template <int I> struct IC { static constexpr int v = I; };
template <int I, int N, class F> __device__ __forceinline__ void sfor(F&& f) {
  if constexpr (I < N) { if (f(IC<I>{})) sfor<I + 1, N>(f); }
}
typedef __attribute__((ext_vector_type(4))) short s16x4;
typedef __attribute__((address_space(3))) s16x4* lds_s16x4p;
__device__ __forceinline__ u32x2 tr_read(u32 lds_addr) {
  return __builtin_bit_cast(u32x2, __builtin_amdgcn_ds_read_tr16_b64_v4i16((lds_s16x4p)lds_addr));
}
__device__ __forceinline__ u32 next_tag(u32 e) { return ((e | 0x40004000u) + 1u) & ~0x40004000u; }
__device__ __forceinline__ float u2f(u32 v) { return __uint_as_float(v); }
#define LDSP(T, a) ((__attribute__((address_space(3))) T*)(a))
template <class T> __device__ __forceinline__ u32 lds_addr(T* p) { u32 a = (u32)(uintptr_t)p; PIN(a); return a; }

#define TAB_LOAD "s_add_u32 s30, s28, wg_tab@rel32@lo+4\ns_addc_u32 s31, s29, wg_tab@rel32@hi+12\nglobal_load_dwordx2 %[te], %[va], s[30:31]\n"
#define FC(k) ".Lfc" #k
#define FC_REGS
#define FX(r, bit, k) "s_bitcmp1_b32 %[" #r "], " #bit "\ns_cselect_b32 %[sb], " FC(k) ", 0\ns_xor_b32 %[sa], %[sa], %[sb]\n"
__global__ void __launch_bounds__(512, 2) fused_kernel(const u16* kc_, const u16* vc_, const u16* wo_, const u16* wq_, const u16* wmod_, const u16* w1_, const u16* w3_,
                                                       unsigned char* ws_, const u16* x_, const u16* cnd_, const u16* w2_, u16* out_) {
  __shared__ __attribute__((aligned(256))) Smem sm;
  const u64 a_ka = (u64)__builtin_amdgcn_kernarg_segment_ptr();
  const int bid = blockIdx.x, tid = threadIdx.x;
  u64 wa, t64; u32x2 te; int tid0;
  u32 ar[14];
  u32 tch2, sa_, sb_, va_;
  u32x4 kr[NL], vr[NL];
  asm volatile(".set .Lfc0, %0\n.set .Lfc1, %1\n.set .Lfc2, %2\n.set .Lfc3, %3\n.set .Lfc4, %4\n.set .Lfc5, %5\n.set .Lfc6, %6\n.set .Lfc7, %7\n.set .Lfc8, %8\n.set .Lfc9, %9\n.set .Lfc10, %10\n.set .Lfc11, %11"
    :: "n"(first_col(PRM0, 0)), "n"(first_col(PRM0, 1)), "n"(first_col(PRM0, 2)), "n"(first_col(PRM0, 3)), "n"(first_col(PRM0, 4)), "n"(first_col(PRM0, 5)),
       "n"(first_col(PRM0, 6)), "n"(first_col(PRM0, 7)), "n"(first_col(PRM0, 8)), "n"(first_col(PRM0, 9)), "n"(first_col(PRM0, 10)), "n"(first_col(PRM0, 11)));
  asm volatile(
    "s_load_dwordx16 s[4:19], %[ka], 0x0\ns_load_dwordx8 s[20:27], %[ka], 0x40\n"
    "s_memrealtime %[t]\n"
    "v_lshlrev_b32 %[va], 3, %[bid]\n"
    "s_getpc_b64 s[28:29]\n.Lcpf_first:\n"
    TAB_LOAD
    "s_cmp_gt_u32 %[bid], 15\ns_cbranch_scc1 .Lno_cpf\n"
    "s_mov_b32 %[sa], .Lend_a-.Lcpf_first\ns_max_i32 %[sa], %[sa], .Lend_b-.Lcpf_first\ns_max_i32 %[sa], %[sa], .Lend_c-.Lcpf_first\ns_max_i32 %[sa], %[sa], .Lend_d-.Lcpf_first\ns_max_i32 %[sa], %[sa], .Lend_e-.Lcpf_first\n"
    "v_lshlrev_b32 %[va], 6, %[tid]\nv_min_u32 %[va], %[sa], %[va]\nglobal_load_dword %[pf], %[va], s[28:29]\n"
    ".Lno_cpf:\n"
    "v_readfirstlane_b32 %[t0], %[tid]\nv_and_b32 %[va], 63, %[tid]\n"
    "s_mov_b32 %[sa], " FC(11) "\n"
    FX(bid, 0, 0) FX(bid, 1, 1) FX(bid, 2, 2) FX(bid, 3, 3) FX(bid, 4, 4) FX(bid, 5, 5) FX(bid, 6, 6) FX(bid, 7, 7) FX(t0, 6, 8) FX(t0, 7, 9) FX(t0, 8, 10)
    "v_lshl_add_u32 %[va], %[va], 4, %[sa]\n"
    "s_waitcnt lgkmcnt(0)\nglobal_load_dwordx4 %[k0], %[va], s[4:5] nt\nglobal_load_dwordx4 %[v0], %[va], s[6:7] nt\n"
    "s_bitcmp1_b32 %[t0], 6\ns_cselect_b64 s[14:15], s[16:17], s[14:15]\n"
    "s_bitcmp1_b32 %[bid], 4\ns_cselect_b64 s[8:9], s[10:11], s[8:9]\ns_cselect_b64 s[12:13], s[14:15], s[12:13]\ns_bitcmp1_b32 %[bid], 5\ns_cselect_b64 %[wa], s[12:13], s[8:9]"
    : "=&{s4}"(ar[0]), "=&{s5}"(ar[1]), "=&{s6}"(ar[2]), "=&{s7}"(ar[3]), "=&{s18}"(ar[4]), "=&{s19}"(ar[5]), "=&{s20}"(ar[6]), "=&{s21}"(ar[7]), "=&{s22}"(ar[8]), "=&{s23}"(ar[9]),
      "=&{s24}"(ar[10]), "=&{s25}"(ar[11]), "=&{s26}"(ar[12]), "=&{s27}"(ar[13]), [wa] "=&s"(wa), [t0] "=&s"(tid0), [t] "=&s"(t64), [te] "=&v"(te), [sa] "=&s"(sa_), [sb] "=&s"(sb_),
      [va] "=&v"(va_), [pf] "=&v"(tch2), [k0] "=&v"(kr[0]), [v0] "=&v"(vr[0])
    : [ka] "s"(a_ka), [bid] "s"(bid), [tid] "v"(tid) : "s8", "s9", "s10", "s11", "s12", "s13", "s14", "s15", "s16", "s17", "s28", "s29", "s30", "s31", "scc", "memory" FC_REGS);
  asm volatile("" : "+s"(ar[0]), "+s"(ar[1]), "+s"(ar[2]), "+s"(ar[3]), "+s"(ar[4]), "+s"(ar[5]), "+s"(ar[6]), "+s"(ar[7]), "+s"(ar[8]), "+s"(ar[9]), "+s"(ar[10]), "+s"(ar[11]), "+s"(ar[12]), "+s"(ar[13]));
  asm volatile("" : "+s"(wa), "+s"(t64), "+s"(tid0), "+s"(sa_));
  const u32 tka = (u32)wall_clock64();
  const u32 t0 = tka;
  __builtin_assume(tid >= 0 && tid < 512 && tid0 >= 0 && tid0 < 512 && bid >= 0 && bid < 256);
  constexpr Prm P = PRM0;
  const int TEND = P.TEND;
  const int xcd = bid & 7, part = bid >> 4 & 3;
  const int lane = tid & 63, wave = tid0 >> 6;
  SB();
  const bool p3 = part == 3;
  u32 pitch = (u32)(0x005600c001000020ull >> (part * 16) & 0xffff) * 32u;
  const int sh = p3 ? 2 : 3;
  u32 wv0 = (u32)((p3 ? (wave >> 1) * 128 : wave * 64) + (part == 0 ? xcd * 512 : 0) + (lane >> sh)) * pitch + ((lane & (p3 ? 3 : 7)) ^ (lane >> (p3 ? 5 : 3))) * 16u;
  const u32 wimg = (u32)(uintptr_t)(&sm.wbuf[0][0][0]);
  u32 wsz = (part == 0 ? 4096u : 512u) * pitch, wld = wimg + (p3 ? (wave & 1) * 32768 + (wave >> 1) * 8192 : wave * 8192);
  PIN(wv0);
  asm volatile("" : "+s"(wsz), "+s"(wld), "+s"(pitch));
  auto karg = [&](int i) { return ar[2 * i] | (u64)ar[2 * i + 1] << 32; };
  const u64 a_kc = karg(0), a_vc = karg(1);
  unsigned char* ws = (unsigned char*)karg(2);
  const u16 *x = (const u16*)karg(3), *cnd = (const u16*)karg(4), *w2 = (const u16*)karg(5);
  u16* out = (u16*)karg(6);
  const rsrc_t wsr = mk_rsrc(ws, WS_END);
  asm volatile("s_waitcnt vmcnt(2)" : "+v"(te));
  const u32 te0 = __builtin_amdgcn_readfirstlane(te[0]), te1 = __builtin_amdgcn_readfirstlane(te[1]);
  const int bl0 = te0 & 127, nl = te0 >> 7 & 63, blk = te0 >> 13 & 63;
  const bool has_w = te0 >> 19 & 1;
  const rsrc_t rw = mk_rsrc((const void*)wa, wsz);
  const u32 wstep = pitch << (6 - sh), wflip = p3 ? 32u : 0u;
  u32 wso = blk << (sh + 4);
  int wleft = 8;
  auto piece = [&]() {
    dma16(rw, wv0, wso, wld);
    wso += wstep; wld += 1024;
    wv0 ^= (wleft & 1) ? wflip : 0u;
    --wleft;
  };
  SB();
  STAMP(23);
  const int b = bid >> 3 & 1, head = xcd * 4 + (bid >> 6 ^ 3);
  const int g = lane >> 4, c = lane & 15;
  const int i8 = b | bid >> 6 << 1, wi = i8 * 8 + xcd;
  const bool is_up = p3 && has_w, modc = part == 2 && wi < 16;
  const size_t base = ((size_t)(b * NH + head) * SK + (SK - WIN)) * DH;
  const rsrc_t rk = mk_rsrc((const u16*)a_kc + base, WIN * DH * 2), rv = mk_rsrc((const u16*)a_vc + base, WIN * DH * 2);
  int tau = te1 & 0xffff;
  const int tauw = te0 >> 24 << 7, wdef = te0 >> 20 & 15;
  int due = P.NF * 256, now = 0;
  auto gate = [&]() {
    while (now < due) { now = (int)(((u32)wall_clock64() - t0) << 8); if (now < due) __builtin_amdgcn_s_sleep(1); }
    due += tau;
  };
  auto until = [&](int t) { while ((int)(((u32)wall_clock64() - t0) << 8) < t); };
  const u32 pslot = dup(G_P + ((b * 32 + head) * 4 + part) * 256);
  const u32 die = __builtin_amdgcn_s_getreg(20 | (2 << 6));
  const bool voter = p3 && i8 == 1, reporter = p3 && i8 < 2;
  int vote = 0, lag = 0;
  const u32 pbo = dup(G_PB) + (xcd * 8 + wave) * 128;
  u32 near = 0;
  u32x2 ep; u32 xs0, xs1, tch1; u32x4 xr0, xr1;
  const rsrc_t rx = mk_rsrc(part == 2 ? cnd : x, (part == 1 || (part == 2 && has_w)) ? 2048u : 0u);
  auto small_ops = [&]() {
    ep = ld8<AUX_SC1>(wsr, wave == 7 ? WS_EPOCH : OOB, 0);
    xs0 = __builtin_amdgcn_raw_buffer_load_b16(rx, tid * 2, 0, 0); xs1 = __builtin_amdgcn_raw_buffer_load_b16(rx, tid * 2, 1024, 0);
    xr0 = ld16<0>(rx, part == 1 ? lane * 16 : OOB, 0); xr1 = ld16<0>(rx, part == 1 ? lane * 16 : OOB, 1024);
    tch1 = __builtin_amdgcn_raw_buffer_load_b32(mk_rsrc(w2 + (size_t)blk * 32 * 512, is_up ? 64u : 0u), tid == 384 ? 0 : OOB, 0, AUX_NT);
  };
  if (has_w) {
    if (modc) now = 1 << 30;
#pragma nounroll
    while (wleft > wdef) { gate(); due += tauw - tau; piece(); }
    now = 0;
  }
  due = max(due, P.TK * 256);
  SB();
  u32 sot = sa_ & 0xffc00u;
  {
    const u32 s1 = (bl0 + lane - 1 - (lane >= SK0 ? NL - nl : 0)) ^ (124 + part);
#pragma unroll
    for (int k = 0; k < 7; ++k) sot ^= (u32)-(int)(s1 >> k & 1u) & col(P, k);
    PIN(sot);
  }
  u32 tag = 0;
  u32x4 bf[8];
  auto wo_prep = [&](int s0 = 0, int s1 = 2) {
    const u32 wi_ = wimg + wave * 8192;
    const int q = (lane >> 2) & 3, pp = lane & 3;
#pragma unroll
    for (int s = s0; s < s1; ++s) {
#pragma unroll
      for (int t = 0; t < 4; ++t) {
        const int r0 = s * 32 + g * 8 + q, r1 = r0 + 4, ch = t * 2 + (pp >> 1);
        const u32x2 lo = tr_read(wi_ + r0 * 128 + ((ch ^ (r0 & 7)) * 16) + (pp & 1) * 8), hi = tr_read(wi_ + r1 * 128 + ((ch ^ (r1 & 7)) * 16) + (pp & 1) * 8);
        bf[s * 4 + t] = u32x4{lo[0], lo[1], hi[0], hi[1]};
      }
    }
  };
  auto gemv_go = [&](u32 hh) {
    sm.hb[tid] = (u16)hh; sm.hb[512 + tid] = (u16)(hh >> 16);
    WAVE_SYNC();
    const u32x4* a = (c & 11) == 0 ? reinterpret_cast<const u32x4*>(&sm.hb[(c >> 2) * 512 + wave * 64 + g * 8]) : &sm.zero[0];
    const u32x4 a0 = a[0], a1 = a[(c & 11) == 0 ? 4 : 0];
    const f32x4 z = {0.f, 0.f, 0.f, 0.f};
    f32x4 d[4];
#pragma unroll
    for (int t = 0; t < 4; ++t) d[t] = mfma16(a0, bf[t], z);
#pragma unroll
    for (int t = 0; t < 4; ++t) d[t] = mfma16(a1, bf[4 + t], d[t]);
    if (lane < 32) {
#pragma unroll
      for (int t = 0; t < 4; ++t) sm.r2[(t * 16 + c) * 20 + g * 8 + wave] = d[t][0];
    }
  };
  auto mod_stage = [&]() {
    wo_prep();
    gemv_go(pack_bf(silu_f(lo_f(xs0)), silu_f(lo_f(xs1))));
    __builtin_amdgcn_s_waitcnt(0xc07f);
    if (lane == 0) *LDSP(volatile u32, (u32)(uintptr_t)&sm.mflag[wave]) = 1u;
    if (wave == 7) {
#pragma nounroll
      for (int spin = 0; spin < SPIN_MAX; ++spin) {
        const u32 f = *LDSP(volatile u32, (u32)(uintptr_t)&sm.mflag[lane & 7]);
        if (all_ok(f != 0u)) break;
        __builtin_amdgcn_s_sleep(1);
      }
      CFENCE();
      st8_sc1(wsr, G_MOD + (blk * 64 + lane) * 8, pack_bf(sum8(&sm.r2[lane * 20]), sum8(&sm.r2[lane * 20 + 8])), tag);
    }
    STAMP(9);
  };
  if (lane < 4) sm.zero[lane] = u32x4{0u, 0u, 0u, 0u};
  if (part == 2 && has_w) {
    if (tid < 8) sm.mflag[tid] = 0u;
    LDS_BARRIER();
    if (__builtin_expect(modc, 0)) {
      small_ops();
      __builtin_amdgcn_raw_buffer_store_b128(u32x4{SENT, SENT, SENT, SENT}, wsr, G_U + wi * 10240 + tid * 16, 0, AUX_SC1);
      __builtin_amdgcn_raw_buffer_store_b128(u32x4{SENT, SENT, SENT, SENT}, wsr, tid < 128 ? G_U + wi * 10240 + tid * 16 : OOB, 8192, AUX_SC1);
      asm volatile("s_waitcnt vmcnt(0)" ::: "memory");
      tag = next_tag(__builtin_amdgcn_readfirstlane(ep[0]));
      mod_stage();
      __builtin_amdgcn_s_barrier();
    }
  }
  SB();
  const int fd = wi * 8 + (lane & 7);
  const int ub = wave >> 2, ud = (wave & 3) * 128 + c * 8 + g * 2;
  u32x4 sg[3]; u32 sx2;
  auto side_loads = [&]() {
    const bool fin = part == 1 && wave == 0;
    sg[0] = ld16<AUX_SC1>(wsr, is_up ? G_MOD + (1024 + ud) * 8 : (fin ? G_MOD + (1024 + fd) * 8 : OOB), 0);
    sg[1] = ld16<AUX_SC1>(wsr, is_up ? G_MOD + (1536 + ud) * 8 : (fin ? G_MOD + (2560 + fd) * 8 : OOB), 0);
    sg[2] = ld16<AUX_SC1>(wsr, is_up ? G_MOD + (2048 + ud) * 8 : OOB, 0);
    sx2 = __builtin_amdgcn_raw_buffer_load_b32(mk_rsrc(x, (is_up || fin) ? 2048u : 0u), is_up ? ub * 1024 + ud * 2 : (g & 1) * 1024 + (fd >> 1) * 4, 0, 0);
  };
  auto side_pin = [&]() { PIN(sg[0]); PIN(sg[1]); PIN(sg[2]); PIN(sx2); };
  const u32 mlo = G_ML + (head * 2 + b) * 64 + (lane & 3) * 16;
  float fmrg = 0.f;
  auto merge_factor = [&](const u32x4& ml) {
    const float mi = u2f(ml[0]);
    float M = fmaxf(mi, dpp<XOR1>(mi)); M = fmaxf(M, dpp<XOR2>(M));
    const float e = __expf(mi - M);
    float L = e * u2f(ml[2]);
    L += dpp<XOR1>(L); L += dpp<XOR2>(L);
    fmrg = rdl(e * __builtin_amdgcn_rcpf(L), part);
  };
  f32x2 ga = {0.f, 0.f}, xin = {0.f, 0.f}, cc1 = {0.f, 0.f};
  float Af = 0.f, Bf = 0.f;
  const int hpos = ub * 512 + ud;
  auto up_prep = [&]() {
    if (!all_ok((sg[0][1] == tag) & (sg[0][3] == tag) & (sg[1][1] == tag) & (sg[1][3] == tag) & (sg[2][1] == tag) & (sg[2][3] == tag))) gather16<3>(wsr, G_MOD + (1024 + ud) * 8, 512 * 8, tag, sg);
    ga = f32x2{ub ? hi_f(sg[0][0]) : lo_f(sg[0][0]), ub ? hi_f(sg[0][2]) : lo_f(sg[0][2])};
    cc1 = f32x2{rbf(1.f + (ub ? hi_f(sg[2][0]) : lo_f(sg[2][0]))), rbf(1.f + (ub ? hi_f(sg[2][2]) : lo_f(sg[2][2])))};
    xin = f32x2{lo_f(sx2), hi_f(sx2)};
    u32* hp = reinterpret_cast<u32*>(&sm.hb[hpos]);
    hp[512] = pack_bf(cc1[0], cc1[1]);
    hp[1024] = ub ? perm_hi(sg[1][0], sg[1][2]) : perm_lo(sg[1][0], sg[1][2]);
  };
  auto up_frags = [&]() {
    const int pp = lane & 3;
    const u32 a = wimg + (pp & 1) * 32768 + ((pp >> 1) * 32 + g * 8 + ((lane >> 2) & 3)) * 64 + (((wave >> 1) ^ ((g & 1) | (pp & 2))) * 16) + (wave & 1) * 8;
#pragma unroll
    for (int rb = 0; rb < 8; ++rb) {
      const u32x2 lo = tr_read(a + rb * 4096), hi = tr_read(a + rb * 4096 + 256);
      bf[rb] = u32x4{lo[0], lo[1], hi[0], hi[1]};
    }
  };
  const u32x4* ap = reinterpret_cast<const u32x4*>(&sm.hb[((lane >> 3) & 1) * 512 + (lane & 1) * 32 + g * 8]);
  const bool par = lane & 8;
  auto blog = [&](int) {};
  auto limit = [&](auto sc) {
    constexpr int s = sc.v;
    WAIT_VM(window(s, 1 + (PRM0.W[s >> 3] >> (4 * (s & 7)) & 7)));
    SB();
  };
  float l0 = 0.f, l1 = 0.f;
  u32x2 gmd[2], tbg;
  int t1 = 0;
#pragma unroll
  for (int i = SK0; i < SK1; ++i) kr[i] = u32x4{0u, 0u, 0u, 0u};
  const int first = SK0 + NL - nl;
  auto left_out = [&](int i) { int f = first; asm volatile("" : "+s"(f)); return i < f; };
  sfor<1, NL>([&](auto ic) {
    constexpr int i = ic.v;
    if constexpr (i >= SK0 && i < SK1) { if (left_out(i)) return true; }
    const u32 so = __builtin_amdgcn_readlane(sot, i);
    gate();
    blog(i);
    if constexpr (i >= RP0 && i < NL - 1) { if (reporter) lag += (int)(((u32)wall_clock64() - t0) << 8) - due + ((int)__builtin_amdgcn_s_getreg(7 | (3 << 11)) + 1) * tau; }
    SB();
    if constexpr (i >= SK0 && i < SK1) ld16_into(kr[i], rk, lane * 16, so); else kr[i] = ld16<AUX_NT>(rk, lane * 16, so);
    if constexpr (i == SMO) small_ops();
    if constexpr (i == SMO + 1) { if (voter) __builtin_amdgcn_raw_buffer_store_b32(0u, wsr, pbo + (lane >> 5) * 8192 + (lane & 31) * 4, 0, AUX_SC1); }
    if constexpr (i == TBL) tbg = ld8<AUX_SC1>(wsr, wave == 7 ? G_TB + xcd * 128 + (lane & 7) * 8 : OOB, 0);
    if constexpr (i == QP) { gmd[0] = ld8<AUX_SC1>(wsr, part == 1 ? G_MOD + tid * 8 : OOB, 0); gmd[1] = ld8<AUX_SC1>(wsr, part == 1 ? G_MOD + tid * 8 : OOB, 4096); }
    SB();
    SB();
    if (i >= 4) limit(ic);
    if (i == 1) STAMP(25);
    if (i == 8) STAMP(12);
    if (i == 20) STAMP(21);
    if (i == 32) STAMP(22);
    if (i == TBP) {
      if (part == 3 && i8 == 0 && wave == 7) { tag = next_tag(__builtin_amdgcn_readfirstlane(ep[0])); if (lane < 8) st8_sc1(wsr, G_TB + lane * 128 + xcd * 8, t0 - P.GS, tag); }
      SB();
    }
    if (i == PRB) {
      if (voter) {
        u64 ta, tb, tc; u32 dm;
        const u32 pa = pbo + (wave & 1) * 8192;
        asm volatile("s_memtime %0\n\ts_waitcnt lgkmcnt(0)\n\ts_load_dword %3, %4, %5\n\ts_waitcnt lgkmcnt(0)\n\ts_memtime %1\n\ts_waitcnt lgkmcnt(0)\n\t"
                     "s_load_dword %3, %4, %6\n\ts_waitcnt lgkmcnt(0)\n\ts_memtime %2\n\ts_waitcnt lgkmcnt(0)"
                     : "=&s"(ta), "=&s"(tb), "=&s"(tc), "=&s"(dm) : "s"(ws), "s"(pa), "s"(pa ^ 8192u));
        const int d = min(max((int)(2 * (u32)tb - (u32)ta - (u32)tc), -500), 500);
        vote = ((wave ^ die) & 1) ? -d : d;
        STAMP(26);
      }
      SB();
    }
    if (i == TBU) {
      if (wave == 7) {
        const bool ok = tbg[1] == tag;
        float lf = ok ? (float)min(max((int)(t0 - tbg[0]), -300), 300) : 0.f, nf = ok ? 1.f : 0.f;
        lf += dpp<XOR1>(lf); nf += dpp<XOR1>(nf); lf += dpp<XOR2>(lf); nf += dpp<XOR2>(nf); lf += dpp<XOR7>(lf); nf += dpp<XOR7>(nf);
        if (lane == 0) sm.bc = u32x2{tag, t0 - (u32)(int)(lf * __builtin_amdgcn_rcpf(fmaxf(nf, 1.f)))};
      }
      LDS_BARRIER();
      const u32x2 bc = sm.bc;
      tag = __builtin_amdgcn_readfirstlane(bc[0]);
      t1 = (__builtin_amdgcn_readfirstlane((int)(bc[1] - t0)) + TEND + P.X1) * 256;
      tau -= __builtin_amdgcn_readfirstlane((int)((float)(int)(t0 - bc[1]) * 256.f * __builtin_amdgcn_rcpf((float)(nl - TBU - 1))));
      SB();
    }
    if (i == NL - 1) {
      if (reporter) {
        if (lane == 0) sm.mflag[wave] = vote * 65536 + min(lag >> 8, 4000);
        LDS_BARRIER();
        if (wave == 7) {
          int s = sm.mflag[lane & 7];
          s += __builtin_amdgcn_mov_dpp(s, XOR1, 0xf, 0xf, true); s += __builtin_amdgcn_mov_dpp(s, XOR2, 0xf, 0xf, true); s += __builtin_amdgcn_mov_dpp(s, XOR7, 0xf, 0xf, true);
          if (lane < 8) st8_sc1(wsr, G_VT + lane * 128 + wi * 8, s, tag);
        }
      }
      SB();
    }
    if (i == LNS) {
      tag = next_tag(__builtin_amdgcn_readfirstlane(ep[0]));
      if (part == 1) {
        float x0[8], x1[8], mb0, ri0, mb1, ri1;
#pragma unroll
        for (int k = 0; k < 4; ++k) { x0[2 * k] = lo_f(xr0[k]); x0[2 * k + 1] = hi_f(xr0[k]); x1[2 * k] = lo_f(xr1[k]); x1[2 * k + 1] = hi_f(xr1[k]); }
        ln_stats2(x0, x1, mb0, ri0, mb1, ri1);
        l0 = rbf(rbf(lo_f(xs0) - mb0) * ri0); l1 = rbf(rbf(lo_f(xs1) - mb1) * ri1);
      }
      PIN(l0); PIN(l1);
      SB();
    }
    if (i == WUSE) {
      PIN(tch1); PIN(tch2);
      if ((part == 1 || (part == 2 && has_w && !modc))) {
        WAIT_VM(WK); CFENCE();
        if (part == 2) mod_stage(); else wo_prep();
      }
#pragma unroll
      for (int k = 0; k < 8; ++k) PIN(bf[k]);
      SB();
    }
    if (i == QP + QD) {
      if (part == 1) {
        u32 gm[2] = {gmd[0][0], gmd[1][0]};
        if (!all_ok((gmd[0][1] == tag) & (gmd[1][1] == tag))) { STAMP(11); gather8<2>(wsr, G_MOD + tid * 8, 512 * 8, tag, gm); }
        STAMP(5);
        gemv_go(pack_bf(rbf(l0 * rbf(1.f + lo_f(gm[1]))) + lo_f(gm[0]), rbf(l1 * rbf(1.f + hi_f(gm[1]))) + hi_f(gm[0])));
      }
      SB();
    }
    if (i == QP + QD + 1) {
      if (part == 1) {
        LDS_BARRIER();
        if (wave == 0) {
          const int bb = lane >> 5, jq = lane & 31;
          st8_sc1(wsr, G_Q + (bb * 2048 + blk * 32 + jq) * 8, pack_bf(sum8(&sm.r2[2 * jq * 20 + bb * 8]), sum8(&sm.r2[(2 * jq + 1) * 20 + bb * 8])), tag);
        }
        STAMP(6);
      }
      SB();
    }
    return true;
  });
  PIN(sot);
  tau = te1 >> 16;
  due += P.TG * 256;
  const u32 qoff = G_Q + ((b * 32 + head) * 64 + c * 4) * 8;
  u32x4 qg[2], mlg;
  u32 qq[4];
  constexpr int NB = (NL + 15) / 16;
  float xlo, sc[NB], pr[NB], m = 0.f;
  f32x4 acc = {0.f, 0.f, 0.f, 0.f};
  u32 pb[NB], pm[4], pl8[8], cpf;
  const u32 adst0 = lds_addr(lane < 32 ? &sm.red[wave][c * 8 + g * 4] : &sm.r2[(lane - 32) * 4]);
  u32 po, rdst, rsrc, xo;
  auto wo_addr = [&]() {
    po = dup(G_P + (((c >> 2 & 1) * 32 + xcd * 4 + (wave >> 1)) * 4 + (c & 3)) * 256 + (wave & 1) * 128 + (c >> 3) * 64 + g * 16) + ((P.NR & 2) ? near : (xcd & 2) * 4096);
    rdst = lane < 32 ? (u32)(uintptr_t)&sm.red2[wave][0] + lane * 16 : (u32)(uintptr_t)&sm.r2[0] + (lane - 32) * 16;
    rsrc = (u32)(uintptr_t)&sm.red2[0][((wave & 1) * 16 + (lane & 15)) * 4 + (lane >> 4)];
    xo = dup(G_XP + xcd * 2048 + (wave & 1) * 1024 + (blk * 64 + (lane & ~1)) * 2) + (lane & 1) * 8192;
    PIN(po); PIN(rdst); PIN(rsrc); PIN(xo);
  };
  u32 pso = wave == 0 ? pslot + c * 16 + (g & 1) * 8 + (g >> 1) * 8192 : OOB;
  PIN(pso);
  const u32 nadd = wdef ? P.LW : (is_up ? P.LU : 0);
  u32 nto = tid == 0 ? G_NT + bid * 8 : OOB;
  PIN(nto);
  auto notices = [&](auto nc_, u32 off, u32x4 (&gn)[nc_.v]) -> int {
    constexpr int N = nc_.v;
#pragma nounroll
    for (int spin = 0; spin < SPIN_MAX; ++spin) {
      bool ok = true;
#pragma unroll
      for (int i = 0; i < N; ++i) ok &= (gn[i][1] == tag) & (gn[i][3] == tag);
      if (all_ok(ok)) break;
      __builtin_amdgcn_s_sleep(10);
      CFENCE();
#pragma unroll
      for (int i = 0; i < N; ++i) gn[i] = ld16<AUX_SC1>(wsr, off, i * 1024);
    }
    int mx = 0;
#pragma unroll
    for (int i = 0; i < N; ++i) mx = max(mx, max((int)(gn[i][0] - t0), (int)(gn[i][2] - t0)));
    return __builtin_amdgcn_readfirstlane((int)wave_max((float)min(mx, 1 << 20)));
  };
  auto pv_row = [&](auto jc) {
    constexpr int i = jc.v;
    const u32 pv = __float_as_uint(bcast16(u2f(pb[i >> 4]), i & 15));
    acc = mfma16(u32x4{pv & pm[0], pv & pm[1], pv & pm[2], pv & pm[3]}, vr[i], acc);
    PIN(acc);
    return true;
  };
  sfor<0, NL - 1>([&](auto ic) {
    constexpr int v = ic.v;
    if constexpr (v >= SK0 && v < SK1) { if (left_out(v)) return true; }
    if constexpr (v > 0) {
      const u32 so = __builtin_amdgcn_readlane(sot, v);
      if constexpr (v >= SK1) { if (v == NL - P.NE) tau = (tau * P.FE) >> 4; }
      gate();
      blog(NL + v - 1);
      SB();
      if constexpr (v >= SK0 && v < SK1) ld16_into(vr[v], rv, lane * 16, so); else vr[v] = ld16<AUX_NT>(rv, lane * 16, so);
      SB();
      SB();
    }
    if constexpr (v == SK0 - 1) {
#pragma unroll
      for (int i = SK0; i < SK1; ++i) { vr[i] = u32x4{0u, 0u, 0u, 0u}; PIN(vr[i]); }
    }
    if constexpr (v == VQ - 4) { qg[0] = ld16<AUX_SC1>(wsr, qoff, 0); qg[1] = ld16<AUX_SC1>(wsr, qoff + 16, 0); }
    if constexpr (v == VSIDE) { side_loads(); tbg = ld8<AUX_SC1>(wsr, wave == 7 ? G_VT + xcd * 128 + c * 8 : OOB, 0); }
    if constexpr (v == CPF) {
      u64 pc; u32 dlt, len;
      asm volatile("s_getpc_b64 %0\n.Lcpf_here:\n\ts_mov_b32 %1, .Ltail_start-.Lcpf_here\n\ts_mov_b32 %2, .Lend_a-.Ltail_start\n\ts_max_i32 %2, %2, .Lend_b-.Ltail_start\n\ts_max_i32 %2, %2, .Lend_c-.Ltail_start\n\ts_max_i32 %2, %2, .Lend_d-.Ltail_start\n\ts_max_i32 %2, %2, .Lend_e-.Ltail_start" : "=s"(pc), "=s"(dlt), "=s"(len) :: "scc");
      pc += dlt;
      const rsrc_t rc = mk_rsrc((const void*)pc, (part == 3 && i8 == 1) ? len : 0u);
      cpf = __builtin_amdgcn_raw_buffer_load_b32(rc, tid * 64, 0, 0);
    }
    if constexpr (v == SK1 + 2) PIN(cpf);
    if constexpr (v == SK1) mlg = ld16<AUX_SC1>(wsr, wave == 0 ? mlo : OOB, 0);
    SB();
    if constexpr (NL + v < 72) limit(IC<NL + v>{});
    if constexpr (v >= SK0 + WV) { WAIT_VM(v < SK1 ? WV : WV + 1); SB(); }
    if constexpr (v == VQ) {
      if (!all_ok((qg[0][1] == tag) & (qg[0][3] == tag) & (qg[1][1] == tag) & (qg[1][3] == tag))) { STAMP(20); gather16<2>(wsr, qoff, 16, tag, qg); }
      STAMP(1); WSTAMP(1);
      qq[0] = qg[0][0]; qq[1] = qg[0][2]; qq[2] = qg[1][0]; qq[3] = qg[1][2];
    }
    if constexpr (v >= VQ && v < VS) {
      constexpr int sl = v - VQ, bt = sl >> 1;
      float xs[8];
#pragma unroll
      for (int i = 0; i < 8; ++i) {
        float d = 0.f;
        if (sl * 8 + i < NL) {
#pragma unroll
          for (int k = 0; k < 4; ++k) d = dot2(kr[sl * 8 + i][k], qq[k], d);
        }
        xs[i] = d;
      }
#pragma unroll
      for (int h = 0; h < 4; ++h) xs[h] = sel_add<XOR7>(xs[h], xs[h + 4], c & 4);
#pragma unroll
      for (int h = 0; h < 2; ++h) xs[h] = sel_add<XOR2>(xs[h], xs[h + 2], c & 2);
      xs[0] = sel_add<XOR1>(xs[0], xs[1], c & 1);
      if constexpr ((sl & 1) || sl == NSL - 1) {
        const float t = (sl & 1) ? sel_add<XOR8>(xlo, xs[0], c & 8) : xs[0] + dpp<XOR8>(xs[0]);
        const float s = rbf(rbf(t) * 0.08838834764831845f);
        sc[bt] = (bt * 16 + c < SK0 || (bt * 16 + c >= first && bt * 16 + c < NL)) ? s : -__builtin_inff();
        PIN(sc[bt]);
      } else {
        xlo = xs[0];
        PIN(xlo);
      }
    }
    if constexpr (v == VS) {
      float mloc = sc[0];
#pragma unroll
      for (int bt = 1; bt < NB; ++bt) mloc = fmaxf(mloc, sc[bt]);
      m = fmaxf(wave_max(mloc), -3.0e38f);
      float ls = 0.f;
#pragma unroll
      for (int bt = 0; bt < NB; ++bt) { pr[bt] = __expf(sc[bt] - m); ls += pr[bt]; }
      const float l = wave_sum(ls);
      if (lane == 0) { sm.ml[wave][0] = m; sm.ml[wave][1] = l; }
      if (c == ((NL - 1) & 15)) sm.pl[g][wave] = pr[NB - 1];
      STAMP(2); WSTAMP(2);
    }
    if constexpr (v >= SK1) pv_row(IC<v - SK1 + SK0 - WV>{});
    if constexpr (v < SK0 || v >= SK0 + WV) sfor<cons_end(v - 1), cons_end(v)>(pv_row);
    if constexpr (v == SK1 + 1) {
      float p[8];
#pragma unroll
      for (int w = 0; w < 8; ++w) p[w] = sm.pl[g][w] * __expf(sm.ml[w][0] - m);
#pragma unroll
      for (int w = 0; w < 8; ++w) pl8[w] = pack_bf(p[w], p[w]);
#pragma unroll
      for (int w = 0; w < 8; ++w) PIN(pl8[w]);
    }
    if constexpr (v == MLS - 1) { side_pin(); if (is_up) up_prep(); PIN(ga); PIN(xin); PIN(cc1); }
    if constexpr (v == UPREP - 1) wo_addr();
    if constexpr (v == UPREP) {
#pragma unroll
      for (int k = 0; k < 8; ++k) asm volatile("" : "=v"(bf[k]));
      if (part == 0) wo_prep(0, 1); else if (is_up) up_frags();
#pragma unroll
      for (int k = 0; k < 8; ++k) PIN(bf[k]);
    }
    if constexpr (v == UPREP + 1) {
      if (is_up) {
        const u32x4* a1 = ap + 128 + ((lane >> 1) & 1) * 128;
        f32x4 d0 = {0.f, 0.f, 0.f, 0.f}, d1 = {0.f, 0.f, 0.f, 0.f};
#pragma unroll
        for (int rb = 0; rb < 4; ++rb) { d0 = mfma16(a1[rb * 8], bf[rb], d0); d1 = mfma16(a1[(rb + 4) * 8], bf[rb + 4], d1); }
        Af = par ? d0[1] + d1[1] : d0[0] + d1[0]; Bf = par ? d0[3] + d1[3] : d0[2] + d1[2];
        Af += dpp<XOR8>(Af); Bf += dpp<XOR8>(Bf);
      }
      PIN(Af); PIN(Bf);
    }
    if (v == 10 || v == 16) STAMP(15 + v / 6);
    if (v == 20) STAMP(18);
    if (v == 32) STAMP(19);
    if constexpr (v == MLS) {
      if (wave == 7) {
        const bool ok = tbg[1] == tag;
        float vt = ok ? (float)((int)tbg[0] >> 16) : 0.f, lg = ok ? (float)(tbg[0] & 0xffffu) : 0.f, nf = ok ? 8.f * (NL - 1 - RP0) : 0.f;
        vt += dpp<XOR1>(vt); lg += dpp<XOR1>(lg); nf += dpp<XOR1>(nf); vt += dpp<XOR2>(vt); lg += dpp<XOR2>(lg); nf += dpp<XOR2>(nf);
        vt += dpp<XOR7>(vt); lg += dpp<XOR7>(lg); nf += dpp<XOR7>(nf); vt += dpp<XOR8>(vt); lg += dpp<XOR8>(lg); nf += dpp<XOR8>(nf);
        lg *= __builtin_amdgcn_rcpf(fmaxf(nf, 1.f));
        if (lane == 0) sm.bc = u32x2{__float_as_uint(vt), (u32)min(max((int)((lg - (float)P.A0) * (float)(P.AG * 32)), 0), P.AC * 256)};
      }
      LDS_BARRIER();
      {
        const u32x2 bc = sm.bc;
        near = ((__builtin_amdgcn_readfirstlane(bc[0]) >> 31 ^ die ^ P.NR ^ 1) & 1) * 8192u;
        t1 += __builtin_amdgcn_readfirstlane(bc[1]);
        tau += __builtin_amdgcn_readfirstlane((int)((float)(int)bc[1] * __builtin_amdgcn_rcpf((float)(nl - MLS - 3))));
      }
      float mm[8], ll[8];
#pragma unroll
      for (int w = 0; w < 8; ++w) { const f32x2 t = *reinterpret_cast<const f32x2*>(&sm.ml[w][0]); mm[w] = t[0]; ll[w] = t[1]; }
      float Mwg = mm[0], Lwg = 0.f;
#pragma unroll
      for (int w = 1; w < 8; ++w) Mwg = fmaxf(Mwg, mm[w]);
      if (wave == 0) {
#pragma unroll
        for (int w = 0; w < 8; ++w) Lwg = fmaf(__expf(mm[w] - Mwg), ll[w], Lwg);
        if (lane == 0) st16_sc1(wsr, G_ML + ((head * 2 + b) * 4 + part) * 16, __float_as_uint(Mwg), __float_as_uint(Lwg), tag);
      }
      const float fw = __expf(m - Mwg);
      m = Mwg;
#pragma unroll
      for (int bt = 0; bt < NB; ++bt) { pb[bt] = pack_bf(pr[bt] * fw, pr[bt] * fw); PIN(pb[bt]); }
#pragma unroll
      for (int k = 0; k < 4; ++k) { pm[k] = (c >> 1 & 3) == k ? ((c & 1) ? 0xffff0000u : 0x0000ffffu) : 0u; PIN(pm[k]); }
    }
    SB();
    return true;
  });
  asm volatile(".Ltail_start:");
  if (wave != 0) {
    pv_row(IC<SK0 - 1>{});
    WAIT_VM(WV); SB();
    sfor<NL - 1 - WV, NL - 1>(pv_row);
    *LDSP(f32x4, adst0) = acc;
    WSTAMP(14);
    LDS_BARRIER();
  } else {
    u32x4 xr[8], am[8];
#pragma unroll
    for (int w = 0; w < 8; ++w) am[w] = u32x4{pl8[w] & pm[0], pl8[w] & pm[1], pl8[w] & pm[2], pl8[w] & pm[3]};
#pragma unroll
    for (int w = 0; w < 8; ++w) PIN(am[w]);
    {
      const u32 so = __builtin_amdgcn_readlane(sot, NL - 1);
      gate();
      SB();
#pragma unroll
      for (int w = 0; w < 8; ++w) xr[w] = ld16<AUX_NT>(rv, lane * 16, so ^ ((w & 1) ? col(P, 7) : 0u) ^ ((w & 2) ? col(P, 8) : 0u) ^ ((w & 4) ? col(P, 9) : 0u));
      SB();
    }
    u32 rsum = lds_addr(&sm.red[0][c * 8 + (g & 1) * 4]);
    pv_row(IC<SK0 - 1>{});
    WAIT_VM(WV + 8); SB();
    sfor<NL - 1 - WV, NL - 3>(pv_row);
    if (__builtin_expect(!all_ok((mlg[1] == tag) & (mlg[3] == tag)), 0)) {
#pragma nounroll
      for (int spin = 0; spin < SPIN_MAX; ++spin) {
        CFENCE();
        mlg = ld16<AUX_SC1>(wsr, mlo, 0);
        if (all_ok((mlg[1] == tag) & (mlg[3] == tag))) break;
      }
    }
    merge_factor(mlg);
    sfor<NL - 3, NL - 1>(pv_row);
    STAMP(14); WSTAMP(14);
    LDS_BARRIER();
    STAMP(15);
    f32x4 t[8];
#pragma unroll
    for (int w = 1; w < 8; ++w) t[w] = LDSP(f32x4, rsum)[w * 32];
    acc = ((acc + t[1]) + (t[2] + t[3])) + ((t[4] + t[5]) + (t[6] + t[7]));
    PIN(acc);
    STAMP(3);
    SB();
#pragma unroll
    for (int w = 0; w < 8; ++w) acc = mfma16(am[w], xr[w], acc);
  }
  WAIT_VM(0);
  acc *= fmrg;
  __builtin_amdgcn_raw_buffer_store_b64(u32x2{min(pack_bf(acc[0], acc[1]), SENT - 1u), min(pack_bf(acc[2], acc[3]), SENT - 1u)}, wsr, pso, 0, AUX_SC1);
  st8_sc1(wsr, nto, (u32)wall_clock64() + nadd, tag);
  SB();
  STAMP(4);

  if (part == 2) FINISH();
  if (part == 0) {
    __builtin_amdgcn_s_barrier();
#pragma nounroll
    while (wleft > 0) piece();
    until(t1);
    STAMP(11);
    CFENCE();
    u32x4 og[2];
    og[0] = ld16<AUX_SC1>(wsr, po, 0);
    SB();
    WAIT_VM(1); CFENCE();
    wo_prep(1, 2);
#pragma unroll
    for (int k = 4; k < 8; ++k) PIN(bf[k]);
    SB();
    f32x4 d[4];
    auto wo_mm = [&]() {
      const f32x4 z = {0.f, 0.f, 0.f, 0.f};
#pragma unroll
      for (int t = 0; t < 4; ++t) { d[t] = mfma16(og[0], bf[t], z); og[1][t] = __float_as_uint(dpp<XOR8>(u2f(og[0][t]))); }
      SB();
#pragma unroll
      for (int t = 0; t < 4; ++t) d[t] = mfma16(og[1], bf[4 + t], d[t]);
    };
    auto p_bad = [&]() { return !all_ok(max(max(og[0][0], og[0][1]), max(og[0][2], og[0][3])) != SENT); };
    wo_mm();
    if (__builtin_expect(p_bad(), 0)) {
#pragma nounroll
      for (int spin = 0; spin < SPIN_MAX; ++spin) {
        __builtin_amdgcn_s_sleep(1);
        CFENCE();
        og[0] = ld16<AUX_SC1>(wsr, po, 0);
        if (!p_bad()) break;
      }
      wo_mm();
      SB();
      asm volatile(".Lend_d:");
    }
    STAMP(5); WSTAMP(5);
    {
      SB();
      f32x4 y; f32x2 h[4];
#pragma unroll
      for (int t = 0; t < 4; ++t) { h[t] = d[t].xy + d[t].zw; PIN(h[t]); }
#pragma unroll
      for (int t = 0; t < 4; ++t) { y[t] = h[t][0] + h[t][1]; PIN(y[t]); }
      *LDSP(f32x4, rdst) = y;
    }
    STAMP(16);
    LDS_BARRIER();
    STAMP(17);
    if (wave < 2) {
      f32x2 v[4];
#pragma unroll
      for (int w = 0; w < 4; ++w) v[w] = f32x2{LDSP(float, rsrc)[w * 256], LDSP(float, rsrc)[w * 256 + 128]};
      const f32x2 h = (v[0] + v[1]) + (v[2] + v[3]);
      const float y = h[0] + h[1];
      __builtin_amdgcn_raw_buffer_store_b32(min(pack_bf(dpp<0xA0>(y), dpp<0xF5>(y)), SENT - 1u), wsr, xo, 0, AUX_SC1);
      asm volatile(".Lend_a:");
    }
    STAMP(6);
    FINISH();
  }
  if (part == 3) {
    if (!has_w) FINISH();
    u32x4 b2[4];
    {
      __builtin_amdgcn_s_barrier();
      const rsrc_t r2 = mk_rsrc(w2 + (size_t)(blk * 32 + wave * 4) * 512, 4096u);
      const u32 l2 = (u32)(uintptr_t)(&sm.w2b[wave * 4][0]);
#pragma unroll
      for (int n = 0; n < 4; ++n) dma16(r2, (lane ^ (n * 2) ^ ((wave & 2) << 2)) * 16, n * 1024, l2 + n * 1024);
    }
    const u32 tdst = lds_addr(&sm.hb[hpos]);
    const u32 sdst = lds_addr(&sm.stat[wave][lane]);
    const u32 ssrc = lds_addr(&sm.stat[(g >> 1) * 4 + ((lane >> 1) & 3)][(lane & 1) * 16]);
    const u32 adst = lds_addr((c < 4 && (g & 1) == 0) ? &sm.actl[g >> 1][wave * 4 + c] : reinterpret_cast<u16*>(&sm.dummy[lane]));
    const u32 asrc = lds_addr(&sm.actl[lane & 1][g * 8]);
    const u32 apa = lds_addr(&sm.hb[(c >> 3) * 512 + (c & 3) * 64 + ((c >> 2) & 1) * 32 + g * 8]);
    const u32 xs2 = (P.NR & 4) ? 16384 : 16384 + 8192;
    u32 xoff = dup(G_XP) + ((P.NR & 4) ? near : 0) + g * 2048 + ub * 1024 + (wave & 3) * 256 + c * 16;
    u32x4 one;
#pragma unroll
    for (int k = 0; k < 4; ++k) one[k] = ((c & 2) == 0 && (c >> 2) == k) ? ((c & 1) ? 0x3f800000u : 0x00003f80u) : 0u;
    PIN(one);
    u32 ooff = dup(G_OP + (blk * 512 + tid) * 8);
    float Afp = Af, Bfp = Bf;
    PIN(xoff); PIN(ooff); PIN(Afp); PIN(Bfp);
    PIN(ga); PIN(xin); PIN(cc1);
    u32 w2a = (u32)(uintptr_t)(&sm.w2b[0][0]) + (g * 8 + ((lane >> 2) & 3)) * 1024 + (wave ^ (g & 1)) * 128 + (lane & 15) * 8;
    PIN(w2a);
    if (wave == 1) {
      due = t1 + P.N1 * 256; gate();
      u32x4 gn[2] = {ld16<AUX_SC1>(wsr, G_NT + lane * 16, 0), ld16<AUX_SC1>(wsr, G_NT + lane * 16, 1024)};
      const int tn = notices(IC<2>{}, G_NT + lane * 16, gn) * 256;
      due = (tn > t1 + 2 * 256 ? max(tn, t1 + P.L0 * 256) + P.L1 * 256 : t1) + P.X2 * 256;
      until(due);
    }
    asm volatile("s_waitcnt vmcnt(0)" ::: "memory");
    STAMP(9);
    __builtin_amdgcn_s_barrier();
    STAMP(11);
    CFENCE();
    u32x4 xg[2] = {ld16<AUX_SC1>(wsr, xoff, 0), ld16<AUX_SC1>(wsr, xoff, xs2)};
    SB();
#pragma unroll
    for (int t = 0; t < 4; ++t) {
      const u32x2 lo = tr_read(w2a ^ (t * 32)), hi = tr_read((w2a ^ (t * 32)) + 4096);
      b2[t] = u32x4{lo[0], lo[1], hi[0], hi[1]};
    }
#pragma unroll
    for (int t = 0; t < 4; ++t) PIN(b2[t]);
    SB();
    const f32x4 z4 = {0.f, 0.f, 0.f, 0.f};
    auto xp_bad = [&]() { return !all_ok(max(max(max(xg[0][0], xg[0][1]), max(xg[0][2], xg[0][3])), max(max(xg[1][0], xg[1][1]), max(xg[1][2], xg[1][3]))) != SENT); };
    f32x4 v = mfma16(one, xg[1], mfma16(one, xg[0], z4));
    if (__builtin_expect(xp_bad(), 0)) {
#pragma nounroll
      for (int spin = 0; spin < SPIN_MAX; ++spin) {
        __builtin_amdgcn_s_sleep(1);
        CFENCE();
        xg[0] = ld16<AUX_SC1>(wsr, xoff, 0); xg[1] = ld16<AUX_SC1>(wsr, xoff, xs2);
        if (!xp_bad()) break;
      }
      v = mfma16(one, xg[1], mfma16(one, xg[0], z4));
      SB();
      asm volatile(".Lend_e:");
    }
    STAMP(5); WSTAMP(5);
    {
      const float a0 = v[0], a1 = v[1];
      const float x10 = fmaf(ga[0], a0, xin[0]), x11 = fmaf(ga[1], a1, xin[1]);
      *LDSP(u32, tdst) = pack_bf(x10 * cc1[0], x11 * cc1[1]);
      float u = swap32_add(x10 + x11, fmaf(x10, x10, x11 * x11));
      u += dpp<XOR1>(u); u += dpp<XOR2>(u); u += dpp<XOR7>(u); u += dpp<XOR8>(u);
      *LDSP(float, sdst) = u;
    }
    STAMP(16);
    LDS_BARRIER();
    STAMP(17);
    {
      const f32x4 z = {0.f, 0.f, 0.f, 0.f};
      const u32x4 af0 = LDSP(u32x4, apa)[0], af1 = LDSP(u32x4, apa)[32];
      float s = LDSP(float, ssrc)[0], q = LDSP(float, ssrc)[32];
      SB();
      f32x4 d[4];
#pragma unroll
      for (int j = 0; j < 4; ++j) d[j] = mfma16(af0, bf[j], z);
#pragma unroll
      for (int j = 0; j < 4; ++j) d[j] = mfma16(af1, bf[4 + j], d[j]);
      s += dpp<XOR1>(s); q += dpp<XOR1>(q); s += dpp<XOR2>(s); q += dpp<XOR2>(q); s += dpp<XOR7>(s); q += dpp<XOR7>(q);
      const float mean = s * (1.f / 512.f);
      const float var = fmaf(q, 1.f / 512.f, -mean * mean);
      const float ri = __builtin_amdgcn_rsqf(fmaxf(var, 0.f) + 1e-5f);
      const float cf = fmaf(-ri * mean, Afp, Bfp);
      float y = (d[0][0] + d[1][1]) + (d[2][2] + d[3][3]);
      y = __builtin_bit_cast(float, __builtin_amdgcn_update_dpp(__builtin_bit_cast(int, y), __builtin_bit_cast(int, y), XOR8, 0xa, 0xf, false));
      y = swap16_add(y, y);
      y = fmaf(y, ri, cf);
      const float up = dpp<0x104>(y);
      *LDSP(u16, adst) = (u16)pack_bf(silu_f(y) * up, 0.f);
    }
    STAMP(18);
    LDS_BARRIER();
    STAMP(10);
    {
      const u32x4 af = *LDSP(u32x4, asrc);
      const f32x4 z = {0.f, 0.f, 0.f, 0.f};
      const f32x4 d0 = mfma16(af, b2[0], z), d1 = mfma16(af, b2[1], z), d2 = mfma16(af, b2[2], z), d3 = mfma16(af, b2[3], z);
      const bool g1 = g & 1, g2 = g & 2;
      const float e0 = g1 ? d1[0] : d0[0], e1 = g1 ? d1[1] : d0[1], f0 = g1 ? d3[0] : d2[0], f1 = g1 ? d3[1] : d2[1];
      const u32x2 ov = {pack_bf(g2 ? f0 : e0, g2 ? f1 : e1), tag};
      __builtin_amdgcn_raw_buffer_store_b64(ov, wsr, ooff, near ^ 8192u, AUX_SC1);
      __builtin_amdgcn_raw_buffer_store_b64(ov, wsr, ooff, near, AUX_SC1);
    }
    asm volatile(".Lend_b:");
    STAMP(6);
    FINISH();
  }
  if (wave == 0) {
    const int d = fd, ul = lane >> 3;
    u32 gm[2] = {sg[0][0], sg[1][0]};
    if (!all_ok((sg[0][1] == tag) & (sg[1][1] == tag))) gather8<2>(wsr, G_MOD + (1024 + d) * 8, 1536 * 8, tag, gm);
    u32 xa0, xa1;
    {
      due = t1 + P.N2 * 256; gate();
      u32x4 gn[2] = {ld16<AUX_SC1>(wsr, G_NT + lane * 16, 0), ld16<AUX_SC1>(wsr, G_NT + lane * 16, 1024)};
      const int tn = notices(IC<2>{}, G_NT + lane * 16, gn) * 256;
      due = (tn > t1 + 2 * 256 ? max(tn, t1 + P.L0 * 256) + P.L1 * 256 : t1) + P.X3 * 256;
      until(due);
    }
#pragma nounroll
    for (int spin = 0; spin < SPIN_MAX; ++spin) {
      CFENCE();
      xa0 = __builtin_amdgcn_raw_buffer_load_b32(wsr, dup(G_XP + ul * 2048) + near + (d >> 1) * 4, 0, AUX_SC1);
      xa1 = __builtin_amdgcn_raw_buffer_load_b32(wsr, dup(G_XP + ul * 2048) + near + (d >> 1) * 4, 1024, AUX_SC1);
      if (all_ok(max(xa0, xa1) != SENT)) break;
      __builtin_amdgcn_s_sleep(1);
    }
    float a0 = (d & 1) ? hi_f(xa0) : lo_f(xa0), a1 = (d & 1) ? hi_f(xa1) : lo_f(xa1);
    a0 += dpp<XOR8>(a0); a1 += dpp<XOR8>(a1);
    a0 = swap16_add(a0, a0); a1 = swap16_add(a1, a1);
    a0 = swap32_add(a0, a0); a1 = swap32_add(a1, a1);
    const bool hb = g & 1;
    float xv = rbf(((d & 1) ? hi_f(sx2) : lo_f(sx2)) + rbf((hb ? hi_f(gm[0]) : lo_f(gm[0])) * rbf(hb ? a1 : a0)));
    float gv = hb ? hi_f(gm[1]) : lo_f(gm[1]);
    PIN(xv); PIN(gv);
    if (wi == 0 && lane == 0) __builtin_amdgcn_raw_buffer_store_b32(tag, wsr, WS_EPOCH, 0, AUX_SC1);
    STAMP(10);
    due = max(due + P.X4 * 256, (int)(((u32)wall_clock64() - t0) << 8) + P.GX * 256);
    const bool last = ul + 40 < NUP;
    u32 o0 = dup(G_OP + (ul * 512 + d) * 8) + ((P.NR & 8) ? near : (ul & 2) * 4096), o5 = last ? o0 : OOB, tg5 = last ? tag : 0u;
    const rsrc_t rout = mk_rsrc(out, 2048u);
    u32 oo = (lane & 40) == 0 ? (g * 512 + d) * 2 : OOB;
    const u32 pk = c == 0 ? 0x00003f80u : (c == 4 ? 0x3f800000u : 0u);
    u32x4 one = {pk, 0u, pk, 0u};
    PIN(o0); PIN(o5); PIN(tg5); PIN(oo); PIN(one);
    __builtin_amdgcn_raw_buffer_store_b16((u16)pack_bf(xv, 0.f), rout, oo, 0, AUX_SC1);
    until(due);
    f32x4 sv;
#pragma nounroll
    for (int spin = 0; spin < SPIN_MAX; ++spin) {
      CFENCE();
      u32x2 og[6];
#pragma unroll
      for (int k = 0; k < 5; ++k) og[k] = ld8<AUX_SC1>(wsr, o0, k * 65536);
      og[5] = ld8<AUX_SC1>(wsr, o5, 5 * 65536);
      sv = f32x4{0.f, 0.f, 0.f, 0.f};
#pragma unroll
      for (int k = 0; k < 3; ++k) sv = mfma16(one, u32x4{og[2 * k][0], og[2 * k][1], og[2 * k + 1][0], og[2 * k + 1][1]}, sv);
      const u32 bad = ((og[0][1] ^ tag) | (og[1][1] ^ tag) | (og[2][1] ^ tag)) | ((og[3][1] ^ tag) | (og[4][1] ^ tag) | (og[5][1] ^ tg5));
      PIN(sv);
      if (all_ok(bad == 0u)) break;
      __builtin_amdgcn_s_sleep(1);
    }
    STAMP(7);
    const float s = sv[0] + dpp<XOR8>(sv[0]);
    __builtin_amdgcn_raw_buffer_store_b16((u16)pack_bf(fmaf(gv, s, xv), 0.f), rout, oo, 0, AUX_SC1);
    asm volatile(".Lend_c:");
  }
  FINISH();
}

extern "C" void kernel_launch(void* const* d_in, const int* in_sizes, int n_in,
                              void* d_out, int out_size, void* d_ws, size_t ws_size,
                              hipStream_t stream) {
  fused_kernel<<<256, 512, 0, stream>>>((const u16*)d_in[1], (const u16*)d_in[2], (const u16*)d_in[4], (const u16*)d_in[3], (const u16*)d_in[10], (const u16*)d_in[6], (const u16*)d_in[7],
                                        (unsigned char*)d_ws, (const u16*)d_in[0], (const u16*)d_in[9], (const u16*)d_in[8], (u16*)d_out);
}
```

```cpp
#define PROBE_MUL 10
#include <hip/hip_runtime.h>
#include <stdint.h>

typedef unsigned int u32;
typedef unsigned short u16;
typedef unsigned long long u64;
typedef __attribute__((ext_vector_type(4))) unsigned u32x4;
typedef __attribute__((ext_vector_type(2))) unsigned u32x2;
typedef __attribute__((ext_vector_type(2))) __bf16 bf16x2;
typedef __attribute__((ext_vector_type(4))) float f32x4;
typedef __attribute__((ext_vector_type(2))) float f32x2;
typedef __amdgpu_buffer_rsrc_t rsrc_t;

constexpr int NH = 32, SK = 16384, DH = 128, WIN = 4096, FF = 1376;
struct Prm { int E0, ES, E1, E2, E2C, EU, TEND, DT_UP, L0, L1, X1, X2, X3, X4, NE, FE, M0, M1, M2, M3, M4, GS, GX, N1, N2, GA, WD, DT_WO, LW, LU, EW, PM, NF, NR, C0, A0, AG, AC, TS, TW, TK, TG, W[9]; };
constexpr Prm PRM0 = {32, 20, 32, 32, 54, 60, 1960, 60, 30, 25, 130, 76, 116, 73, 3, 8, 0x251fb20c, 0x2d45f3ac, 0xff083f7, 0x2b125d6a, 0x396af96d, 0, 40, 20, 60, 0, 4, 100, 45, 15, 0, 0x15c, 15, 14, 5, 75, 0, 100, 510, 236, 60, 20, 0x77777777, 0x77777777, 0x77777777, 0x77777777, 0x77777777, 0x44444477, 0x44444444, 0x44444444, 0x44444444};
constexpr int NL = 37;
constexpr int SK0 = 21, SK1 = 32;
constexpr int NCMIN = (NL - SK1 + SK0) * 8;
constexpr int WK = 8, WV = 5;
constexpr int WUSE = 12, LNS = 7;
constexpr int QP = 14, QD = 4;
constexpr int VQ = 6;
constexpr int NSL = (NL + 7) / 8;
constexpr int VS = VQ + NSL;
constexpr int MLS = VS + 2;
constexpr int VSIDE = MLS - 5, UPREP = MLS + 6;
constexpr int CPF = 17;
constexpr int SMO = 1;
constexpr int RP0 = 24;
constexpr int PRB = 8;
constexpr int TBP = 4, TBL = 11, TBU = 15;
constexpr int window(int s, int w) {
  int n = w;
  for (int u = s - w + 1; u <= s; ++u) n += u == SMO ? 6 : (u == TBL || u == NL + CPF || u == NL + SK1 ? 1 : (u == QP || u == NL + VQ - 4 ? 2 : (u == NL + VSIDE ? 5 : 0)));
  return n;
}
constexpr int cons_end(int v) {
  const int a = v - WV + 1, c = (v - MLS) * 4;
  const int m = a < c ? a : c;
  return m < 0 ? 0 : (m > NL ? NL : m);
}
constexpr u32 col(const Prm& P, int j) { const int m[6] = {P.M0, P.M1, P.M2, P.M3, P.M4, P.PM}; return (u32)(m[j / 3] >> (j % 3 * 10) & 1023) << 10; }
constexpr u32 first_col(const Prm& P, int k) {
  if (k < 4) return col(P, 12 + k) | 1u << (24 + k);
  if (k < 6) return col(P, k - 4);
  if (k < 8) return col(P, 4 + k) | 1u << (16 + k);
  if (k < 11) return col(P, k - 1);
  return (col(P, 2) ^ col(P, 3) ^ col(P, 4) ^ col(P, 5) ^ col(P, 6) ^ col(P, 10) ^ col(P, 11)) | 0xf00000u;
}
constexpr bool one_to_one(const Prm& P) {
  u32 c[10] = {};
  for (int j = 0; j < 10; ++j) c[j] = col(P, j);
  for (int j = 0; j < 10; ++j) {
    if (c[j] == 0) return false;
    const u32 low = c[j] & (0u - c[j]);
    for (int k = j + 1; k < 10; ++k) if (c[k] & low) c[k] ^= c[j];
  }
  return true;
}
static_assert(one_to_one(PRM0), "every KB of the window exactly once");
struct Tab { u32 e[512]; };
constexpr Tab make_tab(const Prm& P) {
  Tab T{};
  for (int bid = 0; bid < 256; ++bid) {
    const int xcd = bid & 7, part = bid >> 4 & 3, i8 = (bid >> 3 & 1) | bid >> 6 << 1, wi = i8 * 8 + xcd;
    const bool has_mod = wi < 48, has_up = wi >= 21;
    const int cb0 = (i8 + P.C0) & 7, cb1 = (i8 + 4) & 7, ms = wi - 20;
    const int mi = wi < 8 ? wi : (wi < 16 ? 8 + ((xcd + 4) & 7) : (wi < 20 ? (wi - 14) * 8 + 3 : 16 + ms / 7 * 8 + ms % 7 + (ms % 7 >= 3 ? 1 : 0)));
    const int ui = xcd * 5 + (xcd > 5 ? xcd - 5 : 0) + (i8 >= 3 ? (xcd >= 5 ? 1 : 0) + i8 - 3 : 0);
    const int wd = cb0 == 3 ? 0 : P.WD;
    const int e[4] = {P.E0 - 4 * wd + (cb0 == 3 ? P.ES : 0), P.E1 + (cb1 == 3 ? P.ES : 0), has_mod ? (wi < 16 ? P.E2C : P.E2) + ((mi & 7) == 3 ? P.ES : 0) : 0, has_up ? P.EU : 0};
    int tw[4] = {}, win[4] = {}, span[4] = {};
    for (int k = 0; k < 4; ++k) {
      const int np = k == 0 ? 8 - wd : 8;
      tw[k] = np ? e[k] * P.TW / (16 * np) : 0;
      span[k] = P.TEND - (P.NF + tw[k] * np / 2 > P.TK ? P.NF + tw[k] * np / 2 : P.TK) - P.TG - (k == 0 && wd ? P.DT_WO : (k == 3 && has_up ? P.DT_UP : 0));
      win[k] = span[k] - (k == 0 && wd ? P.EW : (k == 3 && has_up ? P.GA : 0));
    }
    int n[4] = {NCMIN / 8, NCMIN / 8, NCMIN / 8, NCMIN / 8};
    for (int left = 128 - NCMIN / 2; left > 0; --left) {
      int best = -1;
      for (int k = 3; k >= 0; --k)
        if (n[k] < NL && (best < 0 || n[k] * win[best] < n[best] * win[k])) best = k;
      ++n[best];
    }
    const int c[4] = {0, n[0] - 1, n[0] + n[1] - 2, n[0] + n[1] + n[2] - 3};
    const int nl = n[part];
    const bool has_w = part < 2 || (part == 2 ? has_mod : has_up);
    const int blk = !has_w ? 0 : (part == 0 ? cb0 : (part == 1 ? xcd * 8 + cb1 : (part == 2 ? mi : ui)));
    T.e[2 * bid] = (u32)(c[part] | nl << 7 | blk << 13 | (has_w ? 1 : 0) << 19 | (part == 0 ? wd : 0) << 20) | (u32)tw[part] << 24;
    T.e[2 * bid + 1] = (u32)(span[part] * P.TS / (4 * (nl - 1)) | span[part] * (1024 - P.TS) / (4 * (nl - 1)) << 16);
  }
  return T;
}
constexpr int step_range(const Prm& P, bool mx_) {
  int mx = 0, mn = 1 << 20;
  const Tab T = make_tab(P);
  for (int bid = 0; bid < 256; ++bid) { const int n = T.e[2 * bid] >> 7 & 63; mx = n > mx ? n : mx; mn = n < mn ? n : mn; }
  return mx_ ? mx : mn;
}
extern "C" __attribute__((used, visibility("protected"))) __constant__ const Tab wg_tab = make_tab(PRM0);
static_assert(step_range(PRM0, true) <= NL && step_range(PRM0, false) >= NCMIN / 8, "a wave's key range does not fit its load steps");
static_assert(UPREP + 1 < SK0 && QP + QD + 1 < SK0 && cons_end(SK0 - 1) == SK0 - WV && NL - SK1 == WV, "steps every wave runs");
constexpr int NUP = FF / 32;
constexpr int SPIN_MAX = 1 << 17;

constexpr u32 WS_EPOCH = 0;
constexpr u32 G_TB = 1024;
constexpr u32 G_NT = 2048;
constexpr u32 G_MOD = 4096;
constexpr u32 G_Q = G_MOD + 3072 * 8;
constexpr u32 G_ML = G_Q + 4096 * 8;
constexpr u32 G_U = G_ML + 256 * 16;
constexpr u32 G_P = 0;
constexpr u32 G_XP = G_P + 256 * 256;
constexpr u32 G_OP = G_XP + 8 * 512 * 4;
constexpr u32 G_PB = G_OP + 44 * 512 * 8;
constexpr u32 G_VT = G_U + 2 * (G_PB + 8192);
constexpr u32 WS_END = G_VT + 1024;
__device__ __forceinline__ u32 dup(u32 o) { return G_U + ((o >> 13) << 14) + (o & 8191u); }
constexpr u32 SENT = 0xffffffffu;
constexpr u32 OOB = 0x40000000u;
#define WST 0

constexpr int AUX_NT = 2, AUX_SC1 = 16;

__device__ __forceinline__ float lo_f(u32 v) { return __uint_as_float(v << 16); }
__device__ __forceinline__ float hi_f(u32 v) { return __uint_as_float(v & 0xffff0000u); }
__device__ __forceinline__ float rbf(float x) { return (float)(__bf16)x; }
__device__ __forceinline__ u32 pack_bf(float lo, float hi) {
  bf16x2 r; r[0] = (__bf16)lo; r[1] = (__bf16)hi; return __builtin_bit_cast(u32, r);
}
__device__ __forceinline__ float dot2(u32 k, u32 q, float acc) {
  return __builtin_amdgcn_fdot2_f32_bf16(__builtin_bit_cast(bf16x2, k), __builtin_bit_cast(bf16x2, q), acc, false);
}
template <int CTRL> __device__ __forceinline__ float dpp(float x) {
  return __builtin_bit_cast(float, __builtin_amdgcn_mov_dpp(__builtin_bit_cast(int, x), CTRL, 0xf, 0xf, true));
}
constexpr int XOR1 = 0xB1, XOR2 = 0x4E, XOR7 = 0x141, XOR8 = 0x128;
__device__ __forceinline__ float swap32_add(float a, float b) {
  auto t = __builtin_amdgcn_permlane32_swap(__float_as_uint(a), __float_as_uint(b), false, false);
  return __uint_as_float(t[0]) + __uint_as_float(t[1]);
}
__device__ __forceinline__ float swap16_add(float a, float b) {
  auto t = __builtin_amdgcn_permlane16_swap(__float_as_uint(a), __float_as_uint(b), false, false);
  return __uint_as_float(t[0]) + __uint_as_float(t[1]);
}
__device__ __forceinline__ float wave_sum(float x) {
  x += dpp<XOR1>(x); x += dpp<XOR2>(x); x += dpp<XOR7>(x); x += dpp<XOR8>(x);
  x = swap16_add(x, x);
  return swap32_add(x, x);
}
__device__ __forceinline__ float wave_max(float x) {
  x = fmaxf(x, dpp<XOR1>(x)); x = fmaxf(x, dpp<XOR2>(x)); x = fmaxf(x, dpp<XOR7>(x)); x = fmaxf(x, dpp<XOR8>(x));
  auto s = __builtin_amdgcn_permlane16_swap(__float_as_uint(x), __float_as_uint(x), false, false);
  x = fmaxf(__uint_as_float(s[0]), __uint_as_float(s[1]));
  auto t = __builtin_amdgcn_permlane32_swap(__float_as_uint(x), __float_as_uint(x), false, false);
  return fmaxf(__uint_as_float(t[0]), __uint_as_float(t[1]));
}
template <int CTRL> __device__ __forceinline__ float sel_add(float lo, float hi, bool up) {
  const float t1 = lo + dpp<CTRL>(lo), t2 = hi + dpp<CTRL>(hi);
  return up ? t2 : t1;
}
template <int I> __device__ __forceinline__ float bc16(float x) {
  return __builtin_bit_cast(float, __builtin_amdgcn_update_dpp(0, __builtin_bit_cast(int, x), 0x150 + I, 0xf, 0xf, false));
}
__device__ __forceinline__ float bcast16(float x, int i) {
  switch (i) {
    case 0: return bc16<0>(x); case 1: return bc16<1>(x); case 2: return bc16<2>(x); case 3: return bc16<3>(x);
    case 4: return bc16<4>(x); case 5: return bc16<5>(x); case 6: return bc16<6>(x); case 7: return bc16<7>(x);
    case 8: return bc16<8>(x); case 9: return bc16<9>(x); case 10: return bc16<10>(x); case 11: return bc16<11>(x);
    case 12: return bc16<12>(x); case 13: return bc16<13>(x); case 14: return bc16<14>(x); default: return bc16<15>(x);
  }
}
__device__ __forceinline__ float silu_f(float x) { return x * __builtin_amdgcn_rcpf(1.f + __expf(-x)); }

template <int AUX> __device__ __forceinline__ u32x4 ld16(rsrc_t r, u32 voff, u32 soff) {
  return __builtin_amdgcn_raw_buffer_load_b128(r, voff, soff, AUX);
}
template <int AUX> __device__ __forceinline__ u32x2 ld8(rsrc_t r, u32 voff, u32 soff) {
  return __builtin_amdgcn_raw_buffer_load_b64(r, voff, soff, AUX);
}
__device__ __forceinline__ void st8_sc1(rsrc_t r, u32 voff, u32 v, u32 tag) {
  __builtin_amdgcn_raw_buffer_store_b64(u32x2{v, tag}, r, voff, 0, AUX_SC1);
}
__device__ __forceinline__ void st16_sc1(rsrc_t r, u32 voff, u32 v0, u32 v1, u32 tag) {
  __builtin_amdgcn_raw_buffer_store_b128(u32x4{v0, tag, v1, tag}, r, voff, 0, AUX_SC1);
}
__device__ __forceinline__ rsrc_t mk_rsrc(const void* p, u32 bytes) {
  return __builtin_amdgcn_make_buffer_rsrc(const_cast<void*>(p), 0, bytes, 0x00020000);
}
#define WAIT_VM(n) __builtin_amdgcn_s_waitcnt(((n) & 15) | (((n) >> 4) << 14) | (7 << 4) | (15 << 8))
#define SB() __builtin_amdgcn_sched_barrier(0)
#define CFENCE() asm volatile("" ::: "memory")
__device__ __forceinline__ bool all_ok(bool ok) { return __builtin_amdgcn_ballot_w64(!ok) == 0ull; }

template <int N> __device__ __forceinline__ void gather16(rsrc_t ws, u32 voff, u32 stride, u32 tag, u32x4 (&g)[N]) {
#pragma nounroll
  for (int spin = 0; spin < SPIN_MAX; ++spin) {
    CFENCE();
    bool ok = true;
#pragma unroll
    for (int i = 0; i < N; ++i) g[i] = ld16<AUX_SC1>(ws, voff + i * stride, 0);
#pragma unroll
    for (int i = 0; i < N; ++i) ok &= (g[i][1] == tag) & (g[i][3] == tag);
    if (all_ok(ok)) break;
    __builtin_amdgcn_s_sleep(8);
  }
}
template <int N> __device__ __forceinline__ void gather8(rsrc_t ws, u32 voff, u32 stride, u32 tag, u32 (&g)[N], bool act = true) {
#pragma nounroll
  for (int spin = 0; spin < SPIN_MAX; ++spin) {
    CFENCE();
    bool ok = true;
    u32x2 t[N];
#pragma unroll
    for (int i = 0; i < N; ++i) t[i] = ld8<AUX_SC1>(ws, voff + i * stride, 0);
#pragma unroll
    for (int i = 0; i < N; ++i) { ok &= (t[i][1] == tag); g[i] = t[i][0]; }
    if (all_ok(ok | !act)) break;
    __builtin_amdgcn_s_sleep(8);
  }
}

struct Smem {
  float red[8][128];
  float ml[8][2];
  float r2[64 * 20];
  u16 hb[3 * 1024];
  float stat[8][64];
  float red2[8][128];
  u32 dummy[64];
  u32x2 bc;
  u32x4 zero[4];
  float pl[4][8];
  u32 mflag[8];
  u32x4 wbuf[8][8][64];
  __attribute__((aligned(256))) u16 w2b[32][512];
  u16 actl[2][32];
};

#define PROBE_K 4
#define PROBE_OP 0
#define PROBE_PART -1
#define STAMP(k) do { if ((k) == PROBE_K && tid == 0) pst = wall_clock64(); } while (0)
#define WSTAMP(k) do {} while (0)
#define FINISH() do { STAMP(8); if (tid == 0) { __hip_atomic_store(reinterpret_cast<u64*>(ws + (1u << 20)) + bid * 2, pt0 + (u32)(t0 - (u32)pt0) + (t1 >> 8), __ATOMIC_RELAXED, __HIP_MEMORY_SCOPE_AGENT); \
   __hip_atomic_store(reinterpret_cast<u64*>(ws + (1u << 20)) + bid * 2 + 1, (PROBE_PART < 0 || part == PROBE_PART) ? pst : 0ull, __ATOMIC_RELAXED, __HIP_MEMORY_SCOPE_AGENT); } \
   if (bid == 8 && wave == 0) probe_end(ws, pt0, lane); return; } while (0)
#define PIN(x) asm volatile("" : "+v"(x))
#define LDS_BARRIER() do { __builtin_amdgcn_s_waitcnt(0xc07f); __builtin_amdgcn_s_barrier(); CFENCE(); } while (0)
#define WAVE_SYNC() do { __builtin_amdgcn_fence(__ATOMIC_RELEASE, "wavefront"); __builtin_amdgcn_wave_barrier(); __builtin_amdgcn_fence(__ATOMIC_ACQUIRE, "wavefront"); } while (0)

__device__ __forceinline__ void dma16(rsrc_t r, u32 voff, u32 soff, u32 lds_dst) {
  u32 keep;
  asm volatile("s_mov_b32 %0, m0\n\ts_mov_b32 m0, %4\n\ts_nop 4\n\tbuffer_load_dwordx4 %1, %2, %3 offen nt lds\n\ts_mov_b32 m0, %0"
               : "=&s"(keep) : "v"(voff), "s"(r), "s"(soff), "s"(lds_dst) : "memory");
}
__device__ __forceinline__ void ld16_into(u32x4& d, rsrc_t r, u32 voff, u32 soff) {
  asm volatile("s_nop 4\n\tbuffer_load_dwordx4 %0, %1, %2, %3 offen nt" : "+v"(d) : "v"(voff), "s"(r), "s"(soff) : "memory");
}
__device__ __forceinline__ u32 perm_lo(u32 a, u32 b) { return __builtin_amdgcn_perm(b, a, 0x05040100u); }
__device__ __forceinline__ u32 perm_hi(u32 a, u32 b) { return __builtin_amdgcn_perm(b, a, 0x07060302u); }
__device__ __forceinline__ float rdl(float x, int l) { return __int_as_float(__builtin_amdgcn_readlane(__float_as_int(x), l)); }

typedef __attribute__((ext_vector_type(8))) __bf16 bf16x8;
__device__ __forceinline__ f32x4 mfma16(u32x4 a, u32x4 b, f32x4 c) {
  return __builtin_amdgcn_mfma_f32_16x16x32_bf16(__builtin_bit_cast(bf16x8, a), __builtin_bit_cast(bf16x8, b), c, 0, 0, 0);
}
__device__ __forceinline__ float sum8(const float* p) {
  const f32x4 a = *reinterpret_cast<const f32x4*>(p), b = *reinterpret_cast<const f32x4*>(p + 4);
  return ((a[0] + b[0]) + (a[1] + b[1])) + ((a[2] + b[2]) + (a[3] + b[3]));
}
__device__ __forceinline__ float ln_ri(float var) {
  return __builtin_amdgcn_rcpf(rbf(__builtin_amdgcn_sqrtf(rbf(rbf(fmaxf(var, 0.f)) + 1e-5f))));
}
__device__ __forceinline__ void ln_stats2(const float (&x0)[8], const float (&x1)[8], float& mb0, float& ri0, float& mb1, float& ri1) {
  const float k0 = rdl(x0[0], 0), k1 = rdl(x1[0], 0);
  float s0 = 0.f, s1 = 0.f, q0 = 0.f, q1 = 0.f;
#pragma unroll
  for (int k = 0; k < 8; ++k) {
    const float d0 = x0[k] - k0, d1 = x1[k] - k1;
    s0 += d0; s1 += d1; q0 = fmaf(d0, d0, q0); q1 = fmaf(d1, d1, q1);
  }
  float t = swap16_add(swap32_add(s0, s1), swap32_add(q0, q1));
  t += dpp<XOR1>(t); t += dpp<XOR2>(t); t += dpp<XOR7>(t); t += dpp<XOR8>(t);
  auto u = __builtin_amdgcn_permlane16_swap(__float_as_uint(t), __float_as_uint(t), false, false);
  const float ms = __uint_as_float(u[0]) * (1.f / 512.f), mq = __uint_as_float(u[1]) * (1.f / 512.f);
  const float ri = ln_ri(fmaf(-ms, ms, mq));
  mb0 = rbf(k0 + rdl(ms, 0)); mb1 = rbf(k1 + rdl(ms, 32));
  ri0 = rdl(ri, 0); ri1 = rdl(ri, 32);
}
template <int I> struct IC { static constexpr int v = I; };
template <int I, int N, class F> __device__ __forceinline__ void sfor(F&& f) {
  if constexpr (I < N) { if (f(IC<I>{})) sfor<I + 1, N>(f); }
}
typedef __attribute__((ext_vector_type(4))) short s16x4;
typedef __attribute__((address_space(3))) s16x4* lds_s16x4p;
__device__ __forceinline__ u32x2 tr_read(u32 lds_addr) {
  return __builtin_bit_cast(u32x2, __builtin_amdgcn_ds_read_tr16_b64_v4i16((lds_s16x4p)lds_addr));
}
__device__ __forceinline__ u32 next_tag(u32 e) { return ((e | 0x40004000u) + 1u) & ~0x40004000u; }
__device__ __forceinline__ float u2f(u32 v) { return __uint_as_float(v); }
#define LDSP(T, a) ((__attribute__((address_space(3))) T*)(a))
template <class T> __device__ __forceinline__ u32 lds_addr(T* p) { u32 a = (u32)(uintptr_t)p; PIN(a); return a; }

#define TAB_LOAD "s_add_u32 s30, s28, wg_tab@rel32@lo+4\ns_addc_u32 s31, s29, wg_tab@rel32@hi+12\nglobal_load_dwordx2 %[te], %[va], s[30:31]\n"
#define FC(k) ".Lfc" #k
#define FC_REGS
#define FX(r, bit, k) "s_bitcmp1_b32 %[" #r "], " #bit "\ns_cselect_b32 %[sb], " FC(k) ", 0\ns_xor_b32 %[sa], %[sa], %[sb]\n"
__device__ __forceinline__ void probe_end(unsigned char* ws, u64 pt0, int lane) {
  while (wall_clock64() < pt0 + 5500ull) __builtin_amdgcn_s_sleep(8);
  u64 a[4], c2[4];
  for (int i = 0; i < 4; ++i) {
    a[i] = __hip_atomic_load(reinterpret_cast<u64*>(ws + (1u << 20)) + (i * 64 + lane) * 2, __ATOMIC_RELAXED, __HIP_MEMORY_SCOPE_AGENT);
    c2[i] = __hip_atomic_load(reinterpret_cast<u64*>(ws + (1u << 20)) + (i * 64 + lane) * 2 + 1, __ATOMIC_RELAXED, __HIP_MEMORY_SCOPE_AGENT);
  }
  int mn0 = 1 << 30, mx1 = -(1 << 30), mn1 = 1 << 30, sum = 0, cnt = 0;
  for (int i = 0; i < 4; ++i) {
    const int d0 = (int)(a[i] - pt0);
    mn0 = min(mn0, d0);
    if (c2[i] != 0ull) { const int d1 = (int)(c2[i] - pt0); mx1 = max(mx1, d1); mn1 = min(mn1, d1); sum += d1; cnt += 1; }
  }
  for (int o = 1; o < 64; o <<= 1) { mn0 = min(mn0, __shfl_xor(mn0, o)); mx1 = max(mx1, __shfl_xor(mx1, o)); mn1 = min(mn1, __shfl_xor(mn1, o)); sum += __shfl_xor(sum, o); cnt += __shfl_xor(cnt, o); }
  int X = PROBE_OP == 0 ? mx1 : (PROBE_OP == 1 ? sum / max(cnt, 1) : mn1);
  if (PROBE_K == 102) X = mn0;
  X = mn0 + (X - mn0) * PROBE_MUL;
  const u64 tg = pt0 + (u64)(long long)(X + 8000);
  for (int i = 0; i < 400000 && wall_clock64() < tg; ++i) __builtin_amdgcn_s_sleep(1);
}
__global__ void __launch_bounds__(512, 2) fused_kernel(const u16* kc_, const u16* vc_, const u16* wo_, const u16* wq_, const u16* wmod_, const u16* w1_, const u16* w3_,
                                                       unsigned char* ws_, const u16* x_, const u16* cnd_, const u16* w2_, u16* out_) {
  __shared__ __attribute__((aligned(256))) Smem sm;
  const u64 a_ka = (u64)__builtin_amdgcn_kernarg_segment_ptr();
  const int bid = blockIdx.x, tid = threadIdx.x;
  u64 wa, t64; u32x2 te; int tid0;
  u32 ar[14];
  u32 tch2, sa_, sb_, va_;
  u32x4 kr[NL], vr[NL];
  asm volatile(".set .Lfc0, %0\n.set .Lfc1, %1\n.set .Lfc2, %2\n.set .Lfc3, %3\n.set .Lfc4, %4\n.set .Lfc5, %5\n.set .Lfc6, %6\n.set .Lfc7, %7\n.set .Lfc8, %8\n.set .Lfc9, %9\n.set .Lfc10, %10\n.set .Lfc11, %11"
    :: "n"(first_col(PRM0, 0)), "n"(first_col(PRM0, 1)), "n"(first_col(PRM0, 2)), "n"(first_col(PRM0, 3)), "n"(first_col(PRM0, 4)), "n"(first_col(PRM0, 5)),
       "n"(first_col(PRM0, 6)), "n"(first_col(PRM0, 7)), "n"(first_col(PRM0, 8)), "n"(first_col(PRM0, 9)), "n"(first_col(PRM0, 10)), "n"(first_col(PRM0, 11)));
  asm volatile(
    "s_load_dwordx16 s[4:19], %[ka], 0x0\ns_load_dwordx8 s[20:27], %[ka], 0x40\n"
    "s_memrealtime %[t]\n"
    "v_lshlrev_b32 %[va], 3, %[bid]\n"
    "s_getpc_b64 s[28:29]\n.Lcpf_first:\n"
    TAB_LOAD
    "s_cmp_gt_u32 %[bid], 15\ns_cbranch_scc1 .Lno_cpf\n"
    "s_mov_b32 %[sa], .Lend_a-.Lcpf_first\ns_max_i32 %[sa], %[sa], .Lend_b-.Lcpf_first\ns_max_i32 %[sa], %[sa], .Lend_c-.Lcpf_first\ns_max_i32 %[sa], %[sa], .Lend_d-.Lcpf_first\ns_max_i32 %[sa], %[sa], .Lend_e-.Lcpf_first\n"
    "v_lshlrev_b32 %[va], 6, %[tid]\nv_min_u32 %[va], %[sa], %[va]\nglobal_load_dword %[pf], %[va], s[28:29]\n"
    ".Lno_cpf:\n"
    "v_readfirstlane_b32 %[t0], %[tid]\nv_and_b32 %[va], 63, %[tid]\n"
    "s_mov_b32 %[sa], " FC(11) "\n"
    FX(bid, 0, 0) FX(bid, 1, 1) FX(bid, 2, 2) FX(bid, 3, 3) FX(bid, 4, 4) FX(bid, 5, 5) FX(bid, 6, 6) FX(bid, 7, 7) FX(t0, 6, 8) FX(t0, 7, 9) FX(t0, 8, 10)
    "v_lshl_add_u32 %[va], %[va], 4, %[sa]\n"
    "s_waitcnt lgkmcnt(0)\nglobal_load_dwordx4 %[k0], %[va], s[4:5] nt\nglobal_load_dwordx4 %[v0], %[va], s[6:7] nt\n"
    "s_bitcmp1_b32 %[t0], 6\ns_cselect_b64 s[14:15], s[16:17], s[14:15]\n"
    "s_bitcmp1_b32 %[bid], 4\ns_cselect_b64 s[8:9], s[10:11], s[8:9]\ns_cselect_b64 s[12:13], s[14:15], s[12:13]\ns_bitcmp1_b32 %[bid], 5\ns_cselect_b64 %[wa], s[12:13], s[8:9]"
    : "=&{s4}"(ar[0]), "=&{s5}"(ar[1]), "=&{s6}"(ar[2]), "=&{s7}"(ar[3]), "=&{s18}"(ar[4]), "=&{s19}"(ar[5]), "=&{s20}"(ar[6]), "=&{s21}"(ar[7]), "=&{s22}"(ar[8]), "=&{s23}"(ar[9]),
      "=&{s24}"(ar[10]), "=&{s25}"(ar[11]), "=&{s26}"(ar[12]), "=&{s27}"(ar[13]), [wa] "=&s"(wa), [t0] "=&s"(tid0), [t] "=&s"(t64), [te] "=&v"(te), [sa] "=&s"(sa_), [sb] "=&s"(sb_),
      [va] "=&v"(va_), [pf] "=&v"(tch2), [k0] "=&v"(kr[0]), [v0] "=&v"(vr[0])
    : [ka] "s"(a_ka), [bid] "s"(bid), [tid] "v"(tid) : "s8", "s9", "s10", "s11", "s12", "s13", "s14", "s15", "s16", "s17", "s28", "s29", "s30", "s31", "scc", "memory" FC_REGS);
  asm volatile("" : "+s"(ar[0]), "+s"(ar[1]), "+s"(ar[2]), "+s"(ar[3]), "+s"(ar[4]), "+s"(ar[5]), "+s"(ar[6]), "+s"(ar[7]), "+s"(ar[8]), "+s"(ar[9]), "+s"(ar[10]), "+s"(ar[11]), "+s"(ar[12]), "+s"(ar[13]));
  asm volatile("" : "+s"(wa), "+s"(t64), "+s"(tid0), "+s"(sa_));
  const u32 tka = (u32)wall_clock64();
  const u64 pt0 = t64; u64 pst = (PROBE_K == 100 || PROBE_K == 102) ? pt0 : (PROBE_K == 101 ? pt0 + (u32)(tka - (u32)t64) : 0);
  const u32 t0 = tka;
  __builtin_assume(tid >= 0 && tid < 512 && tid0 >= 0 && tid0 < 512 && bid >= 0 && bid < 256);
  constexpr Prm P = PRM0;
  const int TEND = P.TEND;
  const int xcd = bid & 7, part = bid >> 4 & 3;
  const int lane = tid & 63, wave = tid0 >> 6;
  SB();
  const bool p3 = part == 3;
  u32 pitch = (u32)(0x005600c001000020ull >> (part * 16) & 0xffff) * 32u;
  const int sh = p3 ? 2 : 3;
  u32 wv0 = (u32)((p3 ? (wave >> 1) * 128 : wave * 64) + (part == 0 ? xcd * 512 : 0) + (lane >> sh)) * pitch + ((lane & (p3 ? 3 : 7)) ^ (lane >> (p3 ? 5 : 3))) * 16u;
  const u32 wimg = (u32)(uintptr_t)(&sm.wbuf[0][0][0]);
  u32 wsz = (part == 0 ? 4096u : 512u) * pitch, wld = wimg + (p3 ? (wave & 1) * 32768 + (wave >> 1) * 8192 : wave * 8192);
  PIN(wv0);
  asm volatile("" : "+s"(wsz), "+s"(wld), "+s"(pitch));
  auto karg = [&](int i) { return ar[2 * i] | (u64)ar[2 * i + 1] << 32; };
  const u64 a_kc = karg(0), a_vc = karg(1);
  unsigned char* ws = (unsigned char*)karg(2);
  const u16 *x = (const u16*)karg(3), *cnd = (const u16*)karg(4), *w2 = (const u16*)karg(5);
  u16* out = (u16*)karg(6);
  const rsrc_t wsr = mk_rsrc(ws, WS_END);
  asm volatile("s_waitcnt vmcnt(2)" : "+v"(te));
  const u32 te0 = __builtin_amdgcn_readfirstlane(te[0]), te1 = __builtin_amdgcn_readfirstlane(te[1]);
  const int bl0 = te0 & 127, nl = te0 >> 7 & 63, blk = te0 >> 13 & 63;
  const bool has_w = te0 >> 19 & 1;
  const rsrc_t rw = mk_rsrc((const void*)wa, wsz);
  const u32 wstep = pitch << (6 - sh), wflip = p3 ? 32u : 0u;
  u32 wso = blk << (sh + 4);
  int wleft = 8;
  auto piece = [&]() {
    dma16(rw, wv0, wso, wld);
    wso += wstep; wld += 1024;
    wv0 ^= (wleft & 1) ? wflip : 0u;
    --wleft;
  };
  SB();
  STAMP(23);
  const int b = bid >> 3 & 1, head = xcd * 4 + (bid >> 6 ^ 3);
  const int g = lane >> 4, c = lane & 15;
  const int i8 = b | bid >> 6 << 1, wi = i8 * 8 + xcd;
  const bool is_up = p3 && has_w, modc = part == 2 && wi < 16;
  const size_t base = ((size_t)(b * NH + head) * SK + (SK - WIN)) * DH;
  const rsrc_t rk = mk_rsrc((const u16*)a_kc + base, WIN * DH * 2), rv = mk_rsrc((const u16*)a_vc + base, WIN * DH * 2);
  int tau = te1 & 0xffff;
  const int tauw = te0 >> 24 << 7, wdef = te0 >> 20 & 15;
  int due = P.NF * 256, now = 0;
  auto gate = [&]() {
    while (now < due) { now = (int)(((u32)wall_clock64() - t0) << 8); if (now < due) __builtin_amdgcn_s_sleep(1); }
    due += tau;
  };
  auto until = [&](int t) { while ((int)(((u32)wall_clock64() - t0) << 8) < t); };
  const u32 pslot = dup(G_P + ((b * 32 + head) * 4 + part) * 256);
  const u32 die = __builtin_amdgcn_s_getreg(20 | (2 << 6));
  const bool voter = p3 && i8 == 1, reporter = p3 && i8 < 2;
  int vote = 0, lag = 0;
  const u32 pbo = dup(G_PB) + (xcd * 8 + wave) * 128;
  u32 near = 0;
  u32x2 ep; u32 xs0, xs1, tch1; u32x4 xr0, xr1;
  const rsrc_t rx = mk_rsrc(part == 2 ? cnd : x, (part == 1 || (part == 2 && has_w)) ? 2048u : 0u);
  auto small_ops = [&]() {
    ep = ld8<AUX_SC1>(wsr, wave == 7 ? WS_EPOCH : OOB, 0);
    xs0 = __builtin_amdgcn_raw_buffer_load_b16(rx, tid * 2, 0, 0); xs1 = __builtin_amdgcn_raw_buffer_load_b16(rx, tid * 2, 1024, 0);
    xr0 = ld16<0>(rx, part == 1 ? lane * 16 : OOB, 0); xr1 = ld16<0>(rx, part == 1 ? lane * 16 : OOB, 1024);
    tch1 = __builtin_amdgcn_raw_buffer_load_b32(mk_rsrc(w2 + (size_t)blk * 32 * 512, is_up ? 64u : 0u), tid == 384 ? 0 : OOB, 0, AUX_NT);
  };
  if (has_w) {
    if (modc) now = 1 << 30;
#pragma nounroll
    while (wleft > wdef) { gate(); due += tauw - tau; piece(); }
    now = 0;
  }
  due = max(due, P.TK * 256);
  SB();
  u32 sot = sa_ & 0xffc00u;
  {
    const u32 s1 = (bl0 + lane - 1 - (lane >= SK0 ? NL - nl : 0)) ^ (124 + part);
#pragma unroll
    for (int k = 0; k < 7; ++k) sot ^= (u32)-(int)(s1 >> k & 1u) & col(P, k);
    PIN(sot);
  }
  u32 tag = 0;
  u32x4 bf[8];
  auto wo_prep = [&](int s0 = 0, int s1 = 2) {
    const u32 wi_ = wimg + wave * 8192;
    const int q = (lane >> 2) & 3, pp = lane & 3;
#pragma unroll
    for (int s = s0; s < s1; ++s) {
#pragma unroll
      for (int t = 0; t < 4; ++t) {
        const int r0 = s * 32 + g * 8 + q, r1 = r0 + 4, ch = t * 2 + (pp >> 1);
        const u32x2 lo = tr_read(wi_ + r0 * 128 + ((ch ^ (r0 & 7)) * 16) + (pp & 1) * 8), hi = tr_read(wi_ + r1 * 128 + ((ch ^ (r1 & 7)) * 16) + (pp & 1) * 8);
        bf[s * 4 + t] = u32x4{lo[0], lo[1], hi[0], hi[1]};
      }
    }
  };
  auto gemv_go = [&](u32 hh) {
    sm.hb[tid] = (u16)hh; sm.hb[512 + tid] = (u16)(hh >> 16);
    WAVE_SYNC();
    const u32x4* a = (c & 11) == 0 ? reinterpret_cast<const u32x4*>(&sm.hb[(c >> 2) * 512 + wave * 64 + g * 8]) : &sm.zero[0];
    const u32x4 a0 = a[0], a1 = a[(c & 11) == 0 ? 4 : 0];
    const f32x4 z = {0.f, 0.f, 0.f, 0.f};
    f32x4 d[4];
#pragma unroll
    for (int t = 0; t < 4; ++t) d[t] = mfma16(a0, bf[t], z);
#pragma unroll
    for (int t = 0; t < 4; ++t) d[t] = mfma16(a1, bf[4 + t], d[t]);
    if (lane < 32) {
#pragma unroll
      for (int t = 0; t < 4; ++t) sm.r2[(t * 16 + c) * 20 + g * 8 + wave] = d[t][0];
    }
  };
  auto mod_stage = [&]() {
    wo_prep();
    gemv_go(pack_bf(silu_f(lo_f(xs0)), silu_f(lo_f(xs1))));
    __builtin_amdgcn_s_waitcnt(0xc07f);
    if (lane == 0) *LDSP(volatile u32, (u32)(uintptr_t)&sm.mflag[wave]) = 1u;
    if (wave == 7) {
#pragma nounroll
      for (int spin = 0; spin < SPIN_MAX; ++spin) {
        const u32 f = *LDSP(volatile u32, (u32)(uintptr_t)&sm.mflag[lane & 7]);
        if (all_ok(f != 0u)) break;
        __builtin_amdgcn_s_sleep(1);
      }
      CFENCE();
      st8_sc1(wsr, G_MOD + (blk * 64 + lane) * 8, pack_bf(sum8(&sm.r2[lane * 20]), sum8(&sm.r2[lane * 20 + 8])), tag);
    }
    STAMP(9);
  };
  if (lane < 4) sm.zero[lane] = u32x4{0u, 0u, 0u, 0u};
  if (part == 2 && has_w) {
    if (tid < 8) sm.mflag[tid] = 0u;
    LDS_BARRIER();
    if (__builtin_expect(modc, 0)) {
      small_ops();
      __builtin_amdgcn_raw_buffer_store_b128(u32x4{SENT, SENT, SENT, SENT}, wsr, G_U + wi * 10240 + tid * 16, 0, AUX_SC1);
      __builtin_amdgcn_raw_buffer_store_b128(u32x4{SENT, SENT, SENT, SENT}, wsr, tid < 128 ? G_U + wi * 10240 + tid * 16 : OOB, 8192, AUX_SC1);
      asm volatile("s_waitcnt vmcnt(0)" ::: "memory");
      tag = next_tag(__builtin_amdgcn_readfirstlane(ep[0]));
      mod_stage();
      __builtin_amdgcn_s_barrier();
    }
  }
  SB();
  const int fd = wi * 8 + (lane & 7);
  const int ub = wave >> 2, ud = (wave & 3) * 128 + c * 8 + g * 2;
  u32x4 sg[3]; u32 sx2;
  auto side_loads = [&]() {
    const bool fin = part == 1 && wave == 0;
    sg[0] = ld16<AUX_SC1>(wsr, is_up ? G_MOD + (1024 + ud) * 8 : (fin ? G_MOD + (1024 + fd) * 8 : OOB), 0);
    sg[1] = ld16<AUX_SC1>(wsr, is_up ? G_MOD + (1536 + ud) * 8 : (fin ? G_MOD + (2560 + fd) * 8 : OOB), 0);
    sg[2] = ld16<AUX_SC1>(wsr, is_up ? G_MOD + (2048 + ud) * 8 : OOB, 0);
    sx2 = __builtin_amdgcn_raw_buffer_load_b32(mk_rsrc(x, (is_up || fin) ? 2048u : 0u), is_up ? ub * 1024 + ud * 2 : (g & 1) * 1024 + (fd >> 1) * 4, 0, 0);
  };
  auto side_pin = [&]() { PIN(sg[0]); PIN(sg[1]); PIN(sg[2]); PIN(sx2); };
  const u32 mlo = G_ML + (head * 2 + b) * 64 + (lane & 3) * 16;
  float fmrg = 0.f;
  auto merge_factor = [&](const u32x4& ml) {
    const float mi = u2f(ml[0]);
    float M = fmaxf(mi, dpp<XOR1>(mi)); M = fmaxf(M, dpp<XOR2>(M));
    const float e = __expf(mi - M);
    float L = e * u2f(ml[2]);
    L += dpp<XOR1>(L); L += dpp<XOR2>(L);
    fmrg = rdl(e * __builtin_amdgcn_rcpf(L), part);
  };
  f32x2 ga = {0.f, 0.f}, xin = {0.f, 0.f}, cc1 = {0.f, 0.f};
  float Af = 0.f, Bf = 0.f;
  const int hpos = ub * 512 + ud;
  auto up_prep = [&]() {
    if (!all_ok((sg[0][1] == tag) & (sg[0][3] == tag) & (sg[1][1] == tag) & (sg[1][3] == tag) & (sg[2][1] == tag) & (sg[2][3] == tag))) gather16<3>(wsr, G_MOD + (1024 + ud) * 8, 512 * 8, tag, sg);
    ga = f32x2{ub ? hi_f(sg[0][0]) : lo_f(sg[0][0]), ub ? hi_f(sg[0][2]) : lo_f(sg[0][2])};
    cc1 = f32x2{rbf(1.f + (ub ? hi_f(sg[2][0]) : lo_f(sg[2][0]))), rbf(1.f + (ub ? hi_f(sg[2][2]) : lo_f(sg[2][2])))};
    xin = f32x2{lo_f(sx2), hi_f(sx2)};
    u32* hp = reinterpret_cast<u32*>(&sm.hb[hpos]);
    hp[512] = pack_bf(cc1[0], cc1[1]);
    hp[1024] = ub ? perm_hi(sg[1][0], sg[1][2]) : perm_lo(sg[1][0], sg[1][2]);
  };
  auto up_frags = [&]() {
    const int pp = lane & 3;
    const u32 a = wimg + (pp & 1) * 32768 + ((pp >> 1) * 32 + g * 8 + ((lane >> 2) & 3)) * 64 + (((wave >> 1) ^ ((g & 1) | (pp & 2))) * 16) + (wave & 1) * 8;
#pragma unroll
    for (int rb = 0; rb < 8; ++rb) {
      const u32x2 lo = tr_read(a + rb * 4096), hi = tr_read(a + rb * 4096 + 256);
      bf[rb] = u32x4{lo[0], lo[1], hi[0], hi[1]};
    }
  };
  const u32x4* ap = reinterpret_cast<const u32x4*>(&sm.hb[((lane >> 3) & 1) * 512 + (lane & 1) * 32 + g * 8]);
  const bool par = lane & 8;
  auto blog = [&](int) {};
  auto limit = [&](auto sc) {
    constexpr int s = sc.v;
    WAIT_VM(window(s, 1 + (PRM0.W[s >> 3] >> (4 * (s & 7)) & 7)));
    SB();
  };
  float l0 = 0.f, l1 = 0.f;
  u32x2 gmd[2], tbg;
  int t1 = 0;
#pragma unroll
  for (int i = SK0; i < SK1; ++i) kr[i] = u32x4{0u, 0u, 0u, 0u};
  const int first = SK0 + NL - nl;
  auto left_out = [&](int i) { int f = first; asm volatile("" : "+s"(f)); return i < f; };
  sfor<1, NL>([&](auto ic) {
    constexpr int i = ic.v;
    if constexpr (i >= SK0 && i < SK1) { if (left_out(i)) return true; }
    const u32 so = __builtin_amdgcn_readlane(sot, i);
    gate();
    blog(i);
    if constexpr (i >= RP0 && i < NL - 1) { if (reporter) lag += (int)(((u32)wall_clock64() - t0) << 8) - due + ((int)__builtin_amdgcn_s_getreg(7 | (3 << 11)) + 1) * tau; }
    SB();
    if constexpr (i >= SK0 && i < SK1) ld16_into(kr[i], rk, lane * 16, so); else kr[i] = ld16<AUX_NT>(rk, lane * 16, so);
    if constexpr (i == SMO) small_ops();
    if constexpr (i == SMO + 1) { if (voter) __builtin_amdgcn_raw_buffer_store_b32(0u, wsr, pbo + (lane >> 5) * 8192 + (lane & 31) * 4, 0, AUX_SC1); }
    if constexpr (i == TBL) tbg = ld8<AUX_SC1>(wsr, wave == 7 ? G_TB + xcd * 128 + (lane & 7) * 8 : OOB, 0);
    if constexpr (i == QP) { gmd[0] = ld8<AUX_SC1>(wsr, part == 1 ? G_MOD + tid * 8 : OOB, 0); gmd[1] = ld8<AUX_SC1>(wsr, part == 1 ? G_MOD + tid * 8 : OOB, 4096); }
    SB();
    SB();
    if (i >= 4) limit(ic);
    if (i == 1) STAMP(25);
    if (i == 8) STAMP(12);
    if (i == 20) STAMP(21);
    if (i == 32) STAMP(22);
    if (i == TBP) {
      if (part == 3 && i8 == 0 && wave == 7) { tag = next_tag(__builtin_amdgcn_readfirstlane(ep[0])); if (lane < 8) st8_sc1(wsr, G_TB + lane * 128 + xcd * 8, t0 - P.GS, tag); }
      SB();
    }
    if (i == PRB) {
      if (voter) {
        u64 ta, tb, tc; u32 dm;
        const u32 pa = pbo + (wave & 1) * 8192;
        asm volatile("s_memtime %0\n\ts_waitcnt lgkmcnt(0)\n\ts_load_dword %3, %4, %5\n\ts_waitcnt lgkmcnt(0)\n\ts_memtime %1\n\ts_waitcnt lgkmcnt(0)\n\t"
                     "s_load_dword %3, %4, %6\n\ts_waitcnt lgkmcnt(0)\n\ts_memtime %2\n\ts_waitcnt lgkmcnt(0)"
                     : "=&s"(ta), "=&s"(tb), "=&s"(tc), "=&s"(dm) : "s"(ws), "s"(pa), "s"(pa ^ 8192u));
        const int d = min(max((int)(2 * (u32)tb - (u32)ta - (u32)tc), -500), 500);
        vote = ((wave ^ die) & 1) ? -d : d;
        STAMP(26);
      }
      SB();
    }
    if (i == TBU) {
      if (wave == 7) {
        const bool ok = tbg[1] == tag;
        float lf = ok ? (float)min(max((int)(t0 - tbg[0]), -300), 300) : 0.f, nf = ok ? 1.f : 0.f;
        lf += dpp<XOR1>(lf); nf += dpp<XOR1>(nf); lf += dpp<XOR2>(lf); nf += dpp<XOR2>(nf); lf += dpp<XOR7>(lf); nf += dpp<XOR7>(nf);
        if (lane == 0) sm.bc = u32x2{tag, t0 - (u32)(int)(lf * __builtin_amdgcn_rcpf(fmaxf(nf, 1.f)))};
      }
      LDS_BARRIER();
      const u32x2 bc = sm.bc;
      tag = __builtin_amdgcn_readfirstlane(bc[0]);
      t1 = (__builtin_amdgcn_readfirstlane((int)(bc[1] - t0)) + TEND + P.X1) * 256;
      tau -= __builtin_amdgcn_readfirstlane((int)((float)(int)(t0 - bc[1]) * 256.f * __builtin_amdgcn_rcpf((float)(nl - TBU - 1))));
      SB();
    }
    if (i == NL - 1) {
      if (reporter) {
        if (lane == 0) sm.mflag[wave] = vote * 65536 + min(lag >> 8, 4000);
        LDS_BARRIER();
        if (wave == 7) {
          int s = sm.mflag[lane & 7];
          s += __builtin_amdgcn_mov_dpp(s, XOR1, 0xf, 0xf, true); s += __builtin_amdgcn_mov_dpp(s, XOR2, 0xf, 0xf, true); s += __builtin_amdgcn_mov_dpp(s, XOR7, 0xf, 0xf, true);
          if (lane < 8) st8_sc1(wsr, G_VT + lane * 128 + wi * 8, s, tag);
        }
      }
      SB();
    }
    if (i == LNS) {
      tag = next_tag(__builtin_amdgcn_readfirstlane(ep[0]));
      if (part == 1) {
        float x0[8], x1[8], mb0, ri0, mb1, ri1;
#pragma unroll
        for (int k = 0; k < 4; ++k) { x0[2 * k] = lo_f(xr0[k]); x0[2 * k + 1] = hi_f(xr0[k]); x1[2 * k] = lo_f(xr1[k]); x1[2 * k + 1] = hi_f(xr1[k]); }
        ln_stats2(x0, x1, mb0, ri0, mb1, ri1);
        l0 = rbf(rbf(lo_f(xs0) - mb0) * ri0); l1 = rbf(rbf(lo_f(xs1) - mb1) * ri1);
      }
      PIN(l0); PIN(l1);
      SB();
    }
    if (i == WUSE) {
      PIN(tch1); PIN(tch2);
      if ((part == 1 || (part == 2 && has_w && !modc))) {
        WAIT_VM(WK); CFENCE();
        if (part == 2) mod_stage(); else wo_prep();
      }
#pragma unroll
      for (int k = 0; k < 8; ++k) PIN(bf[k]);
      SB();
    }
    if (i == QP + QD) {
      if (part == 1) {
        u32 gm[2] = {gmd[0][0], gmd[1][0]};
        if (!all_ok((gmd[0][1] == tag) & (gmd[1][1] == tag))) { STAMP(11); gather8<2>(wsr, G_MOD + tid * 8, 512 * 8, tag, gm); }
        STAMP(5);
        gemv_go(pack_bf(rbf(l0 * rbf(1.f + lo_f(gm[1]))) + lo_f(gm[0]), rbf(l1 * rbf(1.f + hi_f(gm[1]))) + hi_f(gm[0])));
      }
      SB();
    }
    if (i == QP + QD + 1) {
      if (part == 1) {
        LDS_BARRIER();
        if (wave == 0) {
          const int bb = lane >> 5, jq = lane & 31;
          st8_sc1(wsr, G_Q + (bb * 2048 + blk * 32 + jq) * 8, pack_bf(sum8(&sm.r2[2 * jq * 20 + bb * 8]), sum8(&sm.r2[(2 * jq + 1) * 20 + bb * 8])), tag);
        }
        STAMP(6);
      }
      SB();
    }
    return true;
  });
  PIN(sot);
  tau = te1 >> 16;
  due += P.TG * 256;
  const u32 qoff = G_Q + ((b * 32 + head) * 64 + c * 4) * 8;
  u32x4 qg[2], mlg;
  u32 qq[4];
  constexpr int NB = (NL + 15) / 16;
  float xlo, sc[NB], pr[NB], m = 0.f;
  f32x4 acc = {0.f, 0.f, 0.f, 0.f};
  u32 pb[NB], pm[4], pl8[8], cpf;
  const u32 adst0 = lds_addr(lane < 32 ? &sm.red[wave][c * 8 + g * 4] : &sm.r2[(lane - 32) * 4]);
  u32 po, rdst, rsrc, xo;
  auto wo_addr = [&]() {
    po = dup(G_P + (((c >> 2 & 1) * 32 + xcd * 4 + (wave >> 1)) * 4 + (c & 3)) * 256 + (wave & 1) * 128 + (c >> 3) * 64 + g * 16) + ((P.NR & 2) ? near : (xcd & 2) * 4096);
    rdst = lane < 32 ? (u32)(uintptr_t)&sm.red2[wave][0] + lane * 16 : (u32)(uintptr_t)&sm.r2[0] + (lane - 32) * 16;
    rsrc = (u32)(uintptr_t)&sm.red2[0][((wave & 1) * 16 + (lane & 15)) * 4 + (lane >> 4)];
    xo = dup(G_XP + xcd * 2048 + (wave & 1) * 1024 + (blk * 64 + (lane & ~1)) * 2) + (lane & 1) * 8192;
    PIN(po); PIN(rdst); PIN(rsrc); PIN(xo);
  };
  u32 pso = wave == 0 ? pslot + c * 16 + (g & 1) * 8 + (g >> 1) * 8192 : OOB;
  PIN(pso);
  const u32 nadd = wdef ? P.LW : (is_up ? P.LU : 0);
  u32 nto = tid == 0 ? G_NT + bid * 8 : OOB;
  PIN(nto);
  auto notices = [&](auto nc_, u32 off, u32x4 (&gn)[nc_.v]) -> int {
    constexpr int N = nc_.v;
#pragma nounroll
    for (int spin = 0; spin < SPIN_MAX; ++spin) {
      bool ok = true;
#pragma unroll
      for (int i = 0; i < N; ++i) ok &= (gn[i][1] == tag) & (gn[i][3] == tag);
      if (all_ok(ok)) break;
      __builtin_amdgcn_s_sleep(10);
      CFENCE();
#pragma unroll
      for (int i = 0; i < N; ++i) gn[i] = ld16<AUX_SC1>(wsr, off, i * 1024);
    }
    int mx = 0;
#pragma unroll
    for (int i = 0; i < N; ++i) mx = max(mx, max((int)(gn[i][0] - t0), (int)(gn[i][2] - t0)));
    return __builtin_amdgcn_readfirstlane((int)wave_max((float)min(mx, 1 << 20)));
  };
  auto pv_row = [&](auto jc) {
    constexpr int i = jc.v;
    const u32 pv = __float_as_uint(bcast16(u2f(pb[i >> 4]), i & 15));
    acc = mfma16(u32x4{pv & pm[0], pv & pm[1], pv & pm[2], pv & pm[3]}, vr[i], acc);
    PIN(acc);
    return true;
  };
  sfor<0, NL - 1>([&](auto ic) {
    constexpr int v = ic.v;
    if constexpr (v >= SK0 && v < SK1) { if (left_out(v)) return true; }
    if constexpr (v > 0) {
      const u32 so = __builtin_amdgcn_readlane(sot, v);
      if constexpr (v >= SK1) { if (v == NL - P.NE) tau = (tau * P.FE) >> 4; }
      gate();
      blog(NL + v - 1);
      SB();
      if constexpr (v >= SK0 && v < SK1) ld16_into(vr[v], rv, lane * 16, so); else vr[v] = ld16<AUX_NT>(rv, lane * 16, so);
      SB();
      SB();
    }
    if constexpr (v == SK0 - 1) {
#pragma unroll
      for (int i = SK0; i < SK1; ++i) { vr[i] = u32x4{0u, 0u, 0u, 0u}; PIN(vr[i]); }
    }
    if constexpr (v == VQ - 4) { qg[0] = ld16<AUX_SC1>(wsr, qoff, 0); qg[1] = ld16<AUX_SC1>(wsr, qoff + 16, 0); }
    if constexpr (v == VSIDE) { side_loads(); tbg = ld8<AUX_SC1>(wsr, wave == 7 ? G_VT + xcd * 128 + c * 8 : OOB, 0); }
    if constexpr (v == CPF) {
      u64 pc; u32 dlt, len;
      asm volatile("s_getpc_b64 %0\n.Lcpf_here:\n\ts_mov_b32 %1, .Ltail_start-.Lcpf_here\n\ts_mov_b32 %2, .Lend_a-.Ltail_start\n\ts_max_i32 %2, %2, .Lend_b-.Ltail_start\n\ts_max_i32 %2, %2, .Lend_c-.Ltail_start\n\ts_max_i32 %2, %2, .Lend_d-.Ltail_start\n\ts_max_i32 %2, %2, .Lend_e-.Ltail_start" : "=s"(pc), "=s"(dlt), "=s"(len) :: "scc");
      pc += dlt;
      const rsrc_t rc = mk_rsrc((const void*)pc, (part == 3 && i8 == 1) ? len : 0u);
      cpf = __builtin_amdgcn_raw_buffer_load_b32(rc, tid * 64, 0, 0);
    }
    if constexpr (v == SK1 + 2) PIN(cpf);
    if constexpr (v == SK1) mlg = ld16<AUX_SC1>(wsr, wave == 0 ? mlo : OOB, 0);
    SB();
    if constexpr (NL + v < 72) limit(IC<NL + v>{});
    if constexpr (v >= SK0 + WV) { WAIT_VM(v < SK1 ? WV : WV + 1); SB(); }
    if constexpr (v == VQ) {
      if (!all_ok((qg[0][1] == tag) & (qg[0][3] == tag) & (qg[1][1] == tag) & (qg[1][3] == tag))) { STAMP(20); gather16<2>(wsr, qoff, 16, tag, qg); }
      STAMP(1); WSTAMP(1);
      qq[0] = qg[0][0]; qq[1] = qg[0][2]; qq[2] = qg[1][0]; qq[3] = qg[1][2];
    }
    if constexpr (v >= VQ && v < VS) {
      constexpr int sl = v - VQ, bt = sl >> 1;
      float xs[8];
#pragma unroll
      for (int i = 0; i < 8; ++i) {
        float d = 0.f;
        if (sl * 8 + i < NL) {
#pragma unroll
          for (int k = 0; k < 4; ++k) d = dot2(kr[sl * 8 + i][k], qq[k], d);
        }
        xs[i] = d;
      }
#pragma unroll
      for (int h = 0; h < 4; ++h) xs[h] = sel_add<XOR7>(xs[h], xs[h + 4], c & 4);
#pragma unroll
      for (int h = 0; h < 2; ++h) xs[h] = sel_add<XOR2>(xs[h], xs[h + 2], c & 2);
      xs[0] = sel_add<XOR1>(xs[0], xs[1], c & 1);
      if constexpr ((sl & 1) || sl == NSL - 1) {
        const float t = (sl & 1) ? sel_add<XOR8>(xlo, xs[0], c & 8) : xs[0] + dpp<XOR8>(xs[0]);
        const float s = rbf(rbf(t) * 0.08838834764831845f);
        sc[bt] = (bt * 16 + c < SK0 || (bt * 16 + c >= first && bt * 16 + c < NL)) ? s : -__builtin_inff();
        PIN(sc[bt]);
      } else {
        xlo = xs[0];
        PIN(xlo);
      }
    }
    if constexpr (v == VS) {
      float mloc = sc[0];
#pragma unroll
      for (int bt = 1; bt < NB; ++bt) mloc = fmaxf(mloc, sc[bt]);
      m = fmaxf(wave_max(mloc), -3.0e38f);
      float ls = 0.f;
#pragma unroll
      for (int bt = 0; bt < NB; ++bt) { pr[bt] = __expf(sc[bt] - m); ls += pr[bt]; }
      const float l = wave_sum(ls);
      if (lane == 0) { sm.ml[wave][0] = m; sm.ml[wave][1] = l; }
      if (c == ((NL - 1) & 15)) sm.pl[g][wave] = pr[NB - 1];
      STAMP(2); WSTAMP(2);
    }
    if constexpr (v >= SK1) pv_row(IC<v - SK1 + SK0 - WV>{});
    if constexpr (v < SK0 || v >= SK0 + WV) sfor<cons_end(v - 1), cons_end(v)>(pv_row);
    if constexpr (v == SK1 + 1) {
      float p[8];
#pragma unroll
      for (int w = 0; w < 8; ++w) p[w] = sm.pl[g][w] * __expf(sm.ml[w][0] - m);
#pragma unroll
      for (int w = 0; w < 8; ++w) pl8[w] = pack_bf(p[w], p[w]);
#pragma unroll
      for (int w = 0; w < 8; ++w) PIN(pl8[w]);
    }
    if constexpr (v == MLS - 1) { side_pin(); if (is_up) up_prep(); PIN(ga); PIN(xin); PIN(cc1); }
    if constexpr (v == UPREP - 1) wo_addr();
    if constexpr (v == UPREP) {
#pragma unroll
      for (int k = 0; k < 8; ++k) asm volatile("" : "=v"(bf[k]));
      if (part == 0) wo_prep(0, 1); else if (is_up) up_frags();
#pragma unroll
      for (int k = 0; k < 8; ++k) PIN(bf[k]);
    }
    if constexpr (v == UPREP + 1) {
      if (is_up) {
        const u32x4* a1 = ap + 128 + ((lane >> 1) & 1) * 128;
        f32x4 d0 = {0.f, 0.f, 0.f, 0.f}, d1 = {0.f, 0.f, 0.f, 0.f};
#pragma unroll
        for (int rb = 0; rb < 4; ++rb) { d0 = mfma16(a1[rb * 8], bf[rb], d0); d1 = mfma16(a1[(rb + 4) * 8], bf[rb + 4], d1); }
        Af = par ? d0[1] + d1[1] : d0[0] + d1[0]; Bf = par ? d0[3] + d1[3] : d0[2] + d1[2];
        Af += dpp<XOR8>(Af); Bf += dpp<XOR8>(Bf);
      }
      PIN(Af); PIN(Bf);
    }
    if (v == 10 || v == 16) STAMP(15 + v / 6);
    if (v == 20) STAMP(18);
    if (v == 32) STAMP(19);
    if constexpr (v == MLS) {
      if (wave == 7) {
        const bool ok = tbg[1] == tag;
        float vt = ok ? (float)((int)tbg[0] >> 16) : 0.f, lg = ok ? (float)(tbg[0] & 0xffffu) : 0.f, nf = ok ? 8.f * (NL - 1 - RP0) : 0.f;
        vt += dpp<XOR1>(vt); lg += dpp<XOR1>(lg); nf += dpp<XOR1>(nf); vt += dpp<XOR2>(vt); lg += dpp<XOR2>(lg); nf += dpp<XOR2>(nf);
        vt += dpp<XOR7>(vt); lg += dpp<XOR7>(lg); nf += dpp<XOR7>(nf); vt += dpp<XOR8>(vt); lg += dpp<XOR8>(lg); nf += dpp<XOR8>(nf);
        lg *= __builtin_amdgcn_rcpf(fmaxf(nf, 1.f));
        if (lane == 0) sm.bc = u32x2{__float_as_uint(vt), (u32)min(max((int)((lg - (float)P.A0) * (float)(P.AG * 32)), 0), P.AC * 256)};
      }
      LDS_BARRIER();
      {
        const u32x2 bc = sm.bc;
        near = ((__builtin_amdgcn_readfirstlane(bc[0]) >> 31 ^ die ^ P.NR ^ 1) & 1) * 8192u;
        t1 += __builtin_amdgcn_readfirstlane(bc[1]);
        tau += __builtin_amdgcn_readfirstlane((int)((float)(int)bc[1] * __builtin_amdgcn_rcpf((float)(nl - MLS - 3))));
      }
      float mm[8], ll[8];
#pragma unroll
      for (int w = 0; w < 8; ++w) { const f32x2 t = *reinterpret_cast<const f32x2*>(&sm.ml[w][0]); mm[w] = t[0]; ll[w] = t[1]; }
      float Mwg = mm[0], Lwg = 0.f;
#pragma unroll
      for (int w = 1; w < 8; ++w) Mwg = fmaxf(Mwg, mm[w]);
      if (wave == 0) {
#pragma unroll
        for (int w = 0; w < 8; ++w) Lwg = fmaf(__expf(mm[w] - Mwg), ll[w], Lwg);
        if (lane == 0) st16_sc1(wsr, G_ML + ((head * 2 + b) * 4 + part) * 16, __float_as_uint(Mwg), __float_as_uint(Lwg), tag);
      }
      const float fw = __expf(m - Mwg);
      m = Mwg;
#pragma unroll
      for (int bt = 0; bt < NB; ++bt) { pb[bt] = pack_bf(pr[bt] * fw, pr[bt] * fw); PIN(pb[bt]); }
#pragma unroll
      for (int k = 0; k < 4; ++k) { pm[k] = (c >> 1 & 3) == k ? ((c & 1) ? 0xffff0000u : 0x0000ffffu) : 0u; PIN(pm[k]); }
    }
    SB();
    return true;
  });
  asm volatile(".Ltail_start:");
  if (wave != 0) {
    pv_row(IC<SK0 - 1>{});
    WAIT_VM(WV); SB();
    sfor<NL - 1 - WV, NL - 1>(pv_row);
    *LDSP(f32x4, adst0) = acc;
    WSTAMP(14);
    LDS_BARRIER();
  } else {
    u32x4 xr[8], am[8];
#pragma unroll
    for (int w = 0; w < 8; ++w) am[w] = u32x4{pl8[w] & pm[0], pl8[w] & pm[1], pl8[w] & pm[2], pl8[w] & pm[3]};
#pragma unroll
    for (int w = 0; w < 8; ++w) PIN(am[w]);
    {
      const u32 so = __builtin_amdgcn_readlane(sot, NL - 1);
      gate();
      SB();
#pragma unroll
      for (int w = 0; w < 8; ++w) xr[w] = ld16<AUX_NT>(rv, lane * 16, so ^ ((w & 1) ? col(P, 7) : 0u) ^ ((w & 2) ? col(P, 8) : 0u) ^ ((w & 4) ? col(P, 9) : 0u));
      SB();
    }
    u32 rsum = lds_addr(&sm.red[0][c * 8 + (g & 1) * 4]);
    pv_row(IC<SK0 - 1>{});
    WAIT_VM(WV + 8); SB();
    sfor<NL - 1 - WV, NL - 3>(pv_row);
    if (__builtin_expect(!all_ok((mlg[1] == tag) & (mlg[3] == tag)), 0)) {
#pragma nounroll
      for (int spin = 0; spin < SPIN_MAX; ++spin) {
        CFENCE();
        mlg = ld16<AUX_SC1>(wsr, mlo, 0);
        if (all_ok((mlg[1] == tag) & (mlg[3] == tag))) break;
      }
    }
    merge_factor(mlg);
    sfor<NL - 3, NL - 1>(pv_row);
    STAMP(14); WSTAMP(14);
    LDS_BARRIER();
    STAMP(15);
    f32x4 t[8];
#pragma unroll
    for (int w = 1; w < 8; ++w) t[w] = LDSP(f32x4, rsum)[w * 32];
    acc = ((acc + t[1]) + (t[2] + t[3])) + ((t[4] + t[5]) + (t[6] + t[7]));
    PIN(acc);
    STAMP(3);
    SB();
#pragma unroll
    for (int w = 0; w < 8; ++w) acc = mfma16(am[w], xr[w], acc);
  }
  WAIT_VM(0);
  acc *= fmrg;
  __builtin_amdgcn_raw_buffer_store_b64(u32x2{min(pack_bf(acc[0], acc[1]), SENT - 1u), min(pack_bf(acc[2], acc[3]), SENT - 1u)}, wsr, pso, 0, AUX_SC1);
  st8_sc1(wsr, nto, (u32)wall_clock64() + nadd, tag);
  SB();
  STAMP(4);

  if (part == 2) FINISH();
  if (part == 0) {
    __builtin_amdgcn_s_barrier();
#pragma nounroll
    while (wleft > 0) piece();
    until(t1);
    STAMP(11);
    CFENCE();
    u32x4 og[2];
    og[0] = ld16<AUX_SC1>(wsr, po, 0);
    SB();
    WAIT_VM(1); CFENCE();
    wo_prep(1, 2);
#pragma unroll
    for (int k = 4; k < 8; ++k) PIN(bf[k]);
    SB();
    f32x4 d[4];
    auto wo_mm = [&]() {
      const f32x4 z = {0.f, 0.f, 0.f, 0.f};
#pragma unroll
      for (int t = 0; t < 4; ++t) { d[t] = mfma16(og[0], bf[t], z); og[1][t] = __float_as_uint(dpp<XOR8>(u2f(og[0][t]))); }
      SB();
#pragma unroll
      for (int t = 0; t < 4; ++t) d[t] = mfma16(og[1], bf[4 + t], d[t]);
    };
    auto p_bad = [&]() { return !all_ok(max(max(og[0][0], og[0][1]), max(og[0][2], og[0][3])) != SENT); };
    wo_mm();
    if (__builtin_expect(p_bad(), 0)) {
#pragma nounroll
      for (int spin = 0; spin < SPIN_MAX; ++spin) {
        __builtin_amdgcn_s_sleep(1);
        CFENCE();
        og[0] = ld16<AUX_SC1>(wsr, po, 0);
        if (!p_bad()) break;
      }
      wo_mm();
      SB();
      asm volatile(".Lend_d:");
    }
    STAMP(5); WSTAMP(5);
    {
      SB();
      f32x4 y; f32x2 h[4];
#pragma unroll
      for (int t = 0; t < 4; ++t) { h[t] = d[t].xy + d[t].zw; PIN(h[t]); }
#pragma unroll
      for (int t = 0; t < 4; ++t) { y[t] = h[t][0] + h[t][1]; PIN(y[t]); }
      *LDSP(f32x4, rdst) = y;
    }
    STAMP(16);
    LDS_BARRIER();
    STAMP(17);
    if (wave < 2) {
      f32x2 v[4];
#pragma unroll
      for (int w = 0; w < 4; ++w) v[w] = f32x2{LDSP(float, rsrc)[w * 256], LDSP(float, rsrc)[w * 256 + 128]};
      const f32x2 h = (v[0] + v[1]) + (v[2] + v[3]);
      const float y = h[0] + h[1];
      __builtin_amdgcn_raw_buffer_store_b32(min(pack_bf(dpp<0xA0>(y), dpp<0xF5>(y)), SENT - 1u), wsr, xo, 0, AUX_SC1);
      asm volatile(".Lend_a:");
    }
    STAMP(6);
    FINISH();
  }
  if (part == 3) {
    if (!has_w) FINISH();
    u32x4 b2[4];
    {
      __builtin_amdgcn_s_barrier();
      const rsrc_t r2 = mk_rsrc(w2 + (size_t)(blk * 32 + wave * 4) * 512, 4096u);
      const u32 l2 = (u32)(uintptr_t)(&sm.w2b[wave * 4][0]);
#pragma unroll
      for (int n = 0; n < 4; ++n) dma16(r2, (lane ^ (n * 2) ^ ((wave & 2) << 2)) * 16, n * 1024, l2 + n * 1024);
    }
    const u32 tdst = lds_addr(&sm.hb[hpos]);
    const u32 sdst = lds_addr(&sm.stat[wave][lane]);
    const u32 ssrc = lds_addr(&sm.stat[(g >> 1) * 4 + ((lane >> 1) & 3)][(lane & 1) * 16]);
    const u32 adst = lds_addr((c < 4 && (g & 1) == 0) ? &sm.actl[g >> 1][wave * 4 + c] : reinterpret_cast<u16*>(&sm.dummy[lane]));
    const u32 asrc = lds_addr(&sm.actl[lane & 1][g * 8]);
    const u32 apa = lds_addr(&sm.hb[(c >> 3) * 512 + (c & 3) * 64 + ((c >> 2) & 1) * 32 + g * 8]);
    const u32 xs2 = (P.NR & 4) ? 16384 : 16384 + 8192;
    u32 xoff = dup(G_XP) + ((P.NR & 4) ? near : 0) + g * 2048 + ub * 1024 + (wave & 3) * 256 + c * 16;
    u32x4 one;
#pragma unroll
    for (int k = 0; k < 4; ++k) one[k] = ((c & 2) == 0 && (c >> 2) == k) ? ((c & 1) ? 0x3f800000u : 0x00003f80u) : 0u;
    PIN(one);
    u32 ooff = dup(G_OP + (blk * 512 + tid) * 8);
    float Afp = Af, Bfp = Bf;
    PIN(xoff); PIN(ooff); PIN(Afp); PIN(Bfp);
    PIN(ga); PIN(xin); PIN(cc1);
    u32 w2a = (u32)(uintptr_t)(&sm.w2b[0][0]) + (g * 8 + ((lane >> 2) & 3)) * 1024 + (wave ^ (g & 1)) * 128 + (lane & 15) * 8;
    PIN(w2a);
    if (wave == 1) {
      due = t1 + P.N1 * 256; gate();
      u32x4 gn[2] = {ld16<AUX_SC1>(wsr, G_NT + lane * 16, 0), ld16<AUX_SC1>(wsr, G_NT + lane * 16, 1024)};
      const int tn = notices(IC<2>{}, G_NT + lane * 16, gn) * 256;
      due = (tn > t1 + 2 * 256 ? max(tn, t1 + P.L0 * 256) + P.L1 * 256 : t1) + P.X2 * 256;
      until(due);
    }
    asm volatile("s_waitcnt vmcnt(0)" ::: "memory");
    STAMP(9);
    __builtin_amdgcn_s_barrier();
    STAMP(11);
    CFENCE();
    u32x4 xg[2] = {ld16<AUX_SC1>(wsr, xoff, 0), ld16<AUX_SC1>(wsr, xoff, xs2)};
    SB();
#pragma unroll
    for (int t = 0; t < 4; ++t) {
      const u32x2 lo = tr_read(w2a ^ (t * 32)), hi = tr_read((w2a ^ (t * 32)) + 4096);
      b2[t] = u32x4{lo[0], lo[1], hi[0], hi[1]};
    }
#pragma unroll
    for (int t = 0; t < 4; ++t) PIN(b2[t]);
    SB();
    const f32x4 z4 = {0.f, 0.f, 0.f, 0.f};
    auto xp_bad = [&]() { return !all_ok(max(max(max(xg[0][0], xg[0][1]), max(xg[0][2], xg[0][3])), max(max(xg[1][0], xg[1][1]), max(xg[1][2], xg[1][3]))) != SENT); };
    f32x4 v = mfma16(one, xg[1], mfma16(one, xg[0], z4));
    if (__builtin_expect(xp_bad(), 0)) {
#pragma nounroll
      for (int spin = 0; spin < SPIN_MAX; ++spin) {
        __builtin_amdgcn_s_sleep(1);
        CFENCE();
        xg[0] = ld16<AUX_SC1>(wsr, xoff, 0); xg[1] = ld16<AUX_SC1>(wsr, xoff, xs2);
        if (!xp_bad()) break;
      }
      v = mfma16(one, xg[1], mfma16(one, xg[0], z4));
      SB();
      asm volatile(".Lend_e:");
    }
    STAMP(5); WSTAMP(5);
    {
      const float a0 = v[0], a1 = v[1];
      const float x10 = fmaf(ga[0], a0, xin[0]), x11 = fmaf(ga[1], a1, xin[1]);
      *LDSP(u32, tdst) = pack_bf(x10 * cc1[0], x11 * cc1[1]);
      float u = swap32_add(x10 + x11, fmaf(x10, x10, x11 * x11));
      u += dpp<XOR1>(u); u += dpp<XOR2>(u); u += dpp<XOR7>(u); u += dpp<XOR8>(u);
      *LDSP(float, sdst) = u;
    }
    STAMP(16);
    LDS_BARRIER();
    STAMP(17);
    {
      const f32x4 z = {0.f, 0.f, 0.f, 0.f};
      const u32x4 af0 = LDSP(u32x4, apa)[0], af1 = LDSP(u32x4, apa)[32];
      float s = LDSP(float, ssrc)[0], q = LDSP(float, ssrc)[32];
      SB();
      f32x4 d[4];
#pragma unroll
      for (int j = 0; j < 4; ++j) d[j] = mfma16(af0, bf[j], z);
#pragma unroll
      for (int j = 0; j < 4; ++j) d[j] = mfma16(af1, bf[4 + j], d[j]);
      s += dpp<XOR1>(s); q += dpp<XOR1>(q); s += dpp<XOR2>(s); q += dpp<XOR2>(q); s += dpp<XOR7>(s); q += dpp<XOR7>(q);
      const float mean = s * (1.f / 512.f);
      const float var = fmaf(q, 1.f / 512.f, -mean * mean);
      const float ri = __builtin_amdgcn_rsqf(fmaxf(var, 0.f) + 1e-5f);
      const float cf = fmaf(-ri * mean, Afp, Bfp);
      float y = (d[0][0] + d[1][1]) + (d[2][2] + d[3][3]);
      y = __builtin_bit_cast(float, __builtin_amdgcn_update_dpp(__builtin_bit_cast(int, y), __builtin_bit_cast(int, y), XOR8, 0xa, 0xf, false));
      y = swap16_add(y, y);
      y = fmaf(y, ri, cf);
      const float up = dpp<0x104>(y);
      *LDSP(u16, adst) = (u16)pack_bf(silu_f(y) * up, 0.f);
    }
    STAMP(18);
    LDS_BARRIER();
    STAMP(10);
    {
      const u32x4 af = *LDSP(u32x4, asrc);
      const f32x4 z = {0.f, 0.f, 0.f, 0.f};
      const f32x4 d0 = mfma16(af, b2[0], z), d1 = mfma16(af, b2[1], z), d2 = mfma16(af, b2[2], z), d3 = mfma16(af, b2[3], z);
      const bool g1 = g & 1, g2 = g & 2;
      const float e0 = g1 ? d1[0] : d0[0], e1 = g1 ? d1[1] : d0[1], f0 = g1 ? d3[0] : d2[0], f1 = g1 ? d3[1] : d2[1];
      const u32x2 ov = {pack_bf(g2 ? f0 : e0, g2 ? f1 : e1), tag};
      __builtin_amdgcn_raw_buffer_store_b64(ov, wsr, ooff, near ^ 8192u, AUX_SC1);
      __builtin_amdgcn_raw_buffer_store_b64(ov, wsr, ooff, near, AUX_SC1);
    }
    asm volatile(".Lend_b:");
    STAMP(6);
    FINISH();
  }
  if (wave == 0) {
    const int d = fd, ul = lane >> 3;
    u32 gm[2] = {sg[0][0], sg[1][0]};
    if (!all_ok((sg[0][1] == tag) & (sg[1][1] == tag))) gather8<2>(wsr, G_MOD + (1024 + d) * 8, 1536 * 8, tag, gm);
    u32 xa0, xa1;
    {
      due = t1 + P.N2 * 256; gate();
      u32x4 gn[2] = {ld16<AUX_SC1>(wsr, G_NT + lane * 16, 0), ld16<AUX_SC1>(wsr, G_NT + lane * 16, 1024)};
      const int tn = notices(IC<2>{}, G_NT + lane * 16, gn) * 256;
      due = (tn > t1 + 2 * 256 ? max(tn, t1 + P.L0 * 256) + P.L1 * 256 : t1) + P.X3 * 256;
      until(due);
    }
#pragma nounroll
    for (int spin = 0; spin < SPIN_MAX; ++spin) {
      CFENCE();
      xa0 = __builtin_amdgcn_raw_buffer_load_b32(wsr, dup(G_XP + ul * 2048) + near + (d >> 1) * 4, 0, AUX_SC1);
      xa1 = __builtin_amdgcn_raw_buffer_load_b32(wsr, dup(G_XP + ul * 2048) + near + (d >> 1) * 4, 1024, AUX_SC1);
      if (all_ok(max(xa0, xa1) != SENT)) break;
      __builtin_amdgcn_s_sleep(1);
    }
    float a0 = (d & 1) ? hi_f(xa0) : lo_f(xa0), a1 = (d & 1) ? hi_f(xa1) : lo_f(xa1);
    a0 += dpp<XOR8>(a0); a1 += dpp<XOR8>(a1);
    a0 = swap16_add(a0, a0); a1 = swap16_add(a1, a1);
    a0 = swap32_add(a0, a0); a1 = swap32_add(a1, a1);
    const bool hb = g & 1;
    float xv = rbf(((d & 1) ? hi_f(sx2) : lo_f(sx2)) + rbf((hb ? hi_f(gm[0]) : lo_f(gm[0])) * rbf(hb ? a1 : a0)));
    float gv = hb ? hi_f(gm[1]) : lo_f(gm[1]);
    PIN(xv); PIN(gv);
    if (wi == 0 && lane == 0) __builtin_amdgcn_raw_buffer_store_b32(tag, wsr, WS_EPOCH, 0, AUX_SC1);
    STAMP(10);
    due = max(due + P.X4 * 256, (int)(((u32)wall_clock64() - t0) << 8) + P.GX * 256);
    const bool last = ul + 40 < NUP;
    u32 o0 = dup(G_OP + (ul * 512 + d) * 8) + ((P.NR & 8) ? near : (ul & 2) * 4096), o5 = last ? o0 : OOB, tg5 = last ? tag : 0u;
    const rsrc_t rout = mk_rsrc(out, 2048u);
    u32 oo = (lane & 40) == 0 ? (g * 512 + d) * 2 : OOB;
    const u32 pk = c == 0 ? 0x00003f80u : (c == 4 ? 0x3f800000u : 0u);
    u32x4 one = {pk, 0u, pk, 0u};
    PIN(o0); PIN(o5); PIN(tg5); PIN(oo); PIN(one);
    __builtin_amdgcn_raw_buffer_store_b16((u16)pack_bf(xv, 0.f), rout, oo, 0, AUX_SC1);
    until(due);
    f32x4 sv;
#pragma nounroll
    for (int spin = 0; spin < SPIN_MAX; ++spin) {
      CFENCE();
      u32x2 og[6];
#pragma unroll
      for (int k = 0; k < 5; ++k) og[k] = ld8<AUX_SC1>(wsr, o0, k * 65536);
      og[5] = ld8<AUX_SC1>(wsr, o5, 5 * 65536);
      sv = f32x4{0.f, 0.f, 0.f, 0.f};
#pragma unroll
      for (int k = 0; k < 3; ++k) sv = mfma16(one, u32x4{og[2 * k][0], og[2 * k][1], og[2 * k + 1][0], og[2 * k + 1][1]}, sv);
      const u32 bad = ((og[0][1] ^ tag) | (og[1][1] ^ tag) | (og[2][1] ^ tag)) | ((og[3][1] ^ tag) | (og[4][1] ^ tag) | (og[5][1] ^ tg5));
      PIN(sv);
      if (all_ok(bad == 0u)) break;
      __builtin_amdgcn_s_sleep(1);
    }
    STAMP(7);
    const float s = sv[0] + dpp<XOR8>(sv[0]);
    __builtin_amdgcn_raw_buffer_store_b16((u16)pack_bf(fmaf(gv, s, xv), 0.f), rout, oo, 0, AUX_SC1);
    asm volatile(".Lend_c:");
  }
  FINISH();
}

extern "C" void kernel_launch(void* const* d_in, const int* in_sizes, int n_in,
                              void* d_out, int out_size, void* d_ws, size_t ws_size,
                              hipStream_t stream) {
  fused_kernel<<<256, 512, 0, stream>>>((const u16*)d_in[1], (const u16*)d_in[2], (const u16*)d_in[4], (const u16*)d_in[3], (const u16*)d_in[10], (const u16*)d_in[6], (const u16*)d_in[7],
                                        (unsigned char*)d_ws, (const u16*)d_in[0], (const u16*)d_in[9], (const u16*)d_in[8], (u16*)d_out);
}
```

```cpp
#include <hip/hip_runtime.h>
#include <stdint.h>

typedef unsigned int u32;
typedef unsigned short u16;
typedef unsigned long long u64;
typedef __attribute__((ext_vector_type(4))) unsigned u32x4;
typedef __attribute__((ext_vector_type(2))) unsigned u32x2;
typedef __attribute__((ext_vector_type(2))) __bf16 bf16x2;
typedef __attribute__((ext_vector_type(4))) float f32x4;
typedef __attribute__((ext_vector_type(2))) float f32x2;
typedef __amdgpu_buffer_rsrc_t rsrc_t;

constexpr int NH = 32, SK = 16384, DH = 128, WIN = 4096, FF = 1376;
struct Prm { int E0, ES, E1, E2, E2C, EU, TEND, DT_UP, L0, L1, X1, X2, X3, X4, NE, FE, M0, M1, M2, M3, M4, GS, GX, N1, N2, GA, WD, DT_WO, LW, LU, EW, PM, NF, NR, C0, A0, AG, AC, TS, TW, TK, TG, W[9]; };
constexpr Prm PRM0 = {32, 20, 32, 32, 54, 58, 1980, 60, 5, 24, 90, 76, 114, 78, 3, 8, 0x2511f55b, 0x2b7425d5, 0x6017665, 0x17e0b9ec, 0x31944305, 0, 32, 18, 58, 0, 4, 100, 45, 15, 0, 0x0, 15, 14, 5, 75, 0, 100, 509, 236, 60, 20, 0x77777777, 0x77777777, 0x77777777, 0x77777777, 0x77777777, 0x44444477, 0x44444444, 0x44444444, 0x44444444};
constexpr int NL = 37;
constexpr int SK0 = 21, SK1 = 32;
constexpr int NCMIN = (NL - SK1 + SK0) * 8;
constexpr int WK = 8, WV = 5;
constexpr int WUSE = 12, LNS = 7;
constexpr int QP = 14, QD = 4;
constexpr int VQ = 6;
constexpr int NSL = (NL + 7) / 8;
constexpr int VS = VQ + NSL;
constexpr int MLS = VS + 2;
constexpr int VSIDE = MLS - 5, UPREP = MLS + 6;
constexpr int CPF = 17;
constexpr int SMO = 1;
constexpr int RP0 = 24;
constexpr int PRB = 8;
constexpr int TBP = 4, TBL = 11, TBU = 15;
constexpr int window(int s, int w) {
  int n = w;
  for (int u = s - w + 1; u <= s; ++u) n += u == SMO ? 6 : (u == TBL || u == NL + CPF || u == NL + SK1 ? 1 : (u == QP || u == NL + VQ - 4 ? 2 : (u == NL + VSIDE ? 5 : 0)));
  return n;
}
constexpr int cons_end(int v) {
  const int a = v - WV + 1, c = (v - MLS) * 4;
  const int m = a < c ? a : c;
  return m < 0 ? 0 : (m > NL ? NL : m);
}
constexpr u32 col(const Prm& P, int j) { const int m[6] = {P.M0, P.M1, P.M2, P.M3, P.M4, P.PM}; return (u32)(m[j / 3] >> (j % 3 * 10) & 1023) << 10; }
constexpr u32 first_col(const Prm& P, int k) {
  if (k < 4) return col(P, 12 + k) | 1u << (24 + k);
  if (k < 6) return col(P, k - 4);
  if (k < 8) return col(P, 4 + k) | 1u << (16 + k);
  if (k < 11) return col(P, k - 1);
  return (col(P, 2) ^ col(P, 3) ^ col(P, 4) ^ col(P, 5) ^ col(P, 6) ^ col(P, 10) ^ col(P, 11)) | 0xf00000u;
}
constexpr bool one_to_one(const Prm& P) {
  u32 c[10] = {};
  for (int j = 0; j < 10; ++j) c[j] = col(P, j);
  for (int j = 0; j < 10; ++j) {
    if (c[j] == 0) return false;
    const u32 low = c[j] & (0u - c[j]);
    for (int k = j + 1; k < 10; ++k) if (c[k] & low) c[k] ^= c[j];
  }
  return true;
}
static_assert(one_to_one(PRM0), "every KB of the window exactly once");
struct Tab { u32 e[512]; };
constexpr Tab make_tab(const Prm& P) {
  Tab T{};
  for (int bid = 0; bid < 256; ++bid) {
    const int xcd = bid & 7, part = bid >> 4 & 3, i8 = (bid >> 3 & 1) | bid >> 6 << 1, wi = i8 * 8 + xcd;
    const bool has_mod = wi < 48, has_up = wi >= 21;
    const int cb0 = (i8 + P.C0) & 7, cb1 = (i8 + 4) & 7, ms = wi - 20;
    const int mi = wi < 8 ? wi : (wi < 16 ? 8 + ((xcd + 4) & 7) : (wi < 20 ? (wi - 14) * 8 + 3 : 16 + ms / 7 * 8 + ms % 7 + (ms % 7 >= 3 ? 1 : 0)));
    const int ui = xcd * 5 + (xcd > 5 ? xcd - 5 : 0) + (i8 >= 3 ? (xcd >= 5 ? 1 : 0) + i8 - 3 : 0);
    const int wd = cb0 == 3 ? 0 : P.WD;
    const int e[4] = {P.E0 - 4 * wd + (cb0 == 3 ? P.ES : 0), P.E1 + (cb1 == 3 ? P.ES : 0), has_mod ? (wi < 16 ? P.E2C : P.E2) + ((mi & 7) == 3 ? P.ES : 0) : 0, has_up ? P.EU : 0};
    int tw[4] = {}, win[4] = {}, span[4] = {};
    for (int k = 0; k < 4; ++k) {
      const int np = k == 0 ? 8 - wd : 8;
      tw[k] = np ? e[k] * P.TW / (16 * np) : 0;
      span[k] = P.TEND - (P.NF + tw[k] * np / 2 > P.TK ? P.NF + tw[k] * np / 2 : P.TK) - P.TG - (k == 0 && wd ? P.DT_WO : (k == 3 && has_up ? P.DT_UP : 0));
      win[k] = span[k] - (k == 0 && wd ? P.EW : (k == 3 && has_up ? P.GA : 0));
    }
    int n[4] = {NCMIN / 8, NCMIN / 8, NCMIN / 8, NCMIN / 8};
    for (int left = 128 - NCMIN / 2; left > 0; --left) {
      int best = -1;
      for (int k = 3; k >= 0; --k)
        if (n[k] < NL && (best < 0 || n[k] * win[best] < n[best] * win[k])) best = k;
      ++n[best];
    }
    const int c[4] = {0, n[0] - 1, n[0] + n[1] - 2, n[0] + n[1] + n[2] - 3};
    const int nl = n[part];
    const bool has_w = part < 2 || (part == 2 ? has_mod : has_up);
    const int blk = !has_w ? 0 : (part == 0 ? cb0 : (part == 1 ? xcd * 8 + cb1 : (part == 2 ? mi : ui)));
    T.e[2 * bid] = (u32)(c[part] | nl << 7 | blk << 13 | (has_w ? 1 : 0) << 19 | (part == 0 ? wd : 0) << 20) | (u32)tw[part] << 24;
    T.e[2 * bid + 1] = (u32)(span[part] * P.TS / (4 * (nl - 1)) | span[part] * (1024 - P.TS) / (4 * (nl - 1)) << 16);
  }
  return T;
}
constexpr int step_range(const Prm& P, bool mx_) {
  int mx = 0, mn = 1 << 20;
  const Tab T = make_tab(P);
  for (int bid = 0; bid < 256; ++bid) { const int n = T.e[2 * bid] >> 7 & 63; mx = n > mx ? n : mx; mn = n < mn ? n : mn; }
  return mx_ ? mx : mn;
}
extern "C" __attribute__((used, visibility("protected"))) __constant__ const Tab wg_tab = make_tab(PRM0);
static_assert(step_range(PRM0, true) <= NL && step_range(PRM0, false) >= NCMIN / 8, "a wave's key range does not fit its load steps");
static_assert(UPREP + 1 < SK0 && QP + QD + 1 < SK0 && cons_end(SK0 - 1) == SK0 - WV && NL - SK1 == WV, "steps every wave runs");
constexpr int NUP = FF / 32;
constexpr int SPIN_MAX = 1 << 17;

constexpr u32 WS_EPOCH = 0;
constexpr u32 G_TB = 1024;
constexpr u32 G_NT = 2048;
constexpr u32 G_MOD = 4096;
constexpr u32 G_Q = G_MOD + 3072 * 8;
constexpr u32 G_ML = G_Q + 4096 * 8;
constexpr u32 G_U = G_ML + 256 * 16;
constexpr u32 G_P = 0;
constexpr u32 G_XP = G_P + 256 * 256;
constexpr u32 G_OP = G_XP + 8 * 512 * 4;
constexpr u32 G_PB = G_OP + 44 * 512 * 8;
constexpr u32 G_VT = G_U + 2 * (G_PB + 8192);
constexpr u32 WS_END = G_VT + 1024;
__device__ __forceinline__ u32 dup(u32 o) { return G_U + ((o >> 13) << 14) + (o & 8191u); }
constexpr u32 SENT = 0xffffffffu;
constexpr u32 OOB = 0x40000000u;
#define WST 0

constexpr int AUX_NT = 2, AUX_SC1 = 16;

__device__ __forceinline__ float lo_f(u32 v) { return __uint_as_float(v << 16); }
__device__ __forceinline__ float hi_f(u32 v) { return __uint_as_float(v & 0xffff0000u); }
__device__ __forceinline__ float rbf(float x) { return (float)(__bf16)x; }
__device__ __forceinline__ u32 pack_bf(float lo, float hi) {
  bf16x2 r; r[0] = (__bf16)lo; r[1] = (__bf16)hi; return __builtin_bit_cast(u32, r);
}
__device__ __forceinline__ float dot2(u32 k, u32 q, float acc) {
  return __builtin_amdgcn_fdot2_f32_bf16(__builtin_bit_cast(bf16x2, k), __builtin_bit_cast(bf16x2, q), acc, false);
}
template <int CTRL> __device__ __forceinline__ float dpp(float x) {
  return __builtin_bit_cast(float, __builtin_amdgcn_mov_dpp(__builtin_bit_cast(int, x), CTRL, 0xf, 0xf, true));
}
constexpr int XOR1 = 0xB1, XOR2 = 0x4E, XOR7 = 0x141, XOR8 = 0x128;
__device__ __forceinline__ float swap32_add(float a, float b) {
  auto t = __builtin_amdgcn_permlane32_swap(__float_as_uint(a), __float_as_uint(b), false, false);
  return __uint_as_float(t[0]) + __uint_as_float(t[1]);
}
__device__ __forceinline__ float swap16_add(float a, float b) {
  auto t = __builtin_amdgcn_permlane16_swap(__float_as_uint(a), __float_as_uint(b), false, false);
  return __uint_as_float(t[0]) + __uint_as_float(t[1]);
}
__device__ __forceinline__ float wave_sum(float x) {
  x += dpp<XOR1>(x); x += dpp<XOR2>(x); x += dpp<XOR7>(x); x += dpp<XOR8>(x);
  x = swap16_add(x, x);
  return swap32_add(x, x);
}
__device__ __forceinline__ float wave_max(float x) {
  x = fmaxf(x, dpp<XOR1>(x)); x = fmaxf(x, dpp<XOR2>(x)); x = fmaxf(x, dpp<XOR7>(x)); x = fmaxf(x, dpp<XOR8>(x));
  auto s = __builtin_amdgcn_permlane16_swap(__float_as_uint(x), __float_as_uint(x), false, false);
  x = fmaxf(__uint_as_float(s[0]), __uint_as_float(s[1]));
  auto t = __builtin_amdgcn_permlane32_swap(__float_as_uint(x), __float_as_uint(x), false, false);
  return fmaxf(__uint_as_float(t[0]), __uint_as_float(t[1]));
}
template <int CTRL> __device__ __forceinline__ float sel_add(float lo, float hi, bool up) {
  const float t1 = lo + dpp<CTRL>(lo), t2 = hi + dpp<CTRL>(hi);
  return up ? t2 : t1;
}
template <int I> __device__ __forceinline__ float bc16(float x) {
  return __builtin_bit_cast(float, __builtin_amdgcn_update_dpp(0, __builtin_bit_cast(int, x), 0x150 + I, 0xf, 0xf, false));
}
__device__ __forceinline__ float bcast16(float x, int i) {
  switch (i) {
    case 0: return bc16<0>(x); case 1: return bc16<1>(x); case 2: return bc16<2>(x); case 3: return bc16<3>(x);
    case 4: return bc16<4>(x); case 5: return bc16<5>(x); case 6: return bc16<6>(x); case 7: return bc16<7>(x);
    case 8: return bc16<8>(x); case 9: return bc16<9>(x); case 10: return bc16<10>(x); case 11: return bc16<11>(x);
    case 12: return bc16<12>(x); case 13: return bc16<13>(x); case 14: return bc16<14>(x); default: return bc16<15>(x);
  }
}
__device__ __forceinline__ float silu_f(float x) { return x * __builtin_amdgcn_rcpf(1.f + __expf(-x)); }

template <int AUX> __device__ __forceinline__ u32x4 ld16(rsrc_t r, u32 voff, u32 soff) {
  return __builtin_amdgcn_raw_buffer_load_b128(r, voff, soff, AUX);
}
template <int AUX> __device__ __forceinline__ u32x2 ld8(rsrc_t r, u32 voff, u32 soff) {
  return __builtin_amdgcn_raw_buffer_load_b64(r, voff, soff, AUX);
}
__device__ __forceinline__ void st8_sc1(rsrc_t r, u32 voff, u32 v, u32 tag) {
  __builtin_amdgcn_raw_buffer_store_b64(u32x2{v, tag}, r, voff, 0, AUX_SC1);
}
__device__ __forceinline__ void st16_sc1(rsrc_t r, u32 voff, u32 v0, u32 v1, u32 tag) {
  __builtin_amdgcn_raw_buffer_store_b128(u32x4{v0, tag, v1, tag}, r, voff, 0, AUX_SC1);
}
__device__ __forceinline__ rsrc_t mk_rsrc(const void* p, u32 bytes) {
  return __builtin_amdgcn_make_buffer_rsrc(const_cast<void*>(p), 0, bytes, 0x00020000);
}
#define WAIT_VM(n) __builtin_amdgcn_s_waitcnt(((n) & 15) | (((n) >> 4) << 14) | (7 << 4) | (15 << 8))
#define SB() __builtin_amdgcn_sched_barrier(0)
#define CFENCE() asm volatile("" ::: "memory")
__device__ __forceinline__ bool all_ok(bool ok) { return __builtin_amdgcn_ballot_w64(!ok) == 0ull; }

template <int N> __device__ __forceinline__ void gather16(rsrc_t ws, u32 voff, u32 stride, u32 tag, u32x4 (&g)[N]) {
#pragma nounroll
  for (int spin = 0; spin < SPIN_MAX; ++spin) {
    CFENCE();
    bool ok = true;
#pragma unroll
    for (int i = 0; i < N; ++i) g[i] = ld16<AUX_SC1>(ws, voff + i * stride, 0);
#pragma unroll
    for (int i = 0; i < N; ++i) ok &= (g[i][1] == tag) & (g[i][3] == tag);
    if (all_ok(ok)) break;
    __builtin_amdgcn_s_sleep(8);
  }
}
template <int N> __device__ __forceinline__ void gather8(rsrc_t ws, u32 voff, u32 stride, u32 tag, u32 (&g)[N], bool act = true) {
#pragma nounroll
  for (int spin = 0; spin < SPIN_MAX; ++spin) {
    CFENCE();
    bool ok = true;
    u32x2 t[N];
#pragma unroll
    for (int i = 0; i < N; ++i) t[i] = ld8<AUX_SC1>(ws, voff + i * stride, 0);
#pragma unroll
    for (int i = 0; i < N; ++i) { ok &= (t[i][1] == tag); g[i] = t[i][0]; }
    if (all_ok(ok | !act)) break;
    __builtin_amdgcn_s_sleep(8);
  }
}

struct Smem {
  float red[8][128];
  float ml[8][2];
  float r2[64 * 20];
  u16 hb[3 * 1024];
  float stat[8][64];
  float red2[8][128];
  u32 dummy[64];
  u32x2 bc;
  u32x4 zero[4];
  float pl[4][8];
  u32 mflag[8];
  u32x4 wbuf[8][8][64];
  __attribute__((aligned(256))) u16 w2b[32][512];
  u16 actl[2][32];
};

#define STAMP(k) do {} while (0)
#define WSTAMP(k) do {} while (0)
#define FINISH() return
#define PIN(x) asm volatile("" : "+v"(x))
#define LDS_BARRIER() do { __builtin_amdgcn_s_waitcnt(0xc07f); __builtin_amdgcn_s_barrier(); CFENCE(); } while (0)
#define WAVE_SYNC() do { __builtin_amdgcn_fence(__ATOMIC_RELEASE, "wavefront"); __builtin_amdgcn_wave_barrier(); __builtin_amdgcn_fence(__ATOMIC_ACQUIRE, "wavefront"); } while (0)

__device__ __forceinline__ void dma16(rsrc_t r, u32 voff, u32 soff, u32 lds_dst) {
  u32 keep;
  asm volatile("s_mov_b32 %0, m0\n\ts_mov_b32 m0, %4\n\ts_nop 4\n\tbuffer_load_dwordx4 %1, %2, %3 offen nt lds\n\ts_mov_b32 m0, %0"
               : "=&s"(keep) : "v"(voff), "s"(r), "s"(soff), "s"(lds_dst) : "memory");
}
__device__ __forceinline__ void ld16_into(u32x4& d, rsrc_t r, u32 voff, u32 soff) {
  asm volatile("s_nop 4\n\tbuffer_load_dwordx4 %0, %1, %2, %3 offen nt" : "+v"(d) : "v"(voff), "s"(r), "s"(soff) : "memory");
}
__device__ __forceinline__ u32 perm_lo(u32 a, u32 b) { return __builtin_amdgcn_perm(b, a, 0x05040100u); }
__device__ __forceinline__ u32 perm_hi(u32 a, u32 b) { return __builtin_amdgcn_perm(b, a, 0x07060302u); }
__device__ __forceinline__ float rdl(float x, int l) { return __int_as_float(__builtin_amdgcn_readlane(__float_as_int(x), l)); }

typedef __attribute__((ext_vector_type(8))) __bf16 bf16x8;
__device__ __forceinline__ f32x4 mfma16(u32x4 a, u32x4 b, f32x4 c) {
  return __builtin_amdgcn_mfma_f32_16x16x32_bf16(__builtin_bit_cast(bf16x8, a), __builtin_bit_cast(bf16x8, b), c, 0, 0, 0);
}
__device__ __forceinline__ float sum8(const float* p) {
  const f32x4 a = *reinterpret_cast<const f32x4*>(p), b = *reinterpret_cast<const f32x4*>(p + 4);
  return ((a[0] + b[0]) + (a[1] + b[1])) + ((a[2] + b[2]) + (a[3] + b[3]));
}
__device__ __forceinline__ float ln_ri(float var) {
  return __builtin_amdgcn_rcpf(rbf(__builtin_amdgcn_sqrtf(rbf(rbf(fmaxf(var, 0.f)) + 1e-5f))));
}
__device__ __forceinline__ void ln_stats2(const float (&x0)[8], const float (&x1)[8], float& mb0, float& ri0, float& mb1, float& ri1) {
  const float k0 = rdl(x0[0], 0), k1 = rdl(x1[0], 0);
  float s0 = 0.f, s1 = 0.f, q0 = 0.f, q1 = 0.f;
#pragma unroll
  for (int k = 0; k < 8; ++k) {
    const float d0 = x0[k] - k0, d1 = x1[k] - k1;
    s0 += d0; s1 += d1; q0 = fmaf(d0, d0, q0); q1 = fmaf(d1, d1, q1);
  }
  float t = swap16_add(swap32_add(s0, s1), swap32_add(q0, q1));
  t += dpp<XOR1>(t); t += dpp<XOR2>(t); t += dpp<XOR7>(t); t += dpp<XOR8>(t);
  auto u = __builtin_amdgcn_permlane16_swap(__float_as_uint(t), __float_as_uint(t), false, false);
  const float ms = __uint_as_float(u[0]) * (1.f / 512.f), mq = __uint_as_float(u[1]) * (1.f / 512.f);
  const float ri = ln_ri(fmaf(-ms, ms, mq));
  mb0 = rbf(k0 + rdl(ms, 0)); mb1 = rbf(k1 + rdl(ms, 32));
  ri0 = rdl(ri, 0); ri1 = rdl(ri, 32);
}
template <int I> struct IC { static constexpr int v = I; };
template <int I, int N, class F> __device__ __forceinline__ void sfor(F&& f) {
  if constexpr (I < N) { if (f(IC<I>{})) sfor<I + 1, N>(f); }
}
typedef __attribute__((ext_vector_type(4))) short s16x4;
typedef __attribute__((address_space(3))) s16x4* lds_s16x4p;
__device__ __forceinline__ u32x2 tr_read(u32 lds_addr) {
  return __builtin_bit_cast(u32x2, __builtin_amdgcn_ds_read_tr16_b64_v4i16((lds_s16x4p)lds_addr));
}
__device__ __forceinline__ u32 next_tag(u32 e) { return ((e | 0x40004000u) + 1u) & ~0x40004000u; }
__device__ __forceinline__ float u2f(u32 v) { return __uint_as_float(v); }
#define LDSP(T, a) ((__attribute__((address_space(3))) T*)(a))
template <class T> __device__ __forceinline__ u32 lds_addr(T* p) { u32 a = (u32)(uintptr_t)p; PIN(a); return a; }

#define TAB_LOAD "s_add_u32 s30, s28, wg_tab@rel32@lo+4\ns_addc_u32 s31, s29, wg_tab@rel32@hi+12\nglobal_load_dwordx2 %[te], %[va], s[30:31]\n"
#define FC(k) ".Lfc" #k
#define FC_REGS
#define FX(r, bit, k) "s_bitcmp1_b32 %[" #r "], " #bit "\ns_cselect_b32 %[sb], " FC(k) ", 0\ns_xor_b32 %[sa], %[sa], %[sb]\n"
__global__ void __launch_bounds__(512, 2) fused_kernel(const u16* kc_, const u16* vc_, const u16* wo_, const u16* wq_, const u16* wmod_, const u16* w1_, const u16* w3_,
                                                       unsigned char* ws_, const u16* x_, const u16* cnd_, const u16* w2_, u16* out_) {
  __shared__ __attribute__((aligned(256))) Smem sm;
  const u64 a_ka = (u64)__builtin_amdgcn_kernarg_segment_ptr();
  const int bid = blockIdx.x, tid = threadIdx.x;
  u64 wa, t64; u32x2 te; int tid0;
  u32 ar[14];
  u32 tch2, sa_, sb_, va_;
  u32x4 kr[NL], vr[NL];
  asm volatile(".set .Lfc0, %0\n.set .Lfc1, %1\n.set .Lfc2, %2\n.set .Lfc3, %3\n.set .Lfc4, %4\n.set .Lfc5, %5\n.set .Lfc6, %6\n.set .Lfc7, %7\n.set .Lfc8, %8\n.set .Lfc9, %9\n.set .Lfc10, %10\n.set .Lfc11, %11"
    :: "n"(first_col(PRM0, 0)), "n"(first_col(PRM0, 1)), "n"(first_col(PRM0, 2)), "n"(first_col(PRM0, 3)), "n"(first_col(PRM0, 4)), "n"(first_col(PRM0, 5)),
       "n"(first_col(PRM0, 6)), "n"(first_col(PRM0, 7)), "n"(first_col(PRM0, 8)), "n"(first_col(PRM0, 9)), "n"(first_col(PRM0, 10)), "n"(first_col(PRM0, 11)));
  asm volatile(
    "s_load_dwordx16 s[4:19], %[ka], 0x0\ns_load_dwordx8 s[20:27], %[ka], 0x40\n"
    "s_memrealtime %[t]\n"
    "v_lshlrev_b32 %[va], 3, %[bid]\n"
    "s_getpc_b64 s[28:29]\n.Lcpf_first:\n"
    TAB_LOAD
    "s_cmp_gt_u32 %[bid], 15\ns_cbranch_scc1 .Lno_cpf\n"
    "s_mov_b32 %[sa], .Lend_a-.Lcpf_first\ns_max_i32 %[sa], %[sa], .Lend_b-.Lcpf_first\ns_max_i32 %[sa], %[sa], .Lend_c-.Lcpf_first\ns_max_i32 %[sa], %[sa], .Lend_d-.Lcpf_first\ns_max_i32 %[sa], %[sa], .Lend_e-.Lcpf_first\n"
    "v_lshlrev_b32 %[va], 6, %[tid]\nv_min_u32 %[va], %[sa], %[va]\nglobal_load_dword %[pf], %[va], s[28:29]\n"
    ".Lno_cpf:\n"
    "v_readfirstlane_b32 %[t0], %[tid]\nv_and_b32 %[va], 63, %[tid]\n"
    "s_mov_b32 %[sa], " FC(11) "\n"
    FX(bid, 0, 0) FX(bid, 1, 1) FX(bid, 2, 2) FX(bid, 3, 3) FX(bid, 4, 4) FX(bid, 5, 5) FX(bid, 6, 6) FX(bid, 7, 7) FX(t0, 6, 8) FX(t0, 7, 9) FX(t0, 8, 10)
    "v_lshl_add_u32 %[va], %[va], 4, %[sa]\n"
    "s_waitcnt lgkmcnt(0)\nglobal_load_dwordx4 %[k0], %[va], s[4:5] nt\nglobal_load_dwordx4 %[v0], %[va], s[6:7] nt\n"
    "s_bitcmp1_b32 %[t0], 6\ns_cselect_b64 s[14:15], s[16:17], s[14:15]\n"
    "s_bitcmp1_b32 %[bid], 4\ns_cselect_b64 s[8:9], s[10:11], s[8:9]\ns_cselect_b64 s[12:13], s[14:15], s[12:13]\ns_bitcmp1_b32 %[bid], 5\ns_cselect_b64 %[wa], s[12:13], s[8:9]"
    : "=&{s4}"(ar[0]), "=&{s5}"(ar[1]), "=&{s6}"(ar[2]), "=&{s7}"(ar[3]), "=&{s18}"(ar[4]), "=&{s19}"(ar[5]), "=&{s20}"(ar[6]), "=&{s21}"(ar[7]), "=&{s22}"(ar[8]), "=&{s23}"(ar[9]),
      "=&{s24}"(ar[10]), "=&{s25}"(ar[11]), "=&{s26}"(ar[12]), "=&{s27}"(ar[13]), [wa] "=&s"(wa), [t0] "=&s"(tid0), [t] "=&s"(t64), [te] "=&v"(te), [sa] "=&s"(sa_), [sb] "=&s"(sb_),
      [va] "=&v"(va_), [pf] "=&v"(tch2), [k0] "=&v"(kr[0]), [v0] "=&v"(vr[0])
    : [ka] "s"(a_ka), [bid] "s"(bid), [tid] "v"(tid) : "s8", "s9", "s10", "s11", "s12", "s13", "s14", "s15", "s16", "s17", "s28", "s29", "s30", "s31", "scc", "memory" FC_REGS);
  asm volatile("" : "+s"(ar[0]), "+s"(ar[1]), "+s"(ar[2]), "+s"(ar[3]), "+s"(ar[4]), "+s"(ar[5]), "+s"(ar[6]), "+s"(ar[7]), "+s"(ar[8]), "+s"(ar[9]), "+s"(ar[10]), "+s"(ar[11]), "+s"(ar[12]), "+s"(ar[13]));
  asm volatile("" : "+s"(wa), "+s"(t64), "+s"(tid0), "+s"(sa_));
  const u32 tka = (u32)wall_clock64();
  const u32 t0 = tka;
  __builtin_assume(tid >= 0 && tid < 512 && tid0 >= 0 && tid0 < 512 && bid >= 0 && bid < 256);
  constexpr Prm P = PRM0;
  const int TEND = P.TEND;
  const int xcd = bid & 7, part = bid >> 4 & 3;
  const int lane = tid & 63, wave = tid0 >> 6;
  SB();
  const bool p3 = part == 3;
  u32 pitch = (u32)(0x005600c001000020ull >> (part * 16) & 0xffff) * 32u;
  const int sh = p3 ? 2 : 3;
  u32 wv0 = (u32)((p3 ? (wave >> 1) * 128 : wave * 64) + (part == 0 ? xcd * 512 : 0) + (lane >> sh)) * pitch + ((lane & (p3 ? 3 : 7)) ^ (lane >> (p3 ? 5 : 3))) * 16u;
  const u32 wimg = (u32)(uintptr_t)(&sm.wbuf[0][0][0]);
  u32 wsz = (part == 0 ? 4096u : 512u) * pitch, wld = wimg + (p3 ? (wave & 1) * 32768 + (wave >> 1) * 8192 : wave * 8192);
  PIN(wv0);
  asm volatile("" : "+s"(wsz), "+s"(wld), "+s"(pitch));
  auto karg = [&](int i) { return ar[2 * i] | (u64)ar[2 * i + 1] << 32; };
  const u64 a_kc = karg(0), a_vc = karg(1);
  unsigned char* ws = (unsigned char*)karg(2);
  const u16 *x = (const u16*)karg(3), *cnd = (const u16*)karg(4), *w2 = (const u16*)karg(5);
  u16* out = (u16*)karg(6);
  const rsrc_t wsr = mk_rsrc(ws, WS_END);
  asm volatile("s_waitcnt vmcnt(2)" : "+v"(te));
  const u32 te0 = __builtin_amdgcn_readfirstlane(te[0]), te1 = __builtin_amdgcn_readfirstlane(te[1]);
  const int bl0 = te0 & 127, nl = te0 >> 7 & 63, blk = te0 >> 13 & 63;
  const bool has_w = te0 >> 19 & 1;
  const rsrc_t rw = mk_rsrc((const void*)wa, wsz);
  const u32 wstep = pitch << (6 - sh), wflip = p3 ? 32u : 0u;
  u32 wso = blk << (sh + 4);
  int wleft = 8;
  auto piece = [&]() {
    dma16(rw, wv0, wso, wld);
    wso += wstep; wld += 1024;
    wv0 ^= (wleft & 1) ? wflip : 0u;
    --wleft;
  };
  SB();
  STAMP(23);
  const int b = bid >> 3 & 1, head = xcd * 4 + (bid >> 6 ^ 3);
  const int g = lane >> 4, c = lane & 15;
  const int i8 = b | bid >> 6 << 1, wi = i8 * 8 + xcd;
  const bool is_up = p3 && has_w, modc = part == 2 && wi < 16;
  const size_t base = ((size_t)(b * NH + head) * SK + (SK - WIN)) * DH;
  const rsrc_t rk = mk_rsrc((const u16*)a_kc + base, WIN * DH * 2), rv = mk_rsrc((const u16*)a_vc + base, WIN * DH * 2);
  int tau = te1 & 0xffff;
  const int tauw = te0 >> 24 << 7, wdef = te0 >> 20 & 15;
  int due = P.NF * 256, now = 0;
  auto gate = [&]() {
    while (now < due) { now = (int)(((u32)wall_clock64() - t0) << 8); if (now < due) __builtin_amdgcn_s_sleep(1); }
    due += tau;
  };
  auto until = [&](int t) { while ((int)(((u32)wall_clock64() - t0) << 8) < t); };
  const u32 pslot = dup(G_P + ((b * 32 + head) * 4 + part) * 256);
  const u32 die = __builtin_amdgcn_s_getreg(20 | (2 << 6));
  const bool voter = p3 && i8 == 1, reporter = p3 && i8 < 2;
  int vote = 0, lag = 0;
  const u32 pbo = dup(G_PB) + (xcd * 8 + wave) * 128;
  u32 near = 0;
  u32x2 ep; u32 xs0, xs1, tch1; u32x4 xr0, xr1;
  const rsrc_t rx = mk_rsrc(part == 2 ? cnd : x, (part == 1 || (part == 2 && has_w)) ? 2048u : 0u);
  auto small_ops = [&]() {
    ep = ld8<AUX_SC1>(wsr, wave == 7 ? WS_EPOCH : OOB, 0);
    xs0 = __builtin_amdgcn_raw_buffer_load_b16(rx, tid * 2, 0, 0); xs1 = __builtin_amdgcn_raw_buffer_load_b16(rx, tid * 2, 1024, 0);
    xr0 = ld16<0>(rx, part == 1 ? lane * 16 : OOB, 0); xr1 = ld16<0>(rx, part == 1 ? lane * 16 : OOB, 1024);
    tch1 = __builtin_amdgcn_raw_buffer_load_b32(mk_rsrc(w2 + (size_t)blk * 32 * 512, is_up ? 64u : 0u), tid == 384 ? 0 : OOB, 0, AUX_NT);
  };
  if (has_w) {
    if (modc) now = 1 << 30;
#pragma nounroll
    while (wleft > wdef) { gate(); due += tauw - tau; piece(); }
    now = 0;
  }
  due = max(due, P.TK * 256);
  SB();
  u32 sot = sa_ & 0xffc00u;
  {
    const u32 s1 = (bl0 + lane - 1 - (lane >= SK0 ? NL - nl : 0)) ^ (124 + part);
#pragma unroll
    for (int k = 0; k < 7; ++k) sot ^= (u32)-(int)(s1 >> k & 1u) & col(P, k);
    PIN(sot);
  }
  u32 tag = 0;
  u32x4 bf[8];
  auto wo_prep = [&](int s0 = 0, int s1 = 2) {
    const u32 wi_ = wimg + wave * 8192;
    const int q = (lane >> 2) & 3, pp = lane & 3;
#pragma unroll
    for (int s = s0; s < s1; ++s) {
#pragma unroll
      for (int t = 0; t < 4; ++t) {
        const int r0 = s * 32 + g * 8 + q, r1 = r0 + 4, ch = t * 2 + (pp >> 1);
        const u32x2 lo = tr_read(wi_ + r0 * 128 + ((ch ^ (r0 & 7)) * 16) + (pp & 1) * 8), hi = tr_read(wi_ + r1 * 128 + ((ch ^ (r1 & 7)) * 16) + (pp & 1) * 8);
        bf[s * 4 + t] = u32x4{lo[0], lo[1], hi[0], hi[1]};
      }
    }
  };
  auto gemv_go = [&](u32 hh) {
    sm.hb[tid] = (u16)hh; sm.hb[512 + tid] = (u16)(hh >> 16);
    WAVE_SYNC();
    const u32x4* a = (c & 11) == 0 ? reinterpret_cast<const u32x4*>(&sm.hb[(c >> 2) * 512 + wave * 64 + g * 8]) : &sm.zero[0];
    const u32x4 a0 = a[0], a1 = a[(c & 11) == 0 ? 4 : 0];
    const f32x4 z = {0.f, 0.f, 0.f, 0.f};
    f32x4 d[4];
#pragma unroll
    for (int t = 0; t < 4; ++t) d[t] = mfma16(a0, bf[t], z);
#pragma unroll
    for (int t = 0; t < 4; ++t) d[t] = mfma16(a1, bf[4 + t], d[t]);
    if (lane < 32) {
#pragma unroll
      for (int t = 0; t < 4; ++t) sm.r2[(t * 16 + c) * 20 + g * 8 + wave] = d[t][0];
    }
  };
  auto mod_stage = [&]() {
    wo_prep();
    gemv_go(pack_bf(silu_f(lo_f(xs0)), silu_f(lo_f(xs1))));
    __builtin_amdgcn_s_waitcnt(0xc07f);
    if (lane == 0) *LDSP(volatile u32, (u32)(uintptr_t)&sm.mflag[wave]) = 1u;
    if (wave == 7) {
#pragma nounroll
      for (int spin = 0; spin < SPIN_MAX; ++spin) {
        const u32 f = *LDSP(volatile u32, (u32)(uintptr_t)&sm.mflag[lane & 7]);
        if (all_ok(f != 0u)) break;
        __builtin_amdgcn_s_sleep(1);
      }
      CFENCE();
      st8_sc1(wsr, G_MOD + (blk * 64 + lane) * 8, pack_bf(sum8(&sm.r2[lane * 20]), sum8(&sm.r2[lane * 20 + 8])), tag);
    }
    STAMP(9);
  };
  if (lane < 4) sm.zero[lane] = u32x4{0u, 0u, 0u, 0u};
  if (part == 2 && has_w) {
    if (tid < 8) sm.mflag[tid] = 0u;
    LDS_BARRIER();
    if (__builtin_expect(modc, 0)) {
      small_ops();
      __builtin_amdgcn_raw_buffer_store_b128(u32x4{SENT, SENT, SENT, SENT}, wsr, G_U + wi * 10240 + tid * 16, 0, AUX_SC1);
      __builtin_amdgcn_raw_buffer_store_b128(u32x4{SENT, SENT, SENT, SENT}, wsr, tid < 128 ? G_U + wi * 10240 + tid * 16 : OOB, 8192, AUX_SC1);
      asm volatile("s_waitcnt vmcnt(0)" ::: "memory");
      tag = next_tag(__builtin_amdgcn_readfirstlane(ep[0]));
      mod_stage();
      __builtin_amdgcn_s_barrier();
    }
  }
  SB();
  const int fd = wi * 8 + (lane & 7);
  const int ub = wave >> 2, ud = (wave & 3) * 128 + c * 8 + g * 2;
  u32x4 sg[3]; u32 sx2;
  auto side_loads = [&]() {
    const bool fin = part == 1 && wave == 0;
    sg[0] = ld16<AUX_SC1>(wsr, is_up ? G_MOD + (1024 + ud) * 8 : (fin ? G_MOD + (1024 + fd) * 8 : OOB), 0);
    sg[1] = ld16<AUX_SC1>(wsr, is_up ? G_MOD + (1536 + ud) * 8 : (fin ? G_MOD + (2560 + fd) * 8 : OOB), 0);
    sg[2] = ld16<AUX_SC1>(wsr, is_up ? G_MOD + (2048 + ud) * 8 : OOB, 0);
    sx2 = __builtin_amdgcn_raw_buffer_load_b32(mk_rsrc(x, (is_up || fin) ? 2048u : 0u), is_up ? ub * 1024 + ud * 2 : (g & 1) * 1024 + (fd >> 1) * 4, 0, 0);
  };
  auto side_pin = [&]() { PIN(sg[0]); PIN(sg[1]); PIN(sg[2]); PIN(sx2); };
  const u32 mlo = G_ML + (head * 2 + b) * 64 + (lane & 3) * 16;
  float fmrg = 0.f;
  auto merge_factor = [&](const u32x4& ml) {
    const float mi = u2f(ml[0]);
    float M = fmaxf(mi, dpp<XOR1>(mi)); M = fmaxf(M, dpp<XOR2>(M));
    const float e = __expf(mi - M);
    float L = e * u2f(ml[2]);
    L += dpp<XOR1>(L); L += dpp<XOR2>(L);
    fmrg = rdl(e * __builtin_amdgcn_rcpf(L), part);
  };
  f32x2 ga = {0.f, 0.f}, xin = {0.f, 0.f}, cc1 = {0.f, 0.f};
  float Af = 0.f, Bf = 0.f;
  const int hpos = ub * 512 + ud;
  auto up_prep = [&]() {
    if (!all_ok((sg[0][1] == tag) & (sg[0][3] == tag) & (sg[1][1] == tag) & (sg[1][3] == tag) & (sg[2][1] == tag) & (sg[2][3] == tag))) gather16<3>(wsr, G_MOD + (1024 + ud) * 8, 512 * 8, tag, sg);
    ga = f32x2{ub ? hi_f(sg[0][0]) : lo_f(sg[0][0]), ub ? hi_f(sg[0][2]) : lo_f(sg[0][2])};
    cc1 = f32x2{rbf(1.f + (ub ? hi_f(sg[2][0]) : lo_f(sg[2][0]))), rbf(1.f + (ub ? hi_f(sg[2][2]) : lo_f(sg[2][2])))};
    xin = f32x2{lo_f(sx2), hi_f(sx2)};
    u32* hp = reinterpret_cast<u32*>(&sm.hb[hpos]);
    hp[512] = pack_bf(cc1[0], cc1[1]);
    hp[1024] = ub ? perm_hi(sg[1][0], sg[1][2]) : perm_lo(sg[1][0], sg[1][2]);
  };
  auto up_frags = [&]() {
    const int pp = lane & 3;
    const u32 a = wimg + (pp & 1) * 32768 + ((pp >> 1) * 32 + g * 8 + ((lane >> 2) & 3)) * 64 + (((wave >> 1) ^ ((g & 1) | (pp & 2))) * 16) + (wave & 1) * 8;
#pragma unroll
    for (int rb = 0; rb < 8; ++rb) {
      const u32x2 lo = tr_read(a + rb * 4096), hi = tr_read(a + rb * 4096 + 256);
      bf[rb] = u32x4{lo[0], lo[1], hi[0], hi[1]};
    }
  };
  const u32x4* ap = reinterpret_cast<const u32x4*>(&sm.hb[((lane >> 3) & 1) * 512 + (lane & 1) * 32 + g * 8]);
  const bool par = lane & 8;
  auto blog = [&](int) {};
  auto limit = [&](auto sc) {
    constexpr int s = sc.v;
    WAIT_VM(window(s, 1 + (PRM0.W[s >> 3] >> (4 * (s & 7)) & 7)));
    SB();
  };
  float l0 = 0.f, l1 = 0.f;
  u32x2 gmd[2], tbg;
  int t1 = 0;
#pragma unroll
  for (int i = SK0; i < SK1; ++i) kr[i] = u32x4{0u, 0u, 0u, 0u};
  const int first = SK0 + NL - nl;
  auto left_out = [&](int i) { int f = first; asm volatile("" : "+s"(f)); return i < f; };
  sfor<1, NL>([&](auto ic) {
    constexpr int i = ic.v;
    if constexpr (i >= SK0 && i < SK1) { if (left_out(i)) return true; }
    const u32 so = __builtin_amdgcn_readlane(sot, i);
    gate();
    blog(i);
    if constexpr (i >= RP0 && i < NL - 1) { if (reporter) lag += (int)(((u32)wall_clock64() - t0) << 8) - due + ((int)__builtin_amdgcn_s_getreg(7 | (3 << 11)) + 1) * tau; }
    SB();
    if constexpr (i >= SK0 && i < SK1) ld16_into(kr[i], rk, lane * 16, so); else kr[i] = ld16<AUX_NT>(rk, lane * 16, so);
    if constexpr (i == SMO) small_ops();
    if constexpr (i == SMO + 1) { if (voter) __builtin_amdgcn_raw_buffer_store_b32(0u, wsr, pbo + (lane >> 5) * 8192 + (lane & 31) * 4, 0, AUX_SC1); }
    if constexpr (i == TBL) tbg = ld8<AUX_SC1>(wsr, wave == 7 ? G_TB + xcd * 128 + (lane & 7) * 8 : OOB, 0);
    if constexpr (i == QP) { gmd[0] = ld8<AUX_SC1>(wsr, part == 1 ? G_MOD + tid * 8 : OOB, 0); gmd[1] = ld8<AUX_SC1>(wsr, part == 1 ? G_MOD + tid * 8 : OOB, 4096); }
    SB();
    SB();
    if (i >= 4) limit(ic);
    if (i == 1) STAMP(25);
    if (i == 8) STAMP(12);
    if (i == 20) STAMP(21);
    if (i == 32) STAMP(22);
    if (i == TBP) {
      if (part == 3 && i8 == 0 && wave == 7) { tag = next_tag(__builtin_amdgcn_readfirstlane(ep[0])); if (lane < 8) st8_sc1(wsr, G_TB + lane * 128 + xcd * 8, t0 - P.GS, tag); }
      SB();
    }
    if (i == PRB) {
      if (voter) {
        u64 ta, tb, tc; u32 dm;
        const u32 pa = pbo + (wave & 1) * 8192;
        asm volatile("s_memtime %0\n\ts_waitcnt lgkmcnt(0)\n\ts_load_dword %3, %4, %5\n\ts_waitcnt lgkmcnt(0)\n\ts_memtime %1\n\ts_waitcnt lgkmcnt(0)\n\t"
                     "s_load_dword %3, %4, %6\n\ts_waitcnt lgkmcnt(0)\n\ts_memtime %2\n\ts_waitcnt lgkmcnt(0)"
                     : "=&s"(ta), "=&s"(tb), "=&s"(tc), "=&s"(dm) : "s"(ws), "s"(pa), "s"(pa ^ 8192u));
        const int d = min(max((int)(2 * (u32)tb - (u32)ta - (u32)tc), -500), 500);
        vote = ((wave ^ die) & 1) ? -d : d;
        STAMP(26);
      }
      SB();
    }
    if (i == TBU) {
      if (wave == 7) {
        const bool ok = tbg[1] == tag;
        float lf = ok ? (float)min(max((int)(t0 - tbg[0]), -300), 300) : 0.f, nf = ok ? 1.f : 0.f;
        lf += dpp<XOR1>(lf); nf += dpp<XOR1>(nf); lf += dpp<XOR2>(lf); nf += dpp<XOR2>(nf); lf += dpp<XOR7>(lf); nf += dpp<XOR7>(nf);
        if (lane == 0) sm.bc = u32x2{tag, t0 - (u32)(int)(lf * __builtin_amdgcn_rcpf(fmaxf(nf, 1.f)))};
      }
      LDS_BARRIER();
      const u32x2 bc = sm.bc;
      tag = __builtin_amdgcn_readfirstlane(bc[0]);
      t1 = (__builtin_amdgcn_readfirstlane((int)(bc[1] - t0)) + TEND + P.X1) * 256;
      tau -= __builtin_amdgcn_readfirstlane((int)((float)(int)(t0 - bc[1]) * 256.f * __builtin_amdgcn_rcpf((float)(nl - TBU - 1))));
      SB();
    }
    if (i == NL - 1) {
      if (reporter) {
        if (lane == 0) sm.mflag[wave] = vote * 65536 + min(lag >> 8, 4000);
        LDS_BARRIER();
        if (wave == 7) {
          int s = sm.mflag[lane & 7];
          s += __builtin_amdgcn_mov_dpp(s, XOR1, 0xf, 0xf, true); s += __builtin_amdgcn_mov_dpp(s, XOR2, 0xf, 0xf, true); s += __builtin_amdgcn_mov_dpp(s, XOR7, 0xf, 0xf, true);
          if (lane < 8) st8_sc1(wsr, G_VT + lane * 128 + wi * 8, s, tag);
        }
      }
      SB();
    }
    if (i == LNS) {
      tag = next_tag(__builtin_amdgcn_readfirstlane(ep[0]));
      if (part == 1) {
        float x0[8], x1[8], mb0, ri0, mb1, ri1;
#pragma unroll
        for (int k = 0; k < 4; ++k) { x0[2 * k] = lo_f(xr0[k]); x0[2 * k + 1] = hi_f(xr0[k]); x1[2 * k] = lo_f(xr1[k]); x1[2 * k + 1] = hi_f(xr1[k]); }
        ln_stats2(x0, x1, mb0, ri0, mb1, ri1);
        l0 = rbf(rbf(lo_f(xs0) - mb0) * ri0); l1 = rbf(rbf(lo_f(xs1) - mb1) * ri1);
      }
      PIN(l0); PIN(l1);
      SB();
    }
    if (i == WUSE) {
      PIN(tch1); PIN(tch2);
      if ((part == 1 || (part == 2 && has_w && !modc))) {
        WAIT_VM(WK); CFENCE();
        if (part == 2) mod_stage(); else wo_prep();
      }
#pragma unroll
      for (int k = 0; k < 8; ++k) PIN(bf[k]);
      SB();
    }
    if (i == QP + QD) {
      if (part == 1) {
        u32 gm[2] = {gmd[0][0], gmd[1][0]};
        if (!all_ok((gmd[0][1] == tag) & (gmd[1][1] == tag))) { STAMP(11); gather8<2>(wsr, G_MOD + tid * 8, 512 * 8, tag, gm); }
        STAMP(5);
        gemv_go(pack_bf(rbf(l0 * rbf(1.f + lo_f(gm[1]))) + lo_f(gm[0]), rbf(l1 * rbf(1.f + hi_f(gm[1]))) + hi_f(gm[0])));
      }
      SB();
    }
    if (i == QP + QD + 1) {
      if (part == 1) {
        LDS_BARRIER();
        if (wave == 0) {
          const int bb = lane >> 5, jq = lane & 31;
          st8_sc1(wsr, G_Q + (bb * 2048 + blk * 32 + jq) * 8, pack_bf(sum8(&sm.r2[2 * jq * 20 + bb * 8]), sum8(&sm.r2[(2 * jq + 1) * 20 + bb * 8])), tag);
        }
        STAMP(6);
      }
      SB();
    }
    return true;
  });
  PIN(sot);
  tau = te1 >> 16;
  due += P.TG * 256;
  const u32 qoff = G_Q + ((b * 32 + head) * 64 + c * 4) * 8;
  u32x4 qg[2], mlg;
  u32 qq[4];
  constexpr int NB = (NL + 15) / 16;
  float xlo, sc[NB], pr[NB], m = 0.f;
  f32x4 acc = {0.f, 0.f, 0.f, 0.f};
  u32 pb[NB], pm[4], pl8[8], cpf;
  const u32 adst0 = lds_addr(lane < 32 ? &sm.red[wave][c * 8 + g * 4] : &sm.r2[(lane - 32) * 4]);
  u32 po, rdst, rsrc, xo;
  auto wo_addr = [&]() {
    po = dup(G_P + (((c >> 2 & 1) * 32 + xcd * 4 + (wave >> 1)) * 4 + (c & 3)) * 256 + (wave & 1) * 128 + (c >> 3) * 64 + g * 16) + ((P.NR & 2) ? near : (xcd & 2) * 4096);
    rdst = lane < 32 ? (u32)(uintptr_t)&sm.red2[wave][0] + lane * 16 : (u32)(uintptr_t)&sm.r2[0] + (lane - 32) * 16;
    rsrc = (u32)(uintptr_t)&sm.red2[0][((wave & 1) * 16 + (lane & 15)) * 4 + (lane >> 4)];
    xo = dup(G_XP + xcd * 2048 + (wave & 1) * 1024 + (blk * 64 + (lane & ~1)) * 2) + (lane & 1) * 8192;
    PIN(po); PIN(rdst); PIN(rsrc); PIN(xo);
  };
  u32 pso = wave == 0 ? pslot + c * 16 + (g & 1) * 8 + (g >> 1) * 8192 : OOB;
  PIN(pso);
  const u32 nadd = wdef ? P.LW : (is_up ? P.LU : 0);
  u32 nto = tid == 0 ? G_NT + bid * 8 : OOB;
  PIN(nto);
  auto notices = [&](auto nc_, u32 off, u32x4 (&gn)[nc_.v]) -> int {
    constexpr int N = nc_.v;
#pragma nounroll
    for (int spin = 0; spin < SPIN_MAX; ++spin) {
      bool ok = true;
#pragma unroll
      for (int i = 0; i < N; ++i) ok &= (gn[i][1] == tag) & (gn[i][3] == tag);
      if (all_ok(ok)) break;
      __builtin_amdgcn_s_sleep(10);
      CFENCE();
#pragma unroll
      for (int i = 0; i < N; ++i) gn[i] = ld16<AUX_SC1>(wsr, off, i * 1024);
    }
    int mx = 0;
#pragma unroll
    for (int i = 0; i < N; ++i) mx = max(mx, max((int)(gn[i][0] - t0), (int)(gn[i][2] - t0)));
    return __builtin_amdgcn_readfirstlane((int)wave_max((float)min(mx, 1 << 20)));
  };
  auto pv_row = [&](auto jc) {
    constexpr int i = jc.v;
    const u32 pv = __float_as_uint(bcast16(u2f(pb[i >> 4]), i & 15));
    acc = mfma16(u32x4{pv & pm[0], pv & pm[1], pv & pm[2], pv & pm[3]}, vr[i], acc);
    PIN(acc);
    return true;
  };
  sfor<0, NL - 1>([&](auto ic) {
    constexpr int v = ic.v;
    if constexpr (v >= SK0 && v < SK1) { if (left_out(v)) return true; }
    if constexpr (v > 0) {
      const u32 so = __builtin_amdgcn_readlane(sot, v);
      if constexpr (v >= SK1) { if (v == NL - P.NE) tau = (tau * P.FE) >> 4; }
      gate();
      blog(NL + v - 1);
      SB();
      if constexpr (v >= SK0 && v < SK1) ld16_into(vr[v], rv, lane * 16, so); else vr[v] = ld16<AUX_NT>(rv, lane * 16, so);
      SB();
      SB();
    }
    if constexpr (v == SK0 - 1) {
#pragma unroll
      for (int i = SK0; i < SK1; ++i) { vr[i] = u32x4{0u, 0u, 0u, 0u}; PIN(vr[i]); }
    }
    if constexpr (v == VQ - 4) { qg[0] = ld16<AUX_SC1>(wsr, qoff, 0); qg[1] = ld16<AUX_SC1>(wsr, qoff + 16, 0); }
    if constexpr (v == VSIDE) { side_loads(); tbg = ld8<AUX_SC1>(wsr, wave == 7 ? G_VT + xcd * 128 + c * 8 : OOB, 0); }
    if constexpr (v == CPF) {
      u64 pc; u32 dlt, len;
      asm volatile("s_getpc_b64 %0\n.Lcpf_here:\n\ts_mov_b32 %1, .Ltail_start-.Lcpf_here\n\ts_mov_b32 %2, .Lend_a-.Ltail_start\n\ts_max_i32 %2, %2, .Lend_b-.Ltail_start\n\ts_max_i32 %2, %2, .Lend_c-.Ltail_start\n\ts_max_i32 %2, %2, .Lend_d-.Ltail_start\n\ts_max_i32 %2, %2, .Lend_e-.Ltail_start" : "=s"(pc), "=s"(dlt), "=s"(len) :: "scc");
      pc += dlt;
      const rsrc_t rc = mk_rsrc((const void*)pc, (part == 3 && i8 == 1) ? len : 0u);
      cpf = __builtin_amdgcn_raw_buffer_load_b32(rc, tid * 64, 0, 0);
    }
    if constexpr (v == SK1 + 2) PIN(cpf);
    if constexpr (v == SK1) mlg = ld16<AUX_SC1>(wsr, wave == 0 ? mlo : OOB, 0);
    SB();
    if constexpr (NL + v < 72) limit(IC<NL + v>{});
    if constexpr (v >= SK0 + WV) { WAIT_VM(v < SK1 ? WV : WV + 1); SB(); }
    if constexpr (v == VQ) {
      if (!all_ok((qg[0][1] == tag) & (qg[0][3] == tag) & (qg[1][1] == tag) & (qg[1][3] == tag))) { STAMP(20); gather16<2>(wsr, qoff, 16, tag, qg); }
      STAMP(1); WSTAMP(1);
      qq[0] = qg[0][0]; qq[1] = qg[0][2]; qq[2] = qg[1][0]; qq[3] = qg[1][2];
    }
    if constexpr (v >= VQ && v < VS) {
      constexpr int sl = v - VQ, bt = sl >> 1;
      float xs[8];
#pragma unroll
      for (int i = 0; i < 8; ++i) {
        float d = 0.f;
        if (sl * 8 + i < NL) {
#pragma unroll
          for (int k = 0; k < 4; ++k) d = dot2(kr[sl * 8 + i][k], qq[k], d);
        }
        xs[i] = d;
      }
#pragma unroll
      for (int h = 0; h < 4; ++h) xs[h] = sel_add<XOR7>(xs[h], xs[h + 4], c & 4);
#pragma unroll
      for (int h = 0; h < 2; ++h) xs[h] = sel_add<XOR2>(xs[h], xs[h + 2], c & 2);
      xs[0] = sel_add<XOR1>(xs[0], xs[1], c & 1);
      if constexpr ((sl & 1) || sl == NSL - 1) {
        const float t = (sl & 1) ? sel_add<XOR8>(xlo, xs[0], c & 8) : xs[0] + dpp<XOR8>(xs[0]);
        const float s = rbf(rbf(t) * 0.08838834764831845f);
        sc[bt] = (bt * 16 + c < SK0 || (bt * 16 + c >= first && bt * 16 + c < NL)) ? s : -__builtin_inff();
        PIN(sc[bt]);
      } else {
        xlo = xs[0];
        PIN(xlo);
      }
    }
    if constexpr (v == VS) {
      float mloc = sc[0];
#pragma unroll
      for (int bt = 1; bt < NB; ++bt) mloc = fmaxf(mloc, sc[bt]);
      m = fmaxf(wave_max(mloc), -3.0e38f);
      float ls = 0.f;
#pragma unroll
      for (int bt = 0; bt < NB; ++bt) { pr[bt] = __expf(sc[bt] - m); ls += pr[bt]; }
      const float l = wave_sum(ls);
      if (lane == 0) { sm.ml[wave][0] = m; sm.ml[wave][1] = l; }
      if (c == ((NL - 1) & 15)) sm.pl[g][wave] = pr[NB - 1];
      STAMP(2); WSTAMP(2);
    }
    if constexpr (v >= SK1) pv_row(IC<v - SK1 + SK0 - WV>{});
    if constexpr (v < SK0 || v >= SK0 + WV) sfor<cons_end(v - 1), cons_end(v)>(pv_row);
    if constexpr (v == SK1 + 1) {
      float p[8];
#pragma unroll
      for (int w = 0; w < 8; ++w) p[w] = sm.pl[g][w] * __expf(sm.ml[w][0] - m);
#pragma unroll
      for (int w = 0; w < 8; ++w) pl8[w] = pack_bf(p[w], p[w]);
#pragma unroll
      for (int w = 0; w < 8; ++w) PIN(pl8[w]);
    }
    if constexpr (v == MLS - 1) { side_pin(); if (is_up) up_prep(); PIN(ga); PIN(xin); PIN(cc1); }
    if constexpr (v == UPREP - 1) wo_addr();
    if constexpr (v == UPREP) {
#pragma unroll
      for (int k = 0; k < 8; ++k) asm volatile("" : "=v"(bf[k]));
      if (part == 0) wo_prep(0, 1); else if (is_up) up_frags();
#pragma unroll
      for (int k = 0; k < 8; ++k) PIN(bf[k]);
    }
    if constexpr (v == UPREP + 1) {
      if (is_up) {
        const u32x4* a1 = ap + 128 + ((lane >> 1) & 1) * 128;
        f32x4 d0 = {0.f, 0.f, 0.f, 0.f}, d1 = {0.f, 0.f, 0.f, 0.f};
#pragma unroll
        for (int rb = 0; rb < 4; ++rb) { d0 = mfma16(a1[rb * 8], bf[rb], d0); d1 = mfma16(a1[(rb + 4) * 8], bf[rb + 4], d1); }
        Af = par ? d0[1] + d1[1] : d0[0] + d1[0]; Bf = par ? d0[3] + d1[3] : d0[2] + d1[2];
        Af += dpp<XOR8>(Af); Bf += dpp<XOR8>(Bf);
      }
      PIN(Af); PIN(Bf);
    }
    if (v == 10 || v == 16) STAMP(15 + v / 6);
    if (v == 20) STAMP(18);
    if (v == 32) STAMP(19);
    if constexpr (v == MLS) {
      if (wave == 7) {
        const bool ok = tbg[1] == tag;
        float vt = ok ? (float)((int)tbg[0] >> 16) : 0.f, lg = ok ? (float)(tbg[0] & 0xffffu) : 0.f, nf = ok ? 8.f * (NL - 1 - RP0) : 0.f;
        vt += dpp<XOR1>(vt); lg += dpp<XOR1>(lg); nf += dpp<XOR1>(nf); vt += dpp<XOR2>(vt); lg += dpp<XOR2>(lg); nf += dpp<XOR2>(nf);
        vt += dpp<XOR7>(vt); lg += dpp<XOR7>(lg); nf += dpp<XOR7>(nf); vt += dpp<XOR8>(vt); lg += dpp<XOR8>(lg); nf += dpp<XOR8>(nf);
        lg *= __builtin_amdgcn_rcpf(fmaxf(nf, 1.f));
        if (lane == 0) sm.bc = u32x2{__float_as_uint(vt), (u32)min(max((int)((lg - (float)P.A0) * (float)(P.AG * 32)), 0), P.AC * 256)};
      }
      LDS_BARRIER();
      {
        const u32x2 bc = sm.bc;
        near = ((__builtin_amdgcn_readfirstlane(bc[0]) >> 31 ^ die ^ P.NR ^ 1) & 1) * 8192u;
        t1 += __builtin_amdgcn_readfirstlane(bc[1]);
        tau += __builtin_amdgcn_readfirstlane((int)((float)(int)bc[1] * __builtin_amdgcn_rcpf((float)(nl - MLS - 3))));
      }
      float mm[8], ll[8];
#pragma unroll
      for (int w = 0; w < 8; ++w) { const f32x2 t = *reinterpret_cast<const f32x2*>(&sm.ml[w][0]); mm[w] = t[0]; ll[w] = t[1]; }
      float Mwg = mm[0], Lwg = 0.f;
#pragma unroll
      for (int w = 1; w < 8; ++w) Mwg = fmaxf(Mwg, mm[w]);
      if (wave == 0) {
#pragma unroll
        for (int w = 0; w < 8; ++w) Lwg = fmaf(__expf(mm[w] - Mwg), ll[w], Lwg);
        if (lane == 0) st16_sc1(wsr, G_ML + ((head * 2 + b) * 4 + part) * 16, __float_as_uint(Mwg), __float_as_uint(Lwg), tag);
      }
      const float fw = __expf(m - Mwg);
      m = Mwg;
#pragma unroll
      for (int bt = 0; bt < NB; ++bt) { pb[bt] = pack_bf(pr[bt] * fw, pr[bt] * fw); PIN(pb[bt]); }
#pragma unroll
      for (int k = 0; k < 4; ++k) { pm[k] = (c >> 1 & 3) == k ? ((c & 1) ? 0xffff0000u : 0x0000ffffu) : 0u; PIN(pm[k]); }
    }
    SB();
    return true;
  });
  asm volatile(".Ltail_start:");
  if (wave != 0) {
    pv_row(IC<SK0 - 1>{});
    WAIT_VM(WV); SB();
    sfor<NL - 1 - WV, NL - 1>(pv_row);
    *LDSP(f32x4, adst0) = acc;
    WSTAMP(14);
    LDS_BARRIER();
  } else {
    u32x4 xr[8], am[8];
#pragma unroll
    for (int w = 0; w < 8; ++w) am[w] = u32x4{pl8[w] & pm[0], pl8[w] & pm[1], pl8[w] & pm[2], pl8[w] & pm[3]};
#pragma unroll
    for (int w = 0; w < 8; ++w) PIN(am[w]);
    {
      const u32 so = __builtin_amdgcn_readlane(sot, NL - 1);
      gate();
      SB();
#pragma unroll
      for (int w = 0; w < 8; ++w) xr[w] = ld16<AUX_NT>(rv, lane * 16, so ^ ((w & 1) ? col(P, 7) : 0u) ^ ((w & 2) ? col(P, 8) : 0u) ^ ((w & 4) ? col(P, 9) : 0u));
      SB();
    }
    u32 rsum = lds_addr(&sm.red[0][c * 8 + (g & 1) * 4]);
    pv_row(IC<SK0 - 1>{});
    WAIT_VM(WV + 8); SB();
    sfor<NL - 1 - WV, NL - 3>(pv_row);
    if (__builtin_expect(!all_ok((mlg[1] == tag) & (mlg[3] == tag)), 0)) {
#pragma nounroll
      for (int spin = 0; spin < SPIN_MAX; ++spin) {
        CFENCE();
        mlg = ld16<AUX_SC1>(wsr, mlo, 0);
        if (all_ok((mlg[1] == tag) & (mlg[3] == tag))) break;
      }
    }
    merge_factor(mlg);
    sfor<NL - 3, NL - 1>(pv_row);
    STAMP(14); WSTAMP(14);
    LDS_BARRIER();
    STAMP(15);
    f32x4 t[8];
#pragma unroll
    for (int w = 1; w < 8; ++w) t[w] = LDSP(f32x4, rsum)[w * 32];
    acc = ((acc + t[1]) + (t[2] + t[3])) + ((t[4] + t[5]) + (t[6] + t[7]));
    PIN(acc);
    STAMP(3);
    SB();
#pragma unroll
    for (int w = 0; w < 8; ++w) acc = mfma16(am[w], xr[w], acc);
  }
  WAIT_VM(0);
  acc *= fmrg;
  __builtin_amdgcn_raw_buffer_store_b64(u32x2{min(pack_bf(acc[0], acc[1]), SENT - 1u), min(pack_bf(acc[2], acc[3]), SENT - 1u)}, wsr, pso, 0, AUX_SC1);
  st8_sc1(wsr, nto, (u32)wall_clock64() + nadd, tag);
  SB();
  STAMP(4);

  if (part == 2) FINISH();
  if (part == 0) {
    __builtin_amdgcn_s_barrier();
#pragma nounroll
    while (wleft > 0) piece();
    bool late = true;
    while ((int)(((u32)wall_clock64() - t0) << 8) < t1 - 20 * 256) if (__builtin_amdgcn_s_getreg(7 | (3 << 11)) == 0) { late = false; break; }
    u32x4 og[2];
    if (__builtin_expect(late, 0)) { until(t1); CFENCE(); og[0] = ld16<AUX_SC1>(wsr, po, 0); SB(); WAIT_VM(1); } else { WAIT_VM(0); }
    CFENCE();
    wo_prep(1, 2);
#pragma unroll
    for (int k = 4; k < 8; ++k) PIN(bf[k]);
    SB();
    if (__builtin_expect(!late, 1)) { until(t1); CFENCE(); og[0] = ld16<AUX_SC1>(wsr, po, 0); }
    STAMP(11);
    SB();
    f32x4 d[4];
    auto wo_mm = [&]() {
      const f32x4 z = {0.f, 0.f, 0.f, 0.f};
#pragma unroll
      for (int t = 0; t < 4; ++t) { d[t] = mfma16(og[0], bf[t], z); og[1][t] = __float_as_uint(dpp<XOR8>(u2f(og[0][t]))); }
      SB();
#pragma unroll
      for (int t = 0; t < 4; ++t) d[t] = mfma16(og[1], bf[4 + t], d[t]);
    };
    auto p_bad = [&]() { return !all_ok(max(max(og[0][0], og[0][1]), max(og[0][2], og[0][3])) != SENT); };
    wo_mm();
    if (__builtin_expect(p_bad(), 0)) {
#pragma nounroll
      for (int spin = 0; spin < SPIN_MAX; ++spin) {
        __builtin_amdgcn_s_sleep(1);
        CFENCE();
        og[0] = ld16<AUX_SC1>(wsr, po, 0);
        if (!p_bad()) break;
      }
      wo_mm();
      SB();
      asm volatile(".Lend_d:");
    }
    STAMP(5); WSTAMP(5);
    {
      SB();
      f32x4 y; f32x2 h[4];
#pragma unroll
      for (int t = 0; t < 4; ++t) { h[t] = d[t].xy + d[t].zw; PIN(h[t]); }
#pragma unroll
      for (int t = 0; t < 4; ++t) { y[t] = h[t][0] + h[t][1]; PIN(y[t]); }
      *LDSP(f32x4, rdst) = y;
    }
    STAMP(16);
    LDS_BARRIER();
    STAMP(17);
    if (wave < 2) {
      f32x2 v[4];
#pragma unroll
      for (int w = 0; w < 4; ++w) v[w] = f32x2{LDSP(float, rsrc)[w * 256], LDSP(float, rsrc)[w * 256 + 128]};
      const f32x2 h = (v[0] + v[1]) + (v[2] + v[3]);
      const float y = h[0] + h[1];
      __builtin_amdgcn_raw_buffer_store_b32(min(pack_bf(dpp<0xA0>(y), dpp<0xF5>(y)), SENT - 1u), wsr, xo, 0, AUX_SC1);
      asm volatile(".Lend_a:");
    }
    STAMP(6);
    FINISH();
  }
  if (part == 3) {
    if (!has_w) FINISH();
    u32x4 b2[4];
    {
      __builtin_amdgcn_s_barrier();
      const rsrc_t r2 = mk_rsrc(w2 + (size_t)(blk * 32 + wave * 4) * 512, 4096u);
      const u32 l2 = (u32)(uintptr_t)(&sm.w2b[wave * 4][0]);
#pragma unroll
      for (int n = 0; n < 4; ++n) dma16(r2, (lane ^ (n * 2) ^ ((wave & 2) << 2)) * 16, n * 1024, l2 + n * 1024);
    }
    const u32 tdst = lds_addr(&sm.hb[hpos]);
    const u32 sdst = lds_addr(&sm.stat[wave][lane]);
    const u32 ssrc = lds_addr(&sm.stat[(g >> 1) * 4 + ((lane >> 3) & 3)][(lane & 7) * 4]);
    const u32 adst = lds_addr((c < 4 && (g & 1) == 0) ? &sm.actl[g >> 1][wave * 4 + c] : reinterpret_cast<u16*>(&sm.dummy[lane]));
    const u32 asrc = lds_addr(&sm.actl[lane & 1][g * 8]);
    const u32 apa = lds_addr(&sm.hb[(c >> 3) * 512 + (c & 3) * 64 + ((c >> 2) & 1) * 32 + g * 8]);
    const u32 xs2 = (P.NR & 4) ? 16384 : 16384 + 8192;
    u32 xoff = dup(G_XP) + ((P.NR & 4) ? near : 0) + g * 2048 + ub * 1024 + (wave & 3) * 256 + c * 16;
    u32x4 one;
#pragma unroll
    for (int k = 0; k < 4; ++k) one[k] = ((c & 2) == 0 && (c >> 2) == k) ? ((c & 1) ? 0x3f800000u : 0x00003f80u) : 0u;
    PIN(one);
    u32 ooff = dup(G_OP + (blk * 512 + tid) * 8);
    float Afp = Af, Bfp = Bf;
    PIN(xoff); PIN(ooff); PIN(Afp); PIN(Bfp);
    f32x2 gc = ga * cc1, xc = xin * cc1;
    PIN(ga); PIN(xin); PIN(gc); PIN(xc);
    u32 w2a = (u32)(uintptr_t)(&sm.w2b[0][0]) + (g * 8 + ((lane >> 2) & 3)) * 1024 + (wave ^ (g & 1)) * 128 + (lane & 15) * 8;
    PIN(w2a);
    if (wave == 1) {
      due = t1 + P.N1 * 256; gate();
      u32x4 gn[2] = {ld16<AUX_SC1>(wsr, G_NT + lane * 16, 0), ld16<AUX_SC1>(wsr, G_NT + lane * 16, 1024)};
      const int tn = notices(IC<2>{}, G_NT + lane * 16, gn) * 256;
      due = (tn > t1 + 2 * 256 ? max(tn, t1 + P.L0 * 256) + P.L1 * 256 : t1) + P.X2 * 256;
      until(due);
    }
    asm volatile("s_waitcnt vmcnt(0)" ::: "memory");
    STAMP(9);
    __builtin_amdgcn_s_barrier();
    STAMP(11);
    CFENCE();
    u32x4 xg[2] = {ld16<AUX_SC1>(wsr, xoff, 0), ld16<AUX_SC1>(wsr, xoff, xs2)};
    SB();
#pragma unroll
    for (int t = 0; t < 4; ++t) {
      const u32x2 lo = tr_read(w2a ^ (t * 32)), hi = tr_read((w2a ^ (t * 32)) + 4096);
      b2[t] = u32x4{lo[0], lo[1], hi[0], hi[1]};
    }
#pragma unroll
    for (int t = 0; t < 4; ++t) PIN(b2[t]);
    SB();
    const f32x4 z4 = {0.f, 0.f, 0.f, 0.f};
    auto xp_bad = [&]() { return !all_ok(max(max(max(xg[0][0], xg[0][1]), max(xg[0][2], xg[0][3])), max(max(xg[1][0], xg[1][1]), max(xg[1][2], xg[1][3]))) != SENT); };
    f32x4 v = mfma16(one, xg[1], mfma16(one, xg[0], z4));
    if (__builtin_expect(xp_bad(), 0)) {
#pragma nounroll
      for (int spin = 0; spin < SPIN_MAX; ++spin) {
        __builtin_amdgcn_s_sleep(1);
        CFENCE();
        xg[0] = ld16<AUX_SC1>(wsr, xoff, 0); xg[1] = ld16<AUX_SC1>(wsr, xoff, xs2);
        if (!xp_bad()) break;
      }
      v = mfma16(one, xg[1], mfma16(one, xg[0], z4));
      SB();
      asm volatile(".Lend_e:");
    }
    STAMP(5); WSTAMP(5);
    {
      const f32x2 a = v.xy, hc = __builtin_elementwise_fma(gc, a, xc), x1 = __builtin_elementwise_fma(ga, a, xin);
      *LDSP(u32, tdst) = pack_bf(hc[0], hc[1]);
      *LDSP(float, sdst) = swap32_add(x1[0] + x1[1], fmaf(x1[0], x1[0], x1[1] * x1[1]));
    }
    STAMP(16);
    LDS_BARRIER();
    STAMP(17);
    {
      const f32x4 z = {0.f, 0.f, 0.f, 0.f};
      const u32x4 af0 = LDSP(u32x4, apa)[0];
      SB();
      const f32x4 s4 = LDSP(f32x4, ssrc)[0], q4 = LDSP(f32x4, ssrc)[8];
      const u32x4 af1 = LDSP(u32x4, apa)[32];
      SB();
      f32x4 d[4];
      d[0] = mfma16(af0, bf[0], z); d[1] = mfma16(af0, bf[1], z);
      SB();
      const f32x2 s2 = s4.xy + s4.zw, q2 = q4.xy + q4.zw;
      float s = s2[0] + s2[1], q = q2[0] + q2[1];
      SB();
      d[2] = mfma16(af0, bf[2], z);
      SB();
      s = swap16_add(s, q);
      SB();
      d[3] = mfma16(af0, bf[3], z);
      SB();
      s += dpp<XOR1>(s); s += dpp<XOR2>(s);
      SB();
      d[0] = mfma16(af1, bf[4], d[0]);
      SB();
      s += dpp<XOR7>(s); s += dpp<XOR8>(s);
      SB();
      d[1] = mfma16(af1, bf[5], d[1]);
      SB();
      const auto sq = __builtin_amdgcn_permlane16_swap(__float_as_uint(s), __float_as_uint(s), false, false);
      const float mean = u2f(sq[0]) * (1.f / 512.f);
      SB();
      d[2] = mfma16(af1, bf[6], d[2]);
      SB();
      const float var = fmaf(u2f(sq[1]), 1.f / 512.f, -mean * mean);
      SB();
      d[3] = mfma16(af1, bf[7], d[3]);
      SB();
      float ri = __builtin_amdgcn_rsqf(fmaxf(var, 0.f) + 1e-5f);
      float cf = fmaf(-ri * mean, Afp, Bfp);
      float rk = ri * -1.44269504f, ck = cf * -1.44269504f;
      PIN(ri); PIN(cf); PIN(rk); PIN(ck);
      SB();
      PIN(d[0]); PIN(d[1]);
      float y = d[0][0] + d[1][1];
      PIN(y); PIN(d[2]);
      y += d[2][2];
      PIN(y); PIN(d[3]);
      y += d[3][3];
      y = __builtin_bit_cast(float, __builtin_amdgcn_update_dpp(__builtin_bit_cast(int, y), __builtin_bit_cast(int, y), XOR8, 0xa, 0xf, false));
      y = swap16_add(y, y);
      const float ex = __builtin_amdgcn_exp2f(fmaf(y, rk, ck));
      y = fmaf(y, ri, cf);
      const float gu = y * dpp<0x104>(y);
      *LDSP(u16, adst) = (u16)pack_bf(gu * __builtin_amdgcn_rcpf(1.f + ex), 0.f);
    }
    STAMP(18);
    LDS_BARRIER();
    STAMP(10);
    {
      const u32x4 af = *LDSP(u32x4, asrc);
      const f32x4 z = {0.f, 0.f, 0.f, 0.f};
      f32x4 d0 = mfma16(af, b2[0], z), d1 = mfma16(af, b2[1], z), d2 = mfma16(af, b2[2], z), d3 = mfma16(af, b2[3], z);
      asm volatile("" : "+v"(d0), "+v"(d1), "+v"(d2), "+v"(d3));
      const bool g1 = g & 1, g2 = g & 2;
      const float e0 = g1 ? d1[0] : d0[0], e1 = g1 ? d1[1] : d0[1], f0 = g1 ? d3[0] : d2[0], f1 = g1 ? d3[1] : d2[1];
      const u32x2 ov = {pack_bf(g2 ? f0 : e0, g2 ? f1 : e1), tag};
      __builtin_amdgcn_raw_buffer_store_b64(ov, wsr, ooff, near ^ 8192u, AUX_SC1);
      __builtin_amdgcn_raw_buffer_store_b64(ov, wsr, ooff, near, AUX_SC1);
    }
    asm volatile(".Lend_b:");
    STAMP(6);
    FINISH();
  }
  if (wave == 0) {
    const int d = fd, ul = lane >> 3;
    u32 gm[2] = {sg[0][0], sg[1][0]};
    if (!all_ok((sg[0][1] == tag) & (sg[1][1] == tag))) gather8<2>(wsr, G_MOD + (1024 + d) * 8, 1536 * 8, tag, gm);
    u32 xa0, xa1;
    {
      due = t1 + P.N2 * 256; gate();
      u32x4 gn[2] = {ld16<AUX_SC1>(wsr, G_NT + lane * 16, 0), ld16<AUX_SC1>(wsr, G_NT + lane * 16, 1024)};
      const int tn = notices(IC<2>{}, G_NT + lane * 16, gn) * 256;
      due = (tn > t1 + 2 * 256 ? max(tn, t1 + P.L0 * 256) + P.L1 * 256 : t1) + P.X3 * 256;
      until(due);
    }
#pragma nounroll
    for (int spin = 0; spin < SPIN_MAX; ++spin) {
      CFENCE();
      xa0 = __builtin_amdgcn_raw_buffer_load_b32(wsr, dup(G_XP + ul * 2048) + near + (d >> 1) * 4, 0, AUX_SC1);
      xa1 = __builtin_amdgcn_raw_buffer_load_b32(wsr, dup(G_XP + ul * 2048) + near + (d >> 1) * 4, 1024, AUX_SC1);
      if (all_ok(max(xa0, xa1) != SENT)) break;
      __builtin_amdgcn_s_sleep(1);
    }
    float a0 = (d & 1) ? hi_f(xa0) : lo_f(xa0), a1 = (d & 1) ? hi_f(xa1) : lo_f(xa1);
    a0 += dpp<XOR8>(a0); a1 += dpp<XOR8>(a1);
    a0 = swap16_add(a0, a0); a1 = swap16_add(a1, a1);
    a0 = swap32_add(a0, a0); a1 = swap32_add(a1, a1);
    const bool hb = g & 1;
    float xv = rbf(((d & 1) ? hi_f(sx2) : lo_f(sx2)) + rbf((hb ? hi_f(gm[0]) : lo_f(gm[0])) * rbf(hb ? a1 : a0)));
    float gv = hb ? hi_f(gm[1]) : lo_f(gm[1]);
    PIN(xv); PIN(gv);
    if (wi == 0 && lane == 0) __builtin_amdgcn_raw_buffer_store_b32(tag, wsr, WS_EPOCH, 0, AUX_SC1);
    STAMP(10);
    due = max(due + P.X4 * 256, (int)(((u32)wall_clock64() - t0) << 8) + P.GX * 256);
    const bool last = ul + 40 < NUP;
    u32 o0 = dup(G_OP + (ul * 512 + d) * 8) + ((P.NR & 8) ? near : (ul & 2) * 4096), o5 = last ? o0 : OOB, tg5 = last ? tag : 0u;
    const rsrc_t rout = mk_rsrc(out, 2048u);
    u32 oo = (lane & 40) == 0 ? (g * 512 + d) * 2 : OOB;
    const u32 pk = c == 0 ? 0x00003f80u : (c == 4 ? 0x3f800000u : 0u);
    u32x4 one = {pk, 0u, pk, 0u};
    PIN(o0); PIN(o5); PIN(tg5); PIN(oo); PIN(one);
    __builtin_amdgcn_raw_buffer_store_b16((u16)pack_bf(xv, 0.f), rout, oo, 0, AUX_SC1);
    until(due);
    f32x4 sv;
#pragma nounroll
    for (int spin = 0; spin < SPIN_MAX; ++spin) {
      CFENCE();
      u32x2 og[6];
#pragma unroll
      for (int k = 0; k < 5; ++k) og[k] = ld8<AUX_SC1>(wsr, o0, k * 65536);
      og[5] = ld8<AUX_SC1>(wsr, o5, 5 * 65536);
      sv = f32x4{0.f, 0.f, 0.f, 0.f};
#pragma unroll
      for (int k = 0; k < 3; ++k) sv = mfma16(one, u32x4{og[2 * k][0], og[2 * k][1], og[2 * k + 1][0], og[2 * k + 1][1]}, sv);
      const u32 bad = ((og[0][1] ^ tag) | (og[1][1] ^ tag) | (og[2][1] ^ tag)) | ((og[3][1] ^ tag) | (og[4][1] ^ tag) | (og[5][1] ^ tg5));
      PIN(sv);
      if (all_ok(bad == 0u)) break;
      __builtin_amdgcn_s_sleep(1);
    }
    STAMP(7);
    const float s = sv[0] + dpp<XOR8>(sv[0]);
    __builtin_amdgcn_raw_buffer_store_b16((u16)pack_bf(fmaf(gv, s, xv), 0.f), rout, oo, 0, AUX_SC1);
    asm volatile(".Lend_c:");
  }
  FINISH();
}

extern "C" void kernel_launch(void* const* d_in, const int* in_sizes, int n_in,
                              void* d_out, int out_size, void* d_ws, size_t ws_size,
                              hipStream_t stream) {
  fused_kernel<<<256, 512, 0, stream>>>((const u16*)d_in[1], (const u16*)d_in[2], (const u16*)d_in[4], (const u16*)d_in[3], (const u16*)d_in[10], (const u16*)d_in[6], (const u16*)d_in[7],
                                        (unsigned char*)d_ws, (const u16*)d_in[0], (const u16*)d_in[9], (const u16*)d_in[8], (u16*)d_out);
}
```

```cpp
#define PROBE_MUL 10
#include <hip/hip_runtime.h>
#include <stdint.h>

typedef unsigned int u32;
typedef unsigned short u16;
typedef unsigned long long u64;
typedef __attribute__((ext_vector_type(4))) unsigned u32x4;
typedef __attribute__((ext_vector_type(2))) unsigned u32x2;
typedef __attribute__((ext_vector_type(2))) __bf16 bf16x2;
typedef __attribute__((ext_vector_type(4))) float f32x4;
typedef __attribute__((ext_vector_type(2))) float f32x2;
typedef __amdgpu_buffer_rsrc_t rsrc_t;

constexpr int NH = 32, SK = 16384, DH = 128, WIN = 4096, FF = 1376;
struct Prm { int E0, ES, E1, E2, E2C, EU, TEND, DT_UP, L0, L1, X1, X2, X3, X4, NE, FE, M0, M1, M2, M3, M4, GS, GX, N1, N2, GA, WD, DT_WO, LW, LU, EW, PM, NF, NR, C0, A0, AG, AC, TS, TW, TK, TG, W[9]; };
constexpr Prm PRM0 = {32, 20, 32, 32, 54, 58, 1980, 60, 5, 24, 92, 76, 114, 80, 3, 8, 0x2511f55b, 0x2b7425d5, 0x6017665, 0x17e0b9ec, 0x31944305, 0, 32, 18, 58, 0, 4, 100, 45, 15, 0, 0x0, 15, 14, 5, 75, 0, 100, 509, 236, 60, 20, 0x77777777, 0x77777777, 0x77777777, 0x77777777, 0x77777777, 0x44444477, 0x44444444, 0x44444444, 0x44444444};
constexpr int NL = 37;
constexpr int SK0 = 21, SK1 = 32;
constexpr int NCMIN = (NL - SK1 + SK0) * 8;
constexpr int WK = 8, WV = 5;
constexpr int WUSE = 12, LNS = 7;
constexpr int QP = 14, QD = 4;
constexpr int VQ = 6;
constexpr int NSL = (NL + 7) / 8;
constexpr int VS = VQ + NSL;
constexpr int MLS = VS + 2;
constexpr int VSIDE = MLS - 5, UPREP = MLS + 6;
constexpr int CPF = 17;
constexpr int SMO = 1;
constexpr int RP0 = 24;
constexpr int PRB = 8;
constexpr int TBP = 4, TBL = 11, TBU = 15;
constexpr int window(int s, int w) {
  int n = w;
  for (int u = s - w + 1; u <= s; ++u) n += u == SMO ? 6 : (u == TBL || u == NL + CPF || u == NL + SK1 ? 1 : (u == QP || u == NL + VQ - 4 ? 2 : (u == NL + VSIDE ? 5 : 0)));
  return n;
}
constexpr int cons_end(int v) {
  const int a = v - WV + 1, c = (v - MLS) * 4;
  const int m = a < c ? a : c;
  return m < 0 ? 0 : (m > NL ? NL : m);
}
constexpr u32 col(const Prm& P, int j) { const int m[6] = {P.M0, P.M1, P.M2, P.M3, P.M4, P.PM}; return (u32)(m[j / 3] >> (j % 3 * 10) & 1023) << 10; }
constexpr u32 first_col(const Prm& P, int k) {
  if (k < 4) return col(P, 12 + k) | 1u << (24 + k);
  if (k < 6) return col(P, k - 4);
  if (k < 8) return col(P, 4 + k) | 1u << (16 + k);
  if (k < 11) return col(P, k - 1);
  return (col(P, 2) ^ col(P, 3) ^ col(P, 4) ^ col(P, 5) ^ col(P, 6) ^ col(P, 10) ^ col(P, 11)) | 0xf00000u;
}
constexpr bool one_to_one(const Prm& P) {
  u32 c[10] = {};
  for (int j = 0; j < 10; ++j) c[j] = col(P, j);
  for (int j = 0; j < 10; ++j) {
    if (c[j] == 0) return false;
    const u32 low = c[j] & (0u - c[j]);
    for (int k = j + 1; k < 10; ++k) if (c[k] & low) c[k] ^= c[j];
  }
  return true;
}
static_assert(one_to_one(PRM0), "every KB of the window exactly once");
struct Tab { u32 e[512]; };
constexpr Tab make_tab(const Prm& P) {
  Tab T{};
  for (int bid = 0; bid < 256; ++bid) {
    const int xcd = bid & 7, part = bid >> 4 & 3, i8 = (bid >> 3 & 1) | bid >> 6 << 1, wi = i8 * 8 + xcd;
    const bool has_mod = wi < 48, has_up = wi >= 21;
    const int cb0 = (i8 + P.C0) & 7, cb1 = (i8 + 4) & 7, ms = wi - 20;
    const int mi = wi < 8 ? wi : (wi < 16 ? 8 + ((xcd + 4) & 7) : (wi < 20 ? (wi - 14) * 8 + 3 : 16 + ms / 7 * 8 + ms % 7 + (ms % 7 >= 3 ? 1 : 0)));
    const int ui = xcd * 5 + (xcd > 5 ? xcd - 5 : 0) + (i8 >= 3 ? (xcd >= 5 ? 1 : 0) + i8 - 3 : 0);
    const int wd = cb0 == 3 ? 0 : P.WD;
    const int e[4] = {P.E0 - 4 * wd + (cb0 == 3 ? P.ES : 0), P.E1 + (cb1 == 3 ? P.ES : 0), has_mod ? (wi < 16 ? P.E2C : P.E2) + ((mi & 7) == 3 ? P.ES : 0) : 0, has_up ? P.EU : 0};
    int tw[4] = {}, win[4] = {}, span[4] = {};
    for (int k = 0; k < 4; ++k) {
      const int np = k == 0 ? 8 - wd : 8;
      tw[k] = np ? e[k] * P.TW / (16 * np) : 0;
      span[k] = P.TEND - (P.NF + tw[k] * np / 2 > P.TK ? P.NF + tw[k] * np / 2 : P.TK) - P.TG - (k == 0 && wd ? P.DT_WO : (k == 3 && has_up ? P.DT_UP : 0));
      win[k] = span[k] - (k == 0 && wd ? P.EW : (k == 3 && has_up ? P.GA : 0));
    }
    int n[4] = {NCMIN / 8, NCMIN / 8, NCMIN / 8, NCMIN / 8};
    for (int left = 128 - NCMIN / 2; left > 0; --left) {
      int best = -1;
      for (int k = 3; k >= 0; --k)
        if (n[k] < NL && (best < 0 || n[k] * win[best] < n[best] * win[k])) best = k;
      ++n[best];
    }
    const int c[4] = {0, n[0] - 1, n[0] + n[1] - 2, n[0] + n[1] + n[2] - 3};
    const int nl = n[part];
    const bool has_w = part < 2 || (part == 2 ? has_mod : has_up);
    const int blk = !has_w ? 0 : (part == 0 ? cb0 : (part == 1 ? xcd * 8 + cb1 : (part == 2 ? mi : ui)));
    T.e[2 * bid] = (u32)(c[part] | nl << 7 | blk << 13 | (has_w ? 1 : 0) << 19 | (part == 0 ? wd : 0) << 20) | (u32)tw[part] << 24;
    T.e[2 * bid + 1] = (u32)(span[part] * P.TS / (4 * (nl - 1)) | span[part] * (1024 - P.TS) / (4 * (nl - 1)) << 16);
  }
  return T;
}
constexpr int step_range(const Prm& P, bool mx_) {
  int mx = 0, mn = 1 << 20;
  const Tab T = make_tab(P);
  for (int bid = 0; bid < 256; ++bid) { const int n = T.e[2 * bid] >> 7 & 63; mx = n > mx ? n : mx; mn = n < mn ? n : mn; }
  return mx_ ? mx : mn;
}
extern "C" __attribute__((used, visibility("protected"))) __constant__ const Tab wg_tab = make_tab(PRM0);
static_assert(step_range(PRM0, true) <= NL && step_range(PRM0, false) >= NCMIN / 8, "a wave's key range does not fit its load steps");
static_assert(UPREP + 1 < SK0 && QP + QD + 1 < SK0 && cons_end(SK0 - 1) == SK0 - WV && NL - SK1 == WV, "steps every wave runs");
constexpr int NUP = FF / 32;
constexpr int SPIN_MAX = 1 << 17;

constexpr u32 WS_EPOCH = 0;
constexpr u32 G_TB = 1024;
constexpr u32 G_NT = 2048;
constexpr u32 G_MOD = 4096;
constexpr u32 G_Q = G_MOD + 3072 * 8;
constexpr u32 G_ML = G_Q + 4096 * 8;
constexpr u32 G_U = G_ML + 256 * 16;
constexpr u32 G_P = 0;
constexpr u32 G_XP = G_P + 256 * 256;
constexpr u32 G_OP = G_XP + 8 * 512 * 4;
constexpr u32 G_PB = G_OP + 44 * 512 * 8;
constexpr u32 G_VT = G_U + 2 * (G_PB + 8192);
constexpr u32 WS_END = G_VT + 1024;
__device__ __forceinline__ u32 dup(u32 o) { return G_U + ((o >> 13) << 14) + (o & 8191u); }
constexpr u32 SENT = 0xffffffffu;
constexpr u32 OOB = 0x40000000u;
#define WST 0

constexpr int AUX_NT = 2, AUX_SC1 = 16;

__device__ __forceinline__ float lo_f(u32 v) { return __uint_as_float(v << 16); }
__device__ __forceinline__ float hi_f(u32 v) { return __uint_as_float(v & 0xffff0000u); }
__device__ __forceinline__ float rbf(float x) { return (float)(__bf16)x; }
__device__ __forceinline__ u32 pack_bf(float lo, float hi) {
  bf16x2 r; r[0] = (__bf16)lo; r[1] = (__bf16)hi; return __builtin_bit_cast(u32, r);
}
__device__ __forceinline__ float dot2(u32 k, u32 q, float acc) {
  return __builtin_amdgcn_fdot2_f32_bf16(__builtin_bit_cast(bf16x2, k), __builtin_bit_cast(bf16x2, q), acc, false);
}
template <int CTRL> __device__ __forceinline__ float dpp(float x) {
  return __builtin_bit_cast(float, __builtin_amdgcn_mov_dpp(__builtin_bit_cast(int, x), CTRL, 0xf, 0xf, true));
}
constexpr int XOR1 = 0xB1, XOR2 = 0x4E, XOR7 = 0x141, XOR8 = 0x128;
__device__ __forceinline__ float swap32_add(float a, float b) {
  auto t = __builtin_amdgcn_permlane32_swap(__float_as_uint(a), __float_as_uint(b), false, false);
  return __uint_as_float(t[0]) + __uint_as_float(t[1]);
}
__device__ __forceinline__ float swap16_add(float a, float b) {
  auto t = __builtin_amdgcn_permlane16_swap(__float_as_uint(a), __float_as_uint(b), false, false);
  return __uint_as_float(t[0]) + __uint_as_float(t[1]);
}
__device__ __forceinline__ float wave_sum(float x) {
  x += dpp<XOR1>(x); x += dpp<XOR2>(x); x += dpp<XOR7>(x); x += dpp<XOR8>(x);
  x = swap16_add(x, x);
  return swap32_add(x, x);
}
__device__ __forceinline__ float wave_max(float x) {
  x = fmaxf(x, dpp<XOR1>(x)); x = fmaxf(x, dpp<XOR2>(x)); x = fmaxf(x, dpp<XOR7>(x)); x = fmaxf(x, dpp<XOR8>(x));
  auto s = __builtin_amdgcn_permlane16_swap(__float_as_uint(x), __float_as_uint(x), false, false);
  x = fmaxf(__uint_as_float(s[0]), __uint_as_float(s[1]));
  auto t = __builtin_amdgcn_permlane32_swap(__float_as_uint(x), __float_as_uint(x), false, false);
  return fmaxf(__uint_as_float(t[0]), __uint_as_float(t[1]));
}
template <int CTRL> __device__ __forceinline__ float sel_add(float lo, float hi, bool up) {
  const float t1 = lo + dpp<CTRL>(lo), t2 = hi + dpp<CTRL>(hi);
  return up ? t2 : t1;
}
template <int I> __device__ __forceinline__ float bc16(float x) {
  return __builtin_bit_cast(float, __builtin_amdgcn_update_dpp(0, __builtin_bit_cast(int, x), 0x150 + I, 0xf, 0xf, false));
}
__device__ __forceinline__ float bcast16(float x, int i) {
  switch (i) {
    case 0: return bc16<0>(x); case 1: return bc16<1>(x); case 2: return bc16<2>(x); case 3: return bc16<3>(x);
    case 4: return bc16<4>(x); case 5: return bc16<5>(x); case 6: return bc16<6>(x); case 7: return bc16<7>(x);
    case 8: return bc16<8>(x); case 9: return bc16<9>(x); case 10: return bc16<10>(x); case 11: return bc16<11>(x);
    case 12: return bc16<12>(x); case 13: return bc16<13>(x); case 14: return bc16<14>(x); default: return bc16<15>(x);
  }
}
__device__ __forceinline__ float silu_f(float x) { return x * __builtin_amdgcn_rcpf(1.f + __expf(-x)); }

template <int AUX> __device__ __forceinline__ u32x4 ld16(rsrc_t r, u32 voff, u32 soff) {
  return __builtin_amdgcn_raw_buffer_load_b128(r, voff, soff, AUX);
}
template <int AUX> __device__ __forceinline__ u32x2 ld8(rsrc_t r, u32 voff, u32 soff) {
  return __builtin_amdgcn_raw_buffer_load_b64(r, voff, soff, AUX);
}
__device__ __forceinline__ void st8_sc1(rsrc_t r, u32 voff, u32 v, u32 tag) {
  __builtin_amdgcn_raw_buffer_store_b64(u32x2{v, tag}, r, voff, 0, AUX_SC1);
}
__device__ __forceinline__ void st16_sc1(rsrc_t r, u32 voff, u32 v0, u32 v1, u32 tag) {
  __builtin_amdgcn_raw_buffer_store_b128(u32x4{v0, tag, v1, tag}, r, voff, 0, AUX_SC1);
}
__device__ __forceinline__ rsrc_t mk_rsrc(const void* p, u32 bytes) {
  return __builtin_amdgcn_make_buffer_rsrc(const_cast<void*>(p), 0, bytes, 0x00020000);
}
#define WAIT_VM(n) __builtin_amdgcn_s_waitcnt(((n) & 15) | (((n) >> 4) << 14) | (7 << 4) | (15 << 8))
#define SB() __builtin_amdgcn_sched_barrier(0)
#define CFENCE() asm volatile("" ::: "memory")
__device__ __forceinline__ bool all_ok(bool ok) { return __builtin_amdgcn_ballot_w64(!ok) == 0ull; }

template <int N> __device__ __forceinline__ void gather16(rsrc_t ws, u32 voff, u32 stride, u32 tag, u32x4 (&g)[N]) {
#pragma nounroll
  for (int spin = 0; spin < SPIN_MAX; ++spin) {
    CFENCE();
    bool ok = true;
#pragma unroll
    for (int i = 0; i < N; ++i) g[i] = ld16<AUX_SC1>(ws, voff + i * stride, 0);
#pragma unroll
    for (int i = 0; i < N; ++i) ok &= (g[i][1] == tag) & (g[i][3] == tag);
    if (all_ok(ok)) break;
    __builtin_amdgcn_s_sleep(8);
  }
}
template <int N> __device__ __forceinline__ void gather8(rsrc_t ws, u32 voff, u32 stride, u32 tag, u32 (&g)[N], bool act = true) {
#pragma nounroll
  for (int spin = 0; spin < SPIN_MAX; ++spin) {
    CFENCE();
    bool ok = true;
    u32x2 t[N];
#pragma unroll
    for (int i = 0; i < N; ++i) t[i] = ld8<AUX_SC1>(ws, voff + i * stride, 0);
#pragma unroll
    for (int i = 0; i < N; ++i) { ok &= (t[i][1] == tag); g[i] = t[i][0]; }
    if (all_ok(ok | !act)) break;
    __builtin_amdgcn_s_sleep(8);
  }
}

struct Smem {
  float red[8][128];
  float ml[8][2];
  float r2[64 * 20];
  u16 hb[3 * 1024];
  float stat[8][64];
  float red2[8][128];
  u32 dummy[64];
  u32x2 bc;
  u32x4 zero[4];
  float pl[4][8];
  u32 mflag[8];
  u32x4 wbuf[8][8][64];
  __attribute__((aligned(256))) u16 w2b[32][512];
  u16 actl[2][32];
};

#define PROBE_K 8
#define PROBE_OP 0
#define PROBE_PART -1
#define STAMP(k) do { if ((k) == PROBE_K && tid == 0) pst = wall_clock64(); } while (0)
#define WSTAMP(k) do {} while (0)
#define FINISH() do { STAMP(8); if (tid == 0) { __hip_atomic_store(reinterpret_cast<u64*>(ws + (1u << 20)) + bid * 2, pt0 + (u32)(t0 - (u32)pt0) + (t1 >> 8), __ATOMIC_RELAXED, __HIP_MEMORY_SCOPE_AGENT); \
   __hip_atomic_store(reinterpret_cast<u64*>(ws + (1u << 20)) + bid * 2 + 1, (PROBE_PART < 0 || part == PROBE_PART) ? pst : 0ull, __ATOMIC_RELAXED, __HIP_MEMORY_SCOPE_AGENT); } \
   if (bid == 8 && wave == 0) probe_end(ws, pt0, lane); return; } while (0)
#define PIN(x) asm volatile("" : "+v"(x))
#define LDS_BARRIER() do { __builtin_amdgcn_s_waitcnt(0xc07f); __builtin_amdgcn_s_barrier(); CFENCE(); } while (0)
#define WAVE_SYNC() do { __builtin_amdgcn_fence(__ATOMIC_RELEASE, "wavefront"); __builtin_amdgcn_wave_barrier(); __builtin_amdgcn_fence(__ATOMIC_ACQUIRE, "wavefront"); } while (0)

__device__ __forceinline__ void dma16(rsrc_t r, u32 voff, u32 soff, u32 lds_dst) {
  u32 keep;
  asm volatile("s_mov_b32 %0, m0\n\ts_mov_b32 m0, %4\n\ts_nop 4\n\tbuffer_load_dwordx4 %1, %2, %3 offen nt lds\n\ts_mov_b32 m0, %0"
               : "=&s"(keep) : "v"(voff), "s"(r), "s"(soff), "s"(lds_dst) : "memory");
}
__device__ __forceinline__ void ld16_into(u32x4& d, rsrc_t r, u32 voff, u32 soff) {
  asm volatile("s_nop 4\n\tbuffer_load_dwordx4 %0, %1, %2, %3 offen nt" : "+v"(d) : "v"(voff), "s"(r), "s"(soff) : "memory");
}
__device__ __forceinline__ u32 perm_lo(u32 a, u32 b) { return __builtin_amdgcn_perm(b, a, 0x05040100u); }
__device__ __forceinline__ u32 perm_hi(u32 a, u32 b) { return __builtin_amdgcn_perm(b, a, 0x07060302u); }
__device__ __forceinline__ float rdl(float x, int l) { return __int_as_float(__builtin_amdgcn_readlane(__float_as_int(x), l)); }

typedef __attribute__((ext_vector_type(8))) __bf16 bf16x8;
__device__ __forceinline__ f32x4 mfma16(u32x4 a, u32x4 b, f32x4 c) {
  return __builtin_amdgcn_mfma_f32_16x16x32_bf16(__builtin_bit_cast(bf16x8, a), __builtin_bit_cast(bf16x8, b), c, 0, 0, 0);
}
__device__ __forceinline__ float sum8(const float* p) {
  const f32x4 a = *reinterpret_cast<const f32x4*>(p), b = *reinterpret_cast<const f32x4*>(p + 4);
  return ((a[0] + b[0]) + (a[1] + b[1])) + ((a[2] + b[2]) + (a[3] + b[3]));
}
__device__ __forceinline__ float ln_ri(float var) {
  return __builtin_amdgcn_rcpf(rbf(__builtin_amdgcn_sqrtf(rbf(rbf(fmaxf(var, 0.f)) + 1e-5f))));
}
__device__ __forceinline__ void ln_stats2(const float (&x0)[8], const float (&x1)[8], float& mb0, float& ri0, float& mb1, float& ri1) {
  const float k0 = rdl(x0[0], 0), k1 = rdl(x1[0], 0);
  float s0 = 0.f, s1 = 0.f, q0 = 0.f, q1 = 0.f;
#pragma unroll
  for (int k = 0; k < 8; ++k) {
    const float d0 = x0[k] - k0, d1 = x1[k] - k1;
    s0 += d0; s1 += d1; q0 = fmaf(d0, d0, q0); q1 = fmaf(d1, d1, q1);
  }
  float t = swap16_add(swap32_add(s0, s1), swap32_add(q0, q1));
  t += dpp<XOR1>(t); t += dpp<XOR2>(t); t += dpp<XOR7>(t); t += dpp<XOR8>(t);
  auto u = __builtin_amdgcn_permlane16_swap(__float_as_uint(t), __float_as_uint(t), false, false);
  const float ms = __uint_as_float(u[0]) * (1.f / 512.f), mq = __uint_as_float(u[1]) * (1.f / 512.f);
  const float ri = ln_ri(fmaf(-ms, ms, mq));
  mb0 = rbf(k0 + rdl(ms, 0)); mb1 = rbf(k1 + rdl(ms, 32));
  ri0 = rdl(ri, 0); ri1 = rdl(ri, 32);
}
template <int I> struct IC { static constexpr int v = I; };
template <int I, int N, class F> __device__ __forceinline__ void sfor(F&& f) {
  if constexpr (I < N) { if (f(IC<I>{})) sfor<I + 1, N>(f); }
}
typedef __attribute__((ext_vector_type(4))) short s16x4;
typedef __attribute__((address_space(3))) s16x4* lds_s16x4p;
__device__ __forceinline__ u32x2 tr_read(u32 lds_addr) {
  return __builtin_bit_cast(u32x2, __builtin_amdgcn_ds_read_tr16_b64_v4i16((lds_s16x4p)lds_addr));
}
__device__ __forceinline__ u32 next_tag(u32 e) { return ((e | 0x40004000u) + 1u) & ~0x40004000u; }
__device__ __forceinline__ float u2f(u32 v) { return __uint_as_float(v); }
#define LDSP(T, a) ((__attribute__((address_space(3))) T*)(a))
template <class T> __device__ __forceinline__ u32 lds_addr(T* p) { u32 a = (u32)(uintptr_t)p; PIN(a); return a; }

#define TAB_LOAD "s_add_u32 s30, s28, wg_tab@rel32@lo+4\ns_addc_u32 s31, s29, wg_tab@rel32@hi+12\nglobal_load_dwordx2 %[te], %[va], s[30:31]\n"
#define FC(k) ".Lfc" #k
#define FC_REGS
#define FX(r, bit, k) "s_bitcmp1_b32 %[" #r "], " #bit "\ns_cselect_b32 %[sb], " FC(k) ", 0\ns_xor_b32 %[sa], %[sa], %[sb]\n"
__device__ __forceinline__ void probe_end(unsigned char* ws, u64 pt0, int lane) {
  while (wall_clock64() < pt0 + 5500ull) __builtin_amdgcn_s_sleep(8);
  u64 a[4], c2[4];
  for (int i = 0; i < 4; ++i) {
    a[i] = __hip_atomic_load(reinterpret_cast<u64*>(ws + (1u << 20)) + (i * 64 + lane) * 2, __ATOMIC_RELAXED, __HIP_MEMORY_SCOPE_AGENT);
    c2[i] = __hip_atomic_load(reinterpret_cast<u64*>(ws + (1u << 20)) + (i * 64 + lane) * 2 + 1, __ATOMIC_RELAXED, __HIP_MEMORY_SCOPE_AGENT);
  }
  int mn0 = 1 << 30, mx1 = -(1 << 30), mn1 = 1 << 30, sum = 0, cnt = 0;
  for (int i = 0; i < 4; ++i) {
    const int d0 = (int)(a[i] - pt0);
    mn0 = min(mn0, d0);
    if (c2[i] != 0ull) { const int d1 = (int)(c2[i] - pt0); mx1 = max(mx1, d1); mn1 = min(mn1, d1); sum += d1; cnt += 1; }
  }
  for (int o = 1; o < 64; o <<= 1) { mn0 = min(mn0, __shfl_xor(mn0, o)); mx1 = max(mx1, __shfl_xor(mx1, o)); mn1 = min(mn1, __shfl_xor(mn1, o)); sum += __shfl_xor(sum, o); cnt += __shfl_xor(cnt, o); }
  int X = PROBE_OP == 0 ? mx1 : (PROBE_OP == 1 ? sum / max(cnt, 1) : mn1);
  if (PROBE_K == 102) X = mn0;
  X = mn0 + (X - mn0) * PROBE_MUL;
  const u64 tg = pt0 + (u64)(long long)(X + 8000);
  for (int i = 0; i < 400000 && wall_clock64() < tg; ++i) __builtin_amdgcn_s_sleep(1);
}
__global__ void __launch_bounds__(512, 2) fused_kernel(const u16* kc_, const u16* vc_, const u16* wo_, const u16* wq_, const u16* wmod_, const u16* w1_, const u16* w3_,
                                                       unsigned char* ws_, const u16* x_, const u16* cnd_, const u16* w2_, u16* out_) {
  __shared__ __attribute__((aligned(256))) Smem sm;
  const u64 a_ka = (u64)__builtin_amdgcn_kernarg_segment_ptr();
  const int bid = blockIdx.x, tid = threadIdx.x;
  u64 wa, t64; u32x2 te; int tid0;
  u32 ar[14];
  u32 tch2, sa_, sb_, va_;
  u32x4 kr[NL], vr[NL];
  asm volatile(".set .Lfc0, %0\n.set .Lfc1, %1\n.set .Lfc2, %2\n.set .Lfc3, %3\n.set .Lfc4, %4\n.set .Lfc5, %5\n.set .Lfc6, %6\n.set .Lfc7, %7\n.set .Lfc8, %8\n.set .Lfc9, %9\n.set .Lfc10, %10\n.set .Lfc11, %11"
    :: "n"(first_col(PRM0, 0)), "n"(first_col(PRM0, 1)), "n"(first_col(PRM0, 2)), "n"(first_col(PRM0, 3)), "n"(first_col(PRM0, 4)), "n"(first_col(PRM0, 5)),
       "n"(first_col(PRM0, 6)), "n"(first_col(PRM0, 7)), "n"(first_col(PRM0, 8)), "n"(first_col(PRM0, 9)), "n"(first_col(PRM0, 10)), "n"(first_col(PRM0, 11)));
  asm volatile(
    "s_load_dwordx16 s[4:19], %[ka], 0x0\ns_load_dwordx8 s[20:27], %[ka], 0x40\n"
    "s_memrealtime %[t]\n"
    "v_lshlrev_b32 %[va], 3, %[bid]\n"
    "s_getpc_b64 s[28:29]\n.Lcpf_first:\n"
    TAB_LOAD
    "s_cmp_gt_u32 %[bid], 15\ns_cbranch_scc1 .Lno_cpf\n"
    "s_mov_b32 %[sa], .Lend_a-.Lcpf_first\ns_max_i32 %[sa], %[sa], .Lend_b-.Lcpf_first\ns_max_i32 %[sa], %[sa], .Lend_c-.Lcpf_first\ns_max_i32 %[sa], %[sa], .Lend_d-.Lcpf_first\ns_max_i32 %[sa], %[sa], .Lend_e-.Lcpf_first\n"
    "v_lshlrev_b32 %[va], 6, %[tid]\nv_min_u32 %[va], %[sa], %[va]\nglobal_load_dword %[pf], %[va], s[28:29]\n"
    ".Lno_cpf:\n"
    "v_readfirstlane_b32 %[t0], %[tid]\nv_and_b32 %[va], 63, %[tid]\n"
    "s_mov_b32 %[sa], " FC(11) "\n"
    FX(bid, 0, 0) FX(bid, 1, 1) FX(bid, 2, 2) FX(bid, 3, 3) FX(bid, 4, 4) FX(bid, 5, 5) FX(bid, 6, 6) FX(bid, 7, 7) FX(t0, 6, 8) FX(t0, 7, 9) FX(t0, 8, 10)
    "v_lshl_add_u32 %[va], %[va], 4, %[sa]\n"
    "s_waitcnt lgkmcnt(0)\nglobal_load_dwordx4 %[k0], %[va], s[4:5] nt\nglobal_load_dwordx4 %[v0], %[va], s[6:7] nt\n"
    "s_bitcmp1_b32 %[t0], 6\ns_cselect_b64 s[14:15], s[16:17], s[14:15]\n"
    "s_bitcmp1_b32 %[bid], 4\ns_cselect_b64 s[8:9], s[10:11], s[8:9]\ns_cselect_b64 s[12:13], s[14:15], s[12:13]\ns_bitcmp1_b32 %[bid], 5\ns_cselect_b64 %[wa], s[12:13], s[8:9]"
    : "=&{s4}"(ar[0]), "=&{s5}"(ar[1]), "=&{s6}"(ar[2]), "=&{s7}"(ar[3]), "=&{s18}"(ar[4]), "=&{s19}"(ar[5]), "=&{s20}"(ar[6]), "=&{s21}"(ar[7]), "=&{s22}"(ar[8]), "=&{s23}"(ar[9]),
      "=&{s24}"(ar[10]), "=&{s25}"(ar[11]), "=&{s26}"(ar[12]), "=&{s27}"(ar[13]), [wa] "=&s"(wa), [t0] "=&s"(tid0), [t] "=&s"(t64), [te] "=&v"(te), [sa] "=&s"(sa_), [sb] "=&s"(sb_),
      [va] "=&v"(va_), [pf] "=&v"(tch2), [k0] "=&v"(kr[0]), [v0] "=&v"(vr[0])
    : [ka] "s"(a_ka), [bid] "s"(bid), [tid] "v"(tid) : "s8", "s9", "s10", "s11", "s12", "s13", "s14", "s15", "s16", "s17", "s28", "s29", "s30", "s31", "scc", "memory" FC_REGS);
  asm volatile("" : "+s"(ar[0]), "+s"(ar[1]), "+s"(ar[2]), "+s"(ar[3]), "+s"(ar[4]), "+s"(ar[5]), "+s"(ar[6]), "+s"(ar[7]), "+s"(ar[8]), "+s"(ar[9]), "+s"(ar[10]), "+s"(ar[11]), "+s"(ar[12]), "+s"(ar[13]));
  asm volatile("" : "+s"(wa), "+s"(t64), "+s"(tid0), "+s"(sa_));
  const u32 tka = (u32)wall_clock64();
  const u64 pt0 = t64; u64 pst = (PROBE_K == 100 || PROBE_K == 102) ? pt0 : (PROBE_K == 101 ? pt0 + (u32)(tka - (u32)t64) : 0);
  const u32 t0 = tka;
  __builtin_assume(tid >= 0 && tid < 512 && tid0 >= 0 && tid0 < 512 && bid >= 0 && bid < 256);
  constexpr Prm P = PRM0;
  const int TEND = P.TEND;
  const int xcd = bid & 7, part = bid >> 4 & 3;
  const int lane = tid & 63, wave = tid0 >> 6;
  SB();
  const bool p3 = part == 3;
  u32 pitch = (u32)(0x005600c001000020ull >> (part * 16) & 0xffff) * 32u;
  const int sh = p3 ? 2 : 3;
  u32 wv0 = (u32)((p3 ? (wave >> 1) * 128 : wave * 64) + (part == 0 ? xcd * 512 : 0) + (lane >> sh)) * pitch + ((lane & (p3 ? 3 : 7)) ^ (lane >> (p3 ? 5 : 3))) * 16u;
  const u32 wimg = (u32)(uintptr_t)(&sm.wbuf[0][0][0]);
  u32 wsz = (part == 0 ? 4096u : 512u) * pitch, wld = wimg + (p3 ? (wave & 1) * 32768 + (wave >> 1) * 8192 : wave * 8192);
  PIN(wv0);
  asm volatile("" : "+s"(wsz), "+s"(wld), "+s"(pitch));
  auto karg = [&](int i) { return ar[2 * i] | (u64)ar[2 * i + 1] << 32; };
  const u64 a_kc = karg(0), a_vc = karg(1);
  unsigned char* ws = (unsigned char*)karg(2);
  const u16 *x = (const u16*)karg(3), *cnd = (const u16*)karg(4), *w2 = (const u16*)karg(5);
  u16* out = (u16*)karg(6);
  const rsrc_t wsr = mk_rsrc(ws, WS_END);
  asm volatile("s_waitcnt vmcnt(2)" : "+v"(te));
  const u32 te0 = __builtin_amdgcn_readfirstlane(te[0]), te1 = __builtin_amdgcn_readfirstlane(te[1]);
  const int bl0 = te0 & 127, nl = te0 >> 7 & 63, blk = te0 >> 13 & 63;
  const bool has_w = te0 >> 19 & 1;
  const rsrc_t rw = mk_rsrc((const void*)wa, wsz);
  const u32 wstep = pitch << (6 - sh), wflip = p3 ? 32u : 0u;
  u32 wso = blk << (sh + 4);
  int wleft = 8;
  auto piece = [&]() {
    dma16(rw, wv0, wso, wld);
    wso += wstep; wld += 1024;
    wv0 ^= (wleft & 1) ? wflip : 0u;
    --wleft;
  };
  SB();
  STAMP(23);
  const int b = bid >> 3 & 1, head = xcd * 4 + (bid >> 6 ^ 3);
  const int g = lane >> 4, c = lane & 15;
  const int i8 = b | bid >> 6 << 1, wi = i8 * 8 + xcd;
  const bool is_up = p3 && has_w, modc = part == 2 && wi < 16;
  const size_t base = ((size_t)(b * NH + head) * SK + (SK - WIN)) * DH;
  const rsrc_t rk = mk_rsrc((const u16*)a_kc + base, WIN * DH * 2), rv = mk_rsrc((const u16*)a_vc + base, WIN * DH * 2);
  int tau = te1 & 0xffff;
  const int tauw = te0 >> 24 << 7, wdef = te0 >> 20 & 15;
  int due = P.NF * 256, now = 0;
  auto gate = [&]() {
    while (now < due) { now = (int)(((u32)wall_clock64() - t0) << 8); if (now < due) __builtin_amdgcn_s_sleep(1); }
    due += tau;
  };
  auto until = [&](int t) { while ((int)(((u32)wall_clock64() - t0) << 8) < t); };
  const u32 pslot = dup(G_P + ((b * 32 + head) * 4 + part) * 256);
  const u32 die = __builtin_amdgcn_s_getreg(20 | (2 << 6));
  const bool voter = p3 && i8 == 1, reporter = p3 && i8 < 2;
  int vote = 0, lag = 0;
  const u32 pbo = dup(G_PB) + (xcd * 8 + wave) * 128;
  u32 near = 0;
  u32x2 ep; u32 xs0, xs1, tch1; u32x4 xr0, xr1;
  const rsrc_t rx = mk_rsrc(part == 2 ? cnd : x, (part == 1 || (part == 2 && has_w)) ? 2048u : 0u);
  auto small_ops = [&]() {
    ep = ld8<AUX_SC1>(wsr, wave == 7 ? WS_EPOCH : OOB, 0);
    xs0 = __builtin_amdgcn_raw_buffer_load_b16(rx, tid * 2, 0, 0); xs1 = __builtin_amdgcn_raw_buffer_load_b16(rx, tid * 2, 1024, 0);
    xr0 = ld16<0>(rx, part == 1 ? lane * 16 : OOB, 0); xr1 = ld16<0>(rx, part == 1 ? lane * 16 : OOB, 1024);
    tch1 = __builtin_amdgcn_raw_buffer_load_b32(mk_rsrc(w2 + (size_t)blk * 32 * 512, is_up ? 64u : 0u), tid == 384 ? 0 : OOB, 0, AUX_NT);
  };
  if (has_w) {
    if (modc) now = 1 << 30;
#pragma nounroll
    while (wleft > wdef) { gate(); due += tauw - tau; piece(); }
    now = 0;
  }
  due = max(due, P.TK * 256);
  SB();
  u32 sot = sa_ & 0xffc00u;
  {
    const u32 s1 = (bl0 + lane - 1 - (lane >= SK0 ? NL - nl : 0)) ^ (124 + part);
#pragma unroll
    for (int k = 0; k < 7; ++k) sot ^= (u32)-(int)(s1 >> k & 1u) & col(P, k);
    PIN(sot);
  }
  u32 tag = 0;
  u32x4 bf[8];
  auto wo_prep = [&](int s0 = 0, int s1 = 2) {
    const u32 wi_ = wimg + wave * 8192;
    const int q = (lane >> 2) & 3, pp = lane & 3;
#pragma unroll
    for (int s = s0; s < s1; ++s) {
#pragma unroll
      for (int t = 0; t < 4; ++t) {
        const int r0 = s * 32 + g * 8 + q, r1 = r0 + 4, ch = t * 2 + (pp >> 1);
        const u32x2 lo = tr_read(wi_ + r0 * 128 + ((ch ^ (r0 & 7)) * 16) + (pp & 1) * 8), hi = tr_read(wi_ + r1 * 128 + ((ch ^ (r1 & 7)) * 16) + (pp & 1) * 8);
        bf[s * 4 + t] = u32x4{lo[0], lo[1], hi[0], hi[1]};
      }
    }
  };
  auto gemv_go = [&](u32 hh) {
    sm.hb[tid] = (u16)hh; sm.hb[512 + tid] = (u16)(hh >> 16);
    WAVE_SYNC();
    const u32x4* a = (c & 11) == 0 ? reinterpret_cast<const u32x4*>(&sm.hb[(c >> 2) * 512 + wave * 64 + g * 8]) : &sm.zero[0];
    const u32x4 a0 = a[0], a1 = a[(c & 11) == 0 ? 4 : 0];
    const f32x4 z = {0.f, 0.f, 0.f, 0.f};
    f32x4 d[4];
#pragma unroll
    for (int t = 0; t < 4; ++t) d[t] = mfma16(a0, bf[t], z);
#pragma unroll
    for (int t = 0; t < 4; ++t) d[t] = mfma16(a1, bf[4 + t], d[t]);
    if (lane < 32) {
#pragma unroll
      for (int t = 0; t < 4; ++t) sm.r2[(t * 16 + c) * 20 + g * 8 + wave] = d[t][0];
    }
  };
  auto mod_stage = [&]() {
    wo_prep();
    gemv_go(pack_bf(silu_f(lo_f(xs0)), silu_f(lo_f(xs1))));
    __builtin_amdgcn_s_waitcnt(0xc07f);
    if (lane == 0) *LDSP(volatile u32, (u32)(uintptr_t)&sm.mflag[wave]) = 1u;
    if (wave == 7) {
#pragma nounroll
      for (int spin = 0; spin < SPIN_MAX; ++spin) {
        const u32 f = *LDSP(volatile u32, (u32)(uintptr_t)&sm.mflag[lane & 7]);
        if (all_ok(f != 0u)) break;
        __builtin_amdgcn_s_sleep(1);
      }
      CFENCE();
      st8_sc1(wsr, G_MOD + (blk * 64 + lane) * 8, pack_bf(sum8(&sm.r2[lane * 20]), sum8(&sm.r2[lane * 20 + 8])), tag);
    }
    STAMP(9);
  };
  if (lane < 4) sm.zero[lane] = u32x4{0u, 0u, 0u, 0u};
  if (part == 2 && has_w) {
    if (tid < 8) sm.mflag[tid] = 0u;
    LDS_BARRIER();
    if (__builtin_expect(modc, 0)) {
      small_ops();
      __builtin_amdgcn_raw_buffer_store_b128(u32x4{SENT, SENT, SENT, SENT}, wsr, G_U + wi * 10240 + tid * 16, 0, AUX_SC1);
      __builtin_amdgcn_raw_buffer_store_b128(u32x4{SENT, SENT, SENT, SENT}, wsr, tid < 128 ? G_U + wi * 10240 + tid * 16 : OOB, 8192, AUX_SC1);
      asm volatile("s_waitcnt vmcnt(0)" ::: "memory");
      tag = next_tag(__builtin_amdgcn_readfirstlane(ep[0]));
      mod_stage();
      __builtin_amdgcn_s_barrier();
    }
  }
  SB();
  const int fd = wi * 8 + (lane & 7);
  const int ub = wave >> 2, ud = (wave & 3) * 128 + c * 8 + g * 2;
  u32x4 sg[3]; u32 sx2;
  auto side_loads = [&]() {
    const bool fin = part == 1 && wave == 0;
    sg[0] = ld16<AUX_SC1>(wsr, is_up ? G_MOD + (1024 + ud) * 8 : (fin ? G_MOD + (1024 + fd) * 8 : OOB), 0);
    sg[1] = ld16<AUX_SC1>(wsr, is_up ? G_MOD + (1536 + ud) * 8 : (fin ? G_MOD + (2560 + fd) * 8 : OOB), 0);
    sg[2] = ld16<AUX_SC1>(wsr, is_up ? G_MOD + (2048 + ud) * 8 : OOB, 0);
    sx2 = __builtin_amdgcn_raw_buffer_load_b32(mk_rsrc(x, (is_up || fin) ? 2048u : 0u), is_up ? ub * 1024 + ud * 2 : (g & 1) * 1024 + (fd >> 1) * 4, 0, 0);
  };
  auto side_pin = [&]() { PIN(sg[0]); PIN(sg[1]); PIN(sg[2]); PIN(sx2); };
  const u32 mlo = G_ML + (head * 2 + b) * 64 + (lane & 3) * 16;
  float fmrg = 0.f;
  auto merge_factor = [&](const u32x4& ml) {
    const float mi = u2f(ml[0]);
    float M = fmaxf(mi, dpp<XOR1>(mi)); M = fmaxf(M, dpp<XOR2>(M));
    const float e = __expf(mi - M);
    float L = e * u2f(ml[2]);
    L += dpp<XOR1>(L); L += dpp<XOR2>(L);
    fmrg = rdl(e * __builtin_amdgcn_rcpf(L), part);
  };
  f32x2 ga = {0.f, 0.f}, xin = {0.f, 0.f}, cc1 = {0.f, 0.f};
  float Af = 0.f, Bf = 0.f;
  const int hpos = ub * 512 + ud;
  auto up_prep = [&]() {
    if (!all_ok((sg[0][1] == tag) & (sg[0][3] == tag) & (sg[1][1] == tag) & (sg[1][3] == tag) & (sg[2][1] == tag) & (sg[2][3] == tag))) gather16<3>(wsr, G_MOD + (1024 + ud) * 8, 512 * 8, tag, sg);
    ga = f32x2{ub ? hi_f(sg[0][0]) : lo_f(sg[0][0]), ub ? hi_f(sg[0][2]) : lo_f(sg[0][2])};
    cc1 = f32x2{rbf(1.f + (ub ? hi_f(sg[2][0]) : lo_f(sg[2][0]))), rbf(1.f + (ub ? hi_f(sg[2][2]) : lo_f(sg[2][2])))};
    xin = f32x2{lo_f(sx2), hi_f(sx2)};
    u32* hp = reinterpret_cast<u32*>(&sm.hb[hpos]);
    hp[512] = pack_bf(cc1[0], cc1[1]);
    hp[1024] = ub ? perm_hi(sg[1][0], sg[1][2]) : perm_lo(sg[1][0], sg[1][2]);
  };
  auto up_frags = [&]() {
    const int pp = lane & 3;
    const u32 a = wimg + (pp & 1) * 32768 + ((pp >> 1) * 32 + g * 8 + ((lane >> 2) & 3)) * 64 + (((wave >> 1) ^ ((g & 1) | (pp & 2))) * 16) + (wave & 1) * 8;
#pragma unroll
    for (int rb = 0; rb < 8; ++rb) {
      const u32x2 lo = tr_read(a + rb * 4096), hi = tr_read(a + rb * 4096 + 256);
      bf[rb] = u32x4{lo[0], lo[1], hi[0], hi[1]};
    }
  };
  const u32x4* ap = reinterpret_cast<const u32x4*>(&sm.hb[((lane >> 3) & 1) * 512 + (lane & 1) * 32 + g * 8]);
  const bool par = lane & 8;
  auto blog = [&](int) {};
  auto limit = [&](auto sc) {
    constexpr int s = sc.v;
    WAIT_VM(window(s, 1 + (PRM0.W[s >> 3] >> (4 * (s & 7)) & 7)));
    SB();
  };
  float l0 = 0.f, l1 = 0.f;
  u32x2 gmd[2], tbg;
  int t1 = 0;
#pragma unroll
  for (int i = SK0; i < SK1; ++i) kr[i] = u32x4{0u, 0u, 0u, 0u};
  const int first = SK0 + NL - nl;
  auto left_out = [&](int i) { int f = first; asm volatile("" : "+s"(f)); return i < f; };
  sfor<1, NL>([&](auto ic) {
    constexpr int i = ic.v;
    if constexpr (i >= SK0 && i < SK1) { if (left_out(i)) return true; }
    const u32 so = __builtin_amdgcn_readlane(sot, i);
    gate();
    blog(i);
    if constexpr (i >= RP0 && i < NL - 1) { if (reporter) lag += (int)(((u32)wall_clock64() - t0) << 8) - due + ((int)__builtin_amdgcn_s_getreg(7 | (3 << 11)) + 1) * tau; }
    SB();
    if constexpr (i >= SK0 && i < SK1) ld16_into(kr[i], rk, lane * 16, so); else kr[i] = ld16<AUX_NT>(rk, lane * 16, so);
    if constexpr (i == SMO) small_ops();
    if constexpr (i == SMO + 1) { if (voter) __builtin_amdgcn_raw_buffer_store_b32(0u, wsr, pbo + (lane >> 5) * 8192 + (lane & 31) * 4, 0, AUX_SC1); }
    if constexpr (i == TBL) tbg = ld8<AUX_SC1>(wsr, wave == 7 ? G_TB + xcd * 128 + (lane & 7) * 8 : OOB, 0);
    if constexpr (i == QP) { gmd[0] = ld8<AUX_SC1>(wsr, part == 1 ? G_MOD + tid * 8 : OOB, 0); gmd[1] = ld8<AUX_SC1>(wsr, part == 1 ? G_MOD + tid * 8 : OOB, 4096); }
    SB();
    SB();
    if (i >= 4) limit(ic);
    if (i == 1) STAMP(25);
    if (i == 8) STAMP(12);
    if (i == 20) STAMP(21);
    if (i == 32) STAMP(22);
    if (i == TBP) {
      if (part == 3 && i8 == 0 && wave == 7) { tag = next_tag(__builtin_amdgcn_readfirstlane(ep[0])); if (lane < 8) st8_sc1(wsr, G_TB + lane * 128 + xcd * 8, t0 - P.GS, tag); }
      SB();
    }
    if (i == PRB) {
      if (voter) {
        u64 ta, tb, tc; u32 dm;
        const u32 pa = pbo + (wave & 1) * 8192;
        asm volatile("s_memtime %0\n\ts_waitcnt lgkmcnt(0)\n\ts_load_dword %3, %4, %5\n\ts_waitcnt lgkmcnt(0)\n\ts_memtime %1\n\ts_waitcnt lgkmcnt(0)\n\t"
                     "s_load_dword %3, %4, %6\n\ts_waitcnt lgkmcnt(0)\n\ts_memtime %2\n\ts_waitcnt lgkmcnt(0)"
                     : "=&s"(ta), "=&s"(tb), "=&s"(tc), "=&s"(dm) : "s"(ws), "s"(pa), "s"(pa ^ 8192u));
        const int d = min(max((int)(2 * (u32)tb - (u32)ta - (u32)tc), -500), 500);
        vote = ((wave ^ die) & 1) ? -d : d;
        STAMP(26);
      }
      SB();
    }
    if (i == TBU) {
      if (wave == 7) {
        const bool ok = tbg[1] == tag;
        float lf = ok ? (float)min(max((int)(t0 - tbg[0]), -300), 300) : 0.f, nf = ok ? 1.f : 0.f;
        lf += dpp<XOR1>(lf); nf += dpp<XOR1>(nf); lf += dpp<XOR2>(lf); nf += dpp<XOR2>(nf); lf += dpp<XOR7>(lf); nf += dpp<XOR7>(nf);
        if (lane == 0) sm.bc = u32x2{tag, t0 - (u32)(int)(lf * __builtin_amdgcn_rcpf(fmaxf(nf, 1.f)))};
      }
      LDS_BARRIER();
      const u32x2 bc = sm.bc;
      tag = __builtin_amdgcn_readfirstlane(bc[0]);
      t1 = (__builtin_amdgcn_readfirstlane((int)(bc[1] - t0)) + TEND + P.X1) * 256;
      tau -= __builtin_amdgcn_readfirstlane((int)((float)(int)(t0 - bc[1]) * 256.f * __builtin_amdgcn_rcpf((float)(nl - TBU - 1))));
      SB();
    }
    if (i == NL - 1) {
      if (reporter) {
        if (lane == 0) sm.mflag[wave] = vote * 65536 + min(lag >> 8, 4000);
        LDS_BARRIER();
        if (wave == 7) {
          int s = sm.mflag[lane & 7];
          s += __builtin_amdgcn_mov_dpp(s, XOR1, 0xf, 0xf, true); s += __builtin_amdgcn_mov_dpp(s, XOR2, 0xf, 0xf, true); s += __builtin_amdgcn_mov_dpp(s, XOR7, 0xf, 0xf, true);
          if (lane < 8) st8_sc1(wsr, G_VT + lane * 128 + wi * 8, s, tag);
        }
      }
      SB();
    }
    if (i == LNS) {
      tag = next_tag(__builtin_amdgcn_readfirstlane(ep[0]));
      if (part == 1) {
        float x0[8], x1[8], mb0, ri0, mb1, ri1;
#pragma unroll
        for (int k = 0; k < 4; ++k) { x0[2 * k] = lo_f(xr0[k]); x0[2 * k + 1] = hi_f(xr0[k]); x1[2 * k] = lo_f(xr1[k]); x1[2 * k + 1] = hi_f(xr1[k]); }
        ln_stats2(x0, x1, mb0, ri0, mb1, ri1);
        l0 = rbf(rbf(lo_f(xs0) - mb0) * ri0); l1 = rbf(rbf(lo_f(xs1) - mb1) * ri1);
      }
      PIN(l0); PIN(l1);
      SB();
    }
    if (i == WUSE) {
      PIN(tch1); PIN(tch2);
      if ((part == 1 || (part == 2 && has_w && !modc))) {
        WAIT_VM(WK); CFENCE();
        if (part == 2) mod_stage(); else wo_prep();
      }
#pragma unroll
      for (int k = 0; k < 8; ++k) PIN(bf[k]);
      SB();
    }
    if (i == QP + QD) {
      if (part == 1) {
        u32 gm[2] = {gmd[0][0], gmd[1][0]};
        if (!all_ok((gmd[0][1] == tag) & (gmd[1][1] == tag))) { STAMP(11); gather8<2>(wsr, G_MOD + tid * 8, 512 * 8, tag, gm); }
        STAMP(5);
        gemv_go(pack_bf(rbf(l0 * rbf(1.f + lo_f(gm[1]))) + lo_f(gm[0]), rbf(l1 * rbf(1.f + hi_f(gm[1]))) + hi_f(gm[0])));
      }
      SB();
    }
    if (i == QP + QD + 1) {
      if (part == 1) {
        LDS_BARRIER();
        if (wave == 0) {
          const int bb = lane >> 5, jq = lane & 31;
          st8_sc1(wsr, G_Q + (bb * 2048 + blk * 32 + jq) * 8, pack_bf(sum8(&sm.r2[2 * jq * 20 + bb * 8]), sum8(&sm.r2[(2 * jq + 1) * 20 + bb * 8])), tag);
        }
        STAMP(6);
      }
      SB();
    }
    return true;
  });
  PIN(sot);
  tau = te1 >> 16;
  due += P.TG * 256;
  const u32 qoff = G_Q + ((b * 32 + head) * 64 + c * 4) * 8;
  u32x4 qg[2], mlg;
  u32 qq[4];
  constexpr int NB = (NL + 15) / 16;
  float xlo, sc[NB], pr[NB], m = 0.f;
  f32x4 acc = {0.f, 0.f, 0.f, 0.f};
  u32 pb[NB], pm[4], pl8[8], cpf;
  const u32 adst0 = lds_addr(lane < 32 ? &sm.red[wave][c * 8 + g * 4] : &sm.r2[(lane - 32) * 4]);
  u32 po, rdst, rsrc, xo;
  auto wo_addr = [&]() {
    po = dup(G_P + (((c >> 2 & 1) * 32 + xcd * 4 + (wave >> 1)) * 4 + (c & 3)) * 256 + (wave & 1) * 128 + (c >> 3) * 64 + g * 16) + ((P.NR & 2) ? near : (xcd & 2) * 4096);
    rdst = lane < 32 ? (u32)(uintptr_t)&sm.red2[wave][0] + lane * 16 : (u32)(uintptr_t)&sm.r2[0] + (lane - 32) * 16;
    rsrc = (u32)(uintptr_t)&sm.red2[0][((wave & 1) * 16 + (lane & 15)) * 4 + (lane >> 4)];
    xo = dup(G_XP + xcd * 2048 + (wave & 1) * 1024 + (blk * 64 + (lane & ~1)) * 2) + (lane & 1) * 8192;
    PIN(po); PIN(rdst); PIN(rsrc); PIN(xo);
  };
  u32 pso = wave == 0 ? pslot + c * 16 + (g & 1) * 8 + (g >> 1) * 8192 : OOB;
  PIN(pso);
  const u32 nadd = wdef ? P.LW : (is_up ? P.LU : 0);
  u32 nto = tid == 0 ? G_NT + bid * 8 : OOB;
  PIN(nto);
  auto notices = [&](auto nc_, u32 off, u32x4 (&gn)[nc_.v]) -> int {
    constexpr int N = nc_.v;
#pragma nounroll
    for (int spin = 0; spin < SPIN_MAX; ++spin) {
      bool ok = true;
#pragma unroll
      for (int i = 0; i < N; ++i) ok &= (gn[i][1] == tag) & (gn[i][3] == tag);
      if (all_ok(ok)) break;
      __builtin_amdgcn_s_sleep(10);
      CFENCE();
#pragma unroll
      for (int i = 0; i < N; ++i) gn[i] = ld16<AUX_SC1>(wsr, off, i * 1024);
    }
    int mx = 0;
#pragma unroll
    for (int i = 0; i < N; ++i) mx = max(mx, max((int)(gn[i][0] - t0), (int)(gn[i][2] - t0)));
    return __builtin_amdgcn_readfirstlane((int)wave_max((float)min(mx, 1 << 20)));
  };
  auto pv_row = [&](auto jc) {
    constexpr int i = jc.v;
    const u32 pv = __float_as_uint(bcast16(u2f(pb[i >> 4]), i & 15));
    acc = mfma16(u32x4{pv & pm[0], pv & pm[1], pv & pm[2], pv & pm[3]}, vr[i], acc);
    PIN(acc);
    return true;
  };
  sfor<0, NL - 1>([&](auto ic) {
    constexpr int v = ic.v;
    if constexpr (v >= SK0 && v < SK1) { if (left_out(v)) return true; }
    if constexpr (v > 0) {
      const u32 so = __builtin_amdgcn_readlane(sot, v);
      if constexpr (v >= SK1) { if (v == NL - P.NE) tau = (tau * P.FE) >> 4; }
      gate();
      blog(NL + v - 1);
      SB();
      if constexpr (v >= SK0 && v < SK1) ld16_into(vr[v], rv, lane * 16, so); else vr[v] = ld16<AUX_NT>(rv, lane * 16, so);
      SB();
      SB();
    }
    if constexpr (v == SK0 - 1) {
#pragma unroll
      for (int i = SK0; i < SK1; ++i) { vr[i] = u32x4{0u, 0u, 0u, 0u}; PIN(vr[i]); }
    }
    if constexpr (v == VQ - 4) { qg[0] = ld16<AUX_SC1>(wsr, qoff, 0); qg[1] = ld16<AUX_SC1>(wsr, qoff + 16, 0); }
    if constexpr (v == VSIDE) { side_loads(); tbg = ld8<AUX_SC1>(wsr, wave == 7 ? G_VT + xcd * 128 + c * 8 : OOB, 0); }
    if constexpr (v == CPF) {
      u64 pc; u32 dlt, len;
      asm volatile("s_getpc_b64 %0\n.Lcpf_here:\n\ts_mov_b32 %1, .Ltail_start-.Lcpf_here\n\ts_mov_b32 %2, .Lend_a-.Ltail_start\n\ts_max_i32 %2, %2, .Lend_b-.Ltail_start\n\ts_max_i32 %2, %2, .Lend_c-.Ltail_start\n\ts_max_i32 %2, %2, .Lend_d-.Ltail_start\n\ts_max_i32 %2, %2, .Lend_e-.Ltail_start" : "=s"(pc), "=s"(dlt), "=s"(len) :: "scc");
      pc += dlt;
      const rsrc_t rc = mk_rsrc((const void*)pc, (part == 3 && i8 == 1) ? len : 0u);
      cpf = __builtin_amdgcn_raw_buffer_load_b32(rc, tid * 64, 0, 0);
    }
    if constexpr (v == SK1 + 2) PIN(cpf);
    if constexpr (v == SK1) mlg = ld16<AUX_SC1>(wsr, wave == 0 ? mlo : OOB, 0);
    SB();
    if constexpr (NL + v < 72) limit(IC<NL + v>{});
    if constexpr (v >= SK0 + WV) { WAIT_VM(v < SK1 ? WV : WV + 1); SB(); }
    if constexpr (v == VQ) {
      if (!all_ok((qg[0][1] == tag) & (qg[0][3] == tag) & (qg[1][1] == tag) & (qg[1][3] == tag))) { STAMP(20); gather16<2>(wsr, qoff, 16, tag, qg); }
      STAMP(1); WSTAMP(1);
      qq[0] = qg[0][0]; qq[1] = qg[0][2]; qq[2] = qg[1][0]; qq[3] = qg[1][2];
    }
    if constexpr (v >= VQ && v < VS) {
      constexpr int sl = v - VQ, bt = sl >> 1;
      float xs[8];
#pragma unroll
      for (int i = 0; i < 8; ++i) {
        float d = 0.f;
        if (sl * 8 + i < NL) {
#pragma unroll
          for (int k = 0; k < 4; ++k) d = dot2(kr[sl * 8 + i][k], qq[k], d);
        }
        xs[i] = d;
      }
#pragma unroll
      for (int h = 0; h < 4; ++h) xs[h] = sel_add<XOR7>(xs[h], xs[h + 4], c & 4);
#pragma unroll
      for (int h = 0; h < 2; ++h) xs[h] = sel_add<XOR2>(xs[h], xs[h + 2], c & 2);
      xs[0] = sel_add<XOR1>(xs[0], xs[1], c & 1);
      if constexpr ((sl & 1) || sl == NSL - 1) {
        const float t = (sl & 1) ? sel_add<XOR8>(xlo, xs[0], c & 8) : xs[0] + dpp<XOR8>(xs[0]);
        const float s = rbf(rbf(t) * 0.08838834764831845f);
        sc[bt] = (bt * 16 + c < SK0 || (bt * 16 + c >= first && bt * 16 + c < NL)) ? s : -__builtin_inff();
        PIN(sc[bt]);
      } else {
        xlo = xs[0];
        PIN(xlo);
      }
    }
    if constexpr (v == VS) {
      float mloc = sc[0];
#pragma unroll
      for (int bt = 1; bt < NB; ++bt) mloc = fmaxf(mloc, sc[bt]);
      m = fmaxf(wave_max(mloc), -3.0e38f);
      float ls = 0.f;
#pragma unroll
      for (int bt = 0; bt < NB; ++bt) { pr[bt] = __expf(sc[bt] - m); ls += pr[bt]; }
      const float l = wave_sum(ls);
      if (lane == 0) { sm.ml[wave][0] = m; sm.ml[wave][1] = l; }
      if (c == ((NL - 1) & 15)) sm.pl[g][wave] = pr[NB - 1];
      STAMP(2); WSTAMP(2);
    }
    if constexpr (v >= SK1) pv_row(IC<v - SK1 + SK0 - WV>{});
    if constexpr (v < SK0 || v >= SK0 + WV) sfor<cons_end(v - 1), cons_end(v)>(pv_row);
    if constexpr (v == SK1 + 1) {
      float p[8];
#pragma unroll
      for (int w = 0; w < 8; ++w) p[w] = sm.pl[g][w] * __expf(sm.ml[w][0] - m);
#pragma unroll
      for (int w = 0; w < 8; ++w) pl8[w] = pack_bf(p[w], p[w]);
#pragma unroll
      for (int w = 0; w < 8; ++w) PIN(pl8[w]);
    }
    if constexpr (v == MLS - 1) { side_pin(); if (is_up) up_prep(); PIN(ga); PIN(xin); PIN(cc1); }
    if constexpr (v == UPREP - 1) wo_addr();
    if constexpr (v == UPREP) {
#pragma unroll
      for (int k = 0; k < 8; ++k) asm volatile("" : "=v"(bf[k]));
      if (part == 0) wo_prep(0, 1); else if (is_up) up_frags();
#pragma unroll
      for (int k = 0; k < 8; ++k) PIN(bf[k]);
    }
    if constexpr (v == UPREP + 1) {
      if (is_up) {
        const u32x4* a1 = ap + 128 + ((lane >> 1) & 1) * 128;
        f32x4 d0 = {0.f, 0.f, 0.f, 0.f}, d1 = {0.f, 0.f, 0.f, 0.f};
#pragma unroll
        for (int rb = 0; rb < 4; ++rb) { d0 = mfma16(a1[rb * 8], bf[rb], d0); d1 = mfma16(a1[(rb + 4) * 8], bf[rb + 4], d1); }
        Af = par ? d0[1] + d1[1] : d0[0] + d1[0]; Bf = par ? d0[3] + d1[3] : d0[2] + d1[2];
        Af += dpp<XOR8>(Af); Bf += dpp<XOR8>(Bf);
      }
      PIN(Af); PIN(Bf);
    }
    if (v == 10 || v == 16) STAMP(15 + v / 6);
    if (v == 20) STAMP(18);
    if (v == 32) STAMP(19);
    if constexpr (v == MLS) {
      if (wave == 7) {
        const bool ok = tbg[1] == tag;
        float vt = ok ? (float)((int)tbg[0] >> 16) : 0.f, lg = ok ? (float)(tbg[0] & 0xffffu) : 0.f, nf = ok ? 8.f * (NL - 1 - RP0) : 0.f;
        vt += dpp<XOR1>(vt); lg += dpp<XOR1>(lg); nf += dpp<XOR1>(nf); vt += dpp<XOR2>(vt); lg += dpp<XOR2>(lg); nf += dpp<XOR2>(nf);
        vt += dpp<XOR7>(vt); lg += dpp<XOR7>(lg); nf += dpp<XOR7>(nf); vt += dpp<XOR8>(vt); lg += dpp<XOR8>(lg); nf += dpp<XOR8>(nf);
        lg *= __builtin_amdgcn_rcpf(fmaxf(nf, 1.f));
        if (lane == 0) sm.bc = u32x2{__float_as_uint(vt), (u32)min(max((int)((lg - (float)P.A0) * (float)(P.AG * 32)), 0), P.AC * 256)};
      }
      LDS_BARRIER();
      {
        const u32x2 bc = sm.bc;
        near = ((__builtin_amdgcn_readfirstlane(bc[0]) >> 31 ^ die ^ P.NR ^ 1) & 1) * 8192u;
        t1 += __builtin_amdgcn_readfirstlane(bc[1]);
        tau += __builtin_amdgcn_readfirstlane((int)((float)(int)bc[1] * __builtin_amdgcn_rcpf((float)(nl - MLS - 3))));
      }
      float mm[8], ll[8];
#pragma unroll
      for (int w = 0; w < 8; ++w) { const f32x2 t = *reinterpret_cast<const f32x2*>(&sm.ml[w][0]); mm[w] = t[0]; ll[w] = t[1]; }
      float Mwg = mm[0], Lwg = 0.f;
#pragma unroll
      for (int w = 1; w < 8; ++w) Mwg = fmaxf(Mwg, mm[w]);
      if (wave == 0) {
#pragma unroll
        for (int w = 0; w < 8; ++w) Lwg = fmaf(__expf(mm[w] - Mwg), ll[w], Lwg);
        if (lane == 0) st16_sc1(wsr, G_ML + ((head * 2 + b) * 4 + part) * 16, __float_as_uint(Mwg), __float_as_uint(Lwg), tag);
      }
      const float fw = __expf(m - Mwg);
      m = Mwg;
#pragma unroll
      for (int bt = 0; bt < NB; ++bt) { pb[bt] = pack_bf(pr[bt] * fw, pr[bt] * fw); PIN(pb[bt]); }
#pragma unroll
      for (int k = 0; k < 4; ++k) { pm[k] = (c >> 1 & 3) == k ? ((c & 1) ? 0xffff0000u : 0x0000ffffu) : 0u; PIN(pm[k]); }
    }
    SB();
    return true;
  });
  asm volatile(".Ltail_start:");
  if (wave != 0) {
    pv_row(IC<SK0 - 1>{});
    WAIT_VM(WV); SB();
    sfor<NL - 1 - WV, NL - 1>(pv_row);
    *LDSP(f32x4, adst0) = acc;
    WSTAMP(14);
    LDS_BARRIER();
  } else {
    u32x4 xr[8], am[8];
#pragma unroll
    for (int w = 0; w < 8; ++w) am[w] = u32x4{pl8[w] & pm[0], pl8[w] & pm[1], pl8[w] & pm[2], pl8[w] & pm[3]};
#pragma unroll
    for (int w = 0; w < 8; ++w) PIN(am[w]);
    {
      const u32 so = __builtin_amdgcn_readlane(sot, NL - 1);
      gate();
      SB();
#pragma unroll
      for (int w = 0; w < 8; ++w) xr[w] = ld16<AUX_NT>(rv, lane * 16, so ^ ((w & 1) ? col(P, 7) : 0u) ^ ((w & 2) ? col(P, 8) : 0u) ^ ((w & 4) ? col(P, 9) : 0u));
      SB();
    }
    u32 rsum = lds_addr(&sm.red[0][c * 8 + (g & 1) * 4]);
    pv_row(IC<SK0 - 1>{});
    WAIT_VM(WV + 8); SB();
    sfor<NL - 1 - WV, NL - 3>(pv_row);
    if (__builtin_expect(!all_ok((mlg[1] == tag) & (mlg[3] == tag)), 0)) {
#pragma nounroll
      for (int spin = 0; spin < SPIN_MAX; ++spin) {
        CFENCE();
        mlg = ld16<AUX_SC1>(wsr, mlo, 0);
        if (all_ok((mlg[1] == tag) & (mlg[3] == tag))) break;
      }
    }
    merge_factor(mlg);
    sfor<NL - 3, NL - 1>(pv_row);
    STAMP(14); WSTAMP(14);
    LDS_BARRIER();
    STAMP(15);
    f32x4 t[8];
#pragma unroll
    for (int w = 1; w < 8; ++w) t[w] = LDSP(f32x4, rsum)[w * 32];
    acc = ((acc + t[1]) + (t[2] + t[3])) + ((t[4] + t[5]) + (t[6] + t[7]));
    PIN(acc);
    STAMP(3);
    SB();
#pragma unroll
    for (int w = 0; w < 8; ++w) acc = mfma16(am[w], xr[w], acc);
  }
  WAIT_VM(0);
  acc *= fmrg;
  __builtin_amdgcn_raw_buffer_store_b64(u32x2{min(pack_bf(acc[0], acc[1]), SENT - 1u), min(pack_bf(acc[2], acc[3]), SENT - 1u)}, wsr, pso, 0, AUX_SC1);
  st8_sc1(wsr, nto, (u32)wall_clock64() + nadd, tag);
  SB();
  STAMP(4);

  if (part == 2) FINISH();
  if (part == 0) {
    __builtin_amdgcn_s_barrier();
#pragma nounroll
    while (wleft > 0) piece();
    bool late = true;
    while ((int)(((u32)wall_clock64() - t0) << 8) < t1 - 20 * 256) if (__builtin_amdgcn_s_getreg(7 | (3 << 11)) == 0) { late = false; break; }
    u32x4 og[2];
    if (__builtin_expect(late, 0)) { until(t1); CFENCE(); og[0] = ld16<AUX_SC1>(wsr, po, 0); SB(); WAIT_VM(1); } else { WAIT_VM(0); }
    CFENCE();
    wo_prep(1, 2);
#pragma unroll
    for (int k = 4; k < 8; ++k) PIN(bf[k]);
    SB();
    if (__builtin_expect(!late, 1)) { until(t1); CFENCE(); og[0] = ld16<AUX_SC1>(wsr, po, 0); }
    STAMP(11);
    SB();
    f32x4 d[4];
    auto wo_mm = [&]() {
      const f32x4 z = {0.f, 0.f, 0.f, 0.f};
#pragma unroll
      for (int t = 0; t < 4; ++t) { d[t] = mfma16(og[0], bf[t], z); og[1][t] = __float_as_uint(dpp<XOR8>(u2f(og[0][t]))); }
      SB();
#pragma unroll
      for (int t = 0; t < 4; ++t) d[t] = mfma16(og[1], bf[4 + t], d[t]);
    };
    auto p_bad = [&]() { return !all_ok(max(max(og[0][0], og[0][1]), max(og[0][2], og[0][3])) != SENT); };
    wo_mm();
    if (__builtin_expect(p_bad(), 0)) {
#pragma nounroll
      for (int spin = 0; spin < SPIN_MAX; ++spin) {
        __builtin_amdgcn_s_sleep(1);
        CFENCE();
        og[0] = ld16<AUX_SC1>(wsr, po, 0);
        if (!p_bad()) break;
      }
      wo_mm();
      SB();
      asm volatile(".Lend_d:");
    }
    STAMP(5); WSTAMP(5);
    {
      SB();
      f32x4 y; f32x2 h[4];
#pragma unroll
      for (int t = 0; t < 4; ++t) { h[t] = d[t].xy + d[t].zw; PIN(h[t]); }
#pragma unroll
      for (int t = 0; t < 4; ++t) { y[t] = h[t][0] + h[t][1]; PIN(y[t]); }
      *LDSP(f32x4, rdst) = y;
    }
    STAMP(16);
    LDS_BARRIER();
    STAMP(17);
    if (wave < 2) {
      f32x2 v[4];
#pragma unroll
      for (int w = 0; w < 4; ++w) v[w] = f32x2{LDSP(float, rsrc)[w * 256], LDSP(float, rsrc)[w * 256 + 128]};
      const f32x2 h = (v[0] + v[1]) + (v[2] + v[3]);
      const float y = h[0] + h[1];
      __builtin_amdgcn_raw_buffer_store_b32(min(pack_bf(dpp<0xA0>(y), dpp<0xF5>(y)), SENT - 1u), wsr, xo, 0, AUX_SC1);
      asm volatile(".Lend_a:");
    }
    STAMP(6);
    FINISH();
  }
  if (part == 3) {
    if (!has_w) FINISH();
    u32x4 b2[4];
    {
      __builtin_amdgcn_s_barrier();
      const rsrc_t r2 = mk_rsrc(w2 + (size_t)(blk * 32 + wave * 4) * 512, 4096u);
      const u32 l2 = (u32)(uintptr_t)(&sm.w2b[wave * 4][0]);
#pragma unroll
      for (int n = 0; n < 4; ++n) dma16(r2, (lane ^ (n * 2) ^ ((wave & 2) << 2)) * 16, n * 1024, l2 + n * 1024);
    }
    const u32 tdst = lds_addr(&sm.hb[hpos]);
    const u32 sdst = lds_addr(&sm.stat[wave][lane]);
    const u32 ssrc = lds_addr(&sm.stat[(g >> 1) * 4 + ((lane >> 3) & 3)][(lane & 7) * 4]);
    const u32 adst = lds_addr((c < 4 && (g & 1) == 0) ? &sm.actl[g >> 1][wave * 4 + c] : reinterpret_cast<u16*>(&sm.dummy[lane]));
    const u32 asrc = lds_addr(&sm.actl[lane & 1][g * 8]);
    const u32 apa = lds_addr(&sm.hb[(c >> 3) * 512 + (c & 3) * 64 + ((c >> 2) & 1) * 32 + g * 8]);
    const u32 xs2 = (P.NR & 4) ? 16384 : 16384 + 8192;
    u32 xoff = dup(G_XP) + ((P.NR & 4) ? near : 0) + g * 2048 + ub * 1024 + (wave & 3) * 256 + c * 16;
    u32x4 one;
#pragma unroll
    for (int k = 0; k < 4; ++k) one[k] = ((c & 2) == 0 && (c >> 2) == k) ? ((c & 1) ? 0x3f800000u : 0x00003f80u) : 0u;
    PIN(one);
    u32 ooff = dup(G_OP + (blk * 512 + tid) * 8);
    float Afp = Af, Bfp = Bf;
    PIN(xoff); PIN(ooff); PIN(Afp); PIN(Bfp);
    f32x2 gc = ga * cc1, xc = xin * cc1;
    PIN(ga); PIN(xin); PIN(gc); PIN(xc);
    u32 w2a = (u32)(uintptr_t)(&sm.w2b[0][0]) + (g * 8 + ((lane >> 2) & 3)) * 1024 + (wave ^ (g & 1)) * 128 + (lane & 15) * 8;
    PIN(w2a);
    if (wave == 1) {
      due = t1 + P.N1 * 256; gate();
      u32x4 gn[2] = {ld16<AUX_SC1>(wsr, G_NT + lane * 16, 0), ld16<AUX_SC1>(wsr, G_NT + lane * 16, 1024)};
      const int tn = notices(IC<2>{}, G_NT + lane * 16, gn) * 256;
      due = (tn > t1 + 2 * 256 ? max(tn, t1 + P.L0 * 256) + P.L1 * 256 : t1) + P.X2 * 256;
      until(due);
    }
    asm volatile("s_waitcnt vmcnt(0)" ::: "memory");
    STAMP(9);
    __builtin_amdgcn_s_barrier();
    STAMP(11);
    CFENCE();
    u32x4 xg[2] = {ld16<AUX_SC1>(wsr, xoff, 0), ld16<AUX_SC1>(wsr, xoff, xs2)};
    SB();
#pragma unroll
    for (int t = 0; t < 4; ++t) {
      const u32x2 lo = tr_read(w2a ^ (t * 32)), hi = tr_read((w2a ^ (t * 32)) + 4096);
      b2[t] = u32x4{lo[0], lo[1], hi[0], hi[1]};
    }
#pragma unroll
    for (int t = 0; t < 4; ++t) PIN(b2[t]);
    SB();
    const f32x4 z4 = {0.f, 0.f, 0.f, 0.f};
    auto xp_bad = [&]() { return !all_ok(max(max(max(xg[0][0], xg[0][1]), max(xg[0][2], xg[0][3])), max(max(xg[1][0], xg[1][1]), max(xg[1][2], xg[1][3]))) != SENT); };
    f32x4 v = mfma16(one, xg[1], mfma16(one, xg[0], z4));
    if (__builtin_expect(xp_bad(), 0)) {
#pragma nounroll
      for (int spin = 0; spin < SPIN_MAX; ++spin) {
        __builtin_amdgcn_s_sleep(1);
        CFENCE();
        xg[0] = ld16<AUX_SC1>(wsr, xoff, 0); xg[1] = ld16<AUX_SC1>(wsr, xoff, xs2);
        if (!xp_bad()) break;
      }
      v = mfma16(one, xg[1], mfma16(one, xg[0], z4));
      SB();
      asm volatile(".Lend_e:");
    }
    STAMP(5); WSTAMP(5);
    {
      const f32x2 a = v.xy, hc = __builtin_elementwise_fma(gc, a, xc), x1 = __builtin_elementwise_fma(ga, a, xin);
      *LDSP(u32, tdst) = pack_bf(hc[0], hc[1]);
      *LDSP(float, sdst) = swap32_add(x1[0] + x1[1], fmaf(x1[0], x1[0], x1[1] * x1[1]));
    }
    STAMP(16);
    LDS_BARRIER();
    STAMP(17);
    {
      const f32x4 z = {0.f, 0.f, 0.f, 0.f};
      const u32x4 af0 = LDSP(u32x4, apa)[0];
      SB();
      const f32x4 s4 = LDSP(f32x4, ssrc)[0], q4 = LDSP(f32x4, ssrc)[8];
      const u32x4 af1 = LDSP(u32x4, apa)[32];
      SB();
      f32x4 d[4];
      d[0] = mfma16(af0, bf[0], z); d[1] = mfma16(af0, bf[1], z);
      SB();
      const f32x2 s2 = s4.xy + s4.zw, q2 = q4.xy + q4.zw;
      float s = s2[0] + s2[1], q = q2[0] + q2[1];
      SB();
      d[2] = mfma16(af0, bf[2], z);
      SB();
      s = swap16_add(s, q);
      SB();
      d[3] = mfma16(af0, bf[3], z);
      SB();
      s += dpp<XOR1>(s); s += dpp<XOR2>(s);
      SB();
      d[0] = mfma16(af1, bf[4], d[0]);
      SB();
      s += dpp<XOR7>(s); s += dpp<XOR8>(s);
      SB();
      d[1] = mfma16(af1, bf[5], d[1]);
      SB();
      const auto sq = __builtin_amdgcn_permlane16_swap(__float_as_uint(s), __float_as_uint(s), false, false);
      const float mean = u2f(sq[0]) * (1.f / 512.f);
      SB();
      d[2] = mfma16(af1, bf[6], d[2]);
      SB();
      const float var = fmaf(u2f(sq[1]), 1.f / 512.f, -mean * mean);
      SB();
      d[3] = mfma16(af1, bf[7], d[3]);
      SB();
      float ri = __builtin_amdgcn_rsqf(fmaxf(var, 0.f) + 1e-5f);
      float cf = fmaf(-ri * mean, Afp, Bfp);
      float rk = ri * -1.44269504f, ck = cf * -1.44269504f;
      PIN(ri); PIN(cf); PIN(rk); PIN(ck);
      SB();
      PIN(d[0]); PIN(d[1]);
      float y = d[0][0] + d[1][1];
      PIN(y); PIN(d[2]);
      y += d[2][2];
      PIN(y); PIN(d[3]);
      y += d[3][3];
      y = __builtin_bit_cast(float, __builtin_amdgcn_update_dpp(__builtin_bit_cast(int, y), __builtin_bit_cast(int, y), XOR8, 0xa, 0xf, false));
      y = swap16_add(y, y);
      const float ex = __builtin_amdgcn_exp2f(fmaf(y, rk, ck));
      y = fmaf(y, ri, cf);
      const float gu = y * dpp<0x104>(y);
      *LDSP(u16, adst) = (u16)pack_bf(gu * __builtin_amdgcn_rcpf(1.f + ex), 0.f);
    }
    STAMP(18);
    LDS_BARRIER();
    STAMP(10);
    {
      const u32x4 af = *LDSP(u32x4, asrc);
      const f32x4 z = {0.f, 0.f, 0.f, 0.f};
      f32x4 d0 = mfma16(af, b2[0], z), d1 = mfma16(af, b2[1], z), d2 = mfma16(af, b2[2], z), d3 = mfma16(af, b2[3], z);
      asm volatile("" : "+v"(d0), "+v"(d1), "+v"(d2), "+v"(d3));
      const bool g1 = g & 1, g2 = g & 2;
      const float e0 = g1 ? d1[0] : d0[0], e1 = g1 ? d1[1] : d0[1], f0 = g1 ? d3[0] : d2[0], f1 = g1 ? d3[1] : d2[1];
      const u32x2 ov = {pack_bf(g2 ? f0 : e0, g2 ? f1 : e1), tag};
      __builtin_amdgcn_raw_buffer_store_b64(ov, wsr, ooff, near ^ 8192u, AUX_SC1);
      __builtin_amdgcn_raw_buffer_store_b64(ov, wsr, ooff, near, AUX_SC1);
    }
    asm volatile(".Lend_b:");
    STAMP(6);
    FINISH();
  }
  if (wave == 0) {
    const int d = fd, ul = lane >> 3;
    u32 gm[2] = {sg[0][0], sg[1][0]};
    if (!all_ok((sg[0][1] == tag) & (sg[1][1] == tag))) gather8<2>(wsr, G_MOD + (1024 + d) * 8, 1536 * 8, tag, gm);
    u32 xa0, xa1;
    {
      due = t1 + P.N2 * 256; gate();
      u32x4 gn[2] = {ld16<AUX_SC1>(wsr, G_NT + lane * 16, 0), ld16<AUX_SC1>(wsr, G_NT + lane * 16, 1024)};
      const int tn = notices(IC<2>{}, G_NT + lane * 16, gn) * 256;
      due = (tn > t1 + 2 * 256 ? max(tn, t1 + P.L0 * 256) + P.L1 * 256 : t1) + P.X3 * 256;
      until(due);
    }
#pragma nounroll
    for (int spin = 0; spin < SPIN_MAX; ++spin) {
      CFENCE();
      xa0 = __builtin_amdgcn_raw_buffer_load_b32(wsr, dup(G_XP + ul * 2048) + near + (d >> 1) * 4, 0, AUX_SC1);
      xa1 = __builtin_amdgcn_raw_buffer_load_b32(wsr, dup(G_XP + ul * 2048) + near + (d >> 1) * 4, 1024, AUX_SC1);
      if (all_ok(max(xa0, xa1) != SENT)) break;
      __builtin_amdgcn_s_sleep(1);
    }
    float a0 = (d & 1) ? hi_f(xa0) : lo_f(xa0), a1 = (d & 1) ? hi_f(xa1) : lo_f(xa1);
    a0 += dpp<XOR8>(a0); a1 += dpp<XOR8>(a1);
    a0 = swap16_add(a0, a0); a1 = swap16_add(a1, a1);
    a0 = swap32_add(a0, a0); a1 = swap32_add(a1, a1);
    const bool hb = g & 1;
    float xv = rbf(((d & 1) ? hi_f(sx2) : lo_f(sx2)) + rbf((hb ? hi_f(gm[0]) : lo_f(gm[0])) * rbf(hb ? a1 : a0)));
    float gv = hb ? hi_f(gm[1]) : lo_f(gm[1]);
    PIN(xv); PIN(gv);
    if (wi == 0 && lane == 0) __builtin_amdgcn_raw_buffer_store_b32(tag, wsr, WS_EPOCH, 0, AUX_SC1);
    STAMP(10);
    due = max(due + P.X4 * 256, (int)(((u32)wall_clock64() - t0) << 8) + P.GX * 256);
    const bool last = ul + 40 < NUP;
    u32 o0 = dup(G_OP + (ul * 512 + d) * 8) + ((P.NR & 8) ? near : (ul & 2) * 4096), o5 = last ? o0 : OOB, tg5 = last ? tag : 0u;
    const rsrc_t rout = mk_rsrc(out, 2048u);
    u32 oo = (lane & 40) == 0 ? (g * 512 + d) * 2 : OOB;
    const u32 pk = c == 0 ? 0x00003f80u : (c == 4 ? 0x3f800000u : 0u);
    u32x4 one = {pk, 0u, pk, 0u};
    PIN(o0); PIN(o5); PIN(tg5); PIN(oo); PIN(one);
    __builtin_amdgcn_raw_buffer_store_b16((u16)pack_bf(xv, 0.f), rout, oo, 0, AUX_SC1);
    until(due);
    f32x4 sv;
#pragma nounroll
    for (int spin = 0; spin < SPIN_MAX; ++spin) {
      CFENCE();
      u32x2 og[6];
#pragma unroll
      for (int k = 0; k < 5; ++k) og[k] = ld8<AUX_SC1>(wsr, o0, k * 65536);
      og[5] = ld8<AUX_SC1>(wsr, o5, 5 * 65536);
      sv = f32x4{0.f, 0.f, 0.f, 0.f};
#pragma unroll
      for (int k = 0; k < 3; ++k) sv = mfma16(one, u32x4{og[2 * k][0], og[2 * k][1], og[2 * k + 1][0], og[2 * k + 1][1]}, sv);
      const u32 bad = ((og[0][1] ^ tag) | (og[1][1] ^ tag) | (og[2][1] ^ tag)) | ((og[3][1] ^ tag) | (og[4][1] ^ tag) | (og[5][1] ^ tg5));
      PIN(sv);
      if (all_ok(bad == 0u)) break;
      __builtin_amdgcn_s_sleep(1);
    }
    STAMP(7);
    const float s = sv[0] + dpp<XOR8>(sv[0]);
    __builtin_amdgcn_raw_buffer_store_b16((u16)pack_bf(fmaf(gv, s, xv), 0.f), rout, oo, 0, AUX_SC1);
    asm volatile(".Lend_c:");
  }
  FINISH();
}

extern "C" void kernel_launch(void* const* d_in, const int* in_sizes, int n_in,
                              void* d_out, int out_size, void* d_ws, size_t ws_size,
                              hipStream_t stream) {
  fused_kernel<<<256, 512, 0, stream>>>((const u16*)d_in[1], (const u16*)d_in[2], (const u16*)d_in[4], (const u16*)d_in[3], (const u16*)d_in[10], (const u16*)d_in[6], (const u16*)d_in[7],
                                        (unsigned char*)d_ws, (const u16*)d_in[0], (const u16*)d_in[9], (const u16*)d_in[8], (u16*)d_out);
}
```

```cpp
#define PROBE_MUL 10
#include <hip/hip_runtime.h>
#include <stdint.h>

typedef unsigned int u32;
typedef unsigned short u16;
typedef unsigned long long u64;
typedef __attribute__((ext_vector_type(4))) unsigned u32x4;
typedef __attribute__((ext_vector_type(2))) unsigned u32x2;
typedef __attribute__((ext_vector_type(2))) __bf16 bf16x2;
typedef __attribute__((ext_vector_type(4))) float f32x4;
typedef __attribute__((ext_vector_type(2))) float f32x2;
typedef __amdgpu_buffer_rsrc_t rsrc_t;

constexpr int NH = 32, SK = 16384, DH = 128, WIN = 4096, FF = 1376;
struct Prm { int E0, ES, E1, E2, E2C, EU, TEND, DT_UP, L0, L1, X1, X2, X3, X4, NE, FE, M0, M1, M2, M3, M4, GS, GX, N1, N2, GA, WD, DT_WO, LW, LU, EW, PM, NF, NR, C0, A0, AG, AC, TS, TW, TK, TG, GB, TT, W[9]; };
constexpr Prm PRM0 = {32, 20, 32, 32, 54, 58, 1978, 60, 12, 30, 88, 70, 100, 89, 3, 8, 0x2511f55b, 0x2b7425d5, 0x6017665, 0x17e0b9ec, 0x31944305, 68, 30, 36, 62, 0, 4, 100, 45, 15, 0, 0x0, 15, 14, 5, 30, 4, 4, 509, 228, 60, 20, 12, 0, 0x77777777, 0x77777777, 0x77777777, 0x77777777, 0x77777777, 0x44444477, 0x44444444, 0x44444444, 0x44444444};
constexpr int NL = 37;
constexpr int SK0 = 21, SK1 = 32;
constexpr int NCMIN = (NL - SK1 + SK0) * 8;
constexpr int WK = 8, WV = 5;
constexpr int WUSE = 12, LNS = 7;
constexpr int QP = 14, QD = 4;
constexpr int VQ = 6;
constexpr int NSL = (NL + 7) / 8;
constexpr int VS = VQ + NSL;
constexpr int MLS = VS + 2;
constexpr int VSIDE = MLS - 5, UPREP = MLS + 6;
constexpr int CPF = 17;
constexpr int SMO = 1;
constexpr int PRB = 19;
constexpr int TBS = 5, TBP = 9, TBL = 13, TBU = 17;
constexpr int window(int s, int w) {
  int n = w;
  for (int u = s - w + 1; u <= s; ++u) n += u == SMO ? 6 : (u == TBL || u == NL + CPF || u == NL + SK1 ? 1 : (u == QP || u == NL + VQ - 4 ? 2 : (u == NL + VSIDE ? 5 : 0)));
  return n;
}
constexpr int cons_end(int v) {
  const int a = v - WV + 1, c = (v - MLS) * 4;
  const int m = a < c ? a : c;
  return m < 0 ? 0 : (m > NL ? NL : m);
}
constexpr u32 col(const Prm& P, int j) { const int m[6] = {P.M0, P.M1, P.M2, P.M3, P.M4, P.PM}; return (u32)(m[j / 3] >> (j % 3 * 10) & 1023) << 10; }
constexpr u32 first_col(const Prm& P, int k) {
  if (k < 4) return col(P, 12 + k) | 1u << (24 + k);
  if (k < 6) return col(P, k - 4);
  if (k < 8) return col(P, 4 + k) | 1u << (16 + k);
  if (k < 11) return col(P, k - 1);
  return (col(P, 2) ^ col(P, 3) ^ col(P, 4) ^ col(P, 5) ^ col(P, 6) ^ col(P, 10) ^ col(P, 11)) | 0xf00000u;
}
constexpr bool one_to_one(const Prm& P) {
  u32 c[10] = {};
  for (int j = 0; j < 10; ++j) c[j] = col(P, j);
  for (int j = 0; j < 10; ++j) {
    if (c[j] == 0) return false;
    const u32 low = c[j] & (0u - c[j]);
    for (int k = j + 1; k < 10; ++k) if (c[k] & low) c[k] ^= c[j];
  }
  return true;
}
static_assert(one_to_one(PRM0), "every KB of the window exactly once");
struct Tab { u32 e[512]; };
constexpr Tab make_tab(const Prm& P) {
  Tab T{};
  for (int bid = 0; bid < 256; ++bid) {
    const int xcd = bid & 7, part = bid >> 4 & 3, i8 = (bid >> 3 & 1) | bid >> 6 << 1, wi = i8 * 8 + xcd;
    const bool has_mod = wi < 48, has_up = wi >= 21;
    const int cb0 = (i8 + P.C0) & 7, cb1 = (i8 + 4) & 7, ms = wi - 20;
    const int mi = wi < 8 ? wi : (wi < 16 ? 8 + ((xcd + 4) & 7) : (wi < 20 ? (wi - 14) * 8 + 3 : 16 + ms / 7 * 8 + ms % 7 + (ms % 7 >= 3 ? 1 : 0)));
    const int ui = xcd * 5 + (xcd > 5 ? xcd - 5 : 0) + (i8 >= 3 ? (xcd >= 5 ? 1 : 0) + i8 - 3 : 0);
    const int wd = cb0 == 3 ? 0 : P.WD;
    const int e[4] = {P.E0 - 4 * wd + (cb0 == 3 ? P.ES : 0), P.E1 + (cb1 == 3 ? P.ES : 0), has_mod ? (wi < 16 ? P.E2C : P.E2) + ((mi & 7) == 3 ? P.ES : 0) : 0, has_up ? P.EU : 0};
    int tw[4] = {}, win[4] = {}, span[4] = {};
    for (int k = 0; k < 4; ++k) {
      const int np = k == 0 ? 8 - wd : 8;
      tw[k] = np ? e[k] * P.TW / (16 * np) : 0;
      span[k] = P.TEND - (P.NF + tw[k] * np / 2 > P.TK ? P.NF + tw[k] * np / 2 : P.TK) - P.TG - (k == 0 ? (wd ? P.DT_WO : P.A0) : (k == 3 && has_up ? P.DT_UP : 0));
      win[k] = span[k] - (k == 0 && wd ? P.EW : (k == 3 && has_up ? P.GA : 0));
    }
    int n[4] = {NCMIN / 8, NCMIN / 8, NCMIN / 8, NCMIN / 8};
    for (int left = 128 - NCMIN / 2; left > 0; --left) {
      int best = -1;
      for (int k = 3; k >= 0; --k)
        if (n[k] < NL && (best < 0 || n[k] * win[best] < n[best] * win[k])) best = k;
      ++n[best];
    }
    const int c[4] = {0, n[0] - 1, n[0] + n[1] - 2, n[0] + n[1] + n[2] - 3};
    const int nl = n[part];
    const bool has_w = part < 2 || (part == 2 ? has_mod : has_up);
    const int blk = !has_w ? 0 : (part == 0 ? cb0 : (part == 1 ? xcd * 8 + cb1 : (part == 2 ? mi : ui)));
    T.e[2 * bid] = (u32)(c[part] | nl << 7 | blk << 13 | (has_w ? 1 : 0) << 19 | (part == 0 ? wd : 0) << 20) | (u32)tw[part] << 24;
    T.e[2 * bid + 1] = (u32)(span[part] * P.TS / (4 * (nl - 1)) | span[part] * (1024 - P.TS) / (4 * (nl - 1)) << 16);
  }
  return T;
}
constexpr int step_range(const Prm& P, bool mx_) {
  int mx = 0, mn = 1 << 20;
  const Tab T = make_tab(P);
  for (int bid = 0; bid < 256; ++bid) { const int n = T.e[2 * bid] >> 7 & 63; mx = n > mx ? n : mx; mn = n < mn ? n : mn; }
  return mx_ ? mx : mn;
}
extern "C" __attribute__((used, visibility("protected"))) __constant__ const Tab wg_tab = make_tab(PRM0);
static_assert(step_range(PRM0, true) <= NL && step_range(PRM0, false) >= NCMIN / 8, "a wave's key range does not fit its load steps");
static_assert(UPREP + 1 < SK0 && QP + QD + 1 < SK0 && cons_end(SK0 - 1) == SK0 - WV && NL - SK1 == WV, "steps every wave runs");
constexpr int NUP = FF / 32;
constexpr int SPIN_MAX = 1 << 17;

constexpr u32 WS_EPOCH = 0;
constexpr u32 G_TB = 1024;
constexpr u32 G_MOD = 4096;
constexpr u32 G_Q = G_MOD + 3072 * 8;
constexpr u32 G_ML = G_Q + 4096 * 8;
constexpr u32 G_U = G_ML + 256 * 16;
constexpr u32 G_P = 0;
constexpr u32 G_XP = G_P + 256 * 256;
constexpr u32 G_OP = G_XP + 8 * 512 * 4;
constexpr u32 G_NT = G_OP + 43 * 512 * 8;
constexpr u32 G_PB = G_OP + 44 * 512 * 8;
constexpr u32 G_VT = G_U + 2 * (G_PB + 8192);
constexpr u32 WS_END = G_VT + 1024;
__device__ __forceinline__ u32 dup(u32 o) { return G_U + ((o >> 13) << 14) + (o & 8191u); }
constexpr u32 SENT = 0xffffffffu;
constexpr u32 OOB = 0x40000000u;
#define WST 0

constexpr int AUX_NT = 2, AUX_SC1 = 16;

__device__ __forceinline__ float lo_f(u32 v) { return __uint_as_float(v << 16); }
__device__ __forceinline__ float hi_f(u32 v) { return __uint_as_float(v & 0xffff0000u); }
__device__ __forceinline__ float rbf(float x) { return (float)(__bf16)x; }
__device__ __forceinline__ u32 pack_bf(float lo, float hi) {
  bf16x2 r; r[0] = (__bf16)lo; r[1] = (__bf16)hi; return __builtin_bit_cast(u32, r);
}
__device__ __forceinline__ float dot2(u32 k, u32 q, float acc) {
  return __builtin_amdgcn_fdot2_f32_bf16(__builtin_bit_cast(bf16x2, k), __builtin_bit_cast(bf16x2, q), acc, false);
}
template <int CTRL> __device__ __forceinline__ float dpp(float x) {
  return __builtin_bit_cast(float, __builtin_amdgcn_mov_dpp(__builtin_bit_cast(int, x), CTRL, 0xf, 0xf, true));
}
constexpr int XOR1 = 0xB1, XOR2 = 0x4E, XOR7 = 0x141, XOR8 = 0x128;
__device__ __forceinline__ float swap32_add(float a, float b) {
  auto t = __builtin_amdgcn_permlane32_swap(__float_as_uint(a), __float_as_uint(b), false, false);
  return __uint_as_float(t[0]) + __uint_as_float(t[1]);
}
__device__ __forceinline__ float swap16_add(float a, float b) {
  auto t = __builtin_amdgcn_permlane16_swap(__float_as_uint(a), __float_as_uint(b), false, false);
  return __uint_as_float(t[0]) + __uint_as_float(t[1]);
}
__device__ __forceinline__ float wave_sum(float x) {
  x += dpp<XOR1>(x); x += dpp<XOR2>(x); x += dpp<XOR7>(x); x += dpp<XOR8>(x);
  x = swap16_add(x, x);
  return swap32_add(x, x);
}
__device__ __forceinline__ float wave_max(float x) {
  x = fmaxf(x, dpp<XOR1>(x)); x = fmaxf(x, dpp<XOR2>(x)); x = fmaxf(x, dpp<XOR7>(x)); x = fmaxf(x, dpp<XOR8>(x));
  auto s = __builtin_amdgcn_permlane16_swap(__float_as_uint(x), __float_as_uint(x), false, false);
  x = fmaxf(__uint_as_float(s[0]), __uint_as_float(s[1]));
  auto t = __builtin_amdgcn_permlane32_swap(__float_as_uint(x), __float_as_uint(x), false, false);
  return fmaxf(__uint_as_float(t[0]), __uint_as_float(t[1]));
}
template <int CTRL> __device__ __forceinline__ float sel_add(float lo, float hi, bool up) {
  const float t1 = lo + dpp<CTRL>(lo), t2 = hi + dpp<CTRL>(hi);
  return up ? t2 : t1;
}
template <int I> __device__ __forceinline__ float bc16(float x) {
  return __builtin_bit_cast(float, __builtin_amdgcn_update_dpp(0, __builtin_bit_cast(int, x), 0x150 + I, 0xf, 0xf, false));
}
__device__ __forceinline__ float bcast16(float x, int i) {
  switch (i) {
    case 0: return bc16<0>(x); case 1: return bc16<1>(x); case 2: return bc16<2>(x); case 3: return bc16<3>(x);
    case 4: return bc16<4>(x); case 5: return bc16<5>(x); case 6: return bc16<6>(x); case 7: return bc16<7>(x);
    case 8: return bc16<8>(x); case 9: return bc16<9>(x); case 10: return bc16<10>(x); case 11: return bc16<11>(x);
    case 12: return bc16<12>(x); case 13: return bc16<13>(x); case 14: return bc16<14>(x); default: return bc16<15>(x);
  }
}
__device__ __forceinline__ float silu_f(float x) { return x * __builtin_amdgcn_rcpf(1.f + __expf(-x)); }

template <int AUX> __device__ __forceinline__ u32x4 ld16(rsrc_t r, u32 voff, u32 soff) {
  return __builtin_amdgcn_raw_buffer_load_b128(r, voff, soff, AUX);
}
template <int AUX> __device__ __forceinline__ u32x2 ld8(rsrc_t r, u32 voff, u32 soff) {
  return __builtin_amdgcn_raw_buffer_load_b64(r, voff, soff, AUX);
}
__device__ __forceinline__ void st8_sc1(rsrc_t r, u32 voff, u32 v, u32 tag) {
  __builtin_amdgcn_raw_buffer_store_b64(u32x2{v, tag}, r, voff, 0, AUX_SC1);
}
__device__ __forceinline__ void st16_sc1(rsrc_t r, u32 voff, u32 v0, u32 v1, u32 tag) {
  __builtin_amdgcn_raw_buffer_store_b128(u32x4{v0, tag, v1, tag}, r, voff, 0, AUX_SC1);
}
__device__ __forceinline__ rsrc_t mk_rsrc(const void* p, u32 bytes) {
  return __builtin_amdgcn_make_buffer_rsrc(const_cast<void*>(p), 0, bytes, 0x00020000);
}
#define WAIT_VM(n) __builtin_amdgcn_s_waitcnt(((n) & 15) | (((n) >> 4) << 14) | (7 << 4) | (15 << 8))
#define SB() __builtin_amdgcn_sched_barrier(0)
#define CFENCE() asm volatile("" ::: "memory")
__device__ __forceinline__ bool all_ok(bool ok) { return __builtin_amdgcn_ballot_w64(!ok) == 0ull; }

template <int N> __device__ __forceinline__ void gather16(rsrc_t ws, u32 voff, u32 stride, u32 tag, u32x4 (&g)[N]) {
#pragma nounroll
  for (int spin = 0; spin < SPIN_MAX; ++spin) {
    CFENCE();
    bool ok = true;
#pragma unroll
    for (int i = 0; i < N; ++i) g[i] = ld16<AUX_SC1>(ws, voff + i * stride, 0);
#pragma unroll
    for (int i = 0; i < N; ++i) ok &= (g[i][1] == tag) & (g[i][3] == tag);
    if (all_ok(ok)) break;
    __builtin_amdgcn_s_sleep(8);
  }
}
template <int N> __device__ __forceinline__ void gather8(rsrc_t ws, u32 voff, u32 stride, u32 tag, u32 (&g)[N], bool act = true) {
#pragma nounroll
  for (int spin = 0; spin < SPIN_MAX; ++spin) {
    CFENCE();
    bool ok = true;
    u32x2 t[N];
#pragma unroll
    for (int i = 0; i < N; ++i) t[i] = ld8<AUX_SC1>(ws, voff + i * stride, 0);
#pragma unroll
    for (int i = 0; i < N; ++i) { ok &= (t[i][1] == tag); g[i] = t[i][0]; }
    if (all_ok(ok | !act)) break;
    __builtin_amdgcn_s_sleep(8);
  }
}

struct Smem {
  float red[8][128];
  float ml[8][2];
  float r2[64 * 20];
  u16 hb[3 * 1024];
  float stat[8][64];
  float red2[8][128];
  u32 dummy[64];
  u32x2 bc;
  u32x4 zero[4];
  float pl[4][8];
  u32 mflag[8];
  u32x4 wbuf[8][8][64];
  __attribute__((aligned(256))) u16 w2b[32][512];
  u16 actl[2][32];
};

#define PROBE_K 8
#define PROBE_OP 0
#define PROBE_PART -1
#define STAMP(k) do { if ((k) == PROBE_K && tid == 0) pst = wall_clock64(); } while (0)
#define WSTAMP(k) do {} while (0)
#define FINISH() do { STAMP(8); if (tid == 0) { __hip_atomic_store(reinterpret_cast<u64*>(ws + (1u << 20)) + bid * 2, pt0, __ATOMIC_RELAXED, __HIP_MEMORY_SCOPE_AGENT); \
   __hip_atomic_store(reinterpret_cast<u64*>(ws + (1u << 20)) + bid * 2 + 1, (PROBE_PART < 0 || part == PROBE_PART) ? pst : 0ull, __ATOMIC_RELAXED, __HIP_MEMORY_SCOPE_AGENT); } \
   if (bid == 8 && wave == 0) probe_end(ws, pt0, lane); return; } while (0)
#define PIN(x) asm volatile("" : "+v"(x))
#define LDS_BARRIER() do { __builtin_amdgcn_s_waitcnt(0xc07f); __builtin_amdgcn_s_barrier(); CFENCE(); } while (0)
#define WAVE_SYNC() do { __builtin_amdgcn_fence(__ATOMIC_RELEASE, "wavefront"); __builtin_amdgcn_wave_barrier(); __builtin_amdgcn_fence(__ATOMIC_ACQUIRE, "wavefront"); } while (0)

__device__ __forceinline__ void dma16(rsrc_t r, u32 voff, u32 soff, u32 lds_dst) {
  u32 keep;
  asm volatile("s_mov_b32 %0, m0\n\ts_mov_b32 m0, %4\n\ts_nop 4\n\tbuffer_load_dwordx4 %1, %2, %3 offen nt lds\n\ts_mov_b32 m0, %0"
               : "=&s"(keep) : "v"(voff), "s"(r), "s"(soff), "s"(lds_dst) : "memory");
}
__device__ __forceinline__ void ld16_into(u32x4& d, rsrc_t r, u32 voff, u32 soff) {
  asm volatile("s_nop 4\n\tbuffer_load_dwordx4 %0, %1, %2, %3 offen nt" : "+v"(d) : "v"(voff), "s"(r), "s"(soff) : "memory");
}
__device__ __forceinline__ u32 perm_lo(u32 a, u32 b) { return __builtin_amdgcn_perm(b, a, 0x05040100u); }
__device__ __forceinline__ u32 perm_hi(u32 a, u32 b) { return __builtin_amdgcn_perm(b, a, 0x07060302u); }
__device__ __forceinline__ float rdl(float x, int l) { return __int_as_float(__builtin_amdgcn_readlane(__float_as_int(x), l)); }

typedef __attribute__((ext_vector_type(8))) __bf16 bf16x8;
__device__ __forceinline__ f32x4 mfma16(u32x4 a, u32x4 b, f32x4 c) {
  return __builtin_amdgcn_mfma_f32_16x16x32_bf16(__builtin_bit_cast(bf16x8, a), __builtin_bit_cast(bf16x8, b), c, 0, 0, 0);
}
__device__ __forceinline__ float sum8(const float* p) {
  const f32x4 a = *reinterpret_cast<const f32x4*>(p), b = *reinterpret_cast<const f32x4*>(p + 4);
  return ((a[0] + b[0]) + (a[1] + b[1])) + ((a[2] + b[2]) + (a[3] + b[3]));
}
__device__ __forceinline__ float ln_ri(float var) {
  return __builtin_amdgcn_rcpf(rbf(__builtin_amdgcn_sqrtf(rbf(rbf(fmaxf(var, 0.f)) + 1e-5f))));
}
__device__ __forceinline__ void ln_stats2(const float (&x0)[8], const float (&x1)[8], float& mb0, float& ri0, float& mb1, float& ri1) {
  const float k0 = rdl(x0[0], 0), k1 = rdl(x1[0], 0);
  float s0 = 0.f, s1 = 0.f, q0 = 0.f, q1 = 0.f;
#pragma unroll
  for (int k = 0; k < 8; ++k) {
    const float d0 = x0[k] - k0, d1 = x1[k] - k1;
    s0 += d0; s1 += d1; q0 = fmaf(d0, d0, q0); q1 = fmaf(d1, d1, q1);
  }
  float t = swap16_add(swap32_add(s0, s1), swap32_add(q0, q1));
  t += dpp<XOR1>(t); t += dpp<XOR2>(t); t += dpp<XOR7>(t); t += dpp<XOR8>(t);
  auto u = __builtin_amdgcn_permlane16_swap(__float_as_uint(t), __float_as_uint(t), false, false);
  const float ms = __uint_as_float(u[0]) * (1.f / 512.f), mq = __uint_as_float(u[1]) * (1.f / 512.f);
  const float ri = ln_ri(fmaf(-ms, ms, mq));
  mb0 = rbf(k0 + rdl(ms, 0)); mb1 = rbf(k1 + rdl(ms, 32));
  ri0 = rdl(ri, 0); ri1 = rdl(ri, 32);
}
template <int I> struct IC { static constexpr int v = I; };
template <int I, int N, class F> __device__ __forceinline__ void sfor(F&& f) {
  if constexpr (I < N) { if (f(IC<I>{})) sfor<I + 1, N>(f); }
}
typedef __attribute__((ext_vector_type(4))) short s16x4;
typedef __attribute__((address_space(3))) s16x4* lds_s16x4p;
__device__ __forceinline__ u32x2 tr_read(u32 lds_addr) {
  return __builtin_bit_cast(u32x2, __builtin_amdgcn_ds_read_tr16_b64_v4i16((lds_s16x4p)lds_addr));
}
__device__ __forceinline__ u32 next_tag(u32 e) { return ((e | 0x40004000u) + 1u) & ~0x40004000u; }
__device__ __forceinline__ float u2f(u32 v) { return __uint_as_float(v); }
#define LDSP(T, a) ((__attribute__((address_space(3))) T*)(a))
template <class T> __device__ __forceinline__ u32 lds_addr(T* p) { u32 a = (u32)(uintptr_t)p; PIN(a); return a; }

#define TAB_LOAD "s_add_u32 s30, s28, wg_tab@rel32@lo+4\ns_addc_u32 s31, s29, wg_tab@rel32@hi+12\nglobal_load_dwordx2 %[te], %[va], s[30:31]\n"
#define FC(k) ".Lfc" #k
#define FC_REGS
#define FX(r, bit, k) "s_bitcmp1_b32 %[" #r "], " #bit "\ns_cselect_b32 %[sb], " FC(k) ", 0\ns_xor_b32 %[sa], %[sa], %[sb]\n"
__device__ __forceinline__ void probe_end(unsigned char* ws, u64 pt0, int lane) {
  while (wall_clock64() < pt0 + 5500ull) __builtin_amdgcn_s_sleep(8);
  u64 a[4], c2[4];
  for (int i = 0; i < 4; ++i) {
    a[i] = __hip_atomic_load(reinterpret_cast<u64*>(ws + (1u << 20)) + (i * 64 + lane) * 2, __ATOMIC_RELAXED, __HIP_MEMORY_SCOPE_AGENT);
    c2[i] = __hip_atomic_load(reinterpret_cast<u64*>(ws + (1u << 20)) + (i * 64 + lane) * 2 + 1, __ATOMIC_RELAXED, __HIP_MEMORY_SCOPE_AGENT);
  }
  int mn0 = 1 << 30, mx1 = -(1 << 30), mn1 = 1 << 30, sum = 0, cnt = 0;
  for (int i = 0; i < 4; ++i) {
    const int d0 = (int)(a[i] - pt0);
    mn0 = min(mn0, d0);
    if (c2[i] != 0ull) { const int d1 = (int)(c2[i] - pt0); mx1 = max(mx1, d1); mn1 = min(mn1, d1); sum += d1; cnt += 1; }
  }
  for (int o = 1; o < 64; o <<= 1) { mn0 = min(mn0, __shfl_xor(mn0, o)); mx1 = max(mx1, __shfl_xor(mx1, o)); mn1 = min(mn1, __shfl_xor(mn1, o)); sum += __shfl_xor(sum, o); cnt += __shfl_xor(cnt, o); }
  int X = PROBE_OP == 0 ? mx1 : (PROBE_OP == 1 ? sum / max(cnt, 1) : mn1);
  if (PROBE_K == 102) X = mn0;
  X = mn0 + (X - mn0) * PROBE_MUL;
  const u64 tg = pt0 + (u64)(long long)(X + 8000);
  for (int i = 0; i < 400000 && wall_clock64() < tg; ++i) __builtin_amdgcn_s_sleep(1);
}
__global__ void __launch_bounds__(512, 2) fused_kernel(const u16* kc_, const u16* vc_, const u16* wo_, const u16* wq_, const u16* wmod_, const u16* w1_, const u16* w3_,
                                                       unsigned char* ws_, const u16* x_, const u16* cnd_, const u16* w2_, u16* out_) {
  __shared__ __attribute__((aligned(256))) Smem sm;
  const u64 a_ka = (u64)__builtin_amdgcn_kernarg_segment_ptr();
  const int bid = blockIdx.x, tid = threadIdx.x;
  u64 wa, t64; u32x2 te; int tid0;
  u32 ar[14];
  u32 tch2, sa_, sb_, va_;
  u32x4 kr[NL], vr[NL];
  asm volatile(".set .Lfc0, %0\n.set .Lfc1, %1\n.set .Lfc2, %2\n.set .Lfc3, %3\n.set .Lfc4, %4\n.set .Lfc5, %5\n.set .Lfc6, %6\n.set .Lfc7, %7\n.set .Lfc8, %8\n.set .Lfc9, %9\n.set .Lfc10, %10\n.set .Lfc11, %11"
    :: "n"(first_col(PRM0, 0)), "n"(first_col(PRM0, 1)), "n"(first_col(PRM0, 2)), "n"(first_col(PRM0, 3)), "n"(first_col(PRM0, 4)), "n"(first_col(PRM0, 5)),
       "n"(first_col(PRM0, 6)), "n"(first_col(PRM0, 7)), "n"(first_col(PRM0, 8)), "n"(first_col(PRM0, 9)), "n"(first_col(PRM0, 10)), "n"(first_col(PRM0, 11)));
  asm volatile(
    "s_load_dwordx16 s[4:19], %[ka], 0x0\ns_load_dwordx8 s[20:27], %[ka], 0x40\n"
    "s_memrealtime %[t]\n"
    "v_lshlrev_b32 %[va], 3, %[bid]\n"
    "s_getpc_b64 s[28:29]\n.Lcpf_first:\n"
    TAB_LOAD
    "s_cmp_gt_u32 %[bid], 15\ns_cbranch_scc1 .Lno_cpf\n"
    "s_mov_b32 %[sa], .Lend_a-.Lcpf_first\ns_max_i32 %[sa], %[sa], .Lend_b-.Lcpf_first\ns_max_i32 %[sa], %[sa], .Lend_c-.Lcpf_first\ns_max_i32 %[sa], %[sa], .Lend_d-.Lcpf_first\ns_max_i32 %[sa], %[sa], .Lend_e-.Lcpf_first\n"
    "v_lshlrev_b32 %[va], 6, %[tid]\nv_min_u32 %[va], %[sa], %[va]\nglobal_load_dword %[pf], %[va], s[28:29]\n"
    ".Lno_cpf:\n"
    "v_readfirstlane_b32 %[t0], %[tid]\nv_and_b32 %[va], 63, %[tid]\n"
    "s_mov_b32 %[sa], " FC(11) "\n"
    FX(bid, 0, 0) FX(bid, 1, 1) FX(bid, 2, 2) FX(bid, 3, 3) FX(bid, 4, 4) FX(bid, 5, 5) FX(bid, 6, 6) FX(bid, 7, 7) FX(t0, 6, 8) FX(t0, 7, 9) FX(t0, 8, 10)
    "v_lshl_add_u32 %[va], %[va], 4, %[sa]\n"
    "s_waitcnt lgkmcnt(0)\nglobal_load_dwordx4 %[k0], %[va], s[4:5] nt\nglobal_load_dwordx4 %[v0], %[va], s[6:7] nt\n"
    "s_bitcmp1_b32 %[t0], 6\ns_cselect_b64 s[14:15], s[16:17], s[14:15]\n"
    "s_bitcmp1_b32 %[bid], 4\ns_cselect_b64 s[8:9], s[10:11], s[8:9]\ns_cselect_b64 s[12:13], s[14:15], s[12:13]\ns_bitcmp1_b32 %[bid], 5\ns_cselect_b64 %[wa], s[12:13], s[8:9]"
    : "=&{s4}"(ar[0]), "=&{s5}"(ar[1]), "=&{s6}"(ar[2]), "=&{s7}"(ar[3]), "=&{s18}"(ar[4]), "=&{s19}"(ar[5]), "=&{s20}"(ar[6]), "=&{s21}"(ar[7]), "=&{s22}"(ar[8]), "=&{s23}"(ar[9]),
      "=&{s24}"(ar[10]), "=&{s25}"(ar[11]), "=&{s26}"(ar[12]), "=&{s27}"(ar[13]), [wa] "=&s"(wa), [t0] "=&s"(tid0), [t] "=&s"(t64), [te] "=&v"(te), [sa] "=&s"(sa_), [sb] "=&s"(sb_),
      [va] "=&v"(va_), [pf] "=&v"(tch2), [k0] "=&v"(kr[0]), [v0] "=&v"(vr[0])
    : [ka] "s"(a_ka), [bid] "s"(bid), [tid] "v"(tid) : "s8", "s9", "s10", "s11", "s12", "s13", "s14", "s15", "s16", "s17", "s28", "s29", "s30", "s31", "scc", "memory" FC_REGS);
  asm volatile("" : "+s"(ar[0]), "+s"(ar[1]), "+s"(ar[2]), "+s"(ar[3]), "+s"(ar[4]), "+s"(ar[5]), "+s"(ar[6]), "+s"(ar[7]), "+s"(ar[8]), "+s"(ar[9]), "+s"(ar[10]), "+s"(ar[11]), "+s"(ar[12]), "+s"(ar[13]));
  asm volatile("" : "+s"(wa), "+s"(t64), "+s"(tid0), "+s"(sa_));
  const u32 tka = (u32)wall_clock64();
  const u64 pt0 = t64; u64 pst = (PROBE_K == 100 || PROBE_K == 102) ? pt0 : (PROBE_K == 101 ? pt0 + (u32)(tka - (u32)t64) : 0);
  const u32 t0 = tka;
  __builtin_assume(tid >= 0 && tid < 512 && tid0 >= 0 && tid0 < 512 && bid >= 0 && bid < 256);
  constexpr Prm P = PRM0;
  const int TEND = P.TEND;
  const int xcd = bid & 7, part = bid >> 4 & 3;
  const int lane = tid & 63, wave = tid0 >> 6;
  SB();
  if ((bid >> 4) == 3 && P.TT) {
    const int j = min((bid & 8) + wave, 9);
    const u64 p = ((__attribute__((address_space(4))) const u64*)a_ka)[2 + j];
    tch2 = __builtin_amdgcn_raw_buffer_load_b32(mk_rsrc((const void*)p, (j < 2 ? 4u << 20 : (j == 2 ? 3u << 20 : ((0x118 >> j & 1) ? FF * 1024u : 2048u)))), lane << 16, 512, AUX_NT);
  }
  const bool p3 = part == 3;
  u32 pitch = (u32)(0x005600c001000020ull >> (part * 16) & 0xffff) * 32u;
  const int sh = p3 ? 2 : 3;
  u32 wv0 = (u32)((p3 ? (wave >> 1) * 128 : wave * 64) + (part == 0 ? xcd * 512 : 0) + (lane >> sh)) * pitch + ((lane & (p3 ? 3 : 7)) ^ (lane >> (p3 ? 5 : 3))) * 16u;
  const u32 wimg = (u32)(uintptr_t)(&sm.wbuf[0][0][0]);
  u32 wsz = (part == 0 ? 4096u : 512u) * pitch, wld = wimg + (p3 ? (wave & 1) * 32768 + (wave >> 1) * 8192 : wave * 8192);
  PIN(wv0);
  asm volatile("" : "+s"(wsz), "+s"(wld), "+s"(pitch));
  auto karg = [&](int i) { return ar[2 * i] | (u64)ar[2 * i + 1] << 32; };
  const u64 a_kc = karg(0), a_vc = karg(1);
  unsigned char* ws = (unsigned char*)karg(2);
  const u16 *x = (const u16*)karg(3), *cnd = (const u16*)karg(4), *w2 = (const u16*)karg(5);
  u16* out = (u16*)karg(6);
  const rsrc_t wsr = mk_rsrc(ws, WS_END);
  asm volatile("s_waitcnt vmcnt(3)\n\ts_cmp_eq_u32 %1, 3\n\ts_cbranch_scc1 1f\n\ts_waitcnt vmcnt(2)\n1:" : "+v"(te) : "s"(P.TT ? bid >> 4 : 0) : "scc");
  const u32 te0 = __builtin_amdgcn_readfirstlane(te[0]), te1 = __builtin_amdgcn_readfirstlane(te[1]);
  const int bl0 = te0 & 127, nl = te0 >> 7 & 63, blk = te0 >> 13 & 63;
  const bool has_w = te0 >> 19 & 1;
  const rsrc_t rw = mk_rsrc((const void*)wa, wsz);
  const u32 wstep = pitch << (6 - sh), wflip = p3 ? 32u : 0u;
  u32 wso = blk << (sh + 4);
  int wleft = 8;
  auto piece = [&]() {
    dma16(rw, wv0, wso, wld);
    wso += wstep; wld += 1024;
    wv0 ^= (wleft & 1) ? wflip : 0u;
    --wleft;
  };
  SB();
  STAMP(23);
  const int b = bid >> 3 & 1, head = xcd * 4 + (bid >> 6 ^ 3);
  const int g = lane >> 4, c = lane & 15;
  const int i8 = b | bid >> 6 << 1, wi = i8 * 8 + xcd;
  const bool is_up = p3 && has_w, modc = part == 2 && wi < 16;
  const size_t base = ((size_t)(b * NH + head) * SK + (SK - WIN)) * DH;
  const rsrc_t rk = mk_rsrc((const u16*)a_kc + base, WIN * DH * 2), rv = mk_rsrc((const u16*)a_vc + base, WIN * DH * 2);
  int tau = te1 & 0xffff;
  const int tauw = te0 >> 24 << 7, wdef = te0 >> 20 & 15;
  int due = P.NF * 256, now = 0;
  auto gate = [&]() {
    while (now < due) { now = (int)(((u32)wall_clock64() - t0) << 8); if (now < due) __builtin_amdgcn_s_sleep(1); }
    due += tau;
  };
  auto until = [&](int t) { while ((int)(((u32)wall_clock64() - t0) << 8) < t); };
  const u32 pslot = dup(G_P + head * 2048 + (b * 4 + part) * 16);
  const u32 die = __builtin_amdgcn_s_getreg(20 | (2 << 6));
  const bool voter = p3 && i8 == 1, reporter = voter, timer = p3 && i8 < 2;
  int vote = 0, lag = 0;
  const u32 pbo = dup(G_PB) + (xcd * 8 + wave) * 128;
  u32 near = 0;
  u32x2 ep; u32 xs0, xs1, tch1; u32x4 xr0, xr1;
  const rsrc_t rx = mk_rsrc(part == 2 ? cnd : x, (part == 1 || (part == 2 && has_w)) ? 2048u : 0u);
  auto small_ops = [&]() {
    ep = ld8<AUX_SC1>(wsr, wave == 7 ? WS_EPOCH : OOB, 0);
    xs0 = __builtin_amdgcn_raw_buffer_load_b16(rx, tid * 2, 0, 0); xs1 = __builtin_amdgcn_raw_buffer_load_b16(rx, tid * 2, 1024, 0);
    xr0 = ld16<0>(rx, part == 1 ? lane * 16 : OOB, 0); xr1 = ld16<0>(rx, part == 1 ? lane * 16 : OOB, 1024);
    tch1 = __builtin_amdgcn_raw_buffer_load_b32(mk_rsrc(w2 + (size_t)blk * 32 * 512, is_up ? 64u : 0u), tid == 384 ? 0 : OOB, 0, AUX_NT);
  };
  if (has_w) {
    if (modc) now = 1 << 30;
#pragma nounroll
    while (wleft > wdef) { gate(); due += tauw - tau; piece(); }
    now = 0;
  }
  due = max(due, P.TK * 256);
  SB();
  u32 sot = sa_ & 0xffc00u;
  {
    const u32 s1 = (bl0 + lane - 1 - (lane >= SK0 ? NL - nl : 0)) ^ (124 + part);
#pragma unroll
    for (int k = 0; k < 7; ++k) sot ^= (u32)-(int)(s1 >> k & 1u) & col(P, k);
    PIN(sot);
  }
  u32 tag = 0;
  u32x4 bf[8];
  auto wo_prep = [&](int s0 = 0, int s1 = 2) {
    const u32 wi_ = wimg + wave * 8192;
    const int q = (lane >> 2) & 3, pp = lane & 3;
#pragma unroll
    for (int s = s0; s < s1; ++s) {
#pragma unroll
      for (int t = 0; t < 4; ++t) {
        const int r0 = s * 32 + g * 8 + q, r1 = r0 + 4, ch = t * 2 + (pp >> 1);
        const u32x2 lo = tr_read(wi_ + r0 * 128 + ((ch ^ (r0 & 7)) * 16) + (pp & 1) * 8), hi = tr_read(wi_ + r1 * 128 + ((ch ^ (r1 & 7)) * 16) + (pp & 1) * 8);
        bf[s * 4 + t] = u32x4{lo[0], lo[1], hi[0], hi[1]};
      }
    }
  };
  auto gemv_go = [&](u32 hh) {
    sm.hb[tid] = (u16)hh; sm.hb[512 + tid] = (u16)(hh >> 16);
    WAVE_SYNC();
    const u32x4* a = (c & 11) == 0 ? reinterpret_cast<const u32x4*>(&sm.hb[(c >> 2) * 512 + wave * 64 + g * 8]) : &sm.zero[0];
    const u32x4 a0 = a[0], a1 = a[(c & 11) == 0 ? 4 : 0];
    const f32x4 z = {0.f, 0.f, 0.f, 0.f};
    f32x4 d[4];
#pragma unroll
    for (int t = 0; t < 4; ++t) d[t] = mfma16(a0, bf[t], z);
#pragma unroll
    for (int t = 0; t < 4; ++t) d[t] = mfma16(a1, bf[4 + t], d[t]);
    if (lane < 32) {
#pragma unroll
      for (int t = 0; t < 4; ++t) sm.r2[(t * 16 + c) * 20 + g * 8 + wave] = d[t][0];
    }
  };
  auto mod_stage = [&]() {
    wo_prep();
    gemv_go(pack_bf(silu_f(lo_f(xs0)), silu_f(lo_f(xs1))));
    __builtin_amdgcn_s_waitcnt(0xc07f);
    if (lane == 0) *LDSP(volatile u32, (u32)(uintptr_t)&sm.mflag[wave]) = 1u;
    if (wave == 7) {
#pragma nounroll
      for (int spin = 0; spin < SPIN_MAX; ++spin) {
        const u32 f = *LDSP(volatile u32, (u32)(uintptr_t)&sm.mflag[lane & 7]);
        if (all_ok(f != 0u)) break;
        __builtin_amdgcn_s_sleep(1);
      }
      CFENCE();
      st8_sc1(wsr, G_MOD + (blk * 64 + lane) * 8, pack_bf(sum8(&sm.r2[lane * 20]), sum8(&sm.r2[lane * 20 + 8])), tag);
    }
    STAMP(9);
  };
  if (lane < 4) sm.zero[lane] = u32x4{0u, 0u, 0u, 0u};
  if (part == 2 && has_w) {
    if (tid < 8) sm.mflag[tid] = 0u;
    LDS_BARRIER();
    if (__builtin_expect(modc, 0)) {
      small_ops();
      __builtin_amdgcn_raw_buffer_store_b128(u32x4{SENT, SENT, SENT, SENT}, wsr, G_U + wi * 10240 + tid * 16, 0, AUX_SC1);
      __builtin_amdgcn_raw_buffer_store_b128(u32x4{SENT, SENT, SENT, SENT}, wsr, tid < 128 ? G_U + wi * 10240 + tid * 16 : OOB, 8192, AUX_SC1);
      asm volatile("s_waitcnt vmcnt(0)" ::: "memory");
      tag = next_tag(__builtin_amdgcn_readfirstlane(ep[0]));
      mod_stage();
      __builtin_amdgcn_s_barrier();
    }
  }
  SB();
  const int fd = wi * 8 + (lane & 3) * 2;
  const int ub = wave >> 2, ud = (wave & 3) * 128 + c * 8 + g * 2;
  u32x4 sg[3]; u32 sx2;
  auto side_loads = [&]() {
    const bool fin = part == 1 && wave == 0;
    sg[0] = ld16<AUX_SC1>(wsr, is_up ? G_MOD + (1024 + ud) * 8 : (fin ? G_MOD + (1024 + fd) * 8 : OOB), 0);
    sg[1] = ld16<AUX_SC1>(wsr, is_up ? G_MOD + (1536 + ud) * 8 : (fin ? G_MOD + (2560 + fd) * 8 : OOB), 0);
    sg[2] = ld16<AUX_SC1>(wsr, is_up ? G_MOD + (2048 + ud) * 8 : OOB, 0);
    sx2 = __builtin_amdgcn_raw_buffer_load_b32(mk_rsrc(x, (is_up || fin) ? 2048u : 0u), is_up ? ub * 1024 + ud * 2 : (g & 1) * 1024 + fd * 2, 0, 0);
  };
  auto side_pin = [&]() { PIN(sg[0]); PIN(sg[1]); PIN(sg[2]); PIN(sx2); };
  const u32 mlo = G_ML + (head * 2 + b) * 64 + (lane & 3) * 16;
  float fmrg = 0.f;
  auto merge_factor = [&](const u32x4& ml) {
    const float mi = u2f(ml[0]);
    float M = fmaxf(mi, dpp<XOR1>(mi)); M = fmaxf(M, dpp<XOR2>(M));
    const float e = __expf(mi - M);
    float L = e * u2f(ml[2]);
    L += dpp<XOR1>(L); L += dpp<XOR2>(L);
    fmrg = rdl(e * __builtin_amdgcn_rcpf(L), part);
  };
  f32x2 ga = {0.f, 0.f}, xin = {0.f, 0.f}, cc1 = {0.f, 0.f};
  float Af = 0.f, Bf = 0.f;
  const int hpos = ub * 512 + ud;
  auto up_prep = [&]() {
    if (!all_ok((sg[0][1] == tag) & (sg[0][3] == tag) & (sg[1][1] == tag) & (sg[1][3] == tag) & (sg[2][1] == tag) & (sg[2][3] == tag))) gather16<3>(wsr, G_MOD + (1024 + ud) * 8, 512 * 8, tag, sg);
    ga = f32x2{ub ? hi_f(sg[0][0]) : lo_f(sg[0][0]), ub ? hi_f(sg[0][2]) : lo_f(sg[0][2])};
    cc1 = f32x2{rbf(1.f + (ub ? hi_f(sg[2][0]) : lo_f(sg[2][0]))), rbf(1.f + (ub ? hi_f(sg[2][2]) : lo_f(sg[2][2])))};
    xin = f32x2{lo_f(sx2), hi_f(sx2)};
    u32* hp = reinterpret_cast<u32*>(&sm.hb[hpos]);
    hp[512] = pack_bf(cc1[0], cc1[1]);
    hp[1024] = ub ? perm_hi(sg[1][0], sg[1][2]) : perm_lo(sg[1][0], sg[1][2]);
  };
  auto up_frags = [&]() {
    const int pp = lane & 3;
    const u32 a = wimg + (pp & 1) * 32768 + ((pp >> 1) * 32 + g * 8 + ((lane >> 2) & 3)) * 64 + (((wave >> 1) ^ ((g & 1) | (pp & 2))) * 16) + (wave & 1) * 8;
#pragma unroll
    for (int rb = 0; rb < 8; ++rb) {
      const u32x2 lo = tr_read(a + rb * 4096), hi = tr_read(a + rb * 4096 + 256);
      bf[rb] = u32x4{lo[0], lo[1], hi[0], hi[1]};
    }
  };
  const u32x4* ap = reinterpret_cast<const u32x4*>(&sm.hb[((lane >> 3) & 1) * 512 + (lane & 1) * 32 + g * 8]);
  const bool par = lane & 8;
  auto limit = [&](auto sc) {
    constexpr int s = sc.v;
    WAIT_VM(window(s, 1 + (PRM0.W[s >> 3] >> (4 * (s & 7)) & 7)));
    SB();
  };
  float l0 = 0.f, l1 = 0.f;
  u32x2 gmd[2], tbg;
  int t1 = 0;
#pragma unroll
  for (int i = SK0; i < SK1; ++i) kr[i] = u32x4{0u, 0u, 0u, 0u};
  const int first = SK0 + NL - nl;
  auto left_out = [&](int i) { int f = first; asm volatile("" : "+s"(f)); return i < f; };
  sfor<1, NL>([&](auto ic) {
    constexpr int i = ic.v;
    if constexpr (i >= SK0 && i < SK1) { if (left_out(i)) return true; }
    const u32 so = __builtin_amdgcn_readlane(sot, i);
    gate();
    if constexpr (i >= TBS && i <= TBP) { if (timer) lag += (int)(((u32)wall_clock64() - t0) << 8) - due + ((int)__builtin_amdgcn_s_getreg(7 | (3 << 11)) + 1) * tau; }
    SB();
    if constexpr (i >= SK0 && i < SK1) ld16_into(kr[i], rk, lane * 16, so); else kr[i] = ld16<AUX_NT>(rk, lane * 16, so);
    if constexpr (i == SMO) small_ops();
    if constexpr (i == SMO + 1) { if (voter) __builtin_amdgcn_raw_buffer_store_b32(0u, wsr, pbo + (lane >> 5) * 8192 + (lane & 31) * 4, 0, AUX_SC1); }
    if constexpr (i == TBL) tbg = ld8<AUX_SC1>(wsr, wave == 7 ? G_TB + xcd * 128 + c * 8 : OOB, 0);
    if constexpr (i == QP) { gmd[0] = ld8<AUX_SC1>(wsr, part == 1 ? G_MOD + tid * 8 : OOB, 0); gmd[1] = ld8<AUX_SC1>(wsr, part == 1 ? G_MOD + tid * 8 : OOB, 4096); }
    SB();
    SB();
    if (i >= 4) limit(ic);
    if (i == 1) STAMP(25);
    if (i == 8) STAMP(12);
    if (i == 20) STAMP(21);
    if (i == 32) STAMP(22);
    if (i == TBP) {
      if (timer) {
        if (lane == 0) sm.mflag[wave] = lag;
        LDS_BARRIER();
        if (wave == 7) {
          int s = sm.mflag[lane & 7];
          s += __builtin_amdgcn_mov_dpp(s, XOR1, 0xf, 0xf, true); s += __builtin_amdgcn_mov_dpp(s, XOR2, 0xf, 0xf, true); s += __builtin_amdgcn_mov_dpp(s, XOR7, 0xf, 0xf, true);
          if (lane < 8) st8_sc1(wsr, G_TB + lane * 128 + wi * 8, t0 << 10 | (u32)min(s / (2048 * (TBP - TBS + 1)), 1023), tag);
        }
      }
      SB();
    }
    if (i == PRB) {
      if (voter) {
        u64 ta, tb, tc; u32 dm;
        const u32 pa = pbo + (wave & 1) * 8192;
        asm volatile("s_memtime %0\n\ts_waitcnt lgkmcnt(0)\n\ts_load_dword %3, %4, %5\n\ts_waitcnt lgkmcnt(0)\n\ts_memtime %1\n\ts_waitcnt lgkmcnt(0)\n\t"
                     "s_load_dword %3, %4, %6\n\ts_waitcnt lgkmcnt(0)\n\ts_memtime %2\n\ts_waitcnt lgkmcnt(0)"
                     : "=&s"(ta), "=&s"(tb), "=&s"(tc), "=&s"(dm) : "s"(ws), "s"(pa), "s"(pa ^ 8192u));
        const int d = min(max((int)(2 * (u32)tb - (u32)ta - (u32)tc), -500), 500);
        vote = ((wave ^ die) & 1) ? -d : d;
        STAMP(26);
      }
      SB();
    }
    if (i == TBU) {
      if (wave == 7) {
        const bool ok = tbg[1] == tag;
        float lf = ok ? (float)min(max((int)((t0 << 10 | 1023u) - tbg[0]) >> 10, -300), 300) : 0.f, nf = ok ? 1.f : 0.f, lg = ok ? (float)(tbg[0] & 1023u) : 0.f;
        lf += dpp<XOR1>(lf); nf += dpp<XOR1>(nf); lg += dpp<XOR1>(lg); lf += dpp<XOR2>(lf); nf += dpp<XOR2>(nf); lg += dpp<XOR2>(lg);
        lf += dpp<XOR7>(lf); nf += dpp<XOR7>(nf); lg += dpp<XOR7>(lg); lf += dpp<XOR8>(lf); nf += dpp<XOR8>(nf); lg += dpp<XOR8>(lg);
        const float rn = __builtin_amdgcn_rcpf(fmaxf(nf, 1.f));
        if (lane == 0) sm.bc = u32x2{tag, t0 - (u32)(int)(lf * rn) + (u32)min(max((int)((lg * rn - (float)P.GS) * (float)P.GB * 0.125f), 0), 150)};
      }
      LDS_BARRIER();
      const u32x2 bc = sm.bc;
      tag = __builtin_amdgcn_readfirstlane(bc[0]);
      t1 = (__builtin_amdgcn_readfirstlane((int)(bc[1] - t0)) + TEND + P.X1) * 256;
      tau -= __builtin_amdgcn_readfirstlane((int)((float)(int)(t0 - bc[1]) * 256.f * __builtin_amdgcn_rcpf((float)(nl - TBU - 1))));
      SB();
    }
    if (i == NL - 1) {
      if (reporter) {
        if (lane == 0) sm.mflag[wave] = vote;
        LDS_BARRIER();
        if (wave == 7) {
          int s = sm.mflag[lane & 7];
          s += __builtin_amdgcn_mov_dpp(s, XOR1, 0xf, 0xf, true); s += __builtin_amdgcn_mov_dpp(s, XOR2, 0xf, 0xf, true); s += __builtin_amdgcn_mov_dpp(s, XOR7, 0xf, 0xf, true);
          if (lane < 8) st8_sc1(wsr, G_VT + lane * 128 + xcd * 8, s, tag);
        }
      }
      SB();
    }
    if (i == LNS) {
      tag = next_tag(__builtin_amdgcn_readfirstlane(ep[0]));
      if (part == 1) {
        float x0[8], x1[8], mb0, ri0, mb1, ri1;
#pragma unroll
        for (int k = 0; k < 4; ++k) { x0[2 * k] = lo_f(xr0[k]); x0[2 * k + 1] = hi_f(xr0[k]); x1[2 * k] = lo_f(xr1[k]); x1[2 * k + 1] = hi_f(xr1[k]); }
        ln_stats2(x0, x1, mb0, ri0, mb1, ri1);
        l0 = rbf(rbf(lo_f(xs0) - mb0) * ri0); l1 = rbf(rbf(lo_f(xs1) - mb1) * ri1);
      }
      PIN(l0); PIN(l1);
      SB();
    }
    if (i == WUSE) {
      PIN(tch1); PIN(tch2);
      if ((part == 1 || (part == 2 && has_w && !modc))) {
        WAIT_VM(WK); CFENCE();
        if (part == 2) mod_stage(); else wo_prep();
      }
#pragma unroll
      for (int k = 0; k < 8; ++k) PIN(bf[k]);
      SB();
    }
    if (i == QP + QD) {
      if (part == 1) {
        u32 gm[2] = {gmd[0][0], gmd[1][0]};
        if (!all_ok((gmd[0][1] == tag) & (gmd[1][1] == tag))) { STAMP(11); gather8<2>(wsr, G_MOD + tid * 8, 512 * 8, tag, gm); }
        STAMP(5);
        gemv_go(pack_bf(rbf(l0 * rbf(1.f + lo_f(gm[1]))) + lo_f(gm[0]), rbf(l1 * rbf(1.f + hi_f(gm[1]))) + hi_f(gm[0])));
      }
      SB();
    }
    if (i == QP + QD + 1) {
      if (part == 1) {
        LDS_BARRIER();
        if (wave == 0) {
          const int bb = lane >> 5, jq = lane & 31;
          st8_sc1(wsr, G_Q + (bb * 2048 + blk * 32 + jq) * 8, pack_bf(sum8(&sm.r2[2 * jq * 20 + bb * 8]), sum8(&sm.r2[(2 * jq + 1) * 20 + bb * 8])), tag);
        }
        STAMP(6);
      }
      SB();
    }
    return true;
  });
  PIN(sot);
  tau = te1 >> 16;
  due += P.TG * 256;
  const u32 qoff = G_Q + ((b * 32 + head) * 64 + c * 4) * 8;
  u32x4 qg[2], mlg;
  u32 qq[4];
  constexpr int NB = (NL + 15) / 16;
  float xlo, sc[NB], pr[NB], m = 0.f;
  f32x4 acc = {0.f, 0.f, 0.f, 0.f};
  u32 pb[NB], pm[4], pl8[8], cpf;
  const u32 adst0 = lds_addr(lane < 32 ? &sm.red[wave][c * 8 + g * 4] : &sm.r2[(lane - 32) * 4]);
  u32 po, rdst, rsrc, xo;
  auto wo_addr = [&]() {
    po = dup(G_P + xcd * 8192) + tid * 16 + near;
    rdst = lane < 32 ? (u32)(uintptr_t)&sm.red2[wave][0] + lane * 16 : (u32)(uintptr_t)&sm.r2[0] + (lane - 32) * 16;
    rsrc = (u32)(uintptr_t)&sm.red2[0][((wave & 1) * 16 + (lane & 15)) * 4 + (lane >> 4)];
    xo = dup(G_XP + xcd * 2048 + (wave & 1) * 1024 + (blk * 64 + (lane & ~1)) * 2) + (lane & 1) * 8192;
    PIN(po); PIN(rdst); PIN(rsrc); PIN(xo);
  };
  u32 pso = (wave == 0 && (g & 1) == 0) ? pslot + (c >> 3) * 1024 + (c & 3) * 256 + (c >> 2 & 1) * 128 + (g >> 1) * 8192 : OOB;
  PIN(pso);
  const u32 nadd = wdef ? P.LW : (is_up ? P.LU : 0);
  u32 nto = tid < 2 ? dup(G_NT) + bid * 8 + tid * 8192 : OOB;
  PIN(nto);
  auto notices = [&]() -> int {
    u32 off = dup(G_NT) + near + lane * 16;
    PIN(off);
    until(due);
    int mx;
#pragma nounroll
    for (int spin = 0; spin < SPIN_MAX; ++spin) {
      CFENCE();
      const u32x4 ga_ = ld16<AUX_SC1>(wsr, off, 0), gb_ = ld16<AUX_SC1>(wsr, off, 1024);
      mx = max(max((int)(ga_[0] - t0), (int)(ga_[2] - t0)), max((int)(gb_[0] - t0), (int)(gb_[2] - t0)));
      if (all_ok((((ga_[1] ^ tag) | (ga_[3] ^ tag)) | ((gb_[1] ^ tag) | (gb_[3] ^ tag))) == 0u)) break;
      for (int k = 0; k < P.AG; ++k) __builtin_amdgcn_s_sleep(1);
    }
    if (all_ok(mx <= (t1 >> 8) - P.AC)) return t1;
    mx = max(mx, __builtin_amdgcn_mov_dpp(mx, XOR1, 0xf, 0xf, true)); mx = max(mx, __builtin_amdgcn_mov_dpp(mx, XOR2, 0xf, 0xf, true));
    mx = max(mx, __builtin_amdgcn_mov_dpp(mx, XOR7, 0xf, 0xf, true)); mx = max(mx, __builtin_amdgcn_mov_dpp(mx, XOR8, 0xf, 0xf, true));
    mx = max(max(__builtin_amdgcn_readlane(mx, 0), __builtin_amdgcn_readlane(mx, 16)), max(__builtin_amdgcn_readlane(mx, 32), __builtin_amdgcn_readlane(mx, 48)));
    return max(min(mx, 1 << 20) * 256, t1 + P.L0 * 256) + P.L1 * 256;
  };
  auto pv_row = [&](auto jc) {
    constexpr int i = jc.v;
    const u32 pv = __float_as_uint(bcast16(u2f(pb[i >> 4]), i & 15));
    acc = mfma16(u32x4{pv & pm[0], pv & pm[1], pv & pm[2], pv & pm[3]}, vr[i], acc);
    PIN(acc);
    return true;
  };
  sfor<0, NL - 1>([&](auto ic) {
    constexpr int v = ic.v;
    if constexpr (v >= SK0 && v < SK1) { if (left_out(v)) return true; }
    if constexpr (v > 0) {
      const u32 so = __builtin_amdgcn_readlane(sot, v);
      if constexpr (v >= SK1) { if (v == NL - P.NE) tau = (tau * P.FE) >> 4; }
      gate();
      SB();
      if constexpr (v >= SK0 && v < SK1) ld16_into(vr[v], rv, lane * 16, so); else vr[v] = ld16<AUX_NT>(rv, lane * 16, so);
      SB();
      SB();
    }
    if constexpr (v == SK0 - 1) {
#pragma unroll
      for (int i = SK0; i < SK1; ++i) { vr[i] = u32x4{0u, 0u, 0u, 0u}; PIN(vr[i]); }
    }
    if constexpr (v == VQ - 4) { qg[0] = ld16<AUX_SC1>(wsr, qoff, 0); qg[1] = ld16<AUX_SC1>(wsr, qoff + 16, 0); }
    if constexpr (v == VSIDE) { side_loads(); tbg = ld8<AUX_SC1>(wsr, wave == 7 ? G_VT + xcd * 128 + (lane & 7) * 8 : OOB, 0); }
    if constexpr (v == CPF) {
      u64 pc; u32 dlt, len;
      asm volatile("s_getpc_b64 %0\n.Lcpf_here:\n\ts_mov_b32 %1, .Ltail_start-.Lcpf_here\n\ts_mov_b32 %2, .Lend_a-.Ltail_start\n\ts_max_i32 %2, %2, .Lend_b-.Ltail_start\n\ts_max_i32 %2, %2, .Lend_c-.Ltail_start\n\ts_max_i32 %2, %2, .Lend_d-.Ltail_start\n\ts_max_i32 %2, %2, .Lend_e-.Ltail_start" : "=s"(pc), "=s"(dlt), "=s"(len) :: "scc");
      pc += dlt;
      const rsrc_t rc = mk_rsrc((const void*)pc, (part == 3 && i8 == 1) ? len : 0u);
      cpf = __builtin_amdgcn_raw_buffer_load_b32(rc, tid * 64, 0, 0);
    }
    if constexpr (v == SK1 + 2) PIN(cpf);
    if constexpr (v == SK1) mlg = ld16<AUX_SC1>(wsr, wave == 0 ? mlo : OOB, 0);
    SB();
    if constexpr (NL + v < 72) limit(IC<NL + v>{});
    if constexpr (v >= SK0 + WV) { WAIT_VM(v < SK1 ? WV : WV + 1); SB(); }
    if constexpr (v == VQ) {
      if (!all_ok((qg[0][1] == tag) & (qg[0][3] == tag) & (qg[1][1] == tag) & (qg[1][3] == tag))) { STAMP(20); gather16<2>(wsr, qoff, 16, tag, qg); }
      STAMP(1); WSTAMP(1);
      qq[0] = qg[0][0]; qq[1] = qg[0][2]; qq[2] = qg[1][0]; qq[3] = qg[1][2];
    }
    if constexpr (v >= VQ && v < VS) {
      constexpr int sl = v - VQ, bt = sl >> 1;
      float xs[8];
#pragma unroll
      for (int i = 0; i < 8; ++i) {
        float d = 0.f;
        if (sl * 8 + i < NL) {
#pragma unroll
          for (int k = 0; k < 4; ++k) d = dot2(kr[sl * 8 + i][k], qq[k], d);
        }
        xs[i] = d;
      }
#pragma unroll
      for (int h = 0; h < 4; ++h) xs[h] = sel_add<XOR7>(xs[h], xs[h + 4], c & 4);
#pragma unroll
      for (int h = 0; h < 2; ++h) xs[h] = sel_add<XOR2>(xs[h], xs[h + 2], c & 2);
      xs[0] = sel_add<XOR1>(xs[0], xs[1], c & 1);
      if constexpr ((sl & 1) || sl == NSL - 1) {
        const float t = (sl & 1) ? sel_add<XOR8>(xlo, xs[0], c & 8) : xs[0] + dpp<XOR8>(xs[0]);
        const float s = rbf(rbf(t) * 0.08838834764831845f);
        sc[bt] = (bt * 16 + c < SK0 || (bt * 16 + c >= first && bt * 16 + c < NL)) ? s : -__builtin_inff();
        PIN(sc[bt]);
      } else {
        xlo = xs[0];
        PIN(xlo);
      }
    }
    if constexpr (v == VS) {
      float mloc = sc[0];
#pragma unroll
      for (int bt = 1; bt < NB; ++bt) mloc = fmaxf(mloc, sc[bt]);
      m = fmaxf(wave_max(mloc), -3.0e38f);
      float ls = 0.f;
#pragma unroll
      for (int bt = 0; bt < NB; ++bt) { pr[bt] = __expf(sc[bt] - m); ls += pr[bt]; }
      const float l = wave_sum(ls);
      if (lane == 0) { sm.ml[wave][0] = m; sm.ml[wave][1] = l; }
      if (c == ((NL - 1) & 15)) sm.pl[g][wave] = pr[NB - 1];
      STAMP(2); WSTAMP(2);
    }
    if constexpr (v >= SK1) pv_row(IC<v - SK1 + SK0 - WV>{});
    if constexpr (v < SK0 || v >= SK0 + WV) sfor<cons_end(v - 1), cons_end(v)>(pv_row);
    if constexpr (v == SK1 + 1) {
      float p[8];
#pragma unroll
      for (int w = 0; w < 8; ++w) p[w] = sm.pl[g][w] * __expf(sm.ml[w][0] - m);
#pragma unroll
      for (int w = 0; w < 8; ++w) pl8[w] = pack_bf(p[w], p[w]);
#pragma unroll
      for (int w = 0; w < 8; ++w) PIN(pl8[w]);
    }
    if constexpr (v == MLS - 1) { side_pin(); if (is_up) up_prep(); PIN(ga); PIN(xin); PIN(cc1); }
    if constexpr (v == UPREP - 1) wo_addr();
    if constexpr (v == UPREP) {
#pragma unroll
      for (int k = 0; k < 8; ++k) asm volatile("" : "=v"(bf[k]));
      if (part == 0) wo_prep(); else if (is_up) up_frags();
#pragma unroll
      for (int k = 0; k < 8; ++k) PIN(bf[k]);
    }
    if constexpr (v == UPREP + 1) {
      if (is_up) {
        const u32x4* a1 = ap + 128 + ((lane >> 1) & 1) * 128;
        f32x4 d0 = {0.f, 0.f, 0.f, 0.f}, d1 = {0.f, 0.f, 0.f, 0.f};
#pragma unroll
        for (int rb = 0; rb < 4; ++rb) { d0 = mfma16(a1[rb * 8], bf[rb], d0); d1 = mfma16(a1[(rb + 4) * 8], bf[rb + 4], d1); }
        Af = par ? d0[1] + d1[1] : d0[0] + d1[0]; Bf = par ? d0[3] + d1[3] : d0[2] + d1[2];
        Af += dpp<XOR8>(Af); Bf += dpp<XOR8>(Bf);
      }
      PIN(Af); PIN(Bf);
    }
    if (v == 10 || v == 16) STAMP(15 + v / 6);
    if (v == 20) STAMP(18);
    if (v == 32) STAMP(19);
    if constexpr (v == MLS) {
      if (wave == 7) {
        int vt = tbg[1] == tag ? (int)tbg[0] : 0;
        vt += __builtin_amdgcn_mov_dpp(vt, XOR1, 0xf, 0xf, true); vt += __builtin_amdgcn_mov_dpp(vt, XOR2, 0xf, 0xf, true); vt += __builtin_amdgcn_mov_dpp(vt, XOR7, 0xf, 0xf, true);
        if (lane == 0) sm.bc[0] = vt;
      }
      LDS_BARRIER();
      near = ((__builtin_amdgcn_readfirstlane(sm.bc[0]) >> 31 ^ die ^ P.NR ^ 1) & 1) * 8192u;
      float mm[8], ll[8];
#pragma unroll
      for (int w = 0; w < 8; ++w) { const f32x2 t = *reinterpret_cast<const f32x2*>(&sm.ml[w][0]); mm[w] = t[0]; ll[w] = t[1]; }
      float Mwg = mm[0], Lwg = 0.f;
#pragma unroll
      for (int w = 1; w < 8; ++w) Mwg = fmaxf(Mwg, mm[w]);
      if (wave == 0) {
#pragma unroll
        for (int w = 0; w < 8; ++w) Lwg = fmaf(__expf(mm[w] - Mwg), ll[w], Lwg);
        if (lane == 0) st16_sc1(wsr, G_ML + ((head * 2 + b) * 4 + part) * 16, __float_as_uint(Mwg), __float_as_uint(Lwg), tag);
      }
      const float fw = __expf(m - Mwg);
      m = Mwg;
#pragma unroll
      for (int bt = 0; bt < NB; ++bt) { pb[bt] = pack_bf(pr[bt] * fw, pr[bt] * fw); PIN(pb[bt]); }
#pragma unroll
      for (int k = 0; k < 4; ++k) { pm[k] = (c >> 1 & 3) == k ? ((c & 1) ? 0xffff0000u : 0x0000ffffu) : 0u; PIN(pm[k]); }
    }
    SB();
    return true;
  });
  asm volatile(".Ltail_start:");
  if (wave != 0) {
    pv_row(IC<SK0 - 1>{});
    WAIT_VM(WV); SB();
    sfor<NL - 1 - WV, NL - 1>(pv_row);
    *LDSP(f32x4, adst0) = acc;
    WSTAMP(14);
    LDS_BARRIER();
  } else {
    u32x4 xr[8], am[8];
#pragma unroll
    for (int w = 0; w < 8; ++w) am[w] = u32x4{pl8[w] & pm[0], pl8[w] & pm[1], pl8[w] & pm[2], pl8[w] & pm[3]};
#pragma unroll
    for (int w = 0; w < 8; ++w) PIN(am[w]);
    {
      const u32 so = __builtin_amdgcn_readlane(sot, NL - 1);
      gate();
      SB();
#pragma unroll
      for (int w = 0; w < 8; ++w) xr[w] = ld16<AUX_NT>(rv, lane * 16, so ^ ((w & 1) ? col(P, 7) : 0u) ^ ((w & 2) ? col(P, 8) : 0u) ^ ((w & 4) ? col(P, 9) : 0u));
      SB();
    }
    u32 rsum = lds_addr(&sm.red[0][c * 8 + (g & 1) * 4]);
    pv_row(IC<SK0 - 1>{});
    WAIT_VM(WV + 8); SB();
    sfor<NL - 1 - WV, NL - 3>(pv_row);
    if (__builtin_expect(!all_ok((mlg[1] == tag) & (mlg[3] == tag)), 0)) {
#pragma nounroll
      for (int spin = 0; spin < SPIN_MAX; ++spin) {
        CFENCE();
        mlg = ld16<AUX_SC1>(wsr, mlo, 0);
        if (all_ok((mlg[1] == tag) & (mlg[3] == tag))) break;
      }
    }
    merge_factor(mlg);
    sfor<NL - 3, NL - 1>(pv_row);
    STAMP(14); WSTAMP(14);
    LDS_BARRIER();
    STAMP(15);
    f32x4 t[8];
#pragma unroll
    for (int w = 1; w < 8; ++w) t[w] = LDSP(f32x4, rsum)[w * 32];
    acc = ((acc + t[1]) + (t[2] + t[3])) + ((t[4] + t[5]) + (t[6] + t[7]));
    PIN(acc);
    STAMP(3);
    SB();
#pragma unroll
    for (int w = 0; w < 8; ++w) acc = mfma16(am[w], xr[w], acc);
  }
  WAIT_VM(0);
  acc *= fmrg;
  {
    const u32 w0 = min(pack_bf(acc[0], acc[1]), SENT - 1u), w1 = min(pack_bf(acc[2], acc[3]), SENT - 1u);
    const auto e0 = __builtin_amdgcn_permlane16_swap(w0, w0, false, false), e1 = __builtin_amdgcn_permlane16_swap(w1, w1, false, false);
    __builtin_amdgcn_raw_buffer_store_b128(u32x4{e0[0], e1[0], e0[1], e1[1]}, wsr, pso, 0, AUX_SC1);
  }
  st8_sc1(wsr, nto, (u32)wall_clock64() + nadd, tag);
  SB();
  STAMP(4);

  if (part == 2) FINISH();
  if (part == 0) {
    __builtin_amdgcn_s_barrier();
#pragma nounroll
    while (wleft > 0) piece();
    bool late = false;
    u32x4 og[2];
    if (wdef) {
      late = true;
      while ((int)(((u32)wall_clock64() - t0) << 8) < t1 - 20 * 256) if (__builtin_amdgcn_s_getreg(7 | (3 << 11)) == 0) { late = false; break; }
      if (__builtin_expect(late, 0)) { until(t1); CFENCE(); og[0] = ld16<AUX_SC1>(wsr, po, 0); SB(); WAIT_VM(1); } else { WAIT_VM(0); }
      CFENCE();
      if (P.WD > 4) wo_prep(0, 1);
      wo_prep(1, 2);
#pragma unroll
      for (int k = 0; k < 8; ++k) PIN(bf[k]);
      SB();
    }
    if (__builtin_expect(!late, 1)) { until(t1); CFENCE(); og[0] = ld16<AUX_SC1>(wsr, po, 0); }
    STAMP(11);
    SB();
    f32x4 d[4];
    auto wo_mm = [&]() {
      const f32x4 z = {0.f, 0.f, 0.f, 0.f};
#pragma unroll
      for (int t = 0; t < 4; ++t) { d[t] = mfma16(og[0], bf[t], z); og[1][t] = __float_as_uint(dpp<XOR8>(u2f(og[0][t]))); }
      SB();
#pragma unroll
      for (int t = 0; t < 4; ++t) d[t] = mfma16(og[1], bf[4 + t], d[t]);
    };
    auto p_bad = [&]() { return !all_ok(max(max(og[0][0], og[0][1]), max(og[0][2], og[0][3])) != SENT); };
    wo_mm();
    if (__builtin_expect(p_bad(), 0)) {
#pragma nounroll
      for (int spin = 0; spin < SPIN_MAX; ++spin) {
        __builtin_amdgcn_s_sleep(1);
        CFENCE();
        og[0] = ld16<AUX_SC1>(wsr, po, 0);
        if (!p_bad()) break;
      }
      wo_mm();
      SB();
      asm volatile(".Lend_d:");
    }
    STAMP(5); WSTAMP(5);
    {
      SB();
      f32x4 y; f32x2 h[4];
#pragma unroll
      for (int t = 0; t < 4; ++t) { h[t] = d[t].xy + d[t].zw; PIN(h[t]); }
#pragma unroll
      for (int t = 0; t < 4; ++t) { y[t] = h[t][0] + h[t][1]; PIN(y[t]); }
      *LDSP(f32x4, rdst) = y;
    }
    STAMP(16);
    LDS_BARRIER();
    STAMP(17);
    if (wave < 2) {
      f32x2 v[4];
#pragma unroll
      for (int w = 0; w < 4; ++w) v[w] = f32x2{LDSP(float, rsrc)[w * 256], LDSP(float, rsrc)[w * 256 + 128]};
      const f32x2 h = (v[0] + v[1]) + (v[2] + v[3]);
      const float y = h[0] + h[1];
      __builtin_amdgcn_raw_buffer_store_b32(min(pack_bf(dpp<0xA0>(y), dpp<0xF5>(y)), SENT - 1u), wsr, xo, 0, AUX_SC1);
      asm volatile(".Lend_a:");
    }
    STAMP(6);
    FINISH();
  }
  if (part == 3) {
    if (!has_w) FINISH();
    u32x4 b2[4];
    {
      __builtin_amdgcn_s_barrier();
      const rsrc_t r2 = mk_rsrc(w2 + (size_t)(blk * 32 + wave * 4) * 512, 4096u);
      const u32 l2 = (u32)(uintptr_t)(&sm.w2b[wave * 4][0]);
#pragma unroll
      for (int n = 0; n < 4; ++n) dma16(r2, (lane ^ (n * 2) ^ ((wave & 2) << 2)) * 16, n * 1024, l2 + n * 1024);
    }
    const u32 tdst = lds_addr(&sm.hb[hpos]);
    const u32 sdst = lds_addr(&sm.stat[wave][lane]);
    const u32 ssrc = lds_addr(&sm.stat[(g >> 1) * 4 + ((lane >> 3) & 3)][(lane & 7) * 4]);
    const u32 adst = lds_addr((c < 4 && (g & 1) == 0) ? &sm.actl[g >> 1][wave * 4 + c] : reinterpret_cast<u16*>(&sm.dummy[lane]));
    const u32 asrc = lds_addr(&sm.actl[lane & 1][g * 8]);
    const u32 apa = lds_addr(&sm.hb[(c >> 3) * 512 + (c & 3) * 64 + ((c >> 2) & 1) * 32 + g * 8]);
    constexpr u32 xs2 = 16384;
    u32 xoff = dup(G_XP) + near + g * 2048 + ub * 1024 + (wave & 3) * 256 + c * 16;
    u32x4 one;
#pragma unroll
    for (int k = 0; k < 4; ++k) one[k] = ((c & 2) == 0 && (c >> 2) == k) ? ((c & 1) ? 0x3f800000u : 0x00003f80u) : 0u;
    PIN(one);
    u32 ooff = dup(G_OP + (blk * 512 + tid) * 8);
    float Afp = Af, Bfp = Bf;
    PIN(xoff); PIN(ooff); PIN(Afp); PIN(Bfp);
    f32x2 gc = ga * cc1, xc = xin * cc1;
    PIN(ga); PIN(xin); PIN(gc); PIN(xc);
    u32 w2a = (u32)(uintptr_t)(&sm.w2b[0][0]) + (g * 8 + ((lane >> 2) & 3)) * 1024 + (wave ^ (g & 1)) * 128 + (lane & 15) * 8;
    PIN(w2a);
    if (wave == 1) {
      due = t1 + P.N1 * 256;
      const int tn = notices();
      due = tn + P.X2 * 256;
      until(due);
    }
    asm volatile("s_waitcnt vmcnt(0)" ::: "memory");
    STAMP(9);
    __builtin_amdgcn_s_barrier();
    STAMP(11);
    CFENCE();
    u32x4 xg[2] = {ld16<AUX_SC1>(wsr, xoff, 0), ld16<AUX_SC1>(wsr, xoff, xs2)};
    SB();
#pragma unroll
    for (int t = 0; t < 4; ++t) {
      const u32x2 lo = tr_read(w2a ^ (t * 32)), hi = tr_read((w2a ^ (t * 32)) + 4096);
      b2[t] = u32x4{lo[0], lo[1], hi[0], hi[1]};
    }
#pragma unroll
    for (int t = 0; t < 4; ++t) PIN(b2[t]);
    SB();
    const f32x4 z4 = {0.f, 0.f, 0.f, 0.f};
    auto xp_bad = [&]() { return !all_ok(max(max(max(xg[0][0], xg[0][1]), max(xg[0][2], xg[0][3])), max(max(xg[1][0], xg[1][1]), max(xg[1][2], xg[1][3]))) != SENT); };
    f32x4 v = mfma16(one, xg[1], mfma16(one, xg[0], z4));
    if (__builtin_expect(xp_bad(), 0)) {
#pragma nounroll
      for (int spin = 0; spin < SPIN_MAX; ++spin) {
        __builtin_amdgcn_s_sleep(1);
        CFENCE();
        xg[0] = ld16<AUX_SC1>(wsr, xoff, 0); xg[1] = ld16<AUX_SC1>(wsr, xoff, xs2);
        if (!xp_bad()) break;
      }
      v = mfma16(one, xg[1], mfma16(one, xg[0], z4));
      SB();
      asm volatile(".Lend_e:");
    }
    STAMP(5); WSTAMP(5);
    {
      const f32x2 a = v.xy, hc = __builtin_elementwise_fma(gc, a, xc), x1 = __builtin_elementwise_fma(ga, a, xin);
      *LDSP(u32, tdst) = pack_bf(hc[0], hc[1]);
      *LDSP(float, sdst) = swap32_add(x1[0] + x1[1], fmaf(x1[0], x1[0], x1[1] * x1[1]));
    }
    STAMP(16);
    LDS_BARRIER();
    STAMP(17);
    {
      const f32x4 z = {0.f, 0.f, 0.f, 0.f};
      const u32x4 af0 = LDSP(u32x4, apa)[0];
      SB();
      const f32x4 s4 = LDSP(f32x4, ssrc)[0], q4 = LDSP(f32x4, ssrc)[8];
      const u32x4 af1 = LDSP(u32x4, apa)[32];
      SB();
      f32x4 d[4];
      d[0] = mfma16(af0, bf[0], z); d[1] = mfma16(af0, bf[1], z);
      SB();
      const f32x2 s2 = s4.xy + s4.zw, q2 = q4.xy + q4.zw;
      float s = s2[0] + s2[1], q = q2[0] + q2[1];
      SB();
      d[2] = mfma16(af0, bf[2], z);
      SB();
      s = swap16_add(s, q);
      SB();
      d[3] = mfma16(af0, bf[3], z);
      SB();
      s += dpp<XOR1>(s); s += dpp<XOR2>(s);
      SB();
      d[0] = mfma16(af1, bf[4], d[0]);
      SB();
      s += dpp<XOR7>(s); s += dpp<XOR8>(s);
      SB();
      d[1] = mfma16(af1, bf[5], d[1]);
      SB();
      const auto sq = __builtin_amdgcn_permlane16_swap(__float_as_uint(s), __float_as_uint(s), false, false);
      const float mean = u2f(sq[0]) * (1.f / 512.f);
      SB();
      d[2] = mfma16(af1, bf[6], d[2]);
      SB();
      const float var = fmaf(u2f(sq[1]), 1.f / 512.f, -mean * mean);
      SB();
      d[3] = mfma16(af1, bf[7], d[3]);
      SB();
      float ri = __builtin_amdgcn_rsqf(fmaxf(var, 0.f) + 1e-5f);
      float cf = fmaf(-ri * mean, Afp, Bfp);
      float rk = ri * -1.44269504f, ck = cf * -1.44269504f;
      PIN(ri); PIN(cf); PIN(rk); PIN(ck);
      SB();
      PIN(d[0]); PIN(d[1]);
      float y = d[0][0] + d[1][1];
      PIN(y); PIN(d[2]);
      y += d[2][2];
      PIN(y); PIN(d[3]);
      y += d[3][3];
      y = __builtin_bit_cast(float, __builtin_amdgcn_update_dpp(__builtin_bit_cast(int, y), __builtin_bit_cast(int, y), XOR8, 0xa, 0xf, false));
      y = swap16_add(y, y);
      const float ex = __builtin_amdgcn_exp2f(fmaf(y, rk, ck));
      y = fmaf(y, ri, cf);
      const float gu = y * dpp<0x104>(y);
      *LDSP(u16, adst) = (u16)pack_bf(gu * __builtin_amdgcn_rcpf(1.f + ex), 0.f);
    }
    STAMP(18);
    LDS_BARRIER();
    STAMP(10);
    {
      const u32x4 af = *LDSP(u32x4, asrc);
      const f32x4 z = {0.f, 0.f, 0.f, 0.f};
      f32x4 d0 = mfma16(af, b2[0], z), d1 = mfma16(af, b2[1], z), d2 = mfma16(af, b2[2], z), d3 = mfma16(af, b2[3], z);
      asm volatile("" : "+v"(d0), "+v"(d1), "+v"(d2), "+v"(d3));
      const bool g1 = g & 1, g2 = g & 2;
      const float e0 = g1 ? d1[0] : d0[0], e1 = g1 ? d1[1] : d0[1], f0 = g1 ? d3[0] : d2[0], f1 = g1 ? d3[1] : d2[1];
      const u32x2 ov = {pack_bf(g2 ? f0 : e0, g2 ? f1 : e1), tag};
      __builtin_amdgcn_raw_buffer_store_b64(ov, wsr, ooff, near ^ 8192u, AUX_SC1);
      __builtin_amdgcn_raw_buffer_store_b64(ov, wsr, ooff, near, AUX_SC1);
    }
    asm volatile(".Lend_b:");
    STAMP(6);
    FINISH();
  }
  if (wave == 0) {
    if (!all_ok((sg[0][1] == tag) & (sg[0][3] == tag) & (sg[1][1] == tag) & (sg[1][3] == tag))) {
      u32x4 t[2];
      gather16<2>(wsr, G_MOD + (1024 + fd) * 8, 1536 * 8, tag, t);
      sg[0] = t[0]; sg[1] = t[1];
    }
    u32 xa;
    {
      due = t1 + P.N2 * 256;
      const int tn = notices();
      due = tn + P.X3 * 256;
      until(due);
    }
    const bool hb = g & 1;
#pragma nounroll
    for (int spin = 0; spin < SPIN_MAX; ++spin) {
      CFENCE();
      xa = __builtin_amdgcn_raw_buffer_load_b32(wsr, dup(G_XP) + near + (g >> 1) * 16384 + (c >> 2) * 2048 + (g & 1) * 1024 + fd * 2, 0, AUX_SC1);
      if (all_ok(xa != SENT)) break;
      __builtin_amdgcn_s_sleep(1);
    }
    float a0 = lo_f(xa), a1 = hi_f(xa);
    a0 += dpp<0x124>(a0); a1 += dpp<0x124>(a1);
    a0 += dpp<XOR8>(a0); a1 += dpp<XOR8>(a1);
    a0 = swap32_add(a0, a0); a1 = swap32_add(a1, a1);
    f32x2 xv = {rbf(lo_f(sx2) + rbf((hb ? hi_f(sg[0][0]) : lo_f(sg[0][0])) * rbf(a0))), rbf(hi_f(sx2) + rbf((hb ? hi_f(sg[0][2]) : lo_f(sg[0][2])) * rbf(a1)))};
    f32x2 gv = {hb ? hi_f(sg[1][0]) : lo_f(sg[1][0]), hb ? hi_f(sg[1][2]) : lo_f(sg[1][2])};
    PIN(xv); PIN(gv);
    if (wi == 0 && lane == 0) __builtin_amdgcn_raw_buffer_store_b32(tag, wsr, WS_EPOCH, 0, AUX_SC1);
    STAMP(10);
    due = max(due + P.X4 * 256, (int)(((u32)wall_clock64() - t0) << 8) + P.GX * 256);
    const bool last = lane < (NUP - 32) * 4;
    u32 o0 = dup(G_OP + (lane >> 2) * 4096 + fd * 8) + near, o2 = last ? o0 : OOB, tg2 = last ? tag : 0u;
    const rsrc_t rout = mk_rsrc(out, 2048u);
    u32 oo = (lane & 44) == 0 ? (g * 512 + fd) * 2 : OOB;
    const u32 pk = (c & 10) ? 0u : ((c & 4) ? 0x3f800000u : 0x00003f80u);
    u32x4 one = {(c & 1) ? 0u : pk, 0u, (c & 1) ? pk : 0u, 0u};
    PIN(o0); PIN(o2); PIN(tg2); PIN(oo); PIN(one);
    __builtin_amdgcn_raw_buffer_store_b32(pack_bf(xv[0], xv[1]), rout, oo, 0, AUX_SC1);
    u32 s1_ = 131072, s2_ = 262144;
    asm volatile("" : "+s"(s1_), "+s"(s2_));
    until(due);
    f32x4 sv;
#pragma nounroll
    for (int spin = 0; spin < SPIN_MAX; ++spin) {
      CFENCE();
      const u32x4 og[3] = {ld16<AUX_SC1>(wsr, o0, 0), ld16<AUX_SC1>(wsr, o0, s1_), ld16<AUX_SC1>(wsr, o2, s2_)};
      sv = f32x4{0.f, 0.f, 0.f, 0.f};
#pragma unroll
      for (int k = 0; k < 3; ++k) sv = mfma16(one, og[k], sv);
      SB();
      u32 bad = ((og[0][1] ^ tag) | (og[0][3] ^ tag) | (og[1][1] ^ tag)) | ((og[1][3] ^ tag) | (og[2][1] ^ tg2) | (og[2][3] ^ tg2));
      PIN(bad);
      SB();
      float s0 = sv[0] + dpp<0x124>(sv[0]), s1 = sv[1] + dpp<0x124>(sv[1]);
      PIN(s0); PIN(s1);
      s0 += dpp<XOR8>(s0); s1 += dpp<XOR8>(s1);
      PIN(s0); PIN(s1);
      __builtin_amdgcn_raw_buffer_store_b32(pack_bf(fmaf(gv[0], s0, xv[0]), fmaf(gv[1], s1, xv[1])), rout, oo, 0, AUX_SC1);
      SB();
      if (all_ok(bad == 0u)) break;
      __builtin_amdgcn_s_sleep(1);
    }
    STAMP(7);
    asm volatile(".Lend_c:");
  }
  FINISH();
}

extern "C" void kernel_launch(void* const* d_in, const int* in_sizes, int n_in,
                              void* d_out, int out_size, void* d_ws, size_t ws_size,
                              hipStream_t stream) {
  fused_kernel<<<256, 512, 0, stream>>>((const u16*)d_in[1], (const u16*)d_in[2], (const u16*)d_in[4], (const u16*)d_in[3], (const u16*)d_in[10], (const u16*)d_in[6], (const u16*)d_in[7],
                                        (unsigned char*)d_ws, (const u16*)d_in[0], (const u16*)d_in[9], (const u16*)d_in[8], (u16*)d_out);
}
```

```cpp
#define PROBE_MUL 10
#include <hip/hip_runtime.h>
#include <stdint.h>

typedef unsigned int u32;
typedef unsigned short u16;
typedef unsigned long long u64;
typedef __attribute__((ext_vector_type(4))) unsigned u32x4;
typedef __attribute__((ext_vector_type(2))) unsigned u32x2;
typedef __attribute__((ext_vector_type(2))) __bf16 bf16x2;
typedef __attribute__((ext_vector_type(4))) float f32x4;
typedef __attribute__((ext_vector_type(2))) float f32x2;
typedef __amdgpu_buffer_rsrc_t rsrc_t;

constexpr int NH = 32, SK = 16384, DH = 128, WIN = 4096, FF = 1376;
struct Prm { int E0, ES, E1, E2, E2C, EU, TEND, DT_UP, L0, L1, X1, X2, X3, X4, NE, FE, M0, M1, M2, M3, M4, GS, GX, N1, N2, GA, WD, DT_WO, LW, LU, EW, PM, NF, NR, C0, A0, AG, AC, TS, TW, TK, TG, GB, TT, W[9]; };
constexpr Prm PRM0 = {32, 20, 32, 32, 54, 58, 1978, 60, 12, 30, 88, 70, 100, 89, 3, 8, 0x2511f55b, 0x2b7425d5, 0x6017665, 0x17e0b9ec, 0x31944305, 68, 30, 36, 62, 0, 4, 100, 45, 15, 0, 0x0, 15, 14, 5, 30, 4, 4, 509, 228, 60, 20, 12, 0, 0x77777777, 0x77777777, 0x77777777, 0x77777777, 0x77777777, 0x44444477, 0x44444444, 0x44444444, 0x44444444};
constexpr int NL = 37;
constexpr int SK0 = 21, SK1 = 32;
constexpr int NCMIN = (NL - SK1 + SK0) * 8;
constexpr int WK = 8, WV = 5;
constexpr int WUSE = 12, LNS = 7;
constexpr int QP = 14, QD = 4;
constexpr int VQ = 6;
constexpr int NSL = (NL + 7) / 8;
constexpr int VS = VQ + NSL;
constexpr int MLS = VS + 2;
constexpr int VSIDE = MLS - 5, UPREP = MLS + 6;
constexpr int CPF = 17;
constexpr int SMO = 1;
constexpr int PRB = 19;
constexpr int TBS = 5, TBP = 9, TBL = 13, TBU = 17;
constexpr int window(int s, int w) {
  int n = w;
  for (int u = s - w + 1; u <= s; ++u) n += u == SMO ? 6 : (u == TBL || u == NL + CPF || u == NL + SK1 ? 1 : (u == QP || u == NL + VQ - 4 ? 2 : (u == NL + VSIDE ? 5 : 0)));
  return n;
}
constexpr int cons_end(int v) {
  const int a = v - WV + 1, c = (v - MLS) * 4;
  const int m = a < c ? a : c;
  return m < 0 ? 0 : (m > NL ? NL : m);
}
constexpr u32 col(const Prm& P, int j) { const int m[6] = {P.M0, P.M1, P.M2, P.M3, P.M4, P.PM}; return (u32)(m[j / 3] >> (j % 3 * 10) & 1023) << 10; }
constexpr u32 first_col(const Prm& P, int k) {
  if (k < 4) return col(P, 12 + k) | 1u << (24 + k);
  if (k < 6) return col(P, k - 4);
  if (k < 8) return col(P, 4 + k) | 1u << (16 + k);
  if (k < 11) return col(P, k - 1);
  return (col(P, 2) ^ col(P, 3) ^ col(P, 4) ^ col(P, 5) ^ col(P, 6) ^ col(P, 10) ^ col(P, 11)) | 0xf00000u;
}
constexpr bool one_to_one(const Prm& P) {
  u32 c[10] = {};
  for (int j = 0; j < 10; ++j) c[j] = col(P, j);
  for (int j = 0; j < 10; ++j) {
    if (c[j] == 0) return false;
    const u32 low = c[j] & (0u - c[j]);
    for (int k = j + 1; k < 10; ++k) if (c[k] & low) c[k] ^= c[j];
  }
  return true;
}
static_assert(one_to_one(PRM0), "every KB of the window exactly once");
struct Tab { u32 e[512]; };
constexpr Tab make_tab(const Prm& P) {
  Tab T{};
  for (int bid = 0; bid < 256; ++bid) {
    const int xcd = bid & 7, part = bid >> 4 & 3, i8 = (bid >> 3 & 1) | bid >> 6 << 1, wi = i8 * 8 + xcd;
    const bool has_mod = wi < 48, has_up = wi >= 21;
    const int cb0 = (i8 + P.C0) & 7, cb1 = (i8 + 4) & 7, ms = wi - 20;
    const int mi = wi < 8 ? wi : (wi < 16 ? 8 + ((xcd + 4) & 7) : (wi < 20 ? (wi - 14) * 8 + 3 : 16 + ms / 7 * 8 + ms % 7 + (ms % 7 >= 3 ? 1 : 0)));
    const int ui = xcd * 5 + (xcd > 5 ? xcd - 5 : 0) + (i8 >= 3 ? (xcd >= 5 ? 1 : 0) + i8 - 3 : 0);
    const int wd = cb0 == 3 ? 0 : P.WD;
    const int e[4] = {P.E0 - 4 * wd + (cb0 == 3 ? P.ES : 0), P.E1 + (cb1 == 3 ? P.ES : 0), has_mod ? (wi < 16 ? P.E2C : P.E2) + ((mi & 7) == 3 ? P.ES : 0) : 0, has_up ? P.EU : 0};
    int tw[4] = {}, win[4] = {}, span[4] = {};
    for (int k = 0; k < 4; ++k) {
      const int np = k == 0 ? 8 - wd : 8;
      tw[k] = np ? e[k] * P.TW / (16 * np) : 0;
      span[k] = P.TEND - (P.NF + tw[k] * np / 2 > P.TK ? P.NF + tw[k] * np / 2 : P.TK) - P.TG - (k == 0 ? (wd ? P.DT_WO : P.A0) : (k == 3 && has_up ? P.DT_UP : 0));
      win[k] = span[k] - (k == 0 && wd ? P.EW : (k == 3 && has_up ? P.GA : 0));
    }
    int n[4] = {NCMIN / 8, NCMIN / 8, NCMIN / 8, NCMIN / 8};
    for (int left = 128 - NCMIN / 2; left > 0; --left) {
      int best = -1;
      for (int k = 3; k >= 0; --k)
        if (n[k] < NL && (best < 0 || n[k] * win[best] < n[best] * win[k])) best = k;
      ++n[best];
    }
    const int c[4] = {0, n[0] - 1, n[0] + n[1] - 2, n[0] + n[1] + n[2] - 3};
    const int nl = n[part];
    const bool has_w = part < 2 || (part == 2 ? has_mod : has_up);
    const int blk = !has_w ? 0 : (part == 0 ? cb0 : (part == 1 ? xcd * 8 + cb1 : (part == 2 ? mi : ui)));
    T.e[2 * bid] = (u32)(c[part] | nl << 7 | blk << 13 | (has_w ? 1 : 0) << 19 | (part == 0 ? wd : 0) << 20) | (u32)tw[part] << 24;
    T.e[2 * bid + 1] = (u32)(span[part] * P.TS / (4 * (nl - 1)) | span[part] * (1024 - P.TS) / (4 * (nl - 1)) << 16);
  }
  return T;
}
constexpr int step_range(const Prm& P, bool mx_) {
  int mx = 0, mn = 1 << 20;
  const Tab T = make_tab(P);
  for (int bid = 0; bid < 256; ++bid) { const int n = T.e[2 * bid] >> 7 & 63; mx = n > mx ? n : mx; mn = n < mn ? n : mn; }
  return mx_ ? mx : mn;
}
extern "C" __attribute__((used, visibility("protected"))) __constant__ const Tab wg_tab = make_tab(PRM0);
static_assert(step_range(PRM0, true) <= NL && step_range(PRM0, false) >= NCMIN / 8, "a wave's key range does not fit its load steps");
static_assert(UPREP + 1 < SK0 && QP + QD + 1 < SK0 && cons_end(SK0 - 1) == SK0 - WV && NL - SK1 == WV, "steps every wave runs");
constexpr int NUP = FF / 32;
constexpr int SPIN_MAX = 1 << 17;

constexpr u32 WS_EPOCH = 0;
constexpr u32 G_TB = 1024;
constexpr u32 G_MOD = 4096;
constexpr u32 G_Q = G_MOD + 3072 * 8;
constexpr u32 G_ML = G_Q + 4096 * 8;
constexpr u32 G_U = G_ML + 256 * 16;
constexpr u32 G_P = 0;
constexpr u32 G_XP = G_P + 256 * 256;
constexpr u32 G_OP = G_XP + 8 * 512 * 4;
constexpr u32 G_NT = G_OP + 43 * 512 * 8;
constexpr u32 G_PB = G_OP + 44 * 512 * 8;
constexpr u32 G_VT = G_U + 2 * (G_PB + 8192);
constexpr u32 WS_END = G_VT + 1024;
__device__ __forceinline__ u32 dup(u32 o) { return G_U + ((o >> 13) << 14) + (o & 8191u); }
constexpr u32 SENT = 0xffffffffu;
constexpr u32 OOB = 0x40000000u;
#define WST 0

constexpr int AUX_NT = 2, AUX_SC1 = 16;

__device__ __forceinline__ float lo_f(u32 v) { return __uint_as_float(v << 16); }
__device__ __forceinline__ float hi_f(u32 v) { return __uint_as_float(v & 0xffff0000u); }
__device__ __forceinline__ float rbf(float x) { return (float)(__bf16)x; }
__device__ __forceinline__ u32 pack_bf(float lo, float hi) {
  bf16x2 r; r[0] = (__bf16)lo; r[1] = (__bf16)hi; return __builtin_bit_cast(u32, r);
}
__device__ __forceinline__ float dot2(u32 k, u32 q, float acc) {
  return __builtin_amdgcn_fdot2_f32_bf16(__builtin_bit_cast(bf16x2, k), __builtin_bit_cast(bf16x2, q), acc, false);
}
template <int CTRL> __device__ __forceinline__ float dpp(float x) {
  return __builtin_bit_cast(float, __builtin_amdgcn_mov_dpp(__builtin_bit_cast(int, x), CTRL, 0xf, 0xf, true));
}
constexpr int XOR1 = 0xB1, XOR2 = 0x4E, XOR7 = 0x141, XOR8 = 0x128;
__device__ __forceinline__ float swap32_add(float a, float b) {
  auto t = __builtin_amdgcn_permlane32_swap(__float_as_uint(a), __float_as_uint(b), false, false);
  return __uint_as_float(t[0]) + __uint_as_float(t[1]);
}
__device__ __forceinline__ float swap16_add(float a, float b) {
  auto t = __builtin_amdgcn_permlane16_swap(__float_as_uint(a), __float_as_uint(b), false, false);
  return __uint_as_float(t[0]) + __uint_as_float(t[1]);
}
__device__ __forceinline__ float wave_sum(float x) {
  x += dpp<XOR1>(x); x += dpp<XOR2>(x); x += dpp<XOR7>(x); x += dpp<XOR8>(x);
  x = swap16_add(x, x);
  return swap32_add(x, x);
}
__device__ __forceinline__ float wave_max(float x) {
  x = fmaxf(x, dpp<XOR1>(x)); x = fmaxf(x, dpp<XOR2>(x)); x = fmaxf(x, dpp<XOR7>(x)); x = fmaxf(x, dpp<XOR8>(x));
  auto s = __builtin_amdgcn_permlane16_swap(__float_as_uint(x), __float_as_uint(x), false, false);
  x = fmaxf(__uint_as_float(s[0]), __uint_as_float(s[1]));
  auto t = __builtin_amdgcn_permlane32_swap(__float_as_uint(x), __float_as_uint(x), false, false);
  return fmaxf(__uint_as_float(t[0]), __uint_as_float(t[1]));
}
template <int CTRL> __device__ __forceinline__ float sel_add(float lo, float hi, bool up) {
  const float t1 = lo + dpp<CTRL>(lo), t2 = hi + dpp<CTRL>(hi);
  return up ? t2 : t1;
}
template <int I> __device__ __forceinline__ float bc16(float x) {
  return __builtin_bit_cast(float, __builtin_amdgcn_update_dpp(0, __builtin_bit_cast(int, x), 0x150 + I, 0xf, 0xf, false));
}
__device__ __forceinline__ float bcast16(float x, int i) {
  switch (i) {
    case 0: return bc16<0>(x); case 1: return bc16<1>(x); case 2: return bc16<2>(x); case 3: return bc16<3>(x);
    case 4: return bc16<4>(x); case 5: return bc16<5>(x); case 6: return bc16<6>(x); case 7: return bc16<7>(x);
    case 8: return bc16<8>(x); case 9: return bc16<9>(x); case 10: return bc16<10>(x); case 11: return bc16<11>(x);
    case 12: return bc16<12>(x); case 13: return bc16<13>(x); case 14: return bc16<14>(x); default: return bc16<15>(x);
  }
}
__device__ __forceinline__ float silu_f(float x) { return x * __builtin_amdgcn_rcpf(1.f + __expf(-x)); }

template <int AUX> __device__ __forceinline__ u32x4 ld16(rsrc_t r, u32 voff, u32 soff) {
  return __builtin_amdgcn_raw_buffer_load_b128(r, voff, soff, AUX);
}
template <int AUX> __device__ __forceinline__ u32x2 ld8(rsrc_t r, u32 voff, u32 soff) {
  return __builtin_amdgcn_raw_buffer_load_b64(r, voff, soff, AUX);
}
__device__ __forceinline__ void st8_sc1(rsrc_t r, u32 voff, u32 v, u32 tag) {
  __builtin_amdgcn_raw_buffer_store_b64(u32x2{v, tag}, r, voff, 0, AUX_SC1);
}
__device__ __forceinline__ void st16_sc1(rsrc_t r, u32 voff, u32 v0, u32 v1, u32 tag) {
  __builtin_amdgcn_raw_buffer_store_b128(u32x4{v0, tag, v1, tag}, r, voff, 0, AUX_SC1);
}
__device__ __forceinline__ rsrc_t mk_rsrc(const void* p, u32 bytes) {
  return __builtin_amdgcn_make_buffer_rsrc(const_cast<void*>(p), 0, bytes, 0x00020000);
}
#define WAIT_VM(n) __builtin_amdgcn_s_waitcnt(((n) & 15) | (((n) >> 4) << 14) | (7 << 4) | (15 << 8))
#define SB() __builtin_amdgcn_sched_barrier(0)
#define CFENCE() asm volatile("" ::: "memory")
__device__ __forceinline__ bool all_ok(bool ok) { return __builtin_amdgcn_ballot_w64(!ok) == 0ull; }

template <int N> __device__ __forceinline__ void gather16(rsrc_t ws, u32 voff, u32 stride, u32 tag, u32x4 (&g)[N]) {
#pragma nounroll
  for (int spin = 0; spin < SPIN_MAX; ++spin) {
    CFENCE();
    bool ok = true;
#pragma unroll
    for (int i = 0; i < N; ++i) g[i] = ld16<AUX_SC1>(ws, voff + i * stride, 0);
#pragma unroll
    for (int i = 0; i < N; ++i) ok &= (g[i][1] == tag) & (g[i][3] == tag);
    if (all_ok(ok)) break;
    __builtin_amdgcn_s_sleep(8);
  }
}
template <int N> __device__ __forceinline__ void gather8(rsrc_t ws, u32 voff, u32 stride, u32 tag, u32 (&g)[N], bool act = true) {
#pragma nounroll
  for (int spin = 0; spin < SPIN_MAX; ++spin) {
    CFENCE();
    bool ok = true;
    u32x2 t[N];
#pragma unroll
    for (int i = 0; i < N; ++i) t[i] = ld8<AUX_SC1>(ws, voff + i * stride, 0);
#pragma unroll
    for (int i = 0; i < N; ++i) { ok &= (t[i][1] == tag); g[i] = t[i][0]; }
    if (all_ok(ok | !act)) break;
    __builtin_amdgcn_s_sleep(8);
  }
}

struct Smem {
  float red[8][128];
  float ml[8][2];
  float r2[64 * 20];
  u16 hb[3 * 1024];
  float stat[8][64];
  float red2[8][128];
  u32 dummy[64];
  u32x2 bc;
  u32x4 zero[4];
  float pl[4][8];
  u32 mflag[8];
  u32x4 wbuf[8][8][64];
  __attribute__((aligned(256))) u16 w2b[32][512];
  u16 actl[2][32];
};

#define PROBE_K 8
#define PROBE_OP 0
#define PROBE_PART -1
#define STAMP(k) do { if ((k) == PROBE_K && tid == 0) pst = wall_clock64(); } while (0)
#define WSTAMP(k) do {} while (0)
#define FINISH() do { STAMP(8); if (tid == 0) { __hip_atomic_store(reinterpret_cast<u64*>(ws + (1u << 20)) + bid * 2, pt0, __ATOMIC_RELAXED, __HIP_MEMORY_SCOPE_AGENT); \
   __hip_atomic_store(reinterpret_cast<u64*>(ws + (1u << 20)) + bid * 2 + 1, has_w ? 0ull : ((u64)(lg[0] / 3840) | (u64)(lg[1] / 3840) << 16 | (u64)(lg[2] / 3840) << 32 | (u64)(lg[3] / 3840) << 48), __ATOMIC_RELAXED, __HIP_MEMORY_SCOPE_AGENT); } \
   if (bid == 8 && wave == 0) probe_end(ws, pt0, lane); return; } while (0)
#define PIN(x) asm volatile("" : "+v"(x))
#define LDS_BARRIER() do { __builtin_amdgcn_s_waitcnt(0xc07f); __builtin_amdgcn_s_barrier(); CFENCE(); } while (0)
#define WAVE_SYNC() do { __builtin_amdgcn_fence(__ATOMIC_RELEASE, "wavefront"); __builtin_amdgcn_wave_barrier(); __builtin_amdgcn_fence(__ATOMIC_ACQUIRE, "wavefront"); } while (0)

__device__ __forceinline__ void dma16(rsrc_t r, u32 voff, u32 soff, u32 lds_dst) {
  u32 keep;
  asm volatile("s_mov_b32 %0, m0\n\ts_mov_b32 m0, %4\n\ts_nop 4\n\tbuffer_load_dwordx4 %1, %2, %3 offen nt lds\n\ts_mov_b32 m0, %0"
               : "=&s"(keep) : "v"(voff), "s"(r), "s"(soff), "s"(lds_dst) : "memory");
}
__device__ __forceinline__ void ld16_into(u32x4& d, rsrc_t r, u32 voff, u32 soff) {
  asm volatile("s_nop 4\n\tbuffer_load_dwordx4 %0, %1, %2, %3 offen nt" : "+v"(d) : "v"(voff), "s"(r), "s"(soff) : "memory");
}
__device__ __forceinline__ u32 perm_lo(u32 a, u32 b) { return __builtin_amdgcn_perm(b, a, 0x05040100u); }
__device__ __forceinline__ u32 perm_hi(u32 a, u32 b) { return __builtin_amdgcn_perm(b, a, 0x07060302u); }
__device__ __forceinline__ float rdl(float x, int l) { return __int_as_float(__builtin_amdgcn_readlane(__float_as_int(x), l)); }

typedef __attribute__((ext_vector_type(8))) __bf16 bf16x8;
__device__ __forceinline__ f32x4 mfma16(u32x4 a, u32x4 b, f32x4 c) {
  return __builtin_amdgcn_mfma_f32_16x16x32_bf16(__builtin_bit_cast(bf16x8, a), __builtin_bit_cast(bf16x8, b), c, 0, 0, 0);
}
__device__ __forceinline__ float sum8(const float* p) {
  const f32x4 a = *reinterpret_cast<const f32x4*>(p), b = *reinterpret_cast<const f32x4*>(p + 4);
  return ((a[0] + b[0]) + (a[1] + b[1])) + ((a[2] + b[2]) + (a[3] + b[3]));
}
__device__ __forceinline__ float ln_ri(float var) {
  return __builtin_amdgcn_rcpf(rbf(__builtin_amdgcn_sqrtf(rbf(rbf(fmaxf(var, 0.f)) + 1e-5f))));
}
__device__ __forceinline__ void ln_stats2(const float (&x0)[8], const float (&x1)[8], float& mb0, float& ri0, float& mb1, float& ri1) {
  const float k0 = rdl(x0[0], 0), k1 = rdl(x1[0], 0);
  float s0 = 0.f, s1 = 0.f, q0 = 0.f, q1 = 0.f;
#pragma unroll
  for (int k = 0; k < 8; ++k) {
    const float d0 = x0[k] - k0, d1 = x1[k] - k1;
    s0 += d0; s1 += d1; q0 = fmaf(d0, d0, q0); q1 = fmaf(d1, d1, q1);
  }
  float t = swap16_add(swap32_add(s0, s1), swap32_add(q0, q1));
  t += dpp<XOR1>(t); t += dpp<XOR2>(t); t += dpp<XOR7>(t); t += dpp<XOR8>(t);
  auto u = __builtin_amdgcn_permlane16_swap(__float_as_uint(t), __float_as_uint(t), false, false);
  const float ms = __uint_as_float(u[0]) * (1.f / 512.f), mq = __uint_as_float(u[1]) * (1.f / 512.f);
  const float ri = ln_ri(fmaf(-ms, ms, mq));
  mb0 = rbf(k0 + rdl(ms, 0)); mb1 = rbf(k1 + rdl(ms, 32));
  ri0 = rdl(ri, 0); ri1 = rdl(ri, 32);
}
template <int I> struct IC { static constexpr int v = I; };
template <int I, int N, class F> __device__ __forceinline__ void sfor(F&& f) {
  if constexpr (I < N) { if (f(IC<I>{})) sfor<I + 1, N>(f); }
}
typedef __attribute__((ext_vector_type(4))) short s16x4;
typedef __attribute__((address_space(3))) s16x4* lds_s16x4p;
__device__ __forceinline__ u32x2 tr_read(u32 lds_addr) {
  return __builtin_bit_cast(u32x2, __builtin_amdgcn_ds_read_tr16_b64_v4i16((lds_s16x4p)lds_addr));
}
__device__ __forceinline__ u32 next_tag(u32 e) { return ((e | 0x40004000u) + 1u) & ~0x40004000u; }
__device__ __forceinline__ float u2f(u32 v) { return __uint_as_float(v); }
#define LDSP(T, a) ((__attribute__((address_space(3))) T*)(a))
template <class T> __device__ __forceinline__ u32 lds_addr(T* p) { u32 a = (u32)(uintptr_t)p; PIN(a); return a; }

#define TAB_LOAD "s_add_u32 s30, s28, wg_tab@rel32@lo+4\ns_addc_u32 s31, s29, wg_tab@rel32@hi+12\nglobal_load_dwordx2 %[te], %[va], s[30:31]\n"
#define FC(k) ".Lfc" #k
#define FC_REGS
#define FX(r, bit, k) "s_bitcmp1_b32 %[" #r "], " #bit "\ns_cselect_b32 %[sb], " FC(k) ", 0\ns_xor_b32 %[sa], %[sa], %[sb]\n"
__device__ __forceinline__ void probe_end(unsigned char* ws, u64 pt0, int lane) {
  while (wall_clock64() < pt0 + 5500ull) __builtin_amdgcn_s_sleep(8);
  u64 a[4], c2[4];
  for (int i = 0; i < 4; ++i) {
    a[i] = __hip_atomic_load(reinterpret_cast<u64*>(ws + (1u << 20)) + (i * 64 + lane) * 2, __ATOMIC_RELAXED, __HIP_MEMORY_SCOPE_AGENT);
    c2[i] = __hip_atomic_load(reinterpret_cast<u64*>(ws + (1u << 20)) + (i * 64 + lane) * 2 + 1, __ATOMIC_RELAXED, __HIP_MEMORY_SCOPE_AGENT);
  }
  int mn0 = 1 << 30, mx1 = -(1 << 30), mn1 = 1 << 30, sum = 0, cnt = 0;
  for (int i = 0; i < 4; ++i) {
    const int d0 = (int)(a[i] - pt0);
    mn0 = min(mn0, d0);
    if (c2[i] != 0ull) { const int d1 = (int)(c2[i] - pt0); mx1 = max(mx1, d1); mn1 = min(mn1, d1); sum += d1; cnt += 1; }
  }
  for (int o = 1; o < 64; o <<= 1) { mn0 = min(mn0, __shfl_xor(mn0, o)); mx1 = max(mx1, __shfl_xor(mx1, o)); mn1 = min(mn1, __shfl_xor(mn1, o)); sum += __shfl_xor(sum, o); cnt += __shfl_xor(cnt, o); }
  int X = 0;
  for (int k = 0; k < 4; ++k) { int s = 0; for (int i = 0; i < 4; ++i) s += (int)(c2[i] >> (16 * k) & 0xffff); for (int o = 1; o < 64; o <<= 1) s += __shfl_xor(s, o); X += min(max(k < 2 ? s / 370 : (s / 37 - 40) / 12, 0), 7) * 100 << (3 * k); }
  const u64 tg = pt0 + (u64)(long long)(X + 8000);
  for (int i = 0; i < 4000000 && wall_clock64() < tg; ++i) __builtin_amdgcn_s_sleep(1);
}
__global__ void __launch_bounds__(512, 2) fused_kernel(const u16* kc_, const u16* vc_, const u16* wo_, const u16* wq_, const u16* wmod_, const u16* w1_, const u16* w3_,
                                                       unsigned char* ws_, const u16* x_, const u16* cnd_, const u16* w2_, u16* out_) {
  __shared__ __attribute__((aligned(256))) Smem sm;
  const u64 a_ka = (u64)__builtin_amdgcn_kernarg_segment_ptr();
  const int bid = blockIdx.x, tid = threadIdx.x;
  u64 wa, t64; u32x2 te; int tid0;
  u32 ar[14];
  u32 tch2, sa_, sb_, va_;
  u32x4 kr[NL], vr[NL];
  asm volatile(".set .Lfc0, %0\n.set .Lfc1, %1\n.set .Lfc2, %2\n.set .Lfc3, %3\n.set .Lfc4, %4\n.set .Lfc5, %5\n.set .Lfc6, %6\n.set .Lfc7, %7\n.set .Lfc8, %8\n.set .Lfc9, %9\n.set .Lfc10, %10\n.set .Lfc11, %11"
    :: "n"(first_col(PRM0, 0)), "n"(first_col(PRM0, 1)), "n"(first_col(PRM0, 2)), "n"(first_col(PRM0, 3)), "n"(first_col(PRM0, 4)), "n"(first_col(PRM0, 5)),
       "n"(first_col(PRM0, 6)), "n"(first_col(PRM0, 7)), "n"(first_col(PRM0, 8)), "n"(first_col(PRM0, 9)), "n"(first_col(PRM0, 10)), "n"(first_col(PRM0, 11)));
  asm volatile(
    "s_load_dwordx16 s[4:19], %[ka], 0x0\ns_load_dwordx8 s[20:27], %[ka], 0x40\n"
    "s_memrealtime %[t]\n"
    "v_lshlrev_b32 %[va], 3, %[bid]\n"
    "s_getpc_b64 s[28:29]\n.Lcpf_first:\n"
    TAB_LOAD
    "s_cmp_gt_u32 %[bid], 15\ns_cbranch_scc1 .Lno_cpf\n"
    "s_mov_b32 %[sa], .Lend_a-.Lcpf_first\ns_max_i32 %[sa], %[sa], .Lend_b-.Lcpf_first\ns_max_i32 %[sa], %[sa], .Lend_c-.Lcpf_first\ns_max_i32 %[sa], %[sa], .Lend_d-.Lcpf_first\ns_max_i32 %[sa], %[sa], .Lend_e-.Lcpf_first\n"
    "v_lshlrev_b32 %[va], 6, %[tid]\nv_min_u32 %[va], %[sa], %[va]\nglobal_load_dword %[pf], %[va], s[28:29]\n"
    ".Lno_cpf:\n"
    "v_readfirstlane_b32 %[t0], %[tid]\nv_and_b32 %[va], 63, %[tid]\n"
    "s_mov_b32 %[sa], " FC(11) "\n"
    FX(bid, 0, 0) FX(bid, 1, 1) FX(bid, 2, 2) FX(bid, 3, 3) FX(bid, 4, 4) FX(bid, 5, 5) FX(bid, 6, 6) FX(bid, 7, 7) FX(t0, 6, 8) FX(t0, 7, 9) FX(t0, 8, 10)
    "v_lshl_add_u32 %[va], %[va], 4, %[sa]\n"
    "s_waitcnt lgkmcnt(0)\nglobal_load_dwordx4 %[k0], %[va], s[4:5] nt\nglobal_load_dwordx4 %[v0], %[va], s[6:7] nt\n"
    "s_bitcmp1_b32 %[t0], 6\ns_cselect_b64 s[14:15], s[16:17], s[14:15]\n"
    "s_bitcmp1_b32 %[bid], 4\ns_cselect_b64 s[8:9], s[10:11], s[8:9]\ns_cselect_b64 s[12:13], s[14:15], s[12:13]\ns_bitcmp1_b32 %[bid], 5\ns_cselect_b64 %[wa], s[12:13], s[8:9]"
    : "=&{s4}"(ar[0]), "=&{s5}"(ar[1]), "=&{s6}"(ar[2]), "=&{s7}"(ar[3]), "=&{s18}"(ar[4]), "=&{s19}"(ar[5]), "=&{s20}"(ar[6]), "=&{s21}"(ar[7]), "=&{s22}"(ar[8]), "=&{s23}"(ar[9]),
      "=&{s24}"(ar[10]), "=&{s25}"(ar[11]), "=&{s26}"(ar[12]), "=&{s27}"(ar[13]), [wa] "=&s"(wa), [t0] "=&s"(tid0), [t] "=&s"(t64), [te] "=&v"(te), [sa] "=&s"(sa_), [sb] "=&s"(sb_),
      [va] "=&v"(va_), [pf] "=&v"(tch2), [k0] "=&v"(kr[0]), [v0] "=&v"(vr[0])
    : [ka] "s"(a_ka), [bid] "s"(bid), [tid] "v"(tid) : "s8", "s9", "s10", "s11", "s12", "s13", "s14", "s15", "s16", "s17", "s28", "s29", "s30", "s31", "scc", "memory" FC_REGS);
  asm volatile("" : "+s"(ar[0]), "+s"(ar[1]), "+s"(ar[2]), "+s"(ar[3]), "+s"(ar[4]), "+s"(ar[5]), "+s"(ar[6]), "+s"(ar[7]), "+s"(ar[8]), "+s"(ar[9]), "+s"(ar[10]), "+s"(ar[11]), "+s"(ar[12]), "+s"(ar[13]));
  asm volatile("" : "+s"(wa), "+s"(t64), "+s"(tid0), "+s"(sa_));
  const u32 tka = (u32)wall_clock64();
  const u64 pt0 = t64; u64 pst = (PROBE_K == 100 || PROBE_K == 102) ? pt0 : (PROBE_K == 101 ? pt0 + (u32)(tka - (u32)t64) : 0);
  const u32 t0 = tka;
  __builtin_assume(tid >= 0 && tid < 512 && tid0 >= 0 && tid0 < 512 && bid >= 0 && bid < 256);
  constexpr Prm P = PRM0;
  const int TEND = P.TEND;
  const int xcd = bid & 7, part = bid >> 4 & 3;
  const int lane = tid & 63, wave = tid0 >> 6;
  SB();
  if ((bid >> 4) == 3 && P.TT) {
    const int j = min((bid & 8) + wave, 9);
    const u64 p = ((__attribute__((address_space(4))) const u64*)a_ka)[2 + j];
    tch2 = __builtin_amdgcn_raw_buffer_load_b32(mk_rsrc((const void*)p, (j < 2 ? 4u << 20 : (j == 2 ? 3u << 20 : ((0x118 >> j & 1) ? FF * 1024u : 2048u)))), lane << 16, 512, AUX_NT);
  }
  const bool p3 = part == 3;
  u32 pitch = (u32)(0x005600c001000020ull >> (part * 16) & 0xffff) * 32u;
  const int sh = p3 ? 2 : 3;
  u32 wv0 = (u32)((p3 ? (wave >> 1) * 128 : wave * 64) + (part == 0 ? xcd * 512 : 0) + (lane >> sh)) * pitch + ((lane & (p3 ? 3 : 7)) ^ (lane >> (p3 ? 5 : 3))) * 16u;
  const u32 wimg = (u32)(uintptr_t)(&sm.wbuf[0][0][0]);
  u32 wsz = (part == 0 ? 4096u : 512u) * pitch, wld = wimg + (p3 ? (wave & 1) * 32768 + (wave >> 1) * 8192 : wave * 8192);
  PIN(wv0);
  asm volatile("" : "+s"(wsz), "+s"(wld), "+s"(pitch));
  auto karg = [&](int i) { return ar[2 * i] | (u64)ar[2 * i + 1] << 32; };
  const u64 a_kc = karg(0), a_vc = karg(1);
  unsigned char* ws = (unsigned char*)karg(2);
  const u16 *x = (const u16*)karg(3), *cnd = (const u16*)karg(4), *w2 = (const u16*)karg(5);
  u16* out = (u16*)karg(6);
  const rsrc_t wsr = mk_rsrc(ws, WS_END);
  asm volatile("s_waitcnt vmcnt(3)\n\ts_cmp_eq_u32 %1, 3\n\ts_cbranch_scc1 1f\n\ts_waitcnt vmcnt(2)\n1:" : "+v"(te) : "s"(P.TT ? bid >> 4 : 0) : "scc");
  const u32 te0 = __builtin_amdgcn_readfirstlane(te[0]), te1 = __builtin_amdgcn_readfirstlane(te[1]);
  const int bl0 = te0 & 127, nl = te0 >> 7 & 63, blk = te0 >> 13 & 63;
  const bool has_w = te0 >> 19 & 1;
  const rsrc_t rw = mk_rsrc((const void*)wa, wsz);
  const u32 wstep = pitch << (6 - sh), wflip = p3 ? 32u : 0u;
  u32 wso = blk << (sh + 4);
  int wleft = 8;
  auto piece = [&]() {
    dma16(rw, wv0, wso, wld);
    wso += wstep; wld += 1024;
    wv0 ^= (wleft & 1) ? wflip : 0u;
    --wleft;
  };
  SB();
  STAMP(23);
  const int b = bid >> 3 & 1, head = xcd * 4 + (bid >> 6 ^ 3);
  const int g = lane >> 4, c = lane & 15;
  const int i8 = b | bid >> 6 << 1, wi = i8 * 8 + xcd;
  const bool is_up = p3 && has_w, modc = part == 2 && wi < 16;
  const size_t base = ((size_t)(b * NH + head) * SK + (SK - WIN)) * DH;
  const rsrc_t rk = mk_rsrc((const u16*)a_kc + base, WIN * DH * 2), rv = mk_rsrc((const u16*)a_vc + base, WIN * DH * 2);
  int tau = te1 & 0xffff;
  const int tauw = te0 >> 24 << 7, wdef = te0 >> 20 & 15;
  int due = P.NF * 256, now = 0;
  auto gate = [&]() {
    while (now < due) { now = (int)(((u32)wall_clock64() - t0) << 8); if (now < due) __builtin_amdgcn_s_sleep(1); }
    due += tau;
  };
  auto until = [&](int t) { while ((int)(((u32)wall_clock64() - t0) << 8) < t); };
  const u32 pslot = dup(G_P + head * 2048 + (b * 4 + part) * 16);
  const u32 die = __builtin_amdgcn_s_getreg(20 | (2 << 6));
  const bool voter = p3 && i8 == 1, reporter = voter, timer = p3 && i8 < 2;
  int vote = 0, lag = 0;
  const u32 pbo = dup(G_PB) + (xcd * 8 + wave) * 128;
  u32 near = 0;
  u32x2 ep; u32 xs0, xs1, tch1; u32x4 xr0, xr1;
  const rsrc_t rx = mk_rsrc(part == 2 ? cnd : x, (part == 1 || (part == 2 && has_w)) ? 2048u : 0u);
  auto small_ops = [&]() {
    ep = ld8<AUX_SC1>(wsr, wave == 7 ? WS_EPOCH : OOB, 0);
    xs0 = __builtin_amdgcn_raw_buffer_load_b16(rx, tid * 2, 0, 0); xs1 = __builtin_amdgcn_raw_buffer_load_b16(rx, tid * 2, 1024, 0);
    xr0 = ld16<0>(rx, part == 1 ? lane * 16 : OOB, 0); xr1 = ld16<0>(rx, part == 1 ? lane * 16 : OOB, 1024);
    tch1 = __builtin_amdgcn_raw_buffer_load_b32(mk_rsrc(w2 + (size_t)blk * 32 * 512, is_up ? 64u : 0u), tid == 384 ? 0 : OOB, 0, AUX_NT);
  };
  if (has_w) {
    if (modc) now = 1 << 30;
#pragma nounroll
    while (wleft > wdef) { gate(); due += tauw - tau; piece(); }
    now = 0;
  }
  due = max(due, P.TK * 256);
  SB();
  u32 sot = sa_ & 0xffc00u;
  {
    const u32 s1 = (bl0 + lane - 1 - (lane >= SK0 ? NL - nl : 0)) ^ (124 + part);
#pragma unroll
    for (int k = 0; k < 7; ++k) sot ^= (u32)-(int)(s1 >> k & 1u) & col(P, k);
    PIN(sot);
  }
  u32 tag = 0;
  u32x4 bf[8];
  auto wo_prep = [&](int s0 = 0, int s1 = 2) {
    const u32 wi_ = wimg + wave * 8192;
    const int q = (lane >> 2) & 3, pp = lane & 3;
#pragma unroll
    for (int s = s0; s < s1; ++s) {
#pragma unroll
      for (int t = 0; t < 4; ++t) {
        const int r0 = s * 32 + g * 8 + q, r1 = r0 + 4, ch = t * 2 + (pp >> 1);
        const u32x2 lo = tr_read(wi_ + r0 * 128 + ((ch ^ (r0 & 7)) * 16) + (pp & 1) * 8), hi = tr_read(wi_ + r1 * 128 + ((ch ^ (r1 & 7)) * 16) + (pp & 1) * 8);
        bf[s * 4 + t] = u32x4{lo[0], lo[1], hi[0], hi[1]};
      }
    }
  };
  auto gemv_go = [&](u32 hh) {
    sm.hb[tid] = (u16)hh; sm.hb[512 + tid] = (u16)(hh >> 16);
    WAVE_SYNC();
    const u32x4* a = (c & 11) == 0 ? reinterpret_cast<const u32x4*>(&sm.hb[(c >> 2) * 512 + wave * 64 + g * 8]) : &sm.zero[0];
    const u32x4 a0 = a[0], a1 = a[(c & 11) == 0 ? 4 : 0];
    const f32x4 z = {0.f, 0.f, 0.f, 0.f};
    f32x4 d[4];
#pragma unroll
    for (int t = 0; t < 4; ++t) d[t] = mfma16(a0, bf[t], z);
#pragma unroll
    for (int t = 0; t < 4; ++t) d[t] = mfma16(a1, bf[4 + t], d[t]);
    if (lane < 32) {
#pragma unroll
      for (int t = 0; t < 4; ++t) sm.r2[(t * 16 + c) * 20 + g * 8 + wave] = d[t][0];
    }
  };
  auto mod_stage = [&]() {
    wo_prep();
    gemv_go(pack_bf(silu_f(lo_f(xs0)), silu_f(lo_f(xs1))));
    __builtin_amdgcn_s_waitcnt(0xc07f);
    if (lane == 0) *LDSP(volatile u32, (u32)(uintptr_t)&sm.mflag[wave]) = 1u;
    if (wave == 7) {
#pragma nounroll
      for (int spin = 0; spin < SPIN_MAX; ++spin) {
        const u32 f = *LDSP(volatile u32, (u32)(uintptr_t)&sm.mflag[lane & 7]);
        if (all_ok(f != 0u)) break;
        __builtin_amdgcn_s_sleep(1);
      }
      CFENCE();
      st8_sc1(wsr, G_MOD + (blk * 64 + lane) * 8, pack_bf(sum8(&sm.r2[lane * 20]), sum8(&sm.r2[lane * 20 + 8])), tag);
    }
    STAMP(9);
  };
  if (lane < 4) sm.zero[lane] = u32x4{0u, 0u, 0u, 0u};
  if (part == 2 && has_w) {
    if (tid < 8) sm.mflag[tid] = 0u;
    LDS_BARRIER();
    if (__builtin_expect(modc, 0)) {
      small_ops();
      __builtin_amdgcn_raw_buffer_store_b128(u32x4{SENT, SENT, SENT, SENT}, wsr, G_U + wi * 10240 + tid * 16, 0, AUX_SC1);
      __builtin_amdgcn_raw_buffer_store_b128(u32x4{SENT, SENT, SENT, SENT}, wsr, tid < 128 ? G_U + wi * 10240 + tid * 16 : OOB, 8192, AUX_SC1);
      asm volatile("s_waitcnt vmcnt(0)" ::: "memory");
      tag = next_tag(__builtin_amdgcn_readfirstlane(ep[0]));
      mod_stage();
      __builtin_amdgcn_s_barrier();
    }
  }
  SB();
  const int fd = wi * 8 + (lane & 3) * 2;
  const int ub = wave >> 2, ud = (wave & 3) * 128 + c * 8 + g * 2;
  u32x4 sg[3]; u32 sx2;
  auto side_loads = [&]() {
    const bool fin = part == 1 && wave == 0;
    sg[0] = ld16<AUX_SC1>(wsr, is_up ? G_MOD + (1024 + ud) * 8 : (fin ? G_MOD + (1024 + fd) * 8 : OOB), 0);
    sg[1] = ld16<AUX_SC1>(wsr, is_up ? G_MOD + (1536 + ud) * 8 : (fin ? G_MOD + (2560 + fd) * 8 : OOB), 0);
    sg[2] = ld16<AUX_SC1>(wsr, is_up ? G_MOD + (2048 + ud) * 8 : OOB, 0);
    sx2 = __builtin_amdgcn_raw_buffer_load_b32(mk_rsrc(x, (is_up || fin) ? 2048u : 0u), is_up ? ub * 1024 + ud * 2 : (g & 1) * 1024 + fd * 2, 0, 0);
  };
  auto side_pin = [&]() { PIN(sg[0]); PIN(sg[1]); PIN(sg[2]); PIN(sx2); };
  const u32 mlo = G_ML + (head * 2 + b) * 64 + (lane & 3) * 16;
  float fmrg = 0.f;
  auto merge_factor = [&](const u32x4& ml) {
    const float mi = u2f(ml[0]);
    float M = fmaxf(mi, dpp<XOR1>(mi)); M = fmaxf(M, dpp<XOR2>(M));
    const float e = __expf(mi - M);
    float L = e * u2f(ml[2]);
    L += dpp<XOR1>(L); L += dpp<XOR2>(L);
    fmrg = rdl(e * __builtin_amdgcn_rcpf(L), part);
  };
  f32x2 ga = {0.f, 0.f}, xin = {0.f, 0.f}, cc1 = {0.f, 0.f};
  float Af = 0.f, Bf = 0.f;
  const int hpos = ub * 512 + ud;
  auto up_prep = [&]() {
    if (!all_ok((sg[0][1] == tag) & (sg[0][3] == tag) & (sg[1][1] == tag) & (sg[1][3] == tag) & (sg[2][1] == tag) & (sg[2][3] == tag))) gather16<3>(wsr, G_MOD + (1024 + ud) * 8, 512 * 8, tag, sg);
    ga = f32x2{ub ? hi_f(sg[0][0]) : lo_f(sg[0][0]), ub ? hi_f(sg[0][2]) : lo_f(sg[0][2])};
    cc1 = f32x2{rbf(1.f + (ub ? hi_f(sg[2][0]) : lo_f(sg[2][0]))), rbf(1.f + (ub ? hi_f(sg[2][2]) : lo_f(sg[2][2])))};
    xin = f32x2{lo_f(sx2), hi_f(sx2)};
    u32* hp = reinterpret_cast<u32*>(&sm.hb[hpos]);
    hp[512] = pack_bf(cc1[0], cc1[1]);
    hp[1024] = ub ? perm_hi(sg[1][0], sg[1][2]) : perm_lo(sg[1][0], sg[1][2]);
  };
  auto up_frags = [&]() {
    const int pp = lane & 3;
    const u32 a = wimg + (pp & 1) * 32768 + ((pp >> 1) * 32 + g * 8 + ((lane >> 2) & 3)) * 64 + (((wave >> 1) ^ ((g & 1) | (pp & 2))) * 16) + (wave & 1) * 8;
#pragma unroll
    for (int rb = 0; rb < 8; ++rb) {
      const u32x2 lo = tr_read(a + rb * 4096), hi = tr_read(a + rb * 4096 + 256);
      bf[rb] = u32x4{lo[0], lo[1], hi[0], hi[1]};
    }
  };
  const u32x4* ap = reinterpret_cast<const u32x4*>(&sm.hb[((lane >> 3) & 1) * 512 + (lane & 1) * 32 + g * 8]);
  const bool par = lane & 8;
  auto limit = [&](auto sc) {
    constexpr int s = sc.v;
    WAIT_VM(window(s, 1 + (PRM0.W[s >> 3] >> (4 * (s & 7)) & 7)));
    SB();
  };
  int lg[4] = {0, 0, 0, 0};
  float l0 = 0.f, l1 = 0.f;
  u32x2 gmd[2], tbg;
  int t1 = 0;
#pragma unroll
  for (int i = SK0; i < SK1; ++i) kr[i] = u32x4{0u, 0u, 0u, 0u};
  const int first = SK0 + NL - nl;
  auto left_out = [&](int i) { int f = first; asm volatile("" : "+s"(f)); return i < f; };
  sfor<1, NL>([&](auto ic) {
    constexpr int i = ic.v;
    if constexpr (i >= SK0 && i < SK1) { if (left_out(i)) return true; }
    const u32 so = __builtin_amdgcn_readlane(sot, i);
    gate();
    if (!has_w) { const int lt = (int)(((u32)wall_clock64() - t0) << 8) - due + tau, fl = (int)__builtin_amdgcn_s_getreg(7 | (3 << 11)) * tau; if (i >= 2 && i < 5) lg[0] += lt * 5; if (i >= 5 && i < 10) { lg[1] += lt * 3; lg[2] += fl * 3; } if (i >= 10 && i < 13) lg[3] += fl * 5; }
    if constexpr (i >= TBS && i <= TBP) { if (timer) lag += (int)(((u32)wall_clock64() - t0) << 8) - due + ((int)__builtin_amdgcn_s_getreg(7 | (3 << 11)) + 1) * tau; }
    SB();
    if constexpr (i >= SK0 && i < SK1) ld16_into(kr[i], rk, lane * 16, so); else kr[i] = ld16<AUX_NT>(rk, lane * 16, so);
    if constexpr (i == SMO) small_ops();
    if constexpr (i == SMO + 1) { if (voter) __builtin_amdgcn_raw_buffer_store_b32(0u, wsr, pbo + (lane >> 5) * 8192 + (lane & 31) * 4, 0, AUX_SC1); }
    if constexpr (i == TBL) tbg = ld8<AUX_SC1>(wsr, wave == 7 ? G_TB + xcd * 128 + c * 8 : OOB, 0);
    if constexpr (i == QP) { gmd[0] = ld8<AUX_SC1>(wsr, part == 1 ? G_MOD + tid * 8 : OOB, 0); gmd[1] = ld8<AUX_SC1>(wsr, part == 1 ? G_MOD + tid * 8 : OOB, 4096); }
    SB();
    SB();
    if (i >= 4) limit(ic);
    if (i == 1) STAMP(25);
    if (i == 8) STAMP(12);
    if (i == 20) STAMP(21);
    if (i == 32) STAMP(22);
    if (i == TBP) {
      if (timer) {
        if (lane == 0) sm.mflag[wave] = lag;
        LDS_BARRIER();
        if (wave == 7) {
          int s = sm.mflag[lane & 7];
          s += __builtin_amdgcn_mov_dpp(s, XOR1, 0xf, 0xf, true); s += __builtin_amdgcn_mov_dpp(s, XOR2, 0xf, 0xf, true); s += __builtin_amdgcn_mov_dpp(s, XOR7, 0xf, 0xf, true);
          if (lane < 8) st8_sc1(wsr, G_TB + lane * 128 + wi * 8, t0 << 10 | (u32)min(s / (2048 * (TBP - TBS + 1)), 1023), tag);
        }
      }
      SB();
    }
    if (i == PRB) {
      if (voter) {
        u64 ta, tb, tc; u32 dm;
        const u32 pa = pbo + (wave & 1) * 8192;
        asm volatile("s_memtime %0\n\ts_waitcnt lgkmcnt(0)\n\ts_load_dword %3, %4, %5\n\ts_waitcnt lgkmcnt(0)\n\ts_memtime %1\n\ts_waitcnt lgkmcnt(0)\n\t"
                     "s_load_dword %3, %4, %6\n\ts_waitcnt lgkmcnt(0)\n\ts_memtime %2\n\ts_waitcnt lgkmcnt(0)"
                     : "=&s"(ta), "=&s"(tb), "=&s"(tc), "=&s"(dm) : "s"(ws), "s"(pa), "s"(pa ^ 8192u));
        const int d = min(max((int)(2 * (u32)tb - (u32)ta - (u32)tc), -500), 500);
        vote = ((wave ^ die) & 1) ? -d : d;
        STAMP(26);
      }
      SB();
    }
    if (i == TBU) {
      if (wave == 7) {
        const bool ok = tbg[1] == tag;
        float lf = ok ? (float)min(max((int)((t0 << 10 | 1023u) - tbg[0]) >> 10, -300), 300) : 0.f, nf = ok ? 1.f : 0.f, lg = ok ? (float)(tbg[0] & 1023u) : 0.f;
        lf += dpp<XOR1>(lf); nf += dpp<XOR1>(nf); lg += dpp<XOR1>(lg); lf += dpp<XOR2>(lf); nf += dpp<XOR2>(nf); lg += dpp<XOR2>(lg);
        lf += dpp<XOR7>(lf); nf += dpp<XOR7>(nf); lg += dpp<XOR7>(lg); lf += dpp<XOR8>(lf); nf += dpp<XOR8>(nf); lg += dpp<XOR8>(lg);
        const float rn = __builtin_amdgcn_rcpf(fmaxf(nf, 1.f));
        if (lane == 0) sm.bc = u32x2{tag, t0 - (u32)(int)(lf * rn) + (u32)min(max((int)((lg * rn - (float)P.GS) * (float)P.GB * 0.125f), 0), 150)};
      }
      LDS_BARRIER();
      const u32x2 bc = sm.bc;
      tag = __builtin_amdgcn_readfirstlane(bc[0]);
      t1 = (__builtin_amdgcn_readfirstlane((int)(bc[1] - t0)) + TEND + P.X1) * 256;
      tau -= __builtin_amdgcn_readfirstlane((int)((float)(int)(t0 - bc[1]) * 256.f * __builtin_amdgcn_rcpf((float)(nl - TBU - 1))));
      SB();
    }
    if (i == NL - 1) {
      if (reporter) {
        if (lane == 0) sm.mflag[wave] = vote;
        LDS_BARRIER();
        if (wave == 7) {
          int s = sm.mflag[lane & 7];
          s += __builtin_amdgcn_mov_dpp(s, XOR1, 0xf, 0xf, true); s += __builtin_amdgcn_mov_dpp(s, XOR2, 0xf, 0xf, true); s += __builtin_amdgcn_mov_dpp(s, XOR7, 0xf, 0xf, true);
          if (lane < 8) st8_sc1(wsr, G_VT + lane * 128 + xcd * 8, s, tag);
        }
      }
      SB();
    }
    if (i == LNS) {
      tag = next_tag(__builtin_amdgcn_readfirstlane(ep[0]));
      if (part == 1) {
        float x0[8], x1[8], mb0, ri0, mb1, ri1;
#pragma unroll
        for (int k = 0; k < 4; ++k) { x0[2 * k] = lo_f(xr0[k]); x0[2 * k + 1] = hi_f(xr0[k]); x1[2 * k] = lo_f(xr1[k]); x1[2 * k + 1] = hi_f(xr1[k]); }
        ln_stats2(x0, x1, mb0, ri0, mb1, ri1);
        l0 = rbf(rbf(lo_f(xs0) - mb0) * ri0); l1 = rbf(rbf(lo_f(xs1) - mb1) * ri1);
      }
      PIN(l0); PIN(l1);
      SB();
    }
    if (i == WUSE) {
      PIN(tch1); PIN(tch2);
      if ((part == 1 || (part == 2 && has_w && !modc))) {
        WAIT_VM(WK); CFENCE();
        if (part == 2) mod_stage(); else wo_prep();
      }
#pragma unroll
      for (int k = 0; k < 8; ++k) PIN(bf[k]);
      SB();
    }
    if (i == QP + QD) {
      if (part == 1) {
        u32 gm[2] = {gmd[0][0], gmd[1][0]};
        if (!all_ok((gmd[0][1] == tag) & (gmd[1][1] == tag))) { STAMP(11); gather8<2>(wsr, G_MOD + tid * 8, 512 * 8, tag, gm); }
        STAMP(5);
        gemv_go(pack_bf(rbf(l0 * rbf(1.f + lo_f(gm[1]))) + lo_f(gm[0]), rbf(l1 * rbf(1.f + hi_f(gm[1]))) + hi_f(gm[0])));
      }
      SB();
    }
    if (i == QP + QD + 1) {
      if (part == 1) {
        LDS_BARRIER();
        if (wave == 0) {
          const int bb = lane >> 5, jq = lane & 31;
          st8_sc1(wsr, G_Q + (bb * 2048 + blk * 32 + jq) * 8, pack_bf(sum8(&sm.r2[2 * jq * 20 + bb * 8]), sum8(&sm.r2[(2 * jq + 1) * 20 + bb * 8])), tag);
        }
        STAMP(6);
      }
      SB();
    }
    return true;
  });
  PIN(sot);
  tau = te1 >> 16;
  due += P.TG * 256;
  const u32 qoff = G_Q + ((b * 32 + head) * 64 + c * 4) * 8;
  u32x4 qg[2], mlg;
  u32 qq[4];
  constexpr int NB = (NL + 15) / 16;
  float xlo, sc[NB], pr[NB], m = 0.f;
  f32x4 acc = {0.f, 0.f, 0.f, 0.f};
  u32 pb[NB], pm[4], pl8[8], cpf;
  const u32 adst0 = lds_addr(lane < 32 ? &sm.red[wave][c * 8 + g * 4] : &sm.r2[(lane - 32) * 4]);
  u32 po, rdst, rsrc, xo;
  auto wo_addr = [&]() {
    po = dup(G_P + xcd * 8192) + tid * 16 + near;
    rdst = lane < 32 ? (u32)(uintptr_t)&sm.red2[wave][0] + lane * 16 : (u32)(uintptr_t)&sm.r2[0] + (lane - 32) * 16;
    rsrc = (u32)(uintptr_t)&sm.red2[0][((wave & 1) * 16 + (lane & 15)) * 4 + (lane >> 4)];
    xo = dup(G_XP + xcd * 2048 + (wave & 1) * 1024 + (blk * 64 + (lane & ~1)) * 2) + (lane & 1) * 8192;
    PIN(po); PIN(rdst); PIN(rsrc); PIN(xo);
  };
  u32 pso = (wave == 0 && (g & 1) == 0) ? pslot + (c >> 3) * 1024 + (c & 3) * 256 + (c >> 2 & 1) * 128 + (g >> 1) * 8192 : OOB;
  PIN(pso);
  const u32 nadd = wdef ? P.LW : (is_up ? P.LU : 0);
  u32 nto = tid < 2 ? dup(G_NT) + bid * 8 + tid * 8192 : OOB;
  PIN(nto);
  auto notices = [&]() -> int {
    u32 off = dup(G_NT) + near + lane * 16;
    PIN(off);
    until(due);
    int mx;
#pragma nounroll
    for (int spin = 0; spin < SPIN_MAX; ++spin) {
      CFENCE();
      const u32x4 ga_ = ld16<AUX_SC1>(wsr, off, 0), gb_ = ld16<AUX_SC1>(wsr, off, 1024);
      mx = max(max((int)(ga_[0] - t0), (int)(ga_[2] - t0)), max((int)(gb_[0] - t0), (int)(gb_[2] - t0)));
      if (all_ok((((ga_[1] ^ tag) | (ga_[3] ^ tag)) | ((gb_[1] ^ tag) | (gb_[3] ^ tag))) == 0u)) break;
      for (int k = 0; k < P.AG; ++k) __builtin_amdgcn_s_sleep(1);
    }
    if (all_ok(mx <= (t1 >> 8) - P.AC)) return t1;
    mx = max(mx, __builtin_amdgcn_mov_dpp(mx, XOR1, 0xf, 0xf, true)); mx = max(mx, __builtin_amdgcn_mov_dpp(mx, XOR2, 0xf, 0xf, true));
    mx = max(mx, __builtin_amdgcn_mov_dpp(mx, XOR7, 0xf, 0xf, true)); mx = max(mx, __builtin_amdgcn_mov_dpp(mx, XOR8, 0xf, 0xf, true));
    mx = max(max(__builtin_amdgcn_readlane(mx, 0), __builtin_amdgcn_readlane(mx, 16)), max(__builtin_amdgcn_readlane(mx, 32), __builtin_amdgcn_readlane(mx, 48)));
    return max(min(mx, 1 << 20) * 256, t1 + P.L0 * 256) + P.L1 * 256;
  };
  auto pv_row = [&](auto jc) {
    constexpr int i = jc.v;
    const u32 pv = __float_as_uint(bcast16(u2f(pb[i >> 4]), i & 15));
    acc = mfma16(u32x4{pv & pm[0], pv & pm[1], pv & pm[2], pv & pm[3]}, vr[i], acc);
    PIN(acc);
    return true;
  };
  sfor<0, NL - 1>([&](auto ic) {
    constexpr int v = ic.v;
    if constexpr (v >= SK0 && v < SK1) { if (left_out(v)) return true; }
    if constexpr (v > 0) {
      const u32 so = __builtin_amdgcn_readlane(sot, v);
      if constexpr (v >= SK1) { if (v == NL - P.NE) tau = (tau * P.FE) >> 4; }
      gate();
      SB();
      if constexpr (v >= SK0 && v < SK1) ld16_into(vr[v], rv, lane * 16, so); else vr[v] = ld16<AUX_NT>(rv, lane * 16, so);
      SB();
      SB();
    }
    if constexpr (v == SK0 - 1) {
#pragma unroll
      for (int i = SK0; i < SK1; ++i) { vr[i] = u32x4{0u, 0u, 0u, 0u}; PIN(vr[i]); }
    }
    if constexpr (v == VQ - 4) { qg[0] = ld16<AUX_SC1>(wsr, qoff, 0); qg[1] = ld16<AUX_SC1>(wsr, qoff + 16, 0); }
    if constexpr (v == VSIDE) { side_loads(); tbg = ld8<AUX_SC1>(wsr, wave == 7 ? G_VT + xcd * 128 + (lane & 7) * 8 : OOB, 0); }
    if constexpr (v == CPF) {
      u64 pc; u32 dlt, len;
      asm volatile("s_getpc_b64 %0\n.Lcpf_here:\n\ts_mov_b32 %1, .Ltail_start-.Lcpf_here\n\ts_mov_b32 %2, .Lend_a-.Ltail_start\n\ts_max_i32 %2, %2, .Lend_b-.Ltail_start\n\ts_max_i32 %2, %2, .Lend_c-.Ltail_start\n\ts_max_i32 %2, %2, .Lend_d-.Ltail_start\n\ts_max_i32 %2, %2, .Lend_e-.Ltail_start" : "=s"(pc), "=s"(dlt), "=s"(len) :: "scc");
      pc += dlt;
      const rsrc_t rc = mk_rsrc((const void*)pc, (part == 3 && i8 == 1) ? len : 0u);
      cpf = __builtin_amdgcn_raw_buffer_load_b32(rc, tid * 64, 0, 0);
    }
    if constexpr (v == SK1 + 2) PIN(cpf);
    if constexpr (v == SK1) mlg = ld16<AUX_SC1>(wsr, wave == 0 ? mlo : OOB, 0);
    SB();
    if constexpr (NL + v < 72) limit(IC<NL + v>{});
    if constexpr (v >= SK0 + WV) { WAIT_VM(v < SK1 ? WV : WV + 1); SB(); }
    if constexpr (v == VQ) {
      if (!all_ok((qg[0][1] == tag) & (qg[0][3] == tag) & (qg[1][1] == tag) & (qg[1][3] == tag))) { STAMP(20); gather16<2>(wsr, qoff, 16, tag, qg); }
      STAMP(1); WSTAMP(1);
      qq[0] = qg[0][0]; qq[1] = qg[0][2]; qq[2] = qg[1][0]; qq[3] = qg[1][2];
    }
    if constexpr (v >= VQ && v < VS) {
      constexpr int sl = v - VQ, bt = sl >> 1;
      float xs[8];
#pragma unroll
      for (int i = 0; i < 8; ++i) {
        float d = 0.f;
        if (sl * 8 + i < NL) {
#pragma unroll
          for (int k = 0; k < 4; ++k) d = dot2(kr[sl * 8 + i][k], qq[k], d);
        }
        xs[i] = d;
      }
#pragma unroll
      for (int h = 0; h < 4; ++h) xs[h] = sel_add<XOR7>(xs[h], xs[h + 4], c & 4);
#pragma unroll
      for (int h = 0; h < 2; ++h) xs[h] = sel_add<XOR2>(xs[h], xs[h + 2], c & 2);
      xs[0] = sel_add<XOR1>(xs[0], xs[1], c & 1);
      if constexpr ((sl & 1) || sl == NSL - 1) {
        const float t = (sl & 1) ? sel_add<XOR8>(xlo, xs[0], c & 8) : xs[0] + dpp<XOR8>(xs[0]);
        const float s = rbf(rbf(t) * 0.08838834764831845f);
        sc[bt] = (bt * 16 + c < SK0 || (bt * 16 + c >= first && bt * 16 + c < NL)) ? s : -__builtin_inff();
        PIN(sc[bt]);
      } else {
        xlo = xs[0];
        PIN(xlo);
      }
    }
    if constexpr (v == VS) {
      float mloc = sc[0];
#pragma unroll
      for (int bt = 1; bt < NB; ++bt) mloc = fmaxf(mloc, sc[bt]);
      m = fmaxf(wave_max(mloc), -3.0e38f);
      float ls = 0.f;
#pragma unroll
      for (int bt = 0; bt < NB; ++bt) { pr[bt] = __expf(sc[bt] - m); ls += pr[bt]; }
      const float l = wave_sum(ls);
      if (lane == 0) { sm.ml[wave][0] = m; sm.ml[wave][1] = l; }
      if (c == ((NL - 1) & 15)) sm.pl[g][wave] = pr[NB - 1];
      STAMP(2); WSTAMP(2);
    }
    if constexpr (v >= SK1) pv_row(IC<v - SK1 + SK0 - WV>{});
    if constexpr (v < SK0 || v >= SK0 + WV) sfor<cons_end(v - 1), cons_end(v)>(pv_row);
    if constexpr (v == SK1 + 1) {
      float p[8];
#pragma unroll
      for (int w = 0; w < 8; ++w) p[w] = sm.pl[g][w] * __expf(sm.ml[w][0] - m);
#pragma unroll
      for (int w = 0; w < 8; ++w) pl8[w] = pack_bf(p[w], p[w]);
#pragma unroll
      for (int w = 0; w < 8; ++w) PIN(pl8[w]);
    }
    if constexpr (v == MLS - 1) { side_pin(); if (is_up) up_prep(); PIN(ga); PIN(xin); PIN(cc1); }
    if constexpr (v == UPREP - 1) wo_addr();
    if constexpr (v == UPREP) {
#pragma unroll
      for (int k = 0; k < 8; ++k) asm volatile("" : "=v"(bf[k]));
      if (part == 0) wo_prep(); else if (is_up) up_frags();
#pragma unroll
      for (int k = 0; k < 8; ++k) PIN(bf[k]);
    }
    if constexpr (v == UPREP + 1) {
      if (is_up) {
        const u32x4* a1 = ap + 128 + ((lane >> 1) & 1) * 128;
        f32x4 d0 = {0.f, 0.f, 0.f, 0.f}, d1 = {0.f, 0.f, 0.f, 0.f};
#pragma unroll
        for (int rb = 0; rb < 4; ++rb) { d0 = mfma16(a1[rb * 8], bf[rb], d0); d1 = mfma16(a1[(rb + 4) * 8], bf[rb + 4], d1); }
        Af = par ? d0[1] + d1[1] : d0[0] + d1[0]; Bf = par ? d0[3] + d1[3] : d0[2] + d1[2];
        Af += dpp<XOR8>(Af); Bf += dpp<XOR8>(Bf);
      }
      PIN(Af); PIN(Bf);
    }
    if (v == 10 || v == 16) STAMP(15 + v / 6);
    if (v == 20) STAMP(18);
    if (v == 32) STAMP(19);
    if constexpr (v == MLS) {
      if (wave == 7) {
        int vt = tbg[1] == tag ? (int)tbg[0] : 0;
        vt += __builtin_amdgcn_mov_dpp(vt, XOR1, 0xf, 0xf, true); vt += __builtin_amdgcn_mov_dpp(vt, XOR2, 0xf, 0xf, true); vt += __builtin_amdgcn_mov_dpp(vt, XOR7, 0xf, 0xf, true);
        if (lane == 0) sm.bc[0] = vt;
      }
      LDS_BARRIER();
      near = ((__builtin_amdgcn_readfirstlane(sm.bc[0]) >> 31 ^ die ^ P.NR ^ 1) & 1) * 8192u;
      float mm[8], ll[8];
#pragma unroll
      for (int w = 0; w < 8; ++w) { const f32x2 t = *reinterpret_cast<const f32x2*>(&sm.ml[w][0]); mm[w] = t[0]; ll[w] = t[1]; }
      float Mwg = mm[0], Lwg = 0.f;
#pragma unroll
      for (int w = 1; w < 8; ++w) Mwg = fmaxf(Mwg, mm[w]);
      if (wave == 0) {
#pragma unroll
        for (int w = 0; w < 8; ++w) Lwg = fmaf(__expf(mm[w] - Mwg), ll[w], Lwg);
        if (lane == 0) st16_sc1(wsr, G_ML + ((head * 2 + b) * 4 + part) * 16, __float_as_uint(Mwg), __float_as_uint(Lwg), tag);
      }
      const float fw = __expf(m - Mwg);
      m = Mwg;
#pragma unroll
      for (int bt = 0; bt < NB; ++bt) { pb[bt] = pack_bf(pr[bt] * fw, pr[bt] * fw); PIN(pb[bt]); }
#pragma unroll
      for (int k = 0; k < 4; ++k) { pm[k] = (c >> 1 & 3) == k ? ((c & 1) ? 0xffff0000u : 0x0000ffffu) : 0u; PIN(pm[k]); }
    }
    SB();
    return true;
  });
  asm volatile(".Ltail_start:");
  if (wave != 0) {
    pv_row(IC<SK0 - 1>{});
    WAIT_VM(WV); SB();
    sfor<NL - 1 - WV, NL - 1>(pv_row);
    *LDSP(f32x4, adst0) = acc;
    WSTAMP(14);
    LDS_BARRIER();
  } else {
    u32x4 xr[8], am[8];
#pragma unroll
    for (int w = 0; w < 8; ++w) am[w] = u32x4{pl8[w] & pm[0], pl8[w] & pm[1], pl8[w] & pm[2], pl8[w] & pm[3]};
#pragma unroll
    for (int w = 0; w < 8; ++w) PIN(am[w]);
    {
      const u32 so = __builtin_amdgcn_readlane(sot, NL - 1);
      gate();
      SB();
#pragma unroll
      for (int w = 0; w < 8; ++w) xr[w] = ld16<AUX_NT>(rv, lane * 16, so ^ ((w & 1) ? col(P, 7) : 0u) ^ ((w & 2) ? col(P, 8) : 0u) ^ ((w & 4) ? col(P, 9) : 0u));
      SB();
    }
    u32 rsum = lds_addr(&sm.red[0][c * 8 + (g & 1) * 4]);
    pv_row(IC<SK0 - 1>{});
    WAIT_VM(WV + 8); SB();
    sfor<NL - 1 - WV, NL - 3>(pv_row);
    if (__builtin_expect(!all_ok((mlg[1] == tag) & (mlg[3] == tag)), 0)) {
#pragma nounroll
      for (int spin = 0; spin < SPIN_MAX; ++spin) {
        CFENCE();
        mlg = ld16<AUX_SC1>(wsr, mlo, 0);
        if (all_ok((mlg[1] == tag) & (mlg[3] == tag))) break;
      }
    }
    merge_factor(mlg);
    sfor<NL - 3, NL - 1>(pv_row);
    STAMP(14); WSTAMP(14);
    LDS_BARRIER();
    STAMP(15);
    f32x4 t[8];
#pragma unroll
    for (int w = 1; w < 8; ++w) t[w] = LDSP(f32x4, rsum)[w * 32];
    acc = ((acc + t[1]) + (t[2] + t[3])) + ((t[4] + t[5]) + (t[6] + t[7]));
    PIN(acc);
    STAMP(3);
    SB();
#pragma unroll
    for (int w = 0; w < 8; ++w) acc = mfma16(am[w], xr[w], acc);
  }
  WAIT_VM(0);
  acc *= fmrg;
  {
    const u32 w0 = min(pack_bf(acc[0], acc[1]), SENT - 1u), w1 = min(pack_bf(acc[2], acc[3]), SENT - 1u);
    const auto e0 = __builtin_amdgcn_permlane16_swap(w0, w0, false, false), e1 = __builtin_amdgcn_permlane16_swap(w1, w1, false, false);
    __builtin_amdgcn_raw_buffer_store_b128(u32x4{e0[0], e1[0], e0[1], e1[1]}, wsr, pso, 0, AUX_SC1);
  }
  st8_sc1(wsr, nto, (u32)wall_clock64() + nadd, tag);
  SB();
  STAMP(4);

  if (part == 2) FINISH();
  if (part == 0) {
    __builtin_amdgcn_s_barrier();
#pragma nounroll
    while (wleft > 0) piece();
    bool late = false;
    u32x4 og[2];
    if (wdef) {
      late = true;
      while ((int)(((u32)wall_clock64() - t0) << 8) < t1 - 20 * 256) if (__builtin_amdgcn_s_getreg(7 | (3 << 11)) == 0) { late = false; break; }
      if (__builtin_expect(late, 0)) { until(t1); CFENCE(); og[0] = ld16<AUX_SC1>(wsr, po, 0); SB(); WAIT_VM(1); } else { WAIT_VM(0); }
      CFENCE();
      if (P.WD > 4) wo_prep(0, 1);
      wo_prep(1, 2);
#pragma unroll
      for (int k = 0; k < 8; ++k) PIN(bf[k]);
      SB();
    }
    if (__builtin_expect(!late, 1)) { until(t1); CFENCE(); og[0] = ld16<AUX_SC1>(wsr, po, 0); }
    STAMP(11);
    SB();
    f32x4 d[4];
    auto wo_mm = [&]() {
      const f32x4 z = {0.f, 0.f, 0.f, 0.f};
#pragma unroll
      for (int t = 0; t < 4; ++t) { d[t] = mfma16(og[0], bf[t], z); og[1][t] = __float_as_uint(dpp<XOR8>(u2f(og[0][t]))); }
      SB();
#pragma unroll
      for (int t = 0; t < 4; ++t) d[t] = mfma16(og[1], bf[4 + t], d[t]);
    };
    auto p_bad = [&]() { return !all_ok(max(max(og[0][0], og[0][1]), max(og[0][2], og[0][3])) != SENT); };
    wo_mm();
    if (__builtin_expect(p_bad(), 0)) {
#pragma nounroll
      for (int spin = 0; spin < SPIN_MAX; ++spin) {
        __builtin_amdgcn_s_sleep(1);
        CFENCE();
        og[0] = ld16<AUX_SC1>(wsr, po, 0);
        if (!p_bad()) break;
      }
      wo_mm();
      SB();
      asm volatile(".Lend_d:");
    }
    STAMP(5); WSTAMP(5);
    {
      SB();
      f32x4 y; f32x2 h[4];
#pragma unroll
      for (int t = 0; t < 4; ++t) { h[t] = d[t].xy + d[t].zw; PIN(h[t]); }
#pragma unroll
      for (int t = 0; t < 4; ++t) { y[t] = h[t][0] + h[t][1]; PIN(y[t]); }
      *LDSP(f32x4, rdst) = y;
    }
    STAMP(16);
    LDS_BARRIER();
    STAMP(17);
    if (wave < 2) {
      f32x2 v[4];
#pragma unroll
      for (int w = 0; w < 4; ++w) v[w] = f32x2{LDSP(float, rsrc)[w * 256], LDSP(float, rsrc)[w * 256 + 128]};
      const f32x2 h = (v[0] + v[1]) + (v[2] + v[3]);
      const float y = h[0] + h[1];
      __builtin_amdgcn_raw_buffer_store_b32(min(pack_bf(dpp<0xA0>(y), dpp<0xF5>(y)), SENT - 1u), wsr, xo, 0, AUX_SC1);
      asm volatile(".Lend_a:");
    }
    STAMP(6);
    FINISH();
  }
  if (part == 3) {
    if (!has_w) FINISH();
    u32x4 b2[4];
    {
      __builtin_amdgcn_s_barrier();
      const rsrc_t r2 = mk_rsrc(w2 + (size_t)(blk * 32 + wave * 4) * 512, 4096u);
      const u32 l2 = (u32)(uintptr_t)(&sm.w2b[wave * 4][0]);
#pragma unroll
      for (int n = 0; n < 4; ++n) dma16(r2, (lane ^ (n * 2) ^ ((wave & 2) << 2)) * 16, n * 1024, l2 + n * 1024);
    }
    const u32 tdst = lds_addr(&sm.hb[hpos]);
    const u32 sdst = lds_addr(&sm.stat[wave][lane]);
    const u32 ssrc = lds_addr(&sm.stat[(g >> 1) * 4 + ((lane >> 3) & 3)][(lane & 7) * 4]);
    const u32 adst = lds_addr((c < 4 && (g & 1) == 0) ? &sm.actl[g >> 1][wave * 4 + c] : reinterpret_cast<u16*>(&sm.dummy[lane]));
    const u32 asrc = lds_addr(&sm.actl[lane & 1][g * 8]);
    const u32 apa = lds_addr(&sm.hb[(c >> 3) * 512 + (c & 3) * 64 + ((c >> 2) & 1) * 32 + g * 8]);
    constexpr u32 xs2 = 16384;
    u32 xoff = dup(G_XP) + near + g * 2048 + ub * 1024 + (wave & 3) * 256 + c * 16;
    u32x4 one;
#pragma unroll
    for (int k = 0; k < 4; ++k) one[k] = ((c & 2) == 0 && (c >> 2) == k) ? ((c & 1) ? 0x3f800000u : 0x00003f80u) : 0u;
    PIN(one);
    u32 ooff = dup(G_OP + (blk * 512 + tid) * 8);
    float Afp = Af, Bfp = Bf;
    PIN(xoff); PIN(ooff); PIN(Afp); PIN(Bfp);
    f32x2 gc = ga * cc1, xc = xin * cc1;
    PIN(ga); PIN(xin); PIN(gc); PIN(xc);
    u32 w2a = (u32)(uintptr_t)(&sm.w2b[0][0]) + (g * 8 + ((lane >> 2) & 3)) * 1024 + (wave ^ (g & 1)) * 128 + (lane & 15) * 8;
    PIN(w2a);
    if (wave == 1) {
      due = t1 + P.N1 * 256;
      const int tn = notices();
      due = tn + P.X2 * 256;
      until(due);
    }
    asm volatile("s_waitcnt vmcnt(0)" ::: "memory");
    STAMP(9);
    __builtin_amdgcn_s_barrier();
    STAMP(11);
    CFENCE();
    u32x4 xg[2] = {ld16<AUX_SC1>(wsr, xoff, 0), ld16<AUX_SC1>(wsr, xoff, xs2)};
    SB();
#pragma unroll
    for (int t = 0; t < 4; ++t) {
      const u32x2 lo = tr_read(w2a ^ (t * 32)), hi = tr_read((w2a ^ (t * 32)) + 4096);
      b2[t] = u32x4{lo[0], lo[1], hi[0], hi[1]};
    }
#pragma unroll
    for (int t = 0; t < 4; ++t) PIN(b2[t]);
    SB();
    const f32x4 z4 = {0.f, 0.f, 0.f, 0.f};
    auto xp_bad = [&]() { return !all_ok(max(max(max(xg[0][0], xg[0][1]), max(xg[0][2], xg[0][3])), max(max(xg[1][0], xg[1][1]), max(xg[1][2], xg[1][3]))) != SENT); };
    f32x4 v = mfma16(one, xg[1], mfma16(one, xg[0], z4));
    if (__builtin_expect(xp_bad(), 0)) {
#pragma nounroll
      for (int spin = 0; spin < SPIN_MAX; ++spin) {
        __builtin_amdgcn_s_sleep(1);
        CFENCE();
        xg[0] = ld16<AUX_SC1>(wsr, xoff, 0); xg[1] = ld16<AUX_SC1>(wsr, xoff, xs2);
        if (!xp_bad()) break;
      }
      v = mfma16(one, xg[1], mfma16(one, xg[0], z4));
      SB();
      asm volatile(".Lend_e:");
    }
    STAMP(5); WSTAMP(5);
    {
      const f32x2 a = v.xy, hc = __builtin_elementwise_fma(gc, a, xc), x1 = __builtin_elementwise_fma(ga, a, xin);
      *LDSP(u32, tdst) = pack_bf(hc[0], hc[1]);
      *LDSP(float, sdst) = swap32_add(x1[0] + x1[1], fmaf(x1[0], x1[0], x1[1] * x1[1]));
    }
    STAMP(16);
    LDS_BARRIER();
    STAMP(17);
    {
      const f32x4 z = {0.f, 0.f, 0.f, 0.f};
      const u32x4 af0 = LDSP(u32x4, apa)[0];
      SB();
      const f32x4 s4 = LDSP(f32x4, ssrc)[0], q4 = LDSP(f32x4, ssrc)[8];
      const u32x4 af1 = LDSP(u32x4, apa)[32];
      SB();
      f32x4 d[4];
      d[0] = mfma16(af0, bf[0], z); d[1] = mfma16(af0, bf[1], z);
      SB();
      const f32x2 s2 = s4.xy + s4.zw, q2 = q4.xy + q4.zw;
      float s = s2[0] + s2[1], q = q2[0] + q2[1];
      SB();
      d[2] = mfma16(af0, bf[2], z);
      SB();
      s = swap16_add(s, q);
      SB();
      d[3] = mfma16(af0, bf[3], z);
      SB();
      s += dpp<XOR1>(s); s += dpp<XOR2>(s);
      SB();
      d[0] = mfma16(af1, bf[4], d[0]);
      SB();
      s += dpp<XOR7>(s); s += dpp<XOR8>(s);
      SB();
      d[1] = mfma16(af1, bf[5], d[1]);
      SB();
      const auto sq = __builtin_amdgcn_permlane16_swap(__float_as_uint(s), __float_as_uint(s), false, false);
      const float mean = u2f(sq[0]) * (1.f / 512.f);
      SB();
      d[2] = mfma16(af1, bf[6], d[2]);
      SB();
      const float var = fmaf(u2f(sq[1]), 1.f / 512.f, -mean * mean);
      SB();
      d[3] = mfma16(af1, bf[7], d[3]);
      SB();
      float ri = __builtin_amdgcn_rsqf(fmaxf(var, 0.f) + 1e-5f);
      float cf = fmaf(-ri * mean, Afp, Bfp);
      float rk = ri * -1.44269504f, ck = cf * -1.44269504f;
      PIN(ri); PIN(cf); PIN(rk); PIN(ck);
      SB();
      PIN(d[0]); PIN(d[1]);
      float y = d[0][0] + d[1][1];
      PIN(y); PIN(d[2]);
      y += d[2][2];
      PIN(y); PIN(d[3]);
      y += d[3][3];
      y = __builtin_bit_cast(float, __builtin_amdgcn_update_dpp(__builtin_bit_cast(int, y), __builtin_bit_cast(int, y), XOR8, 0xa, 0xf, false));
      y = swap16_add(y, y);
      const float ex = __builtin_amdgcn_exp2f(fmaf(y, rk, ck));
      y = fmaf(y, ri, cf);
      const float gu = y * dpp<0x104>(y);
      *LDSP(u16, adst) = (u16)pack_bf(gu * __builtin_amdgcn_rcpf(1.f + ex), 0.f);
    }
    STAMP(18);
    LDS_BARRIER();
    STAMP(10);
    {
      const u32x4 af = *LDSP(u32x4, asrc);
      const f32x4 z = {0.f, 0.f, 0.f, 0.f};
      f32x4 d0 = mfma16(af, b2[0], z), d1 = mfma16(af, b2[1], z), d2 = mfma16(af, b2[2], z), d3 = mfma16(af, b2[3], z);
      asm volatile("" : "+v"(d0), "+v"(d1), "+v"(d2), "+v"(d3));
      const bool g1 = g & 1, g2 = g & 2;
      const float e0 = g1 ? d1[0] : d0[0], e1 = g1 ? d1[1] : d0[1], f0 = g1 ? d3[0] : d2[0], f1 = g1 ? d3[1] : d2[1];
      const u32x2 ov = {pack_bf(g2 ? f0 : e0, g2 ? f1 : e1), tag};
      __builtin_amdgcn_raw_buffer_store_b64(ov, wsr, ooff, near ^ 8192u, AUX_SC1);
      __builtin_amdgcn_raw_buffer_store_b64(ov, wsr, ooff, near, AUX_SC1);
    }
    asm volatile(".Lend_b:");
    STAMP(6);
    FINISH();
  }
  if (wave == 0) {
    if (!all_ok((sg[0][1] == tag) & (sg[0][3] == tag) & (sg[1][1] == tag) & (sg[1][3] == tag))) {
      u32x4 t[2];
      gather16<2>(wsr, G_MOD + (1024 + fd) * 8, 1536 * 8, tag, t);
      sg[0] = t[0]; sg[1] = t[1];
    }
    u32 xa;
    {
      due = t1 + P.N2 * 256;
      const int tn = notices();
      due = tn + P.X3 * 256;
      until(due);
    }
    const bool hb = g & 1;
#pragma nounroll
    for (int spin = 0; spin < SPIN_MAX; ++spin) {
      CFENCE();
      xa = __builtin_amdgcn_raw_buffer_load_b32(wsr, dup(G_XP) + near + (g >> 1) * 16384 + (c >> 2) * 2048 + (g & 1) * 1024 + fd * 2, 0, AUX_SC1);
      if (all_ok(xa != SENT)) break;
      __builtin_amdgcn_s_sleep(1);
    }
    float a0 = lo_f(xa), a1 = hi_f(xa);
    a0 += dpp<0x124>(a0); a1 += dpp<0x124>(a1);
    a0 += dpp<XOR8>(a0); a1 += dpp<XOR8>(a1);
    a0 = swap32_add(a0, a0); a1 = swap32_add(a1, a1);
    f32x2 xv = {rbf(lo_f(sx2) + rbf((hb ? hi_f(sg[0][0]) : lo_f(sg[0][0])) * rbf(a0))), rbf(hi_f(sx2) + rbf((hb ? hi_f(sg[0][2]) : lo_f(sg[0][2])) * rbf(a1)))};
    f32x2 gv = {hb ? hi_f(sg[1][0]) : lo_f(sg[1][0]), hb ? hi_f(sg[1][2]) : lo_f(sg[1][2])};
    PIN(xv); PIN(gv);
    if (wi == 0 && lane == 0) __builtin_amdgcn_raw_buffer_store_b32(tag, wsr, WS_EPOCH, 0, AUX_SC1);
    STAMP(10);
    due = max(due + P.X4 * 256, (int)(((u32)wall_clock64() - t0) << 8) + P.GX * 256);
    const bool last = lane < (NUP - 32) * 4;
    u32 o0 = dup(G_OP + (lane >> 2) * 4096 + fd * 8) + near, o2 = last ? o0 : OOB, tg2 = last ? tag : 0u;
    const rsrc_t rout = mk_rsrc(out, 2048u);
    u32 oo = (lane & 44) == 0 ? (g * 512 + fd) * 2 : OOB;
    const u32 pk = (c & 10) ? 0u : ((c & 4) ? 0x3f800000u : 0x00003f80u);
    u32x4 one = {(c & 1) ? 0u : pk, 0u, (c & 1) ? pk : 0u, 0u};
    PIN(o0); PIN(o2); PIN(tg2); PIN(oo); PIN(one);
    __builtin_amdgcn_raw_buffer_store_b32(pack_bf(xv[0], xv[1]), rout, oo, 0, AUX_SC1);
    u32 s1_ = 131072, s2_ = 262144;
    asm volatile("" : "+s"(s1_), "+s"(s2_));
    until(due);
    f32x4 sv;
#pragma nounroll
    for (int spin = 0; spin < SPIN_MAX; ++spin) {
      CFENCE();
      const u32x4 og[3] = {ld16<AUX_SC1>(wsr, o0, 0), ld16<AUX_SC1>(wsr, o0, s1_), ld16<AUX_SC1>(wsr, o2, s2_)};
      sv = f32x4{0.f, 0.f, 0.f, 0.f};
#pragma unroll
      for (int k = 0; k < 3; ++k) sv = mfma16(one, og[k], sv);
      SB();
      u32 bad = ((og[0][1] ^ tag) | (og[0][3] ^ tag) | (og[1][1] ^ tag)) | ((og[1][3] ^ tag) | (og[2][1] ^ tg2) | (og[2][3] ^ tg2));
      PIN(bad);
      SB();
      float s0 = sv[0] + dpp<0x124>(sv[0]), s1 = sv[1] + dpp<0x124>(sv[1]);
      PIN(s0); PIN(s1);
      s0 += dpp<XOR8>(s0); s1 += dpp<XOR8>(s1);
      PIN(s0); PIN(s1);
      __builtin_amdgcn_raw_buffer_store_b32(pack_bf(fmaf(gv[0], s0, xv[0]), fmaf(gv[1], s1, xv[1])), rout, oo, 0, AUX_SC1);
      SB();
      if (all_ok(bad == 0u)) break;
      __builtin_amdgcn_s_sleep(1);
    }
    STAMP(7);
    asm volatile(".Lend_c:");
  }
  FINISH();
}

extern "C" void kernel_launch(void* const* d_in, const int* in_sizes, int n_in,
                              void* d_out, int out_size, void* d_ws, size_t ws_size,
                              hipStream_t stream) {
  fused_kernel<<<256, 512, 0, stream>>>((const u16*)d_in[1], (const u16*)d_in[2], (const u16*)d_in[4], (const u16*)d_in[3], (const u16*)d_in[10], (const u16*)d_in[6], (const u16*)d_in[7],
                                        (unsigned char*)d_ws, (const u16*)d_in[0], (const u16*)d_in[9], (const u16*)d_in[8], (u16*)d_out);
}
```

```cpp
#define PROBE_MUL 10
#include <hip/hip_runtime.h>
#include <stdint.h>

typedef unsigned int u32;
typedef unsigned short u16;
typedef unsigned long long u64;
typedef __attribute__((ext_vector_type(4))) unsigned u32x4;
typedef __attribute__((ext_vector_type(2))) unsigned u32x2;
typedef __attribute__((ext_vector_type(2))) __bf16 bf16x2;
typedef __attribute__((ext_vector_type(4))) float f32x4;
typedef __attribute__((ext_vector_type(2))) float f32x2;
typedef __amdgpu_buffer_rsrc_t rsrc_t;

constexpr int NH = 32, SK = 16384, DH = 128, WIN = 4096, FF = 1376;
struct Prm { int E0, ES, E1, E2, E2C, EU, TEND, DT_UP, L0, L1, X1, X2, X3, X4, NE, FE, M0, M1, M2, M3, M4, GS, GX, N1, N2, GA, WD, DT_WO, LW, LU, EW, PM, NF, NR, C0, A0, AG, AC, TS, TW, TK, TG, GB, TT, W[9]; };
constexpr Prm PRM0 = {32, 20, 32, 32, 54, 58, 1978, 60, 12, 30, 88, 70, 100, 89, 3, 8, 0x2511f55b, 0x2b7425d5, 0x6017665, 0x17e0b9ec, 0x31944305, 63, 30, 36, 62, 0, 4, 100, 45, 15, 0, 0x0, 15, 14, 5, 30, 4, 4, 509, 228, 60, 20, 7, 0, 0x77777777, 0x77777777, 0x77777777, 0x77777777, 0x77777777, 0x44444477, 0x44444444, 0x44444444, 0x44444444};
constexpr int NL = 37;
constexpr int SK0 = 21, SK1 = 32;
constexpr int NCMIN = (NL - SK1 + SK0) * 8;
constexpr int WK = 8, WV = 5;
constexpr int WUSE = 12, LNS = 7;
constexpr int QP = 14, QD = 4;
constexpr int VQ = 6;
constexpr int NSL = (NL + 7) / 8;
constexpr int VS = VQ + NSL;
constexpr int MLS = VS + 2;
constexpr int VSIDE = MLS - 5, UPREP = MLS + 6;
constexpr int CPF = 17;
constexpr int SMO = 1;
constexpr int PRB = 19;
constexpr int TBS = 7, TBP = 10, TBL = 13, TBU = 17;
constexpr int window(int s, int w) {
  int n = w;
  for (int u = s - w + 1; u <= s; ++u) n += u == SMO ? 6 : (u == TBL || u == NL + CPF || u == NL + SK1 ? 1 : (u == QP || u == NL + VQ - 4 ? 2 : (u == NL + VSIDE ? 5 : 0)));
  return n;
}
constexpr int cons_end(int v) {
  const int a = v - WV + 1, c = (v - MLS) * 4;
  const int m = a < c ? a : c;
  return m < 0 ? 0 : (m > NL ? NL : m);
}
constexpr u32 col(const Prm& P, int j) { const int m[6] = {P.M0, P.M1, P.M2, P.M3, P.M4, P.PM}; return (u32)(m[j / 3] >> (j % 3 * 10) & 1023) << 10; }
constexpr u32 first_col(const Prm& P, int k) {
  if (k < 4) return col(P, 12 + k) | 1u << (24 + k);
  if (k < 6) return col(P, k - 4);
  if (k < 8) return col(P, 4 + k) | 1u << (16 + k);
  if (k < 11) return col(P, k - 1);
  return (col(P, 2) ^ col(P, 3) ^ col(P, 4) ^ col(P, 5) ^ col(P, 6) ^ col(P, 10) ^ col(P, 11)) | 0xf00000u;
}
constexpr bool one_to_one(const Prm& P) {
  u32 c[10] = {};
  for (int j = 0; j < 10; ++j) c[j] = col(P, j);
  for (int j = 0; j < 10; ++j) {
    if (c[j] == 0) return false;
    const u32 low = c[j] & (0u - c[j]);
    for (int k = j + 1; k < 10; ++k) if (c[k] & low) c[k] ^= c[j];
  }
  return true;
}
static_assert(one_to_one(PRM0), "every KB of the window exactly once");
struct Tab { u32 e[512]; };
constexpr Tab make_tab(const Prm& P) {
  Tab T{};
  for (int bid = 0; bid < 256; ++bid) {
    const int xcd = bid & 7, part = bid >> 4 & 3, i8 = (bid >> 3 & 1) | bid >> 6 << 1, wi = i8 * 8 + xcd;
    const bool has_mod = wi < 48, has_up = wi >= 21;
    const int cb0 = (i8 + P.C0) & 7, cb1 = (i8 + 4) & 7, ms = wi - 20;
    const int mi = wi < 8 ? wi : (wi < 16 ? 8 + ((xcd + 4) & 7) : (wi < 20 ? (wi - 14) * 8 + 3 : 16 + ms / 7 * 8 + ms % 7 + (ms % 7 >= 3 ? 1 : 0)));
    const int ui = xcd * 5 + (xcd > 5 ? xcd - 5 : 0) + (i8 >= 3 ? (xcd >= 5 ? 1 : 0) + i8 - 3 : 0);
    const int wd = cb0 == 3 ? 0 : P.WD;
    const int e[4] = {P.E0 - 4 * wd + (cb0 == 3 ? P.ES : 0), P.E1 + (cb1 == 3 ? P.ES : 0), has_mod ? (wi < 16 ? P.E2C : P.E2) + ((mi & 7) == 3 ? P.ES : 0) : 0, has_up ? P.EU : 0};
    int tw[4] = {}, win[4] = {}, span[4] = {};
    for (int k = 0; k < 4; ++k) {
      const int np = k == 0 ? 8 - wd : 8;
      tw[k] = np ? e[k] * P.TW / (16 * np) : 0;
      span[k] = P.TEND - (P.NF + tw[k] * np / 2 > P.TK ? P.NF + tw[k] * np / 2 : P.TK) - P.TG - (k == 0 ? (wd ? P.DT_WO : P.A0) : (k == 3 && has_up ? P.DT_UP : 0));
      win[k] = span[k] - (k == 0 && wd ? P.EW : (k == 3 && has_up ? P.GA : 0));
    }
    int n[4] = {NCMIN / 8, NCMIN / 8, NCMIN / 8, NCMIN / 8};
    for (int left = 128 - NCMIN / 2; left > 0; --left) {
      int best = -1;
      for (int k = 3; k >= 0; --k)
        if (n[k] < NL && (best < 0 || n[k] * win[best] < n[best] * win[k])) best = k;
      ++n[best];
    }
    const int c[4] = {0, n[0] - 1, n[0] + n[1] - 2, n[0] + n[1] + n[2] - 3};
    const int nl = n[part];
    const bool has_w = part < 2 || (part == 2 ? has_mod : has_up);
    const int blk = !has_w ? 0 : (part == 0 ? cb0 : (part == 1 ? xcd * 8 + cb1 : (part == 2 ? mi : ui)));
    T.e[2 * bid] = (u32)(c[part] | nl << 7 | blk << 13 | (has_w ? 1 : 0) << 19 | (part == 0 ? wd : 0) << 20) | (u32)tw[part] << 24;
    T.e[2 * bid + 1] = (u32)(span[part] * P.TS / (4 * (nl - 1)) | span[part] * (1024 - P.TS) / (4 * (nl - 1)) << 16);
  }
  return T;
}
constexpr int step_range(const Prm& P, bool mx_) {
  int mx = 0, mn = 1 << 20;
  const Tab T = make_tab(P);
  for (int bid = 0; bid < 256; ++bid) { const int n = T.e[2 * bid] >> 7 & 63; mx = n > mx ? n : mx; mn = n < mn ? n : mn; }
  return mx_ ? mx : mn;
}
extern "C" __attribute__((used, visibility("protected"))) __constant__ const Tab wg_tab = make_tab(PRM0);
static_assert(step_range(PRM0, true) <= NL && step_range(PRM0, false) >= NCMIN / 8, "a wave's key range does not fit its load steps");
static_assert(UPREP + 1 < SK0 && QP + QD + 1 < SK0 && cons_end(SK0 - 1) == SK0 - WV && NL - SK1 == WV, "steps every wave runs");
constexpr int NUP = FF / 32;
constexpr int SPIN_MAX = 1 << 17;

constexpr u32 WS_EPOCH = 0;
constexpr u32 G_TB = 1024;
constexpr u32 G_MOD = 4096;
constexpr u32 G_Q = G_MOD + 3072 * 8;
constexpr u32 G_ML = G_Q + 4096 * 8;
constexpr u32 G_U = G_ML + 256 * 16;
constexpr u32 G_P = 0;
constexpr u32 G_XP = G_P + 256 * 256;
constexpr u32 G_OP = G_XP + 8 * 512 * 4;
constexpr u32 G_NT = G_OP + 43 * 512 * 8;
constexpr u32 G_PB = G_OP + 44 * 512 * 8;
constexpr u32 G_VT = G_U + 2 * (G_PB + 8192);
constexpr u32 WS_END = G_VT + 1024;
__device__ __forceinline__ u32 dup(u32 o) { return G_U + ((o >> 13) << 14) + (o & 8191u); }
constexpr u32 SENT = 0xffffffffu;
constexpr u32 OOB = 0x40000000u;
#define WST 0

constexpr int AUX_NT = 2, AUX_SC1 = 16;

__device__ __forceinline__ float lo_f(u32 v) { return __uint_as_float(v << 16); }
__device__ __forceinline__ float hi_f(u32 v) { return __uint_as_float(v & 0xffff0000u); }
__device__ __forceinline__ float rbf(float x) { return (float)(__bf16)x; }
__device__ __forceinline__ u32 pack_bf(float lo, float hi) {
  bf16x2 r; r[0] = (__bf16)lo; r[1] = (__bf16)hi; return __builtin_bit_cast(u32, r);
}
__device__ __forceinline__ float dot2(u32 k, u32 q, float acc) {
  return __builtin_amdgcn_fdot2_f32_bf16(__builtin_bit_cast(bf16x2, k), __builtin_bit_cast(bf16x2, q), acc, false);
}
template <int CTRL> __device__ __forceinline__ float dpp(float x) {
  return __builtin_bit_cast(float, __builtin_amdgcn_mov_dpp(__builtin_bit_cast(int, x), CTRL, 0xf, 0xf, true));
}
constexpr int XOR1 = 0xB1, XOR2 = 0x4E, XOR7 = 0x141, XOR8 = 0x128;
__device__ __forceinline__ float swap32_add(float a, float b) {
  auto t = __builtin_amdgcn_permlane32_swap(__float_as_uint(a), __float_as_uint(b), false, false);
  return __uint_as_float(t[0]) + __uint_as_float(t[1]);
}
__device__ __forceinline__ float swap16_add(float a, float b) {
  auto t = __builtin_amdgcn_permlane16_swap(__float_as_uint(a), __float_as_uint(b), false, false);
  return __uint_as_float(t[0]) + __uint_as_float(t[1]);
}
__device__ __forceinline__ float wave_sum(float x) {
  x += dpp<XOR1>(x); x += dpp<XOR2>(x); x += dpp<XOR7>(x); x += dpp<XOR8>(x);
  x = swap16_add(x, x);
  return swap32_add(x, x);
}
__device__ __forceinline__ float wave_max(float x) {
  x = fmaxf(x, dpp<XOR1>(x)); x = fmaxf(x, dpp<XOR2>(x)); x = fmaxf(x, dpp<XOR7>(x)); x = fmaxf(x, dpp<XOR8>(x));
  auto s = __builtin_amdgcn_permlane16_swap(__float_as_uint(x), __float_as_uint(x), false, false);
  x = fmaxf(__uint_as_float(s[0]), __uint_as_float(s[1]));
  auto t = __builtin_amdgcn_permlane32_swap(__float_as_uint(x), __float_as_uint(x), false, false);
  return fmaxf(__uint_as_float(t[0]), __uint_as_float(t[1]));
}
template <int CTRL> __device__ __forceinline__ float sel_add(float lo, float hi, bool up) {
  const float t1 = lo + dpp<CTRL>(lo), t2 = hi + dpp<CTRL>(hi);
  return up ? t2 : t1;
}
template <int I> __device__ __forceinline__ float bc16(float x) {
  return __builtin_bit_cast(float, __builtin_amdgcn_update_dpp(0, __builtin_bit_cast(int, x), 0x150 + I, 0xf, 0xf, false));
}
__device__ __forceinline__ float bcast16(float x, int i) {
  switch (i) {
    case 0: return bc16<0>(x); case 1: return bc16<1>(x); case 2: return bc16<2>(x); case 3: return bc16<3>(x);
    case 4: return bc16<4>(x); case 5: return bc16<5>(x); case 6: return bc16<6>(x); case 7: return bc16<7>(x);
    case 8: return bc16<8>(x); case 9: return bc16<9>(x); case 10: return bc16<10>(x); case 11: return bc16<11>(x);
    case 12: return bc16<12>(x); case 13: return bc16<13>(x); case 14: return bc16<14>(x); default: return bc16<15>(x);
  }
}
__device__ __forceinline__ float silu_f(float x) { return x * __builtin_amdgcn_rcpf(1.f + __expf(-x)); }

template <int AUX> __device__ __forceinline__ u32x4 ld16(rsrc_t r, u32 voff, u32 soff) {
  return __builtin_amdgcn_raw_buffer_load_b128(r, voff, soff, AUX);
}
template <int AUX> __device__ __forceinline__ u32x2 ld8(rsrc_t r, u32 voff, u32 soff) {
  return __builtin_amdgcn_raw_buffer_load_b64(r, voff, soff, AUX);
}
__device__ __forceinline__ void st8_sc1(rsrc_t r, u32 voff, u32 v, u32 tag) {
  __builtin_amdgcn_raw_buffer_store_b64(u32x2{v, tag}, r, voff, 0, AUX_SC1);
}
__device__ __forceinline__ void st16_sc1(rsrc_t r, u32 voff, u32 v0, u32 v1, u32 tag) {
  __builtin_amdgcn_raw_buffer_store_b128(u32x4{v0, tag, v1, tag}, r, voff, 0, AUX_SC1);
}
__device__ __forceinline__ rsrc_t mk_rsrc(const void* p, u32 bytes) {
  return __builtin_amdgcn_make_buffer_rsrc(const_cast<void*>(p), 0, bytes, 0x00020000);
}
#define WAIT_VM(n) __builtin_amdgcn_s_waitcnt(((n) & 15) | (((n) >> 4) << 14) | (7 << 4) | (15 << 8))
#define SB() __builtin_amdgcn_sched_barrier(0)
#define CFENCE() asm volatile("" ::: "memory")
__device__ __forceinline__ bool all_ok(bool ok) { return __builtin_amdgcn_ballot_w64(!ok) == 0ull; }

template <int N> __device__ __forceinline__ void gather16(rsrc_t ws, u32 voff, u32 stride, u32 tag, u32x4 (&g)[N]) {
#pragma nounroll
  for (int spin = 0; spin < SPIN_MAX; ++spin) {
    CFENCE();
    bool ok = true;
#pragma unroll
    for (int i = 0; i < N; ++i) g[i] = ld16<AUX_SC1>(ws, voff + i * stride, 0);
#pragma unroll
    for (int i = 0; i < N; ++i) ok &= (g[i][1] == tag) & (g[i][3] == tag);
    if (all_ok(ok)) break;
    __builtin_amdgcn_s_sleep(8);
  }
}
template <int N> __device__ __forceinline__ void gather8(rsrc_t ws, u32 voff, u32 stride, u32 tag, u32 (&g)[N], bool act = true) {
#pragma nounroll
  for (int spin = 0; spin < SPIN_MAX; ++spin) {
    CFENCE();
    bool ok = true;
    u32x2 t[N];
#pragma unroll
    for (int i = 0; i < N; ++i) t[i] = ld8<AUX_SC1>(ws, voff + i * stride, 0);
#pragma unroll
    for (int i = 0; i < N; ++i) { ok &= (t[i][1] == tag); g[i] = t[i][0]; }
    if (all_ok(ok | !act)) break;
    __builtin_amdgcn_s_sleep(8);
  }
}

struct Smem {
  float red[8][128];
  float ml[8][2];
  float r2[64 * 20];
  u16 hb[3 * 1024];
  float stat[8][64];
  float red2[8][128];
  u32 dummy[64];
  u32x2 bc;
  u32x4 zero[4];
  float pl[4][8];
  u32 mflag[8];
  u32x4 wbuf[8][8][64];
  __attribute__((aligned(256))) u16 w2b[32][512];
  u16 actl[2][32];
};

#define PROBE_K 8
#define PROBE_OP 0
#define PROBE_PART -1
#define STAMP(k) do { if ((k) == PROBE_K && tid == 0) pst = wall_clock64(); } while (0)
#define WSTAMP(k) do {} while (0)
#define FINISH() do { STAMP(8); if (tid == 0) { __hip_atomic_store(reinterpret_cast<u64*>(ws + (1u << 20)) + bid * 2, pt0, __ATOMIC_RELAXED, __HIP_MEMORY_SCOPE_AGENT); \
   __hip_atomic_store(reinterpret_cast<u64*>(ws + (1u << 20)) + bid * 2 + 1, (PROBE_PART < 0 || part == PROBE_PART) ? pst : 0ull, __ATOMIC_RELAXED, __HIP_MEMORY_SCOPE_AGENT); } \
   if (bid == 8 && wave == 0) probe_end(ws, pt0, lane); return; } while (0)
#define PIN(x) asm volatile("" : "+v"(x))
#define LDS_BARRIER() do { __builtin_amdgcn_s_waitcnt(0xc07f); __builtin_amdgcn_s_barrier(); CFENCE(); } while (0)
#define WAVE_SYNC() do { __builtin_amdgcn_fence(__ATOMIC_RELEASE, "wavefront"); __builtin_amdgcn_wave_barrier(); __builtin_amdgcn_fence(__ATOMIC_ACQUIRE, "wavefront"); } while (0)

__device__ __forceinline__ void dma16(rsrc_t r, u32 voff, u32 soff, u32 lds_dst) {
  u32 keep;
  asm volatile("s_mov_b32 %0, m0\n\ts_mov_b32 m0, %4\n\ts_nop 4\n\tbuffer_load_dwordx4 %1, %2, %3 offen nt lds\n\ts_mov_b32 m0, %0"
               : "=&s"(keep) : "v"(voff), "s"(r), "s"(soff), "s"(lds_dst) : "memory");
}
__device__ __forceinline__ void ld16_into(u32x4& d, rsrc_t r, u32 voff, u32 soff) {
  asm volatile("s_nop 4\n\tbuffer_load_dwordx4 %0, %1, %2, %3 offen nt" : "+v"(d) : "v"(voff), "s"(r), "s"(soff) : "memory");
}
__device__ __forceinline__ u32 perm_lo(u32 a, u32 b) { return __builtin_amdgcn_perm(b, a, 0x05040100u); }
__device__ __forceinline__ u32 perm_hi(u32 a, u32 b) { return __builtin_amdgcn_perm(b, a, 0x07060302u); }
__device__ __forceinline__ float rdl(float x, int l) { return __int_as_float(__builtin_amdgcn_readlane(__float_as_int(x), l)); }

typedef __attribute__((ext_vector_type(8))) __bf16 bf16x8;
__device__ __forceinline__ f32x4 mfma16(u32x4 a, u32x4 b, f32x4 c) {
  return __builtin_amdgcn_mfma_f32_16x16x32_bf16(__builtin_bit_cast(bf16x8, a), __builtin_bit_cast(bf16x8, b), c, 0, 0, 0);
}
__device__ __forceinline__ float sum8(const float* p) {
  const f32x4 a = *reinterpret_cast<const f32x4*>(p), b = *reinterpret_cast<const f32x4*>(p + 4);
  return ((a[0] + b[0]) + (a[1] + b[1])) + ((a[2] + b[2]) + (a[3] + b[3]));
}
__device__ __forceinline__ float ln_ri(float var) {
  return __builtin_amdgcn_rcpf(rbf(__builtin_amdgcn_sqrtf(rbf(rbf(fmaxf(var, 0.f)) + 1e-5f))));
}
__device__ __forceinline__ void ln_stats2(const float (&x0)[8], const float (&x1)[8], float& mb0, float& ri0, float& mb1, float& ri1) {
  const float k0 = rdl(x0[0], 0), k1 = rdl(x1[0], 0);
  float s0 = 0.f, s1 = 0.f, q0 = 0.f, q1 = 0.f;
#pragma unroll
  for (int k = 0; k < 8; ++k) {
    const float d0 = x0[k] - k0, d1 = x1[k] - k1;
    s0 += d0; s1 += d1; q0 = fmaf(d0, d0, q0); q1 = fmaf(d1, d1, q1);
  }
  float t = swap16_add(swap32_add(s0, s1), swap32_add(q0, q1));
  t += dpp<XOR1>(t); t += dpp<XOR2>(t); t += dpp<XOR7>(t); t += dpp<XOR8>(t);
  auto u = __builtin_amdgcn_permlane16_swap(__float_as_uint(t), __float_as_uint(t), false, false);
  const float ms = __uint_as_float(u[0]) * (1.f / 512.f), mq = __uint_as_float(u[1]) * (1.f / 512.f);
  const float ri = ln_ri(fmaf(-ms, ms, mq));
  mb0 = rbf(k0 + rdl(ms, 0)); mb1 = rbf(k1 + rdl(ms, 32));
  ri0 = rdl(ri, 0); ri1 = rdl(ri, 32);
}
template <int I> struct IC { static constexpr int v = I; };
template <int I, int N, class F> __device__ __forceinline__ void sfor(F&& f) {
  if constexpr (I < N) { if (f(IC<I>{})) sfor<I + 1, N>(f); }
}
typedef __attribute__((ext_vector_type(4))) short s16x4;
typedef __attribute__((address_space(3))) s16x4* lds_s16x4p;
__device__ __forceinline__ u32x2 tr_read(u32 lds_addr) {
  return __builtin_bit_cast(u32x2, __builtin_amdgcn_ds_read_tr16_b64_v4i16((lds_s16x4p)lds_addr));
}
__device__ __forceinline__ u32 next_tag(u32 e) { return ((e | 0x40004000u) + 1u) & ~0x40004000u; }
__device__ __forceinline__ float u2f(u32 v) { return __uint_as_float(v); }
#define LDSP(T, a) ((__attribute__((address_space(3))) T*)(a))
template <class T> __device__ __forceinline__ u32 lds_addr(T* p) { u32 a = (u32)(uintptr_t)p; PIN(a); return a; }

#define TAB_LOAD "s_add_u32 s30, s28, wg_tab@rel32@lo+4\ns_addc_u32 s31, s29, wg_tab@rel32@hi+12\nglobal_load_dwordx2 %[te], %[va], s[30:31]\n"
#define FC(k) ".Lfc" #k
#define FC_REGS
#define FX(r, bit, k) "s_bitcmp1_b32 %[" #r "], " #bit "\ns_cselect_b32 %[sb], " FC(k) ", 0\ns_xor_b32 %[sa], %[sa], %[sb]\n"
__device__ __forceinline__ void probe_end(unsigned char* ws, u64 pt0, int lane) {
  while (wall_clock64() < pt0 + 5500ull) __builtin_amdgcn_s_sleep(8);
  u64 a[4], c2[4];
  for (int i = 0; i < 4; ++i) {
    a[i] = __hip_atomic_load(reinterpret_cast<u64*>(ws + (1u << 20)) + (i * 64 + lane) * 2, __ATOMIC_RELAXED, __HIP_MEMORY_SCOPE_AGENT);
    c2[i] = __hip_atomic_load(reinterpret_cast<u64*>(ws + (1u << 20)) + (i * 64 + lane) * 2 + 1, __ATOMIC_RELAXED, __HIP_MEMORY_SCOPE_AGENT);
  }
  int mn0 = 1 << 30, mx1 = -(1 << 30), mn1 = 1 << 30, sum = 0, cnt = 0;
  for (int i = 0; i < 4; ++i) {
    const int d0 = (int)(a[i] - pt0);
    mn0 = min(mn0, d0);
    if (c2[i] != 0ull) { const int d1 = (int)(c2[i] - pt0); mx1 = max(mx1, d1); mn1 = min(mn1, d1); sum += d1; cnt += 1; }
  }
  for (int o = 1; o < 64; o <<= 1) { mn0 = min(mn0, __shfl_xor(mn0, o)); mx1 = max(mx1, __shfl_xor(mx1, o)); mn1 = min(mn1, __shfl_xor(mn1, o)); sum += __shfl_xor(sum, o); cnt += __shfl_xor(cnt, o); }
  int X = PROBE_OP == 0 ? mx1 : (PROBE_OP == 1 ? sum / max(cnt, 1) : mn1);
  if (PROBE_K == 102) X = mn0;
  X = mn0 + (X - mn0) * PROBE_MUL;
  const u64 tg = pt0 + (u64)(long long)(X + 8000);
  for (int i = 0; i < 400000 && wall_clock64() < tg; ++i) __builtin_amdgcn_s_sleep(1);
}
__global__ void __launch_bounds__(512, 2) fused_kernel(const u16* kc_, const u16* vc_, const u16* wo_, const u16* wq_, const u16* wmod_, const u16* w1_, const u16* w3_,
                                                       unsigned char* ws_, const u16* x_, const u16* cnd_, const u16* w2_, u16* out_) {
  __shared__ __attribute__((aligned(256))) Smem sm;
  const u64 a_ka = (u64)__builtin_amdgcn_kernarg_segment_ptr();
  const int bid = blockIdx.x, tid = threadIdx.x;
  u64 wa, t64; u32x2 te; int tid0;
  u32 ar[14];
  u32 tch2, sa_, sb_, va_;
  u32x4 kr[NL], vr[NL];
  asm volatile(".set .Lfc0, %0\n.set .Lfc1, %1\n.set .Lfc2, %2\n.set .Lfc3, %3\n.set .Lfc4, %4\n.set .Lfc5, %5\n.set .Lfc6, %6\n.set .Lfc7, %7\n.set .Lfc8, %8\n.set .Lfc9, %9\n.set .Lfc10, %10\n.set .Lfc11, %11"
    :: "n"(first_col(PRM0, 0)), "n"(first_col(PRM0, 1)), "n"(first_col(PRM0, 2)), "n"(first_col(PRM0, 3)), "n"(first_col(PRM0, 4)), "n"(first_col(PRM0, 5)),
       "n"(first_col(PRM0, 6)), "n"(first_col(PRM0, 7)), "n"(first_col(PRM0, 8)), "n"(first_col(PRM0, 9)), "n"(first_col(PRM0, 10)), "n"(first_col(PRM0, 11)));
  asm volatile(
    "s_load_dwordx16 s[4:19], %[ka], 0x0\ns_load_dwordx8 s[20:27], %[ka], 0x40\n"
    "s_memrealtime %[t]\n"
    "v_lshlrev_b32 %[va], 3, %[bid]\n"
    "s_getpc_b64 s[28:29]\n.Lcpf_first:\n"
    TAB_LOAD
    "s_cmp_gt_u32 %[bid], 15\ns_cbranch_scc1 .Lno_cpf\n"
    "s_mov_b32 %[sa], .Lend_a-.Lcpf_first\ns_max_i32 %[sa], %[sa], .Lend_b-.Lcpf_first\ns_max_i32 %[sa], %[sa], .Lend_c-.Lcpf_first\ns_max_i32 %[sa], %[sa], .Lend_d-.Lcpf_first\ns_max_i32 %[sa], %[sa], .Lend_e-.Lcpf_first\n"
    "v_lshlrev_b32 %[va], 6, %[tid]\nv_min_u32 %[va], %[sa], %[va]\nglobal_load_dword %[pf], %[va], s[28:29]\n"
    ".Lno_cpf:\n"
    "v_readfirstlane_b32 %[t0], %[tid]\nv_and_b32 %[va], 63, %[tid]\n"
    "s_mov_b32 %[sa], " FC(11) "\n"
    FX(bid, 0, 0) FX(bid, 1, 1) FX(bid, 2, 2) FX(bid, 3, 3) FX(bid, 4, 4) FX(bid, 5, 5) FX(bid, 6, 6) FX(bid, 7, 7) FX(t0, 6, 8) FX(t0, 7, 9) FX(t0, 8, 10)
    "v_lshl_add_u32 %[va], %[va], 4, %[sa]\n"
    "s_waitcnt lgkmcnt(0)\nglobal_load_dwordx4 %[k0], %[va], s[4:5] nt\nglobal_load_dwordx4 %[v0], %[va], s[6:7] nt\n"
    "s_bitcmp1_b32 %[t0], 6\ns_cselect_b64 s[14:15], s[16:17], s[14:15]\n"
    "s_bitcmp1_b32 %[bid], 4\ns_cselect_b64 s[8:9], s[10:11], s[8:9]\ns_cselect_b64 s[12:13], s[14:15], s[12:13]\ns_bitcmp1_b32 %[bid], 5\ns_cselect_b64 %[wa], s[12:13], s[8:9]"
    : "=&{s4}"(ar[0]), "=&{s5}"(ar[1]), "=&{s6}"(ar[2]), "=&{s7}"(ar[3]), "=&{s18}"(ar[4]), "=&{s19}"(ar[5]), "=&{s20}"(ar[6]), "=&{s21}"(ar[7]), "=&{s22}"(ar[8]), "=&{s23}"(ar[9]),
      "=&{s24}"(ar[10]), "=&{s25}"(ar[11]), "=&{s26}"(ar[12]), "=&{s27}"(ar[13]), [wa] "=&s"(wa), [t0] "=&s"(tid0), [t] "=&s"(t64), [te] "=&v"(te), [sa] "=&s"(sa_), [sb] "=&s"(sb_),
      [va] "=&v"(va_), [pf] "=&v"(tch2), [k0] "=&v"(kr[0]), [v0] "=&v"(vr[0])
    : [ka] "s"(a_ka), [bid] "s"(bid), [tid] "v"(tid) : "s8", "s9", "s10", "s11", "s12", "s13", "s14", "s15", "s16", "s17", "s28", "s29", "s30", "s31", "scc", "memory" FC_REGS);
  asm volatile("" : "+s"(ar[0]), "+s"(ar[1]), "+s"(ar[2]), "+s"(ar[3]), "+s"(ar[4]), "+s"(ar[5]), "+s"(ar[6]), "+s"(ar[7]), "+s"(ar[8]), "+s"(ar[9]), "+s"(ar[10]), "+s"(ar[11]), "+s"(ar[12]), "+s"(ar[13]));
  asm volatile("" : "+s"(wa), "+s"(t64), "+s"(tid0), "+s"(sa_));
  const u32 tka = (u32)wall_clock64();
  const u64 pt0 = t64; u64 pst = (PROBE_K == 100 || PROBE_K == 102) ? pt0 : (PROBE_K == 101 ? pt0 + (u32)(tka - (u32)t64) : 0);
  const u32 t0 = tka;
  __builtin_assume(tid >= 0 && tid < 512 && tid0 >= 0 && tid0 < 512 && bid >= 0 && bid < 256);
  constexpr Prm P = PRM0;
  const int TEND = P.TEND;
  const int xcd = bid & 7, part = bid >> 4 & 3;
  const int lane = tid & 63, wave = tid0 >> 6;
  SB();
  if ((bid >> 4) == 3 && P.TT) {
    const int j = min((bid & 8) + wave, 9);
    const u64 p = ((__attribute__((address_space(4))) const u64*)a_ka)[2 + j];
    tch2 = __builtin_amdgcn_raw_buffer_load_b32(mk_rsrc((const void*)p, (j < 2 ? 4u << 20 : (j == 2 ? 3u << 20 : ((0x118 >> j & 1) ? FF * 1024u : 2048u)))), lane << 16, 512, AUX_NT);
  }
  const bool p3 = part == 3;
  u32 pitch = (u32)(0x005600c001000020ull >> (part * 16) & 0xffff) * 32u;
  const int sh = p3 ? 2 : 3;
  u32 wv0 = (u32)((p3 ? (wave >> 1) * 128 : wave * 64) + (part == 0 ? xcd * 512 : 0) + (lane >> sh)) * pitch + ((lane & (p3 ? 3 : 7)) ^ (lane >> (p3 ? 5 : 3))) * 16u;
  const u32 wimg = (u32)(uintptr_t)(&sm.wbuf[0][0][0]);
  u32 wsz = (part == 0 ? 4096u : 512u) * pitch, wld = wimg + (p3 ? (wave & 1) * 32768 + (wave >> 1) * 8192 : wave * 8192);
  PIN(wv0);
  asm volatile("" : "+s"(wsz), "+s"(wld), "+s"(pitch));
  auto karg = [&](int i) { return ar[2 * i] | (u64)ar[2 * i + 1] << 32; };
  const u64 a_kc = karg(0), a_vc = karg(1);
  unsigned char* ws = (unsigned char*)karg(2);
  const u16 *x = (const u16*)karg(3), *cnd = (const u16*)karg(4), *w2 = (const u16*)karg(5);
  u16* out = (u16*)karg(6);
  const rsrc_t wsr = mk_rsrc(ws, WS_END);
  asm volatile("s_waitcnt vmcnt(3)\n\ts_cmp_eq_u32 %1, 3\n\ts_cbranch_scc1 1f\n\ts_waitcnt vmcnt(2)\n1:" : "+v"(te) : "s"(P.TT ? bid >> 4 : 0) : "scc");
  const u32 te0 = __builtin_amdgcn_readfirstlane(te[0]), te1 = __builtin_amdgcn_readfirstlane(te[1]);
  const int bl0 = te0 & 127, nl = te0 >> 7 & 63, blk = te0 >> 13 & 63;
  const bool has_w = te0 >> 19 & 1;
  const rsrc_t rw = mk_rsrc((const void*)wa, wsz);
  const u32 wstep = pitch << (6 - sh), wflip = p3 ? 32u : 0u;
  u32 wso = blk << (sh + 4);
  int wleft = 8;
  auto piece = [&]() {
    dma16(rw, wv0, wso, wld);
    wso += wstep; wld += 1024;
    wv0 ^= (wleft & 1) ? wflip : 0u;
    --wleft;
  };
  SB();
  STAMP(23);
  const int b = bid >> 3 & 1, head = xcd * 4 + (bid >> 6 ^ 3);
  const int g = lane >> 4, c = lane & 15;
  const int i8 = b | bid >> 6 << 1, wi = i8 * 8 + xcd;
  const bool is_up = p3 && has_w, modc = part == 2 && wi < 16;
  const size_t base = ((size_t)(b * NH + head) * SK + (SK - WIN)) * DH;
  const rsrc_t rk = mk_rsrc((const u16*)a_kc + base, WIN * DH * 2), rv = mk_rsrc((const u16*)a_vc + base, WIN * DH * 2);
  int tau = te1 & 0xffff;
  const int tauw = te0 >> 24 << 7, wdef = te0 >> 20 & 15;
  int due = P.NF * 256, now = 0;
  auto gate = [&]() {
    while (now < due) { now = (int)(((u32)wall_clock64() - t0) << 8); if (now < due) __builtin_amdgcn_s_sleep(1); }
    due += tau;
  };
  auto until = [&](int t) { while ((int)(((u32)wall_clock64() - t0) << 8) < t); };
  const u32 pslot = dup(G_P + head * 2048 + (b * 4 + part) * 16);
  const u32 die = __builtin_amdgcn_s_getreg(20 | (2 << 6));
  const bool voter = p3 && i8 == 1, reporter = voter, timer = p3 && i8 < 2;
  int vote = 0, lag = 0;
  const u32 pbo = dup(G_PB) + (xcd * 8 + wave) * 128;
  u32 near = 0;
  u32x2 ep; u32 xs0, xs1, tch1; u32x4 xr0, xr1;
  const rsrc_t rx = mk_rsrc(part == 2 ? cnd : x, (part == 1 || (part == 2 && has_w)) ? 2048u : 0u);
  auto small_ops = [&]() {
    ep = ld8<AUX_SC1>(wsr, wave == 7 ? WS_EPOCH : OOB, 0);
    xs0 = __builtin_amdgcn_raw_buffer_load_b16(rx, tid * 2, 0, 0); xs1 = __builtin_amdgcn_raw_buffer_load_b16(rx, tid * 2, 1024, 0);
    xr0 = ld16<0>(rx, part == 1 ? lane * 16 : OOB, 0); xr1 = ld16<0>(rx, part == 1 ? lane * 16 : OOB, 1024);
    tch1 = __builtin_amdgcn_raw_buffer_load_b32(mk_rsrc(w2 + (size_t)blk * 32 * 512, is_up ? 64u : 0u), tid == 384 ? 0 : OOB, 0, AUX_NT);
  };
  if (has_w) {
    if (modc) now = 1 << 30;
#pragma nounroll
    while (wleft > wdef) { gate(); due += tauw - tau; piece(); }
    now = 0;
  }
  due = max(due, P.TK * 256);
  SB();
  u32 sot = sa_ & 0xffc00u;
  {
    const u32 s1 = (bl0 + lane - 1 - (lane >= SK0 ? NL - nl : 0)) ^ (124 + part);
#pragma unroll
    for (int k = 0; k < 7; ++k) sot ^= (u32)-(int)(s1 >> k & 1u) & col(P, k);
    PIN(sot);
  }
  u32 tag = 0;
  u32x4 bf[8];
  auto wo_prep = [&](int s0 = 0, int s1 = 2) {
    const u32 wi_ = wimg + wave * 8192;
    const int q = (lane >> 2) & 3, pp = lane & 3;
#pragma unroll
    for (int s = s0; s < s1; ++s) {
#pragma unroll
      for (int t = 0; t < 4; ++t) {
        const int r0 = s * 32 + g * 8 + q, r1 = r0 + 4, ch = t * 2 + (pp >> 1);
        const u32x2 lo = tr_read(wi_ + r0 * 128 + ((ch ^ (r0 & 7)) * 16) + (pp & 1) * 8), hi = tr_read(wi_ + r1 * 128 + ((ch ^ (r1 & 7)) * 16) + (pp & 1) * 8);
        bf[s * 4 + t] = u32x4{lo[0], lo[1], hi[0], hi[1]};
      }
    }
  };
  auto gemv_go = [&](u32 hh) {
    sm.hb[tid] = (u16)hh; sm.hb[512 + tid] = (u16)(hh >> 16);
    WAVE_SYNC();
    const u32x4* a = (c & 11) == 0 ? reinterpret_cast<const u32x4*>(&sm.hb[(c >> 2) * 512 + wave * 64 + g * 8]) : &sm.zero[0];
    const u32x4 a0 = a[0], a1 = a[(c & 11) == 0 ? 4 : 0];
    const f32x4 z = {0.f, 0.f, 0.f, 0.f};
    f32x4 d[4];
#pragma unroll
    for (int t = 0; t < 4; ++t) d[t] = mfma16(a0, bf[t], z);
#pragma unroll
    for (int t = 0; t < 4; ++t) d[t] = mfma16(a1, bf[4 + t], d[t]);
    if (lane < 32) {
#pragma unroll
      for (int t = 0; t < 4; ++t) sm.r2[(t * 16 + c) * 20 + g * 8 + wave] = d[t][0];
    }
  };
  auto mod_stage = [&]() {
    wo_prep();
    gemv_go(pack_bf(silu_f(lo_f(xs0)), silu_f(lo_f(xs1))));
    __builtin_amdgcn_s_waitcnt(0xc07f);
    if (lane == 0) *LDSP(volatile u32, (u32)(uintptr_t)&sm.mflag[wave]) = 1u;
    if (wave == 7) {
#pragma nounroll
      for (int spin = 0; spin < SPIN_MAX; ++spin) {
        const u32 f = *LDSP(volatile u32, (u32)(uintptr_t)&sm.mflag[lane & 7]);
        if (all_ok(f != 0u)) break;
        __builtin_amdgcn_s_sleep(1);
      }
      CFENCE();
      st8_sc1(wsr, G_MOD + (blk * 64 + lane) * 8, pack_bf(sum8(&sm.r2[lane * 20]), sum8(&sm.r2[lane * 20 + 8])), tag);
    }
    STAMP(9);
  };
  if (lane < 4) sm.zero[lane] = u32x4{0u, 0u, 0u, 0u};
  if (part == 2 && has_w) {
    if (tid < 8) sm.mflag[tid] = 0u;
    LDS_BARRIER();
    if (__builtin_expect(modc, 0)) {
      small_ops();
      __builtin_amdgcn_raw_buffer_store_b128(u32x4{SENT, SENT, SENT, SENT}, wsr, G_U + wi * 10240 + tid * 16, 0, AUX_SC1);
      __builtin_amdgcn_raw_buffer_store_b128(u32x4{SENT, SENT, SENT, SENT}, wsr, tid < 128 ? G_U + wi * 10240 + tid * 16 : OOB, 8192, AUX_SC1);
      asm volatile("s_waitcnt vmcnt(0)" ::: "memory");
      tag = next_tag(__builtin_amdgcn_readfirstlane(ep[0]));
      mod_stage();
      __builtin_amdgcn_s_barrier();
    }
  }
  SB();
  const int fd = wi * 8 + (lane & 3) * 2;
  const int ub = wave >> 2, ud = (wave & 3) * 128 + c * 8 + g * 2;
  u32x4 sg[3]; u32 sx2;
  auto side_loads = [&]() {
    const bool fin = part == 1 && wave == 0;
    sg[0] = ld16<AUX_SC1>(wsr, is_up ? G_MOD + (1024 + ud) * 8 : (fin ? G_MOD + (1024 + fd) * 8 : OOB), 0);
    sg[1] = ld16<AUX_SC1>(wsr, is_up ? G_MOD + (1536 + ud) * 8 : (fin ? G_MOD + (2560 + fd) * 8 : OOB), 0);
    sg[2] = ld16<AUX_SC1>(wsr, is_up ? G_MOD + (2048 + ud) * 8 : OOB, 0);
    sx2 = __builtin_amdgcn_raw_buffer_load_b32(mk_rsrc(x, (is_up || fin) ? 2048u : 0u), is_up ? ub * 1024 + ud * 2 : (g & 1) * 1024 + fd * 2, 0, 0);
  };
  auto side_pin = [&]() { PIN(sg[0]); PIN(sg[1]); PIN(sg[2]); PIN(sx2); };
  const u32 mlo = G_ML + (head * 2 + b) * 64 + (lane & 3) * 16;
  float fmrg = 0.f;
  auto merge_factor = [&](const u32x4& ml) {
    const float mi = u2f(ml[0]);
    float M = fmaxf(mi, dpp<XOR1>(mi)); M = fmaxf(M, dpp<XOR2>(M));
    const float e = __expf(mi - M);
    float L = e * u2f(ml[2]);
    L += dpp<XOR1>(L); L += dpp<XOR2>(L);
    fmrg = rdl(e * __builtin_amdgcn_rcpf(L), part);
  };
  f32x2 ga = {0.f, 0.f}, xin = {0.f, 0.f}, cc1 = {0.f, 0.f};
  float Af = 0.f, Bf = 0.f;
  const int hpos = ub * 512 + ud;
  auto up_prep = [&]() {
    if (!all_ok((sg[0][1] == tag) & (sg[0][3] == tag) & (sg[1][1] == tag) & (sg[1][3] == tag) & (sg[2][1] == tag) & (sg[2][3] == tag))) gather16<3>(wsr, G_MOD + (1024 + ud) * 8, 512 * 8, tag, sg);
    ga = f32x2{ub ? hi_f(sg[0][0]) : lo_f(sg[0][0]), ub ? hi_f(sg[0][2]) : lo_f(sg[0][2])};
    cc1 = f32x2{rbf(1.f + (ub ? hi_f(sg[2][0]) : lo_f(sg[2][0]))), rbf(1.f + (ub ? hi_f(sg[2][2]) : lo_f(sg[2][2])))};
    xin = f32x2{lo_f(sx2), hi_f(sx2)};
    u32* hp = reinterpret_cast<u32*>(&sm.hb[hpos]);
    hp[512] = pack_bf(cc1[0], cc1[1]);
    hp[1024] = ub ? perm_hi(sg[1][0], sg[1][2]) : perm_lo(sg[1][0], sg[1][2]);
  };
  auto up_frags = [&]() {
    const int pp = lane & 3;
    const u32 a = wimg + (pp & 1) * 32768 + ((pp >> 1) * 32 + g * 8 + ((lane >> 2) & 3)) * 64 + (((wave >> 1) ^ ((g & 1) | (pp & 2))) * 16) + (wave & 1) * 8;
#pragma unroll
    for (int rb = 0; rb < 8; ++rb) {
      const u32x2 lo = tr_read(a + rb * 4096), hi = tr_read(a + rb * 4096 + 256);
      bf[rb] = u32x4{lo[0], lo[1], hi[0], hi[1]};
    }
  };
  const u32x4* ap = reinterpret_cast<const u32x4*>(&sm.hb[((lane >> 3) & 1) * 512 + (lane & 1) * 32 + g * 8]);
  const bool par = lane & 8;
  auto limit = [&](auto sc) {
    constexpr int s = sc.v;
    WAIT_VM(window(s, 1 + (PRM0.W[s >> 3] >> (4 * (s & 7)) & 7)));
    SB();
  };
  float l0 = 0.f, l1 = 0.f;
  u32x2 gmd[2], tbg;
  int t1 = 0;
#pragma unroll
  for (int i = SK0; i < SK1; ++i) kr[i] = u32x4{0u, 0u, 0u, 0u};
  const int first = SK0 + NL - nl;
  auto left_out = [&](int i) { int f = first; asm volatile("" : "+s"(f)); return i < f; };
  sfor<1, NL>([&](auto ic) {
    constexpr int i = ic.v;
    if constexpr (i >= SK0 && i < SK1) { if (left_out(i)) return true; }
    const u32 so = __builtin_amdgcn_readlane(sot, i);
    gate();
    if constexpr (i >= TBS && i <= TBP) { if (timer) lag += (int)(((u32)wall_clock64() - t0) << 8) - due + ((int)__builtin_amdgcn_s_getreg(7 | (3 << 11)) + 1) * tau; }
    SB();
    if constexpr (i >= SK0 && i < SK1) ld16_into(kr[i], rk, lane * 16, so); else kr[i] = ld16<AUX_NT>(rk, lane * 16, so);
    if constexpr (i == SMO) small_ops();
    if constexpr (i == SMO + 1) { if (voter) __builtin_amdgcn_raw_buffer_store_b32(0u, wsr, pbo + (lane >> 5) * 8192 + (lane & 31) * 4, 0, AUX_SC1); }
    if constexpr (i == TBL) tbg = ld8<AUX_SC1>(wsr, wave == 7 ? G_TB + xcd * 128 + c * 8 : OOB, 0);
    if constexpr (i == QP) { gmd[0] = ld8<AUX_SC1>(wsr, part == 1 ? G_MOD + tid * 8 : OOB, 0); gmd[1] = ld8<AUX_SC1>(wsr, part == 1 ? G_MOD + tid * 8 : OOB, 4096); }
    SB();
    SB();
    if (i >= 4) limit(ic);
    if (i == 1) STAMP(25);
    if (i == 8) STAMP(12);
    if (i == 20) STAMP(21);
    if (i == 32) STAMP(22);
    if (i == TBP) {
      if (timer) {
        if (lane == 0) sm.mflag[wave] = lag;
        LDS_BARRIER();
        if (wave == 7) {
          int s = sm.mflag[lane & 7];
          s += __builtin_amdgcn_mov_dpp(s, XOR1, 0xf, 0xf, true); s += __builtin_amdgcn_mov_dpp(s, XOR2, 0xf, 0xf, true); s += __builtin_amdgcn_mov_dpp(s, XOR7, 0xf, 0xf, true);
          if (lane < 8) st8_sc1(wsr, G_TB + lane * 128 + wi * 8, t0 << 10 | (u32)min(s / (2048 * (TBP - TBS + 1)), 1023), tag);
        }
      }
      SB();
    }
    if (i == PRB) {
      if (voter) {
        u64 ta, tb, tc; u32 dm;
        const u32 pa = pbo + (wave & 1) * 8192;
        asm volatile("s_memtime %0\n\ts_waitcnt lgkmcnt(0)\n\ts_load_dword %3, %4, %5\n\ts_waitcnt lgkmcnt(0)\n\ts_memtime %1\n\ts_waitcnt lgkmcnt(0)\n\t"
                     "s_load_dword %3, %4, %6\n\ts_waitcnt lgkmcnt(0)\n\ts_memtime %2\n\ts_waitcnt lgkmcnt(0)"
                     : "=&s"(ta), "=&s"(tb), "=&s"(tc), "=&s"(dm) : "s"(ws), "s"(pa), "s"(pa ^ 8192u));
        const int d = min(max((int)(2 * (u32)tb - (u32)ta - (u32)tc), -500), 500);
        vote = ((wave ^ die) & 1) ? -d : d;
        STAMP(26);
      }
      SB();
    }
    if (i == TBU) {
      if (wave == 7) {
        const bool ok = tbg[1] == tag;
        float lf = ok ? (float)min(max((int)((t0 << 10 | 1023u) - tbg[0]) >> 10, -300), 300) : 0.f, nf = ok ? 1.f : 0.f, lg = ok ? (float)(tbg[0] & 1023u) : 0.f;
        lf += dpp<XOR1>(lf); nf += dpp<XOR1>(nf); lg += dpp<XOR1>(lg); lf += dpp<XOR2>(lf); nf += dpp<XOR2>(nf); lg += dpp<XOR2>(lg);
        lf += dpp<XOR7>(lf); nf += dpp<XOR7>(nf); lg += dpp<XOR7>(lg); lf += dpp<XOR8>(lf); nf += dpp<XOR8>(nf); lg += dpp<XOR8>(lg);
        const float rn = __builtin_amdgcn_rcpf(fmaxf(nf, 1.f));
        if (lane == 0) sm.bc = u32x2{tag, t0 - (u32)(int)(lf * rn) + (u32)min(max((int)((lg * rn - (float)P.GS) * (float)P.GB * 0.125f), 0), 150)};
      }
      LDS_BARRIER();
      const u32x2 bc = sm.bc;
      tag = __builtin_amdgcn_readfirstlane(bc[0]);
      t1 = (__builtin_amdgcn_readfirstlane((int)(bc[1] - t0)) + TEND + P.X1) * 256;
      tau -= __builtin_amdgcn_readfirstlane((int)((float)(int)(t0 - bc[1]) * 256.f * __builtin_amdgcn_rcpf((float)(nl - TBU - 1))));
      SB();
    }
    if (i == NL - 1) {
      if (reporter) {
        if (lane == 0) sm.mflag[wave] = vote;
        LDS_BARRIER();
        if (wave == 7) {
          int s = sm.mflag[lane & 7];
          s += __builtin_amdgcn_mov_dpp(s, XOR1, 0xf, 0xf, true); s += __builtin_amdgcn_mov_dpp(s, XOR2, 0xf, 0xf, true); s += __builtin_amdgcn_mov_dpp(s, XOR7, 0xf, 0xf, true);
          if (lane < 8) st8_sc1(wsr, G_VT + lane * 128 + xcd * 8, s, tag);
        }
      }
      SB();
    }
    if (i == LNS) {
      tag = next_tag(__builtin_amdgcn_readfirstlane(ep[0]));
      if (part == 1) {
        float x0[8], x1[8], mb0, ri0, mb1, ri1;
#pragma unroll
        for (int k = 0; k < 4; ++k) { x0[2 * k] = lo_f(xr0[k]); x0[2 * k + 1] = hi_f(xr0[k]); x1[2 * k] = lo_f(xr1[k]); x1[2 * k + 1] = hi_f(xr1[k]); }
        ln_stats2(x0, x1, mb0, ri0, mb1, ri1);
        l0 = rbf(rbf(lo_f(xs0) - mb0) * ri0); l1 = rbf(rbf(lo_f(xs1) - mb1) * ri1);
      }
      PIN(l0); PIN(l1);
      SB();
    }
    if (i == WUSE) {
      PIN(tch1); PIN(tch2);
      if ((part == 1 || (part == 2 && has_w && !modc))) {
        WAIT_VM(WK); CFENCE();
        if (part == 2) mod_stage(); else wo_prep();
      }
#pragma unroll
      for (int k = 0; k < 8; ++k) PIN(bf[k]);
      SB();
    }
    if (i == QP + QD) {
      if (part == 1) {
        u32 gm[2] = {gmd[0][0], gmd[1][0]};
        if (!all_ok((gmd[0][1] == tag) & (gmd[1][1] == tag))) { STAMP(11); gather8<2>(wsr, G_MOD + tid * 8, 512 * 8, tag, gm); }
        STAMP(5);
        gemv_go(pack_bf(rbf(l0 * rbf(1.f + lo_f(gm[1]))) + lo_f(gm[0]), rbf(l1 * rbf(1.f + hi_f(gm[1]))) + hi_f(gm[0])));
      }
      SB();
    }
    if (i == QP + QD + 1) {
      if (part == 1) {
        LDS_BARRIER();
        if (wave == 0) {
          const int bb = lane >> 5, jq = lane & 31;
          st8_sc1(wsr, G_Q + (bb * 2048 + blk * 32 + jq) * 8, pack_bf(sum8(&sm.r2[2 * jq * 20 + bb * 8]), sum8(&sm.r2[(2 * jq + 1) * 20 + bb * 8])), tag);
        }
        STAMP(6);
      }
      SB();
    }
    return true;
  });
  PIN(sot);
  tau = te1 >> 16;
  due += P.TG * 256;
  const u32 qoff = G_Q + ((b * 32 + head) * 64 + c * 4) * 8;
  u32x4 qg[2], mlg;
  u32 qq[4];
  constexpr int NB = (NL + 15) / 16;
  float xlo, sc[NB], pr[NB], m = 0.f;
  f32x4 acc = {0.f, 0.f, 0.f, 0.f};
  u32 pb[NB], pm[4], pl8[8], cpf;
  const u32 adst0 = lds_addr(lane < 32 ? &sm.red[wave][c * 8 + g * 4] : &sm.r2[(lane - 32) * 4]);
  u32 po, rdst, rsrc, xo;
  auto wo_addr = [&]() {
    po = dup(G_P + xcd * 8192) + tid * 16 + near;
    rdst = lane < 32 ? (u32)(uintptr_t)&sm.red2[wave][0] + lane * 16 : (u32)(uintptr_t)&sm.r2[0] + (lane - 32) * 16;
    rsrc = (u32)(uintptr_t)&sm.red2[0][((wave & 1) * 16 + (lane & 15)) * 4 + (lane >> 4)];
    xo = dup(G_XP + xcd * 2048 + (wave & 1) * 1024 + (blk * 64 + (lane & ~1)) * 2) + (lane & 1) * 8192;
    PIN(po); PIN(rdst); PIN(rsrc); PIN(xo);
  };
  u32 pso = (wave == 0 && (g & 1) == 0) ? pslot + (c >> 3) * 1024 + (c & 3) * 256 + (c >> 2 & 1) * 128 + (g >> 1) * 8192 : OOB;
  PIN(pso);
  const u32 nadd = wdef ? P.LW : (is_up ? P.LU : 0);
  u32 nto = tid < 2 ? dup(G_NT) + bid * 8 + tid * 8192 : OOB;
  PIN(nto);
  auto notices = [&]() -> int {
    u32 off = dup(G_NT) + near + lane * 16;
    PIN(off);
    until(due);
    int mx;
#pragma nounroll
    for (int spin = 0; spin < SPIN_MAX; ++spin) {
      CFENCE();
      const u32x4 ga_ = ld16<AUX_SC1>(wsr, off, 0), gb_ = ld16<AUX_SC1>(wsr, off, 1024);
      mx = max(max((int)(ga_[0] - t0), (int)(ga_[2] - t0)), max((int)(gb_[0] - t0), (int)(gb_[2] - t0)));
      if (all_ok((((ga_[1] ^ tag) | (ga_[3] ^ tag)) | ((gb_[1] ^ tag) | (gb_[3] ^ tag))) == 0u)) break;
      for (int k = 0; k < P.AG; ++k) __builtin_amdgcn_s_sleep(1);
    }
    if (all_ok(mx <= (t1 >> 8) - P.AC)) return t1;
    mx = max(mx, __builtin_amdgcn_mov_dpp(mx, XOR1, 0xf, 0xf, true)); mx = max(mx, __builtin_amdgcn_mov_dpp(mx, XOR2, 0xf, 0xf, true));
    mx = max(mx, __builtin_amdgcn_mov_dpp(mx, XOR7, 0xf, 0xf, true)); mx = max(mx, __builtin_amdgcn_mov_dpp(mx, XOR8, 0xf, 0xf, true));
    mx = max(max(__builtin_amdgcn_readlane(mx, 0), __builtin_amdgcn_readlane(mx, 16)), max(__builtin_amdgcn_readlane(mx, 32), __builtin_amdgcn_readlane(mx, 48)));
    return max(min(mx, 1 << 20) * 256, t1 + P.L0 * 256) + P.L1 * 256;
  };
  auto pv_row = [&](auto jc) {
    constexpr int i = jc.v;
    const u32 pv = __float_as_uint(bcast16(u2f(pb[i >> 4]), i & 15));
    acc = mfma16(u32x4{pv & pm[0], pv & pm[1], pv & pm[2], pv & pm[3]}, vr[i], acc);
    PIN(acc);
    return true;
  };
  sfor<0, NL - 1>([&](auto ic) {
    constexpr int v = ic.v;
    if constexpr (v >= SK0 && v < SK1) { if (left_out(v)) return true; }
    if constexpr (v > 0) {
      const u32 so = __builtin_amdgcn_readlane(sot, v);
      if constexpr (v >= SK1) { if (v == NL - P.NE) tau = (tau * P.FE) >> 4; }
      gate();
      SB();
      if constexpr (v >= SK0 && v < SK1) ld16_into(vr[v], rv, lane * 16, so); else vr[v] = ld16<AUX_NT>(rv, lane * 16, so);
      SB();
      SB();
    }
    if constexpr (v == SK0 - 1) {
#pragma unroll
      for (int i = SK0; i < SK1; ++i) { vr[i] = u32x4{0u, 0u, 0u, 0u}; PIN(vr[i]); }
    }
    if constexpr (v == VQ - 4) { qg[0] = ld16<AUX_SC1>(wsr, qoff, 0); qg[1] = ld16<AUX_SC1>(wsr, qoff + 16, 0); }
    if constexpr (v == VSIDE) { side_loads(); tbg = ld8<AUX_SC1>(wsr, wave == 7 ? G_VT + xcd * 128 + (lane & 7) * 8 : OOB, 0); }
    if constexpr (v == CPF) {
      u64 pc; u32 dlt, len;
      asm volatile("s_getpc_b64 %0\n.Lcpf_here:\n\ts_mov_b32 %1, .Ltail_start-.Lcpf_here\n\ts_mov_b32 %2, .Lend_a-.Ltail_start\n\ts_max_i32 %2, %2, .Lend_b-.Ltail_start\n\ts_max_i32 %2, %2, .Lend_c-.Ltail_start\n\ts_max_i32 %2, %2, .Lend_d-.Ltail_start\n\ts_max_i32 %2, %2, .Lend_e-.Ltail_start" : "=s"(pc), "=s"(dlt), "=s"(len) :: "scc");
      pc += dlt;
      const rsrc_t rc = mk_rsrc((const void*)pc, (part == 3 && i8 == 1) ? len : 0u);
      cpf = __builtin_amdgcn_raw_buffer_load_b32(rc, tid * 64, 0, 0);
    }
    if constexpr (v == SK1 + 2) PIN(cpf);
    if constexpr (v == SK1) mlg = ld16<AUX_SC1>(wsr, wave == 0 ? mlo : OOB, 0);
    SB();
    if constexpr (NL + v < 72) limit(IC<NL + v>{});
    if constexpr (v >= SK0 + WV) { WAIT_VM(v < SK1 ? WV : WV + 1); SB(); }
    if constexpr (v == VQ) {
      if (!all_ok((qg[0][1] == tag) & (qg[0][3] == tag) & (qg[1][1] == tag) & (qg[1][3] == tag))) { STAMP(20); gather16<2>(wsr, qoff, 16, tag, qg); }
      STAMP(1); WSTAMP(1);
      qq[0] = qg[0][0]; qq[1] = qg[0][2]; qq[2] = qg[1][0]; qq[3] = qg[1][2];
    }
    if constexpr (v >= VQ && v < VS) {
      constexpr int sl = v - VQ, bt = sl >> 1;
      float xs[8];
#pragma unroll
      for (int i = 0; i < 8; ++i) {
        float d = 0.f;
        if (sl * 8 + i < NL) {
#pragma unroll
          for (int k = 0; k < 4; ++k) d = dot2(kr[sl * 8 + i][k], qq[k], d);
        }
        xs[i] = d;
      }
#pragma unroll
      for (int h = 0; h < 4; ++h) xs[h] = sel_add<XOR7>(xs[h], xs[h + 4], c & 4);
#pragma unroll
      for (int h = 0; h < 2; ++h) xs[h] = sel_add<XOR2>(xs[h], xs[h + 2], c & 2);
      xs[0] = sel_add<XOR1>(xs[0], xs[1], c & 1);
      if constexpr ((sl & 1) || sl == NSL - 1) {
        const float t = (sl & 1) ? sel_add<XOR8>(xlo, xs[0], c & 8) : xs[0] + dpp<XOR8>(xs[0]);
        const float s = rbf(rbf(t) * 0.08838834764831845f);
        sc[bt] = (bt * 16 + c < SK0 || (bt * 16 + c >= first && bt * 16 + c < NL)) ? s : -__builtin_inff();
        PIN(sc[bt]);
      } else {
        xlo = xs[0];
        PIN(xlo);
      }
    }
    if constexpr (v == VS) {
      float mloc = sc[0];
#pragma unroll
      for (int bt = 1; bt < NB; ++bt) mloc = fmaxf(mloc, sc[bt]);
      m = fmaxf(wave_max(mloc), -3.0e38f);
      float ls = 0.f;
#pragma unroll
      for (int bt = 0; bt < NB; ++bt) { pr[bt] = __expf(sc[bt] - m); ls += pr[bt]; }
      const float l = wave_sum(ls);
      if (lane == 0) { sm.ml[wave][0] = m; sm.ml[wave][1] = l; }
      if (c == ((NL - 1) & 15)) sm.pl[g][wave] = pr[NB - 1];
      STAMP(2); WSTAMP(2);
    }
    if constexpr (v >= SK1) pv_row(IC<v - SK1 + SK0 - WV>{});
    if constexpr (v < SK0 || v >= SK0 + WV) sfor<cons_end(v - 1), cons_end(v)>(pv_row);
    if constexpr (v == SK1 + 1) {
      float p[8];
#pragma unroll
      for (int w = 0; w < 8; ++w) p[w] = sm.pl[g][w] * __expf(sm.ml[w][0] - m);
#pragma unroll
      for (int w = 0; w < 8; ++w) pl8[w] = pack_bf(p[w], p[w]);
#pragma unroll
      for (int w = 0; w < 8; ++w) PIN(pl8[w]);
    }
    if constexpr (v == MLS - 1) { side_pin(); if (is_up) up_prep(); PIN(ga); PIN(xin); PIN(cc1); }
    if constexpr (v == UPREP - 1) wo_addr();
    if constexpr (v == UPREP) {
#pragma unroll
      for (int k = 0; k < 8; ++k) asm volatile("" : "=v"(bf[k]));
      if (part == 0) wo_prep(); else if (is_up) up_frags();
#pragma unroll
      for (int k = 0; k < 8; ++k) PIN(bf[k]);
    }
    if constexpr (v == UPREP + 1) {
      if (is_up) {
        const u32x4* a1 = ap + 128 + ((lane >> 1) & 1) * 128;
        f32x4 d0 = {0.f, 0.f, 0.f, 0.f}, d1 = {0.f, 0.f, 0.f, 0.f};
#pragma unroll
        for (int rb = 0; rb < 4; ++rb) { d0 = mfma16(a1[rb * 8], bf[rb], d0); d1 = mfma16(a1[(rb + 4) * 8], bf[rb + 4], d1); }
        Af = par ? d0[1] + d1[1] : d0[0] + d1[0]; Bf = par ? d0[3] + d1[3] : d0[2] + d1[2];
        Af += dpp<XOR8>(Af); Bf += dpp<XOR8>(Bf);
      }
      PIN(Af); PIN(Bf);
    }
    if (v == 10 || v == 16) STAMP(15 + v / 6);
    if (v == 20) STAMP(18);
    if (v == 32) STAMP(19);
    if constexpr (v == MLS) {
      if (wave == 7) {
        int vt = tbg[1] == tag ? (int)tbg[0] : 0;
        vt += __builtin_amdgcn_mov_dpp(vt, XOR1, 0xf, 0xf, true); vt += __builtin_amdgcn_mov_dpp(vt, XOR2, 0xf, 0xf, true); vt += __builtin_amdgcn_mov_dpp(vt, XOR7, 0xf, 0xf, true);
        if (lane == 0) sm.bc[0] = vt;
      }
      LDS_BARRIER();
      near = ((__builtin_amdgcn_readfirstlane(sm.bc[0]) >> 31 ^ die ^ P.NR ^ 1) & 1) * 8192u;
      float mm[8], ll[8];
#pragma unroll
      for (int w = 0; w < 8; ++w) { const f32x2 t = *reinterpret_cast<const f32x2*>(&sm.ml[w][0]); mm[w] = t[0]; ll[w] = t[1]; }
      float Mwg = mm[0], Lwg = 0.f;
#pragma unroll
      for (int w = 1; w < 8; ++w) Mwg = fmaxf(Mwg, mm[w]);
      if (wave == 0) {
#pragma unroll
        for (int w = 0; w < 8; ++w) Lwg = fmaf(__expf(mm[w] - Mwg), ll[w], Lwg);
        if (lane == 0) st16_sc1(wsr, G_ML + ((head * 2 + b) * 4 + part) * 16, __float_as_uint(Mwg), __float_as_uint(Lwg), tag);
      }
      const float fw = __expf(m - Mwg);
      m = Mwg;
#pragma unroll
      for (int bt = 0; bt < NB; ++bt) { pb[bt] = pack_bf(pr[bt] * fw, pr[bt] * fw); PIN(pb[bt]); }
#pragma unroll
      for (int k = 0; k < 4; ++k) { pm[k] = (c >> 1 & 3) == k ? ((c & 1) ? 0xffff0000u : 0x0000ffffu) : 0u; PIN(pm[k]); }
    }
    SB();
    return true;
  });
  asm volatile(".Ltail_start:");
  if (wave != 0) {
    pv_row(IC<SK0 - 1>{});
    WAIT_VM(WV); SB();
    sfor<NL - 1 - WV, NL - 1>(pv_row);
    *LDSP(f32x4, adst0) = acc;
    WSTAMP(14);
    LDS_BARRIER();
  } else {
    u32x4 xr[8], am[8];
#pragma unroll
    for (int w = 0; w < 8; ++w) am[w] = u32x4{pl8[w] & pm[0], pl8[w] & pm[1], pl8[w] & pm[2], pl8[w] & pm[3]};
#pragma unroll
    for (int w = 0; w < 8; ++w) PIN(am[w]);
    {
      const u32 so = __builtin_amdgcn_readlane(sot, NL - 1);
      gate();
      SB();
#pragma unroll
      for (int w = 0; w < 8; ++w) xr[w] = ld16<AUX_NT>(rv, lane * 16, so ^ ((w & 1) ? col(P, 7) : 0u) ^ ((w & 2) ? col(P, 8) : 0u) ^ ((w & 4) ? col(P, 9) : 0u));
      SB();
    }
    u32 rsum = lds_addr(&sm.red[0][c * 8 + (g & 1) * 4]);
    pv_row(IC<SK0 - 1>{});
    WAIT_VM(WV + 8); SB();
    sfor<NL - 1 - WV, NL - 3>(pv_row);
    if (__builtin_expect(!all_ok((mlg[1] == tag) & (mlg[3] == tag)), 0)) {
#pragma nounroll
      for (int spin = 0; spin < SPIN_MAX; ++spin) {
        CFENCE();
        mlg = ld16<AUX_SC1>(wsr, mlo, 0);
        if (all_ok((mlg[1] == tag) & (mlg[3] == tag))) break;
      }
    }
    merge_factor(mlg);
    sfor<NL - 3, NL - 1>(pv_row);
    STAMP(14); WSTAMP(14);
    LDS_BARRIER();
    STAMP(15);
    f32x4 t[8];
#pragma unroll
    for (int w = 1; w < 8; ++w) t[w] = LDSP(f32x4, rsum)[w * 32];
    acc = ((acc + t[1]) + (t[2] + t[3])) + ((t[4] + t[5]) + (t[6] + t[7]));
    PIN(acc);
    STAMP(3);
    SB();
#pragma unroll
    for (int w = 0; w < 8; ++w) acc = mfma16(am[w], xr[w], acc);
  }
  WAIT_VM(0);
  acc *= fmrg;
  {
    const u32 w0 = min(pack_bf(acc[0], acc[1]), SENT - 1u), w1 = min(pack_bf(acc[2], acc[3]), SENT - 1u);
    const auto e0 = __builtin_amdgcn_permlane16_swap(w0, w0, false, false), e1 = __builtin_amdgcn_permlane16_swap(w1, w1, false, false);
    __builtin_amdgcn_raw_buffer_store_b128(u32x4{e0[0], e1[0], e0[1], e1[1]}, wsr, pso, 0, AUX_SC1);
  }
  st8_sc1(wsr, nto, (u32)wall_clock64() + nadd, tag);
  SB();
  STAMP(4);

  if (part == 2) FINISH();
  if (part == 0) {
    __builtin_amdgcn_s_barrier();
#pragma nounroll
    while (wleft > 0) piece();
    bool late = false;
    u32x4 og[2];
    if (wdef) {
      late = true;
      while ((int)(((u32)wall_clock64() - t0) << 8) < t1 - 20 * 256) if (__builtin_amdgcn_s_getreg(7 | (3 << 11)) == 0) { late = false; break; }
      if (__builtin_expect(late, 0)) { until(t1); CFENCE(); og[0] = ld16<AUX_SC1>(wsr, po, 0); SB(); WAIT_VM(1); } else { WAIT_VM(0); }
      CFENCE();
      if (P.WD > 4) wo_prep(0, 1);
      wo_prep(1, 2);
#pragma unroll
      for (int k = 0; k < 8; ++k) PIN(bf[k]);
      SB();
    }
    if (__builtin_expect(!late, 1)) { until(t1); CFENCE(); og[0] = ld16<AUX_SC1>(wsr, po, 0); }
    STAMP(11);
    SB();
    f32x4 d[4];
    auto wo_mm = [&]() {
      const f32x4 z = {0.f, 0.f, 0.f, 0.f};
#pragma unroll
      for (int t = 0; t < 4; ++t) { d[t] = mfma16(og[0], bf[t], z); og[1][t] = __float_as_uint(dpp<XOR8>(u2f(og[0][t]))); }
      SB();
#pragma unroll
      for (int t = 0; t < 4; ++t) d[t] = mfma16(og[1], bf[4 + t], d[t]);
    };
    auto p_bad = [&]() { return !all_ok(max(max(og[0][0], og[0][1]), max(og[0][2], og[0][3])) != SENT); };
    wo_mm();
    if (__builtin_expect(p_bad(), 0)) {
#pragma nounroll
      for (int spin = 0; spin < SPIN_MAX; ++spin) {
        __builtin_amdgcn_s_sleep(1);
        CFENCE();
        og[0] = ld16<AUX_SC1>(wsr, po, 0);
        if (!p_bad()) break;
      }
      wo_mm();
      SB();
      asm volatile(".Lend_d:");
    }
    STAMP(5); WSTAMP(5);
    {
      SB();
      f32x4 y; f32x2 h[4];
#pragma unroll
      for (int t = 0; t < 4; ++t) { h[t] = d[t].xy + d[t].zw; PIN(h[t]); }
#pragma unroll
      for (int t = 0; t < 4; ++t) { y[t] = h[t][0] + h[t][1]; PIN(y[t]); }
      *LDSP(f32x4, rdst) = y;
    }
    STAMP(16);
    LDS_BARRIER();
    STAMP(17);
    if (wave < 2) {
      f32x2 v[4];
#pragma unroll
      for (int w = 0; w < 4; ++w) v[w] = f32x2{LDSP(float, rsrc)[w * 256], LDSP(float, rsrc)[w * 256 + 128]};
      const f32x2 h = (v[0] + v[1]) + (v[2] + v[3]);
      const float y = h[0] + h[1];
      __builtin_amdgcn_raw_buffer_store_b32(min(pack_bf(dpp<0xA0>(y), dpp<0xF5>(y)), SENT - 1u), wsr, xo, 0, AUX_SC1);
      asm volatile(".Lend_a:");
    }
    STAMP(6);
    FINISH();
  }
  if (part == 3) {
    if (!has_w) FINISH();
    u32x4 b2[4];
    {
      __builtin_amdgcn_s_barrier();
      const rsrc_t r2 = mk_rsrc(w2 + (size_t)(blk * 32 + wave * 4) * 512, 4096u);
      const u32 l2 = (u32)(uintptr_t)(&sm.w2b[wave * 4][0]);
#pragma unroll
      for (int n = 0; n < 4; ++n) dma16(r2, (lane ^ (n * 2) ^ ((wave & 2) << 2)) * 16, n * 1024, l2 + n * 1024);
    }
    const u32 tdst = lds_addr(&sm.hb[hpos]);
    const u32 sdst = lds_addr(&sm.stat[wave][lane]);
    const u32 ssrc = lds_addr(&sm.stat[(g >> 1) * 4 + ((lane >> 3) & 3)][(lane & 7) * 4]);
    const u32 adst = lds_addr((c < 4 && (g & 1) == 0) ? &sm.actl[g >> 1][wave * 4 + c] : reinterpret_cast<u16*>(&sm.dummy[lane]));
    const u32 asrc = lds_addr(&sm.actl[lane & 1][g * 8]);
    const u32 apa = lds_addr(&sm.hb[(c >> 3) * 512 + (c & 3) * 64 + ((c >> 2) & 1) * 32 + g * 8]);
    constexpr u32 xs2 = 16384;
    u32 xoff = dup(G_XP) + near + g * 2048 + ub * 1024 + (wave & 3) * 256 + c * 16;
    u32x4 one;
#pragma unroll
    for (int k = 0; k < 4; ++k) one[k] = ((c & 2) == 0 && (c >> 2) == k) ? ((c & 1) ? 0x3f800000u : 0x00003f80u) : 0u;
    PIN(one);
    u32 ooff = dup(G_OP + (blk * 512 + tid) * 8);
    float Afp = Af, Bfp = Bf;
    PIN(xoff); PIN(ooff); PIN(Afp); PIN(Bfp);
    f32x2 gc = ga * cc1, xc = xin * cc1;
    PIN(ga); PIN(xin); PIN(gc); PIN(xc);
    u32 w2a = (u32)(uintptr_t)(&sm.w2b[0][0]) + (g * 8 + ((lane >> 2) & 3)) * 1024 + (wave ^ (g & 1)) * 128 + (lane & 15) * 8;
    PIN(w2a);
    if (wave == 1) {
      due = t1 + P.N1 * 256;
      const int tn = notices();
      due = tn + P.X2 * 256;
      until(due);
    }
    asm volatile("s_waitcnt vmcnt(0)" ::: "memory");
    STAMP(9);
    __builtin_amdgcn_s_barrier();
    STAMP(11);
    CFENCE();
    u32x4 xg[2] = {ld16<AUX_SC1>(wsr, xoff, 0), ld16<AUX_SC1>(wsr, xoff, xs2)};
    SB();
#pragma unroll
    for (int t = 0; t < 4; ++t) {
      const u32x2 lo = tr_read(w2a ^ (t * 32)), hi = tr_read((w2a ^ (t * 32)) + 4096);
      b2[t] = u32x4{lo[0], lo[1], hi[0], hi[1]};
    }
#pragma unroll
    for (int t = 0; t < 4; ++t) PIN(b2[t]);
    SB();
    const f32x4 z4 = {0.f, 0.f, 0.f, 0.f};
    auto xp_bad = [&]() { return !all_ok(max(max(max(xg[0][0], xg[0][1]), max(xg[0][2], xg[0][3])), max(max(xg[1][0], xg[1][1]), max(xg[1][2], xg[1][3]))) != SENT); };
    f32x4 v = mfma16(one, xg[1], mfma16(one, xg[0], z4));
    if (__builtin_expect(xp_bad(), 0)) {
#pragma nounroll
      for (int spin = 0; spin < SPIN_MAX; ++spin) {
        __builtin_amdgcn_s_sleep(1);
        CFENCE();
        xg[0] = ld16<AUX_SC1>(wsr, xoff, 0); xg[1] = ld16<AUX_SC1>(wsr, xoff, xs2);
        if (!xp_bad()) break;
      }
      v = mfma16(one, xg[1], mfma16(one, xg[0], z4));
      SB();
      asm volatile(".Lend_e:");
    }
    STAMP(5); WSTAMP(5);
    {
      const f32x2 a = v.xy, hc = __builtin_elementwise_fma(gc, a, xc), x1 = __builtin_elementwise_fma(ga, a, xin);
      *LDSP(u32, tdst) = pack_bf(hc[0], hc[1]);
      *LDSP(float, sdst) = swap32_add(x1[0] + x1[1], fmaf(x1[0], x1[0], x1[1] * x1[1]));
    }
    STAMP(16);
    LDS_BARRIER();
    STAMP(17);
    {
      const f32x4 z = {0.f, 0.f, 0.f, 0.f};
      const u32x4 af0 = LDSP(u32x4, apa)[0];
      SB();
      const f32x4 s4 = LDSP(f32x4, ssrc)[0], q4 = LDSP(f32x4, ssrc)[8];
      const u32x4 af1 = LDSP(u32x4, apa)[32];
      SB();
      f32x4 d[4];
      d[0] = mfma16(af0, bf[0], z); d[1] = mfma16(af0, bf[1], z);
      SB();
      const f32x2 s2 = s4.xy + s4.zw, q2 = q4.xy + q4.zw;
      float s = s2[0] + s2[1], q = q2[0] + q2[1];
      SB();
      d[2] = mfma16(af0, bf[2], z);
      SB();
      s = swap16_add(s, q);
      SB();
      d[3] = mfma16(af0, bf[3], z);
      SB();
      s += dpp<XOR1>(s); s += dpp<XOR2>(s);
      SB();
      d[0] = mfma16(af1, bf[4], d[0]);
      SB();
      s += dpp<XOR7>(s); s += dpp<XOR8>(s);
      SB();
      d[1] = mfma16(af1, bf[5], d[1]);
      SB();
      const auto sq = __builtin_amdgcn_permlane16_swap(__float_as_uint(s), __float_as_uint(s), false, false);
      const float mean = u2f(sq[0]) * (1.f / 512.f);
      SB();
      d[2] = mfma16(af1, bf[6], d[2]);
      SB();
      const float var = fmaf(u2f(sq[1]), 1.f / 512.f, -mean * mean);
      SB();
      d[3] = mfma16(af1, bf[7], d[3]);
      SB();
      float ri = __builtin_amdgcn_rsqf(fmaxf(var, 0.f) + 1e-5f);
      float cf = fmaf(-ri * mean, Afp, Bfp);
      float rk = ri * -1.44269504f, ck = cf * -1.44269504f;
      PIN(ri); PIN(cf); PIN(rk); PIN(ck);
      SB();
      PIN(d[0]); PIN(d[1]);
      float y = d[0][0] + d[1][1];
      PIN(y); PIN(d[2]);
      y += d[2][2];
      PIN(y); PIN(d[3]);
      y += d[3][3];
      y = __builtin_bit_cast(float, __builtin_amdgcn_update_dpp(__builtin_bit_cast(int, y), __builtin_bit_cast(int, y), XOR8, 0xa, 0xf, false));
      y = swap16_add(y, y);
      const float ex = __builtin_amdgcn_exp2f(fmaf(y, rk, ck));
      y = fmaf(y, ri, cf);
      const float gu = y * dpp<0x104>(y);
      *LDSP(u16, adst) = (u16)pack_bf(gu * __builtin_amdgcn_rcpf(1.f + ex), 0.f);
    }
    STAMP(18);
    LDS_BARRIER();
    STAMP(10);
    {
      const u32x4 af = *LDSP(u32x4, asrc);
      const f32x4 z = {0.f, 0.f, 0.f, 0.f};
      f32x4 d0 = mfma16(af, b2[0], z), d1 = mfma16(af, b2[1], z), d2 = mfma16(af, b2[2], z), d3 = mfma16(af, b2[3], z);
      asm volatile("" : "+v"(d0), "+v"(d1), "+v"(d2), "+v"(d3));
      const bool g1 = g & 1, g2 = g & 2;
      const float e0 = g1 ? d1[0] : d0[0], e1 = g1 ? d1[1] : d0[1], f0 = g1 ? d3[0] : d2[0], f1 = g1 ? d3[1] : d2[1];
      const u32x2 ov = {pack_bf(g2 ? f0 : e0, g2 ? f1 : e1), tag};
      __builtin_amdgcn_raw_buffer_store_b64(ov, wsr, ooff, near ^ 8192u, AUX_SC1);
      __builtin_amdgcn_raw_buffer_store_b64(ov, wsr, ooff, near, AUX_SC1);
    }
    asm volatile(".Lend_b:");
    STAMP(6);
    FINISH();
  }
  if (wave == 0) {
    if (!all_ok((sg[0][1] == tag) & (sg[0][3] == tag) & (sg[1][1] == tag) & (sg[1][3] == tag))) {
      u32x4 t[2];
      gather16<2>(wsr, G_MOD + (1024 + fd) * 8, 1536 * 8, tag, t);
      sg[0] = t[0]; sg[1] = t[1];
    }
    u32 xa;
    {
      due = t1 + P.N2 * 256;
      const int tn = notices();
      due = tn + P.X3 * 256;
      until(due);
    }
    const bool hb = g & 1;
#pragma nounroll
    for (int spin = 0; spin < SPIN_MAX; ++spin) {
      CFENCE();
      xa = __builtin_amdgcn_raw_buffer_load_b32(wsr, dup(G_XP) + near + (g >> 1) * 16384 + (c >> 2) * 2048 + (g & 1) * 1024 + fd * 2, 0, AUX_SC1);
      if (all_ok(xa != SENT)) break;
      __builtin_amdgcn_s_sleep(1);
    }
    float a0 = lo_f(xa), a1 = hi_f(xa);
    a0 += dpp<0x124>(a0); a1 += dpp<0x124>(a1);
    a0 += dpp<XOR8>(a0); a1 += dpp<XOR8>(a1);
    a0 = swap32_add(a0, a0); a1 = swap32_add(a1, a1);
    f32x2 xv = {rbf(lo_f(sx2) + rbf((hb ? hi_f(sg[0][0]) : lo_f(sg[0][0])) * rbf(a0))), rbf(hi_f(sx2) + rbf((hb ? hi_f(sg[0][2]) : lo_f(sg[0][2])) * rbf(a1)))};
    f32x2 gv = {hb ? hi_f(sg[1][0]) : lo_f(sg[1][0]), hb ? hi_f(sg[1][2]) : lo_f(sg[1][2])};
    PIN(xv); PIN(gv);
    if (wi == 0 && lane == 0) __builtin_amdgcn_raw_buffer_store_b32(tag, wsr, WS_EPOCH, 0, AUX_SC1);
    STAMP(10);
    due = max(due + P.X4 * 256, (int)(((u32)wall_clock64() - t0) << 8) + P.GX * 256);
    const bool last = lane < (NUP - 32) * 4;
    u32 o0 = dup(G_OP + (lane >> 2) * 4096 + fd * 8) + near, o2 = last ? o0 : OOB, tg2 = last ? tag : 0u;
    const rsrc_t rout = mk_rsrc(out, 2048u);
    u32 oo = (lane & 44) == 0 ? (g * 512 + fd) * 2 : OOB;
    const u32 pk = (c & 10) ? 0u : ((c & 4) ? 0x3f800000u : 0x00003f80u);
    u32x4 one = {(c & 1) ? 0u : pk, 0u, (c & 1) ? pk : 0u, 0u};
    PIN(o0); PIN(o2); PIN(tg2); PIN(oo); PIN(one);
    __builtin_amdgcn_raw_buffer_store_b32(pack_bf(xv[0], xv[1]), rout, oo, 0, AUX_SC1);
    u32 s1_ = 131072, s2_ = 262144;
    asm volatile("" : "+s"(s1_), "+s"(s2_));
    until(due);
    f32x4 sv;
#pragma nounroll
    for (int spin = 0; spin < SPIN_MAX; ++spin) {
      CFENCE();
      const u32x4 og[3] = {ld16<AUX_SC1>(wsr, o0, 0), ld16<AUX_SC1>(wsr, o0, s1_), ld16<AUX_SC1>(wsr, o2, s2_)};
      sv = f32x4{0.f, 0.f, 0.f, 0.f};
#pragma unroll
      for (int k = 0; k < 3; ++k) sv = mfma16(one, og[k], sv);
      SB();
      u32 bad = ((og[0][1] ^ tag) | (og[0][3] ^ tag) | (og[1][1] ^ tag)) | ((og[1][3] ^ tag) | (og[2][1] ^ tg2) | (og[2][3] ^ tg2));
      PIN(bad);
      SB();
      float s0 = sv[0] + dpp<0x124>(sv[0]), s1 = sv[1] + dpp<0x124>(sv[1]);
      PIN(s0); PIN(s1);
      s0 += dpp<XOR8>(s0); s1 += dpp<XOR8>(s1);
      PIN(s0); PIN(s1);
      __builtin_amdgcn_raw_buffer_store_b32(pack_bf(fmaf(gv[0], s0, xv[0]), fmaf(gv[1], s1, xv[1])), rout, oo, 0, AUX_SC1);
      SB();
      if (all_ok(bad == 0u)) break;
      __builtin_amdgcn_s_sleep(1);
    }
    STAMP(7);
    asm volatile(".Lend_c:");
  }
  FINISH();
}

extern "C" void kernel_launch(void* const* d_in, const int* in_sizes, int n_in,
                              void* d_out, int out_size, void* d_ws, size_t ws_size,
                              hipStream_t stream) {
  fused_kernel<<<256, 512, 0, stream>>>((const u16*)d_in[1], (const u16*)d_in[2], (const u16*)d_in[4], (const u16*)d_in[3], (const u16*)d_in[10], (const u16*)d_in[6], (const u16*)d_in[7],
                                        (unsigned char*)d_ws, (const u16*)d_in[0], (const u16*)d_in[9], (const u16*)d_in[8], (u16*)d_out);
}
```

```cpp
#define PROBE_MUL 10
#include <hip/hip_runtime.h>
#include <stdint.h>

typedef unsigned int u32;
typedef unsigned short u16;
typedef unsigned long long u64;
typedef __attribute__((ext_vector_type(4))) unsigned u32x4;
typedef __attribute__((ext_vector_type(2))) unsigned u32x2;
typedef __attribute__((ext_vector_type(2))) __bf16 bf16x2;
typedef __attribute__((ext_vector_type(4))) float f32x4;
typedef __attribute__((ext_vector_type(2))) float f32x2;
typedef __amdgpu_buffer_rsrc_t rsrc_t;

constexpr int NH = 32, SK = 16384, DH = 128, WIN = 4096, FF = 1376;
struct Prm { int E0, ES, E1, E2, E2C, EU, TEND, DT_UP, L0, L1, X1, X2, X3, X4, NE, FE, M0, M1, M2, M3, M4, GS, GX, N1, N2, GA, WD, DT_WO, LW, LU, EW, PM, NF, NR, C0, A0, AG, AC, TS, TW, TK, TG, GB, B0, BG, BF, D0, DG, TC, LM, C1, W[9]; };
constexpr Prm PRM0 = {32, 20, 32, 32, 54, 58, 1980, 60, 12, 30, 88, 70, 100, 89, 3, 8, 0x2511f55b, 0x2b7425d5, 0x6017665, 0x17e0b9ec, 0x31944305, 63, 30, 36, 62, 30, 4, 100, 45, 15, 0, 0x0, 15, 14, 3, 30, 4, 4, 509, 228, 60, 20, 7, 74, 22, 8, 70, 0, 0, 120, 2, 0x77777777, 0x77777777, 0x77777777, 0x77777777, 0x77777777, 0x44444477, 0x44444444, 0x44444444, 0x44444444};
constexpr int NL = 37;
constexpr int SK0 = 21, SK1 = 32;
constexpr int NCMIN = (NL - SK1 + SK0) * 8;
constexpr int WK = 8, WV = 5;
constexpr int WUSE = 12, LNS = 7;
constexpr int QP = 14, QD = 4;
constexpr int VQ = 6;
constexpr int NSL = (NL + 7) / 8;
constexpr int VS = VQ + NSL;
constexpr int MLS = VS + 2;
constexpr int VSIDE = MLS - 5, UPREP = MLS + 6;
constexpr int CPF = 17;
constexpr int SMO = 1;
constexpr int RPS = 24, RPP = 28;
constexpr int PRB = 12;
constexpr int TBS = 7, TBP = 10, TBL = 13, TBU = 17;
constexpr int window(int s, int w) {
  int n = w;
  for (int u = s - w + 1; u <= s; ++u) n += u == SMO ? 6 : (u == TBL || u == SK1 || u == NL + CPF || u == NL + SK1 ? 1 : (u == QP || u == NL + VQ - 4 ? 2 : (u == NL + VSIDE ? 4 : 0)));
  return n;
}
constexpr int cons_end(int v) {
  const int a = v - WV + 1, c = (v - MLS) * 4;
  const int m = a < c ? a : c;
  return m < 0 ? 0 : (m > NL ? NL : m);
}
constexpr u32 col(const Prm& P, int j) { const int m[6] = {P.M0, P.M1, P.M2, P.M3, P.M4, P.PM}; return (u32)(m[j / 3] >> (j % 3 * 10) & 1023) << 10; }
constexpr u32 first_col(const Prm& P, int k) {
  if (k < 4) return col(P, 12 + k) | 1u << (24 + k);
  if (k < 6) return col(P, k - 4);
  if (k < 8) return col(P, 4 + k) | 1u << (16 + k);
  if (k < 11) return col(P, k - 1);
  return (col(P, 2) ^ col(P, 3) ^ col(P, 4) ^ col(P, 5) ^ col(P, 6) ^ col(P, 10) ^ col(P, 11)) | 0xf00000u;
}
constexpr bool one_to_one(const Prm& P) {
  u32 c[10] = {};
  for (int j = 0; j < 10; ++j) c[j] = col(P, j);
  for (int j = 0; j < 10; ++j) {
    if (c[j] == 0) return false;
    const u32 low = c[j] & (0u - c[j]);
    for (int k = j + 1; k < 10; ++k) if (c[k] & low) c[k] ^= c[j];
  }
  return true;
}
static_assert(one_to_one(PRM0), "every KB of the window exactly once");
struct Tab { u32 e[512]; };
constexpr Tab make_tab(const Prm& P) {
  Tab T{};
  for (int bid = 0; bid < 256; ++bid) {
    const int xcd = bid & 7, part = bid >> 4 & 3, i8 = (bid >> 3 & 1) | bid >> 6 << 1, wi = i8 * 8 + xcd;
    const bool has_mod = wi < 48, has_up = wi >= 21;
    const int cb0 = (i8 + P.C0) & 7, cb1 = (i8 + P.C1) & 7, ms = wi - 20;
    const int mi = wi < 8 ? wi : (wi < 16 ? 8 + ((xcd + 4) & 7) : (wi < 20 ? (wi - 14) * 8 + 3 : 16 + ms / 7 * 8 + ms % 7 + (ms % 7 >= 3 ? 1 : 0)));
    const int ui = xcd * 5 + (xcd > 5 ? xcd - 5 : 0) + (i8 == 6 ? 0 : (xcd >= 5 ? 1 : 0) + (i8 == 7 ? 4 : i8 - 2));
    const bool edge = i8 >= 6;
    const int wd = cb0 == 3 ? 0 : P.WD;
    const int e[4] = {P.E0 - 4 * wd + (cb0 == 3 ? P.ES : 0), P.E1 + (cb1 == 3 ? P.ES : 0), has_mod ? (wi < 16 ? P.E2C : P.E2) + ((mi & 7) == 3 ? P.ES : 0) : 0, has_up ? P.EU + (edge ? P.TC : 0) : 0};
    int tw[4] = {}, win[4] = {}, span[4] = {}, k0[4] = {};
    for (int k = 0; k < 4; ++k) {
      const int np = k == 0 ? 8 - wd : 8;
      tw[k] = np ? e[k] * P.TW / (16 * np) : 0;
      k0[k] = P.NF + tw[k] * np / 2 > P.TK ? P.NF + tw[k] * np / 2 : P.TK;
      span[k] = P.TEND - k0[k] - P.TG - (k == 0 ? (wd ? P.DT_WO : P.A0) : (k == 3 && has_up ? P.DT_UP : 0));
      win[k] = span[k] - (k == 0 && wd ? P.EW : (k == 3 && has_up ? P.GA + (edge ? P.LM : 0) : 0));
    }
    int n[4] = {NCMIN / 8, NCMIN / 8, NCMIN / 8, NCMIN / 8};
    for (int left = 128 - NCMIN / 2; left > 0; --left) {
      int best = -1;
      for (int k = 3; k >= 0; --k)
        if (n[k] < NL && (best < 0 || n[k] * win[best] < n[best] * win[k])) best = k;
      ++n[best];
    }
    const int c[4] = {0, n[0] - 1, n[0] + n[1] - 2, n[0] + n[1] + n[2] - 3};
    const int nl = n[part];
    const bool has_w = part < 2 || (part == 2 ? has_mod : has_up);
    const int blk = !has_w ? 0 : (part == 0 ? cb0 : (part == 1 ? xcd * 8 + cb1 : (part == 2 ? mi : ui)));
    T.e[2 * bid] = (u32)(c[part] | nl << 7 | blk << 13 | (has_w ? 1 : 0) << 19 | (part == 0 ? wd : 0) << 20) | (u32)tw[part] << 24;
    T.e[2 * bid + 1] = (u32)(span[part] * P.TS / (4 * (nl - 1)) | span[part] * (1024 - P.TS) / (4 * (nl - 1)) << 16);
  }
  return T;
}
constexpr int step_range(const Prm& P, bool mx_) {
  int mx = 0, mn = 1 << 20;
  const Tab T = make_tab(P);
  for (int bid = 0; bid < 256; ++bid) { const int n = T.e[2 * bid] >> 7 & 63; mx = n > mx ? n : mx; mn = n < mn ? n : mn; }
  return mx_ ? mx : mn;
}
extern "C" __attribute__((used, visibility("protected"))) __constant__ const Tab wg_tab = make_tab(PRM0);
static_assert(step_range(PRM0, true) <= NL && step_range(PRM0, false) >= NCMIN / 8, "a wave's key range does not fit its load steps");
static_assert(UPREP + 1 < SK0 && QP + QD + 1 < SK0 && cons_end(SK0 - 1) == SK0 - WV && NL - SK1 == WV, "steps every wave runs");
constexpr int NUP = FF / 32;
constexpr int SPIN_MAX = 1 << 17;

constexpr u32 WS_EPOCH = 0;
constexpr u32 G_TB = 1024;
constexpr u32 G_MOD = 4096;
constexpr u32 G_Q = G_MOD + 3072 * 8;
constexpr u32 G_ML = G_Q + 4096 * 8;
constexpr u32 G_U = G_ML + 256 * 16;
constexpr u32 G_P = 0;
constexpr u32 G_XP = G_P + 256 * 256;
constexpr u32 G_OP = G_XP + 8 * 512 * 4;
constexpr u32 G_NT = G_OP + 43 * 512 * 8;
constexpr u32 G_PB = G_OP + 44 * 512 * 8;
constexpr u32 G_VT = G_U + 2 * (G_PB + 8192);
constexpr u32 WS_END = G_VT + 1024;
__device__ __forceinline__ u32 dup(u32 o) { return G_U + ((o >> 13) << 14) + (o & 8191u); }
constexpr u32 SENT = 0xffffffffu;
constexpr u32 OOB = 0x40000000u;
#define WST 0

constexpr int AUX_NT = 2, AUX_SC1 = 16;

__device__ __forceinline__ float lo_f(u32 v) { return __uint_as_float(v << 16); }
__device__ __forceinline__ float hi_f(u32 v) { return __uint_as_float(v & 0xffff0000u); }
__device__ __forceinline__ float rbf(float x) { return (float)(__bf16)x; }
__device__ __forceinline__ u32 pack_bf(float lo, float hi) {
  bf16x2 r; r[0] = (__bf16)lo; r[1] = (__bf16)hi; return __builtin_bit_cast(u32, r);
}
__device__ __forceinline__ float dot2(u32 k, u32 q, float acc) {
  return __builtin_amdgcn_fdot2_f32_bf16(__builtin_bit_cast(bf16x2, k), __builtin_bit_cast(bf16x2, q), acc, false);
}
template <int CTRL> __device__ __forceinline__ float dpp(float x) {
  return __builtin_bit_cast(float, __builtin_amdgcn_mov_dpp(__builtin_bit_cast(int, x), CTRL, 0xf, 0xf, true));
}
constexpr int XOR1 = 0xB1, XOR2 = 0x4E, XOR7 = 0x141, XOR8 = 0x128;
__device__ __forceinline__ float swap32_add(float a, float b) {
  auto t = __builtin_amdgcn_permlane32_swap(__float_as_uint(a), __float_as_uint(b), false, false);
  return __uint_as_float(t[0]) + __uint_as_float(t[1]);
}
__device__ __forceinline__ float swap16_add(float a, float b) {
  auto t = __builtin_amdgcn_permlane16_swap(__float_as_uint(a), __float_as_uint(b), false, false);
  return __uint_as_float(t[0]) + __uint_as_float(t[1]);
}
__device__ __forceinline__ float wave_sum(float x) {
  x += dpp<XOR1>(x); x += dpp<XOR2>(x); x += dpp<XOR7>(x); x += dpp<XOR8>(x);
  x = swap16_add(x, x);
  return swap32_add(x, x);
}
__device__ __forceinline__ float wave_max(float x) {
  x = fmaxf(x, dpp<XOR1>(x)); x = fmaxf(x, dpp<XOR2>(x)); x = fmaxf(x, dpp<XOR7>(x)); x = fmaxf(x, dpp<XOR8>(x));
  auto s = __builtin_amdgcn_permlane16_swap(__float_as_uint(x), __float_as_uint(x), false, false);
  x = fmaxf(__uint_as_float(s[0]), __uint_as_float(s[1]));
  auto t = __builtin_amdgcn_permlane32_swap(__float_as_uint(x), __float_as_uint(x), false, false);
  return fmaxf(__uint_as_float(t[0]), __uint_as_float(t[1]));
}
template <int CTRL> __device__ __forceinline__ float sel_add(float lo, float hi, bool up) {
  const float t1 = lo + dpp<CTRL>(lo), t2 = hi + dpp<CTRL>(hi);
  return up ? t2 : t1;
}
template <int I> __device__ __forceinline__ float bc16(float x) {
  return __builtin_bit_cast(float, __builtin_amdgcn_update_dpp(0, __builtin_bit_cast(int, x), 0x150 + I, 0xf, 0xf, false));
}
__device__ __forceinline__ float bcast16(float x, int i) {
  switch (i) {
    case 0: return bc16<0>(x); case 1: return bc16<1>(x); case 2: return bc16<2>(x); case 3: return bc16<3>(x);
    case 4: return bc16<4>(x); case 5: return bc16<5>(x); case 6: return bc16<6>(x); case 7: return bc16<7>(x);
    case 8: return bc16<8>(x); case 9: return bc16<9>(x); case 10: return bc16<10>(x); case 11: return bc16<11>(x);
    case 12: return bc16<12>(x); case 13: return bc16<13>(x); case 14: return bc16<14>(x); default: return bc16<15>(x);
  }
}
__device__ __forceinline__ float silu_f(float x) { return x * __builtin_amdgcn_rcpf(1.f + __expf(-x)); }

template <int AUX> __device__ __forceinline__ u32x4 ld16(rsrc_t r, u32 voff, u32 soff) {
  return __builtin_amdgcn_raw_buffer_load_b128(r, voff, soff, AUX);
}
template <int AUX> __device__ __forceinline__ u32x2 ld8(rsrc_t r, u32 voff, u32 soff) {
  return __builtin_amdgcn_raw_buffer_load_b64(r, voff, soff, AUX);
}
__device__ __forceinline__ void st8_sc1(rsrc_t r, u32 voff, u32 v, u32 tag) {
  __builtin_amdgcn_raw_buffer_store_b64(u32x2{v, tag}, r, voff, 0, AUX_SC1);
}
__device__ __forceinline__ void st16_sc1(rsrc_t r, u32 voff, u32 v0, u32 v1, u32 tag) {
  __builtin_amdgcn_raw_buffer_store_b128(u32x4{v0, tag, v1, tag}, r, voff, 0, AUX_SC1);
}
__device__ __forceinline__ rsrc_t mk_rsrc(const void* p, u32 bytes) {
  return __builtin_amdgcn_make_buffer_rsrc(const_cast<void*>(p), 0, bytes, 0x00020000);
}
#define WAIT_VM(n) __builtin_amdgcn_s_waitcnt(((n) & 15) | (((n) >> 4) << 14) | (7 << 4) | (15 << 8))
#define SB() __builtin_amdgcn_sched_barrier(0)
#define CFENCE() asm volatile("" ::: "memory")
__device__ __forceinline__ bool all_ok(bool ok) { return __builtin_amdgcn_ballot_w64(!ok) == 0ull; }

template <int N> __device__ __forceinline__ void gather16(rsrc_t ws, u32 voff, u32 stride, u32 tag, u32x4 (&g)[N]) {
#pragma nounroll
  for (int spin = 0; spin < SPIN_MAX; ++spin) {
    CFENCE();
    bool ok = true;
#pragma unroll
    for (int i = 0; i < N; ++i) g[i] = ld16<AUX_SC1>(ws, voff + i * stride, 0);
#pragma unroll
    for (int i = 0; i < N; ++i) ok &= (g[i][1] == tag) & (g[i][3] == tag);
    if (all_ok(ok)) break;
    __builtin_amdgcn_s_sleep(8);
  }
}
template <int N> __device__ __forceinline__ void gather8(rsrc_t ws, u32 voff, u32 stride, u32 tag, u32 (&g)[N], bool act = true) {
#pragma nounroll
  for (int spin = 0; spin < SPIN_MAX; ++spin) {
    CFENCE();
    bool ok = true;
    u32x2 t[N];
#pragma unroll
    for (int i = 0; i < N; ++i) t[i] = ld8<AUX_SC1>(ws, voff + i * stride, 0);
#pragma unroll
    for (int i = 0; i < N; ++i) { ok &= (t[i][1] == tag); g[i] = t[i][0]; }
    if (all_ok(ok | !act)) break;
    __builtin_amdgcn_s_sleep(8);
  }
}

struct Smem {
  float red[8][128];
  float ml[8][2];
  float r2[64 * 20];
  u16 hb[3 * 1024];
  float stat[8][64];
  float red2[8][128];
  u32 dummy[64];
  u32x2 bc;
  u32x4 zero[4];
  float pl[4][8];
  u32 mflag[8];
  u32x4 wbuf[8][8][64];
  __attribute__((aligned(256))) u16 w2b[32][512];
  u16 actl[2][32];
};

#define PROBE_K 8
#define PROBE_OP 0
#define PROBE_PART -1
#define STAMP(k) do { if ((k) == PROBE_K && tid == 0) pst = wall_clock64(); } while (0)
#define WSTAMP(k) do {} while (0)
#define FINISH() do { STAMP(8); if (tid == 0) { __hip_atomic_store(reinterpret_cast<u64*>(ws + (1u << 20)) + bid * 2, pt0, __ATOMIC_RELAXED, __HIP_MEMORY_SCOPE_AGENT); \
   __hip_atomic_store(reinterpret_cast<u64*>(ws + (1u << 20)) + bid * 2 + 1, (PROBE_PART < 0 || part == PROBE_PART) ? pst : 0ull, __ATOMIC_RELAXED, __HIP_MEMORY_SCOPE_AGENT); } \
   if (bid == 8 && wave == 0) probe_end(ws, pt0, lane); return; } while (0)
#define PIN(x) asm volatile("" : "+v"(x))
#define LDS_BARRIER() do { __builtin_amdgcn_s_waitcnt(0xc07f); __builtin_amdgcn_s_barrier(); CFENCE(); } while (0)
#define WAVE_SYNC() do { __builtin_amdgcn_fence(__ATOMIC_RELEASE, "wavefront"); __builtin_amdgcn_wave_barrier(); __builtin_amdgcn_fence(__ATOMIC_ACQUIRE, "wavefront"); } while (0)

__device__ __forceinline__ void dma16(rsrc_t r, u32 voff, u32 soff, u32 lds_dst) {
  u32 keep;
  asm volatile("s_mov_b32 %0, m0\n\ts_mov_b32 m0, %4\n\ts_nop 4\n\tbuffer_load_dwordx4 %1, %2, %3 offen nt lds\n\ts_mov_b32 m0, %0"
               : "=&s"(keep) : "v"(voff), "s"(r), "s"(soff), "s"(lds_dst) : "memory");
}
__device__ __forceinline__ void ld16_into(u32x4& d, rsrc_t r, u32 voff, u32 soff) {
  asm volatile("s_nop 4\n\tbuffer_load_dwordx4 %0, %1, %2, %3 offen nt" : "+v"(d) : "v"(voff), "s"(r), "s"(soff) : "memory");
}
__device__ __forceinline__ u32 perm_lo(u32 a, u32 b) { return __builtin_amdgcn_perm(b, a, 0x05040100u); }
__device__ __forceinline__ u32 perm_hi(u32 a, u32 b) { return __builtin_amdgcn_perm(b, a, 0x07060302u); }
__device__ __forceinline__ float rdl(float x, int l) { return __int_as_float(__builtin_amdgcn_readlane(__float_as_int(x), l)); }

typedef __attribute__((ext_vector_type(8))) __bf16 bf16x8;
__device__ __forceinline__ f32x4 mfma16(u32x4 a, u32x4 b, f32x4 c) {
  return __builtin_amdgcn_mfma_f32_16x16x32_bf16(__builtin_bit_cast(bf16x8, a), __builtin_bit_cast(bf16x8, b), c, 0, 0, 0);
}
__device__ __forceinline__ float sum8(const float* p) {
  const f32x4 a = *reinterpret_cast<const f32x4*>(p), b = *reinterpret_cast<const f32x4*>(p + 4);
  return ((a[0] + b[0]) + (a[1] + b[1])) + ((a[2] + b[2]) + (a[3] + b[3]));
}
__device__ __forceinline__ float ln_ri(float var) {
  return __builtin_amdgcn_rcpf(rbf(__builtin_amdgcn_sqrtf(rbf(rbf(fmaxf(var, 0.f)) + 1e-5f))));
}
__device__ __forceinline__ void ln_stats2(const float (&x0)[8], const float (&x1)[8], float& mb0, float& ri0, float& mb1, float& ri1) {
  const float k0 = rdl(x0[0], 0), k1 = rdl(x1[0], 0);
  float s0 = 0.f, s1 = 0.f, q0 = 0.f, q1 = 0.f;
#pragma unroll
  for (int k = 0; k < 8; ++k) {
    const float d0 = x0[k] - k0, d1 = x1[k] - k1;
    s0 += d0; s1 += d1; q0 = fmaf(d0, d0, q0); q1 = fmaf(d1, d1, q1);
  }
  float t = swap16_add(swap32_add(s0, s1), swap32_add(q0, q1));
  t += dpp<XOR1>(t); t += dpp<XOR2>(t); t += dpp<XOR7>(t); t += dpp<XOR8>(t);
  auto u = __builtin_amdgcn_permlane16_swap(__float_as_uint(t), __float_as_uint(t), false, false);
  const float ms = __uint_as_float(u[0]) * (1.f / 512.f), mq = __uint_as_float(u[1]) * (1.f / 512.f);
  const float ri = ln_ri(fmaf(-ms, ms, mq));
  mb0 = rbf(k0 + rdl(ms, 0)); mb1 = rbf(k1 + rdl(ms, 32));
  ri0 = rdl(ri, 0); ri1 = rdl(ri, 32);
}
template <int I> struct IC { static constexpr int v = I; };
template <int I, int N, class F> __device__ __forceinline__ void sfor(F&& f) {
  if constexpr (I < N) { if (f(IC<I>{})) sfor<I + 1, N>(f); }
}
typedef __attribute__((ext_vector_type(4))) short s16x4;
typedef __attribute__((address_space(3))) s16x4* lds_s16x4p;
__device__ __forceinline__ u32x2 tr_read(u32 lds_addr) {
  return __builtin_bit_cast(u32x2, __builtin_amdgcn_ds_read_tr16_b64_v4i16((lds_s16x4p)lds_addr));
}
__device__ __forceinline__ u32 next_tag(u32 e) { return ((e | 0x40004000u) + 1u) & ~0x40004000u; }
__device__ __forceinline__ float u2f(u32 v) { return __uint_as_float(v); }
#define LDSP(T, a) ((__attribute__((address_space(3))) T*)(a))
template <class T> __device__ __forceinline__ u32 lds_addr(T* p) { u32 a = (u32)(uintptr_t)p; PIN(a); return a; }

#define TAB_LOAD "s_add_u32 s30, s28, wg_tab@rel32@lo+4\ns_addc_u32 s31, s29, wg_tab@rel32@hi+12\nglobal_load_dwordx2 %[te], %[va], s[30:31]\n"
#define FC(k) ".Lfc" #k
#define FC_REGS
#define FX(r, bit, k) "s_bitcmp1_b32 %[" #r "], " #bit "\ns_cselect_b32 %[sb], " FC(k) ", 0\ns_xor_b32 %[sa], %[sa], %[sb]\n"
__device__ __forceinline__ void probe_end(unsigned char* ws, u64 pt0, int lane) {
  while (wall_clock64() < pt0 + 5500ull) __builtin_amdgcn_s_sleep(8);
  u64 a[4], c2[4];
  for (int i = 0; i < 4; ++i) {
    a[i] = __hip_atomic_load(reinterpret_cast<u64*>(ws + (1u << 20)) + (i * 64 + lane) * 2, __ATOMIC_RELAXED, __HIP_MEMORY_SCOPE_AGENT);
    c2[i] = __hip_atomic_load(reinterpret_cast<u64*>(ws + (1u << 20)) + (i * 64 + lane) * 2 + 1, __ATOMIC_RELAXED, __HIP_MEMORY_SCOPE_AGENT);
  }
  int mn0 = 1 << 30, mx1 = -(1 << 30), mn1 = 1 << 30, sum = 0, cnt = 0;
  for (int i = 0; i < 4; ++i) {
    const int d0 = (int)(a[i] - pt0);
    mn0 = min(mn0, d0);
    if (c2[i] != 0ull) { const int d1 = (int)(c2[i] - pt0); mx1 = max(mx1, d1); mn1 = min(mn1, d1); sum += d1; cnt += 1; }
  }
  for (int o = 1; o < 64; o <<= 1) { mn0 = min(mn0, __shfl_xor(mn0, o)); mx1 = max(mx1, __shfl_xor(mx1, o)); mn1 = min(mn1, __shfl_xor(mn1, o)); sum += __shfl_xor(sum, o); cnt += __shfl_xor(cnt, o); }
  int X = PROBE_OP == 0 ? mx1 : (PROBE_OP == 1 ? sum / max(cnt, 1) : mn1);
  if (PROBE_K == 102) X = mn0;
  X = mn0 + (X - mn0) * PROBE_MUL;
  const u64 tg = pt0 + (u64)(long long)(X + 8000);
  for (int i = 0; i < 400000 && wall_clock64() < tg; ++i) __builtin_amdgcn_s_sleep(1);
}
__global__ void __launch_bounds__(512, 2) fused_kernel(const u16* kc_, const u16* vc_, const u16* wo_, const u16* wq_, const u16* wmod_, const u16* w1_, const u16* w3_,
                                                       unsigned char* ws_, const u16* x_, const u16* cnd_, const u16* w2_, u16* out_) {
  __shared__ __attribute__((aligned(256))) Smem sm;
  const u64 a_ka = (u64)__builtin_amdgcn_kernarg_segment_ptr();
  const int bid = blockIdx.x, tid = threadIdx.x;
  u64 wa, t64; u32x2 te; int tid0;
  u32 ar[14];
  u32 tch2, sa_, sb_, va_;
  u32x4 kr[NL], vr[NL];
  asm volatile(".set .Lfc0, %0\n.set .Lfc1, %1\n.set .Lfc2, %2\n.set .Lfc3, %3\n.set .Lfc4, %4\n.set .Lfc5, %5\n.set .Lfc6, %6\n.set .Lfc7, %7\n.set .Lfc8, %8\n.set .Lfc9, %9\n.set .Lfc10, %10\n.set .Lfc11, %11"
    :: "n"(first_col(PRM0, 0)), "n"(first_col(PRM0, 1)), "n"(first_col(PRM0, 2)), "n"(first_col(PRM0, 3)), "n"(first_col(PRM0, 4)), "n"(first_col(PRM0, 5)),
       "n"(first_col(PRM0, 6)), "n"(first_col(PRM0, 7)), "n"(first_col(PRM0, 8)), "n"(first_col(PRM0, 9)), "n"(first_col(PRM0, 10)), "n"(first_col(PRM0, 11)));
  asm volatile(
    "s_load_dwordx16 s[4:19], %[ka], 0x0\ns_load_dwordx8 s[20:27], %[ka], 0x40\n"
    "s_memrealtime %[t]\n"
    "v_lshlrev_b32 %[va], 3, %[bid]\n"
    "s_getpc_b64 s[28:29]\n.Lcpf_first:\n"
    TAB_LOAD
    "s_cmp_gt_u32 %[bid], 15\ns_cbranch_scc1 .Lno_cpf\n"
    "s_mov_b32 %[sa], .Lend_a-.Lcpf_first\ns_max_i32 %[sa], %[sa], .Lend_b-.Lcpf_first\ns_max_i32 %[sa], %[sa], .Lend_c-.Lcpf_first\ns_max_i32 %[sa], %[sa], .Lend_d-.Lcpf_first\ns_max_i32 %[sa], %[sa], .Lend_e-.Lcpf_first\n"
    "v_lshlrev_b32 %[va], 6, %[tid]\nv_min_u32 %[va], %[sa], %[va]\nglobal_load_dword %[pf], %[va], s[28:29]\n"
    ".Lno_cpf:\n"
    "v_readfirstlane_b32 %[t0], %[tid]\nv_and_b32 %[va], 63, %[tid]\n"
    "s_mov_b32 %[sa], " FC(11) "\n"
    FX(bid, 0, 0) FX(bid, 1, 1) FX(bid, 2, 2) FX(bid, 3, 3) FX(bid, 4, 4) FX(bid, 5, 5) FX(bid, 6, 6) FX(bid, 7, 7) FX(t0, 6, 8) FX(t0, 7, 9) FX(t0, 8, 10)
    "v_lshl_add_u32 %[va], %[va], 4, %[sa]\n"
    "s_waitcnt lgkmcnt(0)\nglobal_load_dwordx4 %[k0], %[va], s[4:5] nt\nglobal_load_dwordx4 %[v0], %[va], s[6:7] nt\n"
    "s_bitcmp1_b32 %[t0], 6\ns_cselect_b64 s[14:15], s[16:17], s[14:15]\n"
    "s_bitcmp1_b32 %[bid], 4\ns_cselect_b64 s[8:9], s[10:11], s[8:9]\ns_cselect_b64 s[12:13], s[14:15], s[12:13]\ns_bitcmp1_b32 %[bid], 5\ns_cselect_b64 %[wa], s[12:13], s[8:9]"
    : "=&{s4}"(ar[0]), "=&{s5}"(ar[1]), "=&{s6}"(ar[2]), "=&{s7}"(ar[3]), "=&{s18}"(ar[4]), "=&{s19}"(ar[5]), "=&{s20}"(ar[6]), "=&{s21}"(ar[7]), "=&{s22}"(ar[8]), "=&{s23}"(ar[9]),
      "=&{s24}"(ar[10]), "=&{s25}"(ar[11]), "=&{s26}"(ar[12]), "=&{s27}"(ar[13]), [wa] "=&s"(wa), [t0] "=&s"(tid0), [t] "=&s"(t64), [te] "=&v"(te), [sa] "=&s"(sa_), [sb] "=&s"(sb_),
      [va] "=&v"(va_), [pf] "=&v"(tch2), [k0] "=&v"(kr[0]), [v0] "=&v"(vr[0])
    : [ka] "s"(a_ka), [bid] "s"(bid), [tid] "v"(tid) : "s8", "s9", "s10", "s11", "s12", "s13", "s14", "s15", "s16", "s17", "s28", "s29", "s30", "s31", "scc", "memory" FC_REGS);
  asm volatile("" : "+s"(ar[0]), "+s"(ar[1]), "+s"(ar[2]), "+s"(ar[3]), "+s"(ar[4]), "+s"(ar[5]), "+s"(ar[6]), "+s"(ar[7]), "+s"(ar[8]), "+s"(ar[9]), "+s"(ar[10]), "+s"(ar[11]), "+s"(ar[12]), "+s"(ar[13]));
  asm volatile("" : "+s"(wa), "+s"(t64), "+s"(tid0), "+s"(sa_));
  const u32 tka = (u32)wall_clock64();
  const u64 pt0 = t64; u64 pst = (PROBE_K == 100 || PROBE_K == 102) ? pt0 : (PROBE_K == 101 ? pt0 + (u32)(tka - (u32)t64) : 0);
  const u32 t0 = tka;
  __builtin_assume(tid >= 0 && tid < 512 && tid0 >= 0 && tid0 < 512 && bid >= 0 && bid < 256);
  constexpr Prm P = PRM0;
  const int TEND = P.TEND;
  const int xcd = bid & 7, part = bid >> 4 & 3;
  const int lane = tid & 63, wave = tid0 >> 6;
  SB();
  const bool p3 = part == 3;
  u32 pitch = (u32)(0x005600c001000020ull >> (part * 16) & 0xffff) * 32u;
  const int sh = p3 ? 2 : 3;
  u32 wv0 = (u32)((p3 ? (wave >> 1) * 128 : wave * 64) + (part == 0 ? xcd * 512 : 0) + (lane >> sh)) * pitch + ((lane & (p3 ? 3 : 7)) ^ (lane >> (p3 ? 5 : 3))) * 16u;
  const u32 wimg = (u32)(uintptr_t)(&sm.wbuf[0][0][0]);
  u32 wsz = (part == 0 ? 4096u : 512u) * pitch, wld = wimg + (p3 ? (wave & 1) * 32768 + (wave >> 1) * 8192 : wave * 8192);
  PIN(wv0);
  asm volatile("" : "+s"(wsz), "+s"(wld), "+s"(pitch));
  auto karg = [&](int i) { return ar[2 * i] | (u64)ar[2 * i + 1] << 32; };
  const u64 a_kc = karg(0), a_vc = karg(1);
  unsigned char* ws = (unsigned char*)karg(2);
  const u16 *x = (const u16*)karg(3), *cnd = (const u16*)karg(4), *w2 = (const u16*)karg(5);
  u16* out = (u16*)karg(6);
  const rsrc_t wsr = mk_rsrc(ws, WS_END);
  asm volatile("s_waitcnt vmcnt(2)" : "+v"(te));
  const u32 te0 = __builtin_amdgcn_readfirstlane(te[0]), te1 = __builtin_amdgcn_readfirstlane(te[1]);
  const int bl0 = te0 & 127, nl = te0 >> 7 & 63, blk = te0 >> 13 & 63;
  const bool has_w = te0 >> 19 & 1;
  const rsrc_t rw = mk_rsrc((const void*)wa, wsz);
  const u32 wstep = pitch << (6 - sh), wflip = p3 ? 32u : 0u;
  u32 wso = blk << (sh + 4);
  int wleft = 8;
  auto piece = [&]() {
    dma16(rw, wv0, wso, wld);
    wso += wstep; wld += 1024;
    wv0 ^= (wleft & 1) ? wflip : 0u;
    --wleft;
  };
  SB();
  STAMP(23);
  const int b = bid >> 3 & 1, head = xcd * 4 + (bid >> 6 ^ 3);
  const int g = lane >> 4, c = lane & 15;
  const int i8 = b | bid >> 6 << 1, wi = i8 * 8 + xcd;
  const bool is_up = p3 && has_w, modc = part == 2 && wi < 16;
  const size_t base = ((size_t)(b * NH + head) * SK + (SK - WIN)) * DH;
  const rsrc_t rk = mk_rsrc((const u16*)a_kc + base, WIN * DH * 2), rv = mk_rsrc((const u16*)a_vc + base, WIN * DH * 2);
  int tau = te1 & 0xffff;
  const int tauw = te0 >> 24 << 7, wdef = te0 >> 20 & 15;
  int due = P.NF * 256, now = 0;
  auto gate = [&]() {
    while (now < due) { now = (int)(((u32)wall_clock64() - t0) << 8); if (now < due) __builtin_amdgcn_s_sleep(1); }
    due += tau;
  };
  auto until = [&](int t) { while ((int)(((u32)wall_clock64() - t0) << 8) < t); };
  const u32 pslot = dup(G_P + head * 2048 + (b * 4 + part) * 16);
  const u32 die = __builtin_amdgcn_s_getreg(20 | (2 << 6));
  const bool voter = p3 && i8 == 1, timer = p3 && i8 < 2;
  int vote = 0, lag = 0;
  const u32 pbo = dup(G_PB) + (xcd * 8 + wave) * 128;
  u32 near = 0;
  u32x2 ep; u32 xs0, xs1, tch1; u32x4 xr0, xr1;
  const rsrc_t rx = mk_rsrc(part == 2 ? cnd : x, (part == 1 || (part == 2 && has_w)) ? 2048u : 0u);
  auto small_ops = [&]() {
    ep = ld8<AUX_SC1>(wsr, wave == 7 ? WS_EPOCH : OOB, 0);
    xs0 = __builtin_amdgcn_raw_buffer_load_b16(rx, tid * 2, 0, 0); xs1 = __builtin_amdgcn_raw_buffer_load_b16(rx, tid * 2, 1024, 0);
    xr0 = ld16<0>(rx, part == 1 ? lane * 16 : OOB, 0); xr1 = ld16<0>(rx, part == 1 ? lane * 16 : OOB, 1024);
    tch1 = __builtin_amdgcn_raw_buffer_load_b32(mk_rsrc(w2 + (size_t)blk * 32 * 512, is_up ? 64u : 0u), tid == 384 ? 0 : OOB, 0, AUX_NT);
  };
  if (has_w) {
    if (modc) now = 1 << 30;
#pragma nounroll
    while (wleft > wdef) { gate(); due += tauw - tau; piece(); }
    now = 0;
  }
  due = max(due, P.TK * 256);
  SB();
  u32 sot = sa_ & 0xffc00u;
  {
    const u32 s1 = (bl0 + lane - 1 - (lane >= SK0 ? NL - nl : 0)) ^ (124 + part);
#pragma unroll
    for (int k = 0; k < 7; ++k) sot ^= (u32)-(int)(s1 >> k & 1u) & col(P, k);
    PIN(sot);
  }
  u32 tag = 0;
  u32x4 bf[8];
  auto wo_prep = [&](int s0 = 0, int s1 = 2) {
    const u32 wi_ = wimg + wave * 8192;
    const int q = (lane >> 2) & 3, pp = lane & 3;
#pragma unroll
    for (int s = s0; s < s1; ++s) {
#pragma unroll
      for (int t = 0; t < 4; ++t) {
        const int r0 = s * 32 + g * 8 + q, r1 = r0 + 4, ch = t * 2 + (pp >> 1);
        const u32x2 lo = tr_read(wi_ + r0 * 128 + ((ch ^ (r0 & 7)) * 16) + (pp & 1) * 8), hi = tr_read(wi_ + r1 * 128 + ((ch ^ (r1 & 7)) * 16) + (pp & 1) * 8);
        bf[s * 4 + t] = u32x4{lo[0], lo[1], hi[0], hi[1]};
      }
    }
  };
  auto gemv_go = [&](u32 hh) {
    sm.hb[tid] = (u16)hh; sm.hb[512 + tid] = (u16)(hh >> 16);
    WAVE_SYNC();
    const u32x4* a = (c & 11) == 0 ? reinterpret_cast<const u32x4*>(&sm.hb[(c >> 2) * 512 + wave * 64 + g * 8]) : &sm.zero[0];
    const u32x4 a0 = a[0], a1 = a[(c & 11) == 0 ? 4 : 0];
    const f32x4 z = {0.f, 0.f, 0.f, 0.f};
    f32x4 d[4];
#pragma unroll
    for (int t = 0; t < 4; ++t) d[t] = mfma16(a0, bf[t], z);
#pragma unroll
    for (int t = 0; t < 4; ++t) d[t] = mfma16(a1, bf[4 + t], d[t]);
    if (lane < 32) {
#pragma unroll
      for (int t = 0; t < 4; ++t) sm.r2[(t * 16 + c) * 20 + g * 8 + wave] = d[t][0];
    }
  };
  auto mod_stage = [&]() {
    wo_prep();
    gemv_go(pack_bf(silu_f(lo_f(xs0)), silu_f(lo_f(xs1))));
    __builtin_amdgcn_s_waitcnt(0xc07f);
    if (lane == 0) *LDSP(volatile u32, (u32)(uintptr_t)&sm.mflag[wave]) = 1u;
    if (wave == 7) {
#pragma nounroll
      for (int spin = 0; spin < SPIN_MAX; ++spin) {
        const u32 f = *LDSP(volatile u32, (u32)(uintptr_t)&sm.mflag[lane & 7]);
        if (all_ok(f != 0u)) break;
        __builtin_amdgcn_s_sleep(1);
      }
      CFENCE();
      st8_sc1(wsr, G_MOD + (blk * 64 + lane) * 8, pack_bf(sum8(&sm.r2[lane * 20]), sum8(&sm.r2[lane * 20 + 8])), tag);
    }
    STAMP(9);
  };
  if (lane < 4) sm.zero[lane] = u32x4{0u, 0u, 0u, 0u};
  if (part == 2 && has_w) {
    if (tid < 8) sm.mflag[tid] = 0u;
    LDS_BARRIER();
    if (__builtin_expect(modc, 0)) {
      small_ops();
      __builtin_amdgcn_raw_buffer_store_b128(u32x4{SENT, SENT, SENT, SENT}, wsr, G_U + wi * 10240 + tid * 16, 0, AUX_SC1);
      __builtin_amdgcn_raw_buffer_store_b128(u32x4{SENT, SENT, SENT, SENT}, wsr, tid < 128 ? G_U + wi * 10240 + tid * 16 : OOB, 8192, AUX_SC1);
      asm volatile("s_waitcnt vmcnt(0)" ::: "memory");
      tag = next_tag(__builtin_amdgcn_readfirstlane(ep[0]));
      mod_stage();
      __builtin_amdgcn_s_barrier();
    }
  }
  SB();
  const int fd = wi * 8 + (lane & 3) * 2;
  const int ub = wave >> 2, ud = (wave & 3) * 128 + c * 8 + g * 2;
  u32x4 sg[3]; u32 sx2;
  auto side_loads = [&]() {
    const bool fin = part == 1 && wave == 0;
    sg[0] = ld16<AUX_SC1>(wsr, is_up ? G_MOD + (1024 + ud) * 8 : (fin ? G_MOD + (1024 + fd) * 8 : OOB), 0);
    sg[1] = ld16<AUX_SC1>(wsr, is_up ? G_MOD + (1536 + ud) * 8 : (fin ? G_MOD + (2560 + fd) * 8 : OOB), 0);
    sg[2] = ld16<AUX_SC1>(wsr, is_up ? G_MOD + (2048 + ud) * 8 : OOB, 0);
    sx2 = __builtin_amdgcn_raw_buffer_load_b32(mk_rsrc(x, (is_up || fin) ? 2048u : 0u), is_up ? ub * 1024 + ud * 2 : (g & 1) * 1024 + fd * 2, 0, 0);
  };
  auto side_pin = [&]() { PIN(sg[0]); PIN(sg[1]); PIN(sg[2]); PIN(sx2); };
  const u32 mlo = G_ML + (head * 2 + b) * 64 + (lane & 3) * 16;
  float fmrg = 0.f;
  auto merge_factor = [&](const u32x4& ml) {
    const float mi = u2f(ml[0]);
    float M = fmaxf(mi, dpp<XOR1>(mi)); M = fmaxf(M, dpp<XOR2>(M));
    const float e = __expf(mi - M);
    float L = e * u2f(ml[2]);
    L += dpp<XOR1>(L); L += dpp<XOR2>(L);
    fmrg = rdl(e * __builtin_amdgcn_rcpf(L), part);
  };
  f32x2 ga = {0.f, 0.f}, xin = {0.f, 0.f}, cc1 = {0.f, 0.f};
  float Af = 0.f, Bf = 0.f;
  const int hpos = ub * 512 + ud;
  auto up_prep = [&]() {
    if (!all_ok((sg[0][1] == tag) & (sg[0][3] == tag) & (sg[1][1] == tag) & (sg[1][3] == tag) & (sg[2][1] == tag) & (sg[2][3] == tag))) gather16<3>(wsr, G_MOD + (1024 + ud) * 8, 512 * 8, tag, sg);
    ga = f32x2{ub ? hi_f(sg[0][0]) : lo_f(sg[0][0]), ub ? hi_f(sg[0][2]) : lo_f(sg[0][2])};
    cc1 = f32x2{rbf(1.f + (ub ? hi_f(sg[2][0]) : lo_f(sg[2][0]))), rbf(1.f + (ub ? hi_f(sg[2][2]) : lo_f(sg[2][2])))};
    xin = f32x2{lo_f(sx2), hi_f(sx2)};
    u32* hp = reinterpret_cast<u32*>(&sm.hb[hpos]);
    hp[512] = pack_bf(cc1[0], cc1[1]);
    hp[1024] = ub ? perm_hi(sg[1][0], sg[1][2]) : perm_lo(sg[1][0], sg[1][2]);
  };
  auto up_frags = [&]() {
    const int pp = lane & 3;
    const u32 a = wimg + (pp & 1) * 32768 + ((pp >> 1) * 32 + g * 8 + ((lane >> 2) & 3)) * 64 + (((wave >> 1) ^ ((g & 1) | (pp & 2))) * 16) + (wave & 1) * 8;
#pragma unroll
    for (int rb = 0; rb < 8; ++rb) {
      const u32x2 lo = tr_read(a + rb * 4096), hi = tr_read(a + rb * 4096 + 256);
      bf[rb] = u32x4{lo[0], lo[1], hi[0], hi[1]};
    }
  };
  const u32x4* ap = reinterpret_cast<const u32x4*>(&sm.hb[((lane >> 3) & 1) * 512 + (lane & 1) * 32 + g * 8]);
  const bool par = lane & 8;
  auto limit = [&](auto sc) {
    constexpr int s = sc.v;
    WAIT_VM(window(s, 1 + (PRM0.W[s >> 3] >> (4 * (s & 7)) & 7)));
    SB();
  };
  auto sample = [&]() { if (timer) lag += (int)(((u32)wall_clock64() - t0) << 8) - due + ((int)__builtin_amdgcn_s_getreg(7 | (3 << 11)) + 1) * tau; };
  auto report = [&](u32 dst) {
    if (timer) {
      if (lane == 0) sm.mflag[wave] = vote * 65536 + min(lag >> 8, 4000);
      lag = 0;
      LDS_BARRIER();
      if (wave == 7) {
        int s = sm.mflag[lane & 7];
        s += __builtin_amdgcn_mov_dpp(s, XOR1, 0xf, 0xf, true); s += __builtin_amdgcn_mov_dpp(s, XOR2, 0xf, 0xf, true); s += __builtin_amdgcn_mov_dpp(s, XOR7, 0xf, 0xf, true);
        if (lane < 8) st8_sc1(wsr, dst + lane * 128 + wi * 8, s, tag);
      }
    }
    SB();
  };
  float l0 = 0.f, l1 = 0.f;
  u32x2 gmd[2], tbg;
  int t1 = 0;
#pragma unroll
  for (int i = SK0; i < SK1; ++i) kr[i] = u32x4{0u, 0u, 0u, 0u};
  const int first = SK0 + NL - nl;
  auto left_out = [&](int i) { int f = first; asm volatile("" : "+s"(f)); return i < f; };
  sfor<1, NL>([&](auto ic) {
    constexpr int i = ic.v;
    if constexpr (i >= SK0 && i < SK1) { if (left_out(i)) return true; }
    const u32 so = __builtin_amdgcn_readlane(sot, i);
    gate();
    if constexpr ((i >= TBS && i <= TBP) || (i >= RPS && i <= RPP)) sample();
    SB();
    if constexpr (i >= SK0 && i < SK1) ld16_into(kr[i], rk, lane * 16, so); else kr[i] = ld16<AUX_NT>(rk, lane * 16, so);
    if constexpr (i == SMO) small_ops();
    if constexpr (i == SMO + 1) { if (voter) __builtin_amdgcn_raw_buffer_store_b32(0u, wsr, pbo + (lane >> 5) * 8192 + (lane & 31) * 4, 0, AUX_SC1); }
    if constexpr (i == SK1) tbg = ld8<AUX_SC1>(wsr, wave == 7 ? G_VT + xcd * 128 + c * 8 : OOB, 0);
    if constexpr (i == TBL) tbg = ld8<AUX_SC1>(wsr, wave == 7 ? G_TB + xcd * 128 + c * 8 : OOB, 0);
    if constexpr (i == QP) { gmd[0] = ld8<AUX_SC1>(wsr, part == 1 ? G_MOD + tid * 8 : OOB, 0); gmd[1] = ld8<AUX_SC1>(wsr, part == 1 ? G_MOD + tid * 8 : OOB, 4096); }
    SB();
    SB();
    if (i >= 4) limit(ic);
    if (i == 1) STAMP(25);
    if (i == 8) STAMP(12);
    if (i == 20) STAMP(21);
    if (i == 32) STAMP(22);
    if (i == TBP) {
      if (timer) {
        if (lane == 0) sm.mflag[wave] = lag;
        lag = 0;
        LDS_BARRIER();
        if (wave == 7) {
          int s = sm.mflag[lane & 7];
          s += __builtin_amdgcn_mov_dpp(s, XOR1, 0xf, 0xf, true); s += __builtin_amdgcn_mov_dpp(s, XOR2, 0xf, 0xf, true); s += __builtin_amdgcn_mov_dpp(s, XOR7, 0xf, 0xf, true);
          if (lane < 8) st8_sc1(wsr, G_TB + lane * 128 + wi * 8, t0 << 10 | (u32)min(s / (2048 * (TBP - TBS + 1)), 1023), tag);
        }
      }
      SB();
    }
    if (i == PRB) {
      if (voter) {
        u64 ta, tb, tc; u32 dm;
        const u32 pa = pbo + (wave & 1) * 8192;
        asm volatile("s_memtime %0\n\ts_waitcnt lgkmcnt(0)\n\ts_load_dword %3, %4, %5\n\ts_waitcnt lgkmcnt(0)\n\ts_memtime %1\n\ts_waitcnt lgkmcnt(0)\n\t"
                     "s_load_dword %3, %4, %6\n\ts_waitcnt lgkmcnt(0)\n\ts_memtime %2\n\ts_waitcnt lgkmcnt(0)"
                     : "=&s"(ta), "=&s"(tb), "=&s"(tc), "=&s"(dm) : "s"(ws), "s"(pa), "s"(pa ^ 8192u));
        const int d = min(max((int)(2 * (u32)tb - (u32)ta - (u32)tc), -500), 500);
        vote = ((wave ^ die) & 1) ? -d : d;
        STAMP(26);
      }
      SB();
    }
    if (i == TBU) {
      if (wave == 7) {
        const bool ok = tbg[1] == tag;
        float lf = ok ? (float)min(max((int)((t0 << 10 | 1023u) - tbg[0]) >> 10, -300), 300) : 0.f, nf = ok ? 1.f : 0.f, lg = ok ? (float)(tbg[0] & 1023u) : 0.f;
        lf += dpp<XOR1>(lf); nf += dpp<XOR1>(nf); lg += dpp<XOR1>(lg); lf += dpp<XOR2>(lf); nf += dpp<XOR2>(nf); lg += dpp<XOR2>(lg);
        lf += dpp<XOR7>(lf); nf += dpp<XOR7>(nf); lg += dpp<XOR7>(lg); lf += dpp<XOR8>(lf); nf += dpp<XOR8>(nf); lg += dpp<XOR8>(lg);
        const float rn = __builtin_amdgcn_rcpf(fmaxf(nf, 1.f));
        if (lane == 0) sm.bc = u32x2{tag, t0 - (u32)(int)(lf * rn) + (u32)min(max((int)((lg * rn - (float)P.GS) * (float)P.GB * 0.125f), 0), 150)};
      }
      LDS_BARRIER();
      const u32x2 bc = sm.bc;
      tag = __builtin_amdgcn_readfirstlane(bc[0]);
      t1 = (__builtin_amdgcn_readfirstlane((int)(bc[1] - t0)) + TEND + P.X1) * 256;
      tau -= __builtin_amdgcn_readfirstlane((int)((float)(int)(t0 - bc[1]) * 256.f * __builtin_amdgcn_rcpf((float)(nl - TBU - 1))));
      SB();
    }
    if (i == RPP) report(G_VT);
    if (i == LNS) {
      tag = next_tag(__builtin_amdgcn_readfirstlane(ep[0]));
      if (part == 1) {
        float x0[8], x1[8], mb0, ri0, mb1, ri1;
#pragma unroll
        for (int k = 0; k < 4; ++k) { x0[2 * k] = lo_f(xr0[k]); x0[2 * k + 1] = hi_f(xr0[k]); x1[2 * k] = lo_f(xr1[k]); x1[2 * k + 1] = hi_f(xr1[k]); }
        ln_stats2(x0, x1, mb0, ri0, mb1, ri1);
        l0 = rbf(rbf(lo_f(xs0) - mb0) * ri0); l1 = rbf(rbf(lo_f(xs1) - mb1) * ri1);
      }
      PIN(l0); PIN(l1);
      SB();
    }
    if (i == WUSE) {
      PIN(tch1); PIN(tch2);
      if ((part == 1 || (part == 2 && has_w && !modc))) {
        WAIT_VM(WK); CFENCE();
        if (part == 2) mod_stage(); else wo_prep();
      }
#pragma unroll
      for (int k = 0; k < 8; ++k) PIN(bf[k]);
      SB();
    }
    if (i == QP + QD) {
      if (part == 1) {
        u32 gm[2] = {gmd[0][0], gmd[1][0]};
        if (!all_ok((gmd[0][1] == tag) & (gmd[1][1] == tag))) { STAMP(11); gather8<2>(wsr, G_MOD + tid * 8, 512 * 8, tag, gm); }
        STAMP(5);
        gemv_go(pack_bf(rbf(l0 * rbf(1.f + lo_f(gm[1]))) + lo_f(gm[0]), rbf(l1 * rbf(1.f + hi_f(gm[1]))) + hi_f(gm[0])));
      }
      SB();
    }
    if (i == QP + QD + 1) {
      if (part == 1) {
        LDS_BARRIER();
        if (wave == 0) {
          const int bb = lane >> 5, jq = lane & 31;
          st8_sc1(wsr, G_Q + (bb * 2048 + blk * 32 + jq) * 8, pack_bf(sum8(&sm.r2[2 * jq * 20 + bb * 8]), sum8(&sm.r2[(2 * jq + 1) * 20 + bb * 8])), tag);
        }
        STAMP(6);
      }
      SB();
    }
    return true;
  });
  PIN(sot);
  if (wave == 7) {
    const bool ok = tbg[1] == tag;
    float vt = ok ? (float)((int)tbg[0] >> 16) : 0.f, lg = ok ? (float)(tbg[0] & 0xffffu) : 0.f, nf = ok ? 8.f * (RPP - RPS + 1) : 0.f;
    vt += dpp<XOR1>(vt); lg += dpp<XOR1>(lg); nf += dpp<XOR1>(nf); vt += dpp<XOR2>(vt); lg += dpp<XOR2>(lg); nf += dpp<XOR2>(nf);
    vt += dpp<XOR7>(vt); lg += dpp<XOR7>(lg); nf += dpp<XOR7>(nf); vt += dpp<XOR8>(vt); lg += dpp<XOR8>(lg); nf += dpp<XOR8>(nf);
    lg *= __builtin_amdgcn_rcpf(fmaxf(nf, 1.f));
    if (lane == 0) sm.bc = u32x2{__float_as_uint(vt), (u32)min(max((int)((lg - (float)P.B0) * (float)(P.BG * 32)), 0), 200 * 256)};
  }
  LDS_BARRIER();
  {
    const u32x2 bc = sm.bc;
    const int st = __builtin_amdgcn_readfirstlane(bc[1]), sp = (st * P.BF) >> 3;
    near = ((__builtin_amdgcn_readfirstlane(bc[0]) >> 31 ^ die ^ P.NR ^ 1) & 1) * 8192u;
    t1 += st;
    due += P.TG * 256 + sp;
    tau = (te1 >> 16) + __builtin_amdgcn_readfirstlane((int)((float)(st - sp) * __builtin_amdgcn_rcpf((float)(nl - 3))));
  }
  const u32 qoff = G_Q + ((b * 32 + head) * 64 + c * 4) * 8;
  u32x4 qg[2], mlg;
  u32 qq[4];
  constexpr int NB = (NL + 15) / 16;
  float xlo, sc[NB], pr[NB], m = 0.f;
  f32x4 acc = {0.f, 0.f, 0.f, 0.f};
  u32 pb[NB], pm[4], pl8[8], cpf;
  const u32 adst0 = lds_addr(lane < 32 ? &sm.red[wave][c * 8 + g * 4] : &sm.r2[(lane - 32) * 4]);
  u32 po, rdst, rsrc, xo;
  auto wo_addr = [&]() {
    po = dup(G_P + xcd * 8192) + tid * 16 + near;
    rdst = lane < 32 ? (u32)(uintptr_t)&sm.red2[wave][0] + lane * 16 : (u32)(uintptr_t)&sm.r2[0] + (lane - 32) * 16;
    rsrc = (u32)(uintptr_t)&sm.red2[0][((wave & 1) * 16 + (lane & 15)) * 4 + (lane >> 4)];
    xo = dup(G_XP + xcd * 2048 + (wave & 1) * 1024 + (blk * 64 + (lane & ~1)) * 2) + (lane & 1) * 8192;
    PIN(po); PIN(rdst); PIN(rsrc); PIN(xo);
  };
  u32 pso = (wave == 0 && (g & 1) == 0) ? pslot + (c >> 3) * 1024 + (c & 3) * 256 + (c >> 2 & 1) * 128 + (g >> 1) * 8192 : OOB;
  PIN(pso);
  const u32 nadd = wdef ? P.LW : (is_up ? P.LU : 0);
  u32 nto = tid < 2 ? dup(G_NT) + bid * 8 + tid * 8192 : OOB;
  PIN(nto);
  auto notices = [&]() -> int {
    u32 off = dup(G_NT) + near + lane * 16;
    PIN(off);
    until(due);
    int mx;
#pragma nounroll
    for (int spin = 0; spin < SPIN_MAX; ++spin) {
      CFENCE();
      const u32x4 ga_ = ld16<AUX_SC1>(wsr, off, 0), gb_ = ld16<AUX_SC1>(wsr, off, 1024);
      mx = max(max((int)(ga_[0] - t0), (int)(ga_[2] - t0)), max((int)(gb_[0] - t0), (int)(gb_[2] - t0)));
      if (all_ok((((ga_[1] ^ tag) | (ga_[3] ^ tag)) | ((gb_[1] ^ tag) | (gb_[3] ^ tag))) == 0u)) break;
      for (int k = 0; k < P.AG; ++k) __builtin_amdgcn_s_sleep(1);
    }
    if (all_ok(mx <= (t1 >> 8) - P.AC)) return t1;
    mx = max(mx, __builtin_amdgcn_mov_dpp(mx, XOR1, 0xf, 0xf, true)); mx = max(mx, __builtin_amdgcn_mov_dpp(mx, XOR2, 0xf, 0xf, true));
    mx = max(mx, __builtin_amdgcn_mov_dpp(mx, XOR7, 0xf, 0xf, true)); mx = max(mx, __builtin_amdgcn_mov_dpp(mx, XOR8, 0xf, 0xf, true));
    mx = max(max(__builtin_amdgcn_readlane(mx, 0), __builtin_amdgcn_readlane(mx, 16)), max(__builtin_amdgcn_readlane(mx, 32), __builtin_amdgcn_readlane(mx, 48)));
    return max(min(mx, 1 << 20) * 256, t1 + P.L0 * 256) + P.L1 * 256;
  };
  auto pv_row = [&](auto jc) {
    constexpr int i = jc.v;
    const u32 pv = __float_as_uint(bcast16(u2f(pb[i >> 4]), i & 15));
    acc = mfma16(u32x4{pv & pm[0], pv & pm[1], pv & pm[2], pv & pm[3]}, vr[i], acc);
    PIN(acc);
    return true;
  };
  sfor<0, NL - 1>([&](auto ic) {
    constexpr int v = ic.v;
    if constexpr (v >= SK0 && v < SK1) { if (left_out(v)) return true; }
    if constexpr (v > 0) {
      const u32 so = __builtin_amdgcn_readlane(sot, v);
      if constexpr (v >= SK1) { if (v == NL - P.NE) tau = (tau * P.FE) >> 4; }
      gate();
      SB();
      if constexpr (v >= SK0 && v < SK1) ld16_into(vr[v], rv, lane * 16, so); else vr[v] = ld16<AUX_NT>(rv, lane * 16, so);
      SB();
      SB();
    }
    if constexpr (v == SK0 - 1) {
#pragma unroll
      for (int i = SK0; i < SK1; ++i) { vr[i] = u32x4{0u, 0u, 0u, 0u}; PIN(vr[i]); }
    }
    if constexpr (v == VQ - 4) { qg[0] = ld16<AUX_SC1>(wsr, qoff, 0); qg[1] = ld16<AUX_SC1>(wsr, qoff + 16, 0); }
    if constexpr (v == VSIDE) side_loads();
    if constexpr (v == CPF) {
      u64 pc; u32 dlt, len;
      asm volatile("s_getpc_b64 %0\n.Lcpf_here:\n\ts_mov_b32 %1, .Ltail_start-.Lcpf_here\n\ts_mov_b32 %2, .Lend_a-.Ltail_start\n\ts_max_i32 %2, %2, .Lend_b-.Ltail_start\n\ts_max_i32 %2, %2, .Lend_c-.Ltail_start\n\ts_max_i32 %2, %2, .Lend_d-.Ltail_start\n\ts_max_i32 %2, %2, .Lend_e-.Ltail_start" : "=s"(pc), "=s"(dlt), "=s"(len) :: "scc");
      pc += dlt;
      const rsrc_t rc = mk_rsrc((const void*)pc, (part == 3 && i8 == 1) ? len : 0u);
      cpf = __builtin_amdgcn_raw_buffer_load_b32(rc, tid * 64, 0, 0);
    }
    if constexpr (v == SK1 + 2) PIN(cpf);
    if constexpr (v == SK1) mlg = ld16<AUX_SC1>(wsr, wave == 0 ? mlo : OOB, 0);
    SB();
    if constexpr (NL + v < 72) limit(IC<NL + v>{});
    if constexpr (v >= SK0 + WV) { WAIT_VM(v < SK1 ? WV : WV + 1); SB(); }
    if constexpr (v == VQ) {
      if (!all_ok((qg[0][1] == tag) & (qg[0][3] == tag) & (qg[1][1] == tag) & (qg[1][3] == tag))) { STAMP(20); gather16<2>(wsr, qoff, 16, tag, qg); }
      STAMP(1); WSTAMP(1);
      qq[0] = qg[0][0]; qq[1] = qg[0][2]; qq[2] = qg[1][0]; qq[3] = qg[1][2];
    }
    if constexpr (v >= VQ && v < VS) {
      constexpr int sl = v - VQ, bt = sl >> 1;
      float xs[8];
#pragma unroll
      for (int i = 0; i < 8; ++i) {
        float d = 0.f;
        if (sl * 8 + i < NL) {
#pragma unroll
          for (int k = 0; k < 4; ++k) d = dot2(kr[sl * 8 + i][k], qq[k], d);
        }
        xs[i] = d;
      }
#pragma unroll
      for (int h = 0; h < 4; ++h) xs[h] = sel_add<XOR7>(xs[h], xs[h + 4], c & 4);
#pragma unroll
      for (int h = 0; h < 2; ++h) xs[h] = sel_add<XOR2>(xs[h], xs[h + 2], c & 2);
      xs[0] = sel_add<XOR1>(xs[0], xs[1], c & 1);
      if constexpr ((sl & 1) || sl == NSL - 1) {
        const float t = (sl & 1) ? sel_add<XOR8>(xlo, xs[0], c & 8) : xs[0] + dpp<XOR8>(xs[0]);
        const float s = rbf(rbf(t) * 0.08838834764831845f);
        sc[bt] = (bt * 16 + c < SK0 || (bt * 16 + c >= first && bt * 16 + c < NL)) ? s : -__builtin_inff();
        PIN(sc[bt]);
      } else {
        xlo = xs[0];
        PIN(xlo);
      }
    }
    if constexpr (v == VS) {
      float mloc = sc[0];
#pragma unroll
      for (int bt = 1; bt < NB; ++bt) mloc = fmaxf(mloc, sc[bt]);
      m = fmaxf(wave_max(mloc), -3.0e38f);
      float ls = 0.f;
#pragma unroll
      for (int bt = 0; bt < NB; ++bt) { pr[bt] = __expf(sc[bt] - m); ls += pr[bt]; }
      const float l = wave_sum(ls);
      if (lane == 0) { sm.ml[wave][0] = m; sm.ml[wave][1] = l; }
      if (c == ((NL - 1) & 15)) sm.pl[g][wave] = pr[NB - 1];
      STAMP(2); WSTAMP(2);
    }
    if constexpr (v >= SK1) pv_row(IC<v - SK1 + SK0 - WV>{});
    if constexpr (v < SK0 || v >= SK0 + WV) sfor<cons_end(v - 1), cons_end(v)>(pv_row);
    if constexpr (v == SK1 + 1) {
      float p[8];
#pragma unroll
      for (int w = 0; w < 8; ++w) p[w] = sm.pl[g][w] * __expf(sm.ml[w][0] - m);
#pragma unroll
      for (int w = 0; w < 8; ++w) pl8[w] = pack_bf(p[w], p[w]);
#pragma unroll
      for (int w = 0; w < 8; ++w) PIN(pl8[w]);
    }
    if constexpr (v == MLS - 1) { side_pin(); if (is_up) up_prep(); PIN(ga); PIN(xin); PIN(cc1); }
    if constexpr (v == UPREP - 1) wo_addr();
    if constexpr (v == UPREP) {
#pragma unroll
      for (int k = 0; k < 8; ++k) asm volatile("" : "=v"(bf[k]));
      if (part == 0) wo_prep(); else if (is_up) up_frags();
#pragma unroll
      for (int k = 0; k < 8; ++k) PIN(bf[k]);
    }
    if constexpr (v == UPREP + 1) {
      if (is_up) {
        const u32x4* a1 = ap + 128 + ((lane >> 1) & 1) * 128;
        f32x4 d0 = {0.f, 0.f, 0.f, 0.f}, d1 = {0.f, 0.f, 0.f, 0.f};
#pragma unroll
        for (int rb = 0; rb < 4; ++rb) { d0 = mfma16(a1[rb * 8], bf[rb], d0); d1 = mfma16(a1[(rb + 4) * 8], bf[rb + 4], d1); }
        Af = par ? d0[1] + d1[1] : d0[0] + d1[0]; Bf = par ? d0[3] + d1[3] : d0[2] + d1[2];
        Af += dpp<XOR8>(Af); Bf += dpp<XOR8>(Bf);
      }
      PIN(Af); PIN(Bf);
    }
    if (v == 10 || v == 16) STAMP(15 + v / 6);
    if (v == 20) STAMP(18);
    if (v == 32) STAMP(19);
    if constexpr (v == MLS) {
      LDS_BARRIER();
      float mm[8], ll[8];
#pragma unroll
      for (int w = 0; w < 8; ++w) { const f32x2 t = *reinterpret_cast<const f32x2*>(&sm.ml[w][0]); mm[w] = t[0]; ll[w] = t[1]; }
      float Mwg = mm[0], Lwg = 0.f;
#pragma unroll
      for (int w = 1; w < 8; ++w) Mwg = fmaxf(Mwg, mm[w]);
      if (wave == 0) {
#pragma unroll
        for (int w = 0; w < 8; ++w) Lwg = fmaf(__expf(mm[w] - Mwg), ll[w], Lwg);
        if (lane == 0) st16_sc1(wsr, G_ML + ((head * 2 + b) * 4 + part) * 16, __float_as_uint(Mwg), __float_as_uint(Lwg), tag);
      }
      const float fw = __expf(m - Mwg);
      m = Mwg;
#pragma unroll
      for (int bt = 0; bt < NB; ++bt) { pb[bt] = pack_bf(pr[bt] * fw, pr[bt] * fw); PIN(pb[bt]); }
#pragma unroll
      for (int k = 0; k < 4; ++k) { pm[k] = (c >> 1 & 3) == k ? ((c & 1) ? 0xffff0000u : 0x0000ffffu) : 0u; PIN(pm[k]); }
    }
    SB();
    return true;
  });
  asm volatile(".Ltail_start:");
  if (wave != 0) {
    pv_row(IC<SK0 - 1>{});
    WAIT_VM(WV); SB();
    sfor<NL - 1 - WV, NL - 1>(pv_row);
    *LDSP(f32x4, adst0) = acc;
    WSTAMP(14);
    LDS_BARRIER();
  } else {
    u32x4 xr[8], am[8];
#pragma unroll
    for (int w = 0; w < 8; ++w) am[w] = u32x4{pl8[w] & pm[0], pl8[w] & pm[1], pl8[w] & pm[2], pl8[w] & pm[3]};
#pragma unroll
    for (int w = 0; w < 8; ++w) PIN(am[w]);
    {
      const u32 so = __builtin_amdgcn_readlane(sot, NL - 1);
      gate();
      SB();
#pragma unroll
      for (int w = 0; w < 8; ++w) xr[w] = ld16<AUX_NT>(rv, lane * 16, so ^ ((w & 1) ? col(P, 7) : 0u) ^ ((w & 2) ? col(P, 8) : 0u) ^ ((w & 4) ? col(P, 9) : 0u));
      SB();
    }
    u32 rsum = lds_addr(&sm.red[0][c * 8 + (g & 1) * 4]);
    pv_row(IC<SK0 - 1>{});
    WAIT_VM(WV + 8); SB();
    sfor<NL - 1 - WV, NL - 3>(pv_row);
    if (__builtin_expect(!all_ok((mlg[1] == tag) & (mlg[3] == tag)), 0)) {
#pragma nounroll
      for (int spin = 0; spin < SPIN_MAX; ++spin) {
        CFENCE();
        mlg = ld16<AUX_SC1>(wsr, mlo, 0);
        if (all_ok((mlg[1] == tag) & (mlg[3] == tag))) break;
      }
    }
    merge_factor(mlg);
    sfor<NL - 3, NL - 1>(pv_row);
    STAMP(14); WSTAMP(14);
    LDS_BARRIER();
    STAMP(15);
    f32x4 t[8];
#pragma unroll
    for (int w = 1; w < 8; ++w) t[w] = LDSP(f32x4, rsum)[w * 32];
    acc = ((acc + t[1]) + (t[2] + t[3])) + ((t[4] + t[5]) + (t[6] + t[7]));
    PIN(acc);
    STAMP(3);
    SB();
#pragma unroll
    for (int w = 0; w < 8; ++w) acc = mfma16(am[w], xr[w], acc);
  }
  WAIT_VM(0);
  acc *= fmrg;
  {
    const u32 w0 = min(pack_bf(acc[0], acc[1]), SENT - 1u), w1 = min(pack_bf(acc[2], acc[3]), SENT - 1u);
    const auto e0 = __builtin_amdgcn_permlane16_swap(w0, w0, false, false), e1 = __builtin_amdgcn_permlane16_swap(w1, w1, false, false);
    __builtin_amdgcn_raw_buffer_store_b128(u32x4{e0[0], e1[0], e0[1], e1[1]}, wsr, pso, 0, AUX_SC1);
  }
  st8_sc1(wsr, nto, (u32)wall_clock64() + nadd, tag);
  SB();
  STAMP(4);

  if (part == 2) FINISH();
  if (part == 0) {
    __builtin_amdgcn_s_barrier();
#pragma nounroll
    while (wleft > 0) piece();
    bool late = false;
    u32x4 og[2];
    if (wdef) {
      late = true;
      while ((int)(((u32)wall_clock64() - t0) << 8) < t1 - 20 * 256) if (__builtin_amdgcn_s_getreg(7 | (3 << 11)) == 0) { late = false; break; }
      if (__builtin_expect(late, 0)) { until(t1); CFENCE(); og[0] = ld16<AUX_SC1>(wsr, po, 0); SB(); WAIT_VM(1); } else { WAIT_VM(0); }
      CFENCE();
      if (P.WD > 4) wo_prep(0, 1);
      wo_prep(1, 2);
#pragma unroll
      for (int k = 0; k < 8; ++k) PIN(bf[k]);
      SB();
    }
    if (__builtin_expect(!late, 1)) { until(t1); CFENCE(); og[0] = ld16<AUX_SC1>(wsr, po, 0); }
    STAMP(11);
    SB();
    f32x4 d[4];
    auto wo_mm = [&]() {
      const f32x4 z = {0.f, 0.f, 0.f, 0.f};
#pragma unroll
      for (int t = 0; t < 4; ++t) { d[t] = mfma16(og[0], bf[t], z); og[1][t] = __float_as_uint(dpp<XOR8>(u2f(og[0][t]))); }
      SB();
#pragma unroll
      for (int t = 0; t < 4; ++t) d[t] = mfma16(og[1], bf[4 + t], d[t]);
    };
    auto p_bad = [&]() { return !all_ok(max(max(og[0][0], og[0][1]), max(og[0][2], og[0][3])) != SENT); };
    wo_mm();
    if (__builtin_expect(p_bad(), 0)) {
#pragma nounroll
      for (int spin = 0; spin < SPIN_MAX; ++spin) {
        __builtin_amdgcn_s_sleep(1);
        CFENCE();
        og[0] = ld16<AUX_SC1>(wsr, po, 0);
        if (!p_bad()) break;
      }
      wo_mm();
      SB();
      asm volatile(".Lend_d:");
    }
    STAMP(5); WSTAMP(5);
    {
      SB();
      f32x4 y; f32x2 h[4];
#pragma unroll
      for (int t = 0; t < 4; ++t) { h[t] = d[t].xy + d[t].zw; PIN(h[t]); }
#pragma unroll
      for (int t = 0; t < 4; ++t) { y[t] = h[t][0] + h[t][1]; PIN(y[t]); }
      *LDSP(f32x4, rdst) = y;
    }
    STAMP(16);
    LDS_BARRIER();
    STAMP(17);
    if (wave < 2) {
      f32x2 v[4];
#pragma unroll
      for (int w = 0; w < 4; ++w) v[w] = f32x2{LDSP(float, rsrc)[w * 256], LDSP(float, rsrc)[w * 256 + 128]};
      const f32x2 h = (v[0] + v[1]) + (v[2] + v[3]);
      const float y = h[0] + h[1];
      __builtin_amdgcn_raw_buffer_store_b32(min(pack_bf(dpp<0xA0>(y), dpp<0xF5>(y)), SENT - 1u), wsr, xo, 0, AUX_SC1);
      asm volatile(".Lend_a:");
    }
    STAMP(6);
    FINISH();
  }
  if (part == 3) {
    if (!has_w) FINISH();
    u32x4 b2[4];
    {
      __builtin_amdgcn_s_barrier();
      const rsrc_t r2 = mk_rsrc(w2 + (size_t)(blk * 32 + wave * 4) * 512, 4096u);
      const u32 l2 = (u32)(uintptr_t)(&sm.w2b[wave * 4][0]);
#pragma unroll
      for (int n = 0; n < 4; ++n) dma16(r2, (lane ^ (n * 2) ^ ((wave & 2) << 2)) * 16, n * 1024, l2 + n * 1024);
    }
    const u32 tdst = lds_addr(&sm.hb[hpos]);
    const u32 sdst = lds_addr(&sm.stat[wave][lane]);
    const u32 ssrc = lds_addr(&sm.stat[(g >> 1) * 4 + ((lane >> 3) & 3)][(lane & 7) * 4]);
    const u32 adst = lds_addr((c < 4 && (g & 1) == 0) ? &sm.actl[g >> 1][wave * 4 + c] : reinterpret_cast<u16*>(&sm.dummy[lane]));
    const u32 asrc = lds_addr(&sm.actl[lane & 1][g * 8]);
    const u32 apa = lds_addr(&sm.hb[(c >> 3) * 512 + (c & 3) * 64 + ((c >> 2) & 1) * 32 + g * 8]);
    constexpr u32 xs2 = 16384;
    u32 xoff = dup(G_XP) + near + g * 2048 + ub * 1024 + (wave & 3) * 256 + c * 16;
    u32x4 one;
#pragma unroll
    for (int k = 0; k < 4; ++k) one[k] = ((c & 2) == 0 && (c >> 2) == k) ? ((c & 1) ? 0x3f800000u : 0x00003f80u) : 0u;
    PIN(one);
    u32 ooff = dup(G_OP + (blk * 512 + tid) * 8);
    float Afp = Af, Bfp = Bf;
    PIN(xoff); PIN(ooff); PIN(Afp); PIN(Bfp);
    f32x2 gc = ga * cc1, xc = xin * cc1;
    PIN(ga); PIN(xin); PIN(gc); PIN(xc);
    u32 w2a = (u32)(uintptr_t)(&sm.w2b[0][0]) + (g * 8 + ((lane >> 2) & 3)) * 1024 + (wave ^ (g & 1)) * 128 + (lane & 15) * 8;
    PIN(w2a);
    if (wave == 1) {
      due = t1 + P.N1 * 256;
      const int tn = notices();
      due = tn + P.X2 * 256;
      until(due);
    }
    asm volatile("s_waitcnt vmcnt(0)" ::: "memory");
    STAMP(9);
    __builtin_amdgcn_s_barrier();
    STAMP(11);
    CFENCE();
    u32x4 xg[2] = {ld16<AUX_SC1>(wsr, xoff, 0), ld16<AUX_SC1>(wsr, xoff, xs2)};
    SB();
#pragma unroll
    for (int t = 0; t < 4; ++t) {
      const u32x2 lo = tr_read(w2a ^ (t * 32)), hi = tr_read((w2a ^ (t * 32)) + 4096);
      b2[t] = u32x4{lo[0], lo[1], hi[0], hi[1]};
    }
#pragma unroll
    for (int t = 0; t < 4; ++t) PIN(b2[t]);
    SB();
    const f32x4 z4 = {0.f, 0.f, 0.f, 0.f};
    auto xp_bad = [&]() { return !all_ok(max(max(max(xg[0][0], xg[0][1]), max(xg[0][2], xg[0][3])), max(max(xg[1][0], xg[1][1]), max(xg[1][2], xg[1][3]))) != SENT); };
    f32x4 v = mfma16(one, xg[1], mfma16(one, xg[0], z4));
    if (__builtin_expect(xp_bad(), 0)) {
#pragma nounroll
      for (int spin = 0; spin < SPIN_MAX; ++spin) {
        __builtin_amdgcn_s_sleep(1);
        CFENCE();
        xg[0] = ld16<AUX_SC1>(wsr, xoff, 0); xg[1] = ld16<AUX_SC1>(wsr, xoff, xs2);
        if (!xp_bad()) break;
      }
      v = mfma16(one, xg[1], mfma16(one, xg[0], z4));
      SB();
      asm volatile(".Lend_e:");
    }
    STAMP(5); WSTAMP(5);
    {
      const f32x2 a = v.xy, hc = __builtin_elementwise_fma(gc, a, xc), x1 = __builtin_elementwise_fma(ga, a, xin);
      *LDSP(u32, tdst) = pack_bf(hc[0], hc[1]);
      *LDSP(float, sdst) = swap32_add(x1[0] + x1[1], fmaf(x1[0], x1[0], x1[1] * x1[1]));
    }
    STAMP(16);
    LDS_BARRIER();
    STAMP(17);
    {
      const f32x4 z = {0.f, 0.f, 0.f, 0.f};
      const u32x4 af0 = LDSP(u32x4, apa)[0];
      SB();
      const f32x4 s4 = LDSP(f32x4, ssrc)[0], q4 = LDSP(f32x4, ssrc)[8];
      const u32x4 af1 = LDSP(u32x4, apa)[32];
      SB();
      f32x4 d[4];
      d[0] = mfma16(af0, bf[0], z); d[1] = mfma16(af0, bf[1], z);
      SB();
      const f32x2 s2 = s4.xy + s4.zw, q2 = q4.xy + q4.zw;
      float s = s2[0] + s2[1], q = q2[0] + q2[1];
      SB();
      d[2] = mfma16(af0, bf[2], z);
      SB();
      s = swap16_add(s, q);
      SB();
      d[3] = mfma16(af0, bf[3], z);
      SB();
      s += dpp<XOR1>(s); s += dpp<XOR2>(s);
      SB();
      d[0] = mfma16(af1, bf[4], d[0]);
      SB();
      s += dpp<XOR7>(s); s += dpp<XOR8>(s);
      SB();
      d[1] = mfma16(af1, bf[5], d[1]);
      SB();
      const auto sq = __builtin_amdgcn_permlane16_swap(__float_as_uint(s), __float_as_uint(s), false, false);
      const float mean = u2f(sq[0]) * (1.f / 512.f);
      SB();
      d[2] = mfma16(af1, bf[6], d[2]);
      SB();
      const float var = fmaf(u2f(sq[1]), 1.f / 512.f, -mean * mean);
      SB();
      d[3] = mfma16(af1, bf[7], d[3]);
      SB();
      float ri = __builtin_amdgcn_rsqf(fmaxf(var, 0.f) + 1e-5f);
      float cf = fmaf(-ri * mean, Afp, Bfp);
      float rk = ri * -1.44269504f, ck = cf * -1.44269504f;
      PIN(ri); PIN(cf); PIN(rk); PIN(ck);
      SB();
      PIN(d[0]); PIN(d[1]);
      float y = d[0][0] + d[1][1];
      PIN(y); PIN(d[2]);
      y += d[2][2];
      PIN(y); PIN(d[3]);
      y += d[3][3];
      y = __builtin_bit_cast(float, __builtin_amdgcn_update_dpp(__builtin_bit_cast(int, y), __builtin_bit_cast(int, y), XOR8, 0xa, 0xf, false));
      y = swap16_add(y, y);
      const float ex = __builtin_amdgcn_exp2f(fmaf(y, rk, ck));
      y = fmaf(y, ri, cf);
      const float gu = y * dpp<0x104>(y);
      *LDSP(u16, adst) = (u16)pack_bf(gu * __builtin_amdgcn_rcpf(1.f + ex), 0.f);
    }
    STAMP(18);
    LDS_BARRIER();
    STAMP(10);
    {
      const u32x4 af = *LDSP(u32x4, asrc);
      const f32x4 z = {0.f, 0.f, 0.f, 0.f};
      f32x4 d0 = mfma16(af, b2[0], z), d1 = mfma16(af, b2[1], z), d2 = mfma16(af, b2[2], z), d3 = mfma16(af, b2[3], z);
      asm volatile("" : "+v"(d0), "+v"(d1), "+v"(d2), "+v"(d3));
      const bool g1 = g & 1, g2 = g & 2;
      const float e0 = g1 ? d1[0] : d0[0], e1 = g1 ? d1[1] : d0[1], f0 = g1 ? d3[0] : d2[0], f1 = g1 ? d3[1] : d2[1];
      const u32x2 ov = {pack_bf(g2 ? f0 : e0, g2 ? f1 : e1), tag};
      __builtin_amdgcn_raw_buffer_store_b64(ov, wsr, ooff, near ^ 8192u, AUX_SC1);
      __builtin_amdgcn_raw_buffer_store_b64(ov, wsr, ooff, near, AUX_SC1);
    }
    asm volatile(".Lend_b:");
    STAMP(6);
    FINISH();
  }
  if (wave == 0) {
    if (!all_ok((sg[0][1] == tag) & (sg[0][3] == tag) & (sg[1][1] == tag) & (sg[1][3] == tag))) {
      u32x4 t[2];
      gather16<2>(wsr, G_MOD + (1024 + fd) * 8, 1536 * 8, tag, t);
      sg[0] = t[0]; sg[1] = t[1];
    }
    u32 xa;
    {
      due = t1 + P.N2 * 256;
      const int tn = notices();
      due = tn + P.X3 * 256;
      until(due);
    }
    const bool hb = g & 1;
#pragma nounroll
    for (int spin = 0; spin < SPIN_MAX; ++spin) {
      CFENCE();
      xa = __builtin_amdgcn_raw_buffer_load_b32(wsr, dup(G_XP) + near + (g >> 1) * 16384 + (c >> 2) * 2048 + (g & 1) * 1024 + fd * 2, 0, AUX_SC1);
      if (all_ok(xa != SENT)) break;
      __builtin_amdgcn_s_sleep(1);
    }
    float a0 = lo_f(xa), a1 = hi_f(xa);
    a0 += dpp<0x124>(a0); a1 += dpp<0x124>(a1);
    a0 += dpp<XOR8>(a0); a1 += dpp<XOR8>(a1);
    a0 = swap32_add(a0, a0); a1 = swap32_add(a1, a1);
    f32x2 xv = {rbf(lo_f(sx2) + rbf((hb ? hi_f(sg[0][0]) : lo_f(sg[0][0])) * rbf(a0))), rbf(hi_f(sx2) + rbf((hb ? hi_f(sg[0][2]) : lo_f(sg[0][2])) * rbf(a1)))};
    f32x2 gv = {hb ? hi_f(sg[1][0]) : lo_f(sg[1][0]), hb ? hi_f(sg[1][2]) : lo_f(sg[1][2])};
    PIN(xv); PIN(gv);
    if (wi == 0 && lane == 0) __builtin_amdgcn_raw_buffer_store_b32(tag, wsr, WS_EPOCH, 0, AUX_SC1);
    STAMP(10);
    due = max(due + P.X4 * 256, (int)(((u32)wall_clock64() - t0) << 8) + P.GX * 256);
    const bool last = lane < (NUP - 32) * 4;
    u32 o0 = dup(G_OP + (lane >> 2) * 4096 + fd * 8) + near, o2 = last ? o0 : OOB, tg2 = last ? tag : 0u;
    const rsrc_t rout = mk_rsrc(out, 2048u);
    u32 oo = (lane & 44) == 0 ? (g * 512 + fd) * 2 : OOB;
    const u32 pk = (c & 10) ? 0u : ((c & 4) ? 0x3f800000u : 0x00003f80u);
    u32x4 one = {(c & 1) ? 0u : pk, 0u, (c & 1) ? pk : 0u, 0u};
    PIN(o0); PIN(o2); PIN(tg2); PIN(oo); PIN(one);
    __builtin_amdgcn_raw_buffer_store_b32(pack_bf(xv[0], xv[1]), rout, oo, 0, AUX_SC1);
    u32 s1_ = 131072, s2_ = 262144;
    asm volatile("" : "+s"(s1_), "+s"(s2_));
    until(due);
    f32x4 sv;
#pragma nounroll
    for (int spin = 0; spin < SPIN_MAX; ++spin) {
      CFENCE();
      const u32x4 og[3] = {ld16<AUX_SC1>(wsr, o0, 0), ld16<AUX_SC1>(wsr, o0, s1_), ld16<AUX_SC1>(wsr, o2, s2_)};
      sv = f32x4{0.f, 0.f, 0.f, 0.f};
#pragma unroll
      for (int k = 0; k < 3; ++k) sv = mfma16(one, og[k], sv);
      SB();
      u32 bad = ((og[0][1] ^ tag) | (og[0][3] ^ tag) | (og[1][1] ^ tag)) | ((og[1][3] ^ tag) | (og[2][1] ^ tg2) | (og[2][3] ^ tg2));
      PIN(bad);
      SB();
      float s0 = sv[0] + dpp<0x124>(sv[0]), s1 = sv[1] + dpp<0x124>(sv[1]);
      PIN(s0); PIN(s1);
      s0 += dpp<XOR8>(s0); s1 += dpp<XOR8>(s1);
      PIN(s0); PIN(s1);
      __builtin_amdgcn_raw_buffer_store_b32(pack_bf(fmaf(gv[0], s0, xv[0]), fmaf(gv[1], s1, xv[1])), rout, oo, 0, AUX_SC1);
      SB();
      if (all_ok(bad == 0u)) break;
      __builtin_amdgcn_s_sleep(1);
    }
    STAMP(7);
    asm volatile(".Lend_c:");
  }
  FINISH();
}

extern "C" void kernel_launch(void* const* d_in, const int* in_sizes, int n_in,
                              void* d_out, int out_size, void* d_ws, size_t ws_size,
                              hipStream_t stream) {
  fused_kernel<<<256, 512, 0, stream>>>((const u16*)d_in[1], (const u16*)d_in[2], (const u16*)d_in[4], (const u16*)d_in[3], (const u16*)d_in[10], (const u16*)d_in[6], (const u16*)d_in[7],
                                        (unsigned char*)d_ws, (const u16*)d_in[0], (const u16*)d_in[9], (const u16*)d_in[8], (u16*)d_out);
}
```

```cpp
#include <hip/hip_runtime.h>
#include <stdint.h>

typedef unsigned int u32;
typedef unsigned short u16;
typedef unsigned long long u64;
typedef __attribute__((ext_vector_type(4))) unsigned u32x4;
typedef __attribute__((ext_vector_type(2))) unsigned u32x2;
typedef __attribute__((ext_vector_type(2))) __bf16 bf16x2;
typedef __attribute__((ext_vector_type(4))) float f32x4;
typedef __attribute__((ext_vector_type(2))) float f32x2;
typedef __amdgpu_buffer_rsrc_t rsrc_t;

constexpr int NH = 32, SK = 16384, DH = 128, WIN = 4096, FF = 1376;
struct Prm { int E0, ES, E1, E2, E2C, EU, TEND, DT_UP, L0, L1, X1, X2, X3, X4, NE, FE, M0, M1, M2, M3, M4, GS, GX, N1, N2, GA, WD, DT_WO, LW, LU, EW, PM, NF, NR, C0, A0, AG, AC, TS, TW, TK, TG, GB, B0, BG, BF, D0, DG, TC, LM, C1, W[9]; };
constexpr Prm PRM0 = {32, 20, 32, 32, 54, 58, 1980, 60, 12, 30, 88, 70, 100, 89, 3, 8, 0x2511f55b, 0x2b7425d5, 0x6017665, 0x17e0b9ec, 0x31944305, 63, 30, 36, 62, 30, 4, 100, 45, 15, 0, 0x0, 15, 14, 3, 30, 4, 4, 509, 228, 60, 20, 7, 74, 26, 8, 8, 0, 0, 120, 2, 0x77777777, 0x77777777, 0x77777777, 0x77777777, 0x77777777, 0x44444477, 0x44444444, 0x44444444, 0x44444444};
constexpr int NL = 37;
constexpr int SK0 = 21, SK1 = 32;
constexpr int NCMIN = (NL - SK1 + SK0) * 8;
constexpr int WK = 8, WV = 5;
constexpr int WUSE = 12, LNS = 7;
constexpr int QP = 14, QD = 4;
constexpr int VQ = 6;
constexpr int NSL = (NL + 7) / 8;
constexpr int VS = VQ + NSL;
constexpr int MLS = VS + 2;
constexpr int VSIDE = MLS - 5, UPREP = MLS + 6;
constexpr int CPF = 17;
constexpr int SMO = 1;
constexpr int RPS = 24, RPP = 28;
constexpr int PRB = 12;
constexpr int TBS = 7, TBP = 10, TBL = 13, TBU = 17;
constexpr int window(int s, int w) {
  int n = w;
  for (int u = s - w + 1; u <= s; ++u) n += u == SMO ? 6 : (u == TBL || u == SK1 || u == NL + CPF || u == NL + SK1 ? 1 : (u == QP || u == NL + VQ - 4 ? 2 : (u == NL + VSIDE ? 4 : 0)));
  return n;
}
constexpr int cons_end(int v) {
  const int a = v - WV + 1, c = (v - MLS) * 4;
  const int m = a < c ? a : c;
  return m < 0 ? 0 : (m > NL ? NL : m);
}
constexpr u32 col(const Prm& P, int j) { const int m[6] = {P.M0, P.M1, P.M2, P.M3, P.M4, P.PM}; return (u32)(m[j / 3] >> (j % 3 * 10) & 1023) << 10; }
constexpr u32 first_col(const Prm& P, int k) {
  if (k < 4) return col(P, 12 + k) | 1u << (24 + k);
  if (k < 6) return col(P, k - 4);
  if (k < 8) return col(P, 4 + k) | 1u << (16 + k);
  if (k < 11) return col(P, k - 1);
  return (col(P, 2) ^ col(P, 3) ^ col(P, 4) ^ col(P, 5) ^ col(P, 6) ^ col(P, 10) ^ col(P, 11)) | 0xf00000u;
}
constexpr bool one_to_one(const Prm& P) {
  u32 c[10] = {};
  for (int j = 0; j < 10; ++j) c[j] = col(P, j);
  for (int j = 0; j < 10; ++j) {
    if (c[j] == 0) return false;
    const u32 low = c[j] & (0u - c[j]);
    for (int k = j + 1; k < 10; ++k) if (c[k] & low) c[k] ^= c[j];
  }
  return true;
}
static_assert(one_to_one(PRM0), "every KB of the window exactly once");
struct Tab { u32 e[512]; };
constexpr Tab make_tab(const Prm& P) {
  Tab T{};
  for (int bid = 0; bid < 256; ++bid) {
    const int xcd = bid & 7, part = bid >> 4 & 3, i8 = (bid >> 3 & 1) | bid >> 6 << 1, wi = i8 * 8 + xcd;
    const bool has_mod = wi < 48, has_up = wi >= 21;
    const int cb0 = (i8 + P.C0) & 7, cb1 = (i8 + P.C1) & 7, ms = wi - 20;
    const int mi = wi < 8 ? wi : (wi < 16 ? 8 + ((xcd + 4) & 7) : (wi < 20 ? (wi - 14) * 8 + 3 : 16 + ms / 7 * 8 + ms % 7 + (ms % 7 >= 3 ? 1 : 0)));
    const int ui = xcd * 5 + (xcd > 5 ? xcd - 5 : 0) + (i8 == 6 ? 0 : (xcd >= 5 ? 1 : 0) + (i8 == 7 ? 4 : i8 - 2));
    const bool edge = i8 >= 6;
    const int wd = cb0 == 3 ? 0 : P.WD;
    const int e[4] = {P.E0 - 4 * wd + (cb0 == 3 ? P.ES : 0), P.E1 + (cb1 == 3 ? P.ES : 0), has_mod ? (wi < 16 ? P.E2C : P.E2) + ((mi & 7) == 3 ? P.ES : 0) : 0, has_up ? P.EU + (edge ? P.TC : 0) : 0};
    int tw[4] = {}, win[4] = {}, span[4] = {}, k0[4] = {};
    for (int k = 0; k < 4; ++k) {
      const int np = k == 0 ? 8 - wd : 8;
      tw[k] = np ? e[k] * P.TW / (16 * np) : 0;
      k0[k] = P.NF + tw[k] * np / 2 > P.TK ? P.NF + tw[k] * np / 2 : P.TK;
      span[k] = P.TEND - k0[k] - P.TG - (k == 0 ? (wd ? P.DT_WO : P.A0) : (k == 3 && has_up ? P.DT_UP : 0));
      win[k] = span[k] - (k == 0 && wd ? P.EW : (k == 3 && has_up ? P.GA + (edge ? P.LM : 0) : 0));
    }
    int n[4] = {NCMIN / 8, NCMIN / 8, NCMIN / 8, NCMIN / 8};
    for (int left = 128 - NCMIN / 2; left > 0; --left) {
      int best = -1;
      for (int k = 3; k >= 0; --k)
        if (n[k] < NL && (best < 0 || n[k] * win[best] < n[best] * win[k])) best = k;
      ++n[best];
    }
    const int c[4] = {0, n[0] - 1, n[0] + n[1] - 2, n[0] + n[1] + n[2] - 3};
    const int nl = n[part];
    const bool has_w = part < 2 || (part == 2 ? has_mod : has_up);
    const int blk = !has_w ? 0 : (part == 0 ? cb0 : (part == 1 ? xcd * 8 + cb1 : (part == 2 ? mi : ui)));
    T.e[2 * bid] = (u32)(c[part] | nl << 7 | blk << 13 | (has_w ? 1 : 0) << 19 | (part == 0 ? wd : 0) << 20) | (u32)tw[part] << 24;
    T.e[2 * bid + 1] = (u32)(span[part] * P.TS / (4 * (nl - 1)) | span[part] * (1024 - P.TS) / (4 * (nl - 1)) << 16);
  }
  return T;
}
constexpr int step_range(const Prm& P, bool mx_) {
  int mx = 0, mn = 1 << 20;
  const Tab T = make_tab(P);
  for (int bid = 0; bid < 256; ++bid) { const int n = T.e[2 * bid] >> 7 & 63; mx = n > mx ? n : mx; mn = n < mn ? n : mn; }
  return mx_ ? mx : mn;
}
extern "C" __attribute__((used, visibility("protected"))) __constant__ const Tab wg_tab = make_tab(PRM0);
static_assert(step_range(PRM0, true) <= NL && step_range(PRM0, false) >= NCMIN / 8, "a wave's key range does not fit its load steps");
static_assert(UPREP + 1 < SK0 && QP + QD + 1 < SK0 && cons_end(SK0 - 1) == SK0 - WV && NL - SK1 == WV, "steps every wave runs");
constexpr int NUP = FF / 32;
constexpr int SPIN_MAX = 1 << 17;

constexpr u32 WS_EPOCH = 0;
constexpr u32 G_TB = 1024;
constexpr u32 G_MOD = 4096;
constexpr u32 G_Q = G_MOD + 3072 * 8;
constexpr u32 G_ML = G_Q + 4096 * 8;
constexpr u32 G_U = G_ML + 256 * 16;
constexpr u32 G_P = 0;
constexpr u32 G_XP = G_P + 256 * 256;
constexpr u32 G_OP = G_XP + 8 * 512 * 4;
constexpr u32 G_NT = G_OP + 43 * 512 * 8;
constexpr u32 G_PB = G_OP + 44 * 512 * 8;
constexpr u32 G_VT = G_U + 2 * (G_PB + 8192);
constexpr u32 WS_END = G_VT + 1024;
__device__ __forceinline__ u32 dup(u32 o) { return G_U + ((o >> 13) << 14) + (o & 8191u); }
constexpr u32 SENT = 0xffffffffu;
constexpr u32 OOB = 0x40000000u;
#define WST 0

constexpr int AUX_NT = 2, AUX_SC1 = 16;

__device__ __forceinline__ float lo_f(u32 v) { return __uint_as_float(v << 16); }
__device__ __forceinline__ float hi_f(u32 v) { return __uint_as_float(v & 0xffff0000u); }
__device__ __forceinline__ float rbf(float x) { return (float)(__bf16)x; }
__device__ __forceinline__ u32 pack_bf(float lo, float hi) {
  bf16x2 r; r[0] = (__bf16)lo; r[1] = (__bf16)hi; return __builtin_bit_cast(u32, r);
}
__device__ __forceinline__ float dot2(u32 k, u32 q, float acc) {
  return __builtin_amdgcn_fdot2_f32_bf16(__builtin_bit_cast(bf16x2, k), __builtin_bit_cast(bf16x2, q), acc, false);
}
template <int CTRL> __device__ __forceinline__ float dpp(float x) {
  return __builtin_bit_cast(float, __builtin_amdgcn_mov_dpp(__builtin_bit_cast(int, x), CTRL, 0xf, 0xf, true));
}
constexpr int XOR1 = 0xB1, XOR2 = 0x4E, XOR7 = 0x141, XOR8 = 0x128;
__device__ __forceinline__ float swap32_add(float a, float b) {
  auto t = __builtin_amdgcn_permlane32_swap(__float_as_uint(a), __float_as_uint(b), false, false);
  return __uint_as_float(t[0]) + __uint_as_float(t[1]);
}
__device__ __forceinline__ float swap16_add(float a, float b) {
  auto t = __builtin_amdgcn_permlane16_swap(__float_as_uint(a), __float_as_uint(b), false, false);
  return __uint_as_float(t[0]) + __uint_as_float(t[1]);
}
__device__ __forceinline__ float wave_sum(float x) {
  x += dpp<XOR1>(x); x += dpp<XOR2>(x); x += dpp<XOR7>(x); x += dpp<XOR8>(x);
  x = swap16_add(x, x);
  return swap32_add(x, x);
}
__device__ __forceinline__ float wave_max(float x) {
  x = fmaxf(x, dpp<XOR1>(x)); x = fmaxf(x, dpp<XOR2>(x)); x = fmaxf(x, dpp<XOR7>(x)); x = fmaxf(x, dpp<XOR8>(x));
  auto s = __builtin_amdgcn_permlane16_swap(__float_as_uint(x), __float_as_uint(x), false, false);
  x = fmaxf(__uint_as_float(s[0]), __uint_as_float(s[1]));
  auto t = __builtin_amdgcn_permlane32_swap(__float_as_uint(x), __float_as_uint(x), false, false);
  return fmaxf(__uint_as_float(t[0]), __uint_as_float(t[1]));
}
template <int CTRL> __device__ __forceinline__ float sel_add(float lo, float hi, bool up) {
  const float t1 = lo + dpp<CTRL>(lo), t2 = hi + dpp<CTRL>(hi);
  return up ? t2 : t1;
}
template <int I> __device__ __forceinline__ float bc16(float x) {
  return __builtin_bit_cast(float, __builtin_amdgcn_update_dpp(0, __builtin_bit_cast(int, x), 0x150 + I, 0xf, 0xf, false));
}
__device__ __forceinline__ float bcast16(float x, int i) {
  switch (i) {
    case 0: return bc16<0>(x); case 1: return bc16<1>(x); case 2: return bc16<2>(x); case 3: return bc16<3>(x);
    case 4: return bc16<4>(x); case 5: return bc16<5>(x); case 6: return bc16<6>(x); case 7: return bc16<7>(x);
    case 8: return bc16<8>(x); case 9: return bc16<9>(x); case 10: return bc16<10>(x); case 11: return bc16<11>(x);
    case 12: return bc16<12>(x); case 13: return bc16<13>(x); case 14: return bc16<14>(x); default: return bc16<15>(x);
  }
}
__device__ __forceinline__ float silu_f(float x) { return x * __builtin_amdgcn_rcpf(1.f + __expf(-x)); }

template <int AUX> __device__ __forceinline__ u32x4 ld16(rsrc_t r, u32 voff, u32 soff) {
  return __builtin_amdgcn_raw_buffer_load_b128(r, voff, soff, AUX);
}
template <int AUX> __device__ __forceinline__ u32x2 ld8(rsrc_t r, u32 voff, u32 soff) {
  return __builtin_amdgcn_raw_buffer_load_b64(r, voff, soff, AUX);
}
__device__ __forceinline__ void st8_sc1(rsrc_t r, u32 voff, u32 v, u32 tag) {
  __builtin_amdgcn_raw_buffer_store_b64(u32x2{v, tag}, r, voff, 0, AUX_SC1);
}
__device__ __forceinline__ void st16_sc1(rsrc_t r, u32 voff, u32 v0, u32 v1, u32 tag) {
  __builtin_amdgcn_raw_buffer_store_b128(u32x4{v0, tag, v1, tag}, r, voff, 0, AUX_SC1);
}
__device__ __forceinline__ rsrc_t mk_rsrc(const void* p, u32 bytes) {
  return __builtin_amdgcn_make_buffer_rsrc(const_cast<void*>(p), 0, bytes, 0x00020000);
}
#define WAIT_VM(n) __builtin_amdgcn_s_waitcnt(((n) & 15) | (((n) >> 4) << 14) | (7 << 4) | (15 << 8))
#define SB() __builtin_amdgcn_sched_barrier(0)
#define CFENCE() asm volatile("" ::: "memory")
__device__ __forceinline__ bool all_ok(bool ok) { return __builtin_amdgcn_ballot_w64(!ok) == 0ull; }

template <int N> __device__ __forceinline__ void gather16(rsrc_t ws, u32 voff, u32 stride, u32 tag, u32x4 (&g)[N]) {
#pragma nounroll
  for (int spin = 0; spin < SPIN_MAX; ++spin) {
    CFENCE();
    bool ok = true;
#pragma unroll
    for (int i = 0; i < N; ++i) g[i] = ld16<AUX_SC1>(ws, voff + i * stride, 0);
#pragma unroll
    for (int i = 0; i < N; ++i) ok &= (g[i][1] == tag) & (g[i][3] == tag);
    if (all_ok(ok)) break;
    __builtin_amdgcn_s_sleep(8);
  }
}
template <int N> __device__ __forceinline__ void gather8(rsrc_t ws, u32 voff, u32 stride, u32 tag, u32 (&g)[N], bool act = true) {
#pragma nounroll
  for (int spin = 0; spin < SPIN_MAX; ++spin) {
    CFENCE();
    bool ok = true;
    u32x2 t[N];
#pragma unroll
    for (int i = 0; i < N; ++i) t[i] = ld8<AUX_SC1>(ws, voff + i * stride, 0);
#pragma unroll
    for (int i = 0; i < N; ++i) { ok &= (t[i][1] == tag); g[i] = t[i][0]; }
    if (all_ok(ok | !act)) break;
    __builtin_amdgcn_s_sleep(8);
  }
}

struct Smem {
  float red[8][128];
  float ml[8][2];
  float r2[64 * 20];
  u16 hb[3 * 1024];
  float stat[8][64];
  float red2[8][128];
  u32 dummy[64];
  u32x2 bc;
  u32x4 zero[4];
  float pl[4][8];
  u32 mflag[8];
  u32x4 wbuf[8][8][64];
  __attribute__((aligned(256))) u16 w2b[32][512];
  u16 actl[2][32];
};

#define STAMP(k) do {} while (0)
#define WSTAMP(k) do {} while (0)
#define FINISH() return
#define PIN(x) asm volatile("" : "+v"(x))
#define LDS_BARRIER() do { __builtin_amdgcn_s_waitcnt(0xc07f); __builtin_amdgcn_s_barrier(); CFENCE(); } while (0)
#define WAVE_SYNC() do { __builtin_amdgcn_fence(__ATOMIC_RELEASE, "wavefront"); __builtin_amdgcn_wave_barrier(); __builtin_amdgcn_fence(__ATOMIC_ACQUIRE, "wavefront"); } while (0)

__device__ __forceinline__ void dma16(rsrc_t r, u32 voff, u32 soff, u32 lds_dst) {
  u32 keep;
  asm volatile("s_mov_b32 %0, m0\n\ts_mov_b32 m0, %4\n\ts_nop 4\n\tbuffer_load_dwordx4 %1, %2, %3 offen nt lds\n\ts_mov_b32 m0, %0"
               : "=&s"(keep) : "v"(voff), "s"(r), "s"(soff), "s"(lds_dst) : "memory");
}
__device__ __forceinline__ void ld16_into(u32x4& d, rsrc_t r, u32 voff, u32 soff) {
  asm volatile("s_nop 4\n\tbuffer_load_dwordx4 %0, %1, %2, %3 offen nt" : "+v"(d) : "v"(voff), "s"(r), "s"(soff) : "memory");
}
__device__ __forceinline__ u32 perm_lo(u32 a, u32 b) { return __builtin_amdgcn_perm(b, a, 0x05040100u); }
__device__ __forceinline__ u32 perm_hi(u32 a, u32 b) { return __builtin_amdgcn_perm(b, a, 0x07060302u); }
__device__ __forceinline__ float rdl(float x, int l) { return __int_as_float(__builtin_amdgcn_readlane(__float_as_int(x), l)); }

typedef __attribute__((ext_vector_type(8))) __bf16 bf16x8;
__device__ __forceinline__ f32x4 mfma16(u32x4 a, u32x4 b, f32x4 c) {
  return __builtin_amdgcn_mfma_f32_16x16x32_bf16(__builtin_bit_cast(bf16x8, a), __builtin_bit_cast(bf16x8, b), c, 0, 0, 0);
}
__device__ __forceinline__ float sum8(const float* p) {
  const f32x4 a = *reinterpret_cast<const f32x4*>(p), b = *reinterpret_cast<const f32x4*>(p + 4);
  return ((a[0] + b[0]) + (a[1] + b[1])) + ((a[2] + b[2]) + (a[3] + b[3]));
}
__device__ __forceinline__ float ln_ri(float var) {
  return __builtin_amdgcn_rcpf(rbf(__builtin_amdgcn_sqrtf(rbf(rbf(fmaxf(var, 0.f)) + 1e-5f))));
}
__device__ __forceinline__ void ln_stats2(const float (&x0)[8], const float (&x1)[8], float& mb0, float& ri0, float& mb1, float& ri1) {
  const float k0 = rdl(x0[0], 0), k1 = rdl(x1[0], 0);
  float s0 = 0.f, s1 = 0.f, q0 = 0.f, q1 = 0.f;
#pragma unroll
  for (int k = 0; k < 8; ++k) {
    const float d0 = x0[k] - k0, d1 = x1[k] - k1;
    s0 += d0; s1 += d1; q0 = fmaf(d0, d0, q0); q1 = fmaf(d1, d1, q1);
  }
  float t = swap16_add(swap32_add(s0, s1), swap32_add(q0, q1));
  t += dpp<XOR1>(t); t += dpp<XOR2>(t); t += dpp<XOR7>(t); t += dpp<XOR8>(t);
  auto u = __builtin_amdgcn_permlane16_swap(__float_as_uint(t), __float_as_uint(t), false, false);
  const float ms = __uint_as_float(u[0]) * (1.f / 512.f), mq = __uint_as_float(u[1]) * (1.f / 512.f);
  const float ri = ln_ri(fmaf(-ms, ms, mq));
  mb0 = rbf(k0 + rdl(ms, 0)); mb1 = rbf(k1 + rdl(ms, 32));
  ri0 = rdl(ri, 0); ri1 = rdl(ri, 32);
}
template <int I> struct IC { static constexpr int v = I; };
template <int I, int N, class F> __device__ __forceinline__ void sfor(F&& f) {
  if constexpr (I < N) { if (f(IC<I>{})) sfor<I + 1, N>(f); }
}
typedef __attribute__((ext_vector_type(4))) short s16x4;
typedef __attribute__((address_space(3))) s16x4* lds_s16x4p;
__device__ __forceinline__ u32x2 tr_read(u32 lds_addr) {
  return __builtin_bit_cast(u32x2, __builtin_amdgcn_ds_read_tr16_b64_v4i16((lds_s16x4p)lds_addr));
}
__device__ __forceinline__ u32 next_tag(u32 e) { return ((e | 0x40004000u) + 1u) & ~0x40004000u; }
__device__ __forceinline__ float u2f(u32 v) { return __uint_as_float(v); }
#define LDSP(T, a) ((__attribute__((address_space(3))) T*)(a))
template <class T> __device__ __forceinline__ u32 lds_addr(T* p) { u32 a = (u32)(uintptr_t)p; PIN(a); return a; }

#define TAB_LOAD "s_add_u32 s30, s28, wg_tab@rel32@lo+4\ns_addc_u32 s31, s29, wg_tab@rel32@hi+12\nglobal_load_dwordx2 %[te], %[va], s[30:31]\n"
#define FC(k) ".Lfc" #k
#define FC_REGS
#define FX(r, bit, k) "s_bitcmp1_b32 %[" #r "], " #bit "\ns_cselect_b32 %[sb], " FC(k) ", 0\ns_xor_b32 %[sa], %[sa], %[sb]\n"
__global__ void __launch_bounds__(512, 2) fused_kernel(const u16* kc_, const u16* vc_, const u16* wo_, const u16* wq_, const u16* wmod_, const u16* w1_, const u16* w3_,
                                                       unsigned char* ws_, const u16* x_, const u16* cnd_, const u16* w2_, u16* out_) {
  __shared__ __attribute__((aligned(256))) Smem sm;
  const u64 a_ka = (u64)__builtin_amdgcn_kernarg_segment_ptr();
  const int bid = blockIdx.x, tid = threadIdx.x;
  u64 wa, t64; u32x2 te; int tid0;
  u32 ar[14];
  u32 tch2, sa_, sb_, va_;
  u32x4 kr[NL], vr[NL];
  asm volatile(".set .Lfc0, %0\n.set .Lfc1, %1\n.set .Lfc2, %2\n.set .Lfc3, %3\n.set .Lfc4, %4\n.set .Lfc5, %5\n.set .Lfc6, %6\n.set .Lfc7, %7\n.set .Lfc8, %8\n.set .Lfc9, %9\n.set .Lfc10, %10\n.set .Lfc11, %11"
    :: "n"(first_col(PRM0, 0)), "n"(first_col(PRM0, 1)), "n"(first_col(PRM0, 2)), "n"(first_col(PRM0, 3)), "n"(first_col(PRM0, 4)), "n"(first_col(PRM0, 5)),
       "n"(first_col(PRM0, 6)), "n"(first_col(PRM0, 7)), "n"(first_col(PRM0, 8)), "n"(first_col(PRM0, 9)), "n"(first_col(PRM0, 10)), "n"(first_col(PRM0, 11)));
  asm volatile(
    "s_load_dwordx16 s[4:19], %[ka], 0x0\ns_load_dwordx8 s[20:27], %[ka], 0x40\n"
    "s_memrealtime %[t]\n"
    "v_lshlrev_b32 %[va], 3, %[bid]\n"
    "s_getpc_b64 s[28:29]\n.Lcpf_first:\n"
    TAB_LOAD
    "s_cmp_gt_u32 %[bid], 15\ns_cbranch_scc1 .Lno_cpf\n"
    "s_mov_b32 %[sa], .Lend_a-.Lcpf_first\ns_max_i32 %[sa], %[sa], .Lend_b-.Lcpf_first\ns_max_i32 %[sa], %[sa], .Lend_c-.Lcpf_first\ns_max_i32 %[sa], %[sa], .Lend_d-.Lcpf_first\ns_max_i32 %[sa], %[sa], .Lend_e-.Lcpf_first\n"
    "v_lshlrev_b32 %[va], 6, %[tid]\nv_min_u32 %[va], %[sa], %[va]\nglobal_load_dword %[pf], %[va], s[28:29]\n"
    ".Lno_cpf:\n"
    "v_readfirstlane_b32 %[t0], %[tid]\nv_and_b32 %[va], 63, %[tid]\n"
    "s_mov_b32 %[sa], " FC(11) "\n"
    FX(bid, 0, 0) FX(bid, 1, 1) FX(bid, 2, 2) FX(bid, 3, 3) FX(bid, 4, 4) FX(bid, 5, 5) FX(bid, 6, 6) FX(bid, 7, 7) FX(t0, 6, 8) FX(t0, 7, 9) FX(t0, 8, 10)
    "v_lshl_add_u32 %[va], %[va], 4, %[sa]\n"
    "s_waitcnt lgkmcnt(0)\nglobal_load_dwordx4 %[k0], %[va], s[4:5] nt\nglobal_load_dwordx4 %[v0], %[va], s[6:7] nt\n"
    "s_bitcmp1_b32 %[t0], 6\ns_cselect_b64 s[14:15], s[16:17], s[14:15]\n"
    "s_bitcmp1_b32 %[bid], 4\ns_cselect_b64 s[8:9], s[10:11], s[8:9]\ns_cselect_b64 s[12:13], s[14:15], s[12:13]\ns_bitcmp1_b32 %[bid], 5\ns_cselect_b64 %[wa], s[12:13], s[8:9]"
    : "=&{s4}"(ar[0]), "=&{s5}"(ar[1]), "=&{s6}"(ar[2]), "=&{s7}"(ar[3]), "=&{s18}"(ar[4]), "=&{s19}"(ar[5]), "=&{s20}"(ar[6]), "=&{s21}"(ar[7]), "=&{s22}"(ar[8]), "=&{s23}"(ar[9]),
      "=&{s24}"(ar[10]), "=&{s25}"(ar[11]), "=&{s26}"(ar[12]), "=&{s27}"(ar[13]), [wa] "=&s"(wa), [t0] "=&s"(tid0), [t] "=&s"(t64), [te] "=&v"(te), [sa] "=&s"(sa_), [sb] "=&s"(sb_),
      [va] "=&v"(va_), [pf] "=&v"(tch2), [k0] "=&v"(kr[0]), [v0] "=&v"(vr[0])
    : [ka] "s"(a_ka), [bid] "s"(bid), [tid] "v"(tid) : "s8", "s9", "s10", "s11", "s12", "s13", "s14", "s15", "s16", "s17", "s28", "s29", "s30", "s31", "scc", "memory" FC_REGS);
  asm volatile("" : "+s"(ar[0]), "+s"(ar[1]), "+s"(ar[2]), "+s"(ar[3]), "+s"(ar[4]), "+s"(ar[5]), "+s"(ar[6]), "+s"(ar[7]), "+s"(ar[8]), "+s"(ar[9]), "+s"(ar[10]), "+s"(ar[11]), "+s"(ar[12]), "+s"(ar[13]));
  asm volatile("" : "+s"(wa), "+s"(t64), "+s"(tid0), "+s"(sa_));
  const u32 tka = (u32)wall_clock64();
  const u32 t0 = tka;
  __builtin_assume(tid >= 0 && tid < 512 && tid0 >= 0 && tid0 < 512 && bid >= 0 && bid < 256);
  constexpr Prm P = PRM0;
  const int TEND = P.TEND;
  const int xcd = bid & 7, part = bid >> 4 & 3;
  const int lane = tid & 63, wave = tid0 >> 6;
  SB();
  const bool p3 = part == 3;
  u32 pitch = (u32)(0x005600c001000020ull >> (part * 16) & 0xffff) * 32u;
  const int sh = p3 ? 2 : 3;
  u32 wv0 = (u32)((p3 ? (wave >> 1) * 128 : wave * 64) + (part == 0 ? xcd * 512 : 0) + (lane >> sh)) * pitch + ((lane & (p3 ? 3 : 7)) ^ (lane >> (p3 ? 5 : 3))) * 16u;
  const u32 wimg = (u32)(uintptr_t)(&sm.wbuf[0][0][0]);
  u32 wsz = (part == 0 ? 4096u : 512u) * pitch, wld = wimg + (p3 ? (wave & 1) * 32768 + (wave >> 1) * 8192 : wave * 8192);
  PIN(wv0);
  asm volatile("" : "+s"(wsz), "+s"(wld), "+s"(pitch));
  auto karg = [&](int i) { return ar[2 * i] | (u64)ar[2 * i + 1] << 32; };
  const u64 a_kc = karg(0), a_vc = karg(1);
  unsigned char* ws = (unsigned char*)karg(2);
  const u16 *x = (const u16*)karg(3), *cnd = (const u16*)karg(4), *w2 = (const u16*)karg(5);
  u16* out = (u16*)karg(6);
  const rsrc_t wsr = mk_rsrc(ws, WS_END);
  asm volatile("s_waitcnt vmcnt(2)" : "+v"(te));
  const u32 te0 = __builtin_amdgcn_readfirstlane(te[0]), te1 = __builtin_amdgcn_readfirstlane(te[1]);
  const int bl0 = te0 & 127, nl = te0 >> 7 & 63, blk = te0 >> 13 & 63;
  const bool has_w = te0 >> 19 & 1;
  const rsrc_t rw = mk_rsrc((const void*)wa, wsz);
  const u32 wstep = pitch << (6 - sh), wflip = p3 ? 32u : 0u;
  u32 wso = blk << (sh + 4);
  int wleft = 8;
  auto piece = [&]() {
    dma16(rw, wv0, wso, wld);
    wso += wstep; wld += 1024;
    wv0 ^= (wleft & 1) ? wflip : 0u;
    --wleft;
  };
  SB();
  STAMP(23);
  const int b = bid >> 3 & 1, head = xcd * 4 + (bid >> 6 ^ 3);
  const int g = lane >> 4, c = lane & 15;
  const int i8 = b | bid >> 6 << 1, wi = i8 * 8 + xcd;
  const bool is_up = p3 && has_w, modc = part == 2 && wi < 16;
  const size_t base = ((size_t)(b * NH + head) * SK + (SK - WIN)) * DH;
  const rsrc_t rk = mk_rsrc((const u16*)a_kc + base, WIN * DH * 2), rv = mk_rsrc((const u16*)a_vc + base, WIN * DH * 2);
  int tau = te1 & 0xffff;
  const int tauw = te0 >> 24 << 7, wdef = te0 >> 20 & 15;
  int due = P.NF * 256, now = 0;
  auto gate = [&]() {
    while (now < due) { now = (int)(((u32)wall_clock64() - t0) << 8); if (now < due) __builtin_amdgcn_s_sleep(1); }
    due += tau;
  };
  auto until = [&](int t) { while ((int)(((u32)wall_clock64() - t0) << 8) < t); };
  const u32 pslot = dup(G_P + head * 2048 + (b * 4 + part) * 16);
  const u32 die = __builtin_amdgcn_s_getreg(20 | (2 << 6));
  const bool voter = p3 && i8 == 1, timer = p3 && i8 < 2;
  int vote = 0, lag = 0, shift = 0;
  const u32 pbo = dup(G_PB) + (xcd * 8 + wave) * 128;
  u32 near = 0;
  u32x2 ep; u32 xs0, xs1, tch1; u32x4 xr0, xr1;
  const rsrc_t rx = mk_rsrc(part == 2 ? cnd : x, (part == 1 || (part == 2 && has_w)) ? 2048u : 0u);
  auto small_ops = [&]() {
    ep = ld8<AUX_SC1>(wsr, wave == 7 ? WS_EPOCH : OOB, 0);
    xs0 = __builtin_amdgcn_raw_buffer_load_b16(rx, tid * 2, 0, 0); xs1 = __builtin_amdgcn_raw_buffer_load_b16(rx, tid * 2, 1024, 0);
    xr0 = ld16<0>(rx, part == 1 ? lane * 16 : OOB, 0); xr1 = ld16<0>(rx, part == 1 ? lane * 16 : OOB, 1024);
    tch1 = __builtin_amdgcn_raw_buffer_load_b32(mk_rsrc(w2 + (size_t)blk * 32 * 512, is_up ? 64u : 0u), tid == 384 ? 0 : OOB, 0, AUX_NT);
  };
  if (has_w) {
    if (modc) now = 1 << 30;
#pragma nounroll
    while (wleft > wdef) { gate(); due += tauw - tau; piece(); }
    now = 0;
  }
  due = max(due, P.TK * 256);
  SB();
  u32 sot = sa_ & 0xffc00u;
  {
    const u32 s1 = (bl0 + lane - 1 - (lane >= SK0 ? NL - nl : 0)) ^ (124 + part);
#pragma unroll
    for (int k = 0; k < 7; ++k) sot ^= (u32)-(int)(s1 >> k & 1u) & col(P, k);
    PIN(sot);
  }
  u32 tag = 0;
  u32x4 bf[8];
  auto wo_prep = [&](int s0 = 0, int s1 = 2) {
    const u32 wi_ = wimg + wave * 8192;
    const int q = (lane >> 2) & 3, pp = lane & 3;
#pragma unroll
    for (int s = s0; s < s1; ++s) {
#pragma unroll
      for (int t = 0; t < 4; ++t) {
        const int r0 = s * 32 + g * 8 + q, r1 = r0 + 4, ch = t * 2 + (pp >> 1);
        const u32x2 lo = tr_read(wi_ + r0 * 128 + ((ch ^ (r0 & 7)) * 16) + (pp & 1) * 8), hi = tr_read(wi_ + r1 * 128 + ((ch ^ (r1 & 7)) * 16) + (pp & 1) * 8);
        bf[s * 4 + t] = u32x4{lo[0], lo[1], hi[0], hi[1]};
      }
    }
  };
  auto gemv_go = [&](u32 hh) {
    sm.hb[tid] = (u16)hh; sm.hb[512 + tid] = (u16)(hh >> 16);
    WAVE_SYNC();
    const u32x4* a = (c & 11) == 0 ? reinterpret_cast<const u32x4*>(&sm.hb[(c >> 2) * 512 + wave * 64 + g * 8]) : &sm.zero[0];
    const u32x4 a0 = a[0], a1 = a[(c & 11) == 0 ? 4 : 0];
    const f32x4 z = {0.f, 0.f, 0.f, 0.f};
    f32x4 d[4];
#pragma unroll
    for (int t = 0; t < 4; ++t) d[t] = mfma16(a0, bf[t], z);
#pragma unroll
    for (int t = 0; t < 4; ++t) d[t] = mfma16(a1, bf[4 + t], d[t]);
    if (lane < 32) {
#pragma unroll
      for (int t = 0; t < 4; ++t) sm.r2[(t * 16 + c) * 20 + g * 8 + wave] = d[t][0];
    }
  };
  auto mod_stage = [&]() {
    wo_prep();
    gemv_go(pack_bf(silu_f(lo_f(xs0)), silu_f(lo_f(xs1))));
    __builtin_amdgcn_s_waitcnt(0xc07f);
    if (lane == 0) *LDSP(volatile u32, (u32)(uintptr_t)&sm.mflag[wave]) = 1u;
    if (wave == 7) {
#pragma nounroll
      for (int spin = 0; spin < SPIN_MAX; ++spin) {
        const u32 f = *LDSP(volatile u32, (u32)(uintptr_t)&sm.mflag[lane & 7]);
        if (all_ok(f != 0u)) break;
        __builtin_amdgcn_s_sleep(1);
      }
      CFENCE();
      st8_sc1(wsr, G_MOD + (blk * 64 + lane) * 8, pack_bf(sum8(&sm.r2[lane * 20]), sum8(&sm.r2[lane * 20 + 8])), tag);
    }
    STAMP(9);
  };
  if (lane < 4) sm.zero[lane] = u32x4{0u, 0u, 0u, 0u};
  if (part == 2 && has_w) {
    if (tid < 8) sm.mflag[tid] = 0u;
    LDS_BARRIER();
    if (__builtin_expect(modc, 0)) {
      small_ops();
      __builtin_amdgcn_raw_buffer_store_b128(u32x4{SENT, SENT, SENT, SENT}, wsr, G_U + wi * 10240 + tid * 16, 0, AUX_SC1);
      __builtin_amdgcn_raw_buffer_store_b128(u32x4{SENT, SENT, SENT, SENT}, wsr, tid < 128 ? G_U + wi * 10240 + tid * 16 : OOB, 8192, AUX_SC1);
      asm volatile("s_waitcnt vmcnt(0)" ::: "memory");
      tag = next_tag(__builtin_amdgcn_readfirstlane(ep[0]));
      mod_stage();
      __builtin_amdgcn_s_barrier();
    }
  }
  SB();
  const int fd = wi * 8 + (lane & 3) * 2;
  const int ub = wave >> 2, ud = (wave & 3) * 128 + c * 8 + g * 2;
  u32x4 sg[3]; u32 sx2;
  auto side_loads = [&]() {
    const bool fin = part == 1 && wave == 0;
    sg[0] = ld16<AUX_SC1>(wsr, is_up ? G_MOD + (1024 + ud) * 8 : (fin ? G_MOD + (1024 + fd) * 8 : OOB), 0);
    sg[1] = ld16<AUX_SC1>(wsr, is_up ? G_MOD + (1536 + ud) * 8 : (fin ? G_MOD + (2560 + fd) * 8 : OOB), 0);
    sg[2] = ld16<AUX_SC1>(wsr, is_up ? G_MOD + (2048 + ud) * 8 : OOB, 0);
    sx2 = __builtin_amdgcn_raw_buffer_load_b32(mk_rsrc(x, (is_up || fin) ? 2048u : 0u), is_up ? ub * 1024 + ud * 2 : (g & 1) * 1024 + fd * 2, 0, 0);
  };
  auto side_pin = [&]() { PIN(sg[0]); PIN(sg[1]); PIN(sg[2]); PIN(sx2); };
  const u32 mlo = G_ML + (head * 2 + b) * 64 + (lane & 3) * 16;
  float fmrg = 0.f;
  auto merge_factor = [&](const u32x4& ml) {
    const float mi = u2f(ml[0]);
    float M = fmaxf(mi, dpp<XOR1>(mi)); M = fmaxf(M, dpp<XOR2>(M));
    const float e = __expf(mi - M);
    float L = e * u2f(ml[2]);
    L += dpp<XOR1>(L); L += dpp<XOR2>(L);
    fmrg = rdl(e * __builtin_amdgcn_rcpf(L), part);
  };
  f32x2 ga = {0.f, 0.f}, xin = {0.f, 0.f}, cc1 = {0.f, 0.f};
  float Af = 0.f, Bf = 0.f;
  const int hpos = ub * 512 + ud;
  auto up_prep = [&]() {
    if (!all_ok((sg[0][1] == tag) & (sg[0][3] == tag) & (sg[1][1] == tag) & (sg[1][3] == tag) & (sg[2][1] == tag) & (sg[2][3] == tag))) gather16<3>(wsr, G_MOD + (1024 + ud) * 8, 512 * 8, tag, sg);
    ga = f32x2{ub ? hi_f(sg[0][0]) : lo_f(sg[0][0]), ub ? hi_f(sg[0][2]) : lo_f(sg[0][2])};
    cc1 = f32x2{rbf(1.f + (ub ? hi_f(sg[2][0]) : lo_f(sg[2][0]))), rbf(1.f + (ub ? hi_f(sg[2][2]) : lo_f(sg[2][2])))};
    xin = f32x2{lo_f(sx2), hi_f(sx2)};
    u32* hp = reinterpret_cast<u32*>(&sm.hb[hpos]);
    hp[512] = pack_bf(cc1[0], cc1[1]);
    hp[1024] = ub ? perm_hi(sg[1][0], sg[1][2]) : perm_lo(sg[1][0], sg[1][2]);
  };
  auto up_frags = [&]() {
    const int pp = lane & 3;
    const u32 a = wimg + (pp & 1) * 32768 + ((pp >> 1) * 32 + g * 8 + ((lane >> 2) & 3)) * 64 + (((wave >> 1) ^ ((g & 1) | (pp & 2))) * 16) + (wave & 1) * 8;
#pragma unroll
    for (int rb = 0; rb < 8; ++rb) {
      const u32x2 lo = tr_read(a + rb * 4096), hi = tr_read(a + rb * 4096 + 256);
      bf[rb] = u32x4{lo[0], lo[1], hi[0], hi[1]};
    }
  };
  const u32x4* ap = reinterpret_cast<const u32x4*>(&sm.hb[((lane >> 3) & 1) * 512 + (lane & 1) * 32 + g * 8]);
  const bool par = lane & 8;
  auto limit = [&](auto sc) {
    constexpr int s = sc.v;
    WAIT_VM(window(s, 1 + (PRM0.W[s >> 3] >> (4 * (s & 7)) & 7)));
    SB();
  };
  auto sample = [&]() { if (timer) lag += (int)(((u32)wall_clock64() - t0) << 8) - due + ((int)__builtin_amdgcn_s_getreg(7 | (3 << 11)) + 1) * tau; };
  auto report = [&](u32 dst) {
    if (timer) {
      if (lane == 0) sm.mflag[wave] = vote * 65536 + min(lag >> 8, 4000);
      lag = 0;
      LDS_BARRIER();
      if (wave == 7) {
        int s = sm.mflag[lane & 7];
        s += __builtin_amdgcn_mov_dpp(s, XOR1, 0xf, 0xf, true); s += __builtin_amdgcn_mov_dpp(s, XOR2, 0xf, 0xf, true); s += __builtin_amdgcn_mov_dpp(s, XOR7, 0xf, 0xf, true);
        if (lane < 8) st8_sc1(wsr, dst + lane * 128 + wi * 8, s, tag);
      }
    }
    SB();
  };
  float l0 = 0.f, l1 = 0.f;
  u32x2 gmd[2], tbg;
  int t1 = 0;
#pragma unroll
  for (int i = SK0; i < SK1; ++i) kr[i] = u32x4{0u, 0u, 0u, 0u};
  const int first = SK0 + NL - nl;
  auto left_out = [&](int i) { int f = first; asm volatile("" : "+s"(f)); return i < f; };
  sfor<1, NL>([&](auto ic) {
    constexpr int i = ic.v;
    if constexpr (i >= SK0 && i < SK1) { if (left_out(i)) return true; }
    const u32 so = __builtin_amdgcn_readlane(sot, i);
    gate();
    if constexpr ((i >= TBS && i <= TBP) || (i >= RPS && i <= RPP)) sample();
    SB();
    if constexpr (i >= SK0 && i < SK1) ld16_into(kr[i], rk, lane * 16, so); else kr[i] = ld16<AUX_NT>(rk, lane * 16, so);
    if constexpr (i == SMO) small_ops();
    if constexpr (i == SMO + 1) { if (voter) __builtin_amdgcn_raw_buffer_store_b32(0u, wsr, pbo + (lane >> 5) * 8192 + (lane & 31) * 4, 0, AUX_SC1); }
    if constexpr (i == SK1) tbg = ld8<AUX_SC1>(wsr, wave == 7 ? G_VT + xcd * 128 + c * 8 : OOB, 0);
    if constexpr (i == TBL) tbg = ld8<AUX_SC1>(wsr, wave == 7 ? G_TB + xcd * 128 + c * 8 : OOB, 0);
    if constexpr (i == QP) { gmd[0] = ld8<AUX_SC1>(wsr, part == 1 ? G_MOD + tid * 8 : OOB, 0); gmd[1] = ld8<AUX_SC1>(wsr, part == 1 ? G_MOD + tid * 8 : OOB, 4096); }
    SB();
    SB();
    if (i >= 4) limit(ic);
    if (i == 1) STAMP(25);
    if (i == 8) STAMP(12);
    if (i == 20) STAMP(21);
    if (i == 32) STAMP(22);
    if (i == TBP) {
      if (timer) {
        if (lane == 0) sm.mflag[wave] = lag;
        lag = 0;
        LDS_BARRIER();
        if (wave == 7) {
          int s = sm.mflag[lane & 7];
          s += __builtin_amdgcn_mov_dpp(s, XOR1, 0xf, 0xf, true); s += __builtin_amdgcn_mov_dpp(s, XOR2, 0xf, 0xf, true); s += __builtin_amdgcn_mov_dpp(s, XOR7, 0xf, 0xf, true);
          if (lane < 8) st8_sc1(wsr, G_TB + lane * 128 + wi * 8, t0 << 10 | (u32)min(s / (2048 * (TBP - TBS + 1)), 1023), tag);
        }
      }
      SB();
    }
    if (i == PRB) {
      if (voter) {
        u64 ta, tb, tc; u32 dm;
        const u32 pa = pbo + (wave & 1) * 8192;
        asm volatile("s_memtime %0\n\ts_waitcnt lgkmcnt(0)\n\ts_load_dword %3, %4, %5\n\ts_waitcnt lgkmcnt(0)\n\ts_memtime %1\n\ts_waitcnt lgkmcnt(0)\n\t"
                     "s_load_dword %3, %4, %6\n\ts_waitcnt lgkmcnt(0)\n\ts_memtime %2\n\ts_waitcnt lgkmcnt(0)"
                     : "=&s"(ta), "=&s"(tb), "=&s"(tc), "=&s"(dm) : "s"(ws), "s"(pa), "s"(pa ^ 8192u));
        const int d = min(max((int)(2 * (u32)tb - (u32)ta - (u32)tc), -500), 500);
        vote = ((wave ^ die) & 1) ? -d : d;
        STAMP(26);
      }
      SB();
    }
    if (i == TBU) {
      if (wave == 7) {
        const bool ok = tbg[1] == tag;
        float lf = ok ? (float)min(max((int)((t0 << 10 | 1023u) - tbg[0]) >> 10, -300), 300) : 0.f, nf = ok ? 1.f : 0.f, lg = ok ? (float)(tbg[0] & 1023u) : 0.f;
        lf += dpp<XOR1>(lf); nf += dpp<XOR1>(nf); lg += dpp<XOR1>(lg); lf += dpp<XOR2>(lf); nf += dpp<XOR2>(nf); lg += dpp<XOR2>(lg);
        lf += dpp<XOR7>(lf); nf += dpp<XOR7>(nf); lg += dpp<XOR7>(lg); lf += dpp<XOR8>(lf); nf += dpp<XOR8>(nf); lg += dpp<XOR8>(lg);
        const float rn = __builtin_amdgcn_rcpf(fmaxf(nf, 1.f));
        shift = __builtin_amdgcn_readfirstlane(min(max((int)((lg * rn - (float)P.GS) * (float)P.GB * 0.125f), 0), 150));
        if (lane == 0) sm.bc = u32x2{tag, t0 - (u32)(int)(lf * rn) + shift};
      }
      LDS_BARRIER();
      const u32x2 bc = sm.bc;
      tag = __builtin_amdgcn_readfirstlane(bc[0]);
      t1 = (__builtin_amdgcn_readfirstlane((int)(bc[1] - t0)) + TEND + P.X1) * 256;
      tau -= __builtin_amdgcn_readfirstlane((int)((float)(int)(t0 - bc[1]) * 256.f * __builtin_amdgcn_rcpf((float)(nl - TBU - 1))));
      SB();
    }
    if (i == RPP) report(G_VT);
    if (i == LNS) {
      tag = next_tag(__builtin_amdgcn_readfirstlane(ep[0]));
      if (part == 1) {
        float x0[8], x1[8], mb0, ri0, mb1, ri1;
#pragma unroll
        for (int k = 0; k < 4; ++k) { x0[2 * k] = lo_f(xr0[k]); x0[2 * k + 1] = hi_f(xr0[k]); x1[2 * k] = lo_f(xr1[k]); x1[2 * k + 1] = hi_f(xr1[k]); }
        ln_stats2(x0, x1, mb0, ri0, mb1, ri1);
        l0 = rbf(rbf(lo_f(xs0) - mb0) * ri0); l1 = rbf(rbf(lo_f(xs1) - mb1) * ri1);
      }
      PIN(l0); PIN(l1);
      SB();
    }
    if (i == WUSE) {
      PIN(tch1); PIN(tch2);
      if ((part == 1 || (part == 2 && has_w && !modc))) {
        WAIT_VM(WK); CFENCE();
        if (part == 2) mod_stage(); else wo_prep();
      }
#pragma unroll
      for (int k = 0; k < 8; ++k) PIN(bf[k]);
      SB();
    }
    if (i == QP + QD) {
      if (part == 1) {
        u32 gm[2] = {gmd[0][0], gmd[1][0]};
        if (!all_ok((gmd[0][1] == tag) & (gmd[1][1] == tag))) { STAMP(11); gather8<2>(wsr, G_MOD + tid * 8, 512 * 8, tag, gm); }
        STAMP(5);
        gemv_go(pack_bf(rbf(l0 * rbf(1.f + lo_f(gm[1]))) + lo_f(gm[0]), rbf(l1 * rbf(1.f + hi_f(gm[1]))) + hi_f(gm[0])));
      }
      SB();
    }
    if (i == QP + QD + 1) {
      if (part == 1) {
        LDS_BARRIER();
        if (wave == 0) {
          const int bb = lane >> 5, jq = lane & 31;
          st8_sc1(wsr, G_Q + (bb * 2048 + blk * 32 + jq) * 8, pack_bf(sum8(&sm.r2[2 * jq * 20 + bb * 8]), sum8(&sm.r2[(2 * jq + 1) * 20 + bb * 8])), tag);
        }
        STAMP(6);
      }
      SB();
    }
    return true;
  });
  PIN(sot);
  if (wave == 7) {
    const bool ok = tbg[1] == tag;
    float vt = ok ? (float)((int)tbg[0] >> 16) : 0.f, lg = ok ? (float)(tbg[0] & 0xffffu) : 0.f, nf = ok ? 8.f * (RPP - RPS + 1) : 0.f;
    vt += dpp<XOR1>(vt); lg += dpp<XOR1>(lg); nf += dpp<XOR1>(nf); vt += dpp<XOR2>(vt); lg += dpp<XOR2>(lg); nf += dpp<XOR2>(nf);
    vt += dpp<XOR7>(vt); lg += dpp<XOR7>(lg); nf += dpp<XOR7>(nf); vt += dpp<XOR8>(vt); lg += dpp<XOR8>(lg); nf += dpp<XOR8>(nf);
    lg *= __builtin_amdgcn_rcpf(fmaxf(nf, 1.f));
    if (lane == 0) sm.bc = u32x2{__float_as_uint(vt), (u32)min(max((int)((lg - (float)P.B0) * (float)(P.BG * 32)) - shift * P.D0 * 32, 0), 200 * 256)};
  }
  LDS_BARRIER();
  {
    const u32x2 bc = sm.bc;
    const int st = __builtin_amdgcn_readfirstlane(bc[1]), sp = (st * P.BF) >> 3;
    near = ((__builtin_amdgcn_readfirstlane(bc[0]) >> 31 ^ die ^ P.NR ^ 1) & 1) * 8192u;
    t1 += st;
    due += P.TG * 256 + sp;
    tau = (te1 >> 16) + __builtin_amdgcn_readfirstlane((int)((float)(st - sp) * __builtin_amdgcn_rcpf((float)(nl - 3))));
  }
  const u32 qoff = G_Q + ((b * 32 + head) * 64 + c * 4) * 8;
  u32x4 qg[2], mlg;
  u32 qq[4];
  constexpr int NB = (NL + 15) / 16;
  float xlo, sc[NB], pr[NB], m = 0.f;
  f32x4 acc = {0.f, 0.f, 0.f, 0.f};
  u32 pb[NB], pm[4], pl8[8], cpf;
  const u32 adst0 = lds_addr(lane < 32 ? &sm.red[wave][c * 8 + g * 4] : &sm.r2[(lane - 32) * 4]);
  u32 po, rdst, rsrc, xo;
  auto wo_addr = [&]() {
    po = dup(G_P + xcd * 8192) + tid * 16 + near;
    rdst = lane < 32 ? (u32)(uintptr_t)&sm.red2[wave][0] + lane * 16 : (u32)(uintptr_t)&sm.r2[0] + (lane - 32) * 16;
    rsrc = (u32)(uintptr_t)&sm.red2[0][((wave & 1) * 16 + (lane & 15)) * 4 + (lane >> 4)];
    xo = dup(G_XP + xcd * 2048 + (wave & 1) * 1024 + (blk * 64 + (lane & ~1)) * 2) + (lane & 1) * 8192;
    PIN(po); PIN(rdst); PIN(rsrc); PIN(xo);
  };
  u32 pso = (wave == 0 && (g & 1) == 0) ? pslot + (c >> 3) * 1024 + (c & 3) * 256 + (c >> 2 & 1) * 128 + (g >> 1) * 8192 : OOB;
  PIN(pso);
  const u32 nadd = wdef ? P.LW : (is_up ? P.LU : 0);
  u32 nto = tid < 2 ? dup(G_NT) + bid * 8 + tid * 8192 : OOB;
  PIN(nto);
  auto notices = [&]() -> int {
    u32 off = dup(G_NT) + near + lane * 16;
    PIN(off);
    until(due);
    int mx;
#pragma nounroll
    for (int spin = 0; spin < SPIN_MAX; ++spin) {
      CFENCE();
      const u32x4 ga_ = ld16<AUX_SC1>(wsr, off, 0), gb_ = ld16<AUX_SC1>(wsr, off, 1024);
      mx = max(max((int)(ga_[0] - t0), (int)(ga_[2] - t0)), max((int)(gb_[0] - t0), (int)(gb_[2] - t0)));
      if (all_ok((((ga_[1] ^ tag) | (ga_[3] ^ tag)) | ((gb_[1] ^ tag) | (gb_[3] ^ tag))) == 0u)) break;
      for (int k = 0; k < P.AG; ++k) __builtin_amdgcn_s_sleep(1);
    }
    if (all_ok(mx <= (t1 >> 8) - P.AC)) return t1;
    mx = max(mx, __builtin_amdgcn_mov_dpp(mx, XOR1, 0xf, 0xf, true)); mx = max(mx, __builtin_amdgcn_mov_dpp(mx, XOR2, 0xf, 0xf, true));
    mx = max(mx, __builtin_amdgcn_mov_dpp(mx, XOR7, 0xf, 0xf, true)); mx = max(mx, __builtin_amdgcn_mov_dpp(mx, XOR8, 0xf, 0xf, true));
    mx = max(max(__builtin_amdgcn_readlane(mx, 0), __builtin_amdgcn_readlane(mx, 16)), max(__builtin_amdgcn_readlane(mx, 32), __builtin_amdgcn_readlane(mx, 48)));
    return max(min(mx, 1 << 20) * 256, t1 + P.L0 * 256) + P.L1 * 256;
  };
  auto pv_row = [&](auto jc) {
    constexpr int i = jc.v;
    const u32 pv = __float_as_uint(bcast16(u2f(pb[i >> 4]), i & 15));
    acc = mfma16(u32x4{pv & pm[0], pv & pm[1], pv & pm[2], pv & pm[3]}, vr[i], acc);
    PIN(acc);
    return true;
  };
  sfor<0, NL - 1>([&](auto ic) {
    constexpr int v = ic.v;
    if constexpr (v >= SK0 && v < SK1) { if (left_out(v)) return true; }
    if constexpr (v > 0) {
      const u32 so = __builtin_amdgcn_readlane(sot, v);
      if constexpr (v >= SK1) { if (v == NL - P.NE) tau = (tau * P.FE) >> 4; }
      gate();
      SB();
      if constexpr (v >= SK0 && v < SK1) ld16_into(vr[v], rv, lane * 16, so); else vr[v] = ld16<AUX_NT>(rv, lane * 16, so);
      SB();
      SB();
    }
    if constexpr (v == SK0 - 1) {
#pragma unroll
      for (int i = SK0; i < SK1; ++i) { vr[i] = u32x4{0u, 0u, 0u, 0u}; PIN(vr[i]); }
    }
    if constexpr (v == VQ - 4) { qg[0] = ld16<AUX_SC1>(wsr, qoff, 0); qg[1] = ld16<AUX_SC1>(wsr, qoff + 16, 0); }
    if constexpr (v == VSIDE) side_loads();
    if constexpr (v == CPF) {
      u64 pc; u32 dlt, len;
      asm volatile("s_getpc_b64 %0\n.Lcpf_here:\n\ts_mov_b32 %1, .Ltail_start-.Lcpf_here\n\ts_mov_b32 %2, .Lend_a-.Ltail_start\n\ts_max_i32 %2, %2, .Lend_b-.Ltail_start\n\ts_max_i32 %2, %2, .Lend_c-.Ltail_start\n\ts_max_i32 %2, %2, .Lend_d-.Ltail_start\n\ts_max_i32 %2, %2, .Lend_e-.Ltail_start" : "=s"(pc), "=s"(dlt), "=s"(len) :: "scc");
      pc += dlt;
      const rsrc_t rc = mk_rsrc((const void*)pc, (part == 3 && i8 == 1) ? len : 0u);
      cpf = __builtin_amdgcn_raw_buffer_load_b32(rc, tid * 64, 0, 0);
    }
    if constexpr (v == SK1 + 2) PIN(cpf);
    if constexpr (v == SK1) mlg = ld16<AUX_SC1>(wsr, wave == 0 ? mlo : OOB, 0);
    SB();
    if constexpr (NL + v < 72) limit(IC<NL + v>{});
    if constexpr (v >= SK0 + WV) { WAIT_VM(v < SK1 ? WV : WV + 1); SB(); }
    if constexpr (v == VQ) {
      if (!all_ok((qg[0][1] == tag) & (qg[0][3] == tag) & (qg[1][1] == tag) & (qg[1][3] == tag))) { STAMP(20); gather16<2>(wsr, qoff, 16, tag, qg); }
      STAMP(1); WSTAMP(1);
      qq[0] = qg[0][0]; qq[1] = qg[0][2]; qq[2] = qg[1][0]; qq[3] = qg[1][2];
    }
    if constexpr (v >= VQ && v < VS) {
      constexpr int sl = v - VQ, bt = sl >> 1;
      float xs[8];
#pragma unroll
      for (int i = 0; i < 8; ++i) {
        float d = 0.f;
        if (sl * 8 + i < NL) {
#pragma unroll
          for (int k = 0; k < 4; ++k) d = dot2(kr[sl * 8 + i][k], qq[k], d);
        }
        xs[i] = d;
      }
#pragma unroll
      for (int h = 0; h < 4; ++h) xs[h] = sel_add<XOR7>(xs[h], xs[h + 4], c & 4);
#pragma unroll
      for (int h = 0; h < 2; ++h) xs[h] = sel_add<XOR2>(xs[h], xs[h + 2], c & 2);
      xs[0] = sel_add<XOR1>(xs[0], xs[1], c & 1);
      if constexpr ((sl & 1) || sl == NSL - 1) {
        const float t = (sl & 1) ? sel_add<XOR8>(xlo, xs[0], c & 8) : xs[0] + dpp<XOR8>(xs[0]);
        const float s = rbf(rbf(t) * 0.08838834764831845f);
        sc[bt] = (bt * 16 + c < SK0 || (bt * 16 + c >= first && bt * 16 + c < NL)) ? s : -__builtin_inff();
        PIN(sc[bt]);
      } else {
        xlo = xs[0];
        PIN(xlo);
      }
    }
    if constexpr (v == VS) {
      float mloc = sc[0];
#pragma unroll
      for (int bt = 1; bt < NB; ++bt) mloc = fmaxf(mloc, sc[bt]);
      m = fmaxf(wave_max(mloc), -3.0e38f);
      float ls = 0.f;
#pragma unroll
      for (int bt = 0; bt < NB; ++bt) { pr[bt] = __expf(sc[bt] - m); ls += pr[bt]; }
      const float l = wave_sum(ls);
      if (lane == 0) { sm.ml[wave][0] = m; sm.ml[wave][1] = l; }
      if (c == ((NL - 1) & 15)) sm.pl[g][wave] = pr[NB - 1];
      STAMP(2); WSTAMP(2);
    }
    if constexpr (v >= SK1) pv_row(IC<v - SK1 + SK0 - WV>{});
    if constexpr (v < SK0 || v >= SK0 + WV) sfor<cons_end(v - 1), cons_end(v)>(pv_row);
    if constexpr (v == SK1 + 1) {
      float p[8];
#pragma unroll
      for (int w = 0; w < 8; ++w) p[w] = sm.pl[g][w] * __expf(sm.ml[w][0] - m);
#pragma unroll
      for (int w = 0; w < 8; ++w) pl8[w] = pack_bf(p[w], p[w]);
#pragma unroll
      for (int w = 0; w < 8; ++w) PIN(pl8[w]);
    }
    if constexpr (v == MLS - 1) { side_pin(); if (is_up) up_prep(); PIN(ga); PIN(xin); PIN(cc1); }
    if constexpr (v == UPREP - 1) wo_addr();
    if constexpr (v == UPREP) {
#pragma unroll
      for (int k = 0; k < 8; ++k) asm volatile("" : "=v"(bf[k]));
      if (part == 0) wo_prep(); else if (is_up) up_frags();
#pragma unroll
      for (int k = 0; k < 8; ++k) PIN(bf[k]);
    }
    if constexpr (v == UPREP + 1) {
      if (is_up) {
        const u32x4* a1 = ap + 128 + ((lane >> 1) & 1) * 128;
        f32x4 d0 = {0.f, 0.f, 0.f, 0.f}, d1 = {0.f, 0.f, 0.f, 0.f};
#pragma unroll
        for (int rb = 0; rb < 4; ++rb) { d0 = mfma16(a1[rb * 8], bf[rb], d0); d1 = mfma16(a1[(rb + 4) * 8], bf[rb + 4], d1); }
        Af = par ? d0[1] + d1[1] : d0[0] + d1[0]; Bf = par ? d0[3] + d1[3] : d0[2] + d1[2];
        Af += dpp<XOR8>(Af); Bf += dpp<XOR8>(Bf);
      }
      PIN(Af); PIN(Bf);
    }
    if (v == 10 || v == 16) STAMP(15 + v / 6);
    if (v == 20) STAMP(18);
    if (v == 32) STAMP(19);
    if constexpr (v == MLS) {
      LDS_BARRIER();
      float mm[8], ll[8];
#pragma unroll
      for (int w = 0; w < 8; ++w) { const f32x2 t = *reinterpret_cast<const f32x2*>(&sm.ml[w][0]); mm[w] = t[0]; ll[w] = t[1]; }
      float Mwg = mm[0], Lwg = 0.f;
#pragma unroll
      for (int w = 1; w < 8; ++w) Mwg = fmaxf(Mwg, mm[w]);
      if (wave == 0) {
#pragma unroll
        for (int w = 0; w < 8; ++w) Lwg = fmaf(__expf(mm[w] - Mwg), ll[w], Lwg);
        if (lane == 0) st16_sc1(wsr, G_ML + ((head * 2 + b) * 4 + part) * 16, __float_as_uint(Mwg), __float_as_uint(Lwg), tag);
      }
      const float fw = __expf(m - Mwg);
      m = Mwg;
#pragma unroll
      for (int bt = 0; bt < NB; ++bt) { pb[bt] = pack_bf(pr[bt] * fw, pr[bt] * fw); PIN(pb[bt]); }
#pragma unroll
      for (int k = 0; k < 4; ++k) { pm[k] = (c >> 1 & 3) == k ? ((c & 1) ? 0xffff0000u : 0x0000ffffu) : 0u; PIN(pm[k]); }
    }
    SB();
    return true;
  });
  asm volatile(".Ltail_start:");
  if (wave != 0) {
    pv_row(IC<SK0 - 1>{});
    WAIT_VM(WV); SB();
    sfor<NL - 1 - WV, NL - 1>(pv_row);
    *LDSP(f32x4, adst0) = acc;
    WSTAMP(14);
    LDS_BARRIER();
  } else {
    u32x4 xr[8], am[8];
#pragma unroll
    for (int w = 0; w < 8; ++w) am[w] = u32x4{pl8[w] & pm[0], pl8[w] & pm[1], pl8[w] & pm[2], pl8[w] & pm[3]};
#pragma unroll
    for (int w = 0; w < 8; ++w) PIN(am[w]);
    {
      const u32 so = __builtin_amdgcn_readlane(sot, NL - 1);
      gate();
      SB();
#pragma unroll
      for (int w = 0; w < 8; ++w) xr[w] = ld16<AUX_NT>(rv, lane * 16, so ^ ((w & 1) ? col(P, 7) : 0u) ^ ((w & 2) ? col(P, 8) : 0u) ^ ((w & 4) ? col(P, 9) : 0u));
      SB();
    }
    u32 rsum = lds_addr(&sm.red[0][c * 8 + (g & 1) * 4]);
    pv_row(IC<SK0 - 1>{});
    WAIT_VM(WV + 8); SB();
    sfor<NL - 1 - WV, NL - 3>(pv_row);
    if (__builtin_expect(!all_ok((mlg[1] == tag) & (mlg[3] == tag)), 0)) {
#pragma nounroll
      for (int spin = 0; spin < SPIN_MAX; ++spin) {
        CFENCE();
        mlg = ld16<AUX_SC1>(wsr, mlo, 0);
        if (all_ok((mlg[1] == tag) & (mlg[3] == tag))) break;
      }
    }
    merge_factor(mlg);
    sfor<NL - 3, NL - 1>(pv_row);
    STAMP(14); WSTAMP(14);
    LDS_BARRIER();
    STAMP(15);
    f32x4 t[8];
#pragma unroll
    for (int w = 1; w < 8; ++w) t[w] = LDSP(f32x4, rsum)[w * 32];
    acc = ((acc + t[1]) + (t[2] + t[3])) + ((t[4] + t[5]) + (t[6] + t[7]));
    PIN(acc);
    STAMP(3);
    SB();
#pragma unroll
    for (int w = 0; w < 8; ++w) acc = mfma16(am[w], xr[w], acc);
  }
  WAIT_VM(0);
  acc *= fmrg;
  {
    const u32 w0 = min(pack_bf(acc[0], acc[1]), SENT - 1u), w1 = min(pack_bf(acc[2], acc[3]), SENT - 1u);
    const auto e0 = __builtin_amdgcn_permlane16_swap(w0, w0, false, false), e1 = __builtin_amdgcn_permlane16_swap(w1, w1, false, false);
    __builtin_amdgcn_raw_buffer_store_b128(u32x4{e0[0], e1[0], e0[1], e1[1]}, wsr, pso, 0, AUX_SC1);
  }
  st8_sc1(wsr, nto, (u32)wall_clock64() + nadd, tag);
  SB();
  STAMP(4);

  if (part == 2) FINISH();
  if (part == 0) {
    __builtin_amdgcn_s_barrier();
#pragma nounroll
    while (wleft > 0) piece();
    bool late = false;
    u32x4 og[2];
    if (wdef) {
      late = true;
      while ((int)(((u32)wall_clock64() - t0) << 8) < t1 - 20 * 256) if (__builtin_amdgcn_s_getreg(7 | (3 << 11)) == 0) { late = false; break; }
      if (__builtin_expect(late, 0)) { until(t1); CFENCE(); og[0] = ld16<AUX_SC1>(wsr, po, 0); SB(); WAIT_VM(1); } else { WAIT_VM(0); }
      CFENCE();
      if (P.WD > 4) wo_prep(0, 1);
      wo_prep(1, 2);
#pragma unroll
      for (int k = 0; k < 8; ++k) PIN(bf[k]);
      SB();
    }
    if (__builtin_expect(!late, 1)) { until(t1); CFENCE(); og[0] = ld16<AUX_SC1>(wsr, po, 0); }
    STAMP(11);
    SB();
    f32x4 d[4];
    auto wo_mm = [&]() {
      const f32x4 z = {0.f, 0.f, 0.f, 0.f};
#pragma unroll
      for (int t = 0; t < 4; ++t) { d[t] = mfma16(og[0], bf[t], z); og[1][t] = __float_as_uint(dpp<XOR8>(u2f(og[0][t]))); }
      SB();
#pragma unroll
      for (int t = 0; t < 4; ++t) d[t] = mfma16(og[1], bf[4 + t], d[t]);
    };
    auto p_bad = [&]() { return !all_ok(max(max(og[0][0], og[0][1]), max(og[0][2], og[0][3])) != SENT); };
    wo_mm();
    if (__builtin_expect(p_bad(), 0)) {
#pragma nounroll
      for (int spin = 0; spin < SPIN_MAX; ++spin) {
        __builtin_amdgcn_s_sleep(1);
        CFENCE();
        og[0] = ld16<AUX_SC1>(wsr, po, 0);
        if (!p_bad()) break;
      }
      wo_mm();
      SB();
      asm volatile(".Lend_d:");
    }
    STAMP(5); WSTAMP(5);
    {
      SB();
      f32x4 y; f32x2 h[4];
#pragma unroll
      for (int t = 0; t < 4; ++t) { h[t] = d[t].xy + d[t].zw; PIN(h[t]); }
#pragma unroll
      for (int t = 0; t < 4; ++t) { y[t] = h[t][0] + h[t][1]; PIN(y[t]); }
      *LDSP(f32x4, rdst) = y;
    }
    STAMP(16);
    LDS_BARRIER();
    STAMP(17);
    if (wave < 2) {
      f32x2 v[4];
#pragma unroll
      for (int w = 0; w < 4; ++w) v[w] = f32x2{LDSP(float, rsrc)[w * 256], LDSP(float, rsrc)[w * 256 + 128]};
      const f32x2 h = (v[0] + v[1]) + (v[2] + v[3]);
      const float y = h[0] + h[1];
      __builtin_amdgcn_raw_buffer_store_b32(min(pack_bf(dpp<0xA0>(y), dpp<0xF5>(y)), SENT - 1u), wsr, xo, 0, AUX_SC1);
      asm volatile(".Lend_a:");
    }
    STAMP(6);
    FINISH();
  }
  if (part == 3) {
    if (!has_w) FINISH();
    u32x4 b2[4];
    {
      __builtin_amdgcn_s_barrier();
      const rsrc_t r2 = mk_rsrc(w2 + (size_t)(blk * 32 + wave * 4) * 512, 4096u);
      const u32 l2 = (u32)(uintptr_t)(&sm.w2b[wave * 4][0]);
#pragma unroll
      for (int n = 0; n < 4; ++n) dma16(r2, (lane ^ (n * 2) ^ ((wave & 2) << 2)) * 16, n * 1024, l2 + n * 1024);
    }
    const u32 tdst = lds_addr(&sm.hb[hpos]);
    const u32 sdst = lds_addr(&sm.stat[wave][lane]);
    const u32 ssrc = lds_addr(&sm.stat[(g >> 1) * 4 + ((lane >> 3) & 3)][(lane & 7) * 4]);
    const u32 adst = lds_addr((c < 4 && (g & 1) == 0) ? &sm.actl[g >> 1][wave * 4 + c] : reinterpret_cast<u16*>(&sm.dummy[lane]));
    const u32 asrc = lds_addr(&sm.actl[lane & 1][g * 8]);
    const u32 apa = lds_addr(&sm.hb[(c >> 3) * 512 + (c & 3) * 64 + ((c >> 2) & 1) * 32 + g * 8]);
    constexpr u32 xs2 = 16384;
    u32 xoff = dup(G_XP) + near + g * 2048 + ub * 1024 + (wave & 3) * 256 + c * 16;
    u32x4 one;
#pragma unroll
    for (int k = 0; k < 4; ++k) one[k] = ((c & 2) == 0 && (c >> 2) == k) ? ((c & 1) ? 0x3f800000u : 0x00003f80u) : 0u;
    PIN(one);
    u32 ooff = dup(G_OP + (blk * 512 + tid) * 8);
    float Afp = Af, Bfp = Bf;
    PIN(xoff); PIN(ooff); PIN(Afp); PIN(Bfp);
    f32x2 gc = ga * cc1, xc = xin * cc1;
    PIN(ga); PIN(xin); PIN(gc); PIN(xc);
    u32 w2a = (u32)(uintptr_t)(&sm.w2b[0][0]) + (g * 8 + ((lane >> 2) & 3)) * 1024 + (wave ^ (g & 1)) * 128 + (lane & 15) * 8;
    PIN(w2a);
    if (wave == 1) {
      due = t1 + P.N1 * 256;
      const int tn = notices();
      due = tn + P.X2 * 256;
      until(due);
    }
    asm volatile("s_waitcnt vmcnt(0)" ::: "memory");
    STAMP(9);
    __builtin_amdgcn_s_barrier();
    STAMP(11);
    CFENCE();
    u32x4 xg[2] = {ld16<AUX_SC1>(wsr, xoff, 0), ld16<AUX_SC1>(wsr, xoff, xs2)};
    SB();
#pragma unroll
    for (int t = 0; t < 4; ++t) {
      const u32x2 lo = tr_read(w2a ^ (t * 32)), hi = tr_read((w2a ^ (t * 32)) + 4096);
      b2[t] = u32x4{lo[0], lo[1], hi[0], hi[1]};
    }
#pragma unroll
    for (int t = 0; t < 4; ++t) PIN(b2[t]);
    SB();
    const f32x4 z4 = {0.f, 0.f, 0.f, 0.f};
    auto xp_bad = [&]() { return !all_ok(max(max(max(xg[0][0], xg[0][1]), max(xg[0][2], xg[0][3])), max(max(xg[1][0], xg[1][1]), max(xg[1][2], xg[1][3]))) != SENT); };
    f32x4 v = mfma16(one, xg[1], mfma16(one, xg[0], z4));
    if (__builtin_expect(xp_bad(), 0)) {
#pragma nounroll
      for (int spin = 0; spin < SPIN_MAX; ++spin) {
        __builtin_amdgcn_s_sleep(1);
        CFENCE();
        xg[0] = ld16<AUX_SC1>(wsr, xoff, 0); xg[1] = ld16<AUX_SC1>(wsr, xoff, xs2);
        if (!xp_bad()) break;
      }
      v = mfma16(one, xg[1], mfma16(one, xg[0], z4));
      SB();
      asm volatile(".Lend_e:");
    }
    STAMP(5); WSTAMP(5);
    {
      const f32x2 a = v.xy, hc = __builtin_elementwise_fma(gc, a, xc), x1 = __builtin_elementwise_fma(ga, a, xin);
      *LDSP(u32, tdst) = pack_bf(hc[0], hc[1]);
      *LDSP(float, sdst) = swap32_add(x1[0] + x1[1], fmaf(x1[0], x1[0], x1[1] * x1[1]));
    }
    STAMP(16);
    LDS_BARRIER();
    STAMP(17);
    {
      const f32x4 z = {0.f, 0.f, 0.f, 0.f};
      const u32x4 af0 = LDSP(u32x4, apa)[0];
      SB();
      const f32x4 s4 = LDSP(f32x4, ssrc)[0], q4 = LDSP(f32x4, ssrc)[8];
      const u32x4 af1 = LDSP(u32x4, apa)[32];
      SB();
      f32x4 d[4];
      d[0] = mfma16(af0, bf[0], z); d[1] = mfma16(af0, bf[1], z);
      SB();
      const f32x2 s2 = s4.xy + s4.zw, q2 = q4.xy + q4.zw;
      float s = s2[0] + s2[1], q = q2[0] + q2[1];
      SB();
      d[2] = mfma16(af0, bf[2], z);
      SB();
      s = swap16_add(s, q);
      SB();
      d[3] = mfma16(af0, bf[3], z);
      SB();
      s += dpp<XOR1>(s); s += dpp<XOR2>(s);
      SB();
      d[0] = mfma16(af1, bf[4], d[0]);
      SB();
      s += dpp<XOR7>(s); s += dpp<XOR8>(s);
      SB();
      d[1] = mfma16(af1, bf[5], d[1]);
      SB();
      const auto sq = __builtin_amdgcn_permlane16_swap(__float_as_uint(s), __float_as_uint(s), false, false);
      const float mean = u2f(sq[0]) * (1.f / 512.f);
      SB();
      d[2] = mfma16(af1, bf[6], d[2]);
      SB();
      const float var = fmaf(u2f(sq[1]), 1.f / 512.f, -mean * mean);
      SB();
      d[3] = mfma16(af1, bf[7], d[3]);
      SB();
      float ri = __builtin_amdgcn_rsqf(fmaxf(var, 0.f) + 1e-5f);
      float cf = fmaf(-ri * mean, Afp, Bfp);
      float rk = ri * -1.44269504f, ck = cf * -1.44269504f;
      PIN(ri); PIN(cf); PIN(rk); PIN(ck);
      SB();
      PIN(d[0]); PIN(d[1]);
      float y = d[0][0] + d[1][1];
      PIN(y); PIN(d[2]);
      y += d[2][2];
      PIN(y); PIN(d[3]);
      y += d[3][3];
      y = __builtin_bit_cast(float, __builtin_amdgcn_update_dpp(__builtin_bit_cast(int, y), __builtin_bit_cast(int, y), XOR8, 0xa, 0xf, false));
      y = swap16_add(y, y);
      const float ex = __builtin_amdgcn_exp2f(fmaf(y, rk, ck));
      y = fmaf(y, ri, cf);
      const float gu = y * dpp<0x104>(y);
      *LDSP(u16, adst) = (u16)pack_bf(gu * __builtin_amdgcn_rcpf(1.f + ex), 0.f);
    }
    STAMP(18);
    LDS_BARRIER();
    STAMP(10);
    {
      const u32x4 af = *LDSP(u32x4, asrc);
      const f32x4 z = {0.f, 0.f, 0.f, 0.f};
      f32x4 d0 = mfma16(af, b2[0], z), d1 = mfma16(af, b2[1], z), d2 = mfma16(af, b2[2], z), d3 = mfma16(af, b2[3], z);
      asm volatile("" : "+v"(d0), "+v"(d1), "+v"(d2), "+v"(d3));
      const bool g1 = g & 1, g2 = g & 2;
      const float e0 = g1 ? d1[0] : d0[0], e1 = g1 ? d1[1] : d0[1], f0 = g1 ? d3[0] : d2[0], f1 = g1 ? d3[1] : d2[1];
      const u32x2 ov = {pack_bf(g2 ? f0 : e0, g2 ? f1 : e1), tag};
      __builtin_amdgcn_raw_buffer_store_b64(ov, wsr, ooff, near ^ 8192u, AUX_SC1);
      __builtin_amdgcn_raw_buffer_store_b64(ov, wsr, ooff, near, AUX_SC1);
    }
    asm volatile(".Lend_b:");
    STAMP(6);
    FINISH();
  }
  if (wave == 0) {
    if (!all_ok((sg[0][1] == tag) & (sg[0][3] == tag) & (sg[1][1] == tag) & (sg[1][3] == tag))) {
      u32x4 t[2];
      gather16<2>(wsr, G_MOD + (1024 + fd) * 8, 1536 * 8, tag, t);
      sg[0] = t[0]; sg[1] = t[1];
    }
    u32 xa;
    {
      due = t1 + P.N2 * 256;
      const int tn = notices();
      due = tn + P.X3 * 256;
      until(due);
    }
    const bool hb = g & 1;
#pragma nounroll
    for (int spin = 0; spin < SPIN_MAX; ++spin) {
      CFENCE();
      xa = __builtin_amdgcn_raw_buffer_load_b32(wsr, dup(G_XP) + near + (g >> 1) * 16384 + (c >> 2) * 2048 + (g & 1) * 1024 + fd * 2, 0, AUX_SC1);
      if (all_ok(xa != SENT)) break;
      __builtin_amdgcn_s_sleep(1);
    }
    float a0 = lo_f(xa), a1 = hi_f(xa);
    a0 += dpp<0x124>(a0); a1 += dpp<0x124>(a1);
    a0 += dpp<XOR8>(a0); a1 += dpp<XOR8>(a1);
    a0 = swap32_add(a0, a0); a1 = swap32_add(a1, a1);
    f32x2 xv = {rbf(lo_f(sx2) + rbf((hb ? hi_f(sg[0][0]) : lo_f(sg[0][0])) * rbf(a0))), rbf(hi_f(sx2) + rbf((hb ? hi_f(sg[0][2]) : lo_f(sg[0][2])) * rbf(a1)))};
    f32x2 gv = {hb ? hi_f(sg[1][0]) : lo_f(sg[1][0]), hb ? hi_f(sg[1][2]) : lo_f(sg[1][2])};
    PIN(xv); PIN(gv);
    if (wi == 0 && lane == 0) __builtin_amdgcn_raw_buffer_store_b32(tag, wsr, WS_EPOCH, 0, AUX_SC1);
    STAMP(10);
    due = max(due + P.X4 * 256, (int)(((u32)wall_clock64() - t0) << 8) + P.GX * 256);
    const bool last = lane < (NUP - 32) * 4;
    u32 o0 = dup(G_OP + (lane >> 2) * 4096 + fd * 8) + near, o2 = last ? o0 : OOB, tg2 = last ? tag : 0u;
    const rsrc_t rout = mk_rsrc(out, 2048u);
    u32 oo = (lane & 44) == 0 ? (g * 512 + fd) * 2 : OOB;
    const u32 pk = (c & 10) ? 0u : ((c & 4) ? 0x3f800000u : 0x00003f80u);
    u32x4 one = {(c & 1) ? 0u : pk, 0u, (c & 1) ? pk : 0u, 0u};
    PIN(o0); PIN(o2); PIN(tg2); PIN(oo); PIN(one);
    __builtin_amdgcn_raw_buffer_store_b32(pack_bf(xv[0], xv[1]), rout, oo, 0, AUX_SC1);
    u32 s1_ = 131072, s2_ = 262144;
    asm volatile("" : "+s"(s1_), "+s"(s2_));
    until(due);
    f32x4 sv;
#pragma nounroll
    for (int spin = 0; spin < SPIN_MAX; ++spin) {
      CFENCE();
      const u32x4 og[3] = {ld16<AUX_SC1>(wsr, o0, 0), ld16<AUX_SC1>(wsr, o0, s1_), ld16<AUX_SC1>(wsr, o2, s2_)};
      sv = f32x4{0.f, 0.f, 0.f, 0.f};
#pragma unroll
      for (int k = 0; k < 3; ++k) sv = mfma16(one, og[k], sv);
      SB();
      u32 bad = ((og[0][1] ^ tag) | (og[0][3] ^ tag) | (og[1][1] ^ tag)) | ((og[1][3] ^ tag) | (og[2][1] ^ tg2) | (og[2][3] ^ tg2));
      PIN(bad);
      SB();
      float s0 = sv[0] + dpp<0x124>(sv[0]), s1 = sv[1] + dpp<0x124>(sv[1]);
      PIN(s0); PIN(s1);
      s0 += dpp<XOR8>(s0); s1 += dpp<XOR8>(s1);
      PIN(s0); PIN(s1);
      __builtin_amdgcn_raw_buffer_store_b32(pack_bf(fmaf(gv[0], s0, xv[0]), fmaf(gv[1], s1, xv[1])), rout, oo, 0, AUX_SC1);
      SB();
      if (all_ok(bad == 0u)) break;
      __builtin_amdgcn_s_sleep(1);
    }
    STAMP(7);
    asm volatile(".Lend_c:");
  }
  FINISH();
}

extern "C" void kernel_launch(void* const* d_in, const int* in_sizes, int n_in,
                              void* d_out, int out_size, void* d_ws, size_t ws_size,
                              hipStream_t stream) {
  fused_kernel<<<256, 512, 0, stream>>>((const u16*)d_in[1], (const u16*)d_in[2], (const u16*)d_in[4], (const u16*)d_in[3], (const u16*)d_in[10], (const u16*)d_in[6], (const u16*)d_in[7],
                                        (unsigned char*)d_ws, (const u16*)d_in[0], (const u16*)d_in[9], (const u16*)d_in[8], (u16*)d_out);
}
```

```cpp
#define PROBE_MUL 10
#include <hip/hip_runtime.h>
#include <stdint.h>

typedef unsigned int u32;
typedef unsigned short u16;
typedef unsigned long long u64;
typedef __attribute__((ext_vector_type(4))) unsigned u32x4;
typedef __attribute__((ext_vector_type(2))) unsigned u32x2;
typedef __attribute__((ext_vector_type(2))) __bf16 bf16x2;
typedef __attribute__((ext_vector_type(4))) float f32x4;
typedef __attribute__((ext_vector_type(2))) float f32x2;
typedef __amdgpu_buffer_rsrc_t rsrc_t;

constexpr int NH = 32, SK = 16384, DH = 128, WIN = 4096, FF = 1376;
struct Prm { int E0, ES, E1, E2, E2C, EU, TEND, DT_UP, L0, L1, X1, X2, X3, X4, NE, FE, M0, M1, M2, M3, M4, GS, GX, N1, N2, GA, WD, DT_WO, LW, LU, EW, PM, NF, NR, C0, A0, AG, AC, TS, TW, TK, TG, GB, B0, BG, BF, D0, DG, TC, LM, C1, W[9]; };
constexpr Prm PRM0 = {32, 20, 32, 32, 54, 58, 1980, 60, 12, 30, 88, 70, 100, 89, 3, 8, 0x2511f55b, 0x2b7425d5, 0x6017665, 0x17e0b9ec, 0x31944305, 30, 30, 36, 62, 30, 4, 100, 45, 15, 0, 0x0, 15, 14, 3, 30, 4, 4, 509, 228, 60, 20, 8, 74, 26, 8, 8, 0, 0, 120, 2, 0x77777777, 0x77777777, 0x77777777, 0x77777777, 0x77777777, 0x44444477, 0x44444444, 0x44444444, 0x44444444};
constexpr int NL = 37;
constexpr int SK0 = 21, SK1 = 32;
constexpr int NCMIN = (NL - SK1 + SK0) * 8;
constexpr int WK = 8, WV = 5;
constexpr int WUSE = 12, LNS = 7;
constexpr int QP = 14, QD = 4;
constexpr int VQ = 6;
constexpr int NSL = (NL + 7) / 8;
constexpr int VS = VQ + NSL;
constexpr int MLS = VS + 2;
constexpr int VSIDE = MLS - 5, UPREP = MLS + 6;
constexpr int CPF = 17;
constexpr int SMO = 1;
constexpr int RPS = 24, RPP = 28;
constexpr int PRB = 12;
constexpr int TBS = 7, TBP = 10, TBL = 13, TBU = 17;
constexpr int window(int s, int w) {
  int n = w;
  for (int u = s - w + 1; u <= s; ++u) n += u == SMO ? 6 : (u == TBL || u == SK1 || u == NL + CPF || u == NL + SK1 ? 1 : (u == QP || u == NL + VQ - 4 ? 2 : (u == NL + VSIDE ? 4 : 0)));
  return n;
}
constexpr int cons_end(int v) {
  const int a = v - WV + 1, c = (v - MLS) * 4;
  const int m = a < c ? a : c;
  return m < 0 ? 0 : (m > NL ? NL : m);
}
constexpr u32 col(const Prm& P, int j) { const int m[6] = {P.M0, P.M1, P.M2, P.M3, P.M4, P.PM}; return (u32)(m[j / 3] >> (j % 3 * 10) & 1023) << 10; }
constexpr u32 first_col(const Prm& P, int k) {
  if (k < 4) return col(P, 12 + k) | 1u << (24 + k);
  if (k < 6) return col(P, k - 4);
  if (k < 8) return col(P, 4 + k) | 1u << (16 + k);
  if (k < 11) return col(P, k - 1);
  return (col(P, 2) ^ col(P, 3) ^ col(P, 4) ^ col(P, 5) ^ col(P, 6) ^ col(P, 10) ^ col(P, 11)) | 0xf00000u;
}
constexpr bool one_to_one(const Prm& P) {
  u32 c[10] = {};
  for (int j = 0; j < 10; ++j) c[j] = col(P, j);
  for (int j = 0; j < 10; ++j) {
    if (c[j] == 0) return false;
    const u32 low = c[j] & (0u - c[j]);
    for (int k = j + 1; k < 10; ++k) if (c[k] & low) c[k] ^= c[j];
  }
  return true;
}
static_assert(one_to_one(PRM0), "every KB of the window exactly once");
struct Tab { u32 e[512]; };
constexpr Tab make_tab(const Prm& P) {
  Tab T{};
  for (int bid = 0; bid < 256; ++bid) {
    const int xcd = bid & 7, part = bid >> 4 & 3, i8 = (bid >> 3 & 1) | bid >> 6 << 1, wi = i8 * 8 + xcd;
    const bool has_mod = wi < 48, has_up = wi >= 21;
    const int cb0 = (i8 + P.C0) & 7, cb1 = (i8 + P.C1) & 7, ms = wi - 20;
    const int mi = wi < 8 ? wi : (wi < 16 ? 8 + ((xcd + 4) & 7) : (wi < 20 ? (wi - 14) * 8 + 3 : 16 + ms / 7 * 8 + ms % 7 + (ms % 7 >= 3 ? 1 : 0)));
    const int ui = xcd * 5 + (xcd > 5 ? xcd - 5 : 0) + (i8 == 6 ? 0 : (xcd >= 5 ? 1 : 0) + (i8 == 7 ? 4 : i8 - 2));
    const bool edge = i8 >= 6;
    const int wd = cb0 == 3 ? 0 : P.WD;
    const int e[4] = {P.E0 - 4 * wd + (cb0 == 3 ? P.ES : 0), P.E1 + (cb1 == 3 ? P.ES : 0), has_mod ? (wi < 16 ? P.E2C : P.E2) + ((mi & 7) == 3 ? P.ES : 0) : 0, has_up ? P.EU + (edge ? P.TC : 0) : 0};
    int tw[4] = {}, win[4] = {}, span[4] = {}, k0[4] = {};
    for (int k = 0; k < 4; ++k) {
      const int np = k == 0 ? 8 - wd : 8;
      tw[k] = np ? e[k] * P.TW / (16 * np) : 0;
      k0[k] = P.NF + tw[k] * np / 2 > P.TK ? P.NF + tw[k] * np / 2 : P.TK;
      span[k] = P.TEND - k0[k] - P.TG - (k == 0 ? (wd ? P.DT_WO : P.A0) : (k == 3 && has_up ? P.DT_UP : 0));
      win[k] = span[k] - (k == 0 && wd ? P.EW : (k == 3 && has_up ? P.GA + (edge ? P.LM : 0) : 0));
    }
    int n[4] = {NCMIN / 8, NCMIN / 8, NCMIN / 8, NCMIN / 8};
    for (int left = 128 - NCMIN / 2; left > 0; --left) {
      int best = -1;
      for (int k = 3; k >= 0; --k)
        if (n[k] < NL && (best < 0 || n[k] * win[best] < n[best] * win[k])) best = k;
      ++n[best];
    }
    const int c[4] = {0, n[0] - 1, n[0] + n[1] - 2, n[0] + n[1] + n[2] - 3};
    const int nl = n[part];
    const bool has_w = part < 2 || (part == 2 ? has_mod : has_up);
    const int blk = !has_w ? 0 : (part == 0 ? cb0 : (part == 1 ? xcd * 8 + cb1 : (part == 2 ? mi : ui)));
    T.e[2 * bid] = (u32)(c[part] | nl << 7 | blk << 13 | (has_w ? 1 : 0) << 19 | (part == 0 ? wd : 0) << 20) | (u32)tw[part] << 24;
    T.e[2 * bid + 1] = (u32)(span[part] * P.TS / (4 * (nl - 1)) | span[part] * (1024 - P.TS) / (4 * (nl - 1)) << 16);
  }
  return T;
}
constexpr int step_range(const Prm& P, bool mx_) {
  int mx = 0, mn = 1 << 20;
  const Tab T = make_tab(P);
  for (int bid = 0; bid < 256; ++bid) { const int n = T.e[2 * bid] >> 7 & 63; mx = n > mx ? n : mx; mn = n < mn ? n : mn; }
  return mx_ ? mx : mn;
}
extern "C" __attribute__((used, visibility("protected"))) __constant__ const Tab wg_tab = make_tab(PRM0);
static_assert(step_range(PRM0, true) <= NL && step_range(PRM0, false) >= NCMIN / 8, "a wave's key range does not fit its load steps");
static_assert(UPREP + 1 < SK0 && QP + QD + 1 < SK0 && cons_end(SK0 - 1) == SK0 - WV && NL - SK1 == WV, "steps every wave runs");
constexpr int NUP = FF / 32;
constexpr int SPIN_MAX = 1 << 17;

constexpr u32 WS_EPOCH = 0;
constexpr u32 G_TB = 1024;
constexpr u32 G_MOD = 4096;
constexpr u32 G_Q = G_MOD + 3072 * 8;
constexpr u32 G_ML = G_Q + 4096 * 8;
constexpr u32 G_U = G_ML + 256 * 16;
constexpr u32 G_P = 0;
constexpr u32 G_XP = G_P + 256 * 256;
constexpr u32 G_OP = G_XP + 8 * 512 * 4;
constexpr u32 G_NT = G_OP + 43 * 512 * 8;
constexpr u32 G_PB = G_OP + 44 * 512 * 8;
constexpr u32 G_VT = G_U + 2 * (G_PB + 8192);
constexpr u32 WS_END = G_VT + 1024;
__device__ __forceinline__ u32 dup(u32 o) { return G_U + ((o >> 13) << 14) + (o & 8191u); }
constexpr u32 SENT = 0xffffffffu;
constexpr u32 OOB = 0x40000000u;
#define WST 0

constexpr int AUX_NT = 2, AUX_SC1 = 16;

__device__ __forceinline__ float lo_f(u32 v) { return __uint_as_float(v << 16); }
__device__ __forceinline__ float hi_f(u32 v) { return __uint_as_float(v & 0xffff0000u); }
__device__ __forceinline__ float rbf(float x) { return (float)(__bf16)x; }
__device__ __forceinline__ u32 pack_bf(float lo, float hi) {
  bf16x2 r; r[0] = (__bf16)lo; r[1] = (__bf16)hi; return __builtin_bit_cast(u32, r);
}
__device__ __forceinline__ float dot2(u32 k, u32 q, float acc) {
  return __builtin_amdgcn_fdot2_f32_bf16(__builtin_bit_cast(bf16x2, k), __builtin_bit_cast(bf16x2, q), acc, false);
}
template <int CTRL> __device__ __forceinline__ float dpp(float x) {
  return __builtin_bit_cast(float, __builtin_amdgcn_mov_dpp(__builtin_bit_cast(int, x), CTRL, 0xf, 0xf, true));
}
constexpr int XOR1 = 0xB1, XOR2 = 0x4E, XOR7 = 0x141, XOR8 = 0x128;
__device__ __forceinline__ float swap32_add(float a, float b) {
  auto t = __builtin_amdgcn_permlane32_swap(__float_as_uint(a), __float_as_uint(b), false, false);
  return __uint_as_float(t[0]) + __uint_as_float(t[1]);
}
__device__ __forceinline__ float swap16_add(float a, float b) {
  auto t = __builtin_amdgcn_permlane16_swap(__float_as_uint(a), __float_as_uint(b), false, false);
  return __uint_as_float(t[0]) + __uint_as_float(t[1]);
}
__device__ __forceinline__ float wave_sum(float x) {
  x += dpp<XOR1>(x); x += dpp<XOR2>(x); x += dpp<XOR7>(x); x += dpp<XOR8>(x);
  x = swap16_add(x, x);
  return swap32_add(x, x);
}
__device__ __forceinline__ float wave_max(float x) {
  x = fmaxf(x, dpp<XOR1>(x)); x = fmaxf(x, dpp<XOR2>(x)); x = fmaxf(x, dpp<XOR7>(x)); x = fmaxf(x, dpp<XOR8>(x));
  auto s = __builtin_amdgcn_permlane16_swap(__float_as_uint(x), __float_as_uint(x), false, false);
  x = fmaxf(__uint_as_float(s[0]), __uint_as_float(s[1]));
  auto t = __builtin_amdgcn_permlane32_swap(__float_as_uint(x), __float_as_uint(x), false, false);
  return fmaxf(__uint_as_float(t[0]), __uint_as_float(t[1]));
}
template <int CTRL> __device__ __forceinline__ float sel_add(float lo, float hi, bool up) {
  const float t1 = lo + dpp<CTRL>(lo), t2 = hi + dpp<CTRL>(hi);
  return up ? t2 : t1;
}
template <int I> __device__ __forceinline__ float bc16(float x) {
  return __builtin_bit_cast(float, __builtin_amdgcn_update_dpp(0, __builtin_bit_cast(int, x), 0x150 + I, 0xf, 0xf, false));
}
__device__ __forceinline__ float bcast16(float x, int i) {
  switch (i) {
    case 0: return bc16<0>(x); case 1: return bc16<1>(x); case 2: return bc16<2>(x); case 3: return bc16<3>(x);
    case 4: return bc16<4>(x); case 5: return bc16<5>(x); case 6: return bc16<6>(x); case 7: return bc16<7>(x);
    case 8: return bc16<8>(x); case 9: return bc16<9>(x); case 10: return bc16<10>(x); case 11: return bc16<11>(x);
    case 12: return bc16<12>(x); case 13: return bc16<13>(x); case 14: return bc16<14>(x); default: return bc16<15>(x);
  }
}
__device__ __forceinline__ float silu_f(float x) { return x * __builtin_amdgcn_rcpf(1.f + __expf(-x)); }

template <int AUX> __device__ __forceinline__ u32x4 ld16(rsrc_t r, u32 voff, u32 soff) {
  return __builtin_amdgcn_raw_buffer_load_b128(r, voff, soff, AUX);
}
template <int AUX> __device__ __forceinline__ u32x2 ld8(rsrc_t r, u32 voff, u32 soff) {
  return __builtin_amdgcn_raw_buffer_load_b64(r, voff, soff, AUX);
}
__device__ __forceinline__ void st8_sc1(rsrc_t r, u32 voff, u32 v, u32 tag) {
  __builtin_amdgcn_raw_buffer_store_b64(u32x2{v, tag}, r, voff, 0, AUX_SC1);
}
__device__ __forceinline__ void st16_sc1(rsrc_t r, u32 voff, u32 v0, u32 v1, u32 tag) {
  __builtin_amdgcn_raw_buffer_store_b128(u32x4{v0, tag, v1, tag}, r, voff, 0, AUX_SC1);
}
__device__ __forceinline__ rsrc_t mk_rsrc(const void* p, u32 bytes) {
  return __builtin_amdgcn_make_buffer_rsrc(const_cast<void*>(p), 0, bytes, 0x00020000);
}
#define WAIT_VM(n) __builtin_amdgcn_s_waitcnt(((n) & 15) | (((n) >> 4) << 14) | (7 << 4) | (15 << 8))
#define SB() __builtin_amdgcn_sched_barrier(0)
#define CFENCE() asm volatile("" ::: "memory")
__device__ __forceinline__ bool all_ok(bool ok) { return __builtin_amdgcn_ballot_w64(!ok) == 0ull; }

template <int N> __device__ __forceinline__ void gather16(rsrc_t ws, u32 voff, u32 stride, u32 tag, u32x4 (&g)[N]) {
#pragma nounroll
  for (int spin = 0; spin < SPIN_MAX; ++spin) {
    CFENCE();
    bool ok = true;
#pragma unroll
    for (int i = 0; i < N; ++i) g[i] = ld16<AUX_SC1>(ws, voff + i * stride, 0);
#pragma unroll
    for (int i = 0; i < N; ++i) ok &= (g[i][1] == tag) & (g[i][3] == tag);
    if (all_ok(ok)) break;
    __builtin_amdgcn_s_sleep(8);
  }
}
template <int N> __device__ __forceinline__ void gather8(rsrc_t ws, u32 voff, u32 stride, u32 tag, u32 (&g)[N], bool act = true) {
#pragma nounroll
  for (int spin = 0; spin < SPIN_MAX; ++spin) {
    CFENCE();
    bool ok = true;
    u32x2 t[N];
#pragma unroll
    for (int i = 0; i < N; ++i) t[i] = ld8<AUX_SC1>(ws, voff + i * stride, 0);
#pragma unroll
    for (int i = 0; i < N; ++i) { ok &= (t[i][1] == tag); g[i] = t[i][0]; }
    if (all_ok(ok | !act)) break;
    __builtin_amdgcn_s_sleep(8);
  }
}

struct Smem {
  float red[8][128];
  float ml[8][2];
  float r2[64 * 20];
  u16 hb[3 * 1024];
  float stat[8][64];
  float red2[8][128];
  u32 dummy[64];
  u32x2 bc;
  u32x4 zero[4];
  float pl[4][8];
  u32 mflag[8];
  u32x4 wbuf[8][8][64];
  __attribute__((aligned(256))) u16 w2b[32][512];
  u16 actl[2][32];
};

#define PROBE_K 4
#define PROBE_OP 0
#define PROBE_PART -1
#define STAMP(k) do { if ((k) == PROBE_K && tid == 0) pst = wall_clock64(); } while (0)
#define WSTAMP(k) do {} while (0)
#define FINISH() do { STAMP(8); if (tid == 0) { __hip_atomic_store(reinterpret_cast<u64*>(ws + (1u << 20)) + bid * 2, pt0, __ATOMIC_RELAXED, __HIP_MEMORY_SCOPE_AGENT); \
   __hip_atomic_store(reinterpret_cast<u64*>(ws + (1u << 20)) + bid * 2 + 1, (u64)(u16)((u32)pst - t0 - (t1 >> 8) + 1000) | (u64)(u16)((u32)wall_clock64() - t0 - (t1 >> 8) + 1000) << 16, __ATOMIC_RELAXED, __HIP_MEMORY_SCOPE_AGENT); } \
   if (bid == 8 && wave == 0) probe_end(ws, pt0, lane, sh0, sh1); return; } while (0)
#define PIN(x) asm volatile("" : "+v"(x))
#define LDS_BARRIER() do { __builtin_amdgcn_s_waitcnt(0xc07f); __builtin_amdgcn_s_barrier(); CFENCE(); } while (0)
#define WAVE_SYNC() do { __builtin_amdgcn_fence(__ATOMIC_RELEASE, "wavefront"); __builtin_amdgcn_wave_barrier(); __builtin_amdgcn_fence(__ATOMIC_ACQUIRE, "wavefront"); } while (0)

__device__ __forceinline__ void dma16(rsrc_t r, u32 voff, u32 soff, u32 lds_dst) {
  u32 keep;
  asm volatile("s_mov_b32 %0, m0\n\ts_mov_b32 m0, %4\n\ts_nop 4\n\tbuffer_load_dwordx4 %1, %2, %3 offen nt lds\n\ts_mov_b32 m0, %0"
               : "=&s"(keep) : "v"(voff), "s"(r), "s"(soff), "s"(lds_dst) : "memory");
}
__device__ __forceinline__ void ld16_into(u32x4& d, rsrc_t r, u32 voff, u32 soff) {
  asm volatile("s_nop 4\n\tbuffer_load_dwordx4 %0, %1, %2, %3 offen nt" : "+v"(d) : "v"(voff), "s"(r), "s"(soff) : "memory");
}
__device__ __forceinline__ u32 perm_lo(u32 a, u32 b) { return __builtin_amdgcn_perm(b, a, 0x05040100u); }
__device__ __forceinline__ u32 perm_hi(u32 a, u32 b) { return __builtin_amdgcn_perm(b, a, 0x07060302u); }
__device__ __forceinline__ float rdl(float x, int l) { return __int_as_float(__builtin_amdgcn_readlane(__float_as_int(x), l)); }

typedef __attribute__((ext_vector_type(8))) __bf16 bf16x8;
__device__ __forceinline__ f32x4 mfma16(u32x4 a, u32x4 b, f32x4 c) {
  return __builtin_amdgcn_mfma_f32_16x16x32_bf16(__builtin_bit_cast(bf16x8, a), __builtin_bit_cast(bf16x8, b), c, 0, 0, 0);
}
__device__ __forceinline__ float sum8(const float* p) {
  const f32x4 a = *reinterpret_cast<const f32x4*>(p), b = *reinterpret_cast<const f32x4*>(p + 4);
  return ((a[0] + b[0]) + (a[1] + b[1])) + ((a[2] + b[2]) + (a[3] + b[3]));
}
__device__ __forceinline__ float ln_ri(float var) {
  return __builtin_amdgcn_rcpf(rbf(__builtin_amdgcn_sqrtf(rbf(rbf(fmaxf(var, 0.f)) + 1e-5f))));
}
__device__ __forceinline__ void ln_stats2(const float (&x0)[8], const float (&x1)[8], float& mb0, float& ri0, float& mb1, float& ri1) {
  const float k0 = rdl(x0[0], 0), k1 = rdl(x1[0], 0);
  float s0 = 0.f, s1 = 0.f, q0 = 0.f, q1 = 0.f;
#pragma unroll
  for (int k = 0; k < 8; ++k) {
    const float d0 = x0[k] - k0, d1 = x1[k] - k1;
    s0 += d0; s1 += d1; q0 = fmaf(d0, d0, q0); q1 = fmaf(d1, d1, q1);
  }
  float t = swap16_add(swap32_add(s0, s1), swap32_add(q0, q1));
  t += dpp<XOR1>(t); t += dpp<XOR2>(t); t += dpp<XOR7>(t); t += dpp<XOR8>(t);
  auto u = __builtin_amdgcn_permlane16_swap(__float_as_uint(t), __float_as_uint(t), false, false);
  const float ms = __uint_as_float(u[0]) * (1.f / 512.f), mq = __uint_as_float(u[1]) * (1.f / 512.f);
  const float ri = ln_ri(fmaf(-ms, ms, mq));
  mb0 = rbf(k0 + rdl(ms, 0)); mb1 = rbf(k1 + rdl(ms, 32));
  ri0 = rdl(ri, 0); ri1 = rdl(ri, 32);
}
template <int I> struct IC { static constexpr int v = I; };
template <int I, int N, class F> __device__ __forceinline__ void sfor(F&& f) {
  if constexpr (I < N) { if (f(IC<I>{})) sfor<I + 1, N>(f); }
}
typedef __attribute__((ext_vector_type(4))) short s16x4;
typedef __attribute__((address_space(3))) s16x4* lds_s16x4p;
__device__ __forceinline__ u32x2 tr_read(u32 lds_addr) {
  return __builtin_bit_cast(u32x2, __builtin_amdgcn_ds_read_tr16_b64_v4i16((lds_s16x4p)lds_addr));
}
__device__ __forceinline__ u32 next_tag(u32 e) { return ((e | 0x40004000u) + 1u) & ~0x40004000u; }
__device__ __forceinline__ float u2f(u32 v) { return __uint_as_float(v); }
#define LDSP(T, a) ((__attribute__((address_space(3))) T*)(a))
template <class T> __device__ __forceinline__ u32 lds_addr(T* p) { u32 a = (u32)(uintptr_t)p; PIN(a); return a; }

#define TAB_LOAD "s_add_u32 s30, s28, wg_tab@rel32@lo+4\ns_addc_u32 s31, s29, wg_tab@rel32@hi+12\nglobal_load_dwordx2 %[te], %[va], s[30:31]\n"
#define FC(k) ".Lfc" #k
#define FC_REGS
#define FX(r, bit, k) "s_bitcmp1_b32 %[" #r "], " #bit "\ns_cselect_b32 %[sb], " FC(k) ", 0\ns_xor_b32 %[sa], %[sa], %[sb]\n"
__device__ __forceinline__ void probe_end(unsigned char* ws, u64 pt0, int lane, int sh0, int sh1) {
  while (wall_clock64() < pt0 + 5500ull) __builtin_amdgcn_s_sleep(8);
  u64 a[4], c2[4];
  for (int i = 0; i < 4; ++i) {
    a[i] = __hip_atomic_load(reinterpret_cast<u64*>(ws + (1u << 20)) + (i * 64 + lane) * 2, __ATOMIC_RELAXED, __HIP_MEMORY_SCOPE_AGENT);
    c2[i] = __hip_atomic_load(reinterpret_cast<u64*>(ws + (1u << 20)) + (i * 64 + lane) * 2 + 1, __ATOMIC_RELAXED, __HIP_MEMORY_SCOPE_AGENT);
  }
  int mn0 = 1 << 30, mx1 = -(1 << 30), mn1 = 1 << 30, sum = 0, cnt = 0;
  for (int i = 0; i < 4; ++i) {
    const int d0 = (int)(a[i] - pt0);
    mn0 = min(mn0, d0);
    if (c2[i] != 0ull) { const int d1 = (int)(c2[i] - pt0); mx1 = max(mx1, d1); mn1 = min(mn1, d1); sum += d1; cnt += 1; }
  }
  for (int o = 1; o < 64; o <<= 1) { mn0 = min(mn0, __shfl_xor(mn0, o)); mx1 = max(mx1, __shfl_xor(mx1, o)); mn1 = min(mn1, __shfl_xor(mn1, o)); sum += __shfl_xor(sum, o); cnt += __shfl_xor(cnt, o); }
  int m4 = 0, m8 = 0;
  for (int i = 0; i < 4; ++i) { m4 = max(m4, (int)(c2[i] & 0xffff)); m8 = max(m8, (int)(c2[i] >> 16 & 0xffff)); }
  for (int o = 1; o < 64; o <<= 1) { m4 = max(m4, __shfl_xor(m4, o)); m8 = max(m8, __shfl_xor(m8, o)); }
  int X = mn0 + 200 * sh0;
  const u64 tg = pt0 + (u64)(long long)(X + 8000);
  for (int i = 0; i < 4000000 && wall_clock64() < tg; ++i) __builtin_amdgcn_s_sleep(1);
}
__global__ void __launch_bounds__(512, 2) fused_kernel(const u16* kc_, const u16* vc_, const u16* wo_, const u16* wq_, const u16* wmod_, const u16* w1_, const u16* w3_,
                                                       unsigned char* ws_, const u16* x_, const u16* cnd_, const u16* w2_, u16* out_) {
  __shared__ __attribute__((aligned(256))) Smem sm;
  const u64 a_ka = (u64)__builtin_amdgcn_kernarg_segment_ptr();
  const int bid = blockIdx.x, tid = threadIdx.x;
  u64 wa, t64; u32x2 te; int tid0;
  u32 ar[14];
  u32 tch2, sa_, sb_, va_;
  u32x4 kr[NL], vr[NL];
  asm volatile(".set .Lfc0, %0\n.set .Lfc1, %1\n.set .Lfc2, %2\n.set .Lfc3, %3\n.set .Lfc4, %4\n.set .Lfc5, %5\n.set .Lfc6, %6\n.set .Lfc7, %7\n.set .Lfc8, %8\n.set .Lfc9, %9\n.set .Lfc10, %10\n.set .Lfc11, %11"
    :: "n"(first_col(PRM0, 0)), "n"(first_col(PRM0, 1)), "n"(first_col(PRM0, 2)), "n"(first_col(PRM0, 3)), "n"(first_col(PRM0, 4)), "n"(first_col(PRM0, 5)),
       "n"(first_col(PRM0, 6)), "n"(first_col(PRM0, 7)), "n"(first_col(PRM0, 8)), "n"(first_col(PRM0, 9)), "n"(first_col(PRM0, 10)), "n"(first_col(PRM0, 11)));
  asm volatile(
    "s_load_dwordx16 s[4:19], %[ka], 0x0\ns_load_dwordx8 s[20:27], %[ka], 0x40\n"
    "s_memrealtime %[t]\n"
    "v_lshlrev_b32 %[va], 3, %[bid]\n"
    "s_getpc_b64 s[28:29]\n.Lcpf_first:\n"
    TAB_LOAD
    "s_cmp_gt_u32 %[bid], 15\ns_cbranch_scc1 .Lno_cpf\n"
    "s_mov_b32 %[sa], .Lend_a-.Lcpf_first\ns_max_i32 %[sa], %[sa], .Lend_b-.Lcpf_first\ns_max_i32 %[sa], %[sa], .Lend_c-.Lcpf_first\ns_max_i32 %[sa], %[sa], .Lend_d-.Lcpf_first\ns_max_i32 %[sa], %[sa], .Lend_e-.Lcpf_first\n"
    "v_lshlrev_b32 %[va], 6, %[tid]\nv_min_u32 %[va], %[sa], %[va]\nglobal_load_dword %[pf], %[va], s[28:29]\n"
    ".Lno_cpf:\n"
    "v_readfirstlane_b32 %[t0], %[tid]\nv_and_b32 %[va], 63, %[tid]\n"
    "s_mov_b32 %[sa], " FC(11) "\n"
    FX(bid, 0, 0) FX(bid, 1, 1) FX(bid, 2, 2) FX(bid, 3, 3) FX(bid, 4, 4) FX(bid, 5, 5) FX(bid, 6, 6) FX(bid, 7, 7) FX(t0, 6, 8) FX(t0, 7, 9) FX(t0, 8, 10)
    "v_lshl_add_u32 %[va], %[va], 4, %[sa]\n"
    "s_waitcnt lgkmcnt(0)\nglobal_load_dwordx4 %[k0], %[va], s[4:5] nt\nglobal_load_dwordx4 %[v0], %[va], s[6:7] nt\n"
    "s_bitcmp1_b32 %[t0], 6\ns_cselect_b64 s[14:15], s[16:17], s[14:15]\n"
    "s_bitcmp1_b32 %[bid], 4\ns_cselect_b64 s[8:9], s[10:11], s[8:9]\ns_cselect_b64 s[12:13], s[14:15], s[12:13]\ns_bitcmp1_b32 %[bid], 5\ns_cselect_b64 %[wa], s[12:13], s[8:9]"
    : "=&{s4}"(ar[0]), "=&{s5}"(ar[1]), "=&{s6}"(ar[2]), "=&{s7}"(ar[3]), "=&{s18}"(ar[4]), "=&{s19}"(ar[5]), "=&{s20}"(ar[6]), "=&{s21}"(ar[7]), "=&{s22}"(ar[8]), "=&{s23}"(ar[9]),
      "=&{s24}"(ar[10]), "=&{s25}"(ar[11]), "=&{s26}"(ar[12]), "=&{s27}"(ar[13]), [wa] "=&s"(wa), [t0] "=&s"(tid0), [t] "=&s"(t64), [te] "=&v"(te), [sa] "=&s"(sa_), [sb] "=&s"(sb_),
      [va] "=&v"(va_), [pf] "=&v"(tch2), [k0] "=&v"(kr[0]), [v0] "=&v"(vr[0])
    : [ka] "s"(a_ka), [bid] "s"(bid), [tid] "v"(tid) : "s8", "s9", "s10", "s11", "s12", "s13", "s14", "s15", "s16", "s17", "s28", "s29", "s30", "s31", "scc", "memory" FC_REGS);
  asm volatile("" : "+s"(ar[0]), "+s"(ar[1]), "+s"(ar[2]), "+s"(ar[3]), "+s"(ar[4]), "+s"(ar[5]), "+s"(ar[6]), "+s"(ar[7]), "+s"(ar[8]), "+s"(ar[9]), "+s"(ar[10]), "+s"(ar[11]), "+s"(ar[12]), "+s"(ar[13]));
  asm volatile("" : "+s"(wa), "+s"(t64), "+s"(tid0), "+s"(sa_));
  const u32 tka = (u32)wall_clock64();
  const u64 pt0 = t64; u64 pst = (PROBE_K == 100 || PROBE_K == 102) ? pt0 : (PROBE_K == 101 ? pt0 + (u32)(tka - (u32)t64) : 0);
  const u32 t0 = tka;
  __builtin_assume(tid >= 0 && tid < 512 && tid0 >= 0 && tid0 < 512 && bid >= 0 && bid < 256);
  constexpr Prm P = PRM0;
  const int TEND = P.TEND;
  const int xcd = bid & 7, part = bid >> 4 & 3;
  const int lane = tid & 63, wave = tid0 >> 6;
  SB();
  if ((bid >> 4) == 14) {
    const int j = min((bid & 8) + wave, 9);
    const u64 p = ((__attribute__((address_space(4))) const u64*)a_ka)[2 + j];
    tch2 = __builtin_amdgcn_raw_buffer_load_b32(mk_rsrc((const void*)p, (j < 2 ? 4u << 20 : (j == 2 ? 3u << 20 : ((0x118 >> j & 1) ? FF * 1024u : 2048u)))), (lane << 16) + (lane * 4352 & 0x7fff), 512, AUX_NT);
  }
  const bool p3 = part == 3;
  u32 pitch = (u32)(0x005600c001000020ull >> (part * 16) & 0xffff) * 32u;
  const int sh = p3 ? 2 : 3;
  u32 wv0 = (u32)((p3 ? (wave >> 1) * 128 : wave * 64) + (part == 0 ? xcd * 512 : 0) + (lane >> sh)) * pitch + ((lane & (p3 ? 3 : 7)) ^ (lane >> (p3 ? 5 : 3))) * 16u;
  const u32 wimg = (u32)(uintptr_t)(&sm.wbuf[0][0][0]);
  u32 wsz = (part == 0 ? 4096u : 512u) * pitch, wld = wimg + (p3 ? (wave & 1) * 32768 + (wave >> 1) * 8192 : wave * 8192);
  PIN(wv0);
  asm volatile("" : "+s"(wsz), "+s"(wld), "+s"(pitch));
  auto karg = [&](int i) { return ar[2 * i] | (u64)ar[2 * i + 1] << 32; };
  const u64 a_kc = karg(0), a_vc = karg(1);
  unsigned char* ws = (unsigned char*)karg(2);
  const u16 *x = (const u16*)karg(3), *cnd = (const u16*)karg(4), *w2 = (const u16*)karg(5);
  u16* out = (u16*)karg(6);
  const rsrc_t wsr = mk_rsrc(ws, WS_END);
  asm volatile("s_waitcnt vmcnt(3)\n\ts_cmp_eq_u32 %1, 14\n\ts_cbranch_scc1 1f\n\ts_waitcnt vmcnt(2)\n1:" : "+v"(te) : "s"(bid >> 4) : "scc");
  const u32 te0 = __builtin_amdgcn_readfirstlane(te[0]), te1 = __builtin_amdgcn_readfirstlane(te[1]);
  const int bl0 = te0 & 127, nl = te0 >> 7 & 63, blk = te0 >> 13 & 63;
  const bool has_w = te0 >> 19 & 1;
  const rsrc_t rw = mk_rsrc((const void*)wa, wsz);
  const u32 wstep = pitch << (6 - sh), wflip = p3 ? 32u : 0u;
  u32 wso = blk << (sh + 4);
  int wleft = 8;
  auto piece = [&]() {
    dma16(rw, wv0, wso, wld);
    wso += wstep; wld += 1024;
    wv0 ^= (wleft & 1) ? wflip : 0u;
    --wleft;
  };
  SB();
  STAMP(23);
  const int b = bid >> 3 & 1, head = xcd * 4 + (bid >> 6 ^ 3);
  const int g = lane >> 4, c = lane & 15;
  const int i8 = b | bid >> 6 << 1, wi = i8 * 8 + xcd;
  const bool is_up = p3 && has_w, modc = part == 2 && wi < 16;
  const size_t base = ((size_t)(b * NH + head) * SK + (SK - WIN)) * DH;
  const rsrc_t rk = mk_rsrc((const u16*)a_kc + base, WIN * DH * 2), rv = mk_rsrc((const u16*)a_vc + base, WIN * DH * 2);
  int tau = te1 & 0xffff;
  const int tauw = te0 >> 24 << 7, wdef = te0 >> 20 & 15;
  int due = P.NF * 256, now = 0;
  auto gate = [&]() {
    while (now < due) { now = (int)(((u32)wall_clock64() - t0) << 8); if (now < due) __builtin_amdgcn_s_sleep(1); }
    due += tau;
  };
  auto until = [&](int t) { while ((int)(((u32)wall_clock64() - t0) << 8) < t); };
  const u32 pslot = dup(G_P + head * 2048 + (b * 4 + part) * 16);
  const u32 die = __builtin_amdgcn_s_getreg(20 | (2 << 6));
  const bool voter = p3 && i8 == 1, timer = p3 && i8 < 2;
  int vote = 0, lag = 0, shift = 0;
  const u32 pbo = dup(G_PB) + (xcd * 8 + wave) * 128;
  u32 near = 0;
  u32x2 ep; u32 xs0, xs1, tch1; u32x4 xr0, xr1;
  const rsrc_t rx = mk_rsrc(part == 2 ? cnd : x, (part == 1 || (part == 2 && has_w)) ? 2048u : 0u);
  auto small_ops = [&]() {
    ep = ld8<AUX_SC1>(wsr, wave == 7 ? WS_EPOCH : OOB, 0);
    xs0 = __builtin_amdgcn_raw_buffer_load_b16(rx, tid * 2, 0, 0); xs1 = __builtin_amdgcn_raw_buffer_load_b16(rx, tid * 2, 1024, 0);
    xr0 = ld16<0>(rx, part == 1 ? lane * 16 : OOB, 0); xr1 = ld16<0>(rx, part == 1 ? lane * 16 : OOB, 1024);
    tch1 = __builtin_amdgcn_raw_buffer_load_b32(mk_rsrc(w2 + (size_t)blk * 32 * 512, is_up ? 64u : 0u), tid == 384 ? 0 : OOB, 0, AUX_NT);
  };
  if (has_w) {
    if (modc) now = 1 << 30;
#pragma nounroll
    while (wleft > wdef) { gate(); due += tauw - tau; piece(); }
    now = 0;
  }
  due = max(due, P.TK * 256);
  SB();
  u32 sot = sa_ & 0xffc00u;
  {
    const u32 s1 = (bl0 + lane - 1 - (lane >= SK0 ? NL - nl : 0)) ^ (124 + part);
#pragma unroll
    for (int k = 0; k < 7; ++k) sot ^= (u32)-(int)(s1 >> k & 1u) & col(P, k);
    PIN(sot);
  }
  u32 tag = 0;
  u32x4 bf[8];
  auto wo_prep = [&](int s0 = 0, int s1 = 2) {
    const u32 wi_ = wimg + wave * 8192;
    const int q = (lane >> 2) & 3, pp = lane & 3;
#pragma unroll
    for (int s = s0; s < s1; ++s) {
#pragma unroll
      for (int t = 0; t < 4; ++t) {
        const int r0 = s * 32 + g * 8 + q, r1 = r0 + 4, ch = t * 2 + (pp >> 1);
        const u32x2 lo = tr_read(wi_ + r0 * 128 + ((ch ^ (r0 & 7)) * 16) + (pp & 1) * 8), hi = tr_read(wi_ + r1 * 128 + ((ch ^ (r1 & 7)) * 16) + (pp & 1) * 8);
        bf[s * 4 + t] = u32x4{lo[0], lo[1], hi[0], hi[1]};
      }
    }
  };
  auto gemv_go = [&](u32 hh) {
    sm.hb[tid] = (u16)hh; sm.hb[512 + tid] = (u16)(hh >> 16);
    WAVE_SYNC();
    const u32x4* a = (c & 11) == 0 ? reinterpret_cast<const u32x4*>(&sm.hb[(c >> 2) * 512 + wave * 64 + g * 8]) : &sm.zero[0];
    const u32x4 a0 = a[0], a1 = a[(c & 11) == 0 ? 4 : 0];
    const f32x4 z = {0.f, 0.f, 0.f, 0.f};
    f32x4 d[4];
#pragma unroll
    for (int t = 0; t < 4; ++t) d[t] = mfma16(a0, bf[t], z);
#pragma unroll
    for (int t = 0; t < 4; ++t) d[t] = mfma16(a1, bf[4 + t], d[t]);
    if (lane < 32) {
#pragma unroll
      for (int t = 0; t < 4; ++t) sm.r2[(t * 16 + c) * 20 + g * 8 + wave] = d[t][0];
    }
  };
  auto mod_stage = [&]() {
    wo_prep();
    gemv_go(pack_bf(silu_f(lo_f(xs0)), silu_f(lo_f(xs1))));
    __builtin_amdgcn_s_waitcnt(0xc07f);
    if (lane == 0) *LDSP(volatile u32, (u32)(uintptr_t)&sm.mflag[wave]) = 1u;
    if (wave == 7) {
#pragma nounroll
      for (int spin = 0; spin < SPIN_MAX; ++spin) {
        const u32 f = *LDSP(volatile u32, (u32)(uintptr_t)&sm.mflag[lane & 7]);
        if (all_ok(f != 0u)) break;
        __builtin_amdgcn_s_sleep(1);
      }
      CFENCE();
      st8_sc1(wsr, G_MOD + (blk * 64 + lane) * 8, pack_bf(sum8(&sm.r2[lane * 20]), sum8(&sm.r2[lane * 20 + 8])), tag);
    }
    STAMP(9);
  };
  if (lane < 4) sm.zero[lane] = u32x4{0u, 0u, 0u, 0u};
  if (part == 2 && has_w) {
    if (tid < 8) sm.mflag[tid] = 0u;
    LDS_BARRIER();
    if (__builtin_expect(modc, 0)) {
      small_ops();
      __builtin_amdgcn_raw_buffer_store_b128(u32x4{SENT, SENT, SENT, SENT}, wsr, G_U + wi * 10240 + tid * 16, 0, AUX_SC1);
      __builtin_amdgcn_raw_buffer_store_b128(u32x4{SENT, SENT, SENT, SENT}, wsr, tid < 128 ? G_U + wi * 10240 + tid * 16 : OOB, 8192, AUX_SC1);
      asm volatile("s_waitcnt vmcnt(0)" ::: "memory");
      tag = next_tag(__builtin_amdgcn_readfirstlane(ep[0]));
      mod_stage();
      __builtin_amdgcn_s_barrier();
    }
  }
  SB();
  const int fd = wi * 8 + (lane & 3) * 2;
  const int ub = wave >> 2, ud = (wave & 3) * 128 + c * 8 + g * 2;
  u32x4 sg[3]; u32 sx2;
  auto side_loads = [&]() {
    const bool fin = part == 1 && wave == 0;
    sg[0] = ld16<AUX_SC1>(wsr, is_up ? G_MOD + (1024 + ud) * 8 : (fin ? G_MOD + (1024 + fd) * 8 : OOB), 0);
    sg[1] = ld16<AUX_SC1>(wsr, is_up ? G_MOD + (1536 + ud) * 8 : (fin ? G_MOD + (2560 + fd) * 8 : OOB), 0);
    sg[2] = ld16<AUX_SC1>(wsr, is_up ? G_MOD + (2048 + ud) * 8 : OOB, 0);
    sx2 = __builtin_amdgcn_raw_buffer_load_b32(mk_rsrc(x, (is_up || fin) ? 2048u : 0u), is_up ? ub * 1024 + ud * 2 : (g & 1) * 1024 + fd * 2, 0, 0);
  };
  auto side_pin = [&]() { PIN(sg[0]); PIN(sg[1]); PIN(sg[2]); PIN(sx2); };
  const u32 mlo = G_ML + (head * 2 + b) * 64 + (lane & 3) * 16;
  float fmrg = 0.f;
  auto merge_factor = [&](const u32x4& ml) {
    const float mi = u2f(ml[0]);
    float M = fmaxf(mi, dpp<XOR1>(mi)); M = fmaxf(M, dpp<XOR2>(M));
    const float e = __expf(mi - M);
    float L = e * u2f(ml[2]);
    L += dpp<XOR1>(L); L += dpp<XOR2>(L);
    fmrg = rdl(e * __builtin_amdgcn_rcpf(L), part);
  };
  f32x2 ga = {0.f, 0.f}, xin = {0.f, 0.f}, cc1 = {0.f, 0.f};
  float Af = 0.f, Bf = 0.f;
  const int hpos = ub * 512 + ud;
  auto up_prep = [&]() {
    if (!all_ok((sg[0][1] == tag) & (sg[0][3] == tag) & (sg[1][1] == tag) & (sg[1][3] == tag) & (sg[2][1] == tag) & (sg[2][3] == tag))) gather16<3>(wsr, G_MOD + (1024 + ud) * 8, 512 * 8, tag, sg);
    ga = f32x2{ub ? hi_f(sg[0][0]) : lo_f(sg[0][0]), ub ? hi_f(sg[0][2]) : lo_f(sg[0][2])};
    cc1 = f32x2{rbf(1.f + (ub ? hi_f(sg[2][0]) : lo_f(sg[2][0]))), rbf(1.f + (ub ? hi_f(sg[2][2]) : lo_f(sg[2][2])))};
    xin = f32x2{lo_f(sx2), hi_f(sx2)};
    u32* hp = reinterpret_cast<u32*>(&sm.hb[hpos]);
    hp[512] = pack_bf(cc1[0], cc1[1]);
    hp[1024] = ub ? perm_hi(sg[1][0], sg[1][2]) : perm_lo(sg[1][0], sg[1][2]);
  };
  auto up_frags = [&]() {
    const int pp = lane & 3;
    const u32 a = wimg + (pp & 1) * 32768 + ((pp >> 1) * 32 + g * 8 + ((lane >> 2) & 3)) * 64 + (((wave >> 1) ^ ((g & 1) | (pp & 2))) * 16) + (wave & 1) * 8;
#pragma unroll
    for (int rb = 0; rb < 8; ++rb) {
      const u32x2 lo = tr_read(a + rb * 4096), hi = tr_read(a + rb * 4096 + 256);
      bf[rb] = u32x4{lo[0], lo[1], hi[0], hi[1]};
    }
  };
  const u32x4* ap = reinterpret_cast<const u32x4*>(&sm.hb[((lane >> 3) & 1) * 512 + (lane & 1) * 32 + g * 8]);
  const bool par = lane & 8;
  auto limit = [&](auto sc) {
    constexpr int s = sc.v;
    WAIT_VM(window(s, 1 + (PRM0.W[s >> 3] >> (4 * (s & 7)) & 7)));
    SB();
  };
  auto sample = [&]() { if (timer) lag += (int)(((u32)wall_clock64() - t0) << 8) - due + ((int)__builtin_amdgcn_s_getreg(7 | (3 << 11)) + 1) * tau; };
  auto report = [&](u32 dst) {
    if (timer) {
      if (lane == 0) sm.mflag[wave] = vote * 65536 + min(lag >> 8, 4000);
      lag = 0;
      LDS_BARRIER();
      if (wave == 7) {
        int s = sm.mflag[lane & 7];
        s += __builtin_amdgcn_mov_dpp(s, XOR1, 0xf, 0xf, true); s += __builtin_amdgcn_mov_dpp(s, XOR2, 0xf, 0xf, true); s += __builtin_amdgcn_mov_dpp(s, XOR7, 0xf, 0xf, true);
        if (lane < 8) st8_sc1(wsr, dst + lane * 128 + wi * 8, s, tag);
      }
    }
    SB();
  };
  int sh0 = 0, sh1 = 0;
  float l0 = 0.f, l1 = 0.f;
  u32x2 gmd[2], tbg;
  int t1 = 0;
#pragma unroll
  for (int i = SK0; i < SK1; ++i) kr[i] = u32x4{0u, 0u, 0u, 0u};
  const int first = SK0 + NL - nl;
  auto left_out = [&](int i) { int f = first; asm volatile("" : "+s"(f)); return i < f; };
  sfor<1, NL>([&](auto ic) {
    constexpr int i = ic.v;
    if constexpr (i >= SK0 && i < SK1) { if (left_out(i)) return true; }
    const u32 so = __builtin_amdgcn_readlane(sot, i);
    gate();
    if constexpr ((i >= TBS && i <= TBP) || (i >= RPS && i <= RPP)) sample();
    SB();
    if constexpr (i >= SK0 && i < SK1) ld16_into(kr[i], rk, lane * 16, so); else kr[i] = ld16<AUX_NT>(rk, lane * 16, so);
    if constexpr (i == SMO) small_ops();
    if constexpr (i == SMO + 1) { if (voter) __builtin_amdgcn_raw_buffer_store_b32(0u, wsr, pbo + (lane >> 5) * 8192 + (lane & 31) * 4, 0, AUX_SC1); }
    if constexpr (i == SK1) tbg = ld8<AUX_SC1>(wsr, wave == 7 ? G_VT + xcd * 128 + c * 8 : OOB, 0);
    if constexpr (i == TBL) tbg = ld8<AUX_SC1>(wsr, wave == 7 ? G_TB + xcd * 128 + c * 8 : OOB, 0);
    if constexpr (i == QP) { gmd[0] = ld8<AUX_SC1>(wsr, part == 1 ? G_MOD + tid * 8 : OOB, 0); gmd[1] = ld8<AUX_SC1>(wsr, part == 1 ? G_MOD + tid * 8 : OOB, 4096); }
    SB();
    SB();
    if (i >= 4) limit(ic);
    if (i == 1) STAMP(25);
    if (i == 8) STAMP(12);
    if (i == 20) STAMP(21);
    if (i == 32) STAMP(22);
    if (i == TBP) {
      if (timer) {
        if (lane == 0) sm.mflag[wave] = lag;
        lag = 0;
        LDS_BARRIER();
        if (wave == 7) {
          int s = sm.mflag[lane & 7];
          s += __builtin_amdgcn_mov_dpp(s, XOR1, 0xf, 0xf, true); s += __builtin_amdgcn_mov_dpp(s, XOR2, 0xf, 0xf, true); s += __builtin_amdgcn_mov_dpp(s, XOR7, 0xf, 0xf, true);
          if (lane < 8) st8_sc1(wsr, G_TB + lane * 128 + wi * 8, t0 << 10 | (u32)min(s / (2048 * (TBP - TBS + 1)), 1023), tag);
        }
      }
      SB();
    }
    if (i == PRB) {
      if (voter) {
        u64 ta, tb, tc; u32 dm;
        const u32 pa = pbo + (wave & 1) * 8192;
        asm volatile("s_memtime %0\n\ts_waitcnt lgkmcnt(0)\n\ts_load_dword %3, %4, %5\n\ts_waitcnt lgkmcnt(0)\n\ts_memtime %1\n\ts_waitcnt lgkmcnt(0)\n\t"
                     "s_load_dword %3, %4, %6\n\ts_waitcnt lgkmcnt(0)\n\ts_memtime %2\n\ts_waitcnt lgkmcnt(0)"
                     : "=&s"(ta), "=&s"(tb), "=&s"(tc), "=&s"(dm) : "s"(ws), "s"(pa), "s"(pa ^ 8192u));
        const int d = min(max((int)(2 * (u32)tb - (u32)ta - (u32)tc), -500), 500);
        vote = ((wave ^ die) & 1) ? -d : d;
        STAMP(26);
      }
      SB();
    }
    if (i == TBU) {
      if (wave == 7) {
        const bool ok = tbg[1] == tag;
        float lf = ok ? (float)min(max((int)((t0 << 10 | 1023u) - tbg[0]) >> 10, -300), 300) : 0.f, nf = ok ? 1.f : 0.f, lg = ok ? (float)(tbg[0] & 1023u) : 0.f;
        lf += dpp<XOR1>(lf); nf += dpp<XOR1>(nf); lg += dpp<XOR1>(lg); lf += dpp<XOR2>(lf); nf += dpp<XOR2>(nf); lg += dpp<XOR2>(lg);
        lf += dpp<XOR7>(lf); nf += dpp<XOR7>(nf); lg += dpp<XOR7>(lg); lf += dpp<XOR8>(lf); nf += dpp<XOR8>(nf); lg += dpp<XOR8>(lg);
        const float rn = __builtin_amdgcn_rcpf(fmaxf(nf, 1.f));
        if (lane == 0) sm.pl[3][7] = __uint_as_float((u32)min(max((int)((lg * rn - (float)P.GS) * (float)P.GB * 0.125f), 0), 150));
        shift = __builtin_amdgcn_readfirstlane(min(max((int)((lg * rn - (float)P.GS) * (float)P.GB * 0.125f), 0), 150));
        if (lane == 0) sm.bc = u32x2{tag, t0 - (u32)(int)(lf * rn) + shift};
      }
      LDS_BARRIER();
      const u32x2 bc = sm.bc;
      tag = __builtin_amdgcn_readfirstlane(bc[0]);
      sh0 = __builtin_amdgcn_readfirstlane(__float_as_uint(sm.pl[3][7]));
      t1 = (__builtin_amdgcn_readfirstlane((int)(bc[1] - t0)) + TEND + P.X1) * 256;
      tau -= __builtin_amdgcn_readfirstlane((int)((float)(int)(t0 - bc[1]) * 256.f * __builtin_amdgcn_rcpf((float)(nl - TBU - 1))));
      SB();
    }
    if (i == RPP) report(G_VT);
    if (i == LNS) {
      tag = next_tag(__builtin_amdgcn_readfirstlane(ep[0]));
      if (part == 1) {
        float x0[8], x1[8], mb0, ri0, mb1, ri1;
#pragma unroll
        for (int k = 0; k < 4; ++k) { x0[2 * k] = lo_f(xr0[k]); x0[2 * k + 1] = hi_f(xr0[k]); x1[2 * k] = lo_f(xr1[k]); x1[2 * k + 1] = hi_f(xr1[k]); }
        ln_stats2(x0, x1, mb0, ri0, mb1, ri1);
        l0 = rbf(rbf(lo_f(xs0) - mb0) * ri0); l1 = rbf(rbf(lo_f(xs1) - mb1) * ri1);
      }
      PIN(l0); PIN(l1);
      SB();
    }
    if (i == WUSE) {
      PIN(tch1); PIN(tch2);
      if ((part == 1 || (part == 2 && has_w && !modc))) {
        WAIT_VM(WK); CFENCE();
        if (part == 2) mod_stage(); else wo_prep();
      }
#pragma unroll
      for (int k = 0; k < 8; ++k) PIN(bf[k]);
      SB();
    }
    if (i == QP + QD) {
      if (part == 1) {
        u32 gm[2] = {gmd[0][0], gmd[1][0]};
        if (!all_ok((gmd[0][1] == tag) & (gmd[1][1] == tag))) { STAMP(11); gather8<2>(wsr, G_MOD + tid * 8, 512 * 8, tag, gm); }
        STAMP(5);
        gemv_go(pack_bf(rbf(l0 * rbf(1.f + lo_f(gm[1]))) + lo_f(gm[0]), rbf(l1 * rbf(1.f + hi_f(gm[1]))) + hi_f(gm[0])));
      }
      SB();
    }
    if (i == QP + QD + 1) {
      if (part == 1) {
        LDS_BARRIER();
        if (wave == 0) {
          const int bb = lane >> 5, jq = lane & 31;
          st8_sc1(wsr, G_Q + (bb * 2048 + blk * 32 + jq) * 8, pack_bf(sum8(&sm.r2[2 * jq * 20 + bb * 8]), sum8(&sm.r2[(2 * jq + 1) * 20 + bb * 8])), tag);
        }
        STAMP(6);
      }
      SB();
    }
    return true;
  });
  PIN(sot);
  if (wave == 7) {
    const bool ok = tbg[1] == tag;
    float vt = ok ? (float)((int)tbg[0] >> 16) : 0.f, lg = ok ? (float)(tbg[0] & 0xffffu) : 0.f, nf = ok ? 8.f * (RPP - RPS + 1) : 0.f;
    vt += dpp<XOR1>(vt); lg += dpp<XOR1>(lg); nf += dpp<XOR1>(nf); vt += dpp<XOR2>(vt); lg += dpp<XOR2>(lg); nf += dpp<XOR2>(nf);
    vt += dpp<XOR7>(vt); lg += dpp<XOR7>(lg); nf += dpp<XOR7>(nf); vt += dpp<XOR8>(vt); lg += dpp<XOR8>(lg); nf += dpp<XOR8>(nf);
    lg *= __builtin_amdgcn_rcpf(fmaxf(nf, 1.f));
    if (lane == 0) sm.bc = u32x2{__float_as_uint(vt), (u32)min(max((int)((lg - (float)P.B0) * (float)(P.BG * 32)) - shift * P.D0 * 32, 0), 200 * 256)};
  }
  LDS_BARRIER();
  {
    const u32x2 bc = sm.bc;
    const int st = __builtin_amdgcn_readfirstlane(bc[1]), sp = (st * P.BF) >> 3;
    near = ((__builtin_amdgcn_readfirstlane(bc[0]) >> 31 ^ die ^ P.NR ^ 1) & 1) * 8192u;
    t1 += st; sh1 = st >> 8;
    due += P.TG * 256 + sp;
    tau = (te1 >> 16) + __builtin_amdgcn_readfirstlane((int)((float)(st - sp) * __builtin_amdgcn_rcpf((float)(nl - 3))));
  }
  const u32 qoff = G_Q + ((b * 32 + head) * 64 + c * 4) * 8;
  u32x4 qg[2], mlg;
  u32 qq[4];
  constexpr int NB = (NL + 15) / 16;
  float xlo, sc[NB], pr[NB], m = 0.f;
  f32x4 acc = {0.f, 0.f, 0.f, 0.f};
  u32 pb[NB], pm[4], pl8[8], cpf;
  const u32 adst0 = lds_addr(lane < 32 ? &sm.red[wave][c * 8 + g * 4] : &sm.r2[(lane - 32) * 4]);
  u32 po, rdst, rsrc, xo;
  auto wo_addr = [&]() {
    po = dup(G_P + xcd * 8192) + tid * 16 + near;
    rdst = lane < 32 ? (u32)(uintptr_t)&sm.red2[wave][0] + lane * 16 : (u32)(uintptr_t)&sm.r2[0] + (lane - 32) * 16;
    rsrc = (u32)(uintptr_t)&sm.red2[0][((wave & 1) * 16 + (lane & 15)) * 4 + (lane >> 4)];
    xo = dup(G_XP + xcd * 2048 + (wave & 1) * 1024 + (blk * 64 + (lane & ~1)) * 2) + (lane & 1) * 8192;
    PIN(po); PIN(rdst); PIN(rsrc); PIN(xo);
  };
  u32 pso = (wave == 0 && (g & 1) == 0) ? pslot + (c >> 3) * 1024 + (c & 3) * 256 + (c >> 2 & 1) * 128 + (g >> 1) * 8192 : OOB;
  PIN(pso);
  const u32 nadd = wdef ? P.LW : (is_up ? P.LU : 0);
  u32 nto = tid < 2 ? dup(G_NT) + bid * 8 + tid * 8192 : OOB;
  PIN(nto);
  auto notices = [&]() -> int {
    u32 off = dup(G_NT) + near + lane * 16;
    PIN(off);
    until(due);
    int mx;
#pragma nounroll
    for (int spin = 0; spin < SPIN_MAX; ++spin) {
      CFENCE();
      const u32x4 ga_ = ld16<AUX_SC1>(wsr, off, 0), gb_ = ld16<AUX_SC1>(wsr, off, 1024);
      mx = max(max((int)(ga_[0] - t0), (int)(ga_[2] - t0)), max((int)(gb_[0] - t0), (int)(gb_[2] - t0)));
      if (all_ok((((ga_[1] ^ tag) | (ga_[3] ^ tag)) | ((gb_[1] ^ tag) | (gb_[3] ^ tag))) == 0u)) break;
      for (int k = 0; k < P.AG; ++k) __builtin_amdgcn_s_sleep(1);
    }
    if (all_ok(mx <= (t1 >> 8) - P.AC)) return t1;
    mx = max(mx, __builtin_amdgcn_mov_dpp(mx, XOR1, 0xf, 0xf, true)); mx = max(mx, __builtin_amdgcn_mov_dpp(mx, XOR2, 0xf, 0xf, true));
    mx = max(mx, __builtin_amdgcn_mov_dpp(mx, XOR7, 0xf, 0xf, true)); mx = max(mx, __builtin_amdgcn_mov_dpp(mx, XOR8, 0xf, 0xf, true));
    mx = max(max(__builtin_amdgcn_readlane(mx, 0), __builtin_amdgcn_readlane(mx, 16)), max(__builtin_amdgcn_readlane(mx, 32), __builtin_amdgcn_readlane(mx, 48)));
    return max(min(mx, 1 << 20) * 256, t1 + P.L0 * 256) + P.L1 * 256;
  };
  auto pv_row = [&](auto jc) {
    constexpr int i = jc.v;
    const u32 pv = __float_as_uint(bcast16(u2f(pb[i >> 4]), i & 15));
    acc = mfma16(u32x4{pv & pm[0], pv & pm[1], pv & pm[2], pv & pm[3]}, vr[i], acc);
    PIN(acc);
    return true;
  };
  sfor<0, NL - 1>([&](auto ic) {
    constexpr int v = ic.v;
    if constexpr (v >= SK0 && v < SK1) { if (left_out(v)) return true; }
    if constexpr (v > 0) {
      const u32 so = __builtin_amdgcn_readlane(sot, v);
      if constexpr (v >= SK1) { if (v == NL - P.NE) tau = (tau * P.FE) >> 4; }
      gate();
      SB();
      if constexpr (v >= SK0 && v < SK1) ld16_into(vr[v], rv, lane * 16, so); else vr[v] = ld16<AUX_NT>(rv, lane * 16, so);
      SB();
      SB();
    }
    if constexpr (v == SK0 - 1) {
#pragma unroll
      for (int i = SK0; i < SK1; ++i) { vr[i] = u32x4{0u, 0u, 0u, 0u}; PIN(vr[i]); }
    }
    if constexpr (v == VQ - 4) { qg[0] = ld16<AUX_SC1>(wsr, qoff, 0); qg[1] = ld16<AUX_SC1>(wsr, qoff + 16, 0); }
    if constexpr (v == VSIDE) side_loads();
    if constexpr (v == CPF) {
      u64 pc; u32 dlt, len;
      asm volatile("s_getpc_b64 %0\n.Lcpf_here:\n\ts_mov_b32 %1, .Ltail_start-.Lcpf_here\n\ts_mov_b32 %2, .Lend_a-.Ltail_start\n\ts_max_i32 %2, %2, .Lend_b-.Ltail_start\n\ts_max_i32 %2, %2, .Lend_c-.Ltail_start\n\ts_max_i32 %2, %2, .Lend_d-.Ltail_start\n\ts_max_i32 %2, %2, .Lend_e-.Ltail_start" : "=s"(pc), "=s"(dlt), "=s"(len) :: "scc");
      pc += dlt;
      const rsrc_t rc = mk_rsrc((const void*)pc, (part == 3 && i8 == 1) ? len : 0u);
      cpf = __builtin_amdgcn_raw_buffer_load_b32(rc, tid * 64, 0, 0);
    }
    if constexpr (v == SK1 + 2) PIN(cpf);
    if constexpr (v == SK1) mlg = ld16<AUX_SC1>(wsr, wave == 0 ? mlo : OOB, 0);
    SB();
    if constexpr (NL + v < 72) limit(IC<NL + v>{});
    if constexpr (v >= SK0 + WV) { WAIT_VM(v < SK1 ? WV : WV + 1); SB(); }
    if constexpr (v == VQ) {
      if (!all_ok((qg[0][1] == tag) & (qg[0][3] == tag) & (qg[1][1] == tag) & (qg[1][3] == tag))) { STAMP(20); gather16<2>(wsr, qoff, 16, tag, qg); }
      STAMP(1); WSTAMP(1);
      qq[0] = qg[0][0]; qq[1] = qg[0][2]; qq[2] = qg[1][0]; qq[3] = qg[1][2];
    }
    if constexpr (v >= VQ && v < VS) {
      constexpr int sl = v - VQ, bt = sl >> 1;
      float xs[8];
#pragma unroll
      for (int i = 0; i < 8; ++i) {
        float d = 0.f;
        if (sl * 8 + i < NL) {
#pragma unroll
          for (int k = 0; k < 4; ++k) d = dot2(kr[sl * 8 + i][k], qq[k], d);
        }
        xs[i] = d;
      }
#pragma unroll
      for (int h = 0; h < 4; ++h) xs[h] = sel_add<XOR7>(xs[h], xs[h + 4], c & 4);
#pragma unroll
      for (int h = 0; h < 2; ++h) xs[h] = sel_add<XOR2>(xs[h], xs[h + 2], c & 2);
      xs[0] = sel_add<XOR1>(xs[0], xs[1], c & 1);
      if constexpr ((sl & 1) || sl == NSL - 1) {
        const float t = (sl & 1) ? sel_add<XOR8>(xlo, xs[0], c & 8) : xs[0] + dpp<XOR8>(xs[0]);
        const float s = rbf(rbf(t) * 0.08838834764831845f);
        sc[bt] = (bt * 16 + c < SK0 || (bt * 16 + c >= first && bt * 16 + c < NL)) ? s : -__builtin_inff();
        PIN(sc[bt]);
      } else {
        xlo = xs[0];
        PIN(xlo);
      }
    }
    if constexpr (v == VS) {
      float mloc = sc[0];
#pragma unroll
      for (int bt = 1; bt < NB; ++bt) mloc = fmaxf(mloc, sc[bt]);
      m = fmaxf(wave_max(mloc), -3.0e38f);
      float ls = 0.f;
#pragma unroll
      for (int bt = 0; bt < NB; ++bt) { pr[bt] = __expf(sc[bt] - m); ls += pr[bt]; }
      const float l = wave_sum(ls);
      if (lane == 0) { sm.ml[wave][0] = m; sm.ml[wave][1] = l; }
      if (c == ((NL - 1) & 15)) sm.pl[g][wave] = pr[NB - 1];
      STAMP(2); WSTAMP(2);
    }
    if constexpr (v >= SK1) pv_row(IC<v - SK1 + SK0 - WV>{});
    if constexpr (v < SK0 || v >= SK0 + WV) sfor<cons_end(v - 1), cons_end(v)>(pv_row);
    if constexpr (v == SK1 + 1) {
      float p[8];
#pragma unroll
      for (int w = 0; w < 8; ++w) p[w] = sm.pl[g][w] * __expf(sm.ml[w][0] - m);
#pragma unroll
      for (int w = 0; w < 8; ++w) pl8[w] = pack_bf(p[w], p[w]);
#pragma unroll
      for (int w = 0; w < 8; ++w) PIN(pl8[w]);
    }
    if constexpr (v == MLS - 1) { side_pin(); if (is_up) up_prep(); PIN(ga); PIN(xin); PIN(cc1); }
    if constexpr (v == UPREP - 1) wo_addr();
    if constexpr (v == UPREP) {
#pragma unroll
      for (int k = 0; k < 8; ++k) asm volatile("" : "=v"(bf[k]));
      if (part == 0) wo_prep(); else if (is_up) up_frags();
#pragma unroll
      for (int k = 0; k < 8; ++k) PIN(bf[k]);
    }
    if constexpr (v == UPREP + 1) {
      if (is_up) {
        const u32x4* a1 = ap + 128 + ((lane >> 1) & 1) * 128;
        f32x4 d0 = {0.f, 0.f, 0.f, 0.f}, d1 = {0.f, 0.f, 0.f, 0.f};
#pragma unroll
        for (int rb = 0; rb < 4; ++rb) { d0 = mfma16(a1[rb * 8], bf[rb], d0); d1 = mfma16(a1[(rb + 4) * 8], bf[rb + 4], d1); }
        Af = par ? d0[1] + d1[1] : d0[0] + d1[0]; Bf = par ? d0[3] + d1[3] : d0[2] + d1[2];
        Af += dpp<XOR8>(Af); Bf += dpp<XOR8>(Bf);
      }
      PIN(Af); PIN(Bf);
    }
    if (v == 10 || v == 16) STAMP(15 + v / 6);
    if (v == 20) STAMP(18);
    if (v == 32) STAMP(19);
    if constexpr (v == MLS) {
      LDS_BARRIER();
      float mm[8], ll[8];
#pragma unroll
      for (int w = 0; w < 8; ++w) { const f32x2 t = *reinterpret_cast<const f32x2*>(&sm.ml[w][0]); mm[w] = t[0]; ll[w] = t[1]; }
      float Mwg = mm[0], Lwg = 0.f;
#pragma unroll
      for (int w = 1; w < 8; ++w) Mwg = fmaxf(Mwg, mm[w]);
      if (wave == 0) {
#pragma unroll
        for (int w = 0; w < 8; ++w) Lwg = fmaf(__expf(mm[w] - Mwg), ll[w], Lwg);
        if (lane == 0) st16_sc1(wsr, G_ML + ((head * 2 + b) * 4 + part) * 16, __float_as_uint(Mwg), __float_as_uint(Lwg), tag);
      }
      const float fw = __expf(m - Mwg);
      m = Mwg;
#pragma unroll
      for (int bt = 0; bt < NB; ++bt) { pb[bt] = pack_bf(pr[bt] * fw, pr[bt] * fw); PIN(pb[bt]); }
#pragma unroll
      for (int k = 0; k < 4; ++k) { pm[k] = (c >> 1 & 3) == k ? ((c & 1) ? 0xffff0000u : 0x0000ffffu) : 0u; PIN(pm[k]); }
    }
    SB();
    return true;
  });
  asm volatile(".Ltail_start:");
  if (wave != 0) {
    pv_row(IC<SK0 - 1>{});
    WAIT_VM(WV); SB();
    sfor<NL - 1 - WV, NL - 1>(pv_row);
    *LDSP(f32x4, adst0) = acc;
    WSTAMP(14);
    LDS_BARRIER();
  } else {
    u32x4 xr[8], am[8];
#pragma unroll
    for (int w = 0; w < 8; ++w) am[w] = u32x4{pl8[w] & pm[0], pl8[w] & pm[1], pl8[w] & pm[2], pl8[w] & pm[3]};
#pragma unroll
    for (int w = 0; w < 8; ++w) PIN(am[w]);
    {
      const u32 so = __builtin_amdgcn_readlane(sot, NL - 1);
      gate();
      SB();
#pragma unroll
      for (int w = 0; w < 8; ++w) xr[w] = ld16<AUX_NT>(rv, lane * 16, so ^ ((w & 1) ? col(P, 7) : 0u) ^ ((w & 2) ? col(P, 8) : 0u) ^ ((w & 4) ? col(P, 9) : 0u));
      SB();
    }
    u32 rsum = lds_addr(&sm.red[0][c * 8 + (g & 1) * 4]);
    pv_row(IC<SK0 - 1>{});
    WAIT_VM(WV + 8); SB();
    sfor<NL - 1 - WV, NL - 3>(pv_row);
    if (__builtin_expect(!all_ok((mlg[1] == tag) & (mlg[3] == tag)), 0)) {
#pragma nounroll
      for (int spin = 0; spin < SPIN_MAX; ++spin) {
        CFENCE();
        mlg = ld16<AUX_SC1>(wsr, mlo, 0);
        if (all_ok((mlg[1] == tag) & (mlg[3] == tag))) break;
      }
    }
    merge_factor(mlg);
    sfor<NL - 3, NL - 1>(pv_row);
    STAMP(14); WSTAMP(14);
    LDS_BARRIER();
    STAMP(15);
    f32x4 t[8];
#pragma unroll
    for (int w = 1; w < 8; ++w) t[w] = LDSP(f32x4, rsum)[w * 32];
    acc = ((acc + t[1]) + (t[2] + t[3])) + ((t[4] + t[5]) + (t[6] + t[7]));
    PIN(acc);
    STAMP(3);
    SB();
#pragma unroll
    for (int w = 0; w < 8; ++w) acc = mfma16(am[w], xr[w], acc);
  }
  WAIT_VM(0);
  acc *= fmrg;
  {
    const u32 w0 = min(pack_bf(acc[0], acc[1]), SENT - 1u), w1 = min(pack_bf(acc[2], acc[3]), SENT - 1u);
    const auto e0 = __builtin_amdgcn_permlane16_swap(w0, w0, false, false), e1 = __builtin_amdgcn_permlane16_swap(w1, w1, false, false);
    __builtin_amdgcn_raw_buffer_store_b128(u32x4{e0[0], e1[0], e0[1], e1[1]}, wsr, pso, 0, AUX_SC1);
  }
  st8_sc1(wsr, nto, (u32)wall_clock64() + nadd, tag);
  SB();
  STAMP(4);

  if (part == 2) FINISH();
  if (part == 0) {
    __builtin_amdgcn_s_barrier();
#pragma nounroll
    while (wleft > 0) piece();
    bool late = false;
    u32x4 og[2];
    if (wdef) {
      late = true;
      while ((int)(((u32)wall_clock64() - t0) << 8) < t1 - 20 * 256) if (__builtin_amdgcn_s_getreg(7 | (3 << 11)) == 0) { late = false; break; }
      if (__builtin_expect(late, 0)) { until(t1); CFENCE(); og[0] = ld16<AUX_SC1>(wsr, po, 0); SB(); WAIT_VM(1); } else { WAIT_VM(0); }
      CFENCE();
      if (P.WD > 4) wo_prep(0, 1);
      wo_prep(1, 2);
#pragma unroll
      for (int k = 0; k < 8; ++k) PIN(bf[k]);
      SB();
    }
    if (__builtin_expect(!late, 1)) { until(t1); CFENCE(); og[0] = ld16<AUX_SC1>(wsr, po, 0); }
    STAMP(11);
    SB();
    f32x4 d[4];
    auto wo_mm = [&]() {
      const f32x4 z = {0.f, 0.f, 0.f, 0.f};
#pragma unroll
      for (int t = 0; t < 4; ++t) { d[t] = mfma16(og[0], bf[t], z); og[1][t] = __float_as_uint(dpp<XOR8>(u2f(og[0][t]))); }
      SB();
#pragma unroll
      for (int t = 0; t < 4; ++t) d[t] = mfma16(og[1], bf[4 + t], d[t]);
    };
    auto p_bad = [&]() { return !all_ok(max(max(og[0][0], og[0][1]), max(og[0][2], og[0][3])) != SENT); };
    wo_mm();
    if (__builtin_expect(p_bad(), 0)) {
#pragma nounroll
      for (int spin = 0; spin < SPIN_MAX; ++spin) {
        __builtin_amdgcn_s_sleep(1);
        CFENCE();
        og[0] = ld16<AUX_SC1>(wsr, po, 0);
        if (!p_bad()) break;
      }
      wo_mm();
      SB();
      asm volatile(".Lend_d:");
    }
    STAMP(5); WSTAMP(5);
    {
      SB();
      f32x4 y; f32x2 h[4];
#pragma unroll
      for (int t = 0; t < 4; ++t) { h[t] = d[t].xy + d[t].zw; PIN(h[t]); }
#pragma unroll
      for (int t = 0; t < 4; ++t) { y[t] = h[t][0] + h[t][1]; PIN(y[t]); }
      *LDSP(f32x4, rdst) = y;
    }
    STAMP(16);
    LDS_BARRIER();
    STAMP(17);
    if (wave < 2) {
      f32x2 v[4];
#pragma unroll
      for (int w = 0; w < 4; ++w) v[w] = f32x2{LDSP(float, rsrc)[w * 256], LDSP(float, rsrc)[w * 256 + 128]};
      const f32x2 h = (v[0] + v[1]) + (v[2] + v[3]);
      const float y = h[0] + h[1];
      __builtin_amdgcn_raw_buffer_store_b32(min(pack_bf(dpp<0xA0>(y), dpp<0xF5>(y)), SENT - 1u), wsr, xo, 0, AUX_SC1);
      asm volatile(".Lend_a:");
    }
    STAMP(6);
    FINISH();
  }
  if (part == 3) {
    if (!has_w) FINISH();
    u32x4 b2[4];
    {
      __builtin_amdgcn_s_barrier();
      const rsrc_t r2 = mk_rsrc(w2 + (size_t)(blk * 32 + wave * 4) * 512, 4096u);
      const u32 l2 = (u32)(uintptr_t)(&sm.w2b[wave * 4][0]);
#pragma unroll
      for (int n = 0; n < 4; ++n) dma16(r2, (lane ^ (n * 2) ^ ((wave & 2) << 2)) * 16, n * 1024, l2 + n * 1024);
    }
    const u32 tdst = lds_addr(&sm.hb[hpos]);
    const u32 sdst = lds_addr(&sm.stat[wave][lane]);
    const u32 ssrc = lds_addr(&sm.stat[(g >> 1) * 4 + ((lane >> 3) & 3)][(lane & 7) * 4]);
    const u32 adst = lds_addr((c < 4 && (g & 1) == 0) ? &sm.actl[g >> 1][wave * 4 + c] : reinterpret_cast<u16*>(&sm.dummy[lane]));
    const u32 asrc = lds_addr(&sm.actl[lane & 1][g * 8]);
    const u32 apa = lds_addr(&sm.hb[(c >> 3) * 512 + (c & 3) * 64 + ((c >> 2) & 1) * 32 + g * 8]);
    constexpr u32 xs2 = 16384;
    u32 xoff = dup(G_XP) + near + g * 2048 + ub * 1024 + (wave & 3) * 256 + c * 16;
    u32x4 one;
#pragma unroll
    for (int k = 0; k < 4; ++k) one[k] = ((c & 2) == 0 && (c >> 2) == k) ? ((c & 1) ? 0x3f800000u : 0x00003f80u) : 0u;
    PIN(one);
    u32 ooff = dup(G_OP + (blk * 512 + tid) * 8);
    float Afp = Af, Bfp = Bf;
    PIN(xoff); PIN(ooff); PIN(Afp); PIN(Bfp);
    f32x2 gc = ga * cc1, xc = xin * cc1;
    PIN(ga); PIN(xin); PIN(gc); PIN(xc);
    u32 w2a = (u32)(uintptr_t)(&sm.w2b[0][0]) + (g * 8 + ((lane >> 2) & 3)) * 1024 + (wave ^ (g & 1)) * 128 + (lane & 15) * 8;
    PIN(w2a);
    if (wave == 1) {
      due = t1 + P.N1 * 256;
      const int tn = notices();
      due = tn + P.X2 * 256;
      until(due);
    }
    asm volatile("s_waitcnt vmcnt(0)" ::: "memory");
    STAMP(9);
    __builtin_amdgcn_s_barrier();
    STAMP(11);
    CFENCE();
    u32x4 xg[2] = {ld16<AUX_SC1>(wsr, xoff, 0), ld16<AUX_SC1>(wsr, xoff, xs2)};
    SB();
#pragma unroll
    for (int t = 0; t < 4; ++t) {
      const u32x2 lo = tr_read(w2a ^ (t * 32)), hi = tr_read((w2a ^ (t * 32)) + 4096);
      b2[t] = u32x4{lo[0], lo[1], hi[0], hi[1]};
    }
#pragma unroll
    for (int t = 0; t < 4; ++t) PIN(b2[t]);
    SB();
    const f32x4 z4 = {0.f, 0.f, 0.f, 0.f};
    auto xp_bad = [&]() { return !all_ok(max(max(max(xg[0][0], xg[0][1]), max(xg[0][2], xg[0][3])), max(max(xg[1][0], xg[1][1]), max(xg[1][2], xg[1][3]))) != SENT); };
    f32x4 v = mfma16(one, xg[1], mfma16(one, xg[0], z4));
    if (__builtin_expect(xp_bad(), 0)) {
#pragma nounroll
      for (int spin = 0; spin < SPIN_MAX; ++spin) {
        __builtin_amdgcn_s_sleep(1);
        CFENCE();
        xg[0] = ld16<AUX_SC1>(wsr, xoff, 0); xg[1] = ld16<AUX_SC1>(wsr, xoff, xs2);
        if (!xp_bad()) break;
      }
      v = mfma16(one, xg[1], mfma16(one, xg[0], z4));
      SB();
      asm volatile(".Lend_e:");
    }
    STAMP(5); WSTAMP(5);
    {
      const f32x2 a = v.xy, hc = __builtin_elementwise_fma(gc, a, xc), x1 = __builtin_elementwise_fma(ga, a, xin);
      *LDSP(u32, tdst) = pack_bf(hc[0], hc[1]);
      *LDSP(float, sdst) = swap32_add(x1[0] + x1[1], fmaf(x1[0], x1[0], x1[1] * x1[1]));
    }
    STAMP(16);
    LDS_BARRIER();
    STAMP(17);
    {
      const f32x4 z = {0.f, 0.f, 0.f, 0.f};
      const u32x4 af0 = LDSP(u32x4, apa)[0];
      SB();
      const f32x4 s4 = LDSP(f32x4, ssrc)[0], q4 = LDSP(f32x4, ssrc)[8];
      const u32x4 af1 = LDSP(u32x4, apa)[32];
      SB();
      f32x4 d[4];
      d[0] = mfma16(af0, bf[0], z); d[1] = mfma16(af0, bf[1], z);
      SB();
      const f32x2 s2 = s4.xy + s4.zw, q2 = q4.xy + q4.zw;
      float s = s2[0] + s2[1], q = q2[0] + q2[1];
      SB();
      d[2] = mfma16(af0, bf[2], z);
      SB();
      s = swap16_add(s, q);
      SB();
      d[3] = mfma16(af0, bf[3], z);
      SB();
      s += dpp<XOR1>(s); s += dpp<XOR2>(s);
      SB();
      d[0] = mfma16(af1, bf[4], d[0]);
      SB();
      s += dpp<XOR7>(s); s += dpp<XOR8>(s);
      SB();
      d[1] = mfma16(af1, bf[5], d[1]);
      SB();
      const auto sq = __builtin_amdgcn_permlane16_swap(__float_as_uint(s), __float_as_uint(s), false, false);
      const float mean = u2f(sq[0]) * (1.f / 512.f);
      SB();
      d[2] = mfma16(af1, bf[6], d[2]);
      SB();
      const float var = fmaf(u2f(sq[1]), 1.f / 512.f, -mean * mean);
      SB();
      d[3] = mfma16(af1, bf[7], d[3]);
      SB();
      float ri = __builtin_amdgcn_rsqf(fmaxf(var, 0.f) + 1e-5f);
      float cf = fmaf(-ri * mean, Afp, Bfp);
      float rk = ri * -1.44269504f, ck = cf * -1.44269504f;
      PIN(ri); PIN(cf); PIN(rk); PIN(ck);
      SB();
      PIN(d[0]); PIN(d[1]);
      float y = d[0][0] + d[1][1];
      PIN(y); PIN(d[2]);
      y += d[2][2];
      PIN(y); PIN(d[3]);
      y += d[3][3];
      y = __builtin_bit_cast(float, __builtin_amdgcn_update_dpp(__builtin_bit_cast(int, y), __builtin_bit_cast(int, y), XOR8, 0xa, 0xf, false));
      y = swap16_add(y, y);
      const float ex = __builtin_amdgcn_exp2f(fmaf(y, rk, ck));
      y = fmaf(y, ri, cf);
      const float gu = y * dpp<0x104>(y);
      *LDSP(u16, adst) = (u16)pack_bf(gu * __builtin_amdgcn_rcpf(1.f + ex), 0.f);
    }
    STAMP(18);
    LDS_BARRIER();
    STAMP(10);
    {
      const u32x4 af = *LDSP(u32x4, asrc);
      const f32x4 z = {0.f, 0.f, 0.f, 0.f};
      f32x4 d0 = mfma16(af, b2[0], z), d1 = mfma16(af, b2[1], z), d2 = mfma16(af, b2[2], z), d3 = mfma16(af, b2[3], z);
      asm volatile("" : "+v"(d0), "+v"(d1), "+v"(d2), "+v"(d3));
      const bool g1 = g & 1, g2 = g & 2;
      const float e0 = g1 ? d1[0] : d0[0], e1 = g1 ? d1[1] : d0[1], f0 = g1 ? d3[0] : d2[0], f1 = g1 ? d3[1] : d2[1];
      const u32x2 ov = {pack_bf(g2 ? f0 : e0, g2 ? f1 : e1), tag};
      __builtin_amdgcn_raw_buffer_store_b64(ov, wsr, ooff, near ^ 8192u, AUX_SC1);
      __builtin_amdgcn_raw_buffer_store_b64(ov, wsr, ooff, near, AUX_SC1);
    }
    asm volatile(".Lend_b:");
    STAMP(6);
    FINISH();
  }
  if (wave == 0) {
    if (!all_ok((sg[0][1] == tag) & (sg[0][3] == tag) & (sg[1][1] == tag) & (sg[1][3] == tag))) {
      u32x4 t[2];
      gather16<2>(wsr, G_MOD + (1024 + fd) * 8, 1536 * 8, tag, t);
      sg[0] = t[0]; sg[1] = t[1];
    }
    u32 xa;
    {
      due = t1 + P.N2 * 256;
      const int tn = notices();
      due = tn + P.X3 * 256;
      until(due);
    }
    const bool hb = g & 1;
#pragma nounroll
    for (int spin = 0; spin < SPIN_MAX; ++spin) {
      CFENCE();
      xa = __builtin_amdgcn_raw_buffer_load_b32(wsr, dup(G_XP) + near + (g >> 1) * 16384 + (c >> 2) * 2048 + (g & 1) * 1024 + fd * 2, 0, AUX_SC1);
      if (all_ok(xa != SENT)) break;
      __builtin_amdgcn_s_sleep(1);
    }
    float a0 = lo_f(xa), a1 = hi_f(xa);
    a0 += dpp<0x124>(a0); a1 += dpp<0x124>(a1);
    a0 += dpp<XOR8>(a0); a1 += dpp<XOR8>(a1);
    a0 = swap32_add(a0, a0); a1 = swap32_add(a1, a1);
    f32x2 xv = {rbf(lo_f(sx2) + rbf((hb ? hi_f(sg[0][0]) : lo_f(sg[0][0])) * rbf(a0))), rbf(hi_f(sx2) + rbf((hb ? hi_f(sg[0][2]) : lo_f(sg[0][2])) * rbf(a1)))};
    f32x2 gv = {hb ? hi_f(sg[1][0]) : lo_f(sg[1][0]), hb ? hi_f(sg[1][2]) : lo_f(sg[1][2])};
    PIN(xv); PIN(gv);
    if (wi == 0 && lane == 0) __builtin_amdgcn_raw_buffer_store_b32(tag, wsr, WS_EPOCH, 0, AUX_SC1);
    STAMP(10);
    due = max(due + P.X4 * 256, (int)(((u32)wall_clock64() - t0) << 8) + P.GX * 256);
    const bool last = lane < (NUP - 32) * 4;
    u32 o0 = dup(G_OP + (lane >> 2) * 4096 + fd * 8) + near, o2 = last ? o0 : OOB, tg2 = last ? tag : 0u;
    const rsrc_t rout = mk_rsrc(out, 2048u);
    u32 oo = (lane & 44) == 0 ? (g * 512 + fd) * 2 : OOB;
    const u32 pk = (c & 10) ? 0u : ((c & 4) ? 0x3f800000u : 0x00003f80u);
    u32x4 one = {(c & 1) ? 0u : pk, 0u, (c & 1) ? pk : 0u, 0u};
    PIN(o0); PIN(o2); PIN(tg2); PIN(oo); PIN(one);
    __builtin_amdgcn_raw_buffer_store_b32(pack_bf(xv[0], xv[1]), rout, oo, 0, AUX_SC1);
    u32 s1_ = 131072, s2_ = 262144;
    asm volatile("" : "+s"(s1_), "+s"(s2_));
    until(due);
    f32x4 sv;
#pragma nounroll
    for (int spin = 0; spin < SPIN_MAX; ++spin) {
      CFENCE();
      const u32x4 og[3] = {ld16<AUX_SC1>(wsr, o0, 0), ld16<AUX_SC1>(wsr, o0, s1_), ld16<AUX_SC1>(wsr, o2, s2_)};
      sv = f32x4{0.f, 0.f, 0.f, 0.f};
#pragma unroll
      for (int k = 0; k < 3; ++k) sv = mfma16(one, og[k], sv);
      SB();
      u32 bad = ((og[0][1] ^ tag) | (og[0][3] ^ tag) | (og[1][1] ^ tag)) | ((og[1][3] ^ tag) | (og[2][1] ^ tg2) | (og[2][3] ^ tg2));
      PIN(bad);
      SB();
      float s0 = sv[0] + dpp<0x124>(sv[0]), s1 = sv[1] + dpp<0x124>(sv[1]);
      PIN(s0); PIN(s1);
      s0 += dpp<XOR8>(s0); s1 += dpp<XOR8>(s1);
      PIN(s0); PIN(s1);
      __builtin_amdgcn_raw_buffer_store_b32(pack_bf(fmaf(gv[0], s0, xv[0]), fmaf(gv[1], s1, xv[1])), rout, oo, 0, AUX_SC1);
      SB();
      if (all_ok(bad == 0u)) break;
      __builtin_amdgcn_s_sleep(1);
    }
    STAMP(7);
    asm volatile(".Lend_c:");
  }
  FINISH();
}

extern "C" void kernel_launch(void* const* d_in, const int* in_sizes, int n_in,
                              void* d_out, int out_size, void* d_ws, size_t ws_size,
                              hipStream_t stream) {
  fused_kernel<<<256, 512, 0, stream>>>((const u16*)d_in[1], (const u16*)d_in[2], (const u16*)d_in[4], (const u16*)d_in[3], (const u16*)d_in[10], (const u16*)d_in[6], (const u16*)d_in[7],
                                        (unsigned char*)d_ws, (const u16*)d_in[0], (const u16*)d_in[9], (const u16*)d_in[8], (u16*)d_out);
}
```

```cpp
#define PROBE_MUL 10
#include <hip/hip_runtime.h>
#include <stdint.h>

typedef unsigned int u32;
typedef unsigned short u16;
typedef unsigned long long u64;
typedef __attribute__((ext_vector_type(4))) unsigned u32x4;
typedef __attribute__((ext_vector_type(2))) unsigned u32x2;
typedef __attribute__((ext_vector_type(2))) __bf16 bf16x2;
typedef __attribute__((ext_vector_type(4))) float f32x4;
typedef __attribute__((ext_vector_type(2))) float f32x2;
typedef __amdgpu_buffer_rsrc_t rsrc_t;

constexpr int NH = 32, SK = 16384, DH = 128, WIN = 4096, FF = 1376;
struct Prm { int E0, ES, E1, E2, E2C, EU, TEND, DT_UP, L0, L1, X1, X2, X3, X4, NE, FE, M0, M1, M2, M3, M4, GS, GX, N1, N2, GA, WD, DT_WO, LW, LU, EW, PM, NF, NR, C0, A0, AG, AC, TS, TW, TK, TG, GB, B0, BG, BF, D0, DG, TC, LM, C1, F0, FG, FF_, W[9]; };
constexpr Prm PRM0 = {32, 20, 32, 32, 54, 56, 1980, 60, 12, 30, 88, 70, 100, 89, 3, 9, 0x2511f55b, 0x2b7425d5, 0x6017665, 0x17e0b9ec, 0x31944305, 62, 30, 36, 62, 30, 4, 100, 45, 15, 0, 0x0, 15, 14, 3, 30, 4, 4, 508, 220, 75, 6, 9, 74, 26, 8, 8, 0, 0, 120, 2, 67, 22, 4, 0x77777777, 0x77777777, 0x77777777, 0x77777777, 0x77777777, 0x44444477, 0x44444444, 0x44444444, 0x44444444};
constexpr int NL = 37;
constexpr int SK0 = 21, SK1 = 32;
constexpr int NCMIN = (NL - SK1 + SK0) * 8;
constexpr int WK = 8, WV = 5;
constexpr int WUSE = 12, LNS = 7;
constexpr int QP = 14, QD = 4;
constexpr int VQ = 6;
constexpr int NSL = (NL + 7) / 8;
constexpr int VS = VQ + NSL;
constexpr int MLS = VS + 2;
constexpr int VSIDE = MLS - 5, UPREP = MLS + 6;
constexpr int CPF = 17;
constexpr int SMO = 1;
constexpr int RVS = 5, RVP = 8, RVL = 12, RVU = 16;
constexpr int RPS = 24, RPP = 28;
constexpr int PRB = 12;
constexpr int TBS = 7, TBP = 10, TBL = 13, TBU = 17;
constexpr int window(int s, int w) {
  int n = w;
  for (int u = s - w + 1; u <= s; ++u) n += u == SMO ? 6 : (u == TBL || u == SK1 || u == NL + CPF || u == NL + RVL || u == NL + SK1 ? 1 : (u == QP || u == NL + VQ - 4 ? 2 : (u == NL + VSIDE ? 4 : 0)));
  return n;
}
constexpr int cons_end(int v) {
  const int a = v - WV + 1, c = (v - MLS) * 4;
  const int m = a < c ? a : c;
  return m < 0 ? 0 : (m > NL ? NL : m);
}
constexpr u32 col(const Prm& P, int j) { const int m[6] = {P.M0, P.M1, P.M2, P.M3, P.M4, P.PM}; return (u32)(m[j / 3] >> (j % 3 * 10) & 1023) << 10; }
constexpr u32 first_col(const Prm& P, int k) {
  if (k < 4) return col(P, 12 + k) | 1u << (24 + k);
  if (k < 6) return col(P, k - 4);
  if (k < 8) return col(P, 4 + k) | 1u << (16 + k);
  if (k < 11) return col(P, k - 1);
  return (col(P, 2) ^ col(P, 3) ^ col(P, 4) ^ col(P, 5) ^ col(P, 6) ^ col(P, 10) ^ col(P, 11)) | 0xf00000u;
}
constexpr bool one_to_one(const Prm& P) {
  u32 c[10] = {};
  for (int j = 0; j < 10; ++j) c[j] = col(P, j);
  for (int j = 0; j < 10; ++j) {
    if (c[j] == 0) return false;
    const u32 low = c[j] & (0u - c[j]);
    for (int k = j + 1; k < 10; ++k) if (c[k] & low) c[k] ^= c[j];
  }
  return true;
}
static_assert(one_to_one(PRM0), "every KB of the window exactly once");
struct Tab { u32 e[512]; };
constexpr Tab make_tab(const Prm& P) {
  Tab T{};
  for (int bid = 0; bid < 256; ++bid) {
    const int xcd = bid & 7, part = bid >> 4 & 3, i8 = (bid >> 3 & 1) | bid >> 6 << 1, wi = i8 * 8 + xcd;
    const bool has_mod = wi < 48, has_up = wi >= 21;
    const int cb0 = (i8 + P.C0) & 7, cb1 = (i8 + P.C1) & 7, ms = wi - 20;
    const int mi = wi < 8 ? wi : (wi < 16 ? 8 + ((xcd + 4) & 7) : (wi < 20 ? (wi - 14) * 8 + 3 : 16 + ms / 7 * 8 + ms % 7 + (ms % 7 >= 3 ? 1 : 0)));
    const int ui = xcd * 5 + (xcd > 5 ? xcd - 5 : 0) + (i8 == 6 ? 0 : (xcd >= 5 ? 1 : 0) + (i8 == 7 ? 4 : i8 - 2));
    const bool edge = i8 >= 6;
    const int wd = cb0 == 3 ? 0 : P.WD;
    const int e[4] = {P.E0 - 4 * wd + (cb0 == 3 ? P.ES : 0), P.E1 + (cb1 == 3 ? P.ES : 0), has_mod ? (wi < 16 ? P.E2C : P.E2) + ((mi & 7) == 3 ? P.ES : 0) : 0, has_up ? P.EU + (edge ? P.TC : 0) : 0};
    int tw[4] = {}, win[4] = {}, span[4] = {}, k0[4] = {};
    for (int k = 0; k < 4; ++k) {
      const int np = k == 0 ? 8 - wd : 8;
      tw[k] = np ? e[k] * P.TW / (16 * np) : 0;
      k0[k] = P.NF + tw[k] * np / 2 > P.TK ? P.NF + tw[k] * np / 2 : P.TK;
      span[k] = P.TEND - k0[k] - P.TG - (k == 0 ? (wd ? P.DT_WO : P.A0) : (k == 3 && has_up ? P.DT_UP : 0));
      win[k] = span[k] - (k == 0 && wd ? P.EW : (k == 3 && has_up ? P.GA + (edge ? P.LM : 0) : 0));
    }
    int n[4] = {NCMIN / 8, NCMIN / 8, NCMIN / 8, NCMIN / 8};
    for (int left = 128 - NCMIN / 2; left > 0; --left) {
      int best = -1;
      for (int k = 3; k >= 0; --k)
        if (n[k] < NL && (best < 0 || n[k] * win[best] < n[best] * win[k])) best = k;
      ++n[best];
    }
    const int c[4] = {0, n[0] - 1, n[0] + n[1] - 2, n[0] + n[1] + n[2] - 3};
    const int nl = n[part];
    const bool has_w = part < 2 || (part == 2 ? has_mod : has_up);
    const int blk = !has_w ? 0 : (part == 0 ? cb0 : (part == 1 ? xcd * 8 + cb1 : (part == 2 ? mi : ui)));
    T.e[2 * bid] = (u32)(c[part] | nl << 7 | blk << 13 | (has_w ? 1 : 0) << 19 | (part == 0 ? wd : 0) << 20) | (u32)tw[part] << 24;
    T.e[2 * bid + 1] = (u32)(span[part] * P.TS / (4 * (nl - 1)) | span[part] * (1024 - P.TS) / (4 * (nl - 1)) << 16);
  }
  return T;
}
constexpr int step_range(const Prm& P, bool mx_) {
  int mx = 0, mn = 1 << 20;
  const Tab T = make_tab(P);
  for (int bid = 0; bid < 256; ++bid) { const int n = T.e[2 * bid] >> 7 & 63; mx = n > mx ? n : mx; mn = n < mn ? n : mn; }
  return mx_ ? mx : mn;
}
extern "C" __attribute__((used, visibility("protected"))) __constant__ const Tab wg_tab = make_tab(PRM0);
static_assert(step_range(PRM0, true) <= NL && step_range(PRM0, false) >= NCMIN / 8, "a wave's key range does not fit its load steps");
static_assert(UPREP + 1 < SK0 && QP + QD + 1 < SK0 && cons_end(SK0 - 1) == SK0 - WV && NL - SK1 == WV, "steps every wave runs");
constexpr int NUP = FF / 32;
constexpr int SPIN_MAX = 1 << 17;

constexpr u32 WS_EPOCH = 0;
constexpr u32 G_TB = 1024;
constexpr u32 G_MOD = 4096;
constexpr u32 G_Q = G_MOD + 3072 * 8;
constexpr u32 G_ML = G_Q + 4096 * 8;
constexpr u32 G_U = G_ML + 256 * 16;
constexpr u32 G_P = 0;
constexpr u32 G_XP = G_P + 256 * 256;
constexpr u32 G_OP = G_XP + 8 * 512 * 4;
constexpr u32 G_NT = G_OP + 43 * 512 * 8;
constexpr u32 G_PB = G_OP + 44 * 512 * 8;
constexpr u32 G_VT = G_U + 2 * (G_PB + 8192);
constexpr u32 WS_END = G_VT + 2048;
__device__ __forceinline__ u32 dup(u32 o) { return G_U + ((o >> 13) << 14) + (o & 8191u); }
constexpr u32 SENT = 0xffffffffu;
constexpr u32 OOB = 0x40000000u;
#define WST 0

constexpr int AUX_NT = 2, AUX_SC1 = 16;

__device__ __forceinline__ float lo_f(u32 v) { return __uint_as_float(v << 16); }
__device__ __forceinline__ float hi_f(u32 v) { return __uint_as_float(v & 0xffff0000u); }
__device__ __forceinline__ float rbf(float x) { return (float)(__bf16)x; }
__device__ __forceinline__ u32 pack_bf(float lo, float hi) {
  bf16x2 r; r[0] = (__bf16)lo; r[1] = (__bf16)hi; return __builtin_bit_cast(u32, r);
}
__device__ __forceinline__ float dot2(u32 k, u32 q, float acc) {
  return __builtin_amdgcn_fdot2_f32_bf16(__builtin_bit_cast(bf16x2, k), __builtin_bit_cast(bf16x2, q), acc, false);
}
template <int CTRL> __device__ __forceinline__ float dpp(float x) {
  return __builtin_bit_cast(float, __builtin_amdgcn_mov_dpp(__builtin_bit_cast(int, x), CTRL, 0xf, 0xf, true));
}
constexpr int XOR1 = 0xB1, XOR2 = 0x4E, XOR7 = 0x141, XOR8 = 0x128;
__device__ __forceinline__ float swap32_add(float a, float b) {
  auto t = __builtin_amdgcn_permlane32_swap(__float_as_uint(a), __float_as_uint(b), false, false);
  return __uint_as_float(t[0]) + __uint_as_float(t[1]);
}
__device__ __forceinline__ float swap16_add(float a, float b) {
  auto t = __builtin_amdgcn_permlane16_swap(__float_as_uint(a), __float_as_uint(b), false, false);
  return __uint_as_float(t[0]) + __uint_as_float(t[1]);
}
__device__ __forceinline__ float wave_sum(float x) {
  x += dpp<XOR1>(x); x += dpp<XOR2>(x); x += dpp<XOR7>(x); x += dpp<XOR8>(x);
  x = swap16_add(x, x);
  return swap32_add(x, x);
}
__device__ __forceinline__ float wave_max(float x) {
  x = fmaxf(x, dpp<XOR1>(x)); x = fmaxf(x, dpp<XOR2>(x)); x = fmaxf(x, dpp<XOR7>(x)); x = fmaxf(x, dpp<XOR8>(x));
  auto s = __builtin_amdgcn_permlane16_swap(__float_as_uint(x), __float_as_uint(x), false, false);
  x = fmaxf(__uint_as_float(s[0]), __uint_as_float(s[1]));
  auto t = __builtin_amdgcn_permlane32_swap(__float_as_uint(x), __float_as_uint(x), false, false);
  return fmaxf(__uint_as_float(t[0]), __uint_as_float(t[1]));
}
template <int CTRL> __device__ __forceinline__ float sel_add(float lo, float hi, bool up) {
  const float t1 = lo + dpp<CTRL>(lo), t2 = hi + dpp<CTRL>(hi);
  return up ? t2 : t1;
}
template <int I> __device__ __forceinline__ float bc16(float x) {
  return __builtin_bit_cast(float, __builtin_amdgcn_update_dpp(0, __builtin_bit_cast(int, x), 0x150 + I, 0xf, 0xf, false));
}
__device__ __forceinline__ float bcast16(float x, int i) {
  switch (i) {
    case 0: return bc16<0>(x); case 1: return bc16<1>(x); case 2: return bc16<2>(x); case 3: return bc16<3>(x);
    case 4: return bc16<4>(x); case 5: return bc16<5>(x); case 6: return bc16<6>(x); case 7: return bc16<7>(x);
    case 8: return bc16<8>(x); case 9: return bc16<9>(x); case 10: return bc16<10>(x); case 11: return bc16<11>(x);
    case 12: return bc16<12>(x); case 13: return bc16<13>(x); case 14: return bc16<14>(x); default: return bc16<15>(x);
  }
}
__device__ __forceinline__ float silu_f(float x) { return x * __builtin_amdgcn_rcpf(1.f + __expf(-x)); }

template <int AUX> __device__ __forceinline__ u32x4 ld16(rsrc_t r, u32 voff, u32 soff) {
  return __builtin_amdgcn_raw_buffer_load_b128(r, voff, soff, AUX);
}
template <int AUX> __device__ __forceinline__ u32x2 ld8(rsrc_t r, u32 voff, u32 soff) {
  return __builtin_amdgcn_raw_buffer_load_b64(r, voff, soff, AUX);
}
__device__ __forceinline__ void st8_sc1(rsrc_t r, u32 voff, u32 v, u32 tag) {
  __builtin_amdgcn_raw_buffer_store_b64(u32x2{v, tag}, r, voff, 0, AUX_SC1);
}
__device__ __forceinline__ void st16_sc1(rsrc_t r, u32 voff, u32 v0, u32 v1, u32 tag) {
  __builtin_amdgcn_raw_buffer_store_b128(u32x4{v0, tag, v1, tag}, r, voff, 0, AUX_SC1);
}
__device__ __forceinline__ rsrc_t mk_rsrc(const void* p, u32 bytes) {
  return __builtin_amdgcn_make_buffer_rsrc(const_cast<void*>(p), 0, bytes, 0x00020000);
}
#define WAIT_VM(n) __builtin_amdgcn_s_waitcnt(((n) & 15) | (((n) >> 4) << 14) | (7 << 4) | (15 << 8))
#define SB() __builtin_amdgcn_sched_barrier(0)
#define CFENCE() asm volatile("" ::: "memory")
__device__ __forceinline__ bool all_ok(bool ok) { return __builtin_amdgcn_ballot_w64(!ok) == 0ull; }

template <int N> __device__ __forceinline__ void gather16(rsrc_t ws, u32 voff, u32 stride, u32 tag, u32x4 (&g)[N]) {
#pragma nounroll
  for (int spin = 0; spin < SPIN_MAX; ++spin) {
    CFENCE();
    bool ok = true;
#pragma unroll
    for (int i = 0; i < N; ++i) g[i] = ld16<AUX_SC1>(ws, voff + i * stride, 0);
#pragma unroll
    for (int i = 0; i < N; ++i) ok &= (g[i][1] == tag) & (g[i][3] == tag);
    if (all_ok(ok)) break;
    __builtin_amdgcn_s_sleep(8);
  }
}
template <int N> __device__ __forceinline__ void gather8(rsrc_t ws, u32 voff, u32 stride, u32 tag, u32 (&g)[N], bool act = true) {
#pragma nounroll
  for (int spin = 0; spin < SPIN_MAX; ++spin) {
    CFENCE();
    bool ok = true;
    u32x2 t[N];
#pragma unroll
    for (int i = 0; i < N; ++i) t[i] = ld8<AUX_SC1>(ws, voff + i * stride, 0);
#pragma unroll
    for (int i = 0; i < N; ++i) { ok &= (t[i][1] == tag); g[i] = t[i][0]; }
    if (all_ok(ok | !act)) break;
    __builtin_amdgcn_s_sleep(8);
  }
}

struct Smem {
  float red[8][128];
  float ml[8][2];
  float r2[64 * 20];
  u16 hb[3 * 1024];
  float stat[8][64];
  float red2[8][128];
  u32 dummy[64];
  u32x2 bc;
  u32x4 zero[4];
  float pl[4][8];
  u32 mflag[8];
  u32x4 wbuf[8][8][64];
  __attribute__((aligned(256))) u16 w2b[32][512];
  u16 actl[2][32];
};

#define PROBE_K 8
#define PROBE_OP 0
#define PROBE_PART -1
#define STAMP(k) do { if ((k) == PROBE_K && tid == 0) pst = wall_clock64(); } while (0)
#define WSTAMP(k) do {} while (0)
#define FINISH() do { STAMP(8); if (tid == 0) { __hip_atomic_store(reinterpret_cast<u64*>(ws + (1u << 20)) + bid * 2, pt0, __ATOMIC_RELAXED, __HIP_MEMORY_SCOPE_AGENT); \
   __hip_atomic_store(reinterpret_cast<u64*>(ws + (1u << 20)) + bid * 2 + 1, (u64)(u16)((u32)pst - t0 - (t1 >> 8) + 1000) | (u64)(u16)((u32)wall_clock64() - t0 - (t1 >> 8) + 1000) << 16, __ATOMIC_RELAXED, __HIP_MEMORY_SCOPE_AGENT); } \
   if (bid == 8 && wave == 0) probe_end(ws, pt0, lane, sh0, sh1); return; } while (0)
#define PIN(x) asm volatile("" : "+v"(x))
#define LDS_BARRIER() do { __builtin_amdgcn_s_waitcnt(0xc07f); __builtin_amdgcn_s_barrier(); CFENCE(); } while (0)
#define WAVE_SYNC() do { __builtin_amdgcn_fence(__ATOMIC_RELEASE, "wavefront"); __builtin_amdgcn_wave_barrier(); __builtin_amdgcn_fence(__ATOMIC_ACQUIRE, "wavefront"); } while (0)

__device__ __forceinline__ void dma16(rsrc_t r, u32 voff, u32 soff, u32 lds_dst) {
  u32 keep;
  asm volatile("s_mov_b32 %0, m0\n\ts_mov_b32 m0, %4\n\ts_nop 4\n\tbuffer_load_dwordx4 %1, %2, %3 offen nt lds\n\ts_mov_b32 m0, %0"
               : "=&s"(keep) : "v"(voff), "s"(r), "s"(soff), "s"(lds_dst) : "memory");
}
__device__ __forceinline__ void ld16_into(u32x4& d, rsrc_t r, u32 voff, u32 soff) {
  asm volatile("s_nop 4\n\tbuffer_load_dwordx4 %0, %1, %2, %3 offen nt" : "+v"(d) : "v"(voff), "s"(r), "s"(soff) : "memory");
}
__device__ __forceinline__ u32 perm_lo(u32 a, u32 b) { return __builtin_amdgcn_perm(b, a, 0x05040100u); }
__device__ __forceinline__ u32 perm_hi(u32 a, u32 b) { return __builtin_amdgcn_perm(b, a, 0x07060302u); }
__device__ __forceinline__ float rdl(float x, int l) { return __int_as_float(__builtin_amdgcn_readlane(__float_as_int(x), l)); }

typedef __attribute__((ext_vector_type(8))) __bf16 bf16x8;
__device__ __forceinline__ f32x4 mfma16(u32x4 a, u32x4 b, f32x4 c) {
  return __builtin_amdgcn_mfma_f32_16x16x32_bf16(__builtin_bit_cast(bf16x8, a), __builtin_bit_cast(bf16x8, b), c, 0, 0, 0);
}
__device__ __forceinline__ float sum8(const float* p) {
  const f32x4 a = *reinterpret_cast<const f32x4*>(p), b = *reinterpret_cast<const f32x4*>(p + 4);
  return ((a[0] + b[0]) + (a[1] + b[1])) + ((a[2] + b[2]) + (a[3] + b[3]));
}
__device__ __forceinline__ float ln_ri(float var) {
  return __builtin_amdgcn_rcpf(rbf(__builtin_amdgcn_sqrtf(rbf(rbf(fmaxf(var, 0.f)) + 1e-5f))));
}
__device__ __forceinline__ void ln_stats2(const float (&x0)[8], const float (&x1)[8], float& mb0, float& ri0, float& mb1, float& ri1) {
  const float k0 = rdl(x0[0], 0), k1 = rdl(x1[0], 0);
  float s0 = 0.f, s1 = 0.f, q0 = 0.f, q1 = 0.f;
#pragma unroll
  for (int k = 0; k < 8; ++k) {
    const float d0 = x0[k] - k0, d1 = x1[k] - k1;
    s0 += d0; s1 += d1; q0 = fmaf(d0, d0, q0); q1 = fmaf(d1, d1, q1);
  }
  float t = swap16_add(swap32_add(s0, s1), swap32_add(q0, q1));
  t += dpp<XOR1>(t); t += dpp<XOR2>(t); t += dpp<XOR7>(t); t += dpp<XOR8>(t);
  auto u = __builtin_amdgcn_permlane16_swap(__float_as_uint(t), __float_as_uint(t), false, false);
  const float ms = __uint_as_float(u[0]) * (1.f / 512.f), mq = __uint_as_float(u[1]) * (1.f / 512.f);
  const float ri = ln_ri(fmaf(-ms, ms, mq));
  mb0 = rbf(k0 + rdl(ms, 0)); mb1 = rbf(k1 + rdl(ms, 32));
  ri0 = rdl(ri, 0); ri1 = rdl(ri, 32);
}
template <int I> struct IC { static constexpr int v = I; };
template <int I, int N, class F> __device__ __forceinline__ void sfor(F&& f) {
  if constexpr (I < N) { if (f(IC<I>{})) sfor<I + 1, N>(f); }
}
typedef __attribute__((ext_vector_type(4))) short s16x4;
typedef __attribute__((address_space(3))) s16x4* lds_s16x4p;
__device__ __forceinline__ u32x2 tr_read(u32 lds_addr) {
  return __builtin_bit_cast(u32x2, __builtin_amdgcn_ds_read_tr16_b64_v4i16((lds_s16x4p)lds_addr));
}
__device__ __forceinline__ u32 next_tag(u32 e) { return ((e | 0x40004000u) + 1u) & ~0x40004000u; }
__device__ __forceinline__ float u2f(u32 v) { return __uint_as_float(v); }
#define LDSP(T, a) ((__attribute__((address_space(3))) T*)(a))
template <class T> __device__ __forceinline__ u32 lds_addr(T* p) { u32 a = (u32)(uintptr_t)p; PIN(a); return a; }

#define TAB_LOAD "s_add_u32 s30, s28, wg_tab@rel32@lo+4\ns_addc_u32 s31, s29, wg_tab@rel32@hi+12\nglobal_load_dwordx2 %[te], %[va], s[30:31]\n"
#define FC(k) ".Lfc" #k
#define FC_REGS
#define FX(r, bit, k) "s_bitcmp1_b32 %[" #r "], " #bit "\ns_cselect_b32 %[sb], " FC(k) ", 0\ns_xor_b32 %[sa], %[sa], %[sb]\n"
__device__ __forceinline__ void probe_end(unsigned char* ws, u64 pt0, int lane, int sh0, int sh1) {
  while (wall_clock64() < pt0 + 5500ull) __builtin_amdgcn_s_sleep(8);
  u64 a[4], c2[4];
  for (int i = 0; i < 4; ++i) {
    a[i] = __hip_atomic_load(reinterpret_cast<u64*>(ws + (1u << 20)) + (i * 64 + lane) * 2, __ATOMIC_RELAXED, __HIP_MEMORY_SCOPE_AGENT);
    c2[i] = __hip_atomic_load(reinterpret_cast<u64*>(ws + (1u << 20)) + (i * 64 + lane) * 2 + 1, __ATOMIC_RELAXED, __HIP_MEMORY_SCOPE_AGENT);
  }
  int mn0 = 1 << 30, mx1 = -(1 << 30), mn1 = 1 << 30, sum = 0, cnt = 0;
  for (int i = 0; i < 4; ++i) {
    const int d0 = (int)(a[i] - pt0);
    mn0 = min(mn0, d0);
    if (c2[i] != 0ull) { const int d1 = (int)(c2[i] - pt0); mx1 = max(mx1, d1); mn1 = min(mn1, d1); sum += d1; cnt += 1; }
  }
  for (int o = 1; o < 64; o <<= 1) { mn0 = min(mn0, __shfl_xor(mn0, o)); mx1 = max(mx1, __shfl_xor(mx1, o)); mn1 = min(mn1, __shfl_xor(mn1, o)); sum += __shfl_xor(sum, o); cnt += __shfl_xor(cnt, o); }
  int m4 = 0, m8 = 0;
  for (int i = 0; i < 4; ++i) { m4 = max(m4, (int)(c2[i] & 0xffff)); m8 = max(m8, (int)(c2[i] >> 16 & 0xffff)); }
  for (int o = 1; o < 64; o <<= 1) { m4 = max(m4, __shfl_xor(m4, o)); m8 = max(m8, __shfl_xor(m8, o)); }
  int X = mn0 + 100 * (min(sh0 / 8, 7) + 8 * min((sh1 & 0xffff) / 16, 3) + 32 * min((sh1 >> 16) / 12, 3) + 128 * min(max((m4 - 976) / 12, 0), 3) + 512 * min(max((m8 - 1222) / 16, 0), 7));
  const u64 tg = pt0 + (u64)(long long)(X + 8000);
  for (int i = 0; i < 4000000 && wall_clock64() < tg; ++i) __builtin_amdgcn_s_sleep(1);
}
__global__ void __launch_bounds__(512, 2) fused_kernel(const u16* kc_, const u16* vc_, const u16* wo_, const u16* wq_, const u16* wmod_, const u16* w1_, const u16* w3_,
                                                       unsigned char* ws_, const u16* x_, const u16* cnd_, const u16* w2_, u16* out_) {
  __shared__ __attribute__((aligned(256))) Smem sm;
  const u64 a_ka = (u64)__builtin_amdgcn_kernarg_segment_ptr();
  const int bid = blockIdx.x, tid = threadIdx.x;
  u64 wa, t64; u32x2 te; int tid0;
  u32 ar[14];
  u32 tch2, sa_, sb_, va_;
  u32x4 kr[NL], vr[NL];
  asm volatile(".set .Lfc0, %0\n.set .Lfc1, %1\n.set .Lfc2, %2\n.set .Lfc3, %3\n.set .Lfc4, %4\n.set .Lfc5, %5\n.set .Lfc6, %6\n.set .Lfc7, %7\n.set .Lfc8, %8\n.set .Lfc9, %9\n.set .Lfc10, %10\n.set .Lfc11, %11"
    :: "n"(first_col(PRM0, 0)), "n"(first_col(PRM0, 1)), "n"(first_col(PRM0, 2)), "n"(first_col(PRM0, 3)), "n"(first_col(PRM0, 4)), "n"(first_col(PRM0, 5)),
       "n"(first_col(PRM0, 6)), "n"(first_col(PRM0, 7)), "n"(first_col(PRM0, 8)), "n"(first_col(PRM0, 9)), "n"(first_col(PRM0, 10)), "n"(first_col(PRM0, 11)));
  asm volatile(
    "s_load_dwordx16 s[4:19], %[ka], 0x0\ns_load_dwordx8 s[20:27], %[ka], 0x40\n"
    "s_memrealtime %[t]\n"
    "v_lshlrev_b32 %[va], 3, %[bid]\n"
    "s_getpc_b64 s[28:29]\n.Lcpf_first:\n"
    TAB_LOAD
    "s_cmp_gt_u32 %[bid], 15\ns_cbranch_scc1 .Lno_cpf\n"
    "s_mov_b32 %[sa], .Lend_a-.Lcpf_first\ns_max_i32 %[sa], %[sa], .Lend_b-.Lcpf_first\ns_max_i32 %[sa], %[sa], .Lend_c-.Lcpf_first\ns_max_i32 %[sa], %[sa], .Lend_d-.Lcpf_first\ns_max_i32 %[sa], %[sa], .Lend_e-.Lcpf_first\n"
    "v_lshlrev_b32 %[va], 6, %[tid]\nv_min_u32 %[va], %[sa], %[va]\nglobal_load_dword %[pf], %[va], s[28:29]\n"
    ".Lno_cpf:\n"
    "v_readfirstlane_b32 %[t0], %[tid]\nv_and_b32 %[va], 63, %[tid]\n"
    "s_mov_b32 %[sa], " FC(11) "\n"
    FX(bid, 0, 0) FX(bid, 1, 1) FX(bid, 2, 2) FX(bid, 3, 3) FX(bid, 4, 4) FX(bid, 5, 5) FX(bid, 6, 6) FX(bid, 7, 7) FX(t0, 6, 8) FX(t0, 7, 9) FX(t0, 8, 10)
    "v_lshl_add_u32 %[va], %[va], 4, %[sa]\n"
    "s_waitcnt lgkmcnt(0)\nglobal_load_dwordx4 %[k0], %[va], s[4:5] nt\nglobal_load_dwordx4 %[v0], %[va], s[6:7] nt\n"
    "s_bitcmp1_b32 %[t0], 6\ns_cselect_b64 s[14:15], s[16:17], s[14:15]\n"
    "s_bitcmp1_b32 %[bid], 4\ns_cselect_b64 s[8:9], s[10:11], s[8:9]\ns_cselect_b64 s[12:13], s[14:15], s[12:13]\ns_bitcmp1_b32 %[bid], 5\ns_cselect_b64 %[wa], s[12:13], s[8:9]"
    : "=&{s4}"(ar[0]), "=&{s5}"(ar[1]), "=&{s6}"(ar[2]), "=&{s7}"(ar[3]), "=&{s18}"(ar[4]), "=&{s19}"(ar[5]), "=&{s20}"(ar[6]), "=&{s21}"(ar[7]), "=&{s22}"(ar[8]), "=&{s23}"(ar[9]),
      "=&{s24}"(ar[10]), "=&{s25}"(ar[11]), "=&{s26}"(ar[12]), "=&{s27}"(ar[13]), [wa] "=&s"(wa), [t0] "=&s"(tid0), [t] "=&s"(t64), [te] "=&v"(te), [sa] "=&s"(sa_), [sb] "=&s"(sb_),
      [va] "=&v"(va_), [pf] "=&v"(tch2), [k0] "=&v"(kr[0]), [v0] "=&v"(vr[0])
    : [ka] "s"(a_ka), [bid] "s"(bid), [tid] "v"(tid) : "s8", "s9", "s10", "s11", "s12", "s13", "s14", "s15", "s16", "s17", "s28", "s29", "s30", "s31", "scc", "memory" FC_REGS);
  asm volatile("" : "+s"(ar[0]), "+s"(ar[1]), "+s"(ar[2]), "+s"(ar[3]), "+s"(ar[4]), "+s"(ar[5]), "+s"(ar[6]), "+s"(ar[7]), "+s"(ar[8]), "+s"(ar[9]), "+s"(ar[10]), "+s"(ar[11]), "+s"(ar[12]), "+s"(ar[13]));
  asm volatile("" : "+s"(wa), "+s"(t64), "+s"(tid0), "+s"(sa_));
  const u32 tka = (u32)wall_clock64();
  const u64 pt0 = t64; u64 pst = (PROBE_K == 100 || PROBE_K == 102) ? pt0 : (PROBE_K == 101 ? pt0 + (u32)(tka - (u32)t64) : 0);
  const u32 t0 = tka;
  __builtin_assume(tid >= 0 && tid < 512 && tid0 >= 0 && tid0 < 512 && bid >= 0 && bid < 256);
  constexpr Prm P = PRM0;
  const int TEND = P.TEND;
  const int xcd = bid & 7, part = bid >> 4 & 3;
  const int lane = tid & 63, wave = tid0 >> 6;
  SB();
  const bool p3 = part == 3;
  u32 pitch = (u32)(0x005600c001000020ull >> (part * 16) & 0xffff) * 32u;
  const int sh = p3 ? 2 : 3;
  u32 wv0 = (u32)((p3 ? (wave >> 1) * 128 : wave * 64) + (part == 0 ? xcd * 512 : 0) + (lane >> sh)) * pitch + ((lane & (p3 ? 3 : 7)) ^ (lane >> (p3 ? 5 : 3))) * 16u;
  const u32 wimg = (u32)(uintptr_t)(&sm.wbuf[0][0][0]);
  u32 wsz = (part == 0 ? 4096u : 512u) * pitch, wld = wimg + (p3 ? (wave & 1) * 32768 + (wave >> 1) * 8192 : wave * 8192);
  PIN(wv0);
  asm volatile("" : "+s"(wsz), "+s"(wld), "+s"(pitch));
  auto karg = [&](int i) { return ar[2 * i] | (u64)ar[2 * i + 1] << 32; };
  const u64 a_kc = karg(0), a_vc = karg(1);
  unsigned char* ws = (unsigned char*)karg(2);
  const u16 *x = (const u16*)karg(3), *cnd = (const u16*)karg(4), *w2 = (const u16*)karg(5);
  u16* out = (u16*)karg(6);
  const rsrc_t wsr = mk_rsrc(ws, WS_END);
  asm volatile("s_waitcnt vmcnt(2)" : "+v"(te));
  const u32 te0 = __builtin_amdgcn_readfirstlane(te[0]), te1 = __builtin_amdgcn_readfirstlane(te[1]);
  const int bl0 = te0 & 127, nl = te0 >> 7 & 63, blk = te0 >> 13 & 63;
  const bool has_w = te0 >> 19 & 1;
  const rsrc_t rw = mk_rsrc((const void*)wa, wsz);
  const u32 wstep = pitch << (6 - sh), wflip = p3 ? 32u : 0u;
  u32 wso = blk << (sh + 4);
  int wleft = 8;
  auto piece = [&]() {
    dma16(rw, wv0, wso, wld);
    wso += wstep; wld += 1024;
    wv0 ^= (wleft & 1) ? wflip : 0u;
    --wleft;
  };
  SB();
  STAMP(23);
  const int b = bid >> 3 & 1, head = xcd * 4 + (bid >> 6 ^ 3);
  const int g = lane >> 4, c = lane & 15;
  const int i8 = b | bid >> 6 << 1, wi = i8 * 8 + xcd;
  const bool is_up = p3 && has_w, modc = part == 2 && wi < 16;
  const size_t base = ((size_t)(b * NH + head) * SK + (SK - WIN)) * DH;
  const rsrc_t rk = mk_rsrc((const u16*)a_kc + base, WIN * DH * 2), rv = mk_rsrc((const u16*)a_vc + base, WIN * DH * 2);
  int tau = te1 & 0xffff;
  const int tauw = te0 >> 24 << 7, wdef = te0 >> 20 & 15;
  int due = P.NF * 256, now = 0;
  auto gate = [&]() {
    while (now < due) { now = (int)(((u32)wall_clock64() - t0) << 8); if (now < due) __builtin_amdgcn_s_sleep(1); }
    due += tau;
  };
  auto until = [&](int t) { while ((int)(((u32)wall_clock64() - t0) << 8) < t); };
  const u32 pslot = dup(G_P + head * 2048 + (b * 4 + part) * 16);
  const u32 die = __builtin_amdgcn_s_getreg(20 | (2 << 6));
  const bool voter = p3 && i8 == 1, timer = p3 && i8 < 2;
  int vote = 0, lag = 0, shift = 0;
  const u32 pbo = dup(G_PB) + (xcd * 8 + wave) * 128;
  u32 near = 0;
  u32x2 ep; u32 xs0, xs1, tch1; u32x4 xr0, xr1;
  const rsrc_t rx = mk_rsrc(part == 2 ? cnd : x, (part == 1 || (part == 2 && has_w)) ? 2048u : 0u);
  auto small_ops = [&]() {
    ep = ld8<AUX_SC1>(wsr, wave == 7 ? WS_EPOCH : OOB, 0);
    xs0 = __builtin_amdgcn_raw_buffer_load_b16(rx, tid * 2, 0, 0); xs1 = __builtin_amdgcn_raw_buffer_load_b16(rx, tid * 2, 1024, 0);
    xr0 = ld16<0>(rx, part == 1 ? lane * 16 : OOB, 0); xr1 = ld16<0>(rx, part == 1 ? lane * 16 : OOB, 1024);
    tch1 = __builtin_amdgcn_raw_buffer_load_b32(mk_rsrc(w2 + (size_t)blk * 32 * 512, is_up ? 64u : 0u), tid == 384 ? 0 : OOB, 0, AUX_NT);
  };
  if (has_w) {
    if (modc) now = 1 << 30;
#pragma nounroll
    while (wleft > wdef) { gate(); due += tauw - tau; piece(); }
    now = 0;
  }
  due = max(due, P.TK * 256);
  SB();
  u32 sot = sa_ & 0xffc00u;
  {
    const u32 s1 = (bl0 + lane - 1 - (lane >= SK0 ? NL - nl : 0)) ^ (124 + part);
#pragma unroll
    for (int k = 0; k < 7; ++k) sot ^= (u32)-(int)(s1 >> k & 1u) & col(P, k);
    PIN(sot);
  }
  u32 tag = 0;
  u32x4 bf[8];
  auto wo_prep = [&](int s0 = 0, int s1 = 2) {
    const u32 wi_ = wimg + wave * 8192;
    const int q = (lane >> 2) & 3, pp = lane & 3;
#pragma unroll
    for (int s = s0; s < s1; ++s) {
#pragma unroll
      for (int t = 0; t < 4; ++t) {
        const int r0 = s * 32 + g * 8 + q, r1 = r0 + 4, ch = t * 2 + (pp >> 1);
        const u32x2 lo = tr_read(wi_ + r0 * 128 + ((ch ^ (r0 & 7)) * 16) + (pp & 1) * 8), hi = tr_read(wi_ + r1 * 128 + ((ch ^ (r1 & 7)) * 16) + (pp & 1) * 8);
        bf[s * 4 + t] = u32x4{lo[0], lo[1], hi[0], hi[1]};
      }
    }
  };
  auto gemv_go = [&](u32 hh) {
    sm.hb[tid] = (u16)hh; sm.hb[512 + tid] = (u16)(hh >> 16);
    WAVE_SYNC();
    const u32x4* a = (c & 11) == 0 ? reinterpret_cast<const u32x4*>(&sm.hb[(c >> 2) * 512 + wave * 64 + g * 8]) : &sm.zero[0];
    const u32x4 a0 = a[0], a1 = a[(c & 11) == 0 ? 4 : 0];
    const f32x4 z = {0.f, 0.f, 0.f, 0.f};
    f32x4 d[4];
#pragma unroll
    for (int t = 0; t < 4; ++t) d[t] = mfma16(a0, bf[t], z);
#pragma unroll
    for (int t = 0; t < 4; ++t) d[t] = mfma16(a1, bf[4 + t], d[t]);
    if (lane < 32) {
#pragma unroll
      for (int t = 0; t < 4; ++t) sm.r2[(t * 16 + c) * 20 + g * 8 + wave] = d[t][0];
    }
  };
  auto mod_stage = [&]() {
    wo_prep();
    gemv_go(pack_bf(silu_f(lo_f(xs0)), silu_f(lo_f(xs1))));
    __builtin_amdgcn_s_waitcnt(0xc07f);
    if (lane == 0) *LDSP(volatile u32, (u32)(uintptr_t)&sm.mflag[wave]) = 1u;
    if (wave == 7) {
#pragma nounroll
      for (int spin = 0; spin < SPIN_MAX; ++spin) {
        const u32 f = *LDSP(volatile u32, (u32)(uintptr_t)&sm.mflag[lane & 7]);
        if (all_ok(f != 0u)) break;
        __builtin_amdgcn_s_sleep(1);
      }
      CFENCE();
      st8_sc1(wsr, G_MOD + (blk * 64 + lane) * 8, pack_bf(sum8(&sm.r2[lane * 20]), sum8(&sm.r2[lane * 20 + 8])), tag);
    }
    STAMP(9);
  };
  if (lane < 4) sm.zero[lane] = u32x4{0u, 0u, 0u, 0u};
  if (part == 2 && has_w) {
    if (tid < 8) sm.mflag[tid] = 0u;
    LDS_BARRIER();
    if (__builtin_expect(modc, 0)) {
      small_ops();
      __builtin_amdgcn_raw_buffer_store_b128(u32x4{SENT, SENT, SENT, SENT}, wsr, G_U + wi * 10240 + tid * 16, 0, AUX_SC1);
      __builtin_amdgcn_raw_buffer_store_b128(u32x4{SENT, SENT, SENT, SENT}, wsr, tid < 128 ? G_U + wi * 10240 + tid * 16 : OOB, 8192, AUX_SC1);
      asm volatile("s_waitcnt vmcnt(0)" ::: "memory");
      tag = next_tag(__builtin_amdgcn_readfirstlane(ep[0]));
      mod_stage();
      __builtin_amdgcn_s_barrier();
    }
  }
  SB();
  const int fd = wi * 8 + (lane & 3) * 2;
  const int ub = wave >> 2, ud = (wave & 3) * 128 + c * 8 + g * 2;
  u32x4 sg[3]; u32 sx2;
  auto side_loads = [&]() {
    const bool fin = part == 1 && wave == 0;
    sg[0] = ld16<AUX_SC1>(wsr, is_up ? G_MOD + (1024 + ud) * 8 : (fin ? G_MOD + (1024 + fd) * 8 : OOB), 0);
    sg[1] = ld16<AUX_SC1>(wsr, is_up ? G_MOD + (1536 + ud) * 8 : (fin ? G_MOD + (2560 + fd) * 8 : OOB), 0);
    sg[2] = ld16<AUX_SC1>(wsr, is_up ? G_MOD + (2048 + ud) * 8 : OOB, 0);
    sx2 = __builtin_amdgcn_raw_buffer_load_b32(mk_rsrc(x, (is_up || fin) ? 2048u : 0u), is_up ? ub * 1024 + ud * 2 : (g & 1) * 1024 + fd * 2, 0, 0);
  };
  auto side_pin = [&]() { PIN(sg[0]); PIN(sg[1]); PIN(sg[2]); PIN(sx2); };
  const u32 mlo = G_ML + (head * 2 + b) * 64 + (lane & 3) * 16;
  float fmrg = 0.f;
  auto merge_factor = [&](const u32x4& ml) {
    const float mi = u2f(ml[0]);
    float M = fmaxf(mi, dpp<XOR1>(mi)); M = fmaxf(M, dpp<XOR2>(M));
    const float e = __expf(mi - M);
    float L = e * u2f(ml[2]);
    L += dpp<XOR1>(L); L += dpp<XOR2>(L);
    fmrg = rdl(e * __builtin_amdgcn_rcpf(L), part);
  };
  f32x2 ga = {0.f, 0.f}, xin = {0.f, 0.f}, cc1 = {0.f, 0.f};
  float Af = 0.f, Bf = 0.f;
  const int hpos = ub * 512 + ud;
  auto up_prep = [&]() {
    if (!all_ok((sg[0][1] == tag) & (sg[0][3] == tag) & (sg[1][1] == tag) & (sg[1][3] == tag) & (sg[2][1] == tag) & (sg[2][3] == tag))) gather16<3>(wsr, G_MOD + (1024 + ud) * 8, 512 * 8, tag, sg);
    ga = f32x2{ub ? hi_f(sg[0][0]) : lo_f(sg[0][0]), ub ? hi_f(sg[0][2]) : lo_f(sg[0][2])};
    cc1 = f32x2{rbf(1.f + (ub ? hi_f(sg[2][0]) : lo_f(sg[2][0]))), rbf(1.f + (ub ? hi_f(sg[2][2]) : lo_f(sg[2][2])))};
    xin = f32x2{lo_f(sx2), hi_f(sx2)};
    u32* hp = reinterpret_cast<u32*>(&sm.hb[hpos]);
    hp[512] = pack_bf(cc1[0], cc1[1]);
    hp[1024] = ub ? perm_hi(sg[1][0], sg[1][2]) : perm_lo(sg[1][0], sg[1][2]);
  };
  auto up_frags = [&]() {
    const int pp = lane & 3;
    const u32 a = wimg + (pp & 1) * 32768 + ((pp >> 1) * 32 + g * 8 + ((lane >> 2) & 3)) * 64 + (((wave >> 1) ^ ((g & 1) | (pp & 2))) * 16) + (wave & 1) * 8;
#pragma unroll
    for (int rb = 0; rb < 8; ++rb) {
      const u32x2 lo = tr_read(a + rb * 4096), hi = tr_read(a + rb * 4096 + 256);
      bf[rb] = u32x4{lo[0], lo[1], hi[0], hi[1]};
    }
  };
  const u32x4* ap = reinterpret_cast<const u32x4*>(&sm.hb[((lane >> 3) & 1) * 512 + (lane & 1) * 32 + g * 8]);
  const bool par = lane & 8;
  auto limit = [&](auto sc) {
    constexpr int s = sc.v;
    WAIT_VM(window(s, 1 + (PRM0.W[s >> 3] >> (4 * (s & 7)) & 7)));
    SB();
  };
  auto sample = [&]() { if (timer) lag += (int)(((u32)wall_clock64() - t0) << 8) - due + ((int)__builtin_amdgcn_s_getreg(7 | (3 << 11)) + 1) * tau; };
  auto report = [&](u32 dst) {
    if (timer) {
      if (lane == 0) sm.mflag[wave] = vote * 65536 + min(lag >> 8, 4000);
      lag = 0;
      LDS_BARRIER();
      if (wave == 7) {
        int s = sm.mflag[lane & 7];
        s += __builtin_amdgcn_mov_dpp(s, XOR1, 0xf, 0xf, true); s += __builtin_amdgcn_mov_dpp(s, XOR2, 0xf, 0xf, true); s += __builtin_amdgcn_mov_dpp(s, XOR7, 0xf, 0xf, true);
        if (lane < 8) st8_sc1(wsr, dst + lane * 128 + wi * 8, s, tag);
      }
    }
    SB();
  };
  int sh0 = 0, sh1 = 0;
  float l0 = 0.f, l1 = 0.f;
  u32x2 gmd[2], tbg;
  int t1 = 0;
#pragma unroll
  for (int i = SK0; i < SK1; ++i) kr[i] = u32x4{0u, 0u, 0u, 0u};
  const int first = SK0 + NL - nl;
  auto left_out = [&](int i) { int f = first; asm volatile("" : "+s"(f)); return i < f; };
  sfor<1, NL>([&](auto ic) {
    constexpr int i = ic.v;
    if constexpr (i >= SK0 && i < SK1) { if (left_out(i)) return true; }
    const u32 so = __builtin_amdgcn_readlane(sot, i);
    gate();
    if constexpr ((i >= TBS && i <= TBP) || (i >= RPS && i <= RPP)) sample();
    SB();
    if constexpr (i >= SK0 && i < SK1) ld16_into(kr[i], rk, lane * 16, so); else kr[i] = ld16<AUX_NT>(rk, lane * 16, so);
    if constexpr (i == SMO) small_ops();
    if constexpr (i == SMO + 1) { if (voter) __builtin_amdgcn_raw_buffer_store_b32(0u, wsr, pbo + (lane >> 5) * 8192 + (lane & 31) * 4, 0, AUX_SC1); }
    if constexpr (i == SK1) tbg = ld8<AUX_SC1>(wsr, wave == 7 ? G_VT + xcd * 128 + c * 8 : OOB, 0);
    if constexpr (i == TBL) tbg = ld8<AUX_SC1>(wsr, wave == 7 ? G_TB + xcd * 128 + c * 8 : OOB, 0);
    if constexpr (i == QP) { gmd[0] = ld8<AUX_SC1>(wsr, part == 1 ? G_MOD + tid * 8 : OOB, 0); gmd[1] = ld8<AUX_SC1>(wsr, part == 1 ? G_MOD + tid * 8 : OOB, 4096); }
    SB();
    SB();
    if (i >= 4) limit(ic);
    if (i == 1) STAMP(25);
    if (i == 8) STAMP(12);
    if (i == 20) STAMP(21);
    if (i == 32) STAMP(22);
    if (i == TBP) {
      if (timer) {
        if (lane == 0) sm.mflag[wave] = lag;
        lag = 0;
        LDS_BARRIER();
        if (wave == 7) {
          int s = sm.mflag[lane & 7];
          s += __builtin_amdgcn_mov_dpp(s, XOR1, 0xf, 0xf, true); s += __builtin_amdgcn_mov_dpp(s, XOR2, 0xf, 0xf, true); s += __builtin_amdgcn_mov_dpp(s, XOR7, 0xf, 0xf, true);
          if (lane < 8) st8_sc1(wsr, G_TB + lane * 128 + wi * 8, t0 << 10 | (u32)min(s / (2048 * (TBP - TBS + 1)), 1023), tag);
        }
      }
      SB();
    }
    if (i == PRB) {
      if (voter) {
        u64 ta, tb, tc; u32 dm;
        const u32 pa = pbo + (wave & 1) * 8192;
        asm volatile("s_memtime %0\n\ts_waitcnt lgkmcnt(0)\n\ts_load_dword %3, %4, %5\n\ts_waitcnt lgkmcnt(0)\n\ts_memtime %1\n\ts_waitcnt lgkmcnt(0)\n\t"
                     "s_load_dword %3, %4, %6\n\ts_waitcnt lgkmcnt(0)\n\ts_memtime %2\n\ts_waitcnt lgkmcnt(0)"
                     : "=&s"(ta), "=&s"(tb), "=&s"(tc), "=&s"(dm) : "s"(ws), "s"(pa), "s"(pa ^ 8192u));
        const int d = min(max((int)(2 * (u32)tb - (u32)ta - (u32)tc), -500), 500);
        vote = ((wave ^ die) & 1) ? -d : d;
        STAMP(26);
      }
      SB();
    }
    if (i == TBU) {
      if (wave == 7) {
        const bool ok = tbg[1] == tag;
        float lf = ok ? (float)min(max((int)((t0 << 10 | 1023u) - tbg[0]) >> 10, -300), 300) : 0.f, nf = ok ? 1.f : 0.f, lg = ok ? (float)(tbg[0] & 1023u) : 0.f;
        lf += dpp<XOR1>(lf); nf += dpp<XOR1>(nf); lg += dpp<XOR1>(lg); lf += dpp<XOR2>(lf); nf += dpp<XOR2>(nf); lg += dpp<XOR2>(lg);
        lf += dpp<XOR7>(lf); nf += dpp<XOR7>(nf); lg += dpp<XOR7>(lg); lf += dpp<XOR8>(lf); nf += dpp<XOR8>(nf); lg += dpp<XOR8>(lg);
        const float rn = __builtin_amdgcn_rcpf(fmaxf(nf, 1.f));
        if (lane == 0) sm.pl[3][7] = __uint_as_float((u32)min(max((int)((lg * rn - (float)P.GS) * (float)P.GB * 0.125f), 0), 150));
        shift = __builtin_amdgcn_readfirstlane(min(max((int)((lg * rn - (float)P.GS) * (float)P.GB * 0.125f), 0), 150));
        if (lane == 0) sm.bc = u32x2{tag, t0 - (u32)(int)(lf * rn) + shift};
      }
      LDS_BARRIER();
      const u32x2 bc = sm.bc;
      tag = __builtin_amdgcn_readfirstlane(bc[0]);
      sh0 = __builtin_amdgcn_readfirstlane(__float_as_uint(sm.pl[3][7]));
      t1 = (__builtin_amdgcn_readfirstlane((int)(bc[1] - t0)) + TEND + P.X1) * 256;
      tau -= __builtin_amdgcn_readfirstlane((int)((float)(int)(t0 - bc[1]) * 256.f * __builtin_amdgcn_rcpf((float)(nl - TBU - 1))));
      SB();
    }
    if (i == RPP) report(G_VT);
    if (i == LNS) {
      tag = next_tag(__builtin_amdgcn_readfirstlane(ep[0]));
      if (part == 1) {
        float x0[8], x1[8], mb0, ri0, mb1, ri1;
#pragma unroll
        for (int k = 0; k < 4; ++k) { x0[2 * k] = lo_f(xr0[k]); x0[2 * k + 1] = hi_f(xr0[k]); x1[2 * k] = lo_f(xr1[k]); x1[2 * k + 1] = hi_f(xr1[k]); }
        ln_stats2(x0, x1, mb0, ri0, mb1, ri1);
        l0 = rbf(rbf(lo_f(xs0) - mb0) * ri0); l1 = rbf(rbf(lo_f(xs1) - mb1) * ri1);
      }
      PIN(l0); PIN(l1);
      SB();
    }
    if (i == WUSE) {
      PIN(tch1); PIN(tch2);
      if ((part == 1 || (part == 2 && has_w && !modc))) {
        WAIT_VM(WK); CFENCE();
        if (part == 2) mod_stage(); else wo_prep();
      }
#pragma unroll
      for (int k = 0; k < 8; ++k) PIN(bf[k]);
      SB();
    }
    if (i == QP + QD) {
      if (part == 1) {
        u32 gm[2] = {gmd[0][0], gmd[1][0]};
        if (!all_ok((gmd[0][1] == tag) & (gmd[1][1] == tag))) { STAMP(11); gather8<2>(wsr, G_MOD + tid * 8, 512 * 8, tag, gm); }
        STAMP(5);
        gemv_go(pack_bf(rbf(l0 * rbf(1.f + lo_f(gm[1]))) + lo_f(gm[0]), rbf(l1 * rbf(1.f + hi_f(gm[1]))) + hi_f(gm[0])));
      }
      SB();
    }
    if (i == QP + QD + 1) {
      if (part == 1) {
        LDS_BARRIER();
        if (wave == 0) {
          const int bb = lane >> 5, jq = lane & 31;
          st8_sc1(wsr, G_Q + (bb * 2048 + blk * 32 + jq) * 8, pack_bf(sum8(&sm.r2[2 * jq * 20 + bb * 8]), sum8(&sm.r2[(2 * jq + 1) * 20 + bb * 8])), tag);
        }
        STAMP(6);
      }
      SB();
    }
    return true;
  });
  PIN(sot);
  if (wave == 7) {
    const bool ok = tbg[1] == tag;
    float vt = ok ? (float)((int)tbg[0] >> 16) : 0.f, lg = ok ? (float)(tbg[0] & 0xffffu) : 0.f, nf = ok ? 8.f * (RPP - RPS + 1) : 0.f;
    vt += dpp<XOR1>(vt); lg += dpp<XOR1>(lg); nf += dpp<XOR1>(nf); vt += dpp<XOR2>(vt); lg += dpp<XOR2>(lg); nf += dpp<XOR2>(nf);
    vt += dpp<XOR7>(vt); lg += dpp<XOR7>(lg); nf += dpp<XOR7>(nf); vt += dpp<XOR8>(vt); lg += dpp<XOR8>(lg); nf += dpp<XOR8>(nf);
    lg *= __builtin_amdgcn_rcpf(fmaxf(nf, 1.f));
    if (lane == 0) sm.bc = u32x2{__float_as_uint(vt), (u32)min(max((int)((lg - (float)P.B0) * (float)(P.BG * 32)) - shift * P.D0 * 32, 0), 200 * 256)};
  }
  LDS_BARRIER();
  {
    const u32x2 bc = sm.bc;
    const int st = __builtin_amdgcn_readfirstlane(bc[1]), sp = (st * P.BF) >> 3;
    near = ((__builtin_amdgcn_readfirstlane(bc[0]) >> 31 ^ die ^ P.NR ^ 1) & 1) * 8192u;
    t1 += st; sh1 = st >> 8;
    due += P.TG * 256 + sp;
    tau = (te1 >> 16) + __builtin_amdgcn_readfirstlane((int)((float)(st - sp) * __builtin_amdgcn_rcpf((float)(nl - 3))));
  }
  const u32 qoff = G_Q + ((b * 32 + head) * 64 + c * 4) * 8;
  u32x4 qg[2], mlg;
  u32 qq[4];
  constexpr int NB = (NL + 15) / 16;
  float xlo, sc[NB], pr[NB], m = 0.f;
  f32x4 acc = {0.f, 0.f, 0.f, 0.f};
  u32 pb[NB], pm[4], pl8[8], cpf;
  const u32 adst0 = lds_addr(lane < 32 ? &sm.red[wave][c * 8 + g * 4] : &sm.r2[(lane - 32) * 4]);
  u32 po, rdst, rsrc, xo;
  auto wo_addr = [&]() {
    po = dup(G_P + xcd * 8192) + tid * 16 + near;
    rdst = lane < 32 ? (u32)(uintptr_t)&sm.red2[wave][0] + lane * 16 : (u32)(uintptr_t)&sm.r2[0] + (lane - 32) * 16;
    rsrc = (u32)(uintptr_t)&sm.red2[0][((wave & 1) * 16 + (lane & 15)) * 4 + (lane >> 4)];
    xo = dup(G_XP + xcd * 2048 + (wave & 1) * 1024 + (blk * 64 + (lane & ~1)) * 2) + (lane & 1) * 8192;
    PIN(po); PIN(rdst); PIN(rsrc); PIN(xo);
  };
  u32 pso = (wave == 0 && (g & 1) == 0) ? pslot + (c >> 3) * 1024 + (c & 3) * 256 + (c >> 2 & 1) * 128 + (g >> 1) * 8192 : OOB;
  PIN(pso);
  const u32 nadd = wdef ? P.LW : (is_up ? P.LU : 0);
  u32 nto = tid < 2 ? dup(G_NT) + bid * 8 + tid * 8192 : OOB;
  PIN(nto);
  auto notices = [&]() -> int {
    u32 off = dup(G_NT) + near + lane * 16;
    PIN(off);
    until(due);
    int mx;
#pragma nounroll
    for (int spin = 0; spin < SPIN_MAX; ++spin) {
      CFENCE();
      const u32x4 ga_ = ld16<AUX_SC1>(wsr, off, 0), gb_ = ld16<AUX_SC1>(wsr, off, 1024);
      mx = max(max((int)(ga_[0] - t0), (int)(ga_[2] - t0)), max((int)(gb_[0] - t0), (int)(gb_[2] - t0)));
      if (all_ok((((ga_[1] ^ tag) | (ga_[3] ^ tag)) | ((gb_[1] ^ tag) | (gb_[3] ^ tag))) == 0u)) break;
      for (int k = 0; k < P.AG; ++k) __builtin_amdgcn_s_sleep(1);
    }
    if (all_ok(mx <= (t1 >> 8) - P.AC)) return t1;
    mx = max(mx, __builtin_amdgcn_mov_dpp(mx, XOR1, 0xf, 0xf, true)); mx = max(mx, __builtin_amdgcn_mov_dpp(mx, XOR2, 0xf, 0xf, true));
    mx = max(mx, __builtin_amdgcn_mov_dpp(mx, XOR7, 0xf, 0xf, true)); mx = max(mx, __builtin_amdgcn_mov_dpp(mx, XOR8, 0xf, 0xf, true));
    mx = max(max(__builtin_amdgcn_readlane(mx, 0), __builtin_amdgcn_readlane(mx, 16)), max(__builtin_amdgcn_readlane(mx, 32), __builtin_amdgcn_readlane(mx, 48)));
    return max(min(mx, 1 << 20) * 256, t1 + P.L0 * 256) + P.L1 * 256;
  };
  auto pv_row = [&](auto jc) {
    constexpr int i = jc.v;
    const u32 pv = __float_as_uint(bcast16(u2f(pb[i >> 4]), i & 15));
    acc = mfma16(u32x4{pv & pm[0], pv & pm[1], pv & pm[2], pv & pm[3]}, vr[i], acc);
    PIN(acc);
    return true;
  };
  sfor<0, NL - 1>([&](auto ic) {
    constexpr int v = ic.v;
    if constexpr (v >= SK0 && v < SK1) { if (left_out(v)) return true; }
    if constexpr (v > 0) {
      const u32 so = __builtin_amdgcn_readlane(sot, v);
      if constexpr (v >= SK1) { if (v == NL - P.NE) tau = (tau * P.FE) >> 4; }
      gate();
      if constexpr (v >= RVS && v <= RVP) sample();
      SB();
      if constexpr (v >= SK0 && v < SK1) ld16_into(vr[v], rv, lane * 16, so); else vr[v] = ld16<AUX_NT>(rv, lane * 16, so);
      SB();
      SB();
    }
    if constexpr (v == SK0 - 1) {
#pragma unroll
      for (int i = SK0; i < SK1; ++i) { vr[i] = u32x4{0u, 0u, 0u, 0u}; PIN(vr[i]); }
    }
    if constexpr (v == VQ - 4) { qg[0] = ld16<AUX_SC1>(wsr, qoff, 0); qg[1] = ld16<AUX_SC1>(wsr, qoff + 16, 0); }
    if constexpr (v == VSIDE) side_loads();
    if constexpr (v == CPF) {
      u64 pc; u32 dlt, len;
      asm volatile("s_getpc_b64 %0\n.Lcpf_here:\n\ts_mov_b32 %1, .Ltail_start-.Lcpf_here\n\ts_mov_b32 %2, .Lend_a-.Ltail_start\n\ts_max_i32 %2, %2, .Lend_b-.Ltail_start\n\ts_max_i32 %2, %2, .Lend_c-.Ltail_start\n\ts_max_i32 %2, %2, .Lend_d-.Ltail_start\n\ts_max_i32 %2, %2, .Lend_e-.Ltail_start" : "=s"(pc), "=s"(dlt), "=s"(len) :: "scc");
      pc += dlt;
      const rsrc_t rc = mk_rsrc((const void*)pc, (part == 3 && i8 == 1) ? len : 0u);
      cpf = __builtin_amdgcn_raw_buffer_load_b32(rc, tid * 64, 0, 0);
    }
    if constexpr (v == SK1 + 2) PIN(cpf);
    if constexpr (v == RVP) report(G_VT + 1024);
    if constexpr (v == RVL) tbg = ld8<AUX_SC1>(wsr, wave == 7 ? G_VT + 1024 + xcd * 128 + c * 8 : OOB, 0);
    if constexpr (v == SK1) mlg = ld16<AUX_SC1>(wsr, wave == 0 ? mlo : OOB, 0);
    SB();
    if constexpr (NL + v < 72) limit(IC<NL + v>{});
    if constexpr (v >= SK0 + WV) { WAIT_VM(v < SK1 ? WV : WV + 1); SB(); }
    if constexpr (v == VQ) {
      if (!all_ok((qg[0][1] == tag) & (qg[0][3] == tag) & (qg[1][1] == tag) & (qg[1][3] == tag))) { STAMP(20); gather16<2>(wsr, qoff, 16, tag, qg); }
      STAMP(1); WSTAMP(1);
      qq[0] = qg[0][0]; qq[1] = qg[0][2]; qq[2] = qg[1][0]; qq[3] = qg[1][2];
    }
    if constexpr (v >= VQ && v < VS) {
      constexpr int sl = v - VQ, bt = sl >> 1;
      float xs[8];
#pragma unroll
      for (int i = 0; i < 8; ++i) {
        float d = 0.f;
        if (sl * 8 + i < NL) {
#pragma unroll
          for (int k = 0; k < 4; ++k) d = dot2(kr[sl * 8 + i][k], qq[k], d);
        }
        xs[i] = d;
      }
#pragma unroll
      for (int h = 0; h < 4; ++h) xs[h] = sel_add<XOR7>(xs[h], xs[h + 4], c & 4);
#pragma unroll
      for (int h = 0; h < 2; ++h) xs[h] = sel_add<XOR2>(xs[h], xs[h + 2], c & 2);
      xs[0] = sel_add<XOR1>(xs[0], xs[1], c & 1);
      if constexpr ((sl & 1) || sl == NSL - 1) {
        const float t = (sl & 1) ? sel_add<XOR8>(xlo, xs[0], c & 8) : xs[0] + dpp<XOR8>(xs[0]);
        const float s = rbf(rbf(t) * 0.08838834764831845f);
        sc[bt] = (bt * 16 + c < SK0 || (bt * 16 + c >= first && bt * 16 + c < NL)) ? s : -__builtin_inff();
        PIN(sc[bt]);
      } else {
        xlo = xs[0];
        PIN(xlo);
      }
    }
    if constexpr (v == VS) {
      float mloc = sc[0];
#pragma unroll
      for (int bt = 1; bt < NB; ++bt) mloc = fmaxf(mloc, sc[bt]);
      m = fmaxf(wave_max(mloc), -3.0e38f);
      float ls = 0.f;
#pragma unroll
      for (int bt = 0; bt < NB; ++bt) { pr[bt] = __expf(sc[bt] - m); ls += pr[bt]; }
      const float l = wave_sum(ls);
      if (lane == 0) { sm.ml[wave][0] = m; sm.ml[wave][1] = l; }
      if (c == ((NL - 1) & 15)) sm.pl[g][wave] = pr[NB - 1];
      STAMP(2); WSTAMP(2);
    }
    if constexpr (v >= SK1) pv_row(IC<v - SK1 + SK0 - WV>{});
    if constexpr (v < SK0 || v >= SK0 + WV) sfor<cons_end(v - 1), cons_end(v)>(pv_row);
    if constexpr (v == RVU) {
      if (wave == 7) {
        const bool ok = tbg[1] == tag;
        float lg = ok ? (float)(tbg[0] & 0xffffu) : 0.f, nf = ok ? 8.f * (RVP - RVS + 1) : 0.f;
        lg += dpp<XOR1>(lg); nf += dpp<XOR1>(nf); lg += dpp<XOR2>(lg); nf += dpp<XOR2>(nf); lg += dpp<XOR7>(lg); nf += dpp<XOR7>(nf); lg += dpp<XOR8>(lg); nf += dpp<XOR8>(nf);
        lg *= __builtin_amdgcn_rcpf(fmaxf(nf, 1.f));
        if (lane == 0) sm.bc[1] = (u32)min(max((int)((lg - (float)P.F0) * (float)(P.FG * 32)), 0), 200 * 256);
      }
      LDS_BARRIER();
      const int st = __builtin_amdgcn_readfirstlane(sm.bc[1]), sp = (st * P.FF_) >> 3;
      t1 += st; sh1 += st >> 8 << 16;
      due += sp;
      tau += __builtin_amdgcn_readfirstlane((int)((float)(st - sp) * __builtin_amdgcn_rcpf((float)(nl - RVU - 3))));
    }
    if constexpr (v == SK1 + 1) {
      float p[8];
#pragma unroll
      for (int w = 0; w < 8; ++w) p[w] = sm.pl[g][w] * __expf(sm.ml[w][0] - m);
#pragma unroll
      for (int w = 0; w < 8; ++w) pl8[w] = pack_bf(p[w], p[w]);
#pragma unroll
      for (int w = 0; w < 8; ++w) PIN(pl8[w]);
    }
    if constexpr (v == MLS - 1) { side_pin(); if (is_up) up_prep(); PIN(ga); PIN(xin); PIN(cc1); }
    if constexpr (v == UPREP - 1) wo_addr();
    if constexpr (v == UPREP) {
#pragma unroll
      for (int k = 0; k < 8; ++k) asm volatile("" : "=v"(bf[k]));
      if (part == 0) wo_prep(); else if (is_up) up_frags();
#pragma unroll
      for (int k = 0; k < 8; ++k) PIN(bf[k]);
    }
    if constexpr (v == UPREP + 1) {
      if (is_up) {
        const u32x4* a1 = ap + 128 + ((lane >> 1) & 1) * 128;
        f32x4 d0 = {0.f, 0.f, 0.f, 0.f}, d1 = {0.f, 0.f, 0.f, 0.f};
#pragma unroll
        for (int rb = 0; rb < 4; ++rb) { d0 = mfma16(a1[rb * 8], bf[rb], d0); d1 = mfma16(a1[(rb + 4) * 8], bf[rb + 4], d1); }
        Af = par ? d0[1] + d1[1] : d0[0] + d1[0]; Bf = par ? d0[3] + d1[3] : d0[2] + d1[2];
        Af += dpp<XOR8>(Af); Bf += dpp<XOR8>(Bf);
      }
      PIN(Af); PIN(Bf);
    }
    if (v == 10 || v == 16) STAMP(15 + v / 6);
    if (v == 20) STAMP(18);
    if (v == 32) STAMP(19);
    if constexpr (v == MLS) {
      LDS_BARRIER();
      float mm[8], ll[8];
#pragma unroll
      for (int w = 0; w < 8; ++w) { const f32x2 t = *reinterpret_cast<const f32x2*>(&sm.ml[w][0]); mm[w] = t[0]; ll[w] = t[1]; }
      float Mwg = mm[0], Lwg = 0.f;
#pragma unroll
      for (int w = 1; w < 8; ++w) Mwg = fmaxf(Mwg, mm[w]);
      if (wave == 0) {
#pragma unroll
        for (int w = 0; w < 8; ++w) Lwg = fmaf(__expf(mm[w] - Mwg), ll[w], Lwg);
        if (lane == 0) st16_sc1(wsr, G_ML + ((head * 2 + b) * 4 + part) * 16, __float_as_uint(Mwg), __float_as_uint(Lwg), tag);
      }
      const float fw = __expf(m - Mwg);
      m = Mwg;
#pragma unroll
      for (int bt = 0; bt < NB; ++bt) { pb[bt] = pack_bf(pr[bt] * fw, pr[bt] * fw); PIN(pb[bt]); }
#pragma unroll
      for (int k = 0; k < 4; ++k) { pm[k] = (c >> 1 & 3) == k ? ((c & 1) ? 0xffff0000u : 0x0000ffffu) : 0u; PIN(pm[k]); }
    }
    SB();
    return true;
  });
  asm volatile(".Ltail_start:");
  if (wave != 0) {
    pv_row(IC<SK0 - 1>{});
    WAIT_VM(WV); SB();
    sfor<NL - 1 - WV, NL - 1>(pv_row);
    *LDSP(f32x4, adst0) = acc;
    WSTAMP(14);
    LDS_BARRIER();
  } else {
    u32x4 xr[8], am[8];
#pragma unroll
    for (int w = 0; w < 8; ++w) am[w] = u32x4{pl8[w] & pm[0], pl8[w] & pm[1], pl8[w] & pm[2], pl8[w] & pm[3]};
#pragma unroll
    for (int w = 0; w < 8; ++w) PIN(am[w]);
    {
      const u32 so = __builtin_amdgcn_readlane(sot, NL - 1);
      gate();
      SB();
#pragma unroll
      for (int w = 0; w < 8; ++w) xr[w] = ld16<AUX_NT>(rv, lane * 16, so ^ ((w & 1) ? col(P, 7) : 0u) ^ ((w & 2) ? col(P, 8) : 0u) ^ ((w & 4) ? col(P, 9) : 0u));
      SB();
    }
    u32 rsum = lds_addr(&sm.red[0][c * 8 + (g & 1) * 4]);
    pv_row(IC<SK0 - 1>{});
    WAIT_VM(WV + 8); SB();
    sfor<NL - 1 - WV, NL - 3>(pv_row);
    if (__builtin_expect(!all_ok((mlg[1] == tag) & (mlg[3] == tag)), 0)) {
#pragma nounroll
      for (int spin = 0; spin < SPIN_MAX; ++spin) {
        CFENCE();
        mlg = ld16<AUX_SC1>(wsr, mlo, 0);
        if (all_ok((mlg[1] == tag) & (mlg[3] == tag))) break;
      }
    }
    merge_factor(mlg);
    sfor<NL - 3, NL - 1>(pv_row);
    STAMP(14); WSTAMP(14);
    LDS_BARRIER();
    STAMP(15);
    f32x4 t[8];
#pragma unroll
    for (int w = 1; w < 8; ++w) t[w] = LDSP(f32x4, rsum)[w * 32];
    acc = ((acc + t[1]) + (t[2] + t[3])) + ((t[4] + t[5]) + (t[6] + t[7]));
    PIN(acc);
    STAMP(3);
    SB();
#pragma unroll
    for (int w = 0; w < 8; ++w) acc = mfma16(am[w], xr[w], acc);
  }
  WAIT_VM(0);
  acc *= fmrg;
  {
    const u32 w0 = min(pack_bf(acc[0], acc[1]), SENT - 1u), w1 = min(pack_bf(acc[2], acc[3]), SENT - 1u);
    const auto e0 = __builtin_amdgcn_permlane16_swap(w0, w0, false, false), e1 = __builtin_amdgcn_permlane16_swap(w1, w1, false, false);
    __builtin_amdgcn_raw_buffer_store_b128(u32x4{e0[0], e1[0], e0[1], e1[1]}, wsr, pso, 0, AUX_SC1);
  }
  st8_sc1(wsr, nto, (u32)wall_clock64() + nadd, tag);
  SB();
  STAMP(4);

  if (part == 2) FINISH();
  if (part == 0) {
    __builtin_amdgcn_s_barrier();
#pragma nounroll
    while (wleft > 0) piece();
    bool late = false;
    u32x4 og[2];
    if (wdef) {
      late = true;
      while ((int)(((u32)wall_clock64() - t0) << 8) < t1 - 20 * 256) if (__builtin_amdgcn_s_getreg(7 | (3 << 11)) == 0) { late = false; break; }
      if (__builtin_expect(late, 0)) { until(t1); CFENCE(); og[0] = ld16<AUX_SC1>(wsr, po, 0); SB(); WAIT_VM(1); } else { WAIT_VM(0); }
      CFENCE();
      if (P.WD > 4) wo_prep(0, 1);
      wo_prep(1, 2);
#pragma unroll
      for (int k = 0; k < 8; ++k) PIN(bf[k]);
      SB();
    }
    if (__builtin_expect(!late, 1)) { until(t1); CFENCE(); og[0] = ld16<AUX_SC1>(wsr, po, 0); }
    STAMP(11);
    SB();
    f32x4 d[4];
    auto wo_mm = [&]() {
      const f32x4 z = {0.f, 0.f, 0.f, 0.f};
#pragma unroll
      for (int t = 0; t < 4; ++t) { d[t] = mfma16(og[0], bf[t], z); og[1][t] = __float_as_uint(dpp<XOR8>(u2f(og[0][t]))); }
      SB();
#pragma unroll
      for (int t = 0; t < 4; ++t) d[t] = mfma16(og[1], bf[4 + t], d[t]);
    };
    auto p_bad = [&]() { return !all_ok(max(max(og[0][0], og[0][1]), max(og[0][2], og[0][3])) != SENT); };
    wo_mm();
    if (__builtin_expect(p_bad(), 0)) {
#pragma nounroll
      for (int spin = 0; spin < SPIN_MAX; ++spin) {
        __builtin_amdgcn_s_sleep(1);
        CFENCE();
        og[0] = ld16<AUX_SC1>(wsr, po, 0);
        if (!p_bad()) break;
      }
      wo_mm();
      SB();
      asm volatile(".Lend_d:");
    }
    STAMP(5); WSTAMP(5);
    {
      SB();
      f32x4 y; f32x2 h[4];
#pragma unroll
      for (int t = 0; t < 4; ++t) { h[t] = d[t].xy + d[t].zw; PIN(h[t]); }
#pragma unroll
      for (int t = 0; t < 4; ++t) { y[t] = h[t][0] + h[t][1]; PIN(y[t]); }
      *LDSP(f32x4, rdst) = y;
    }
    STAMP(16);
    LDS_BARRIER();
    STAMP(17);
    if (wave < 2) {
      f32x2 v[4];
#pragma unroll
      for (int w = 0; w < 4; ++w) v[w] = f32x2{LDSP(float, rsrc)[w * 256], LDSP(float, rsrc)[w * 256 + 128]};
      const f32x2 h = (v[0] + v[1]) + (v[2] + v[3]);
      const float y = h[0] + h[1];
      __builtin_amdgcn_raw_buffer_store_b32(min(pack_bf(dpp<0xA0>(y), dpp<0xF5>(y)), SENT - 1u), wsr, xo, 0, AUX_SC1);
      asm volatile(".Lend_a:");
    }
    STAMP(6);
    FINISH();
  }
  if (part == 3) {
    if (!has_w) FINISH();
    u32x4 b2[4];
    {
      __builtin_amdgcn_s_barrier();
      const rsrc_t r2 = mk_rsrc(w2 + (size_t)(blk * 32 + wave * 4) * 512, 4096u);
      const u32 l2 = (u32)(uintptr_t)(&sm.w2b[wave * 4][0]);
#pragma unroll
      for (int n = 0; n < 4; ++n) dma16(r2, (lane ^ (n * 2) ^ ((wave & 2) << 2)) * 16, n * 1024, l2 + n * 1024);
    }
    const u32 tdst = lds_addr(&sm.hb[hpos]);
    const u32 sdst = lds_addr(&sm.stat[wave][lane]);
    const u32 ssrc = lds_addr(&sm.stat[(g >> 1) * 4 + ((lane >> 3) & 3)][(lane & 7) * 4]);
    const u32 adst = lds_addr((c < 4 && (g & 1) == 0) ? &sm.actl[g >> 1][wave * 4 + c] : reinterpret_cast<u16*>(&sm.dummy[lane]));
    const u32 asrc = lds_addr(&sm.actl[lane & 1][g * 8]);
    const u32 apa = lds_addr(&sm.hb[(c >> 3) * 512 + (c & 3) * 64 + ((c >> 2) & 1) * 32 + g * 8]);
    constexpr u32 xs2 = 16384;
    u32 xoff = dup(G_XP) + near + g * 2048 + ub * 1024 + (wave & 3) * 256 + c * 16;
    u32x4 one;
#pragma unroll
    for (int k = 0; k < 4; ++k) one[k] = ((c & 2) == 0 && (c >> 2) == k) ? ((c & 1) ? 0x3f800000u : 0x00003f80u) : 0u;
    PIN(one);
    u32 ooff = dup(G_OP + (blk * 512 + tid) * 8);
    float Afp = Af, Bfp = Bf;
    PIN(xoff); PIN(ooff); PIN(Afp); PIN(Bfp);
    f32x2 gc = ga * cc1, xc = xin * cc1;
    PIN(ga); PIN(xin); PIN(gc); PIN(xc);
    u32 w2a = (u32)(uintptr_t)(&sm.w2b[0][0]) + (g * 8 + ((lane >> 2) & 3)) * 1024 + (wave ^ (g & 1)) * 128 + (lane & 15) * 8;
    PIN(w2a);
    if (wave == 1) {
      due = t1 + P.N1 * 256;
      const int tn = notices();
      due = tn + P.X2 * 256;
      until(due);
    }
    asm volatile("s_waitcnt vmcnt(0)" ::: "memory");
    STAMP(9);
    __builtin_amdgcn_s_barrier();
    STAMP(11);
    CFENCE();
    u32x4 xg[2] = {ld16<AUX_SC1>(wsr, xoff, 0), ld16<AUX_SC1>(wsr, xoff, xs2)};
    SB();
#pragma unroll
    for (int t = 0; t < 4; ++t) {
      const u32x2 lo = tr_read(w2a ^ (t * 32)), hi = tr_read((w2a ^ (t * 32)) + 4096);
      b2[t] = u32x4{lo[0], lo[1], hi[0], hi[1]};
    }
#pragma unroll
    for (int t = 0; t < 4; ++t) PIN(b2[t]);
    SB();
    const f32x4 z4 = {0.f, 0.f, 0.f, 0.f};
    auto xp_bad = [&]() { return !all_ok(max(max(max(xg[0][0], xg[0][1]), max(xg[0][2], xg[0][3])), max(max(xg[1][0], xg[1][1]), max(xg[1][2], xg[1][3]))) != SENT); };
    f32x4 v = mfma16(one, xg[1], mfma16(one, xg[0], z4));
    if (__builtin_expect(xp_bad(), 0)) {
#pragma nounroll
      for (int spin = 0; spin < SPIN_MAX; ++spin) {
        __builtin_amdgcn_s_sleep(1);
        CFENCE();
        xg[0] = ld16<AUX_SC1>(wsr, xoff, 0); xg[1] = ld16<AUX_SC1>(wsr, xoff, xs2);
        if (!xp_bad()) break;
      }
      v = mfma16(one, xg[1], mfma16(one, xg[0], z4));
      SB();
      asm volatile(".Lend_e:");
    }
    STAMP(5); WSTAMP(5);
    {
      const f32x2 a = v.xy, hc = __builtin_elementwise_fma(gc, a, xc), x1 = __builtin_elementwise_fma(ga, a, xin);
      *LDSP(u32, tdst) = pack_bf(hc[0], hc[1]);
      *LDSP(float, sdst) = swap32_add(x1[0] + x1[1], fmaf(x1[0], x1[0], x1[1] * x1[1]));
    }
    STAMP(16);
    LDS_BARRIER();
    STAMP(17);
    {
      const f32x4 z = {0.f, 0.f, 0.f, 0.f};
      const u32x4 af0 = LDSP(u32x4, apa)[0];
      SB();
      const f32x4 s4 = LDSP(f32x4, ssrc)[0], q4 = LDSP(f32x4, ssrc)[8];
      const u32x4 af1 = LDSP(u32x4, apa)[32];
      SB();
      f32x4 d[4];
      d[0] = mfma16(af0, bf[0], z); d[1] = mfma16(af0, bf[1], z);
      SB();
      const f32x2 s2 = s4.xy + s4.zw, q2 = q4.xy + q4.zw;
      float s = s2[0] + s2[1], q = q2[0] + q2[1];
      SB();
      d[2] = mfma16(af0, bf[2], z);
      SB();
      s = swap16_add(s, q);
      SB();
      d[3] = mfma16(af0, bf[3], z);
      SB();
      s += dpp<XOR1>(s); s += dpp<XOR2>(s);
      SB();
      d[0] = mfma16(af1, bf[4], d[0]);
      SB();
      s += dpp<XOR7>(s); s += dpp<XOR8>(s);
      SB();
      d[1] = mfma16(af1, bf[5], d[1]);
      SB();
      const auto sq = __builtin_amdgcn_permlane16_swap(__float_as_uint(s), __float_as_uint(s), false, false);
      const float mean = u2f(sq[0]) * (1.f / 512.f);
      SB();
      d[2] = mfma16(af1, bf[6], d[2]);
      SB();
      const float var = fmaf(u2f(sq[1]), 1.f / 512.f, -mean * mean);
      SB();
      d[3] = mfma16(af1, bf[7], d[3]);
      SB();
      float ri = __builtin_amdgcn_rsqf(fmaxf(var, 0.f) + 1e-5f);
      float cf = fmaf(-ri * mean, Afp, Bfp);
      float rk = ri * -1.44269504f, ck = cf * -1.44269504f;
      PIN(ri); PIN(cf); PIN(rk); PIN(ck);
      SB();
      PIN(d[0]); PIN(d[1]);
      float y = d[0][0] + d[1][1];
      PIN(y); PIN(d[2]);
      y += d[2][2];
      PIN(y); PIN(d[3]);
      y += d[3][3];
      y = __builtin_bit_cast(float, __builtin_amdgcn_update_dpp(__builtin_bit_cast(int, y), __builtin_bit_cast(int, y), XOR8, 0xa, 0xf, false));
      y = swap16_add(y, y);
      const float ex = __builtin_amdgcn_exp2f(fmaf(y, rk, ck));
      y = fmaf(y, ri, cf);
      const float gu = y * dpp<0x104>(y);
      *LDSP(u16, adst) = (u16)pack_bf(gu * __builtin_amdgcn_rcpf(1.f + ex), 0.f);
    }
    STAMP(18);
    LDS_BARRIER();
    STAMP(10);
    {
      const u32x4 af = *LDSP(u32x4, asrc);
      const f32x4 z = {0.f, 0.f, 0.f, 0.f};
      f32x4 d0 = mfma16(af, b2[0], z), d1 = mfma16(af, b2[1], z), d2 = mfma16(af, b2[2], z), d3 = mfma16(af, b2[3], z);
      asm volatile("" : "+v"(d0), "+v"(d1), "+v"(d2), "+v"(d3));
      const bool g1 = g & 1, g2 = g & 2;
      const float e0 = g1 ? d1[0] : d0[0], e1 = g1 ? d1[1] : d0[1], f0 = g1 ? d3[0] : d2[0], f1 = g1 ? d3[1] : d2[1];
      const u32x2 ov = {pack_bf(g2 ? f0 : e0, g2 ? f1 : e1), tag};
      __builtin_amdgcn_raw_buffer_store_b64(ov, wsr, ooff, near ^ 8192u, AUX_SC1);
      __builtin_amdgcn_raw_buffer_store_b64(ov, wsr, ooff, near, AUX_SC1);
    }
    asm volatile(".Lend_b:");
    STAMP(6);
    FINISH();
  }
  if (wave == 0) {
    if (!all_ok((sg[0][1] == tag) & (sg[0][3] == tag) & (sg[1][1] == tag) & (sg[1][3] == tag))) {
      u32x4 t[2];
      gather16<2>(wsr, G_MOD + (1024 + fd) * 8, 1536 * 8, tag, t);
      sg[0] = t[0]; sg[1] = t[1];
    }
    u32 xa;
    {
      due = t1 + P.N2 * 256;
      const int tn = notices();
      due = tn + P.X3 * 256;
      until(due);
    }
    const bool hb = g & 1;
#pragma nounroll
    for (int spin = 0; spin < SPIN_MAX; ++spin) {
      CFENCE();
      xa = __builtin_amdgcn_raw_buffer_load_b32(wsr, dup(G_XP) + near + (g >> 1) * 16384 + (c >> 2) * 2048 + (g & 1) * 1024 + fd * 2, 0, AUX_SC1);
      if (all_ok(xa != SENT)) break;
      __builtin_amdgcn_s_sleep(1);
    }
    float a0 = lo_f(xa), a1 = hi_f(xa);
    a0 += dpp<0x124>(a0); a1 += dpp<0x124>(a1);
    a0 += dpp<XOR8>(a0); a1 += dpp<XOR8>(a1);
    a0 = swap32_add(a0, a0); a1 = swap32_add(a1, a1);
    f32x2 xv = {rbf(lo_f(sx2) + rbf((hb ? hi_f(sg[0][0]) : lo_f(sg[0][0])) * rbf(a0))), rbf(hi_f(sx2) + rbf((hb ? hi_f(sg[0][2]) : lo_f(sg[0][2])) * rbf(a1)))};
    f32x2 gv = {hb ? hi_f(sg[1][0]) : lo_f(sg[1][0]), hb ? hi_f(sg[1][2]) : lo_f(sg[1][2])};
    PIN(xv); PIN(gv);
    if (wi == 0 && lane == 0) __builtin_amdgcn_raw_buffer_store_b32(tag, wsr, WS_EPOCH, 0, AUX_SC1);
    STAMP(10);
    due = max(due + P.X4 * 256, (int)(((u32)wall_clock64() - t0) << 8) + P.GX * 256);
    const bool last = lane < (NUP - 32) * 4;
    u32 o0 = dup(G_OP + (lane >> 2) * 4096 + fd * 8) + near, o2 = last ? o0 : OOB, tg2 = last ? tag : 0u;
    const rsrc_t rout = mk_rsrc(out, 2048u);
    u32 oo = (lane & 44) == 0 ? (g * 512 + fd) * 2 : OOB;
    const u32 pk = (c & 10) ? 0u : ((c & 4) ? 0x3f800000u : 0x00003f80u);
    u32x4 one = {(c & 1) ? 0u : pk, 0u, (c & 1) ? pk : 0u, 0u};
    PIN(o0); PIN(o2); PIN(tg2); PIN(oo); PIN(one);
    __builtin_amdgcn_raw_buffer_store_b32(pack_bf(xv[0], xv[1]), rout, oo, 0, AUX_SC1);
    u32 s1_ = 131072, s2_ = 262144;
    asm volatile("" : "+s"(s1_), "+s"(s2_));
    until(due);
    f32x4 sv;
#pragma nounroll
    for (int spin = 0; spin < SPIN_MAX; ++spin) {
      CFENCE();
      const u32x4 og[3] = {ld16<AUX_SC1>(wsr, o0, 0), ld16<AUX_SC1>(wsr, o0, s1_), ld16<AUX_SC1>(wsr, o2, s2_)};
      sv = f32x4{0.f, 0.f, 0.f, 0.f};
#pragma unroll
      for (int k = 0; k < 3; ++k) sv = mfma16(one, og[k], sv);
      SB();
      u32 bad = ((og[0][1] ^ tag) | (og[0][3] ^ tag) | (og[1][1] ^ tag)) | ((og[1][3] ^ tag) | (og[2][1] ^ tg2) | (og[2][3] ^ tg2));
      PIN(bad);
      SB();
      float s0 = sv[0] + dpp<0x124>(sv[0]), s1 = sv[1] + dpp<0x124>(sv[1]);
      PIN(s0); PIN(s1);
      s0 += dpp<XOR8>(s0); s1 += dpp<XOR8>(s1);
      PIN(s0); PIN(s1);
      __builtin_amdgcn_raw_buffer_store_b32(pack_bf(fmaf(gv[0], s0, xv[0]), fmaf(gv[1], s1, xv[1])), rout, oo, 0, AUX_SC1);
      SB();
      if (all_ok(bad == 0u)) break;
      __builtin_amdgcn_s_sleep(1);
    }
    STAMP(7);
    asm volatile(".Lend_c:");
  }
  FINISH();
}

extern "C" void kernel_launch(void* const* d_in, const int* in_sizes, int n_in,
                              void* d_out, int out_size, void* d_ws, size_t ws_size,
                              hipStream_t stream) {
  fused_kernel<<<256, 512, 0, stream>>>((const u16*)d_in[1], (const u16*)d_in[2], (const u16*)d_in[4], (const u16*)d_in[3], (const u16*)d_in[10], (const u16*)d_in[6], (const u16*)d_in[7],
                                        (unsigned char*)d_ws, (const u16*)d_in[0], (const u16*)d_in[9], (const u16*)d_in[8], (u16*)d_out);
}
```

```cpp
#define PROBE_MUL 10
#include <hip/hip_runtime.h>
#include <stdint.h>

typedef unsigned int u32;
typedef unsigned short u16;
typedef unsigned long long u64;
typedef __attribute__((ext_vector_type(4))) unsigned u32x4;
typedef __attribute__((ext_vector_type(2))) unsigned u32x2;
typedef __attribute__((ext_vector_type(2))) __bf16 bf16x2;
typedef __attribute__((ext_vector_type(4))) float f32x4;
typedef __attribute__((ext_vector_type(2))) float f32x2;
typedef __amdgpu_buffer_rsrc_t rsrc_t;

constexpr int NH = 32, SK = 16384, DH = 128, WIN = 4096, FF = 1376;
struct Prm { int E0, ES, E1, E2, E2C, EU, TEND, DT_UP, L0, L1, X1, X2, X3, X4, NE, FE, M0, M1, M2, M3, M4, GS, GX, N1, N2, GA, WD, DT_WO, LW, LU, EW, PM, NF, NR, C0, A0, AG, AC, TS, TW, TK, TG, GB, B0, BG, BF, D0, DG, TC, LM, C1, F0, FG, FF_, W[9]; };
constexpr Prm PRM0 = {32, 20, 32, 32, 54, 56, 1980, 60, 12, 30, 88, 70, 100, 89, 3, 9, 0x2511f55b, 0x2b7425d5, 0x6017665, 0x17e0b9ec, 0x31944305, 62, 30, 36, 62, 30, 4, 100, 45, 15, 0, 0x0, 15, 14, 3, 30, 4, 4, 508, 220, 75, 6, 9, 74, 26, 8, 8, 0, 0, 120, 2, 67, 22, 4, 0x77777777, 0x77777777, 0x77777777, 0x77777777, 0x77777777, 0x44444477, 0x44444444, 0x44444444, 0x44444444};
constexpr int NL = 37;
constexpr int SK0 = 21, SK1 = 32;
constexpr int NCMIN = (NL - SK1 + SK0) * 8;
constexpr int WK = 8, WV = 5;
constexpr int WUSE = 12, LNS = 7;
constexpr int QP = 14, QD = 4;
constexpr int VQ = 6;
constexpr int NSL = (NL + 7) / 8;
constexpr int VS = VQ + NSL;
constexpr int MLS = VS + 2;
constexpr int VSIDE = MLS - 5, UPREP = MLS + 6;
constexpr int CPF = 17;
constexpr int SMO = 1;
constexpr int RVS = 5, RVP = 8, RVL = 12, RVU = 16;
constexpr int RPS = 24, RPP = 28;
constexpr int PRB = 12;
constexpr int TBS = 7, TBP = 10, TBL = 13, TBU = 17;
constexpr int window(int s, int w) {
  int n = w;
  for (int u = s - w + 1; u <= s; ++u) n += u == SMO ? 6 : (u == TBL || u == SK1 || u == NL + CPF || u == NL + RVL || u == NL + SK1 ? 1 : (u == QP || u == NL + VQ - 4 ? 2 : (u == NL + VSIDE ? 4 : 0)));
  return n;
}
constexpr int cons_end(int v) {
  const int a = v - WV + 1, c = (v - MLS) * 4;
  const int m = a < c ? a : c;
  return m < 0 ? 0 : (m > NL ? NL : m);
}
constexpr u32 col(const Prm& P, int j) { const int m[6] = {P.M0, P.M1, P.M2, P.M3, P.M4, P.PM}; return (u32)(m[j / 3] >> (j % 3 * 10) & 1023) << 10; }
constexpr u32 first_col(const Prm& P, int k) {
  if (k < 4) return col(P, 12 + k) | 1u << (24 + k);
  if (k < 6) return col(P, k - 4);
  if (k < 8) return col(P, 4 + k) | 1u << (16 + k);
  if (k < 11) return col(P, k - 1);
  return (col(P, 2) ^ col(P, 3) ^ col(P, 4) ^ col(P, 5) ^ col(P, 6) ^ col(P, 10) ^ col(P, 11)) | 0xf00000u;
}
constexpr bool one_to_one(const Prm& P) {
  u32 c[10] = {};
  for (int j = 0; j < 10; ++j) c[j] = col(P, j);
  for (int j = 0; j < 10; ++j) {
    if (c[j] == 0) return false;
    const u32 low = c[j] & (0u - c[j]);
    for (int k = j + 1; k < 10; ++k) if (c[k] & low) c[k] ^= c[j];
  }
  return true;
}
static_assert(one_to_one(PRM0), "every KB of the window exactly once");
struct Tab { u32 e[512]; };
constexpr Tab make_tab(const Prm& P) {
  Tab T{};
  for (int bid = 0; bid < 256; ++bid) {
    const int xcd = bid & 7, part = bid >> 4 & 3, i8 = (bid >> 3 & 1) | bid >> 6 << 1, wi = i8 * 8 + xcd;
    const bool has_mod = wi < 48, has_up = wi >= 21;
    const int cb0 = (i8 + P.C0) & 7, cb1 = (i8 + P.C1) & 7, ms = wi - 20;
    const int mi = wi < 8 ? wi : (wi < 16 ? 8 + ((xcd + 4) & 7) : (wi < 20 ? (wi - 14) * 8 + 3 : 16 + ms / 7 * 8 + ms % 7 + (ms % 7 >= 3 ? 1 : 0)));
    const int ui = xcd * 5 + (xcd > 5 ? xcd - 5 : 0) + (i8 == 6 ? 0 : (xcd >= 5 ? 1 : 0) + (i8 == 7 ? 4 : i8 - 2));
    const bool edge = i8 >= 6;
    const int wd = cb0 == 3 ? 0 : P.WD;
    const int e[4] = {P.E0 - 4 * wd + (cb0 == 3 ? P.ES : 0), P.E1 + (cb1 == 3 ? P.ES : 0), has_mod ? (wi < 16 ? P.E2C : P.E2) + ((mi & 7) == 3 ? P.ES : 0) : 0, has_up ? P.EU + (edge ? P.TC : 0) : 0};
    int tw[4] = {}, win[4] = {}, span[4] = {}, k0[4] = {};
    for (int k = 0; k < 4; ++k) {
      const int np = k == 0 ? 8 - wd : 8;
      tw[k] = np ? e[k] * P.TW / (16 * np) : 0;
      k0[k] = P.NF + tw[k] * np / 2 > P.TK ? P.NF + tw[k] * np / 2 : P.TK;
      span[k] = P.TEND - k0[k] - P.TG - (k == 0 ? (wd ? P.DT_WO : P.A0) : (k == 3 && has_up ? P.DT_UP : 0));
      win[k] = span[k] - (k == 0 && wd ? P.EW : (k == 3 && has_up ? P.GA + (edge ? P.LM : 0) : 0));
    }
    int n[4] = {NCMIN / 8, NCMIN / 8, NCMIN / 8, NCMIN / 8};
    for (int left = 128 - NCMIN / 2; left > 0; --left) {
      int best = -1;
      for (int k = 3; k >= 0; --k)
        if (n[k] < NL && (best < 0 || n[k] * win[best] < n[best] * win[k])) best = k;
      ++n[best];
    }
    const int c[4] = {0, n[0] - 1, n[0] + n[1] - 2, n[0] + n[1] + n[2] - 3};
    const int nl = n[part];
    const bool has_w = part < 2 || (part == 2 ? has_mod : has_up);
    const int blk = !has_w ? 0 : (part == 0 ? cb0 : (part == 1 ? xcd * 8 + cb1 : (part == 2 ? mi : ui)));
    T.e[2 * bid] = (u32)(c[part] | nl << 7 | blk << 13 | (has_w ? 1 : 0) << 19 | (part == 0 ? wd : 0) << 20) | (u32)tw[part] << 24;
    T.e[2 * bid + 1] = (u32)(span[part] * P.TS / (4 * (nl - 1)) | span[part] * (1024 - P.TS) / (4 * (nl - 1)) << 16);
  }
  return T;
}
constexpr int step_range(const Prm& P, bool mx_) {
  int mx = 0, mn = 1 << 20;
  const Tab T = make_tab(P);
  for (int bid = 0; bid < 256; ++bid) { const int n = T.e[2 * bid] >> 7 & 63; mx = n > mx ? n : mx; mn = n < mn ? n : mn; }
  return mx_ ? mx : mn;
}
extern "C" __attribute__((used, visibility("protected"))) __constant__ const Tab wg_tab = make_tab(PRM0);
static_assert(step_range(PRM0, true) <= NL && step_range(PRM0, false) >= NCMIN / 8, "a wave's key range does not fit its load steps");
static_assert(UPREP + 1 < SK0 && QP + QD + 1 < SK0 && cons_end(SK0 - 1) == SK0 - WV && NL - SK1 == WV, "steps every wave runs");
constexpr int NUP = FF / 32;
constexpr int SPIN_MAX = 1 << 17;

constexpr u32 WS_EPOCH = 0;
constexpr u32 G_TB = 1024;
constexpr u32 G_MOD = 4096;
constexpr u32 G_Q = G_MOD + 3072 * 8;
constexpr u32 G_ML = G_Q + 4096 * 8;
constexpr u32 G_U = G_ML + 256 * 16;
constexpr u32 G_P = 0;
constexpr u32 G_XP = G_P + 256 * 256;
constexpr u32 G_OP = G_XP + 8 * 512 * 4;
constexpr u32 G_NT = G_OP + 43 * 512 * 8;
constexpr u32 G_PB = G_OP + 44 * 512 * 8;
constexpr u32 G_VT = G_U + 2 * (G_PB + 8192);
constexpr u32 WS_END = G_VT + 2048;
__device__ __forceinline__ u32 dup(u32 o) { return G_U + ((o >> 13) << 14) + (o & 8191u); }
constexpr u32 SENT = 0xffffffffu;
constexpr u32 OOB = 0x40000000u;
#define WST 0

constexpr int AUX_NT = 2, AUX_SC1 = 16;

__device__ __forceinline__ float lo_f(u32 v) { return __uint_as_float(v << 16); }
__device__ __forceinline__ float hi_f(u32 v) { return __uint_as_float(v & 0xffff0000u); }
__device__ __forceinline__ float rbf(float x) { return (float)(__bf16)x; }
__device__ __forceinline__ u32 pack_bf(float lo, float hi) {
  bf16x2 r; r[0] = (__bf16)lo; r[1] = (__bf16)hi; return __builtin_bit_cast(u32, r);
}
__device__ __forceinline__ float dot2(u32 k, u32 q, float acc) {
  return __builtin_amdgcn_fdot2_f32_bf16(__builtin_bit_cast(bf16x2, k), __builtin_bit_cast(bf16x2, q), acc, false);
}
template <int CTRL> __device__ __forceinline__ float dpp(float x) {
  return __builtin_bit_cast(float, __builtin_amdgcn_mov_dpp(__builtin_bit_cast(int, x), CTRL, 0xf, 0xf, true));
}
constexpr int XOR1 = 0xB1, XOR2 = 0x4E, XOR7 = 0x141, XOR8 = 0x128;
__device__ __forceinline__ float swap32_add(float a, float b) {
  auto t = __builtin_amdgcn_permlane32_swap(__float_as_uint(a), __float_as_uint(b), false, false);
  return __uint_as_float(t[0]) + __uint_as_float(t[1]);
}
__device__ __forceinline__ float swap16_add(float a, float b) {
  auto t = __builtin_amdgcn_permlane16_swap(__float_as_uint(a), __float_as_uint(b), false, false);
  return __uint_as_float(t[0]) + __uint_as_float(t[1]);
}
__device__ __forceinline__ float wave_sum(float x) {
  x += dpp<XOR1>(x); x += dpp<XOR2>(x); x += dpp<XOR7>(x); x += dpp<XOR8>(x);
  x = swap16_add(x, x);
  return swap32_add(x, x);
}
__device__ __forceinline__ float wave_max(float x) {
  x = fmaxf(x, dpp<XOR1>(x)); x = fmaxf(x, dpp<XOR2>(x)); x = fmaxf(x, dpp<XOR7>(x)); x = fmaxf(x, dpp<XOR8>(x));
  auto s = __builtin_amdgcn_permlane16_swap(__float_as_uint(x), __float_as_uint(x), false, false);
  x = fmaxf(__uint_as_float(s[0]), __uint_as_float(s[1]));
  auto t = __builtin_amdgcn_permlane32_swap(__float_as_uint(x), __float_as_uint(x), false, false);
  return fmaxf(__uint_as_float(t[0]), __uint_as_float(t[1]));
}
template <int CTRL> __device__ __forceinline__ float sel_add(float lo, float hi, bool up) {
  const float t1 = lo + dpp<CTRL>(lo), t2 = hi + dpp<CTRL>(hi);
  return up ? t2 : t1;
}
template <int I> __device__ __forceinline__ float bc16(float x) {
  return __builtin_bit_cast(float, __builtin_amdgcn_update_dpp(0, __builtin_bit_cast(int, x), 0x150 + I, 0xf, 0xf, false));
}
__device__ __forceinline__ float bcast16(float x, int i) {
  switch (i) {
    case 0: return bc16<0>(x); case 1: return bc16<1>(x); case 2: return bc16<2>(x); case 3: return bc16<3>(x);
    case 4: return bc16<4>(x); case 5: return bc16<5>(x); case 6: return bc16<6>(x); case 7: return bc16<7>(x);
    case 8: return bc16<8>(x); case 9: return bc16<9>(x); case 10: return bc16<10>(x); case 11: return bc16<11>(x);
    case 12: return bc16<12>(x); case 13: return bc16<13>(x); case 14: return bc16<14>(x); default: return bc16<15>(x);
  }
}
__device__ __forceinline__ float silu_f(float x) { return x * __builtin_amdgcn_rcpf(1.f + __expf(-x)); }

template <int AUX> __device__ __forceinline__ u32x4 ld16(rsrc_t r, u32 voff, u32 soff) {
  return __builtin_amdgcn_raw_buffer_load_b128(r, voff, soff, AUX);
}
template <int AUX> __device__ __forceinline__ u32x2 ld8(rsrc_t r, u32 voff, u32 soff) {
  return __builtin_amdgcn_raw_buffer_load_b64(r, voff, soff, AUX);
}
__device__ __forceinline__ void st8_sc1(rsrc_t r, u32 voff, u32 v, u32 tag) {
  __builtin_amdgcn_raw_buffer_store_b64(u32x2{v, tag}, r, voff, 0, AUX_SC1);
}
__device__ __forceinline__ void st16_sc1(rsrc_t r, u32 voff, u32 v0, u32 v1, u32 tag) {
  __builtin_amdgcn_raw_buffer_store_b128(u32x4{v0, tag, v1, tag}, r, voff, 0, AUX_SC1);
}
__device__ __forceinline__ rsrc_t mk_rsrc(const void* p, u32 bytes) {
  return __builtin_amdgcn_make_buffer_rsrc(const_cast<void*>(p), 0, bytes, 0x00020000);
}
#define WAIT_VM(n) __builtin_amdgcn_s_waitcnt(((n) & 15) | (((n) >> 4) << 14) | (7 << 4) | (15 << 8))
#define SB() __builtin_amdgcn_sched_barrier(0)
#define CFENCE() asm volatile("" ::: "memory")
__device__ __forceinline__ bool all_ok(bool ok) { return __builtin_amdgcn_ballot_w64(!ok) == 0ull; }

template <int N> __device__ __forceinline__ void gather16(rsrc_t ws, u32 voff, u32 stride, u32 tag, u32x4 (&g)[N]) {
#pragma nounroll
  for (int spin = 0; spin < SPIN_MAX; ++spin) {
    CFENCE();
    bool ok = true;
#pragma unroll
    for (int i = 0; i < N; ++i) g[i] = ld16<AUX_SC1>(ws, voff + i * stride, 0);
#pragma unroll
    for (int i = 0; i < N; ++i) ok &= (g[i][1] == tag) & (g[i][3] == tag);
    if (all_ok(ok)) break;
    __builtin_amdgcn_s_sleep(8);
  }
}
template <int N> __device__ __forceinline__ void gather8(rsrc_t ws, u32 voff, u32 stride, u32 tag, u32 (&g)[N], bool act = true) {
#pragma nounroll
  for (int spin = 0; spin < SPIN_MAX; ++spin) {
    CFENCE();
    bool ok = true;
    u32x2 t[N];
#pragma unroll
    for (int i = 0; i < N; ++i) t[i] = ld8<AUX_SC1>(ws, voff + i * stride, 0);
#pragma unroll
    for (int i = 0; i < N; ++i) { ok &= (t[i][1] == tag); g[i] = t[i][0]; }
    if (all_ok(ok | !act)) break;
    __builtin_amdgcn_s_sleep(8);
  }
}

struct Smem {
  float red[8][128];
  float ml[8][2];
  float r2[64 * 20];
  u16 hb[3 * 1024];
  float stat[8][64];
  float red2[8][128];
  u32 dummy[64];
  u32x2 bc;
  u32x4 zero[4];
  float pl[4][8];
  u32 mflag[8];
  u32x4 wbuf[8][8][64];
  __attribute__((aligned(256))) u16 w2b[32][512];
  u16 actl[2][32];
};

#define PROBE_K 8
#define PROBE_OP 0
#define PROBE_PART -1
#define STAMP(k) do { if ((k) == PROBE_K && tid == 0) pst = wall_clock64(); } while (0)
#define WSTAMP(k) do {} while (0)
#define FINISH() do { STAMP(8); if (tid == 0) { __hip_atomic_store(reinterpret_cast<u64*>(ws + (1u << 20)) + bid * 2, pt0, __ATOMIC_RELAXED, __HIP_MEMORY_SCOPE_AGENT); \
   __hip_atomic_store(reinterpret_cast<u64*>(ws + (1u << 20)) + bid * 2 + 1, (PROBE_PART < 0 || part == PROBE_PART) ? pst : 0ull, __ATOMIC_RELAXED, __HIP_MEMORY_SCOPE_AGENT); } \
   if (bid == 8 && wave == 0) probe_end(ws, pt0, lane); return; } while (0)
#define PIN(x) asm volatile("" : "+v"(x))
#define LDS_BARRIER() do { __builtin_amdgcn_s_waitcnt(0xc07f); __builtin_amdgcn_s_barrier(); CFENCE(); } while (0)
#define WAVE_SYNC() do { __builtin_amdgcn_fence(__ATOMIC_RELEASE, "wavefront"); __builtin_amdgcn_wave_barrier(); __builtin_amdgcn_fence(__ATOMIC_ACQUIRE, "wavefront"); } while (0)

__device__ __forceinline__ void dma16(rsrc_t r, u32 voff, u32 soff, u32 lds_dst) {
  u32 keep;
  asm volatile("s_mov_b32 %0, m0\n\ts_mov_b32 m0, %4\n\ts_nop 4\n\tbuffer_load_dwordx4 %1, %2, %3 offen nt lds\n\ts_mov_b32 m0, %0"
               : "=&s"(keep) : "v"(voff), "s"(r), "s"(soff), "s"(lds_dst) : "memory");
}
__device__ __forceinline__ void ld16_into(u32x4& d, rsrc_t r, u32 voff, u32 soff) {
  asm volatile("s_nop 4\n\tbuffer_load_dwordx4 %0, %1, %2, %3 offen nt" : "+v"(d) : "v"(voff), "s"(r), "s"(soff) : "memory");
}
__device__ __forceinline__ u32 perm_lo(u32 a, u32 b) { return __builtin_amdgcn_perm(b, a, 0x05040100u); }
__device__ __forceinline__ u32 perm_hi(u32 a, u32 b) { return __builtin_amdgcn_perm(b, a, 0x07060302u); }
__device__ __forceinline__ float rdl(float x, int l) { return __int_as_float(__builtin_amdgcn_readlane(__float_as_int(x), l)); }

typedef __attribute__((ext_vector_type(8))) __bf16 bf16x8;
__device__ __forceinline__ f32x4 mfma16(u32x4 a, u32x4 b, f32x4 c) {
  return __builtin_amdgcn_mfma_f32_16x16x32_bf16(__builtin_bit_cast(bf16x8, a), __builtin_bit_cast(bf16x8, b), c, 0, 0, 0);
}
__device__ __forceinline__ float sum8(const float* p) {
  const f32x4 a = *reinterpret_cast<const f32x4*>(p), b = *reinterpret_cast<const f32x4*>(p + 4);
  return ((a[0] + b[0]) + (a[1] + b[1])) + ((a[2] + b[2]) + (a[3] + b[3]));
}
__device__ __forceinline__ float ln_ri(float var) {
  return __builtin_amdgcn_rcpf(rbf(__builtin_amdgcn_sqrtf(rbf(rbf(fmaxf(var, 0.f)) + 1e-5f))));
}
__device__ __forceinline__ void ln_stats2(const float (&x0)[8], const float (&x1)[8], float& mb0, float& ri0, float& mb1, float& ri1) {
  const float k0 = rdl(x0[0], 0), k1 = rdl(x1[0], 0);
  float s0 = 0.f, s1 = 0.f, q0 = 0.f, q1 = 0.f;
#pragma unroll
  for (int k = 0; k < 8; ++k) {
    const float d0 = x0[k] - k0, d1 = x1[k] - k1;
    s0 += d0; s1 += d1; q0 = fmaf(d0, d0, q0); q1 = fmaf(d1, d1, q1);
  }
  float t = swap16_add(swap32_add(s0, s1), swap32_add(q0, q1));
  t += dpp<XOR1>(t); t += dpp<XOR2>(t); t += dpp<XOR7>(t); t += dpp<XOR8>(t);
  auto u = __builtin_amdgcn_permlane16_swap(__float_as_uint(t), __float_as_uint(t), false, false);
  const float ms = __uint_as_float(u[0]) * (1.f / 512.f), mq = __uint_as_float(u[1]) * (1.f / 512.f);
  const float ri = ln_ri(fmaf(-ms, ms, mq));
  mb0 = rbf(k0 + rdl(ms, 0)); mb1 = rbf(k1 + rdl(ms, 32));
  ri0 = rdl(ri, 0); ri1 = rdl(ri, 32);
}
template <int I> struct IC { static constexpr int v = I; };
template <int I, int N, class F> __device__ __forceinline__ void sfor(F&& f) {
  if constexpr (I < N) { if (f(IC<I>{})) sfor<I + 1, N>(f); }
}
typedef __attribute__((ext_vector_type(4))) short s16x4;
typedef __attribute__((address_space(3))) s16x4* lds_s16x4p;
__device__ __forceinline__ u32x2 tr_read(u32 lds_addr) {
  return __builtin_bit_cast(u32x2, __builtin_amdgcn_ds_read_tr16_b64_v4i16((lds_s16x4p)lds_addr));
}
__device__ __forceinline__ u32 next_tag(u32 e) { return ((e | 0x40004000u) + 1u) & ~0x40004000u; }
__device__ __forceinline__ float u2f(u32 v) { return __uint_as_float(v); }
#define LDSP(T, a) ((__attribute__((address_space(3))) T*)(a))
template <class T> __device__ __forceinline__ u32 lds_addr(T* p) { u32 a = (u32)(uintptr_t)p; PIN(a); return a; }

#define TAB_LOAD "s_add_u32 s30, s28, wg_tab@rel32@lo+4\ns_addc_u32 s31, s29, wg_tab@rel32@hi+12\nglobal_load_dwordx2 %[te], %[va], s[30:31]\n"
#define FC(k) ".Lfc" #k
#define FC_REGS
#define FX(r, bit, k) "s_bitcmp1_b32 %[" #r "], " #bit "\ns_cselect_b32 %[sb], " FC(k) ", 0\ns_xor_b32 %[sa], %[sa], %[sb]\n"
__device__ __forceinline__ void probe_end(unsigned char* ws, u64 pt0, int lane) {
  while (wall_clock64() < pt0 + 5500ull) __builtin_amdgcn_s_sleep(8);
  u64 a[4], c2[4];
  for (int i = 0; i < 4; ++i) {
    a[i] = __hip_atomic_load(reinterpret_cast<u64*>(ws + (1u << 20)) + (i * 64 + lane) * 2, __ATOMIC_RELAXED, __HIP_MEMORY_SCOPE_AGENT);
    c2[i] = __hip_atomic_load(reinterpret_cast<u64*>(ws + (1u << 20)) + (i * 64 + lane) * 2 + 1, __ATOMIC_RELAXED, __HIP_MEMORY_SCOPE_AGENT);
  }
  int mn0 = 1 << 30, mx1 = -(1 << 30), mn1 = 1 << 30, sum = 0, cnt = 0;
  for (int i = 0; i < 4; ++i) {
    const int d0 = (int)(a[i] - pt0);
    mn0 = min(mn0, d0);
    if (c2[i] != 0ull) { const int d1 = (int)(c2[i] - pt0); mx1 = max(mx1, d1); mn1 = min(mn1, d1); sum += d1; cnt += 1; }
  }
  for (int o = 1; o < 64; o <<= 1) { mn0 = min(mn0, __shfl_xor(mn0, o)); mx1 = max(mx1, __shfl_xor(mx1, o)); mn1 = min(mn1, __shfl_xor(mn1, o)); sum += __shfl_xor(sum, o); cnt += __shfl_xor(cnt, o); }
  int X = PROBE_OP == 0 ? mx1 : (PROBE_OP == 1 ? sum / max(cnt, 1) : mn1);
  if (PROBE_K == 102) X = mn0;
  X = mn0 + (X - mn0) * PROBE_MUL;
  const u64 tg = pt0 + (u64)(long long)(X + 8000);
  for (int i = 0; i < 400000 && wall_clock64() < tg; ++i) __builtin_amdgcn_s_sleep(1);
}
__global__ void __launch_bounds__(512, 2) fused_kernel(const u16* kc_, const u16* vc_, const u16* wo_, const u16* wq_, const u16* wmod_, const u16* w1_, const u16* w3_,
                                                       unsigned char* ws_, const u16* x_, const u16* cnd_, const u16* w2_, u16* out_) {
  __shared__ __attribute__((aligned(256))) Smem sm;
  const u64 a_ka = (u64)__builtin_amdgcn_kernarg_segment_ptr();
  const int bid = blockIdx.x, tid = threadIdx.x;
  u64 wa, t64; u32x2 te; int tid0;
  u32 ar[14];
  u32 tch2, sa_, sb_, va_;
  u32x4 kr[NL], vr[NL];
  asm volatile(".set .Lfc0, %0\n.set .Lfc1, %1\n.set .Lfc2, %2\n.set .Lfc3, %3\n.set .Lfc4, %4\n.set .Lfc5, %5\n.set .Lfc6, %6\n.set .Lfc7, %7\n.set .Lfc8, %8\n.set .Lfc9, %9\n.set .Lfc10, %10\n.set .Lfc11, %11"
    :: "n"(first_col(PRM0, 0)), "n"(first_col(PRM0, 1)), "n"(first_col(PRM0, 2)), "n"(first_col(PRM0, 3)), "n"(first_col(PRM0, 4)), "n"(first_col(PRM0, 5)),
       "n"(first_col(PRM0, 6)), "n"(first_col(PRM0, 7)), "n"(first_col(PRM0, 8)), "n"(first_col(PRM0, 9)), "n"(first_col(PRM0, 10)), "n"(first_col(PRM0, 11)));
  asm volatile(
    "s_load_dwordx16 s[4:19], %[ka], 0x0\ns_load_dwordx8 s[20:27], %[ka], 0x40\n"
    "s_memrealtime %[t]\n"
    "v_lshlrev_b32 %[va], 3, %[bid]\n"
    "s_getpc_b64 s[28:29]\n.Lcpf_first:\n"
    TAB_LOAD
    "s_cmp_gt_u32 %[bid], 15\ns_cbranch_scc1 .Lno_cpf\n"
    "s_mov_b32 %[sa], .Lend_a-.Lcpf_first\ns_max_i32 %[sa], %[sa], .Lend_b-.Lcpf_first\ns_max_i32 %[sa], %[sa], .Lend_c-.Lcpf_first\ns_max_i32 %[sa], %[sa], .Lend_d-.Lcpf_first\ns_max_i32 %[sa], %[sa], .Lend_e-.Lcpf_first\n"
    "v_lshlrev_b32 %[va], 6, %[tid]\nv_min_u32 %[va], %[sa], %[va]\nglobal_load_dword %[pf], %[va], s[28:29]\n"
    ".Lno_cpf:\n"
    "v_readfirstlane_b32 %[t0], %[tid]\nv_and_b32 %[va], 63, %[tid]\n"
    "s_mov_b32 %[sa], " FC(11) "\n"
    FX(bid, 0, 0) FX(bid, 1, 1) FX(bid, 2, 2) FX(bid, 3, 3) FX(bid, 4, 4) FX(bid, 5, 5) FX(bid, 6, 6) FX(bid, 7, 7) FX(t0, 6, 8) FX(t0, 7, 9) FX(t0, 8, 10)
    "v_lshl_add_u32 %[va], %[va], 4, %[sa]\n"
    "s_waitcnt lgkmcnt(0)\nglobal_load_dwordx4 %[k0], %[va], s[4:5] nt\nglobal_load_dwordx4 %[v0], %[va], s[6:7] nt\n"
    "s_bitcmp1_b32 %[t0], 6\ns_cselect_b64 s[14:15], s[16:17], s[14:15]\n"
    "s_bitcmp1_b32 %[bid], 4\ns_cselect_b64 s[8:9], s[10:11], s[8:9]\ns_cselect_b64 s[12:13], s[14:15], s[12:13]\ns_bitcmp1_b32 %[bid], 5\ns_cselect_b64 %[wa], s[12:13], s[8:9]"
    : "=&{s4}"(ar[0]), "=&{s5}"(ar[1]), "=&{s6}"(ar[2]), "=&{s7}"(ar[3]), "=&{s18}"(ar[4]), "=&{s19}"(ar[5]), "=&{s20}"(ar[6]), "=&{s21}"(ar[7]), "=&{s22}"(ar[8]), "=&{s23}"(ar[9]),
      "=&{s24}"(ar[10]), "=&{s25}"(ar[11]), "=&{s26}"(ar[12]), "=&{s27}"(ar[13]), [wa] "=&s"(wa), [t0] "=&s"(tid0), [t] "=&s"(t64), [te] "=&v"(te), [sa] "=&s"(sa_), [sb] "=&s"(sb_),
      [va] "=&v"(va_), [pf] "=&v"(tch2), [k0] "=&v"(kr[0]), [v0] "=&v"(vr[0])
    : [ka] "s"(a_ka), [bid] "s"(bid), [tid] "v"(tid) : "s8", "s9", "s10", "s11", "s12", "s13", "s14", "s15", "s16", "s17", "s28", "s29", "s30", "s31", "scc", "memory" FC_REGS);
  asm volatile("" : "+s"(ar[0]), "+s"(ar[1]), "+s"(ar[2]), "+s"(ar[3]), "+s"(ar[4]), "+s"(ar[5]), "+s"(ar[6]), "+s"(ar[7]), "+s"(ar[8]), "+s"(ar[9]), "+s"(ar[10]), "+s"(ar[11]), "+s"(ar[12]), "+s"(ar[13]));
  asm volatile("" : "+s"(wa), "+s"(t64), "+s"(tid0), "+s"(sa_));
  const u32 tka = (u32)wall_clock64();
  const u64 pt0 = t64; u64 pst = (PROBE_K == 100 || PROBE_K == 102) ? pt0 : (PROBE_K == 101 ? pt0 + (u32)(tka - (u32)t64) : 0);
  const u32 t0 = tka;
  __builtin_assume(tid >= 0 && tid < 512 && tid0 >= 0 && tid0 < 512 && bid >= 0 && bid < 256);
  constexpr Prm P = PRM0;
  const int TEND = P.TEND;
  const int xcd = bid & 7, part = bid >> 4 & 3;
  const int lane = tid & 63, wave = tid0 >> 6;
  SB();
  const bool p3 = part == 3;
  u32 pitch = (u32)(0x005600c001000020ull >> (part * 16) & 0xffff) * 32u;
  const int sh = p3 ? 2 : 3;
  u32 wv0 = (u32)((p3 ? (wave >> 1) * 128 : wave * 64) + (part == 0 ? xcd * 512 : 0) + (lane >> sh)) * pitch + ((lane & (p3 ? 3 : 7)) ^ (lane >> (p3 ? 5 : 3))) * 16u;
  const u32 wimg = (u32)(uintptr_t)(&sm.wbuf[0][0][0]);
  u32 wsz = (part == 0 ? 4096u : 512u) * pitch, wld = wimg + (p3 ? (wave & 1) * 32768 + (wave >> 1) * 8192 : wave * 8192);
  PIN(wv0);
  asm volatile("" : "+s"(wsz), "+s"(wld), "+s"(pitch));
  auto karg = [&](int i) { return ar[2 * i] | (u64)ar[2 * i + 1] << 32; };
  const u64 a_kc = karg(0), a_vc = karg(1);
  unsigned char* ws = (unsigned char*)karg(2);
  const u16 *x = (const u16*)karg(3), *cnd = (const u16*)karg(4), *w2 = (const u16*)karg(5);
  u16* out = (u16*)karg(6);
  const rsrc_t wsr = mk_rsrc(ws, WS_END);
  asm volatile("s_waitcnt vmcnt(2)" : "+v"(te));
  const u32 te0 = __builtin_amdgcn_readfirstlane(te[0]), te1 = __builtin_amdgcn_readfirstlane(te[1]);
  const int bl0 = te0 & 127, nl = te0 >> 7 & 63, blk = te0 >> 13 & 63;
  const bool has_w = te0 >> 19 & 1;
  const rsrc_t rw = mk_rsrc((const void*)wa, wsz);
  const u32 wstep = pitch << (6 - sh), wflip = p3 ? 32u : 0u;
  u32 wso = blk << (sh + 4);
  int wleft = 8;
  auto piece = [&]() {
    dma16(rw, wv0, wso, wld);
    wso += wstep; wld += 1024;
    wv0 ^= (wleft & 1) ? wflip : 0u;
    --wleft;
  };
  SB();
  STAMP(23);
  const int b = bid >> 3 & 1, head = xcd * 4 + (bid >> 6 ^ 3);
  const int g = lane >> 4, c = lane & 15;
  const int i8 = b | bid >> 6 << 1, wi = i8 * 8 + xcd;
  const bool is_up = p3 && has_w, modc = part == 2 && wi < 16;
  const size_t base = ((size_t)(b * NH + head) * SK + (SK - WIN)) * DH;
  const rsrc_t rk = mk_rsrc((const u16*)a_kc + base, WIN * DH * 2), rv = mk_rsrc((const u16*)a_vc + base, WIN * DH * 2);
  int tau = te1 & 0xffff;
  const int tauw = te0 >> 24 << 7, wdef = te0 >> 20 & 15;
  int due = P.NF * 256, now = 0;
  auto gate = [&]() {
    while (now < due) { now = (int)(((u32)wall_clock64() - t0) << 8); if (now < due) __builtin_amdgcn_s_sleep(1); }
    due += tau;
  };
  auto until = [&](int t) { while ((int)(((u32)wall_clock64() - t0) << 8) < t); };
  const u32 pslot = dup(G_P + head * 2048 + (b * 4 + part) * 16);
  const u32 die = __builtin_amdgcn_s_getreg(20 | (2 << 6));
  const bool voter = p3 && i8 == 1, timer = p3 && i8 < 2;
  int vote = 0, lag = 0, shift = 0;
  const u32 pbo = dup(G_PB) + (xcd * 8 + wave) * 128;
  u32 near = 0;
  u32x2 ep; u32 xs0, xs1, tch1; u32x4 xr0, xr1;
  const rsrc_t rx = mk_rsrc(part == 2 ? cnd : x, (part == 1 || (part == 2 && has_w)) ? 2048u : 0u);
  auto small_ops = [&]() {
    ep = ld8<AUX_SC1>(wsr, wave == 7 ? WS_EPOCH : OOB, 0);
    xs0 = __builtin_amdgcn_raw_buffer_load_b16(rx, tid * 2, 0, 0); xs1 = __builtin_amdgcn_raw_buffer_load_b16(rx, tid * 2, 1024, 0);
    xr0 = ld16<0>(rx, part == 1 ? lane * 16 : OOB, 0); xr1 = ld16<0>(rx, part == 1 ? lane * 16 : OOB, 1024);
    tch1 = __builtin_amdgcn_raw_buffer_load_b32(mk_rsrc(w2 + (size_t)blk * 32 * 512, is_up ? 64u : 0u), tid == 384 ? 0 : OOB, 0, AUX_NT);
  };
  if (has_w) {
    if (modc) now = 1 << 30;
#pragma nounroll
    while (wleft > wdef) { gate(); due += tauw - tau; piece(); }
    now = 0;
  }
  due = max(due, P.TK * 256);
  SB();
  u32 sot = sa_ & 0xffc00u;
  {
    const u32 s1 = (bl0 + lane - 1 - (lane >= SK0 ? NL - nl : 0)) ^ (124 + part);
#pragma unroll
    for (int k = 0; k < 7; ++k) sot ^= (u32)-(int)(s1 >> k & 1u) & col(P, k);
    PIN(sot);
  }
  u32 tag = 0;
  u32x4 bf[8];
  auto wo_prep = [&](int s0 = 0, int s1 = 2) {
    const u32 wi_ = wimg + wave * 8192;
    const int q = (lane >> 2) & 3, pp = lane & 3;
#pragma unroll
    for (int s = s0; s < s1; ++s) {
#pragma unroll
      for (int t = 0; t < 4; ++t) {
        const int r0 = s * 32 + g * 8 + q, r1 = r0 + 4, ch = t * 2 + (pp >> 1);
        const u32x2 lo = tr_read(wi_ + r0 * 128 + ((ch ^ (r0 & 7)) * 16) + (pp & 1) * 8), hi = tr_read(wi_ + r1 * 128 + ((ch ^ (r1 & 7)) * 16) + (pp & 1) * 8);
        bf[s * 4 + t] = u32x4{lo[0], lo[1], hi[0], hi[1]};
      }
    }
  };
  auto gemv_go = [&](u32 hh) {
    sm.hb[tid] = (u16)hh; sm.hb[512 + tid] = (u16)(hh >> 16);
    WAVE_SYNC();
    const u32x4* a = (c & 11) == 0 ? reinterpret_cast<const u32x4*>(&sm.hb[(c >> 2) * 512 + wave * 64 + g * 8]) : &sm.zero[0];
    const u32x4 a0 = a[0], a1 = a[(c & 11) == 0 ? 4 : 0];
    const f32x4 z = {0.f, 0.f, 0.f, 0.f};
    f32x4 d[4];
#pragma unroll
    for (int t = 0; t < 4; ++t) d[t] = mfma16(a0, bf[t], z);
#pragma unroll
    for (int t = 0; t < 4; ++t) d[t] = mfma16(a1, bf[4 + t], d[t]);
    if (lane < 32) {
#pragma unroll
      for (int t = 0; t < 4; ++t) sm.r2[(t * 16 + c) * 20 + g * 8 + wave] = d[t][0];
    }
  };
  auto mod_stage = [&]() {
    wo_prep();
    gemv_go(pack_bf(silu_f(lo_f(xs0)), silu_f(lo_f(xs1))));
    __builtin_amdgcn_s_waitcnt(0xc07f);
    if (lane == 0) *LDSP(volatile u32, (u32)(uintptr_t)&sm.mflag[wave]) = 1u;
    if (wave == 7) {
#pragma nounroll
      for (int spin = 0; spin < SPIN_MAX; ++spin) {
        const u32 f = *LDSP(volatile u32, (u32)(uintptr_t)&sm.mflag[lane & 7]);
        if (all_ok(f != 0u)) break;
        __builtin_amdgcn_s_sleep(1);
      }
      CFENCE();
      st8_sc1(wsr, G_MOD + (blk * 64 + lane) * 8, pack_bf(sum8(&sm.r2[lane * 20]), sum8(&sm.r2[lane * 20 + 8])), tag);
    }
    STAMP(9);
  };
  if (lane < 4) sm.zero[lane] = u32x4{0u, 0u, 0u, 0u};
  if (part == 2 && has_w) {
    if (tid < 8) sm.mflag[tid] = 0u;
    LDS_BARRIER();
    if (__builtin_expect(modc, 0)) {
      small_ops();
      __builtin_amdgcn_raw_buffer_store_b128(u32x4{SENT, SENT, SENT, SENT}, wsr, G_U + wi * 10240 + tid * 16, 0, AUX_SC1);
      __builtin_amdgcn_raw_buffer_store_b128(u32x4{SENT, SENT, SENT, SENT}, wsr, tid < 128 ? G_U + wi * 10240 + tid * 16 : OOB, 8192, AUX_SC1);
      asm volatile("s_waitcnt vmcnt(0)" ::: "memory");
      tag = next_tag(__builtin_amdgcn_readfirstlane(ep[0]));
      mod_stage();
      __builtin_amdgcn_s_barrier();
    }
  }
  SB();
  const int fd = wi * 8 + (lane & 3) * 2;
  const int ub = wave >> 2, ud = (wave & 3) * 128 + c * 8 + g * 2;
  u32x4 sg[3]; u32 sx2;
  auto side_loads = [&]() {
    const bool fin = part == 1 && wave == 0;
    sg[0] = ld16<AUX_SC1>(wsr, is_up ? G_MOD + (1024 + ud) * 8 : (fin ? G_MOD + (1024 + fd) * 8 : OOB), 0);
    sg[1] = ld16<AUX_SC1>(wsr, is_up ? G_MOD + (1536 + ud) * 8 : (fin ? G_MOD + (2560 + fd) * 8 : OOB), 0);
    sg[2] = ld16<AUX_SC1>(wsr, is_up ? G_MOD + (2048 + ud) * 8 : OOB, 0);
    sx2 = __builtin_amdgcn_raw_buffer_load_b32(mk_rsrc(x, (is_up || fin) ? 2048u : 0u), is_up ? ub * 1024 + ud * 2 : (g & 1) * 1024 + fd * 2, 0, 0);
  };
  auto side_pin = [&]() { PIN(sg[0]); PIN(sg[1]); PIN(sg[2]); PIN(sx2); };
  const u32 mlo = G_ML + (head * 2 + b) * 64 + (lane & 3) * 16;
  float fmrg = 0.f;
  auto merge_factor = [&](const u32x4& ml) {
    const float mi = u2f(ml[0]);
    float M = fmaxf(mi, dpp<XOR1>(mi)); M = fmaxf(M, dpp<XOR2>(M));
    const float e = __expf(mi - M);
    float L = e * u2f(ml[2]);
    L += dpp<XOR1>(L); L += dpp<XOR2>(L);
    fmrg = rdl(e * __builtin_amdgcn_rcpf(L), part);
  };
  f32x2 ga = {0.f, 0.f}, xin = {0.f, 0.f}, cc1 = {0.f, 0.f};
  float Af = 0.f, Bf = 0.f;
  const int hpos = ub * 512 + ud;
  auto up_prep = [&]() {
    if (!all_ok((sg[0][1] == tag) & (sg[0][3] == tag) & (sg[1][1] == tag) & (sg[1][3] == tag) & (sg[2][1] == tag) & (sg[2][3] == tag))) gather16<3>(wsr, G_MOD + (1024 + ud) * 8, 512 * 8, tag, sg);
    ga = f32x2{ub ? hi_f(sg[0][0]) : lo_f(sg[0][0]), ub ? hi_f(sg[0][2]) : lo_f(sg[0][2])};
    cc1 = f32x2{rbf(1.f + (ub ? hi_f(sg[2][0]) : lo_f(sg[2][0]))), rbf(1.f + (ub ? hi_f(sg[2][2]) : lo_f(sg[2][2])))};
    xin = f32x2{lo_f(sx2), hi_f(sx2)};
    u32* hp = reinterpret_cast<u32*>(&sm.hb[hpos]);
    hp[512] = pack_bf(cc1[0], cc1[1]);
    hp[1024] = ub ? perm_hi(sg[1][0], sg[1][2]) : perm_lo(sg[1][0], sg[1][2]);
  };
  auto up_frags = [&]() {
    const int pp = lane & 3;
    const u32 a = wimg + (pp & 1) * 32768 + ((pp >> 1) * 32 + g * 8 + ((lane >> 2) & 3)) * 64 + (((wave >> 1) ^ ((g & 1) | (pp & 2))) * 16) + (wave & 1) * 8;
#pragma unroll
    for (int rb = 0; rb < 8; ++rb) {
      const u32x2 lo = tr_read(a + rb * 4096), hi = tr_read(a + rb * 4096 + 256);
      bf[rb] = u32x4{lo[0], lo[1], hi[0], hi[1]};
    }
  };
  const u32x4* ap = reinterpret_cast<const u32x4*>(&sm.hb[((lane >> 3) & 1) * 512 + (lane & 1) * 32 + g * 8]);
  const bool par = lane & 8;
  auto limit = [&](auto sc) {
    constexpr int s = sc.v;
    WAIT_VM(window(s, 1 + (PRM0.W[s >> 3] >> (4 * (s & 7)) & 7)));
    SB();
  };
  auto sample = [&]() { if (timer) lag += (int)(((u32)wall_clock64() - t0) << 8) - due + ((int)__builtin_amdgcn_s_getreg(7 | (3 << 11)) + 1) * tau; };
  auto report = [&](u32 dst) {
    if (timer) {
      if (lane == 0) sm.mflag[wave] = vote * 65536 + min(lag >> 8, 4000);
      lag = 0;
      LDS_BARRIER();
      if (wave == 7) {
        int s = sm.mflag[lane & 7];
        s += __builtin_amdgcn_mov_dpp(s, XOR1, 0xf, 0xf, true); s += __builtin_amdgcn_mov_dpp(s, XOR2, 0xf, 0xf, true); s += __builtin_amdgcn_mov_dpp(s, XOR7, 0xf, 0xf, true);
        if (lane < 8) st8_sc1(wsr, dst + lane * 128 + wi * 8, s, tag);
      }
    }
    SB();
  };
  float l0 = 0.f, l1 = 0.f;
  u32x2 gmd[2], tbg;
  int t1 = 0;
#pragma unroll
  for (int i = SK0; i < SK1; ++i) kr[i] = u32x4{0u, 0u, 0u, 0u};
  const int first = SK0 + NL - nl;
  auto left_out = [&](int i) { int f = first; asm volatile("" : "+s"(f)); return i < f; };
  sfor<1, NL>([&](auto ic) {
    constexpr int i = ic.v;
    if constexpr (i >= SK0 && i < SK1) { if (left_out(i)) return true; }
    const u32 so = __builtin_amdgcn_readlane(sot, i);
    gate();
    if constexpr ((i >= TBS && i <= TBP) || (i >= RPS && i <= RPP)) sample();
    SB();
    if constexpr (i >= SK0 && i < SK1) ld16_into(kr[i], rk, lane * 16, so); else kr[i] = ld16<AUX_NT>(rk, lane * 16, so);
    if constexpr (i == SMO) small_ops();
    if constexpr (i == SMO + 1) { if (voter) __builtin_amdgcn_raw_buffer_store_b32(0u, wsr, pbo + (lane >> 5) * 8192 + (lane & 31) * 4, 0, AUX_SC1); }
    if constexpr (i == SK1) tbg = ld8<AUX_SC1>(wsr, wave == 7 ? G_VT + xcd * 128 + c * 8 : OOB, 0);
    if constexpr (i == TBL) tbg = ld8<AUX_SC1>(wsr, wave == 7 ? G_TB + xcd * 128 + c * 8 : OOB, 0);
    if constexpr (i == QP) { gmd[0] = ld8<AUX_SC1>(wsr, part == 1 ? G_MOD + tid * 8 : OOB, 0); gmd[1] = ld8<AUX_SC1>(wsr, part == 1 ? G_MOD + tid * 8 : OOB, 4096); }
    SB();
    SB();
    if (i >= 4) limit(ic);
    if (i == 1) STAMP(25);
    if (i == 8) STAMP(12);
    if (i == 20) STAMP(21);
    if (i == 32) STAMP(22);
    if (i == TBP) {
      if (timer) {
        if (lane == 0) sm.mflag[wave] = lag;
        lag = 0;
        LDS_BARRIER();
        if (wave == 7) {
          int s = sm.mflag[lane & 7];
          s += __builtin_amdgcn_mov_dpp(s, XOR1, 0xf, 0xf, true); s += __builtin_amdgcn_mov_dpp(s, XOR2, 0xf, 0xf, true); s += __builtin_amdgcn_mov_dpp(s, XOR7, 0xf, 0xf, true);
          if (lane < 8) st8_sc1(wsr, G_TB + lane * 128 + wi * 8, t0 << 10 | (u32)min(s / (2048 * (TBP - TBS + 1)), 1023), tag);
        }
      }
      SB();
    }
    if (i == PRB) {
      if (voter) {
        u64 ta, tb, tc; u32 dm;
        const u32 pa = pbo + (wave & 1) * 8192;
        asm volatile("s_memtime %0\n\ts_waitcnt lgkmcnt(0)\n\ts_load_dword %3, %4, %5\n\ts_waitcnt lgkmcnt(0)\n\ts_memtime %1\n\ts_waitcnt lgkmcnt(0)\n\t"
                     "s_load_dword %3, %4, %6\n\ts_waitcnt lgkmcnt(0)\n\ts_memtime %2\n\ts_waitcnt lgkmcnt(0)"
                     : "=&s"(ta), "=&s"(tb), "=&s"(tc), "=&s"(dm) : "s"(ws), "s"(pa), "s"(pa ^ 8192u));
        const int d = min(max((int)(2 * (u32)tb - (u32)ta - (u32)tc), -500), 500);
        vote = ((wave ^ die) & 1) ? -d : d;
        STAMP(26);
      }
      SB();
    }
    if (i == TBU) {
      if (wave == 7) {
        const bool ok = tbg[1] == tag;
        float lf = ok ? (float)min(max((int)((t0 << 10 | 1023u) - tbg[0]) >> 10, -300), 300) : 0.f, nf = ok ? 1.f : 0.f, lg = ok ? (float)(tbg[0] & 1023u) : 0.f;
        lf += dpp<XOR1>(lf); nf += dpp<XOR1>(nf); lg += dpp<XOR1>(lg); lf += dpp<XOR2>(lf); nf += dpp<XOR2>(nf); lg += dpp<XOR2>(lg);
        lf += dpp<XOR7>(lf); nf += dpp<XOR7>(nf); lg += dpp<XOR7>(lg); lf += dpp<XOR8>(lf); nf += dpp<XOR8>(nf); lg += dpp<XOR8>(lg);
        const float rn = __builtin_amdgcn_rcpf(fmaxf(nf, 1.f));
        shift = __builtin_amdgcn_readfirstlane(min(max((int)((lg * rn - (float)P.GS) * (float)P.GB * 0.125f), 0), 150));
        if (lane == 0) sm.bc = u32x2{tag, t0 - (u32)(int)(lf * rn) + shift};
      }
      LDS_BARRIER();
      const u32x2 bc = sm.bc;
      tag = __builtin_amdgcn_readfirstlane(bc[0]);
      t1 = (__builtin_amdgcn_readfirstlane((int)(bc[1] - t0)) + TEND + P.X1) * 256;
      tau -= __builtin_amdgcn_readfirstlane((int)((float)(int)(t0 - bc[1]) * 256.f * __builtin_amdgcn_rcpf((float)(nl - TBU - 1))));
      SB();
    }
    if (i == RPP) report(G_VT);
    if (i == LNS) {
      tag = next_tag(__builtin_amdgcn_readfirstlane(ep[0]));
      if (part == 1) {
        float x0[8], x1[8], mb0, ri0, mb1, ri1;
#pragma unroll
        for (int k = 0; k < 4; ++k) { x0[2 * k] = lo_f(xr0[k]); x0[2 * k + 1] = hi_f(xr0[k]); x1[2 * k] = lo_f(xr1[k]); x1[2 * k + 1] = hi_f(xr1[k]); }
        ln_stats2(x0, x1, mb0, ri0, mb1, ri1);
        l0 = rbf(rbf(lo_f(xs0) - mb0) * ri0); l1 = rbf(rbf(lo_f(xs1) - mb1) * ri1);
      }
      PIN(l0); PIN(l1);
      SB();
    }
    if (i == WUSE) {
      PIN(tch1); PIN(tch2);
      if ((part == 1 || (part == 2 && has_w && !modc))) {
        WAIT_VM(WK); CFENCE();
        if (part == 2) mod_stage(); else wo_prep();
      }
#pragma unroll
      for (int k = 0; k < 8; ++k) PIN(bf[k]);
      SB();
    }
    if (i == QP + QD) {
      if (part == 1) {
        u32 gm[2] = {gmd[0][0], gmd[1][0]};
        if (!all_ok((gmd[0][1] == tag) & (gmd[1][1] == tag))) { STAMP(11); gather8<2>(wsr, G_MOD + tid * 8, 512 * 8, tag, gm); }
        STAMP(5);
        gemv_go(pack_bf(rbf(l0 * rbf(1.f + lo_f(gm[1]))) + lo_f(gm[0]), rbf(l1 * rbf(1.f + hi_f(gm[1]))) + hi_f(gm[0])));
      }
      SB();
    }
    if (i == QP + QD + 1) {
      if (part == 1) {
        LDS_BARRIER();
        if (wave == 0) {
          const int bb = lane >> 5, jq = lane & 31;
          st8_sc1(wsr, G_Q + (bb * 2048 + blk * 32 + jq) * 8, pack_bf(sum8(&sm.r2[2 * jq * 20 + bb * 8]), sum8(&sm.r2[(2 * jq + 1) * 20 + bb * 8])), tag);
        }
        STAMP(6);
      }
      SB();
    }
    return true;
  });
  PIN(sot);
  if (wave == 7) {
    const bool ok = tbg[1] == tag;
    float vt = ok ? (float)((int)tbg[0] >> 16) : 0.f, lg = ok ? (float)(tbg[0] & 0xffffu) : 0.f, nf = ok ? 8.f * (RPP - RPS + 1) : 0.f;
    vt += dpp<XOR1>(vt); lg += dpp<XOR1>(lg); nf += dpp<XOR1>(nf); vt += dpp<XOR2>(vt); lg += dpp<XOR2>(lg); nf += dpp<XOR2>(nf);
    vt += dpp<XOR7>(vt); lg += dpp<XOR7>(lg); nf += dpp<XOR7>(nf); vt += dpp<XOR8>(vt); lg += dpp<XOR8>(lg); nf += dpp<XOR8>(nf);
    lg *= __builtin_amdgcn_rcpf(fmaxf(nf, 1.f));
    if (lane == 0) sm.bc = u32x2{__float_as_uint(vt), (u32)min(max((int)((lg - (float)P.B0) * (float)(P.BG * 32)) - shift * P.D0 * 32, 0), 200 * 256)};
  }
  LDS_BARRIER();
  {
    const u32x2 bc = sm.bc;
    const int st = __builtin_amdgcn_readfirstlane(bc[1]), sp = (st * P.BF) >> 3;
    near = ((__builtin_amdgcn_readfirstlane(bc[0]) >> 31 ^ die ^ P.NR ^ 1) & 1) * 8192u;
    t1 += st;
    due += P.TG * 256 + sp;
    tau = (te1 >> 16) + __builtin_amdgcn_readfirstlane((int)((float)(st - sp) * __builtin_amdgcn_rcpf((float)(nl - 3))));
  }
  const u32 qoff = G_Q + ((b * 32 + head) * 64 + c * 4) * 8;
  u32x4 qg[2], mlg;
  u32 qq[4];
  constexpr int NB = (NL + 15) / 16;
  float xlo, sc[NB], pr[NB], m = 0.f;
  f32x4 acc = {0.f, 0.f, 0.f, 0.f};
  u32 pb[NB], pm[4], pl8[8], cpf;
  const u32 adst0 = lds_addr(lane < 32 ? &sm.red[wave][c * 8 + g * 4] : &sm.r2[(lane - 32) * 4]);
  u32 po, rdst, rsrc, xo;
  auto wo_addr = [&]() {
    po = dup(G_P + xcd * 8192) + tid * 16 + near;
    rdst = lane < 32 ? (u32)(uintptr_t)&sm.red2[wave][0] + lane * 16 : (u32)(uintptr_t)&sm.r2[0] + (lane - 32) * 16;
    rsrc = (u32)(uintptr_t)&sm.red2[0][((wave & 1) * 16 + (lane & 15)) * 4 + (lane >> 4)];
    xo = dup(G_XP + xcd * 2048 + (wave & 1) * 1024 + (blk * 64 + (lane & ~1)) * 2) + (lane & 1) * 8192;
    PIN(po); PIN(rdst); PIN(rsrc); PIN(xo);
  };
  u32 pso = (wave == 0 && (g & 1) == 0) ? pslot + (c >> 3) * 1024 + (c & 3) * 256 + (c >> 2 & 1) * 128 + (g >> 1) * 8192 : OOB;
  PIN(pso);
  const u32 nadd = wdef ? P.LW : (is_up ? P.LU : 0);
  u32 nto = tid < 2 ? dup(G_NT) + bid * 8 + tid * 8192 : OOB;
  PIN(nto);
  auto notices = [&]() -> int {
    u32 off = dup(G_NT) + near + lane * 16;
    PIN(off);
    until(due);
    int mx;
#pragma nounroll
    for (int spin = 0; spin < SPIN_MAX; ++spin) {
      CFENCE();
      const u32x4 ga_ = ld16<AUX_SC1>(wsr, off, 0), gb_ = ld16<AUX_SC1>(wsr, off, 1024);
      mx = max(max((int)(ga_[0] - t0), (int)(ga_[2] - t0)), max((int)(gb_[0] - t0), (int)(gb_[2] - t0)));
      if (all_ok((((ga_[1] ^ tag) | (ga_[3] ^ tag)) | ((gb_[1] ^ tag) | (gb_[3] ^ tag))) == 0u)) break;
      for (int k = 0; k < P.AG; ++k) __builtin_amdgcn_s_sleep(1);
    }
    if (all_ok(mx <= (t1 >> 8) - P.AC)) return t1;
    mx = max(mx, __builtin_amdgcn_mov_dpp(mx, XOR1, 0xf, 0xf, true)); mx = max(mx, __builtin_amdgcn_mov_dpp(mx, XOR2, 0xf, 0xf, true));
    mx = max(mx, __builtin_amdgcn_mov_dpp(mx, XOR7, 0xf, 0xf, true)); mx = max(mx, __builtin_amdgcn_mov_dpp(mx, XOR8, 0xf, 0xf, true));
    mx = max(max(__builtin_amdgcn_readlane(mx, 0), __builtin_amdgcn_readlane(mx, 16)), max(__builtin_amdgcn_readlane(mx, 32), __builtin_amdgcn_readlane(mx, 48)));
    return max(min(mx, 1 << 20) * 256, t1 + P.L0 * 256) + P.L1 * 256;
  };
  auto pv_row = [&](auto jc) {
    constexpr int i = jc.v;
    const u32 pv = __float_as_uint(bcast16(u2f(pb[i >> 4]), i & 15));
    acc = mfma16(u32x4{pv & pm[0], pv & pm[1], pv & pm[2], pv & pm[3]}, vr[i], acc);
    PIN(acc);
    return true;
  };
  sfor<0, NL - 1>([&](auto ic) {
    constexpr int v = ic.v;
    if constexpr (v >= SK0 && v < SK1) { if (left_out(v)) return true; }
    if constexpr (v > 0) {
      const u32 so = __builtin_amdgcn_readlane(sot, v);
      if constexpr (v >= SK1) { if (v == NL - P.NE) tau = (tau * P.FE) >> 4; }
      gate();
      if constexpr (v >= RVS && v <= RVP) sample();
      SB();
      if constexpr (v >= SK0 && v < SK1) ld16_into(vr[v], rv, lane * 16, so); else vr[v] = ld16<AUX_NT>(rv, lane * 16, so);
      SB();
      SB();
    }
    if constexpr (v == SK0 - 1) {
#pragma unroll
      for (int i = SK0; i < SK1; ++i) { vr[i] = u32x4{0u, 0u, 0u, 0u}; PIN(vr[i]); }
    }
    if constexpr (v == VQ - 4) { qg[0] = ld16<AUX_SC1>(wsr, qoff, 0); qg[1] = ld16<AUX_SC1>(wsr, qoff + 16, 0); }
    if constexpr (v == VSIDE) side_loads();
    if constexpr (v == CPF) {
      u64 pc; u32 dlt, len;
      asm volatile("s_getpc_b64 %0\n.Lcpf_here:\n\ts_mov_b32 %1, .Ltail_start-.Lcpf_here\n\ts_mov_b32 %2, .Lend_a-.Ltail_start\n\ts_max_i32 %2, %2, .Lend_b-.Ltail_start\n\ts_max_i32 %2, %2, .Lend_c-.Ltail_start\n\ts_max_i32 %2, %2, .Lend_d-.Ltail_start\n\ts_max_i32 %2, %2, .Lend_e-.Ltail_start" : "=s"(pc), "=s"(dlt), "=s"(len) :: "scc");
      pc += dlt;
      const rsrc_t rc = mk_rsrc((const void*)pc, (part == 3 && i8 == 1) ? len : 0u);
      cpf = __builtin_amdgcn_raw_buffer_load_b32(rc, tid * 64, 0, 0);
    }
    if constexpr (v == SK1 + 2) PIN(cpf);
    if constexpr (v == RVP) report(G_VT + 1024);
    if constexpr (v == RVL) tbg = ld8<AUX_SC1>(wsr, wave == 7 ? G_VT + 1024 + xcd * 128 + c * 8 : OOB, 0);
    if constexpr (v == SK1) mlg = ld16<AUX_SC1>(wsr, wave == 0 ? mlo : OOB, 0);
    SB();
    if constexpr (NL + v < 72) limit(IC<NL + v>{});
    if constexpr (v >= SK0 + WV) { WAIT_VM(v < SK1 ? WV : WV + 1); SB(); }
    if constexpr (v == VQ) {
      if (!all_ok((qg[0][1] == tag) & (qg[0][3] == tag) & (qg[1][1] == tag) & (qg[1][3] == tag))) { STAMP(20); gather16<2>(wsr, qoff, 16, tag, qg); }
      STAMP(1); WSTAMP(1);
      qq[0] = qg[0][0]; qq[1] = qg[0][2]; qq[2] = qg[1][0]; qq[3] = qg[1][2];
    }
    if constexpr (v >= VQ && v < VS) {
      constexpr int sl = v - VQ, bt = sl >> 1;
      float xs[8];
#pragma unroll
      for (int i = 0; i < 8; ++i) {
        float d = 0.f;
        if (sl * 8 + i < NL) {
#pragma unroll
          for (int k = 0; k < 4; ++k) d = dot2(kr[sl * 8 + i][k], qq[k], d);
        }
        xs[i] = d;
      }
#pragma unroll
      for (int h = 0; h < 4; ++h) xs[h] = sel_add<XOR7>(xs[h], xs[h + 4], c & 4);
#pragma unroll
      for (int h = 0; h < 2; ++h) xs[h] = sel_add<XOR2>(xs[h], xs[h + 2], c & 2);
      xs[0] = sel_add<XOR1>(xs[0], xs[1], c & 1);
      if constexpr ((sl & 1) || sl == NSL - 1) {
        const float t = (sl & 1) ? sel_add<XOR8>(xlo, xs[0], c & 8) : xs[0] + dpp<XOR8>(xs[0]);
        const float s = rbf(rbf(t) * 0.08838834764831845f);
        sc[bt] = (bt * 16 + c < SK0 || (bt * 16 + c >= first && bt * 16 + c < NL)) ? s : -__builtin_inff();
        PIN(sc[bt]);
      } else {
        xlo = xs[0];
        PIN(xlo);
      }
    }
    if constexpr (v == VS) {
      float mloc = sc[0];
#pragma unroll
      for (int bt = 1; bt < NB; ++bt) mloc = fmaxf(mloc, sc[bt]);
      m = fmaxf(wave_max(mloc), -3.0e38f);
      float ls = 0.f;
#pragma unroll
      for (int bt = 0; bt < NB; ++bt) { pr[bt] = __expf(sc[bt] - m); ls += pr[bt]; }
      const float l = wave_sum(ls);
      if (lane == 0) { sm.ml[wave][0] = m; sm.ml[wave][1] = l; }
      if (c == ((NL - 1) & 15)) sm.pl[g][wave] = pr[NB - 1];
      STAMP(2); WSTAMP(2);
    }
    if constexpr (v >= SK1) pv_row(IC<v - SK1 + SK0 - WV>{});
    if constexpr (v < SK0 || v >= SK0 + WV) sfor<cons_end(v - 1), cons_end(v)>(pv_row);
    if constexpr (v == RVU) {
      if (wave == 7) {
        const bool ok = tbg[1] == tag;
        float lg = ok ? (float)(tbg[0] & 0xffffu) : 0.f, nf = ok ? 8.f * (RVP - RVS + 1) : 0.f;
        lg += dpp<XOR1>(lg); nf += dpp<XOR1>(nf); lg += dpp<XOR2>(lg); nf += dpp<XOR2>(nf); lg += dpp<XOR7>(lg); nf += dpp<XOR7>(nf); lg += dpp<XOR8>(lg); nf += dpp<XOR8>(nf);
        lg *= __builtin_amdgcn_rcpf(fmaxf(nf, 1.f));
        if (lane == 0) sm.bc[1] = (u32)min(max((int)((lg - (float)P.F0) * (float)(P.FG * 32)), 0), 200 * 256);
      }
      LDS_BARRIER();
      const int st = __builtin_amdgcn_readfirstlane(sm.bc[1]), sp = (st * P.FF_) >> 3;
      t1 += st;
      due += sp;
      tau += __builtin_amdgcn_readfirstlane((int)((float)(st - sp) * __builtin_amdgcn_rcpf((float)(nl - RVU - 3))));
    }
    if constexpr (v == SK1 + 1) {
      float p[8];
#pragma unroll
      for (int w = 0; w < 8; ++w) p[w] = sm.pl[g][w] * __expf(sm.ml[w][0] - m);
#pragma unroll
      for (int w = 0; w < 8; ++w) pl8[w] = pack_bf(p[w], p[w]);
#pragma unroll
      for (int w = 0; w < 8; ++w) PIN(pl8[w]);
    }
    if constexpr (v == MLS - 1) { side_pin(); if (is_up) up_prep(); PIN(ga); PIN(xin); PIN(cc1); }
    if constexpr (v == UPREP - 1) wo_addr();
    if constexpr (v == UPREP) {
#pragma unroll
      for (int k = 0; k < 8; ++k) asm volatile("" : "=v"(bf[k]));
      if (part == 0) wo_prep(); else if (is_up) up_frags();
#pragma unroll
      for (int k = 0; k < 8; ++k) PIN(bf[k]);
    }
    if constexpr (v == UPREP + 1) {
      if (is_up) {
        const u32x4* a1 = ap + 128 + ((lane >> 1) & 1) * 128;
        f32x4 d0 = {0.f, 0.f, 0.f, 0.f}, d1 = {0.f, 0.f, 0.f, 0.f};
#pragma unroll
        for (int rb = 0; rb < 4; ++rb) { d0 = mfma16(a1[rb * 8], bf[rb], d0); d1 = mfma16(a1[(rb + 4) * 8], bf[rb + 4], d1); }
        Af = par ? d0[1] + d1[1] : d0[0] + d1[0]; Bf = par ? d0[3] + d1[3] : d0[2] + d1[2];
        Af += dpp<XOR8>(Af); Bf += dpp<XOR8>(Bf);
      }
      PIN(Af); PIN(Bf);
    }
    if (v == 10 || v == 16) STAMP(15 + v / 6);
    if (v == 20) STAMP(18);
    if (v == 32) STAMP(19);
    if constexpr (v == MLS) {
      LDS_BARRIER();
      float mm[8], ll[8];
#pragma unroll
      for (int w = 0; w < 8; ++w) { const f32x2 t = *reinterpret_cast<const f32x2*>(&sm.ml[w][0]); mm[w] = t[0]; ll[w] = t[1]; }
      float Mwg = mm[0], Lwg = 0.f;
#pragma unroll
      for (int w = 1; w < 8; ++w) Mwg = fmaxf(Mwg, mm[w]);
      if (wave == 0) {
#pragma unroll
        for (int w = 0; w < 8; ++w) Lwg = fmaf(__expf(mm[w] - Mwg), ll[w], Lwg);
        if (lane == 0) st16_sc1(wsr, G_ML + ((head * 2 + b) * 4 + part) * 16, __float_as_uint(Mwg), __float_as_uint(Lwg), tag);
      }
      const float fw = __expf(m - Mwg);
      m = Mwg;
#pragma unroll
      for (int bt = 0; bt < NB; ++bt) { pb[bt] = pack_bf(pr[bt] * fw, pr[bt] * fw); PIN(pb[bt]); }
#pragma unroll
      for (int k = 0; k < 4; ++k) { pm[k] = (c >> 1 & 3) == k ? ((c & 1) ? 0xffff0000u : 0x0000ffffu) : 0u; PIN(pm[k]); }
    }
    SB();
    return true;
  });
  asm volatile(".Ltail_start:");
  if (wave != 0) {
    pv_row(IC<SK0 - 1>{});
    WAIT_VM(WV); SB();
    sfor<NL - 1 - WV, NL - 1>(pv_row);
    *LDSP(f32x4, adst0) = acc;
    WSTAMP(14);
    LDS_BARRIER();
  } else {
    u32x4 xr[8], am[8];
#pragma unroll
    for (int w = 0; w < 8; ++w) am[w] = u32x4{pl8[w] & pm[0], pl8[w] & pm[1], pl8[w] & pm[2], pl8[w] & pm[3]};
#pragma unroll
    for (int w = 0; w < 8; ++w) PIN(am[w]);
    {
      const u32 so = __builtin_amdgcn_readlane(sot, NL - 1);
      gate();
      SB();
#pragma unroll
      for (int w = 0; w < 8; ++w) xr[w] = ld16<AUX_NT>(rv, lane * 16, so ^ ((w & 1) ? col(P, 7) : 0u) ^ ((w & 2) ? col(P, 8) : 0u) ^ ((w & 4) ? col(P, 9) : 0u));
      SB();
    }
    u32 rsum = lds_addr(&sm.red[0][c * 8 + (g & 1) * 4]);
    pv_row(IC<SK0 - 1>{});
    WAIT_VM(WV + 8); SB();
    sfor<NL - 1 - WV, NL - 3>(pv_row);
    if (__builtin_expect(!all_ok((mlg[1] == tag) & (mlg[3] == tag)), 0)) {
#pragma nounroll
      for (int spin = 0; spin < SPIN_MAX; ++spin) {
        CFENCE();
        mlg = ld16<AUX_SC1>(wsr, mlo, 0);
        if (all_ok((mlg[1] == tag) & (mlg[3] == tag))) break;
      }
    }
    merge_factor(mlg);
    sfor<NL - 3, NL - 1>(pv_row);
    STAMP(14); WSTAMP(14);
    LDS_BARRIER();
    STAMP(15);
    f32x4 t[8];
#pragma unroll
    for (int w = 1; w < 8; ++w) t[w] = LDSP(f32x4, rsum)[w * 32];
    acc = ((acc + t[1]) + (t[2] + t[3])) + ((t[4] + t[5]) + (t[6] + t[7]));
    PIN(acc);
    STAMP(3);
    SB();
#pragma unroll
    for (int w = 0; w < 8; ++w) acc = mfma16(am[w], xr[w], acc);
  }
  WAIT_VM(0);
  acc *= fmrg;
  {
    const u32 w0 = min(pack_bf(acc[0], acc[1]), SENT - 1u), w1 = min(pack_bf(acc[2], acc[3]), SENT - 1u);
    const auto e0 = __builtin_amdgcn_permlane16_swap(w0, w0, false, false), e1 = __builtin_amdgcn_permlane16_swap(w1, w1, false, false);
    __builtin_amdgcn_raw_buffer_store_b128(u32x4{e0[0], e1[0], e0[1], e1[1]}, wsr, pso, 0, AUX_SC1);
  }
  st8_sc1(wsr, nto, (u32)wall_clock64() + nadd, tag);
  SB();
  STAMP(4);

  if (part == 2) FINISH();
  if (part == 0) {
    __builtin_amdgcn_s_barrier();
#pragma nounroll
    while (wleft > 0) piece();
    bool late = false;
    u32x4 og[2];
    if (wdef) {
      late = true;
      while ((int)(((u32)wall_clock64() - t0) << 8) < t1 - 20 * 256) if (__builtin_amdgcn_s_getreg(7 | (3 << 11)) == 0) { late = false; break; }
      if (__builtin_expect(late, 0)) { until(t1); CFENCE(); og[0] = ld16<AUX_SC1>(wsr, po, 0); SB(); WAIT_VM(1); } else { WAIT_VM(0); }
      CFENCE();
      if (P.WD > 4) wo_prep(0, 1);
      wo_prep(1, 2);
#pragma unroll
      for (int k = 0; k < 8; ++k) PIN(bf[k]);
      SB();
    }
    if (__builtin_expect(!late, 1)) { until(t1); CFENCE(); og[0] = ld16<AUX_SC1>(wsr, po, 0); }
    STAMP(11);
    SB();
    f32x4 d[4];
    auto wo_mm = [&]() {
      const f32x4 z = {0.f, 0.f, 0.f, 0.f};
#pragma unroll
      for (int t = 0; t < 4; ++t) { d[t] = mfma16(og[0], bf[t], z); og[1][t] = __float_as_uint(dpp<XOR8>(u2f(og[0][t]))); }
      SB();
#pragma unroll
      for (int t = 0; t < 4; ++t) d[t] = mfma16(og[1], bf[4 + t], d[t]);
    };
    auto p_bad = [&]() { return !all_ok(max(max(og[0][0], og[0][1]), max(og[0][2], og[0][3])) != SENT); };
    wo_mm();
    if (__builtin_expect(p_bad(), 0)) {
#pragma nounroll
      for (int spin = 0; spin < SPIN_MAX; ++spin) {
        __builtin_amdgcn_s_sleep(1);
        CFENCE();
        og[0] = ld16<AUX_SC1>(wsr, po, 0);
        if (!p_bad()) break;
      }
      wo_mm();
      SB();
      asm volatile(".Lend_d:");
    }
    STAMP(5); WSTAMP(5);
    {
      SB();
      f32x4 y; f32x2 h[4];
#pragma unroll
      for (int t = 0; t < 4; ++t) { h[t] = d[t].xy + d[t].zw; PIN(h[t]); }
#pragma unroll
      for (int t = 0; t < 4; ++t) { y[t] = h[t][0] + h[t][1]; PIN(y[t]); }
      *LDSP(f32x4, rdst) = y;
    }
    STAMP(16);
    LDS_BARRIER();
    STAMP(17);
    if (wave < 2) {
      f32x2 v[4];
#pragma unroll
      for (int w = 0; w < 4; ++w) v[w] = f32x2{LDSP(float, rsrc)[w * 256], LDSP(float, rsrc)[w * 256 + 128]};
      const f32x2 h = (v[0] + v[1]) + (v[2] + v[3]);
      const float y = h[0] + h[1];
      __builtin_amdgcn_raw_buffer_store_b32(min(pack_bf(dpp<0xA0>(y), dpp<0xF5>(y)), SENT - 1u), wsr, xo, 0, AUX_SC1);
      asm volatile(".Lend_a:");
    }
    STAMP(6);
    FINISH();
  }
  if (part == 3) {
    if (!has_w) FINISH();
    u32x4 b2[4];
    {
      __builtin_amdgcn_s_barrier();
      const rsrc_t r2 = mk_rsrc(w2 + (size_t)(blk * 32 + wave * 4) * 512, 4096u);
      const u32 l2 = (u32)(uintptr_t)(&sm.w2b[wave * 4][0]);
#pragma unroll
      for (int n = 0; n < 4; ++n) dma16(r2, (lane ^ (n * 2) ^ ((wave & 2) << 2)) * 16, n * 1024, l2 + n * 1024);
    }
    const u32 tdst = lds_addr(&sm.hb[hpos]);
    const u32 sdst = lds_addr(&sm.stat[wave][lane]);
    const u32 ssrc = lds_addr(&sm.stat[(g >> 1) * 4 + ((lane >> 3) & 3)][(lane & 7) * 4]);
    const u32 adst = lds_addr((c < 4 && (g & 1) == 0) ? &sm.actl[g >> 1][wave * 4 + c] : reinterpret_cast<u16*>(&sm.dummy[lane]));
    const u32 asrc = lds_addr(&sm.actl[lane & 1][g * 8]);
    const u32 apa = lds_addr(&sm.hb[(c >> 3) * 512 + (c & 3) * 64 + ((c >> 2) & 1) * 32 + g * 8]);
    constexpr u32 xs2 = 16384;
    u32 xoff = dup(G_XP) + near + g * 2048 + ub * 1024 + (wave & 3) * 256 + c * 16;
    u32x4 one;
#pragma unroll
    for (int k = 0; k < 4; ++k) one[k] = ((c & 2) == 0 && (c >> 2) == k) ? ((c & 1) ? 0x3f800000u : 0x00003f80u) : 0u;
    PIN(one);
    u32 ooff = dup(G_OP + (blk * 512 + tid) * 8);
    float Afp = Af, Bfp = Bf;
    PIN(xoff); PIN(ooff); PIN(Afp); PIN(Bfp);
    f32x2 gc = ga * cc1, xc = xin * cc1;
    PIN(ga); PIN(xin); PIN(gc); PIN(xc);
    u32 w2a = (u32)(uintptr_t)(&sm.w2b[0][0]) + (g * 8 + ((lane >> 2) & 3)) * 1024 + (wave ^ (g & 1)) * 128 + (lane & 15) * 8;
    PIN(w2a);
    if (wave == 1) {
      due = t1 + P.N1 * 256;
      const int tn = notices();
      due = tn + P.X2 * 256;
      until(due);
    }
    asm volatile("s_waitcnt vmcnt(0)" ::: "memory");
    STAMP(9);
    __builtin_amdgcn_s_barrier();
    STAMP(11);
    CFENCE();
    u32x4 xg[2] = {ld16<AUX_SC1>(wsr, xoff, 0), ld16<AUX_SC1>(wsr, xoff, xs2)};
    SB();
#pragma unroll
    for (int t = 0; t < 4; ++t) {
      const u32x2 lo = tr_read(w2a ^ (t * 32)), hi = tr_read((w2a ^ (t * 32)) + 4096);
      b2[t] = u32x4{lo[0], lo[1], hi[0], hi[1]};
    }
#pragma unroll
    for (int t = 0; t < 4; ++t) PIN(b2[t]);
    SB();
    const f32x4 z4 = {0.f, 0.f, 0.f, 0.f};
    auto xp_bad = [&]() { return !all_ok(max(max(max(xg[0][0], xg[0][1]), max(xg[0][2], xg[0][3])), max(max(xg[1][0], xg[1][1]), max(xg[1][2], xg[1][3]))) != SENT); };
    f32x4 v = mfma16(one, xg[1], mfma16(one, xg[0], z4));
    if (__builtin_expect(xp_bad(), 0)) {
#pragma nounroll
      for (int spin = 0; spin < SPIN_MAX; ++spin) {
        __builtin_amdgcn_s_sleep(1);
        CFENCE();
        xg[0] = ld16<AUX_SC1>(wsr, xoff, 0); xg[1] = ld16<AUX_SC1>(wsr, xoff, xs2);
        if (!xp_bad()) break;
      }
      v = mfma16(one, xg[1], mfma16(one, xg[0], z4));
      SB();
      asm volatile(".Lend_e:");
    }
    STAMP(5); WSTAMP(5);
    {
      const f32x2 a = v.xy, hc = __builtin_elementwise_fma(gc, a, xc), x1 = __builtin_elementwise_fma(ga, a, xin);
      *LDSP(u32, tdst) = pack_bf(hc[0], hc[1]);
      *LDSP(float, sdst) = swap32_add(x1[0] + x1[1], fmaf(x1[0], x1[0], x1[1] * x1[1]));
    }
    STAMP(16);
    LDS_BARRIER();
    STAMP(17);
    {
      const f32x4 z = {0.f, 0.f, 0.f, 0.f};
      const u32x4 af0 = LDSP(u32x4, apa)[0];
      SB();
      const f32x4 s4 = LDSP(f32x4, ssrc)[0], q4 = LDSP(f32x4, ssrc)[8];
      const u32x4 af1 = LDSP(u32x4, apa)[32];
      SB();
      f32x4 d[4];
      d[0] = mfma16(af0, bf[0], z); d[1] = mfma16(af0, bf[1], z);
      SB();
      const f32x2 s2 = s4.xy + s4.zw, q2 = q4.xy + q4.zw;
      float s = s2[0] + s2[1], q = q2[0] + q2[1];
      SB();
      d[2] = mfma16(af0, bf[2], z);
      SB();
      s = swap16_add(s, q);
      SB();
      d[3] = mfma16(af0, bf[3], z);
      SB();
      s += dpp<XOR1>(s); s += dpp<XOR2>(s);
      SB();
      d[0] = mfma16(af1, bf[4], d[0]);
      SB();
      s += dpp<XOR7>(s); s += dpp<XOR8>(s);
      SB();
      d[1] = mfma16(af1, bf[5], d[1]);
      SB();
      const auto sq = __builtin_amdgcn_permlane16_swap(__float_as_uint(s), __float_as_uint(s), false, false);
      const float mean = u2f(sq[0]) * (1.f / 512.f);
      SB();
      d[2] = mfma16(af1, bf[6], d[2]);
      SB();
      const float var = fmaf(u2f(sq[1]), 1.f / 512.f, -mean * mean);
      SB();
      d[3] = mfma16(af1, bf[7], d[3]);
      SB();
      float ri = __builtin_amdgcn_rsqf(fmaxf(var, 0.f) + 1e-5f);
      float cf = fmaf(-ri * mean, Afp, Bfp);
      float rk = ri * -1.44269504f, ck = cf * -1.44269504f;
      PIN(ri); PIN(cf); PIN(rk); PIN(ck);
      SB();
      PIN(d[0]); PIN(d[1]);
      float y = d[0][0] + d[1][1];
      PIN(y); PIN(d[2]);
      y += d[2][2];
      PIN(y); PIN(d[3]);
      y += d[3][3];
      y = __builtin_bit_cast(float, __builtin_amdgcn_update_dpp(__builtin_bit_cast(int, y), __builtin_bit_cast(int, y), XOR8, 0xa, 0xf, false));
      y = swap16_add(y, y);
      const float ex = __builtin_amdgcn_exp2f(fmaf(y, rk, ck));
      y = fmaf(y, ri, cf);
      const float gu = y * dpp<0x104>(y);
      *LDSP(u16, adst) = (u16)pack_bf(gu * __builtin_amdgcn_rcpf(1.f + ex), 0.f);
    }
    STAMP(18);
    LDS_BARRIER();
    STAMP(10);
    {
      const u32x4 af = *LDSP(u32x4, asrc);
      const f32x4 z = {0.f, 0.f, 0.f, 0.f};
      f32x4 d0 = mfma16(af, b2[0], z), d1 = mfma16(af, b2[1], z), d2 = mfma16(af, b2[2], z), d3 = mfma16(af, b2[3], z);
      asm volatile("" : "+v"(d0), "+v"(d1), "+v"(d2), "+v"(d3));
      const bool g1 = g & 1, g2 = g & 2;
      const float e0 = g1 ? d1[0] : d0[0], e1 = g1 ? d1[1] : d0[1], f0 = g1 ? d3[0] : d2[0], f1 = g1 ? d3[1] : d2[1];
      const u32x2 ov = {pack_bf(g2 ? f0 : e0, g2 ? f1 : e1), tag};
      __builtin_amdgcn_raw_buffer_store_b64(ov, wsr, ooff, near ^ 8192u, AUX_SC1);
      __builtin_amdgcn_raw_buffer_store_b64(ov, wsr, ooff, near, AUX_SC1);
    }
    asm volatile(".Lend_b:");
    STAMP(6);
    FINISH();
  }
  if (wave == 0) {
    if (!all_ok((sg[0][1] == tag) & (sg[0][3] == tag) & (sg[1][1] == tag) & (sg[1][3] == tag))) {
      u32x4 t[2];
      gather16<2>(wsr, G_MOD + (1024 + fd) * 8, 1536 * 8, tag, t);
      sg[0] = t[0]; sg[1] = t[1];
    }
    u32 xa;
    {
      due = t1 + P.N2 * 256;
      const int tn = notices();
      due = tn + P.X3 * 256;
      until(due);
    }
    const bool hb = g & 1;
#pragma nounroll
    for (int spin = 0; spin < SPIN_MAX; ++spin) {
      CFENCE();
      xa = __builtin_amdgcn_raw_buffer_load_b32(wsr, dup(G_XP) + near + (g >> 1) * 16384 + (c >> 2) * 2048 + (g & 1) * 1024 + fd * 2, 0, AUX_SC1);
      if (all_ok(xa != SENT)) break;
      __builtin_amdgcn_s_sleep(1);
    }
    float a0 = lo_f(xa), a1 = hi_f(xa);
    a0 += dpp<0x124>(a0); a1 += dpp<0x124>(a1);
    a0 += dpp<XOR8>(a0); a1 += dpp<XOR8>(a1);
    a0 = swap32_add(a0, a0); a1 = swap32_add(a1, a1);
    f32x2 xv = {rbf(lo_f(sx2) + rbf((hb ? hi_f(sg[0][0]) : lo_f(sg[0][0])) * rbf(a0))), rbf(hi_f(sx2) + rbf((hb ? hi_f(sg[0][2]) : lo_f(sg[0][2])) * rbf(a1)))};
    f32x2 gv = {hb ? hi_f(sg[1][0]) : lo_f(sg[1][0]), hb ? hi_f(sg[1][2]) : lo_f(sg[1][2])};
    PIN(xv); PIN(gv);
    if (wi == 0 && lane == 0) __builtin_amdgcn_raw_buffer_store_b32(tag, wsr, WS_EPOCH, 0, AUX_SC1);
    STAMP(10);
    due = max(due + P.X4 * 256, (int)(((u32)wall_clock64() - t0) << 8) + P.GX * 256);
    const bool last = lane < (NUP - 32) * 4;
    u32 o0 = dup(G_OP + (lane >> 2) * 4096 + fd * 8) + near, o2 = last ? o0 : OOB, tg2 = last ? tag : 0u;
    const rsrc_t rout = mk_rsrc(out, 2048u);
    u32 oo = (lane & 44) == 0 ? (g * 512 + fd) * 2 : OOB;
    const u32 pk = (c & 10) ? 0u : ((c & 4) ? 0x3f800000u : 0x00003f80u);
    u32x4 one = {(c & 1) ? 0u : pk, 0u, (c & 1) ? pk : 0u, 0u};
    PIN(o0); PIN(o2); PIN(tg2); PIN(oo); PIN(one);
    __builtin_amdgcn_raw_buffer_store_b32(pack_bf(xv[0], xv[1]), rout, oo, 0, AUX_SC1);
    u32 s1_ = 131072, s2_ = 262144;
    asm volatile("" : "+s"(s1_), "+s"(s2_));
    until(due);
    f32x4 sv;
#pragma nounroll
    for (int spin = 0; spin < SPIN_MAX; ++spin) {
      CFENCE();
      const u32x4 og[3] = {ld16<AUX_SC1>(wsr, o0, 0), ld16<AUX_SC1>(wsr, o0, s1_), ld16<AUX_SC1>(wsr, o2, s2_)};
      sv = f32x4{0.f, 0.f, 0.f, 0.f};
#pragma unroll
      for (int k = 0; k < 3; ++k) sv = mfma16(one, og[k], sv);
      SB();
      u32 bad = ((og[0][1] ^ tag) | (og[0][3] ^ tag) | (og[1][1] ^ tag)) | ((og[1][3] ^ tag) | (og[2][1] ^ tg2) | (og[2][3] ^ tg2));
      PIN(bad);
      SB();
      float s0 = sv[0] + dpp<0x124>(sv[0]), s1 = sv[1] + dpp<0x124>(sv[1]);
      PIN(s0); PIN(s1);
      s0 += dpp<XOR8>(s0); s1 += dpp<XOR8>(s1);
      PIN(s0); PIN(s1);
      __builtin_amdgcn_raw_buffer_store_b32(pack_bf(fmaf(gv[0], s0, xv[0]), fmaf(gv[1], s1, xv[1])), rout, oo, 0, AUX_SC1);
      SB();
      if (all_ok(bad == 0u)) break;
      __builtin_amdgcn_s_sleep(1);
    }
    STAMP(7);
    asm volatile(".Lend_c:");
  }
  FINISH();
}

extern "C" void kernel_launch(void* const* d_in, const int* in_sizes, int n_in,
                              void* d_out, int out_size, void* d_ws, size_t ws_size,
                              hipStream_t stream) {
  fused_kernel<<<256, 512, 0, stream>>>((const u16*)d_in[1], (const u16*)d_in[2], (const u16*)d_in[4], (const u16*)d_in[3], (const u16*)d_in[10], (const u16*)d_in[6], (const u16*)d_in[7],
                                        (unsigned char*)d_ws, (const u16*)d_in[0], (const u16*)d_in[9], (const u16*)d_in[8], (u16*)d_out);
}
```

```cpp
#define PROBE_MUL 10
#include <hip/hip_runtime.h>
#include <stdint.h>

typedef unsigned int u32;
typedef unsigned short u16;
typedef unsigned long long u64;
typedef __attribute__((ext_vector_type(4))) unsigned u32x4;
typedef __attribute__((ext_vector_type(2))) unsigned u32x2;
typedef __attribute__((ext_vector_type(2))) __bf16 bf16x2;
typedef __attribute__((ext_vector_type(4))) float f32x4;
typedef __attribute__((ext_vector_type(2))) float f32x2;
typedef __amdgpu_buffer_rsrc_t rsrc_t;

constexpr int NH = 32, SK = 16384, DH = 128, WIN = 4096, FF = 1376;
struct Prm { int E0, ES, E1, E2, E2C, EU, TEND, DT_UP, L0, L1, X1, X2, X3, X4, NE, FE, M0, M1, M2, M3, M4, GS, GX, N1, N2, GA, WD, DT_WO, LW, LU, EW, PM, NF, NR, C0, A0, AG, AC, TS, TW, TK, TG, GB, B0, BG, BF, D0, DG, TC, LM, C1, F0, FG, FF_, W[9]; };
constexpr Prm PRM0 = {32, 20, 32, 32, 54, 56, 1976, 60, 12, 30, 86, 70, 100, 89, 3, 9, 0x2511f55b, 0x2b7425d5, 0x6017665, 0x17e0b9ec, 0x31944305, 62, 30, 36, 62, 30, 4, 100, 45, 15, 12, 0x0, 15, 14, 3, 30, 4, 4, 508, 220, 75, 6, 9, 74, 26, 8, 8, 3, 0, 120, 2, 67, 18, 4, 0x77777777, 0x77777777, 0x77777777, 0x77777777, 0x77777777, 0x44444477, 0x44444444, 0x44444444, 0x44444444};
constexpr int NL = 37;
constexpr int SK0 = 21, SK1 = 32;
constexpr int NCMIN = (NL - SK1 + SK0) * 8;
constexpr int WK = 8, WV = 5;
constexpr int WUSE = 12, LNS = 7;
constexpr int QP = 14, QD = 4;
constexpr int VQ = 6;
constexpr int NSL = (NL + 7) / 8;
constexpr int VS = VQ + NSL;
constexpr int MLS = VS + 2;
constexpr int VSIDE = MLS - 5, UPREP = MLS + 6;
constexpr int CPF = 17;
constexpr int SMO = 1;
constexpr int RVS = 5, RVP = 8, RVL = 12, RVU = 16;
constexpr int RPS = 24, RPP = 28;
constexpr int PRB = 12;
constexpr int TBS = 7, TBP = 10, TBL = 13, TBU = 17;
constexpr int window(int s, int w) {
  int n = w;
  for (int u = s - w + 1; u <= s; ++u) n += u == SMO ? 6 : (u == TBL || u == SK1 || u == NL + CPF || u == NL + RVL || u == NL + SK1 ? 1 : (u == QP || u == NL + VQ - 4 ? 2 : (u == NL + VSIDE ? 4 : 0)));
  return n;
}
constexpr int cons_end(int v) {
  const int a = v - WV + 1, c = (v - MLS) * 4;
  const int m = a < c ? a : c;
  return m < 0 ? 0 : (m > NL ? NL : m);
}
constexpr u32 col(const Prm& P, int j) { const int m[6] = {P.M0, P.M1, P.M2, P.M3, P.M4, P.PM}; return (u32)(m[j / 3] >> (j % 3 * 10) & 1023) << 10; }
constexpr u32 first_col(const Prm& P, int k) {
  if (k < 4) return col(P, 12 + k) | 1u << (24 + k);
  if (k < 6) return col(P, k - 4);
  if (k < 8) return col(P, 4 + k) | 1u << (16 + k);
  if (k < 11) return col(P, k - 1);
  return (col(P, 2) ^ col(P, 3) ^ col(P, 4) ^ col(P, 5) ^ col(P, 6) ^ col(P, 10) ^ col(P, 11)) | 0xf00000u;
}
constexpr bool one_to_one(const Prm& P) {
  u32 c[10] = {};
  for (int j = 0; j < 10; ++j) c[j] = col(P, j);
  for (int j = 0; j < 10; ++j) {
    if (c[j] == 0) return false;
    const u32 low = c[j] & (0u - c[j]);
    for (int k = j + 1; k < 10; ++k) if (c[k] & low) c[k] ^= c[j];
  }
  return true;
}
static_assert(one_to_one(PRM0), "every KB of the window exactly once");
struct Tab { u32 e[512]; };
constexpr Tab make_tab(const Prm& P) {
  Tab T{};
  for (int bid = 0; bid < 256; ++bid) {
    const int xcd = bid & 7, part = bid >> 4 & 3, i8 = (bid >> 3 & 1) | bid >> 6 << 1, wi = i8 * 8 + xcd;
    const bool has_mod = wi < 48, has_up = wi >= 21;
    const int cb0 = (i8 + P.C0) & 7, cb1 = (i8 + P.C1) & 7, ms = wi - 20;
    const int mi = wi < 8 ? wi : (wi < 16 ? 8 + ((xcd + 4) & 7) : (wi < 20 ? (wi - 14) * 8 + 3 : 16 + ms / 7 * 8 + ms % 7 + (ms % 7 >= 3 ? 1 : 0)));
    const int ui = xcd * 5 + (xcd > 5 ? xcd - 5 : 0) + (i8 == 6 ? 0 : (xcd >= 5 ? 1 : 0) + (i8 == 7 ? 4 : i8 - 2));
    const bool edge = i8 >= 6;
    const int wd = cb0 == 3 ? 0 : P.WD;
    const int e[4] = {P.E0 - 4 * wd + (cb0 == 3 ? P.ES : 0), P.E1 + (cb1 == 3 ? P.ES : 0), has_mod ? (wi < 16 ? P.E2C : P.E2) + ((mi & 7) == 3 ? P.ES : 0) : 0, has_up ? P.EU + (edge ? P.TC : 0) : 0};
    int tw[4] = {}, win[4] = {}, span[4] = {}, k0[4] = {};
    for (int k = 0; k < 4; ++k) {
      const int np = k == 0 ? 8 - wd : 8;
      tw[k] = np ? e[k] * P.TW / (16 * np) : 0;
      k0[k] = P.NF + tw[k] * np / 2 > P.TK ? P.NF + tw[k] * np / 2 : P.TK;
      span[k] = P.TEND - k0[k] - P.TG - (k == 0 ? (wd ? P.DT_WO : P.A0) : (k == 3 && has_up ? P.DT_UP : 0));
      win[k] = span[k] - (k == 0 && wd ? 0 : (k == 3 && has_up ? P.GA + (edge ? P.LM : 0) : 0));
    }
    int n[4] = {NCMIN / 8, NCMIN / 8, NCMIN / 8, NCMIN / 8};
    for (int left = 128 - NCMIN / 2; left > 0; --left) {
      int best = -1;
      for (int k = 3; k >= 0; --k)
        if (n[k] < NL && (best < 0 || n[k] * win[best] < n[best] * win[k])) best = k;
      ++n[best];
    }
    const int c[4] = {0, n[0] - 1, n[0] + n[1] - 2, n[0] + n[1] + n[2] - 3};
    const int nl = n[part];
    const bool has_w = part < 2 || (part == 2 ? has_mod : has_up);
    const int blk = !has_w ? 0 : (part == 0 ? cb0 : (part == 1 ? xcd * 8 + cb1 : (part == 2 ? mi : ui)));
    T.e[2 * bid] = (u32)(c[part] | nl << 7 | blk << 13 | (has_w ? 1 : 0) << 19 | (part == 0 ? wd : 0) << 20) | (u32)tw[part] << 24;
    T.e[2 * bid + 1] = (u32)(span[part] * P.TS / (4 * (nl - 1)) | span[part] * (1024 - P.TS) / (4 * (nl - 1)) << 16);
  }
  return T;
}
constexpr int step_range(const Prm& P, bool mx_) {
  int mx = 0, mn = 1 << 20;
  const Tab T = make_tab(P);
  for (int bid = 0; bid < 256; ++bid) { const int n = T.e[2 * bid] >> 7 & 63; mx = n > mx ? n : mx; mn = n < mn ? n : mn; }
  return mx_ ? mx : mn;
}
extern "C" __attribute__((used, visibility("protected"))) __constant__ const Tab wg_tab = make_tab(PRM0);
static_assert(step_range(PRM0, true) <= NL && step_range(PRM0, false) >= NCMIN / 8, "a wave's key range does not fit its load steps");
static_assert(UPREP + 1 < SK0 && QP + QD + 1 < SK0 && cons_end(SK0 - 1) == SK0 - WV && NL - SK1 == WV, "steps every wave runs");
constexpr int NUP = FF / 32;
constexpr int SPIN_MAX = 1 << 17;

constexpr u32 WS_EPOCH = 0;
constexpr u32 G_TB = 1024;
constexpr u32 G_MOD = 4096;
constexpr u32 G_Q = G_MOD + 3072 * 8;
constexpr u32 G_ML = G_Q + 4096 * 8;
constexpr u32 G_U = G_ML + 256 * 16;
constexpr u32 G_P = 0;
constexpr u32 G_XP = G_P + 256 * 256;
constexpr u32 G_OP = G_XP + 8 * 512 * 4;
constexpr u32 G_NT = G_OP + 43 * 512 * 8;
constexpr u32 G_PB = G_OP + 44 * 512 * 8;
constexpr u32 G_VT = G_U + 2 * (G_PB + 8192);
constexpr u32 WS_END = G_VT + 2048;
__device__ __forceinline__ u32 dup(u32 o) { return G_U + ((o >> 13) << 14) + (o & 8191u); }
constexpr u32 SENT = 0xffffffffu;
constexpr u32 OOB = 0x40000000u;
#define WST 0

constexpr int AUX_NT = 2, AUX_SC1 = 16;

__device__ __forceinline__ float lo_f(u32 v) { return __uint_as_float(v << 16); }
__device__ __forceinline__ float hi_f(u32 v) { return __uint_as_float(v & 0xffff0000u); }
__device__ __forceinline__ float rbf(float x) { return (float)(__bf16)x; }
__device__ __forceinline__ u32 pack_bf(float lo, float hi) {
  bf16x2 r; r[0] = (__bf16)lo; r[1] = (__bf16)hi; return __builtin_bit_cast(u32, r);
}
__device__ __forceinline__ float dot2(u32 k, u32 q, float acc) {
  return __builtin_amdgcn_fdot2_f32_bf16(__builtin_bit_cast(bf16x2, k), __builtin_bit_cast(bf16x2, q), acc, false);
}
template <int CTRL> __device__ __forceinline__ float dpp(float x) {
  return __builtin_bit_cast(float, __builtin_amdgcn_mov_dpp(__builtin_bit_cast(int, x), CTRL, 0xf, 0xf, true));
}
constexpr int XOR1 = 0xB1, XOR2 = 0x4E, XOR7 = 0x141, XOR8 = 0x128;
__device__ __forceinline__ float swap32_add(float a, float b) {
  auto t = __builtin_amdgcn_permlane32_swap(__float_as_uint(a), __float_as_uint(b), false, false);
  return __uint_as_float(t[0]) + __uint_as_float(t[1]);
}
__device__ __forceinline__ float swap16_add(float a, float b) {
  auto t = __builtin_amdgcn_permlane16_swap(__float_as_uint(a), __float_as_uint(b), false, false);
  return __uint_as_float(t[0]) + __uint_as_float(t[1]);
}
__device__ __forceinline__ float wave_sum(float x) {
  x += dpp<XOR1>(x); x += dpp<XOR2>(x); x += dpp<XOR7>(x); x += dpp<XOR8>(x);
  x = swap16_add(x, x);
  return swap32_add(x, x);
}
__device__ __forceinline__ float wave_max(float x) {
  x = fmaxf(x, dpp<XOR1>(x)); x = fmaxf(x, dpp<XOR2>(x)); x = fmaxf(x, dpp<XOR7>(x)); x = fmaxf(x, dpp<XOR8>(x));
  auto s = __builtin_amdgcn_permlane16_swap(__float_as_uint(x), __float_as_uint(x), false, false);
  x = fmaxf(__uint_as_float(s[0]), __uint_as_float(s[1]));
  auto t = __builtin_amdgcn_permlane32_swap(__float_as_uint(x), __float_as_uint(x), false, false);
  return fmaxf(__uint_as_float(t[0]), __uint_as_float(t[1]));
}
template <int CTRL> __device__ __forceinline__ float sel_add(float lo, float hi, bool up) {
  const float t1 = lo + dpp<CTRL>(lo), t2 = hi + dpp<CTRL>(hi);
  return up ? t2 : t1;
}
template <int I> __device__ __forceinline__ float bc16(float x) {
  return __builtin_bit_cast(float, __builtin_amdgcn_update_dpp(0, __builtin_bit_cast(int, x), 0x150 + I, 0xf, 0xf, false));
}
__device__ __forceinline__ float bcast16(float x, int i) {
  switch (i) {
    case 0: return bc16<0>(x); case 1: return bc16<1>(x); case 2: return bc16<2>(x); case 3: return bc16<3>(x);
    case 4: return bc16<4>(x); case 5: return bc16<5>(x); case 6: return bc16<6>(x); case 7: return bc16<7>(x);
    case 8: return bc16<8>(x); case 9: return bc16<9>(x); case 10: return bc16<10>(x); case 11: return bc16<11>(x);
    case 12: return bc16<12>(x); case 13: return bc16<13>(x); case 14: return bc16<14>(x); default: return bc16<15>(x);
  }
}
__device__ __forceinline__ float silu_f(float x) { return x * __builtin_amdgcn_rcpf(1.f + __expf(-x)); }

template <int AUX> __device__ __forceinline__ u32x4 ld16(rsrc_t r, u32 voff, u32 soff) {
  return __builtin_amdgcn_raw_buffer_load_b128(r, voff, soff, AUX);
}
template <int AUX> __device__ __forceinline__ u32x2 ld8(rsrc_t r, u32 voff, u32 soff) {
  return __builtin_amdgcn_raw_buffer_load_b64(r, voff, soff, AUX);
}
__device__ __forceinline__ void st8_sc1(rsrc_t r, u32 voff, u32 v, u32 tag) {
  __builtin_amdgcn_raw_buffer_store_b64(u32x2{v, tag}, r, voff, 0, AUX_SC1);
}
__device__ __forceinline__ void st16_sc1(rsrc_t r, u32 voff, u32 v0, u32 v1, u32 tag) {
  __builtin_amdgcn_raw_buffer_store_b128(u32x4{v0, tag, v1, tag}, r, voff, 0, AUX_SC1);
}
__device__ __forceinline__ rsrc_t mk_rsrc(const void* p, u32 bytes) {
  return __builtin_amdgcn_make_buffer_rsrc(const_cast<void*>(p), 0, bytes, 0x00020000);
}
#define WAIT_VM(n) __builtin_amdgcn_s_waitcnt(((n) & 15) | (((n) >> 4) << 14) | (7 << 4) | (15 << 8))
#define SB() __builtin_amdgcn_sched_barrier(0)
#define CFENCE() asm volatile("" ::: "memory")
__device__ __forceinline__ bool all_ok(bool ok) { return __builtin_amdgcn_ballot_w64(!ok) == 0ull; }

template <int N> __device__ __forceinline__ void gather16(rsrc_t ws, u32 voff, u32 stride, u32 tag, u32x4 (&g)[N]) {
#pragma nounroll
  for (int spin = 0; spin < SPIN_MAX; ++spin) {
    CFENCE();
    bool ok = true;
#pragma unroll
    for (int i = 0; i < N; ++i) g[i] = ld16<AUX_SC1>(ws, voff + i * stride, 0);
#pragma unroll
    for (int i = 0; i < N; ++i) ok &= (g[i][1] == tag) & (g[i][3] == tag);
    if (all_ok(ok)) break;
    __builtin_amdgcn_s_sleep(8);
  }
}
template <int N> __device__ __forceinline__ void gather8(rsrc_t ws, u32 voff, u32 stride, u32 tag, u32 (&g)[N], bool act = true) {
#pragma nounroll
  for (int spin = 0; spin < SPIN_MAX; ++spin) {
    CFENCE();
    bool ok = true;
    u32x2 t[N];
#pragma unroll
    for (int i = 0; i < N; ++i) t[i] = ld8<AUX_SC1>(ws, voff + i * stride, 0);
#pragma unroll
    for (int i = 0; i < N; ++i) { ok &= (t[i][1] == tag); g[i] = t[i][0]; }
    if (all_ok(ok | !act)) break;
    __builtin_amdgcn_s_sleep(8);
  }
}

struct Smem {
  float red[8][128];
  float ml[8][2];
  float r2[64 * 20];
  u16 hb[3 * 1024];
  float stat[8][64];
  float red2[8][128];
  u32 dummy[64];
  u32x2 bc;
  u32x4 zero[4];
  float pl[4][8];
  u32 mflag[8];
  u32x4 wbuf[8][8][64];
  __attribute__((aligned(256))) u16 w2b[32][512];
  u16 actl[2][32];
};

#define PROBE_K 8
#define PROBE_OP 0
#define PROBE_PART -1
#define STAMP(k) do { if ((k) == PROBE_K && tid == 0) pst = wall_clock64(); } while (0)
#define WSTAMP(k) do {} while (0)
#define FINISH() do { STAMP(8); if (tid == 0) { __hip_atomic_store(reinterpret_cast<u64*>(ws + (1u << 20)) + bid * 2, pt0, __ATOMIC_RELAXED, __HIP_MEMORY_SCOPE_AGENT); \
   __hip_atomic_store(reinterpret_cast<u64*>(ws + (1u << 20)) + bid * 2 + 1, (PROBE_PART < 0 || part == PROBE_PART) ? pst : 0ull, __ATOMIC_RELAXED, __HIP_MEMORY_SCOPE_AGENT); } \
   if (bid == 8 && wave == 0) probe_end(ws, pt0, lane); return; } while (0)
#define PIN(x) asm volatile("" : "+v"(x))
#define LDS_BARRIER() do { __builtin_amdgcn_s_waitcnt(0xc07f); __builtin_amdgcn_s_barrier(); CFENCE(); } while (0)
#define WAVE_SYNC() do { __builtin_amdgcn_fence(__ATOMIC_RELEASE, "wavefront"); __builtin_amdgcn_wave_barrier(); __builtin_amdgcn_fence(__ATOMIC_ACQUIRE, "wavefront"); } while (0)

__device__ __forceinline__ void dma16(rsrc_t r, u32 voff, u32 soff, u32 lds_dst) {
  u32 keep;
  asm volatile("s_mov_b32 %0, m0\n\ts_mov_b32 m0, %4\n\ts_nop 4\n\tbuffer_load_dwordx4 %1, %2, %3 offen nt lds\n\ts_mov_b32 m0, %0"
               : "=&s"(keep) : "v"(voff), "s"(r), "s"(soff), "s"(lds_dst) : "memory");
}
__device__ __forceinline__ void ld16_into(u32x4& d, rsrc_t r, u32 voff, u32 soff) {
  asm volatile("s_nop 4\n\tbuffer_load_dwordx4 %0, %1, %2, %3 offen nt" : "+v"(d) : "v"(voff), "s"(r), "s"(soff) : "memory");
}
__device__ __forceinline__ u32 perm_lo(u32 a, u32 b) { return __builtin_amdgcn_perm(b, a, 0x05040100u); }
__device__ __forceinline__ u32 perm_hi(u32 a, u32 b) { return __builtin_amdgcn_perm(b, a, 0x07060302u); }
__device__ __forceinline__ float rdl(float x, int l) { return __int_as_float(__builtin_amdgcn_readlane(__float_as_int(x), l)); }

typedef __attribute__((ext_vector_type(8))) __bf16 bf16x8;
__device__ __forceinline__ f32x4 mfma16(u32x4 a, u32x4 b, f32x4 c) {
  return __builtin_amdgcn_mfma_f32_16x16x32_bf16(__builtin_bit_cast(bf16x8, a), __builtin_bit_cast(bf16x8, b), c, 0, 0, 0);
}
__device__ __forceinline__ float sum8(const float* p) {
  const f32x4 a = *reinterpret_cast<const f32x4*>(p), b = *reinterpret_cast<const f32x4*>(p + 4);
  return ((a[0] + b[0]) + (a[1] + b[1])) + ((a[2] + b[2]) + (a[3] + b[3]));
}
__device__ __forceinline__ float ln_ri(float var) {
  return __builtin_amdgcn_rcpf(rbf(__builtin_amdgcn_sqrtf(rbf(rbf(fmaxf(var, 0.f)) + 1e-5f))));
}
__device__ __forceinline__ void ln_stats2(const float (&x0)[8], const float (&x1)[8], float& mb0, float& ri0, float& mb1, float& ri1) {
  const float k0 = rdl(x0[0], 0), k1 = rdl(x1[0], 0);
  float s0 = 0.f, s1 = 0.f, q0 = 0.f, q1 = 0.f;
#pragma unroll
  for (int k = 0; k < 8; ++k) {
    const float d0 = x0[k] - k0, d1 = x1[k] - k1;
    s0 += d0; s1 += d1; q0 = fmaf(d0, d0, q0); q1 = fmaf(d1, d1, q1);
  }
  float t = swap16_add(swap32_add(s0, s1), swap32_add(q0, q1));
  t += dpp<XOR1>(t); t += dpp<XOR2>(t); t += dpp<XOR7>(t); t += dpp<XOR8>(t);
  auto u = __builtin_amdgcn_permlane16_swap(__float_as_uint(t), __float_as_uint(t), false, false);
  const float ms = __uint_as_float(u[0]) * (1.f / 512.f), mq = __uint_as_float(u[1]) * (1.f / 512.f);
  const float ri = ln_ri(fmaf(-ms, ms, mq));
  mb0 = rbf(k0 + rdl(ms, 0)); mb1 = rbf(k1 + rdl(ms, 32));
  ri0 = rdl(ri, 0); ri1 = rdl(ri, 32);
}
template <int I> struct IC { static constexpr int v = I; };
template <int I, int N, class F> __device__ __forceinline__ void sfor(F&& f) {
  if constexpr (I < N) { if (f(IC<I>{})) sfor<I + 1, N>(f); }
}
typedef __attribute__((ext_vector_type(4))) short s16x4;
typedef __attribute__((address_space(3))) s16x4* lds_s16x4p;
__device__ __forceinline__ u32x2 tr_read(u32 lds_addr) {
  return __builtin_bit_cast(u32x2, __builtin_amdgcn_ds_read_tr16_b64_v4i16((lds_s16x4p)lds_addr));
}
__device__ __forceinline__ u32 next_tag(u32 e) { return ((e | 0x40004000u) + 1u) & ~0x40004000u; }
__device__ __forceinline__ float u2f(u32 v) { return __uint_as_float(v); }
#define LDSP(T, a) ((__attribute__((address_space(3))) T*)(a))
template <class T> __device__ __forceinline__ u32 lds_addr(T* p) { u32 a = (u32)(uintptr_t)p; PIN(a); return a; }

#define TAB_LOAD "s_add_u32 s30, s28, wg_tab@rel32@lo+4\ns_addc_u32 s31, s29, wg_tab@rel32@hi+12\nglobal_load_dwordx2 %[te], %[va], s[30:31]\n"
#define FC(k) ".Lfc" #k
#define FC_REGS
#define FX(r, bit, k) "s_bitcmp1_b32 %[" #r "], " #bit "\ns_cselect_b32 %[sb], " FC(k) ", 0\ns_xor_b32 %[sa], %[sa], %[sb]\n"
__device__ __forceinline__ void probe_end(unsigned char* ws, u64 pt0, int lane) {
  while (wall_clock64() < pt0 + 5500ull) __builtin_amdgcn_s_sleep(8);
  u64 a[4], c2[4];
  for (int i = 0; i < 4; ++i) {
    a[i] = __hip_atomic_load(reinterpret_cast<u64*>(ws + (1u << 20)) + (i * 64 + lane) * 2, __ATOMIC_RELAXED, __HIP_MEMORY_SCOPE_AGENT);
    c2[i] = __hip_atomic_load(reinterpret_cast<u64*>(ws + (1u << 20)) + (i * 64 + lane) * 2 + 1, __ATOMIC_RELAXED, __HIP_MEMORY_SCOPE_AGENT);
  }
  int mn0 = 1 << 30, mx1 = -(1 << 30), mn1 = 1 << 30, sum = 0, cnt = 0;
  for (int i = 0; i < 4; ++i) {
    const int d0 = (int)(a[i] - pt0);
    mn0 = min(mn0, d0);
    if (c2[i] != 0ull) { const int d1 = (int)(c2[i] - pt0); mx1 = max(mx1, d1); mn1 = min(mn1, d1); sum += d1; cnt += 1; }
  }
  for (int o = 1; o < 64; o <<= 1) { mn0 = min(mn0, __shfl_xor(mn0, o)); mx1 = max(mx1, __shfl_xor(mx1, o)); mn1 = min(mn1, __shfl_xor(mn1, o)); sum += __shfl_xor(sum, o); cnt += __shfl_xor(cnt, o); }
  int X = PROBE_OP == 0 ? mx1 : (PROBE_OP == 1 ? sum / max(cnt, 1) : mn1);
  if (PROBE_K == 102) X = mn0;
  X = mn0 + (X - mn0) * PROBE_MUL;
  const u64 tg = pt0 + (u64)(long long)(X + 8000);
  for (int i = 0; i < 400000 && wall_clock64() < tg; ++i) __builtin_amdgcn_s_sleep(1);
}
__global__ void __launch_bounds__(512, 2) fused_kernel(const u16* kc_, const u16* vc_, const u16* wo_, const u16* wq_, const u16* wmod_, const u16* w1_, const u16* w3_,
                                                       unsigned char* ws_, const u16* x_, const u16* cnd_, const u16* w2_, u16* out_) {
  __shared__ __attribute__((aligned(256))) Smem sm;
  const u64 a_ka = (u64)__builtin_amdgcn_kernarg_segment_ptr();
  const int bid = blockIdx.x, tid = threadIdx.x;
  u64 wa, t64; u32x2 te; int tid0;
  u32 ar[14];
  u32 tch2, sa_, sb_, va_;
  u32x4 kr[NL], vr[NL];
  asm volatile(".set .Lfc0, %0\n.set .Lfc1, %1\n.set .Lfc2, %2\n.set .Lfc3, %3\n.set .Lfc4, %4\n.set .Lfc5, %5\n.set .Lfc6, %6\n.set .Lfc7, %7\n.set .Lfc8, %8\n.set .Lfc9, %9\n.set .Lfc10, %10\n.set .Lfc11, %11"
    :: "n"(first_col(PRM0, 0)), "n"(first_col(PRM0, 1)), "n"(first_col(PRM0, 2)), "n"(first_col(PRM0, 3)), "n"(first_col(PRM0, 4)), "n"(first_col(PRM0, 5)),
       "n"(first_col(PRM0, 6)), "n"(first_col(PRM0, 7)), "n"(first_col(PRM0, 8)), "n"(first_col(PRM0, 9)), "n"(first_col(PRM0, 10)), "n"(first_col(PRM0, 11)));
  asm volatile(
    "s_load_dwordx16 s[4:19], %[ka], 0x0\ns_load_dwordx8 s[20:27], %[ka], 0x40\n"
    "s_memrealtime %[t]\n"
    "v_lshlrev_b32 %[va], 3, %[bid]\n"
    "s_getpc_b64 s[28:29]\n.Lcpf_first:\n"
    TAB_LOAD
    "s_cmp_gt_u32 %[bid], 15\ns_cbranch_scc1 .Lno_cpf\n"
    "s_mov_b32 %[sa], .Lend_a-.Lcpf_first\ns_max_i32 %[sa], %[sa], .Lend_b-.Lcpf_first\ns_max_i32 %[sa], %[sa], .Lend_c-.Lcpf_first\ns_max_i32 %[sa], %[sa], .Lend_d-.Lcpf_first\ns_max_i32 %[sa], %[sa], .Lend_e-.Lcpf_first\n"
    "v_lshlrev_b32 %[va], 6, %[tid]\nv_min_u32 %[va], %[sa], %[va]\nglobal_load_dword %[pf], %[va], s[28:29]\n"
    ".Lno_cpf:\n"
    "v_readfirstlane_b32 %[t0], %[tid]\nv_and_b32 %[va], 63, %[tid]\n"
    "s_mov_b32 %[sa], " FC(11) "\n"
    FX(bid, 0, 0) FX(bid, 1, 1) FX(bid, 2, 2) FX(bid, 3, 3) FX(bid, 4, 4) FX(bid, 5, 5) FX(bid, 6, 6) FX(bid, 7, 7) FX(t0, 6, 8) FX(t0, 7, 9) FX(t0, 8, 10)
    "v_lshl_add_u32 %[va], %[va], 4, %[sa]\n"
    "s_waitcnt lgkmcnt(0)\nglobal_load_dwordx4 %[k0], %[va], s[4:5] nt\nglobal_load_dwordx4 %[v0], %[va], s[6:7] nt\n"
    "s_bitcmp1_b32 %[t0], 6\ns_cselect_b64 s[14:15], s[16:17], s[14:15]\n"
    "s_bitcmp1_b32 %[bid], 4\ns_cselect_b64 s[8:9], s[10:11], s[8:9]\ns_cselect_b64 s[12:13], s[14:15], s[12:13]\ns_bitcmp1_b32 %[bid], 5\ns_cselect_b64 %[wa], s[12:13], s[8:9]"
    : "=&{s4}"(ar[0]), "=&{s5}"(ar[1]), "=&{s6}"(ar[2]), "=&{s7}"(ar[3]), "=&{s18}"(ar[4]), "=&{s19}"(ar[5]), "=&{s20}"(ar[6]), "=&{s21}"(ar[7]), "=&{s22}"(ar[8]), "=&{s23}"(ar[9]),
      "=&{s24}"(ar[10]), "=&{s25}"(ar[11]), "=&{s26}"(ar[12]), "=&{s27}"(ar[13]), [wa] "=&s"(wa), [t0] "=&s"(tid0), [t] "=&s"(t64), [te] "=&v"(te), [sa] "=&s"(sa_), [sb] "=&s"(sb_),
      [va] "=&v"(va_), [pf] "=&v"(tch2), [k0] "=&v"(kr[0]), [v0] "=&v"(vr[0])
    : [ka] "s"(a_ka), [bid] "s"(bid), [tid] "v"(tid) : "s8", "s9", "s10", "s11", "s12", "s13", "s14", "s15", "s16", "s17", "s28", "s29", "s30", "s31", "scc", "memory" FC_REGS);
  asm volatile("" : "+s"(ar[0]), "+s"(ar[1]), "+s"(ar[2]), "+s"(ar[3]), "+s"(ar[4]), "+s"(ar[5]), "+s"(ar[6]), "+s"(ar[7]), "+s"(ar[8]), "+s"(ar[9]), "+s"(ar[10]), "+s"(ar[11]), "+s"(ar[12]), "+s"(ar[13]));
  asm volatile("" : "+s"(wa), "+s"(t64), "+s"(tid0), "+s"(sa_));
  const u32 tka = (u32)wall_clock64();
  const u64 pt0 = t64; u64 pst = (PROBE_K == 100 || PROBE_K == 102) ? pt0 : (PROBE_K == 101 ? pt0 + (u32)(tka - (u32)t64) : 0);
  const u32 t0 = tka;
  __builtin_assume(tid >= 0 && tid < 512 && tid0 >= 0 && tid0 < 512 && bid >= 0 && bid < 256);
  constexpr Prm P = PRM0;
  const int TEND = P.TEND;
  const int xcd = bid & 7, part = bid >> 4 & 3;
  const int lane = tid & 63, wave = tid0 >> 6;
  SB();
  const bool p3 = part == 3;
  u32 pitch = (u32)(0x005600c001000020ull >> (part * 16) & 0xffff) * 32u;
  const int sh = p3 ? 2 : 3;
  u32 wv0 = (u32)((p3 ? (wave >> 1) * 128 : wave * 64) + (part == 0 ? xcd * 512 : 0) + (lane >> sh)) * pitch + ((lane & (p3 ? 3 : 7)) ^ (lane >> (p3 ? 5 : 3))) * 16u;
  const u32 wimg = (u32)(uintptr_t)(&sm.wbuf[0][0][0]);
  u32 wsz = (part == 0 ? 4096u : 512u) * pitch, wld = wimg + (p3 ? (wave & 1) * 32768 + (wave >> 1) * 8192 : wave * 8192);
  PIN(wv0);
  asm volatile("" : "+s"(wsz), "+s"(wld), "+s"(pitch));
  auto karg = [&](int i) { return ar[2 * i] | (u64)ar[2 * i + 1] << 32; };
  const u64 a_kc = karg(0), a_vc = karg(1);
  unsigned char* ws = (unsigned char*)karg(2);
  const u16 *x = (const u16*)karg(3), *cnd = (const u16*)karg(4), *w2 = (const u16*)karg(5);
  u16* out = (u16*)karg(6);
  const rsrc_t wsr = mk_rsrc(ws, WS_END);
  asm volatile("s_waitcnt vmcnt(2)" : "+v"(te));
  const u32 te0 = __builtin_amdgcn_readfirstlane(te[0]), te1 = __builtin_amdgcn_readfirstlane(te[1]);
  const int bl0 = te0 & 127, nl = te0 >> 7 & 63, blk = te0 >> 13 & 63;
  const bool has_w = te0 >> 19 & 1;
  const rsrc_t rw = mk_rsrc((const void*)wa, wsz);
  const u32 wstep = pitch << (6 - sh), wflip = p3 ? 32u : 0u;
  u32 wso = blk << (sh + 4);
  int wleft = 8;
  auto piece = [&]() {
    dma16(rw, wv0, wso, wld);
    wso += wstep; wld += 1024;
    wv0 ^= (wleft & 1) ? wflip : 0u;
    --wleft;
  };
  SB();
  STAMP(23);
  const int b = bid >> 3 & 1, head = xcd * 4 + (bid >> 6 ^ 3);
  const int g = lane >> 4, c = lane & 15;
  const int i8 = b | bid >> 6 << 1, wi = i8 * 8 + xcd;
  const bool is_up = p3 && has_w, modc = part == 2 && wi < 16;
  const size_t base = ((size_t)(b * NH + head) * SK + (SK - WIN)) * DH;
  const rsrc_t rk = mk_rsrc((const u16*)a_kc + base, WIN * DH * 2), rv = mk_rsrc((const u16*)a_vc + base, WIN * DH * 2);
  int tau = te1 & 0xffff;
  const int tauw = te0 >> 24 << 7, wdef = te0 >> 20 & 15;
  int due = P.NF * 256, now = 0;
  auto gate = [&]() {
    while (now < due) { now = (int)(((u32)wall_clock64() - t0) << 8); if (now < due) __builtin_amdgcn_s_sleep(1); }
    due += tau;
  };
  auto until = [&](int t) { while ((int)(((u32)wall_clock64() - t0) << 8) < t); };
  const u32 pslot = dup(G_P + head * 2048 + (b * 4 + part) * 16);
  const u32 die = __builtin_amdgcn_s_getreg(20 | (2 << 6));
  const bool voter = p3 && i8 == 1, timer = p3 && i8 < 2;
  int vote = 0, lag = 0, shift = 0;
  const u32 pbo = dup(G_PB) + (xcd * 8 + wave) * 128;
  u32 near = 0;
  u32x2 ep; u32 xs0, xs1, tch1; u32x4 xr0, xr1;
  const rsrc_t rx = mk_rsrc(part == 2 ? cnd : x, (part == 1 || (part == 2 && has_w)) ? 2048u : 0u);
  auto small_ops = [&]() {
    ep = ld8<AUX_SC1>(wsr, wave == 7 ? WS_EPOCH : OOB, 0);
    xs0 = __builtin_amdgcn_raw_buffer_load_b16(rx, tid * 2, 0, 0); xs1 = __builtin_amdgcn_raw_buffer_load_b16(rx, tid * 2, 1024, 0);
    xr0 = ld16<0>(rx, part == 1 ? lane * 16 : OOB, 0); xr1 = ld16<0>(rx, part == 1 ? lane * 16 : OOB, 1024);
    tch1 = __builtin_amdgcn_raw_buffer_load_b32(mk_rsrc(w2 + (size_t)blk * 32 * 512, is_up ? 64u : 0u), tid == 384 ? 0 : OOB, 0, AUX_NT);
  };
  if (has_w) {
    if (modc) now = 1 << 30;
#pragma nounroll
    while (wleft > wdef) { gate(); due += tauw - tau; piece(); }
    now = 0;
  }
  due = max(due, P.TK * 256);
  SB();
  u32 sot = sa_ & 0xffc00u;
  {
    const u32 s1 = (bl0 + lane - 1 - (lane >= SK0 ? NL - nl : 0)) ^ (124 + part);
#pragma unroll
    for (int k = 0; k < 7; ++k) sot ^= (u32)-(int)(s1 >> k & 1u) & col(P, k);
    PIN(sot);
  }
  u32 tag = 0;
  u32x4 bf[8];
  auto wo_prep = [&](int s0 = 0, int s1 = 2) {
    const u32 wi_ = wimg + wave * 8192;
    const int q = (lane >> 2) & 3, pp = lane & 3;
#pragma unroll
    for (int s = s0; s < s1; ++s) {
#pragma unroll
      for (int t = 0; t < 4; ++t) {
        const int r0 = s * 32 + g * 8 + q, r1 = r0 + 4, ch = t * 2 + (pp >> 1);
        const u32x2 lo = tr_read(wi_ + r0 * 128 + ((ch ^ (r0 & 7)) * 16) + (pp & 1) * 8), hi = tr_read(wi_ + r1 * 128 + ((ch ^ (r1 & 7)) * 16) + (pp & 1) * 8);
        bf[s * 4 + t] = u32x4{lo[0], lo[1], hi[0], hi[1]};
      }
    }
  };
  auto gemv_go = [&](u32 hh) {
    sm.hb[tid] = (u16)hh; sm.hb[512 + tid] = (u16)(hh >> 16);
    WAVE_SYNC();
    const u32x4* a = (c & 11) == 0 ? reinterpret_cast<const u32x4*>(&sm.hb[(c >> 2) * 512 + wave * 64 + g * 8]) : &sm.zero[0];
    const u32x4 a0 = a[0], a1 = a[(c & 11) == 0 ? 4 : 0];
    const f32x4 z = {0.f, 0.f, 0.f, 0.f};
    f32x4 d[4];
#pragma unroll
    for (int t = 0; t < 4; ++t) d[t] = mfma16(a0, bf[t], z);
#pragma unroll
    for (int t = 0; t < 4; ++t) d[t] = mfma16(a1, bf[4 + t], d[t]);
    if (lane < 32) {
#pragma unroll
      for (int t = 0; t < 4; ++t) sm.r2[(t * 16 + c) * 20 + g * 8 + wave] = d[t][0];
    }
  };
  auto mod_stage = [&]() {
    wo_prep();
    gemv_go(pack_bf(silu_f(lo_f(xs0)), silu_f(lo_f(xs1))));
    __builtin_amdgcn_s_waitcnt(0xc07f);
    if (lane == 0) *LDSP(volatile u32, (u32)(uintptr_t)&sm.mflag[wave]) = 1u;
    if (wave == 7) {
#pragma nounroll
      for (int spin = 0; spin < SPIN_MAX; ++spin) {
        const u32 f = *LDSP(volatile u32, (u32)(uintptr_t)&sm.mflag[lane & 7]);
        if (all_ok(f != 0u)) break;
        __builtin_amdgcn_s_sleep(1);
      }
      CFENCE();
      st8_sc1(wsr, G_MOD + (blk * 64 + lane) * 8, pack_bf(sum8(&sm.r2[lane * 20]), sum8(&sm.r2[lane * 20 + 8])), tag);
    }
    STAMP(9);
  };
  if (lane < 4) sm.zero[lane] = u32x4{0u, 0u, 0u, 0u};
  if (part == 2 && has_w) {
    if (tid < 8) sm.mflag[tid] = 0u;
    LDS_BARRIER();
    if (__builtin_expect(modc, 0)) {
      small_ops();
      __builtin_amdgcn_raw_buffer_store_b128(u32x4{SENT, SENT, SENT, SENT}, wsr, G_U + wi * 10240 + tid * 16, 0, AUX_SC1);
      __builtin_amdgcn_raw_buffer_store_b128(u32x4{SENT, SENT, SENT, SENT}, wsr, tid < 128 ? G_U + wi * 10240 + tid * 16 : OOB, 8192, AUX_SC1);
      asm volatile("s_waitcnt vmcnt(0)" ::: "memory");
      tag = next_tag(__builtin_amdgcn_readfirstlane(ep[0]));
      mod_stage();
      __builtin_amdgcn_s_barrier();
    }
  }
  SB();
  const int fd = wi * 8 + (lane & 3) * 2;
  const int ub = wave >> 2, ud = (wave & 3) * 128 + c * 8 + g * 2;
  u32x4 sg[3]; u32 sx2;
  auto side_loads = [&]() {
    const bool fin = part == 1 && wave == 0;
    sg[0] = ld16<AUX_SC1>(wsr, is_up ? G_MOD + (1024 + ud) * 8 : (fin ? G_MOD + (1024 + fd) * 8 : OOB), 0);
    sg[1] = ld16<AUX_SC1>(wsr, is_up ? G_MOD + (1536 + ud) * 8 : (fin ? G_MOD + (2560 + fd) * 8 : OOB), 0);
    sg[2] = ld16<AUX_SC1>(wsr, is_up ? G_MOD + (2048 + ud) * 8 : OOB, 0);
    sx2 = __builtin_amdgcn_raw_buffer_load_b32(mk_rsrc(x, (is_up || fin) ? 2048u : 0u), is_up ? ub * 1024 + ud * 2 : (g & 1) * 1024 + fd * 2, 0, 0);
  };
  auto side_pin = [&]() { PIN(sg[0]); PIN(sg[1]); PIN(sg[2]); PIN(sx2); };
  const u32 mlo = G_ML + (head * 2 + b) * 64 + (lane & 3) * 16;
  float fmrg = 0.f;
  auto merge_factor = [&](const u32x4& ml) {
    const float mi = u2f(ml[0]);
    float M = fmaxf(mi, dpp<XOR1>(mi)); M = fmaxf(M, dpp<XOR2>(M));
    const float e = __expf(mi - M);
    float L = e * u2f(ml[2]);
    L += dpp<XOR1>(L); L += dpp<XOR2>(L);
    fmrg = rdl(e * __builtin_amdgcn_rcpf(L), part);
  };
  f32x2 ga = {0.f, 0.f}, xin = {0.f, 0.f}, cc1 = {0.f, 0.f};
  float Af = 0.f, Bf = 0.f;
  const int hpos = ub * 512 + ud;
  auto up_prep = [&]() {
    if (!all_ok((sg[0][1] == tag) & (sg[0][3] == tag) & (sg[1][1] == tag) & (sg[1][3] == tag) & (sg[2][1] == tag) & (sg[2][3] == tag))) gather16<3>(wsr, G_MOD + (1024 + ud) * 8, 512 * 8, tag, sg);
    ga = f32x2{ub ? hi_f(sg[0][0]) : lo_f(sg[0][0]), ub ? hi_f(sg[0][2]) : lo_f(sg[0][2])};
    cc1 = f32x2{rbf(1.f + (ub ? hi_f(sg[2][0]) : lo_f(sg[2][0]))), rbf(1.f + (ub ? hi_f(sg[2][2]) : lo_f(sg[2][2])))};
    xin = f32x2{lo_f(sx2), hi_f(sx2)};
    u32* hp = reinterpret_cast<u32*>(&sm.hb[hpos]);
    hp[512] = pack_bf(cc1[0], cc1[1]);
    hp[1024] = ub ? perm_hi(sg[1][0], sg[1][2]) : perm_lo(sg[1][0], sg[1][2]);
  };
  auto up_frags = [&]() {
    const int pp = lane & 3;
    const u32 a = wimg + (pp & 1) * 32768 + ((pp >> 1) * 32 + g * 8 + ((lane >> 2) & 3)) * 64 + (((wave >> 1) ^ ((g & 1) | (pp & 2))) * 16) + (wave & 1) * 8;
#pragma unroll
    for (int rb = 0; rb < 8; ++rb) {
      const u32x2 lo = tr_read(a + rb * 4096), hi = tr_read(a + rb * 4096 + 256);
      bf[rb] = u32x4{lo[0], lo[1], hi[0], hi[1]};
    }
  };
  const u32x4* ap = reinterpret_cast<const u32x4*>(&sm.hb[((lane >> 3) & 1) * 512 + (lane & 1) * 32 + g * 8]);
  const bool par = lane & 8;
  auto limit = [&](auto sc) {
    constexpr int s = sc.v;
    WAIT_VM(window(s, 1 + (PRM0.W[s >> 3] >> (4 * (s & 7)) & 7)));
    SB();
  };
  auto sample = [&]() { if (timer) lag += (int)(((u32)wall_clock64() - t0) << 8) - due + ((int)__builtin_amdgcn_s_getreg(7 | (3 << 11)) + 1) * tau; };
  auto report = [&](u32 dst) {
    if (timer) {
      if (lane == 0) sm.mflag[wave] = vote * 65536 + min(lag >> 8, 4000);
      lag = 0;
      LDS_BARRIER();
      if (wave == 7) {
        int s = sm.mflag[lane & 7];
        s += __builtin_amdgcn_mov_dpp(s, XOR1, 0xf, 0xf, true); s += __builtin_amdgcn_mov_dpp(s, XOR2, 0xf, 0xf, true); s += __builtin_amdgcn_mov_dpp(s, XOR7, 0xf, 0xf, true);
        if (lane < 8) st8_sc1(wsr, dst + lane * 128 + wi * 8, s, tag);
      }
    }
    SB();
  };
  float l0 = 0.f, l1 = 0.f;
  u32x2 gmd[2], tbg;
  int t1 = 0;
#pragma unroll
  for (int i = SK0; i < SK1; ++i) kr[i] = u32x4{0u, 0u, 0u, 0u};
  const int first = SK0 + NL - nl;
  auto left_out = [&](int i) { int f = first; asm volatile("" : "+s"(f)); return i < f; };
  sfor<1, NL>([&](auto ic) {
    constexpr int i = ic.v;
    if constexpr (i >= SK0 && i < SK1) { if (left_out(i)) return true; }
    const u32 so = __builtin_amdgcn_readlane(sot, i);
    gate();
    if constexpr ((i >= TBS && i <= TBP) || (i >= RPS && i <= RPP)) sample();
    SB();
    if constexpr (i >= SK0 && i < SK1) ld16_into(kr[i], rk, lane * 16, so); else kr[i] = ld16<AUX_NT>(rk, lane * 16, so);
    if constexpr (i == SMO) small_ops();
    if constexpr (i == SMO + 1) { if (voter) __builtin_amdgcn_raw_buffer_store_b32(0u, wsr, pbo + (lane >> 5) * 8192 + (lane & 31) * 4, 0, AUX_SC1); }
    if constexpr (i == SK1) tbg = ld8<AUX_SC1>(wsr, wave == 7 ? G_VT + xcd * 128 + c * 8 : OOB, 0);
    if constexpr (i == TBL) tbg = ld8<AUX_SC1>(wsr, wave == 7 ? G_TB + xcd * 128 + c * 8 : OOB, 0);
    if constexpr (i == QP) { gmd[0] = ld8<AUX_SC1>(wsr, part == 1 ? G_MOD + tid * 8 : OOB, 0); gmd[1] = ld8<AUX_SC1>(wsr, part == 1 ? G_MOD + tid * 8 : OOB, 4096); }
    SB();
    SB();
    if (i >= 4) limit(ic);
    if (i == 1) STAMP(25);
    if (i == 8) STAMP(12);
    if (i == 20) STAMP(21);
    if (i == 32) STAMP(22);
    if (i == TBP) {
      if (timer) {
        if (lane == 0) sm.mflag[wave] = lag;
        lag = 0;
        LDS_BARRIER();
        if (wave == 7) {
          int s = sm.mflag[lane & 7];
          s += __builtin_amdgcn_mov_dpp(s, XOR1, 0xf, 0xf, true); s += __builtin_amdgcn_mov_dpp(s, XOR2, 0xf, 0xf, true); s += __builtin_amdgcn_mov_dpp(s, XOR7, 0xf, 0xf, true);
          if (lane < 8) st8_sc1(wsr, G_TB + lane * 128 + wi * 8, t0 << 10 | (u32)min(s / (2048 * (TBP - TBS + 1)), 1023), tag);
        }
      }
      SB();
    }
    if (i == PRB) {
      if (voter) {
        u64 ta, tb, tc; u32 dm;
        const u32 pa = pbo + (wave & 1) * 8192;
        asm volatile("s_memtime %0\n\ts_waitcnt lgkmcnt(0)\n\ts_load_dword %3, %4, %5\n\ts_waitcnt lgkmcnt(0)\n\ts_memtime %1\n\ts_waitcnt lgkmcnt(0)\n\t"
                     "s_load_dword %3, %4, %6\n\ts_waitcnt lgkmcnt(0)\n\ts_memtime %2\n\ts_waitcnt lgkmcnt(0)"
                     : "=&s"(ta), "=&s"(tb), "=&s"(tc), "=&s"(dm) : "s"(ws), "s"(pa), "s"(pa ^ 8192u));
        const int d = min(max((int)(2 * (u32)tb - (u32)ta - (u32)tc), -500), 500);
        vote = ((wave ^ die) & 1) ? -d : d;
        STAMP(26);
      }
      SB();
    }
    if (i == TBU) {
      if (wave == 7) {
        const bool ok = tbg[1] == tag;
        float lf = ok ? (float)min(max((int)((t0 << 10 | 1023u) - tbg[0]) >> 10, -300), 300) : 0.f, nf = ok ? 1.f : 0.f, lg = ok ? (float)(tbg[0] & 1023u) : 0.f;
        lf += dpp<XOR1>(lf); nf += dpp<XOR1>(nf); lg += dpp<XOR1>(lg); lf += dpp<XOR2>(lf); nf += dpp<XOR2>(nf); lg += dpp<XOR2>(lg);
        lf += dpp<XOR7>(lf); nf += dpp<XOR7>(nf); lg += dpp<XOR7>(lg); lf += dpp<XOR8>(lf); nf += dpp<XOR8>(nf); lg += dpp<XOR8>(lg);
        const float rn = __builtin_amdgcn_rcpf(fmaxf(nf, 1.f));
        shift = __builtin_amdgcn_readfirstlane(min(max((int)((lg * rn - (float)P.GS) * (float)P.GB * 0.125f), 0), 150));
        if (lane == 0) sm.bc = u32x2{tag, t0 - (u32)(int)(lf * rn) + shift};
      }
      LDS_BARRIER();
      const u32x2 bc = sm.bc;
      tag = __builtin_amdgcn_readfirstlane(bc[0]);
      t1 = (__builtin_amdgcn_readfirstlane((int)(bc[1] - t0)) + TEND + P.X1) * 256;
      tau -= __builtin_amdgcn_readfirstlane((int)((float)(int)(t0 - bc[1]) * 256.f * __builtin_amdgcn_rcpf((float)(nl - TBU - 1))));
      SB();
    }
    if (i == RPP) report(G_VT);
    if (i == LNS) {
      tag = next_tag(__builtin_amdgcn_readfirstlane(ep[0]));
      if (part == 1) {
        float x0[8], x1[8], mb0, ri0, mb1, ri1;
#pragma unroll
        for (int k = 0; k < 4; ++k) { x0[2 * k] = lo_f(xr0[k]); x0[2 * k + 1] = hi_f(xr0[k]); x1[2 * k] = lo_f(xr1[k]); x1[2 * k + 1] = hi_f(xr1[k]); }
        ln_stats2(x0, x1, mb0, ri0, mb1, ri1);
        l0 = rbf(rbf(lo_f(xs0) - mb0) * ri0); l1 = rbf(rbf(lo_f(xs1) - mb1) * ri1);
      }
      PIN(l0); PIN(l1);
      SB();
    }
    if (i == WUSE) {
      PIN(tch1); PIN(tch2);
      if ((part == 1 || (part == 2 && has_w && !modc))) {
        WAIT_VM(WK); CFENCE();
        if (part == 2) mod_stage(); else wo_prep();
      }
#pragma unroll
      for (int k = 0; k < 8; ++k) PIN(bf[k]);
      SB();
    }
    if (i == QP + QD) {
      if (part == 1) {
        u32 gm[2] = {gmd[0][0], gmd[1][0]};
        if (!all_ok((gmd[0][1] == tag) & (gmd[1][1] == tag))) { STAMP(11); gather8<2>(wsr, G_MOD + tid * 8, 512 * 8, tag, gm); }
        STAMP(5);
        gemv_go(pack_bf(rbf(l0 * rbf(1.f + lo_f(gm[1]))) + lo_f(gm[0]), rbf(l1 * rbf(1.f + hi_f(gm[1]))) + hi_f(gm[0])));
      }
      SB();
    }
    if (i == QP + QD + 1) {
      if (part == 1) {
        LDS_BARRIER();
        if (wave == 0) {
          const int bb = lane >> 5, jq = lane & 31;
          st8_sc1(wsr, G_Q + (bb * 2048 + blk * 32 + jq) * 8, pack_bf(sum8(&sm.r2[2 * jq * 20 + bb * 8]), sum8(&sm.r2[(2 * jq + 1) * 20 + bb * 8])), tag);
        }
        STAMP(6);
      }
      SB();
    }
    return true;
  });
  PIN(sot);
  if (wave == 7) {
    const bool ok = tbg[1] == tag;
    float vt = ok ? (float)((int)tbg[0] >> 16) : 0.f, lg = ok ? (float)(tbg[0] & 0xffffu) : 0.f, nf = ok ? 8.f * (RPP - RPS + 1) : 0.f;
    vt += dpp<XOR1>(vt); lg += dpp<XOR1>(lg); nf += dpp<XOR1>(nf); vt += dpp<XOR2>(vt); lg += dpp<XOR2>(lg); nf += dpp<XOR2>(nf);
    vt += dpp<XOR7>(vt); lg += dpp<XOR7>(lg); nf += dpp<XOR7>(nf); vt += dpp<XOR8>(vt); lg += dpp<XOR8>(lg); nf += dpp<XOR8>(nf);
    lg *= __builtin_amdgcn_rcpf(fmaxf(nf, 1.f));
    if (lane == 0) sm.bc = u32x2{(__float_as_uint(vt) & 0x80000000u) | (u32)min(shift * P.DG * 32, P.EW * 256), (u32)min(max((int)((lg - (float)P.B0) * (float)(P.BG * 32)) - shift * P.D0 * 32, 0), 200 * 256)};
  }
  LDS_BARRIER();
  {
    const u32x2 bc = sm.bc;
    const int st = __builtin_amdgcn_readfirstlane(bc[1]), sp = (st * P.BF) >> 3;
    near = ((__builtin_amdgcn_readfirstlane(bc[0]) >> 31 ^ die ^ P.NR ^ 1) & 1) * 8192u;
    t1 += st + (__builtin_amdgcn_readfirstlane(bc[0]) & 0xffff);
    due += P.TG * 256 + sp;
    tau = (te1 >> 16) + __builtin_amdgcn_readfirstlane((int)((float)(st - sp) * __builtin_amdgcn_rcpf((float)(nl - 3))));
  }
  const u32 qoff = G_Q + ((b * 32 + head) * 64 + c * 4) * 8;
  u32x4 qg[2], mlg;
  u32 qq[4];
  constexpr int NB = (NL + 15) / 16;
  float xlo, sc[NB], pr[NB], m = 0.f;
  f32x4 acc = {0.f, 0.f, 0.f, 0.f};
  u32 pb[NB], pm[4], pl8[8], cpf;
  const u32 adst0 = lds_addr(lane < 32 ? &sm.red[wave][c * 8 + g * 4] : &sm.r2[(lane - 32) * 4]);
  u32 po, rdst, rsrc, xo;
  auto wo_addr = [&]() {
    po = dup(G_P + xcd * 8192) + tid * 16 + near;
    rdst = lane < 32 ? (u32)(uintptr_t)&sm.red2[wave][0] + lane * 16 : (u32)(uintptr_t)&sm.r2[0] + (lane - 32) * 16;
    rsrc = (u32)(uintptr_t)&sm.red2[0][((wave & 1) * 16 + (lane & 15)) * 4 + (lane >> 4)];
    xo = dup(G_XP + xcd * 2048 + (wave & 1) * 1024 + (blk * 64 + (lane & ~1)) * 2) + (lane & 1) * 8192;
    PIN(po); PIN(rdst); PIN(rsrc); PIN(xo);
  };
  u32 pso = (wave == 0 && (g & 1) == 0) ? pslot + (c >> 3) * 1024 + (c & 3) * 256 + (c >> 2 & 1) * 128 + (g >> 1) * 8192 : OOB;
  PIN(pso);
  const u32 nadd = wdef ? P.LW : (is_up ? P.LU : 0);
  u32 nto = tid < 2 ? dup(G_NT) + bid * 8 + tid * 8192 : OOB;
  PIN(nto);
  auto notices = [&]() -> int {
    u32 off = dup(G_NT) + near + lane * 16;
    PIN(off);
    until(due);
    int mx;
#pragma nounroll
    for (int spin = 0; spin < SPIN_MAX; ++spin) {
      CFENCE();
      const u32x4 ga_ = ld16<AUX_SC1>(wsr, off, 0), gb_ = ld16<AUX_SC1>(wsr, off, 1024);
      mx = max(max((int)(ga_[0] - t0), (int)(ga_[2] - t0)), max((int)(gb_[0] - t0), (int)(gb_[2] - t0)));
      if (all_ok((((ga_[1] ^ tag) | (ga_[3] ^ tag)) | ((gb_[1] ^ tag) | (gb_[3] ^ tag))) == 0u)) break;
      for (int k = 0; k < P.AG; ++k) __builtin_amdgcn_s_sleep(1);
    }
    if (all_ok(mx <= (t1 >> 8) - P.AC)) return t1;
    mx = max(mx, __builtin_amdgcn_mov_dpp(mx, XOR1, 0xf, 0xf, true)); mx = max(mx, __builtin_amdgcn_mov_dpp(mx, XOR2, 0xf, 0xf, true));
    mx = max(mx, __builtin_amdgcn_mov_dpp(mx, XOR7, 0xf, 0xf, true)); mx = max(mx, __builtin_amdgcn_mov_dpp(mx, XOR8, 0xf, 0xf, true));
    mx = max(max(__builtin_amdgcn_readlane(mx, 0), __builtin_amdgcn_readlane(mx, 16)), max(__builtin_amdgcn_readlane(mx, 32), __builtin_amdgcn_readlane(mx, 48)));
    return max(min(mx, 1 << 20) * 256, t1 + P.L0 * 256) + P.L1 * 256;
  };
  auto pv_row = [&](auto jc) {
    constexpr int i = jc.v;
    const u32 pv = __float_as_uint(bcast16(u2f(pb[i >> 4]), i & 15));
    acc = mfma16(u32x4{pv & pm[0], pv & pm[1], pv & pm[2], pv & pm[3]}, vr[i], acc);
    PIN(acc);
    return true;
  };
  sfor<0, NL - 1>([&](auto ic) {
    constexpr int v = ic.v;
    if constexpr (v >= SK0 && v < SK1) { if (left_out(v)) return true; }
    if constexpr (v > 0) {
      const u32 so = __builtin_amdgcn_readlane(sot, v);
      if constexpr (v >= SK1) { if (v == NL - P.NE) tau = (tau * P.FE) >> 4; }
      gate();
      if constexpr (v >= RVS && v <= RVP) sample();
      SB();
      if constexpr (v >= SK0 && v < SK1) ld16_into(vr[v], rv, lane * 16, so); else vr[v] = ld16<AUX_NT>(rv, lane * 16, so);
      SB();
      SB();
    }
    if constexpr (v == SK0 - 1) {
#pragma unroll
      for (int i = SK0; i < SK1; ++i) { vr[i] = u32x4{0u, 0u, 0u, 0u}; PIN(vr[i]); }
    }
    if constexpr (v == VQ - 4) { qg[0] = ld16<AUX_SC1>(wsr, qoff, 0); qg[1] = ld16<AUX_SC1>(wsr, qoff + 16, 0); }
    if constexpr (v == VSIDE) side_loads();
    if constexpr (v == CPF) {
      u64 pc; u32 dlt, len;
      asm volatile("s_getpc_b64 %0\n.Lcpf_here:\n\ts_mov_b32 %1, .Ltail_start-.Lcpf_here\n\ts_mov_b32 %2, .Lend_a-.Ltail_start\n\ts_max_i32 %2, %2, .Lend_b-.Ltail_start\n\ts_max_i32 %2, %2, .Lend_c-.Ltail_start\n\ts_max_i32 %2, %2, .Lend_d-.Ltail_start\n\ts_max_i32 %2, %2, .Lend_e-.Ltail_start" : "=s"(pc), "=s"(dlt), "=s"(len) :: "scc");
      pc += dlt;
      const rsrc_t rc = mk_rsrc((const void*)pc, (part == 3 && i8 == 1) ? len : 0u);
      cpf = __builtin_amdgcn_raw_buffer_load_b32(rc, tid * 64, 0, 0);
    }
    if constexpr (v == SK1 + 2) PIN(cpf);
    if constexpr (v == RVP) report(G_VT + 1024);
    if constexpr (v == RVL) tbg = ld8<AUX_SC1>(wsr, wave == 7 ? G_VT + 1024 + xcd * 128 + c * 8 : OOB, 0);
    if constexpr (v == SK1) mlg = ld16<AUX_SC1>(wsr, wave == 0 ? mlo : OOB, 0);
    SB();
    if constexpr (NL + v < 72) limit(IC<NL + v>{});
    if constexpr (v >= SK0 + WV) { WAIT_VM(v < SK1 ? WV : WV + 1); SB(); }
    if constexpr (v == VQ) {
      if (!all_ok((qg[0][1] == tag) & (qg[0][3] == tag) & (qg[1][1] == tag) & (qg[1][3] == tag))) { STAMP(20); gather16<2>(wsr, qoff, 16, tag, qg); }
      STAMP(1); WSTAMP(1);
      qq[0] = qg[0][0]; qq[1] = qg[0][2]; qq[2] = qg[1][0]; qq[3] = qg[1][2];
    }
    if constexpr (v >= VQ && v < VS) {
      constexpr int sl = v - VQ, bt = sl >> 1;
      float xs[8];
#pragma unroll
      for (int i = 0; i < 8; ++i) {
        float d = 0.f;
        if (sl * 8 + i < NL) {
#pragma unroll
          for (int k = 0; k < 4; ++k) d = dot2(kr[sl * 8 + i][k], qq[k], d);
        }
        xs[i] = d;
      }
#pragma unroll
      for (int h = 0; h < 4; ++h) xs[h] = sel_add<XOR7>(xs[h], xs[h + 4], c & 4);
#pragma unroll
      for (int h = 0; h < 2; ++h) xs[h] = sel_add<XOR2>(xs[h], xs[h + 2], c & 2);
      xs[0] = sel_add<XOR1>(xs[0], xs[1], c & 1);
      if constexpr ((sl & 1) || sl == NSL - 1) {
        const float t = (sl & 1) ? sel_add<XOR8>(xlo, xs[0], c & 8) : xs[0] + dpp<XOR8>(xs[0]);
        const float s = rbf(rbf(t) * 0.08838834764831845f);
        sc[bt] = (bt * 16 + c < SK0 || (bt * 16 + c >= first && bt * 16 + c < NL)) ? s : -__builtin_inff();
        PIN(sc[bt]);
      } else {
        xlo = xs[0];
        PIN(xlo);
      }
    }
    if constexpr (v == VS) {
      float mloc = sc[0];
#pragma unroll
      for (int bt = 1; bt < NB; ++bt) mloc = fmaxf(mloc, sc[bt]);
      m = fmaxf(wave_max(mloc), -3.0e38f);
      float ls = 0.f;
#pragma unroll
      for (int bt = 0; bt < NB; ++bt) { pr[bt] = __expf(sc[bt] - m); ls += pr[bt]; }
      const float l = wave_sum(ls);
      if (lane == 0) { sm.ml[wave][0] = m; sm.ml[wave][1] = l; }
      if (c == ((NL - 1) & 15)) sm.pl[g][wave] = pr[NB - 1];
      STAMP(2); WSTAMP(2);
    }
    if constexpr (v >= SK1) pv_row(IC<v - SK1 + SK0 - WV>{});
    if constexpr (v < SK0 || v >= SK0 + WV) sfor<cons_end(v - 1), cons_end(v)>(pv_row);
    if constexpr (v == RVU) {
      if (wave == 7) {
        const bool ok = tbg[1] == tag;
        float lg = ok ? (float)(tbg[0] & 0xffffu) : 0.f, nf = ok ? 8.f * (RVP - RVS + 1) : 0.f;
        lg += dpp<XOR1>(lg); nf += dpp<XOR1>(nf); lg += dpp<XOR2>(lg); nf += dpp<XOR2>(nf); lg += dpp<XOR7>(lg); nf += dpp<XOR7>(nf); lg += dpp<XOR8>(lg); nf += dpp<XOR8>(nf);
        lg *= __builtin_amdgcn_rcpf(fmaxf(nf, 1.f));
        if (lane == 0) sm.bc[1] = (u32)min(max((int)((lg - (float)P.F0) * (float)(P.FG * 32)), 0), 200 * 256);
      }
      LDS_BARRIER();
      const int st = __builtin_amdgcn_readfirstlane(sm.bc[1]), sp = (st * P.FF_) >> 3;
      t1 += st;
      due += sp;
      tau += __builtin_amdgcn_readfirstlane((int)((float)(st - sp) * __builtin_amdgcn_rcpf((float)(nl - RVU - 3))));
    }
    if constexpr (v == SK1 + 1) {
      float p[8];
#pragma unroll
      for (int w = 0; w < 8; ++w) p[w] = sm.pl[g][w] * __expf(sm.ml[w][0] - m);
#pragma unroll
      for (int w = 0; w < 8; ++w) pl8[w] = pack_bf(p[w], p[w]);
#pragma unroll
      for (int w = 0; w < 8; ++w) PIN(pl8[w]);
    }
    if constexpr (v == MLS - 1) { side_pin(); if (is_up) up_prep(); PIN(ga); PIN(xin); PIN(cc1); }
    if constexpr (v == UPREP - 1) wo_addr();
    if constexpr (v == UPREP) {
#pragma unroll
      for (int k = 0; k < 8; ++k) asm volatile("" : "=v"(bf[k]));
      if (part == 0) wo_prep(); else if (is_up) up_frags();
#pragma unroll
      for (int k = 0; k < 8; ++k) PIN(bf[k]);
    }
    if constexpr (v == UPREP + 1) {
      if (is_up) {
        const u32x4* a1 = ap + 128 + ((lane >> 1) & 1) * 128;
        f32x4 d0 = {0.f, 0.f, 0.f, 0.f}, d1 = {0.f, 0.f, 0.f, 0.f};
#pragma unroll
        for (int rb = 0; rb < 4; ++rb) { d0 = mfma16(a1[rb * 8], bf[rb], d0); d1 = mfma16(a1[(rb + 4) * 8], bf[rb + 4], d1); }
        Af = par ? d0[1] + d1[1] : d0[0] + d1[0]; Bf = par ? d0[3] + d1[3] : d0[2] + d1[2];
        Af += dpp<XOR8>(Af); Bf += dpp<XOR8>(Bf);
      }
      PIN(Af); PIN(Bf);
    }
    if (v == 10 || v == 16) STAMP(15 + v / 6);
    if (v == 20) STAMP(18);
    if (v == 32) STAMP(19);
    if constexpr (v == MLS) {
      LDS_BARRIER();
      float mm[8], ll[8];
#pragma unroll
      for (int w = 0; w < 8; ++w) { const f32x2 t = *reinterpret_cast<const f32x2*>(&sm.ml[w][0]); mm[w] = t[0]; ll[w] = t[1]; }
      float Mwg = mm[0], Lwg = 0.f;
#pragma unroll
      for (int w = 1; w < 8; ++w) Mwg = fmaxf(Mwg, mm[w]);
      if (wave == 0) {
#pragma unroll
        for (int w = 0; w < 8; ++w) Lwg = fmaf(__expf(mm[w] - Mwg), ll[w], Lwg);
        if (lane == 0) st16_sc1(wsr, G_ML + ((head * 2 + b) * 4 + part) * 16, __float_as_uint(Mwg), __float_as_uint(Lwg), tag);
      }
      const float fw = __expf(m - Mwg);
      m = Mwg;
#pragma unroll
      for (int bt = 0; bt < NB; ++bt) { pb[bt] = pack_bf(pr[bt] * fw, pr[bt] * fw); PIN(pb[bt]); }
#pragma unroll
      for (int k = 0; k < 4; ++k) { pm[k] = (c >> 1 & 3) == k ? ((c & 1) ? 0xffff0000u : 0x0000ffffu) : 0u; PIN(pm[k]); }
    }
    SB();
    return true;
  });
  asm volatile(".Ltail_start:");
  if (wave != 0) {
    pv_row(IC<SK0 - 1>{});
    WAIT_VM(WV); SB();
    sfor<NL - 1 - WV, NL - 1>(pv_row);
    *LDSP(f32x4, adst0) = acc;
    WSTAMP(14);
    LDS_BARRIER();
  } else {
    u32x4 xr[8], am[8];
#pragma unroll
    for (int w = 0; w < 8; ++w) am[w] = u32x4{pl8[w] & pm[0], pl8[w] & pm[1], pl8[w] & pm[2], pl8[w] & pm[3]};
#pragma unroll
    for (int w = 0; w < 8; ++w) PIN(am[w]);
    {
      const u32 so = __builtin_amdgcn_readlane(sot, NL - 1);
      gate();
      SB();
#pragma unroll
      for (int w = 0; w < 8; ++w) xr[w] = ld16<AUX_NT>(rv, lane * 16, so ^ ((w & 1) ? col(P, 7) : 0u) ^ ((w & 2) ? col(P, 8) : 0u) ^ ((w & 4) ? col(P, 9) : 0u));
      SB();
    }
    u32 rsum = lds_addr(&sm.red[0][c * 8 + (g & 1) * 4]);
    pv_row(IC<SK0 - 1>{});
    WAIT_VM(WV + 8); SB();
    sfor<NL - 1 - WV, NL - 3>(pv_row);
    if (__builtin_expect(!all_ok((mlg[1] == tag) & (mlg[3] == tag)), 0)) {
#pragma nounroll
      for (int spin = 0; spin < SPIN_MAX; ++spin) {
        CFENCE();
        mlg = ld16<AUX_SC1>(wsr, mlo, 0);
        if (all_ok((mlg[1] == tag) & (mlg[3] == tag))) break;
      }
    }
    merge_factor(mlg);
    sfor<NL - 3, NL - 1>(pv_row);
    STAMP(14); WSTAMP(14);
    LDS_BARRIER();
    STAMP(15);
    f32x4 t[8];
#pragma unroll
    for (int w = 1; w < 8; ++w) t[w] = LDSP(f32x4, rsum)[w * 32];
    acc = ((acc + t[1]) + (t[2] + t[3])) + ((t[4] + t[5]) + (t[6] + t[7]));
    PIN(acc);
    STAMP(3);
    SB();
#pragma unroll
    for (int w = 0; w < 8; ++w) acc = mfma16(am[w], xr[w], acc);
  }
  WAIT_VM(0);
  acc *= fmrg;
  {
    const u32 w0 = min(pack_bf(acc[0], acc[1]), SENT - 1u), w1 = min(pack_bf(acc[2], acc[3]), SENT - 1u);
    const auto e0 = __builtin_amdgcn_permlane16_swap(w0, w0, false, false), e1 = __builtin_amdgcn_permlane16_swap(w1, w1, false, false);
    __builtin_amdgcn_raw_buffer_store_b128(u32x4{e0[0], e1[0], e0[1], e1[1]}, wsr, pso, 0, AUX_SC1);
  }
  st8_sc1(wsr, nto, (u32)wall_clock64() + nadd, tag);
  SB();
  STAMP(4);

  if (part == 2) FINISH();
  if (part == 0) {
    __builtin_amdgcn_s_barrier();
#pragma nounroll
    while (wleft > 0) piece();
    bool late = false;
    u32x4 og[2];
    if (wdef) {
      late = true;
      while ((int)(((u32)wall_clock64() - t0) << 8) < t1 - 20 * 256) if (__builtin_amdgcn_s_getreg(7 | (3 << 11)) == 0) { late = false; break; }
      if (__builtin_expect(late, 0)) { until(t1); CFENCE(); og[0] = ld16<AUX_SC1>(wsr, po, 0); SB(); WAIT_VM(1); } else { WAIT_VM(0); }
      CFENCE();
      if (P.WD > 4) wo_prep(0, 1);
      wo_prep(1, 2);
#pragma unroll
      for (int k = 0; k < 8; ++k) PIN(bf[k]);
      SB();
    }
    if (__builtin_expect(!late, 1)) { until(t1); CFENCE(); og[0] = ld16<AUX_SC1>(wsr, po, 0); }
    STAMP(11);
    SB();
    f32x4 d[4];
    auto wo_mm = [&]() {
      const f32x4 z = {0.f, 0.f, 0.f, 0.f};
#pragma unroll
      for (int t = 0; t < 4; ++t) { d[t] = mfma16(og[0], bf[t], z); og[1][t] = __float_as_uint(dpp<XOR8>(u2f(og[0][t]))); }
      SB();
#pragma unroll
      for (int t = 0; t < 4; ++t) d[t] = mfma16(og[1], bf[4 + t], d[t]);
    };
    auto p_bad = [&]() { return !all_ok(max(max(og[0][0], og[0][1]), max(og[0][2], og[0][3])) != SENT); };
    wo_mm();
    if (__builtin_expect(p_bad(), 0)) {
#pragma nounroll
      for (int spin = 0; spin < SPIN_MAX; ++spin) {
        __builtin_amdgcn_s_sleep(1);
        CFENCE();
        og[0] = ld16<AUX_SC1>(wsr, po, 0);
        if (!p_bad()) break;
      }
      wo_mm();
      SB();
      asm volatile(".Lend_d:");
    }
    STAMP(5); WSTAMP(5);
    {
      SB();
      f32x4 y; f32x2 h[4];
#pragma unroll
      for (int t = 0; t < 4; ++t) { h[t] = d[t].xy + d[t].zw; PIN(h[t]); }
#pragma unroll
      for (int t = 0; t < 4; ++t) { y[t] = h[t][0] + h[t][1]; PIN(y[t]); }
      *LDSP(f32x4, rdst) = y;
    }
    STAMP(16);
    LDS_BARRIER();
    STAMP(17);
    if (wave < 2) {
      f32x2 v[4];
#pragma unroll
      for (int w = 0; w < 4; ++w) v[w] = f32x2{LDSP(float, rsrc)[w * 256], LDSP(float, rsrc)[w * 256 + 128]};
      const f32x2 h = (v[0] + v[1]) + (v[2] + v[3]);
      const float y = h[0] + h[1];
      __builtin_amdgcn_raw_buffer_store_b32(min(pack_bf(dpp<0xA0>(y), dpp<0xF5>(y)), SENT - 1u), wsr, xo, 0, AUX_SC1);
      asm volatile(".Lend_a:");
    }
    STAMP(6);
    FINISH();
  }
  if (part == 3) {
    if (!has_w) FINISH();
    u32x4 b2[4];
    {
      __builtin_amdgcn_s_barrier();
      const rsrc_t r2 = mk_rsrc(w2 + (size_t)(blk * 32 + wave * 4) * 512, 4096u);
      const u32 l2 = (u32)(uintptr_t)(&sm.w2b[wave * 4][0]);
#pragma unroll
      for (int n = 0; n < 4; ++n) dma16(r2, (lane ^ (n * 2) ^ ((wave & 2) << 2)) * 16, n * 1024, l2 + n * 1024);
    }
    const u32 tdst = lds_addr(&sm.hb[hpos]);
    const u32 sdst = lds_addr(&sm.stat[wave][lane]);
    const u32 ssrc = lds_addr(&sm.stat[(g >> 1) * 4 + ((lane >> 3) & 3)][(lane & 7) * 4]);
    const u32 adst = lds_addr((c < 4 && (g & 1) == 0) ? &sm.actl[g >> 1][wave * 4 + c] : reinterpret_cast<u16*>(&sm.dummy[lane]));
    const u32 asrc = lds_addr(&sm.actl[lane & 1][g * 8]);
    const u32 apa = lds_addr(&sm.hb[(c >> 3) * 512 + (c & 3) * 64 + ((c >> 2) & 1) * 32 + g * 8]);
    constexpr u32 xs2 = 16384;
    u32 xoff = dup(G_XP) + near + g * 2048 + ub * 1024 + (wave & 3) * 256 + c * 16;
    u32x4 one;
#pragma unroll
    for (int k = 0; k < 4; ++k) one[k] = ((c & 2) == 0 && (c >> 2) == k) ? ((c & 1) ? 0x3f800000u : 0x00003f80u) : 0u;
    PIN(one);
    u32 ooff = dup(G_OP + (blk * 512 + tid) * 8);
    float Afp = Af, Bfp = Bf;
    PIN(xoff); PIN(ooff); PIN(Afp); PIN(Bfp);
    f32x2 gc = ga * cc1, xc = xin * cc1;
    PIN(ga); PIN(xin); PIN(gc); PIN(xc);
    u32 w2a = (u32)(uintptr_t)(&sm.w2b[0][0]) + (g * 8 + ((lane >> 2) & 3)) * 1024 + (wave ^ (g & 1)) * 128 + (lane & 15) * 8;
    PIN(w2a);
    if (wave == 1) {
      due = t1 + P.N1 * 256;
      const int tn = notices();
      due = tn + P.X2 * 256;
      until(due);
    }
    asm volatile("s_waitcnt vmcnt(0)" ::: "memory");
    STAMP(9);
    __builtin_amdgcn_s_barrier();
    STAMP(11);
    CFENCE();
    u32x4 xg[2] = {ld16<AUX_SC1>(wsr, xoff, 0), ld16<AUX_SC1>(wsr, xoff, xs2)};
    SB();
#pragma unroll
    for (int t = 0; t < 4; ++t) {
      const u32x2 lo = tr_read(w2a ^ (t * 32)), hi = tr_read((w2a ^ (t * 32)) + 4096);
      b2[t] = u32x4{lo[0], lo[1], hi[0], hi[1]};
    }
#pragma unroll
    for (int t = 0; t < 4; ++t) PIN(b2[t]);
    SB();
    const f32x4 z4 = {0.f, 0.f, 0.f, 0.f};
    auto xp_bad = [&]() { return !all_ok(max(max(max(xg[0][0], xg[0][1]), max(xg[0][2], xg[0][3])), max(max(xg[1][0], xg[1][1]), max(xg[1][2], xg[1][3]))) != SENT); };
    f32x4 v = mfma16(one, xg[1], mfma16(one, xg[0], z4));
    if (__builtin_expect(xp_bad(), 0)) {
#pragma nounroll
      for (int spin = 0; spin < SPIN_MAX; ++spin) {
        __builtin_amdgcn_s_sleep(1);
        CFENCE();
        xg[0] = ld16<AUX_SC1>(wsr, xoff, 0); xg[1] = ld16<AUX_SC1>(wsr, xoff, xs2);
        if (!xp_bad()) break;
      }
      v = mfma16(one, xg[1], mfma16(one, xg[0], z4));
      SB();
      asm volatile(".Lend_e:");
    }
    STAMP(5); WSTAMP(5);
    {
      const f32x2 a = v.xy, hc = __builtin_elementwise_fma(gc, a, xc), x1 = __builtin_elementwise_fma(ga, a, xin);
      *LDSP(u32, tdst) = pack_bf(hc[0], hc[1]);
      *LDSP(float, sdst) = swap32_add(x1[0] + x1[1], fmaf(x1[0], x1[0], x1[1] * x1[1]));
    }
    STAMP(16);
    LDS_BARRIER();
    STAMP(17);
    {
      const f32x4 z = {0.f, 0.f, 0.f, 0.f};
      const u32x4 af0 = LDSP(u32x4, apa)[0];
      SB();
      const f32x4 s4 = LDSP(f32x4, ssrc)[0], q4 = LDSP(f32x4, ssrc)[8];
      const u32x4 af1 = LDSP(u32x4, apa)[32];
      SB();
      f32x4 d[4];
      d[0] = mfma16(af0, bf[0], z); d[1] = mfma16(af0, bf[1], z);
      SB();
      const f32x2 s2 = s4.xy + s4.zw, q2 = q4.xy + q4.zw;
      float s = s2[0] + s2[1], q = q2[0] + q2[1];
      SB();
      d[2] = mfma16(af0, bf[2], z);
      SB();
      s = swap16_add(s, q);
      SB();
      d[3] = mfma16(af0, bf[3], z);
      SB();
      s += dpp<XOR1>(s); s += dpp<XOR2>(s);
      SB();
      d[0] = mfma16(af1, bf[4], d[0]);
      SB();
      s += dpp<XOR7>(s); s += dpp<XOR8>(s);
      SB();
      d[1] = mfma16(af1, bf[5], d[1]);
      SB();
      const auto sq = __builtin_amdgcn_permlane16_swap(__float_as_uint(s), __float_as_uint(s), false, false);
      const float mean = u2f(sq[0]) * (1.f / 512.f);
      SB();
      d[2] = mfma16(af1, bf[6], d[2]);
      SB();
      const float var = fmaf(u2f(sq[1]), 1.f / 512.f, -mean * mean);
      SB();
      d[3] = mfma16(af1, bf[7], d[3]);
      SB();
      float ri = __builtin_amdgcn_rsqf(fmaxf(var, 0.f) + 1e-5f);
      float cf = fmaf(-ri * mean, Afp, Bfp);
      float rk = ri * -1.44269504f, ck = cf * -1.44269504f;
      PIN(ri); PIN(cf); PIN(rk); PIN(ck);
      SB();
      PIN(d[0]); PIN(d[1]);
      float y = d[0][0] + d[1][1];
      PIN(y); PIN(d[2]);
      y += d[2][2];
      PIN(y); PIN(d[3]);
      y += d[3][3];
      y = __builtin_bit_cast(float, __builtin_amdgcn_update_dpp(__builtin_bit_cast(int, y), __builtin_bit_cast(int, y), XOR8, 0xa, 0xf, false));
      y = swap16_add(y, y);
      const float ex = __builtin_amdgcn_exp2f(fmaf(y, rk, ck));
      y = fmaf(y, ri, cf);
      const float gu = y * dpp<0x104>(y);
      *LDSP(u16, adst) = (u16)pack_bf(gu * __builtin_amdgcn_rcpf(1.f + ex), 0.f);
    }
    STAMP(18);
    LDS_BARRIER();
    STAMP(10);
    {
      const u32x4 af = *LDSP(u32x4, asrc);
      const f32x4 z = {0.f, 0.f, 0.f, 0.f};
      f32x4 d0 = mfma16(af, b2[0], z), d1 = mfma16(af, b2[1], z), d2 = mfma16(af, b2[2], z), d3 = mfma16(af, b2[3], z);
      asm volatile("" : "+v"(d0), "+v"(d1), "+v"(d2), "+v"(d3));
      const bool g1 = g & 1, g2 = g & 2;
      const float e0 = g1 ? d1[0] : d0[0], e1 = g1 ? d1[1] : d0[1], f0 = g1 ? d3[0] : d2[0], f1 = g1 ? d3[1] : d2[1];
      const u32x2 ov = {pack_bf(g2 ? f0 : e0, g2 ? f1 : e1), tag};
      __builtin_amdgcn_raw_buffer_store_b64(ov, wsr, ooff, near ^ 8192u, AUX_SC1);
      __builtin_amdgcn_raw_buffer_store_b64(ov, wsr, ooff, near, AUX_SC1);
    }
    asm volatile(".Lend_b:");
    STAMP(6);
    FINISH();
  }
  if (wave == 0) {
    if (!all_ok((sg[0][1] == tag) & (sg[0][3] == tag) & (sg[1][1] == tag) & (sg[1][3] == tag))) {
      u32x4 t[2];
      gather16<2>(wsr, G_MOD + (1024 + fd) * 8, 1536 * 8, tag, t);
      sg[0] = t[0]; sg[1] = t[1];
    }
    u32 xa;
    {
      due = t1 + P.N2 * 256;
      const int tn = notices();
      due = tn + P.X3 * 256;
      until(due);
    }
    const bool hb = g & 1;
#pragma nounroll
    for (int spin = 0; spin < SPIN_MAX; ++spin) {
      CFENCE();
      xa = __builtin_amdgcn_raw_buffer_load_b32(wsr, dup(G_XP) + near + (g >> 1) * 16384 + (c >> 2) * 2048 + (g & 1) * 1024 + fd * 2, 0, AUX_SC1);
      if (all_ok(xa != SENT)) break;
      __builtin_amdgcn_s_sleep(1);
    }
    float a0 = lo_f(xa), a1 = hi_f(xa);
    a0 += dpp<0x124>(a0); a1 += dpp<0x124>(a1);
    a0 += dpp<XOR8>(a0); a1 += dpp<XOR8>(a1);
    a0 = swap32_add(a0, a0); a1 = swap32_add(a1, a1);
    f32x2 xv = {rbf(lo_f(sx2) + rbf((hb ? hi_f(sg[0][0]) : lo_f(sg[0][0])) * rbf(a0))), rbf(hi_f(sx2) + rbf((hb ? hi_f(sg[0][2]) : lo_f(sg[0][2])) * rbf(a1)))};
    f32x2 gv = {hb ? hi_f(sg[1][0]) : lo_f(sg[1][0]), hb ? hi_f(sg[1][2]) : lo_f(sg[1][2])};
    PIN(xv); PIN(gv);
    if (wi == 0 && lane == 0) __builtin_amdgcn_raw_buffer_store_b32(tag, wsr, WS_EPOCH, 0, AUX_SC1);
    STAMP(10);
    due = max(due + P.X4 * 256, (int)(((u32)wall_clock64() - t0) << 8) + P.GX * 256);
    const bool last = lane < (NUP - 32) * 4;
    u32 o0 = dup(G_OP + (lane >> 2) * 4096 + fd * 8) + near, o2 = last ? o0 : OOB, tg2 = last ? tag : 0u;
    const rsrc_t rout = mk_rsrc(out, 2048u);
    u32 oo = (lane & 44) == 0 ? (g * 512 + fd) * 2 : OOB;
    const u32 pk = (c & 10) ? 0u : ((c & 4) ? 0x3f800000u : 0x00003f80u);
    u32x4 one = {(c & 1) ? 0u : pk, 0u, (c & 1) ? pk : 0u, 0u};
    PIN(o0); PIN(o2); PIN(tg2); PIN(oo); PIN(one);
    __builtin_amdgcn_raw_buffer_store_b32(pack_bf(xv[0], xv[1]), rout, oo, 0, AUX_SC1);
    u32 s1_ = 131072, s2_ = 262144;
    asm volatile("" : "+s"(s1_), "+s"(s2_));
    until(due);
    f32x4 sv;
#pragma nounroll
    for (int spin = 0; spin < SPIN_MAX; ++spin) {
      CFENCE();
      const u32x4 og[3] = {ld16<AUX_SC1>(wsr, o0, 0), ld16<AUX_SC1>(wsr, o0, s1_), ld16<AUX_SC1>(wsr, o2, s2_)};
      sv = f32x4{0.f, 0.f, 0.f, 0.f};
#pragma unroll
      for (int k = 0; k < 3; ++k) sv = mfma16(one, og[k], sv);
      SB();
      u32 bad = ((og[0][1] ^ tag) | (og[0][3] ^ tag) | (og[1][1] ^ tag)) | ((og[1][3] ^ tag) | (og[2][1] ^ tg2) | (og[2][3] ^ tg2));
      PIN(bad);
      SB();
      float s0 = sv[0] + dpp<0x124>(sv[0]), s1 = sv[1] + dpp<0x124>(sv[1]);
      PIN(s0); PIN(s1);
      s0 += dpp<XOR8>(s0); s1 += dpp<XOR8>(s1);
      PIN(s0); PIN(s1);
      __builtin_amdgcn_raw_buffer_store_b32(pack_bf(fmaf(gv[0], s0, xv[0]), fmaf(gv[1], s1, xv[1])), rout, oo, 0, AUX_SC1);
      SB();
      if (all_ok(bad == 0u)) break;
      __builtin_amdgcn_s_sleep(1);
    }
    STAMP(7);
    asm volatile(".Lend_c:");
  }
  FINISH();
}

extern "C" void kernel_launch(void* const* d_in, const int* in_sizes, int n_in,
                              void* d_out, int out_size, void* d_ws, size_t ws_size,
                              hipStream_t stream) {
  fused_kernel<<<256, 512, 0, stream>>>((const u16*)d_in[1], (const u16*)d_in[2], (const u16*)d_in[4], (const u16*)d_in[3], (const u16*)d_in[10], (const u16*)d_in[6], (const u16*)d_in[7],
                                        (unsigned char*)d_ws, (const u16*)d_in[0], (const u16*)d_in[9], (const u16*)d_in[8], (u16*)d_out);
}
```

```cpp
#include <hip/hip_runtime.h>
#include <stdint.h>

typedef unsigned int u32;
typedef unsigned short u16;
typedef unsigned long long u64;
typedef __attribute__((ext_vector_type(4))) unsigned u32x4;
typedef __attribute__((ext_vector_type(2))) unsigned u32x2;
typedef __attribute__((ext_vector_type(2))) __bf16 bf16x2;
typedef __attribute__((ext_vector_type(4))) float f32x4;
typedef __attribute__((ext_vector_type(2))) float f32x2;
typedef __amdgpu_buffer_rsrc_t rsrc_t;

constexpr int NH = 32, SK = 16384, DH = 128, WIN = 4096, FF = 1376;
struct Prm { int E0, ES, E1, E2, E2C, EU, TEND, DT_UP, L0, L1, X1, X2, X3, X4, NE, FE, M0, M1, M2, M3, M4, GS, GX, N1, N2, GA, WD, DT_WO, LW, LU, EW, PM, NF, NR, C0, A0, AG, AC, TS, TW, TK, TG, GB, B0, BG, BF, D0, DG, TC, LM, C1, F0, FG, FF_, W[9]; };
constexpr Prm PRM0 = {32, 20, 32, 32, 54, 56, 1976, 60, 12, 30, 86, 70, 100, 89, 3, 9, 0x2511f55b, 0x2b7425d5, 0x6017665, 0x17e0b9ec, 0x31944305, 62, 30, 36, 62, 30, 4, 100, 45, 15, 12, 0x0, 15, 14, 3, 30, 4, 4, 508, 220, 75, 6, 9, 74, 26, 8, 8, 3, 0, 120, 2, 67, 18, 4, 0x77777777, 0x77777777, 0x77777777, 0x77777777, 0x77777777, 0x44444477, 0x44444444, 0x44444444, 0x44444444};
constexpr int NL = 37;
constexpr int SK0 = 21, SK1 = 32;
constexpr int NCMIN = (NL - SK1 + SK0) * 8;
constexpr int WK = 8, WV = 5;
constexpr int WUSE = 12, LNS = 7;
constexpr int QP = 14, QD = 4;
constexpr int VQ = 6;
constexpr int NSL = (NL + 7) / 8;
constexpr int VS = VQ + NSL;
constexpr int MLS = VS + 2;
constexpr int VSIDE = MLS - 5, UPREP = MLS + 6;
constexpr int CPF = 17;
constexpr int SMO = 1;
constexpr int RVS = 5, RVP = 8, RVL = 12, RVU = 16;
constexpr int RPS = 24, RPP = 28;
constexpr int PRB = 12;
constexpr int TBS = 7, TBP = 10, TBL = 13, TBU = 17;
constexpr int window(int s, int w) {
  int n = w;
  for (int u = s - w + 1; u <= s; ++u) n += u == SMO ? 6 : (u == TBL || u == SK1 || u == NL + CPF || u == NL + RVL || u == NL + SK1 ? 1 : (u == QP || u == NL + VQ - 4 ? 2 : (u == NL + VSIDE ? 4 : 0)));
  return n;
}
constexpr int cons_end(int v) {
  const int a = v - WV + 1, c = (v - MLS) * 4;
  const int m = a < c ? a : c;
  return m < 0 ? 0 : (m > NL ? NL : m);
}
constexpr u32 col(const Prm& P, int j) { const int m[6] = {P.M0, P.M1, P.M2, P.M3, P.M4, P.PM}; return (u32)(m[j / 3] >> (j % 3 * 10) & 1023) << 10; }
constexpr u32 first_col(const Prm& P, int k) {
  if (k < 4) return col(P, 12 + k) | 1u << (24 + k);
  if (k < 6) return col(P, k - 4);
  if (k < 8) return col(P, 4 + k) | 1u << (16 + k);
  if (k < 11) return col(P, k - 1);
  return (col(P, 2) ^ col(P, 3) ^ col(P, 4) ^ col(P, 5) ^ col(P, 6) ^ col(P, 10) ^ col(P, 11)) | 0xf00000u;
}
constexpr bool one_to_one(const Prm& P) {
  u32 c[10] = {};
  for (int j = 0; j < 10; ++j) c[j] = col(P, j);
  for (int j = 0; j < 10; ++j) {
    if (c[j] == 0) return false;
    const u32 low = c[j] & (0u - c[j]);
    for (int k = j + 1; k < 10; ++k) if (c[k] & low) c[k] ^= c[j];
  }
  return true;
}
static_assert(one_to_one(PRM0), "every KB of the window exactly once");
struct Tab { u32 e[512]; };
constexpr Tab make_tab(const Prm& P) {
  Tab T{};
  for (int bid = 0; bid < 256; ++bid) {
    const int xcd = bid & 7, part = bid >> 4 & 3, i8 = (bid >> 3 & 1) | bid >> 6 << 1, wi = i8 * 8 + xcd;
    const bool has_mod = wi < 48, has_up = wi >= 21;
    const int cb0 = (i8 + P.C0) & 7, cb1 = (i8 + P.C1) & 7, ms = wi - 20;
    const int mi = wi < 8 ? wi : (wi < 16 ? 8 + ((xcd + 4) & 7) : (wi < 20 ? (wi - 14) * 8 + 3 : 16 + ms / 7 * 8 + ms % 7 + (ms % 7 >= 3 ? 1 : 0)));
    const int ui = xcd * 5 + (xcd > 5 ? xcd - 5 : 0) + (i8 == 6 ? 0 : (xcd >= 5 ? 1 : 0) + (i8 == 7 ? 4 : i8 - 2));
    const bool edge = i8 >= 6;
    const int wd = cb0 == 3 ? 0 : P.WD;
    const int e[4] = {P.E0 - 4 * wd + (cb0 == 3 ? P.ES : 0), P.E1 + (cb1 == 3 ? P.ES : 0), has_mod ? (wi < 16 ? P.E2C : P.E2) + ((mi & 7) == 3 ? P.ES : 0) : 0, has_up ? P.EU + (edge ? P.TC : 0) : 0};
    int tw[4] = {}, win[4] = {}, span[4] = {}, k0[4] = {};
    for (int k = 0; k < 4; ++k) {
      const int np = k == 0 ? 8 - wd : 8;
      tw[k] = np ? e[k] * P.TW / (16 * np) : 0;
      k0[k] = P.NF + tw[k] * np / 2 > P.TK ? P.NF + tw[k] * np / 2 : P.TK;
      span[k] = P.TEND - k0[k] - P.TG - (k == 0 ? (wd ? P.DT_WO : P.A0) : (k == 3 && has_up ? P.DT_UP : 0));
      win[k] = span[k] - (k == 0 && wd ? 0 : (k == 3 && has_up ? P.GA + (edge ? P.LM : 0) : 0));
    }
    int n[4] = {NCMIN / 8, NCMIN / 8, NCMIN / 8, NCMIN / 8};
    for (int left = 128 - NCMIN / 2; left > 0; --left) {
      int best = -1;
      for (int k = 3; k >= 0; --k)
        if (n[k] < NL && (best < 0 || n[k] * win[best] < n[best] * win[k])) best = k;
      ++n[best];
    }
    const int c[4] = {0, n[0] - 1, n[0] + n[1] - 2, n[0] + n[1] + n[2] - 3};
    const int nl = n[part];
    const bool has_w = part < 2 || (part == 2 ? has_mod : has_up);
    const int blk = !has_w ? 0 : (part == 0 ? cb0 : (part == 1 ? xcd * 8 + cb1 : (part == 2 ? mi : ui)));
    T.e[2 * bid] = (u32)(c[part] | nl << 7 | blk << 13 | (has_w ? 1 : 0) << 19 | (part == 0 ? wd : 0) << 20) | (u32)tw[part] << 24;
    T.e[2 * bid + 1] = (u32)(span[part] * P.TS / (4 * (nl - 1)) | span[part] * (1024 - P.TS) / (4 * (nl - 1)) << 16);
  }
  return T;
}
constexpr int step_range(const Prm& P, bool mx_) {
  int mx = 0, mn = 1 << 20;
  const Tab T = make_tab(P);
  for (int bid = 0; bid < 256; ++bid) { const int n = T.e[2 * bid] >> 7 & 63; mx = n > mx ? n : mx; mn = n < mn ? n : mn; }
  return mx_ ? mx : mn;
}
extern "C" __attribute__((used, visibility("protected"))) __constant__ const Tab wg_tab = make_tab(PRM0);
static_assert(step_range(PRM0, true) <= NL && step_range(PRM0, false) >= NCMIN / 8, "a wave's key range does not fit its load steps");
static_assert(UPREP + 1 < SK0 && QP + QD + 1 < SK0 && cons_end(SK0 - 1) == SK0 - WV && NL - SK1 == WV, "steps every wave runs");
constexpr int NUP = FF / 32;
constexpr int SPIN_MAX = 1 << 17;

constexpr u32 WS_EPOCH = 0;
constexpr u32 G_TB = 1024;
constexpr u32 G_MOD = 4096;
constexpr u32 G_Q = G_MOD + 3072 * 8;
constexpr u32 G_ML = G_Q + 4096 * 8;
constexpr u32 G_U = G_ML + 256 * 16;
constexpr u32 G_P = 0;
constexpr u32 G_XP = G_P + 256 * 256;
constexpr u32 G_OP = G_XP + 8 * 512 * 4;
constexpr u32 G_NT = G_OP + 43 * 512 * 8;
constexpr u32 G_PB = G_OP + 44 * 512 * 8;
constexpr u32 G_VT = G_U + 2 * (G_PB + 8192);
constexpr u32 WS_END = G_VT + 2048;
__device__ __forceinline__ u32 dup(u32 o) { return G_U + ((o >> 13) << 14) + (o & 8191u); }
constexpr u32 SENT = 0xffffffffu;
constexpr u32 OOB = 0x40000000u;
#define WST 0

constexpr int AUX_NT = 2, AUX_SC1 = 16;

__device__ __forceinline__ float lo_f(u32 v) { return __uint_as_float(v << 16); }
__device__ __forceinline__ float hi_f(u32 v) { return __uint_as_float(v & 0xffff0000u); }
__device__ __forceinline__ float rbf(float x) { return (float)(__bf16)x; }
__device__ __forceinline__ u32 pack_bf(float lo, float hi) {
  bf16x2 r; r[0] = (__bf16)lo; r[1] = (__bf16)hi; return __builtin_bit_cast(u32, r);
}
__device__ __forceinline__ float dot2(u32 k, u32 q, float acc) {
  return __builtin_amdgcn_fdot2_f32_bf16(__builtin_bit_cast(bf16x2, k), __builtin_bit_cast(bf16x2, q), acc, false);
}
template <int CTRL> __device__ __forceinline__ float dpp(float x) {
  return __builtin_bit_cast(float, __builtin_amdgcn_mov_dpp(__builtin_bit_cast(int, x), CTRL, 0xf, 0xf, true));
}
constexpr int XOR1 = 0xB1, XOR2 = 0x4E, XOR7 = 0x141, XOR8 = 0x128;
__device__ __forceinline__ float swap32_add(float a, float b) {
  auto t = __builtin_amdgcn_permlane32_swap(__float_as_uint(a), __float_as_uint(b), false, false);
  return __uint_as_float(t[0]) + __uint_as_float(t[1]);
}
__device__ __forceinline__ float swap16_add(float a, float b) {
  auto t = __builtin_amdgcn_permlane16_swap(__float_as_uint(a), __float_as_uint(b), false, false);
  return __uint_as_float(t[0]) + __uint_as_float(t[1]);
}
__device__ __forceinline__ float wave_sum(float x) {
  x += dpp<XOR1>(x); x += dpp<XOR2>(x); x += dpp<XOR7>(x); x += dpp<XOR8>(x);
  x = swap16_add(x, x);
  return swap32_add(x, x);
}
__device__ __forceinline__ float wave_max(float x) {
  x = fmaxf(x, dpp<XOR1>(x)); x = fmaxf(x, dpp<XOR2>(x)); x = fmaxf(x, dpp<XOR7>(x)); x = fmaxf(x, dpp<XOR8>(x));
  auto s = __builtin_amdgcn_permlane16_swap(__float_as_uint(x), __float_as_uint(x), false, false);
  x = fmaxf(__uint_as_float(s[0]), __uint_as_float(s[1]));
  auto t = __builtin_amdgcn_permlane32_swap(__float_as_uint(x), __float_as_uint(x), false, false);
  return fmaxf(__uint_as_float(t[0]), __uint_as_float(t[1]));
}
template <int CTRL> __device__ __forceinline__ float sel_add(float lo, float hi, bool up) {
  const float t1 = lo + dpp<CTRL>(lo), t2 = hi + dpp<CTRL>(hi);
  return up ? t2 : t1;
}
template <int I> __device__ __forceinline__ float bc16(float x) {
  return __builtin_bit_cast(float, __builtin_amdgcn_update_dpp(0, __builtin_bit_cast(int, x), 0x150 + I, 0xf, 0xf, false));
}
__device__ __forceinline__ float bcast16(float x, int i) {
  switch (i) {
    case 0: return bc16<0>(x); case 1: return bc16<1>(x); case 2: return bc16<2>(x); case 3: return bc16<3>(x);
    case 4: return bc16<4>(x); case 5: return bc16<5>(x); case 6: return bc16<6>(x); case 7: return bc16<7>(x);
    case 8: return bc16<8>(x); case 9: return bc16<9>(x); case 10: return bc16<10>(x); case 11: return bc16<11>(x);
    case 12: return bc16<12>(x); case 13: return bc16<13>(x); case 14: return bc16<14>(x); default: return bc16<15>(x);
  }
}
__device__ __forceinline__ float silu_f(float x) { return x * __builtin_amdgcn_rcpf(1.f + __expf(-x)); }

template <int AUX> __device__ __forceinline__ u32x4 ld16(rsrc_t r, u32 voff, u32 soff) {
  return __builtin_amdgcn_raw_buffer_load_b128(r, voff, soff, AUX);
}
template <int AUX> __device__ __forceinline__ u32x2 ld8(rsrc_t r, u32 voff, u32 soff) {
  return __builtin_amdgcn_raw_buffer_load_b64(r, voff, soff, AUX);
}
__device__ __forceinline__ void st8_sc1(rsrc_t r, u32 voff, u32 v, u32 tag) {
  __builtin_amdgcn_raw_buffer_store_b64(u32x2{v, tag}, r, voff, 0, AUX_SC1);
}
__device__ __forceinline__ void st16_sc1(rsrc_t r, u32 voff, u32 v0, u32 v1, u32 tag) {
  __builtin_amdgcn_raw_buffer_store_b128(u32x4{v0, tag, v1, tag}, r, voff, 0, AUX_SC1);
}
__device__ __forceinline__ rsrc_t mk_rsrc(const void* p, u32 bytes) {
  return __builtin_amdgcn_make_buffer_rsrc(const_cast<void*>(p), 0, bytes, 0x00020000);
}
#define WAIT_VM(n) __builtin_amdgcn_s_waitcnt(((n) & 15) | (((n) >> 4) << 14) | (7 << 4) | (15 << 8))
#define SB() __builtin_amdgcn_sched_barrier(0)
#define CFENCE() asm volatile("" ::: "memory")
__device__ __forceinline__ bool all_ok(bool ok) { return __builtin_amdgcn_ballot_w64(!ok) == 0ull; }

template <int N> __device__ __forceinline__ void gather16(rsrc_t ws, u32 voff, u32 stride, u32 tag, u32x4 (&g)[N]) {
#pragma nounroll
  for (int spin = 0; spin < SPIN_MAX; ++spin) {
    CFENCE();
    bool ok = true;
#pragma unroll
    for (int i = 0; i < N; ++i) g[i] = ld16<AUX_SC1>(ws, voff + i * stride, 0);
#pragma unroll
    for (int i = 0; i < N; ++i) ok &= (g[i][1] == tag) & (g[i][3] == tag);
    if (all_ok(ok)) break;
    __builtin_amdgcn_s_sleep(8);
  }
}
template <int N> __device__ __forceinline__ void gather8(rsrc_t ws, u32 voff, u32 stride, u32 tag, u32 (&g)[N], bool act = true) {
#pragma nounroll
  for (int spin = 0; spin < SPIN_MAX; ++spin) {
    CFENCE();
    bool ok = true;
    u32x2 t[N];
#pragma unroll
    for (int i = 0; i < N; ++i) t[i] = ld8<AUX_SC1>(ws, voff + i * stride, 0);
#pragma unroll
    for (int i = 0; i < N; ++i) { ok &= (t[i][1] == tag); g[i] = t[i][0]; }
    if (all_ok(ok | !act)) break;
    __builtin_amdgcn_s_sleep(8);
  }
}

struct Smem {
  float red[8][128];
  float ml[8][2];
  float r2[64 * 20];
  u16 hb[3 * 1024];
  float stat[8][64];
  float red2[8][128];
  u32 dummy[64];
  u32x2 bc;
  u32x4 zero[4];
  float pl[4][8];
  u32 mflag[8];
  u32x4 wbuf[8][8][64];
  __attribute__((aligned(256))) u16 w2b[32][512];
  u16 actl[2][32];
};

#define STAMP(k) do {} while (0)
#define WSTAMP(k) do {} while (0)
#define FINISH() return
#define PIN(x) asm volatile("" : "+v"(x))
#define LDS_BARRIER() do { __builtin_amdgcn_s_waitcnt(0xc07f); __builtin_amdgcn_s_barrier(); CFENCE(); } while (0)
#define WAVE_SYNC() do { __builtin_amdgcn_fence(__ATOMIC_RELEASE, "wavefront"); __builtin_amdgcn_wave_barrier(); __builtin_amdgcn_fence(__ATOMIC_ACQUIRE, "wavefront"); } while (0)

__device__ __forceinline__ void dma16(rsrc_t r, u32 voff, u32 soff, u32 lds_dst) {
  u32 keep;
  asm volatile("s_mov_b32 %0, m0\n\ts_mov_b32 m0, %4\n\ts_nop 4\n\tbuffer_load_dwordx4 %1, %2, %3 offen nt lds\n\ts_mov_b32 m0, %0"
               : "=&s"(keep) : "v"(voff), "s"(r), "s"(soff), "s"(lds_dst) : "memory");
}
__device__ __forceinline__ void ld16_into(u32x4& d, rsrc_t r, u32 voff, u32 soff) {
  asm volatile("s_nop 4\n\tbuffer_load_dwordx4 %0, %1, %2, %3 offen nt" : "+v"(d) : "v"(voff), "s"(r), "s"(soff) : "memory");
}
__device__ __forceinline__ u32 perm_lo(u32 a, u32 b) { return __builtin_amdgcn_perm(b, a, 0x05040100u); }
__device__ __forceinline__ u32 perm_hi(u32 a, u32 b) { return __builtin_amdgcn_perm(b, a, 0x07060302u); }
__device__ __forceinline__ float rdl(float x, int l) { return __int_as_float(__builtin_amdgcn_readlane(__float_as_int(x), l)); }

typedef __attribute__((ext_vector_type(8))) __bf16 bf16x8;
__device__ __forceinline__ f32x4 mfma16(u32x4 a, u32x4 b, f32x4 c) {
  return __builtin_amdgcn_mfma_f32_16x16x32_bf16(__builtin_bit_cast(bf16x8, a), __builtin_bit_cast(bf16x8, b), c, 0, 0, 0);
}
__device__ __forceinline__ float sum8(const float* p) {
  const f32x4 a = *reinterpret_cast<const f32x4*>(p), b = *reinterpret_cast<const f32x4*>(p + 4);
  return ((a[0] + b[0]) + (a[1] + b[1])) + ((a[2] + b[2]) + (a[3] + b[3]));
}
__device__ __forceinline__ float ln_ri(float var) {
  return __builtin_amdgcn_rcpf(rbf(__builtin_amdgcn_sqrtf(rbf(rbf(fmaxf(var, 0.f)) + 1e-5f))));
}
__device__ __forceinline__ void ln_stats2(const float (&x0)[8], const float (&x1)[8], float& mb0, float& ri0, float& mb1, float& ri1) {
  const float k0 = rdl(x0[0], 0), k1 = rdl(x1[0], 0);
  float s0 = 0.f, s1 = 0.f, q0 = 0.f, q1 = 0.f;
#pragma unroll
  for (int k = 0; k < 8; ++k) {
    const float d0 = x0[k] - k0, d1 = x1[k] - k1;
    s0 += d0; s1 += d1; q0 = fmaf(d0, d0, q0); q1 = fmaf(d1, d1, q1);
  }
  float t = swap16_add(swap32_add(s0, s1), swap32_add(q0, q1));
  t += dpp<XOR1>(t); t += dpp<XOR2>(t); t += dpp<XOR7>(t); t += dpp<XOR8>(t);
  auto u = __builtin_amdgcn_permlane16_swap(__float_as_uint(t), __float_as_uint(t), false, false);
  const float ms = __uint_as_float(u[0]) * (1.f / 512.f), mq = __uint_as_float(u[1]) * (1.f / 512.f);
  const float ri = ln_ri(fmaf(-ms, ms, mq));
  mb0 = rbf(k0 + rdl(ms, 0)); mb1 = rbf(k1 + rdl(ms, 32));
  ri0 = rdl(ri, 0); ri1 = rdl(ri, 32);
}
template <int I> struct IC { static constexpr int v = I; };
template <int I, int N, class F> __device__ __forceinline__ void sfor(F&& f) {
  if constexpr (I < N) { if (f(IC<I>{})) sfor<I + 1, N>(f); }
}
typedef __attribute__((ext_vector_type(4))) short s16x4;
typedef __attribute__((address_space(3))) s16x4* lds_s16x4p;
__device__ __forceinline__ u32x2 tr_read(u32 lds_addr) {
  return __builtin_bit_cast(u32x2, __builtin_amdgcn_ds_read_tr16_b64_v4i16((lds_s16x4p)lds_addr));
}
__device__ __forceinline__ u32 next_tag(u32 e) { return ((e | 0x40004000u) + 1u) & ~0x40004000u; }
__device__ __forceinline__ float u2f(u32 v) { return __uint_as_float(v); }
#define LDSP(T, a) ((__attribute__((address_space(3))) T*)(a))
template <class T> __device__ __forceinline__ u32 lds_addr(T* p) { u32 a = (u32)(uintptr_t)p; PIN(a); return a; }

#define TAB_LOAD "s_add_u32 s30, s28, wg_tab@rel32@lo+4\ns_addc_u32 s31, s29, wg_tab@rel32@hi+12\nglobal_load_dwordx2 %[te], %[va], s[30:31]\n"
#define FC(k) ".Lfc" #k
#define FC_REGS
#define FX(r, bit, k) "s_bitcmp1_b32 %[" #r "], " #bit "\ns_cselect_b32 %[sb], " FC(k) ", 0\ns_xor_b32 %[sa], %[sa], %[sb]\n"
__global__ void __launch_bounds__(512, 2) fused_kernel(const u16* kc_, const u16* vc_, const u16* wo_, const u16* wq_, const u16* wmod_, const u16* w1_, const u16* w3_,
                                                       unsigned char* ws_, const u16* x_, const u16* cnd_, const u16* w2_, u16* out_) {
  __shared__ __attribute__((aligned(256))) Smem sm;
  const u64 a_ka = (u64)__builtin_amdgcn_kernarg_segment_ptr();
  const int bid = blockIdx.x, tid = threadIdx.x;
  u64 wa, t64; u32x2 te; int tid0;
  u32 ar[14];
  u32 tch2, sa_, sb_, va_;
  u32x4 kr[NL], vr[NL];
  asm volatile(".set .Lfc0, %0\n.set .Lfc1, %1\n.set .Lfc2, %2\n.set .Lfc3, %3\n.set .Lfc4, %4\n.set .Lfc5, %5\n.set .Lfc6, %6\n.set .Lfc7, %7\n.set .Lfc8, %8\n.set .Lfc9, %9\n.set .Lfc10, %10\n.set .Lfc11, %11"
    :: "n"(first_col(PRM0, 0)), "n"(first_col(PRM0, 1)), "n"(first_col(PRM0, 2)), "n"(first_col(PRM0, 3)), "n"(first_col(PRM0, 4)), "n"(first_col(PRM0, 5)),
       "n"(first_col(PRM0, 6)), "n"(first_col(PRM0, 7)), "n"(first_col(PRM0, 8)), "n"(first_col(PRM0, 9)), "n"(first_col(PRM0, 10)), "n"(first_col(PRM0, 11)));
  asm volatile(
    "s_load_dwordx16 s[4:19], %[ka], 0x0\ns_load_dwordx8 s[20:27], %[ka], 0x40\n"
    "s_memrealtime %[t]\n"
    "v_lshlrev_b32 %[va], 3, %[bid]\n"
    "s_getpc_b64 s[28:29]\n.Lcpf_first:\n"
    TAB_LOAD
    "s_cmp_gt_u32 %[bid], 15\ns_cbranch_scc1 .Lno_cpf\n"
    "s_mov_b32 %[sa], .Lend_a-.Lcpf_first\ns_max_i32 %[sa], %[sa], .Lend_b-.Lcpf_first\ns_max_i32 %[sa], %[sa], .Lend_c-.Lcpf_first\ns_max_i32 %[sa], %[sa], .Lend_d-.Lcpf_first\ns_max_i32 %[sa], %[sa], .Lend_e-.Lcpf_first\n"
    "v_lshlrev_b32 %[va], 6, %[tid]\nv_min_u32 %[va], %[sa], %[va]\nglobal_load_dword %[pf], %[va], s[28:29]\n"
    ".Lno_cpf:\n"
    "v_readfirstlane_b32 %[t0], %[tid]\nv_and_b32 %[va], 63, %[tid]\n"
    "s_mov_b32 %[sa], " FC(11) "\n"
    FX(bid, 0, 0) FX(bid, 1, 1) FX(bid, 2, 2) FX(bid, 3, 3) FX(bid, 4, 4) FX(bid, 5, 5) FX(bid, 6, 6) FX(bid, 7, 7) FX(t0, 6, 8) FX(t0, 7, 9) FX(t0, 8, 10)
    "v_lshl_add_u32 %[va], %[va], 4, %[sa]\n"
    "s_waitcnt lgkmcnt(0)\nglobal_load_dwordx4 %[k0], %[va], s[4:5] nt\nglobal_load_dwordx4 %[v0], %[va], s[6:7] nt\n"
    "s_bitcmp1_b32 %[t0], 6\ns_cselect_b64 s[14:15], s[16:17], s[14:15]\n"
    "s_bitcmp1_b32 %[bid], 4\ns_cselect_b64 s[8:9], s[10:11], s[8:9]\ns_cselect_b64 s[12:13], s[14:15], s[12:13]\ns_bitcmp1_b32 %[bid], 5\ns_cselect_b64 %[wa], s[12:13], s[8:9]"
    : "=&{s4}"(ar[0]), "=&{s5}"(ar[1]), "=&{s6}"(ar[2]), "=&{s7}"(ar[3]), "=&{s18}"(ar[4]), "=&{s19}"(ar[5]), "=&{s20}"(ar[6]), "=&{s21}"(ar[7]), "=&{s22}"(ar[8]), "=&{s23}"(ar[9]),
      "=&{s24}"(ar[10]), "=&{s25}"(ar[11]), "=&{s26}"(ar[12]), "=&{s27}"(ar[13]), [wa] "=&s"(wa), [t0] "=&s"(tid0), [t] "=&s"(t64), [te] "=&v"(te), [sa] "=&s"(sa_), [sb] "=&s"(sb_),
      [va] "=&v"(va_), [pf] "=&v"(tch2), [k0] "=&v"(kr[0]), [v0] "=&v"(vr[0])
    : [ka] "s"(a_ka), [bid] "s"(bid), [tid] "v"(tid) : "s8", "s9", "s10", "s11", "s12", "s13", "s14", "s15", "s16", "s17", "s28", "s29", "s30", "s31", "scc", "memory" FC_REGS);
  asm volatile("" : "+s"(ar[0]), "+s"(ar[1]), "+s"(ar[2]), "+s"(ar[3]), "+s"(ar[4]), "+s"(ar[5]), "+s"(ar[6]), "+s"(ar[7]), "+s"(ar[8]), "+s"(ar[9]), "+s"(ar[10]), "+s"(ar[11]), "+s"(ar[12]), "+s"(ar[13]));
  asm volatile("" : "+s"(wa), "+s"(t64), "+s"(tid0), "+s"(sa_));
  const u32 tka = (u32)wall_clock64();
  const u32 t0 = tka;
  __builtin_assume(tid >= 0 && tid < 512 && tid0 >= 0 && tid0 < 512 && bid >= 0 && bid < 256);
  constexpr Prm P = PRM0;
  const int TEND = P.TEND;
  const int xcd = bid & 7, part = bid >> 4 & 3;
  const int lane = tid & 63, wave = tid0 >> 6;
  SB();
  const bool p3 = part == 3;
  u32 pitch = (u32)(0x005600c001000020ull >> (part * 16) & 0xffff) * 32u;
  const int sh = p3 ? 2 : 3;
  u32 wv0 = (u32)((p3 ? (wave >> 1) * 128 : wave * 64) + (part == 0 ? xcd * 512 : 0) + (lane >> sh)) * pitch + ((lane & (p3 ? 3 : 7)) ^ (lane >> (p3 ? 5 : 3))) * 16u;
  const u32 wimg = (u32)(uintptr_t)(&sm.wbuf[0][0][0]);
  u32 wsz = (part == 0 ? 4096u : 512u) * pitch, wld = wimg + (p3 ? (wave & 1) * 32768 + (wave >> 1) * 8192 : wave * 8192);
  PIN(wv0);
  asm volatile("" : "+s"(wsz), "+s"(wld), "+s"(pitch));
  auto karg = [&](int i) { return ar[2 * i] | (u64)ar[2 * i + 1] << 32; };
  const u64 a_kc = karg(0), a_vc = karg(1);
  unsigned char* ws = (unsigned char*)karg(2);
  const u16 *x = (const u16*)karg(3), *cnd = (const u16*)karg(4), *w2 = (const u16*)karg(5);
  u16* out = (u16*)karg(6);
  const rsrc_t wsr = mk_rsrc(ws, WS_END);
  asm volatile("s_waitcnt vmcnt(2)" : "+v"(te));
  const u32 te0 = __builtin_amdgcn_readfirstlane(te[0]), te1 = __builtin_amdgcn_readfirstlane(te[1]);
  const int bl0 = te0 & 127, nl = te0 >> 7 & 63, blk = te0 >> 13 & 63;
  const bool has_w = te0 >> 19 & 1;
  const rsrc_t rw = mk_rsrc((const void*)wa, wsz);
  const u32 wstep = pitch << (6 - sh), wflip = p3 ? 32u : 0u;
  u32 wso = blk << (sh + 4);
  int wleft = 8;
  auto piece = [&]() {
    dma16(rw, wv0, wso, wld);
    wso += wstep; wld += 1024;
    wv0 ^= (wleft & 1) ? wflip : 0u;
    --wleft;
  };
  SB();
  STAMP(23);
  const int b = bid >> 3 & 1, head = xcd * 4 + (bid >> 6 ^ 3);
  const int g = lane >> 4, c = lane & 15;
  const int i8 = b | bid >> 6 << 1, wi = i8 * 8 + xcd;
  const bool is_up = p3 && has_w, modc = part == 2 && wi < 16;
  const size_t base = ((size_t)(b * NH + head) * SK + (SK - WIN)) * DH;
  const rsrc_t rk = mk_rsrc((const u16*)a_kc + base, WIN * DH * 2), rv = mk_rsrc((const u16*)a_vc + base, WIN * DH * 2);
  int tau = te1 & 0xffff;
  const int tauw = te0 >> 24 << 7, wdef = te0 >> 20 & 15;
  int due = P.NF * 256, now = 0;
  auto gate = [&]() {
    while (now < due) { now = (int)(((u32)wall_clock64() - t0) << 8); if (now < due) __builtin_amdgcn_s_sleep(1); }
    due += tau;
  };
  auto until = [&](int t) { while ((int)(((u32)wall_clock64() - t0) << 8) < t); };
  const u32 pslot = dup(G_P + head * 2048 + (b * 4 + part) * 16);
  const u32 die = __builtin_amdgcn_s_getreg(20 | (2 << 6));
  const bool voter = p3 && i8 == 1, timer = p3 && i8 < 2;
  int vote = 0, lag = 0, shift = 0;
  const u32 pbo = dup(G_PB) + (xcd * 8 + wave) * 128;
  u32 near = 0;
  u32x2 ep; u32 xs0, xs1, tch1; u32x4 xr0, xr1;
  const rsrc_t rx = mk_rsrc(part == 2 ? cnd : x, (part == 1 || (part == 2 && has_w)) ? 2048u : 0u);
  auto small_ops = [&]() {
    ep = ld8<AUX_SC1>(wsr, wave == 7 ? WS_EPOCH : OOB, 0);
    xs0 = __builtin_amdgcn_raw_buffer_load_b16(rx, tid * 2, 0, 0); xs1 = __builtin_amdgcn_raw_buffer_load_b16(rx, tid * 2, 1024, 0);
    xr0 = ld16<0>(rx, part == 1 ? lane * 16 : OOB, 0); xr1 = ld16<0>(rx, part == 1 ? lane * 16 : OOB, 1024);
    tch1 = __builtin_amdgcn_raw_buffer_load_b32(mk_rsrc(w2 + (size_t)blk * 32 * 512, is_up ? 64u : 0u), tid == 384 ? 0 : OOB, 0, AUX_NT);
  };
  if (has_w) {
    if (modc) now = 1 << 30;
#pragma nounroll
    while (wleft > wdef) { gate(); due += tauw - tau; piece(); }
    now = 0;
  }
  due = max(due, P.TK * 256);
  SB();
  u32 sot = sa_ & 0xffc00u;
  {
    const u32 s1 = (bl0 + lane - 1 - (lane >= SK0 ? NL - nl : 0)) ^ (124 + part);
#pragma unroll
    for (int k = 0; k < 7; ++k) sot ^= (u32)-(int)(s1 >> k & 1u) & col(P, k);
    PIN(sot);
  }
  u32 tag = 0;
  u32x4 bf[8];
  auto wo_prep = [&](int s0 = 0, int s1 = 2) {
    const u32 wi_ = wimg + wave * 8192;
    const int q = (lane >> 2) & 3, pp = lane & 3;
#pragma unroll
    for (int s = s0; s < s1; ++s) {
#pragma unroll
      for (int t = 0; t < 4; ++t) {
        const int r0 = s * 32 + g * 8 + q, r1 = r0 + 4, ch = t * 2 + (pp >> 1);
        const u32x2 lo = tr_read(wi_ + r0 * 128 + ((ch ^ (r0 & 7)) * 16) + (pp & 1) * 8), hi = tr_read(wi_ + r1 * 128 + ((ch ^ (r1 & 7)) * 16) + (pp & 1) * 8);
        bf[s * 4 + t] = u32x4{lo[0], lo[1], hi[0], hi[1]};
      }
    }
  };
  auto gemv_go = [&](u32 hh) {
    sm.hb[tid] = (u16)hh; sm.hb[512 + tid] = (u16)(hh >> 16);
    WAVE_SYNC();
    const u32x4* a = (c & 11) == 0 ? reinterpret_cast<const u32x4*>(&sm.hb[(c >> 2) * 512 + wave * 64 + g * 8]) : &sm.zero[0];
    const u32x4 a0 = a[0], a1 = a[(c & 11) == 0 ? 4 : 0];
    const f32x4 z = {0.f, 0.f, 0.f, 0.f};
    f32x4 d[4];
#pragma unroll
    for (int t = 0; t < 4; ++t) d[t] = mfma16(a0, bf[t], z);
#pragma unroll
    for (int t = 0; t < 4; ++t) d[t] = mfma16(a1, bf[4 + t], d[t]);
    if (lane < 32) {
#pragma unroll
      for (int t = 0; t < 4; ++t) sm.r2[(t * 16 + c) * 20 + g * 8 + wave] = d[t][0];
    }
  };
  auto mod_stage = [&]() {
    wo_prep();
    gemv_go(pack_bf(silu_f(lo_f(xs0)), silu_f(lo_f(xs1))));
    __builtin_amdgcn_s_waitcnt(0xc07f);
    if (lane == 0) *LDSP(volatile u32, (u32)(uintptr_t)&sm.mflag[wave]) = 1u;
    if (wave == 7) {
#pragma nounroll
      for (int spin = 0; spin < SPIN_MAX; ++spin) {
        const u32 f = *LDSP(volatile u32, (u32)(uintptr_t)&sm.mflag[lane & 7]);
        if (all_ok(f != 0u)) break;
        __builtin_amdgcn_s_sleep(1);
      }
      CFENCE();
      st8_sc1(wsr, G_MOD + (blk * 64 + lane) * 8, pack_bf(sum8(&sm.r2[lane * 20]), sum8(&sm.r2[lane * 20 + 8])), tag);
    }
    STAMP(9);
  };
  if (lane < 4) sm.zero[lane] = u32x4{0u, 0u, 0u, 0u};
  if (part == 2 && has_w) {
    if (tid < 8) sm.mflag[tid] = 0u;
    LDS_BARRIER();
    if (__builtin_expect(modc, 0)) {
      small_ops();
      __builtin_amdgcn_raw_buffer_store_b128(u32x4{SENT, SENT, SENT, SENT}, wsr, G_U + wi * 10240 + tid * 16, 0, AUX_SC1);
      __builtin_amdgcn_raw_buffer_store_b128(u32x4{SENT, SENT, SENT, SENT}, wsr, tid < 128 ? G_U + wi * 10240 + tid * 16 : OOB, 8192, AUX_SC1);
      asm volatile("s_waitcnt vmcnt(0)" ::: "memory");
      tag = next_tag(__builtin_amdgcn_readfirstlane(ep[0]));
      mod_stage();
      __builtin_amdgcn_s_barrier();
    }
  }
  SB();
  const int fd = wi * 8 + (lane & 3) * 2;
  const int ub = wave >> 2, ud = (wave & 3) * 128 + c * 8 + g * 2;
  u32x4 sg[3]; u32 sx2;
  auto side_loads = [&]() {
    const bool fin = part == 1 && wave == 0;
    sg[0] = ld16<AUX_SC1>(wsr, is_up ? G_MOD + (1024 + ud) * 8 : (fin ? G_MOD + (1024 + fd) * 8 : OOB), 0);
    sg[1] = ld16<AUX_SC1>(wsr, is_up ? G_MOD + (1536 + ud) * 8 : (fin ? G_MOD + (2560 + fd) * 8 : OOB), 0);
    sg[2] = ld16<AUX_SC1>(wsr, is_up ? G_MOD + (2048 + ud) * 8 : OOB, 0);
    sx2 = __builtin_amdgcn_raw_buffer_load_b32(mk_rsrc(x, (is_up || fin) ? 2048u : 0u), is_up ? ub * 1024 + ud * 2 : (g & 1) * 1024 + fd * 2, 0, 0);
  };
  auto side_pin = [&]() { PIN(sg[0]); PIN(sg[1]); PIN(sg[2]); PIN(sx2); };
  const u32 mlo = G_ML + (head * 2 + b) * 64 + (lane & 3) * 16;
  float fmrg = 0.f;
  auto merge_factor = [&](const u32x4& ml) {
    const float mi = u2f(ml[0]);
    float M = fmaxf(mi, dpp<XOR1>(mi)); M = fmaxf(M, dpp<XOR2>(M));
    const float e = __expf(mi - M);
    float L = e * u2f(ml[2]);
    L += dpp<XOR1>(L); L += dpp<XOR2>(L);
    fmrg = rdl(e * __builtin_amdgcn_rcpf(L), part);
  };
  f32x2 ga = {0.f, 0.f}, xin = {0.f, 0.f}, cc1 = {0.f, 0.f};
  float Af = 0.f, Bf = 0.f;
  const int hpos = ub * 512 + ud;
  auto up_prep = [&]() {
    if (!all_ok((sg[0][1] == tag) & (sg[0][3] == tag) & (sg[1][1] == tag) & (sg[1][3] == tag) & (sg[2][1] == tag) & (sg[2][3] == tag))) gather16<3>(wsr, G_MOD + (1024 + ud) * 8, 512 * 8, tag, sg);
    ga = f32x2{ub ? hi_f(sg[0][0]) : lo_f(sg[0][0]), ub ? hi_f(sg[0][2]) : lo_f(sg[0][2])};
    cc1 = f32x2{rbf(1.f + (ub ? hi_f(sg[2][0]) : lo_f(sg[2][0]))), rbf(1.f + (ub ? hi_f(sg[2][2]) : lo_f(sg[2][2])))};
    xin = f32x2{lo_f(sx2), hi_f(sx2)};
    u32* hp = reinterpret_cast<u32*>(&sm.hb[hpos]);
    hp[512] = pack_bf(cc1[0], cc1[1]);
    hp[1024] = ub ? perm_hi(sg[1][0], sg[1][2]) : perm_lo(sg[1][0], sg[1][2]);
  };
  auto up_frags = [&]() {
    const int pp = lane & 3;
    const u32 a = wimg + (pp & 1) * 32768 + ((pp >> 1) * 32 + g * 8 + ((lane >> 2) & 3)) * 64 + (((wave >> 1) ^ ((g & 1) | (pp & 2))) * 16) + (wave & 1) * 8;
#pragma unroll
    for (int rb = 0; rb < 8; ++rb) {
      const u32x2 lo = tr_read(a + rb * 4096), hi = tr_read(a + rb * 4096 + 256);
      bf[rb] = u32x4{lo[0], lo[1], hi[0], hi[1]};
    }
  };
  const u32x4* ap = reinterpret_cast<const u32x4*>(&sm.hb[((lane >> 3) & 1) * 512 + (lane & 1) * 32 + g * 8]);
  const bool par = lane & 8;
  auto limit = [&](auto sc) {
    constexpr int s = sc.v;
    WAIT_VM(window(s, 1 + (PRM0.W[s >> 3] >> (4 * (s & 7)) & 7)));
    SB();
  };
  auto sample = [&]() { if (timer) lag += (int)(((u32)wall_clock64() - t0) << 8) - due + ((int)__builtin_amdgcn_s_getreg(7 | (3 << 11)) + 1) * tau; };
  auto report = [&](u32 dst) {
    if (timer) {
      if (lane == 0) sm.mflag[wave] = vote * 65536 + min(lag >> 8, 4000);
      lag = 0;
      LDS_BARRIER();
      if (wave == 7) {
        int s = sm.mflag[lane & 7];
        s += __builtin_amdgcn_mov_dpp(s, XOR1, 0xf, 0xf, true); s += __builtin_amdgcn_mov_dpp(s, XOR2, 0xf, 0xf, true); s += __builtin_amdgcn_mov_dpp(s, XOR7, 0xf, 0xf, true);
        if (lane < 8) st8_sc1(wsr, dst + lane * 128 + wi * 8, s, tag);
      }
    }
    SB();
  };
  float l0 = 0.f, l1 = 0.f;
  u32x2 gmd[2], tbg;
  int t1 = 0;
#pragma unroll
  for (int i = SK0; i < SK1; ++i) kr[i] = u32x4{0u, 0u, 0u, 0u};
  const int first = SK0 + NL - nl;
  auto left_out = [&](int i) { int f = first; asm volatile("" : "+s"(f)); return i < f; };
  sfor<1, NL>([&](auto ic) {
    constexpr int i = ic.v;
    if constexpr (i >= SK0 && i < SK1) { if (left_out(i)) return true; }
    const u32 so = __builtin_amdgcn_readlane(sot, i);
    gate();
    if constexpr ((i >= TBS && i <= TBP) || (i >= RPS && i <= RPP)) sample();
    SB();
    if constexpr (i >= SK0 && i < SK1) ld16_into(kr[i], rk, lane * 16, so); else kr[i] = ld16<AUX_NT>(rk, lane * 16, so);
    if constexpr (i == SMO) small_ops();
    if constexpr (i == SMO + 1) { if (voter) __builtin_amdgcn_raw_buffer_store_b32(0u, wsr, pbo + (lane >> 5) * 8192 + (lane & 31) * 4, 0, AUX_SC1); }
    if constexpr (i == SK1) tbg = ld8<AUX_SC1>(wsr, wave == 7 ? G_VT + xcd * 128 + c * 8 : OOB, 0);
    if constexpr (i == TBL) tbg = ld8<AUX_SC1>(wsr, wave == 7 ? G_TB + xcd * 128 + c * 8 : OOB, 0);
    if constexpr (i == QP) { gmd[0] = ld8<AUX_SC1>(wsr, part == 1 ? G_MOD + tid * 8 : OOB, 0); gmd[1] = ld8<AUX_SC1>(wsr, part == 1 ? G_MOD + tid * 8 : OOB, 4096); }
    SB();
    SB();
    if (i >= 4) limit(ic);
    if (i == 1) STAMP(25);
    if (i == 8) STAMP(12);
    if (i == 20) STAMP(21);
    if (i == 32) STAMP(22);
    if (i == TBP) {
      if (timer) {
        if (lane == 0) sm.mflag[wave] = lag;
        lag = 0;
        LDS_BARRIER();
        if (wave == 7) {
          int s = sm.mflag[lane & 7];
          s += __builtin_amdgcn_mov_dpp(s, XOR1, 0xf, 0xf, true); s += __builtin_amdgcn_mov_dpp(s, XOR2, 0xf, 0xf, true); s += __builtin_amdgcn_mov_dpp(s, XOR7, 0xf, 0xf, true);
          if (lane < 8) st8_sc1(wsr, G_TB + lane * 128 + wi * 8, t0 << 10 | (u32)min(s / (2048 * (TBP - TBS + 1)), 1023), tag);
        }
      }
      SB();
    }
    if (i == PRB) {
      if (voter) {
        u64 ta, tb, tc; u32 dm;
        const u32 pa = pbo + (wave & 1) * 8192;
        asm volatile("s_memtime %0\n\ts_waitcnt lgkmcnt(0)\n\ts_load_dword %3, %4, %5\n\ts_waitcnt lgkmcnt(0)\n\ts_memtime %1\n\ts_waitcnt lgkmcnt(0)\n\t"
                     "s_load_dword %3, %4, %6\n\ts_waitcnt lgkmcnt(0)\n\ts_memtime %2\n\ts_waitcnt lgkmcnt(0)"
                     : "=&s"(ta), "=&s"(tb), "=&s"(tc), "=&s"(dm) : "s"(ws), "s"(pa), "s"(pa ^ 8192u));
        const int d = min(max((int)(2 * (u32)tb - (u32)ta - (u32)tc), -500), 500);
        vote = ((wave ^ die) & 1) ? -d : d;
        STAMP(26);
      }
      SB();
    }
    if (i == TBU) {
      if (wave == 7) {
        const bool ok = tbg[1] == tag;
        float lf = ok ? (float)min(max((int)((t0 << 10 | 1023u) - tbg[0]) >> 10, -300), 300) : 0.f, nf = ok ? 1.f : 0.f, lg = ok ? (float)(tbg[0] & 1023u) : 0.f;
        lf += dpp<XOR1>(lf); nf += dpp<XOR1>(nf); lg += dpp<XOR1>(lg); lf += dpp<XOR2>(lf); nf += dpp<XOR2>(nf); lg += dpp<XOR2>(lg);
        lf += dpp<XOR7>(lf); nf += dpp<XOR7>(nf); lg += dpp<XOR7>(lg); lf += dpp<XOR8>(lf); nf += dpp<XOR8>(nf); lg += dpp<XOR8>(lg);
        const float rn = __builtin_amdgcn_rcpf(fmaxf(nf, 1.f));
        shift = __builtin_amdgcn_readfirstlane(min(max((int)((lg * rn - (float)P.GS) * (float)P.GB * 0.125f), 0), 150));
        if (lane == 0) sm.bc = u32x2{tag, t0 - (u32)(int)(lf * rn) + shift};
      }
      LDS_BARRIER();
      const u32x2 bc = sm.bc;
      tag = __builtin_amdgcn_readfirstlane(bc[0]);
      t1 = (__builtin_amdgcn_readfirstlane((int)(bc[1] - t0)) + TEND + P.X1) * 256;
      tau -= __builtin_amdgcn_readfirstlane((int)((float)(int)(t0 - bc[1]) * 256.f * __builtin_amdgcn_rcpf((float)(nl - TBU - 1))));
      SB();
    }
    if (i == RPP) report(G_VT);
    if (i == LNS) {
      tag = next_tag(__builtin_amdgcn_readfirstlane(ep[0]));
      if (part == 1) {
        float x0[8], x1[8], mb0, ri0, mb1, ri1;
#pragma unroll
        for (int k = 0; k < 4; ++k) { x0[2 * k] = lo_f(xr0[k]); x0[2 * k + 1] = hi_f(xr0[k]); x1[2 * k] = lo_f(xr1[k]); x1[2 * k + 1] = hi_f(xr1[k]); }
        ln_stats2(x0, x1, mb0, ri0, mb1, ri1);
        l0 = rbf(rbf(lo_f(xs0) - mb0) * ri0); l1 = rbf(rbf(lo_f(xs1) - mb1) * ri1);
      }
      PIN(l0); PIN(l1);
      SB();
    }
    if (i == WUSE) {
      PIN(tch1); PIN(tch2);
      if ((part == 1 || (part == 2 && has_w && !modc))) {
        WAIT_VM(WK); CFENCE();
        if (part == 2) mod_stage(); else wo_prep();
      }
#pragma unroll
      for (int k = 0; k < 8; ++k) PIN(bf[k]);
      SB();
    }
    if (i == QP + QD) {
      if (part == 1) {
        u32 gm[2] = {gmd[0][0], gmd[1][0]};
        if (!all_ok((gmd[0][1] == tag) & (gmd[1][1] == tag))) { STAMP(11); gather8<2>(wsr, G_MOD + tid * 8, 512 * 8, tag, gm); }
        STAMP(5);
        gemv_go(pack_bf(rbf(l0 * rbf(1.f + lo_f(gm[1]))) + lo_f(gm[0]), rbf(l1 * rbf(1.f + hi_f(gm[1]))) + hi_f(gm[0])));
      }
      SB();
    }
    if (i == QP + QD + 1) {
      if (part == 1) {
        LDS_BARRIER();
        if (wave == 0) {
          const int bb = lane >> 5, jq = lane & 31;
          st8_sc1(wsr, G_Q + (bb * 2048 + blk * 32 + jq) * 8, pack_bf(sum8(&sm.r2[2 * jq * 20 + bb * 8]), sum8(&sm.r2[(2 * jq + 1) * 20 + bb * 8])), tag);
        }
        STAMP(6);
      }
      SB();
    }
    return true;
  });
  PIN(sot);
  if (wave == 7) {
    const bool ok = tbg[1] == tag;
    float vt = ok ? (float)((int)tbg[0] >> 16) : 0.f, lg = ok ? (float)(tbg[0] & 0xffffu) : 0.f, nf = ok ? 8.f * (RPP - RPS + 1) : 0.f;
    vt += dpp<XOR1>(vt); lg += dpp<XOR1>(lg); nf += dpp<XOR1>(nf); vt += dpp<XOR2>(vt); lg += dpp<XOR2>(lg); nf += dpp<XOR2>(nf);
    vt += dpp<XOR7>(vt); lg += dpp<XOR7>(lg); nf += dpp<XOR7>(nf); vt += dpp<XOR8>(vt); lg += dpp<XOR8>(lg); nf += dpp<XOR8>(nf);
    lg *= __builtin_amdgcn_rcpf(fmaxf(nf, 1.f));
    if (lane == 0) sm.bc = u32x2{(__float_as_uint(vt) & 0x80000000u) | (u32)min(shift * P.DG * 32, P.EW * 256), (u32)min(max((int)((lg - (float)P.B0) * (float)(P.BG * 32)) - shift * P.D0 * 32, 0), 200 * 256)};
  }
  LDS_BARRIER();
  {
    const u32x2 bc = sm.bc;
    const int st = __builtin_amdgcn_readfirstlane(bc[1]), sp = (st * P.BF) >> 3;
    near = ((__builtin_amdgcn_readfirstlane(bc[0]) >> 31 ^ die ^ P.NR ^ 1) & 1) * 8192u;
    t1 += st + (__builtin_amdgcn_readfirstlane(bc[0]) & 0xffff);
    due += P.TG * 256 + sp;
    tau = (te1 >> 16) + __builtin_amdgcn_readfirstlane((int)((float)(st - sp) * __builtin_amdgcn_rcpf((float)(nl - 3))));
  }
  const u32 qoff = G_Q + ((b * 32 + head) * 64 + c * 4) * 8;
  u32x4 qg[2], mlg;
  u32 qq[4];
  constexpr int NB = (NL + 15) / 16;
  float xlo, sc[NB], pr[NB], m = 0.f;
  f32x4 acc = {0.f, 0.f, 0.f, 0.f};
  u32 pb[NB], pm[4], pl8[8], cpf;
  const u32 adst0 = lds_addr(lane < 32 ? &sm.red[wave][c * 8 + g * 4] : &sm.r2[(lane - 32) * 4]);
  u32 po, rdst, rsrc, xo;
  auto wo_addr = [&]() {
    po = dup(G_P + xcd * 8192) + tid * 16 + near;
    rdst = lane < 32 ? (u32)(uintptr_t)&sm.red2[wave][0] + lane * 16 : (u32)(uintptr_t)&sm.r2[0] + (lane - 32) * 16;
    rsrc = (u32)(uintptr_t)&sm.red2[0][((wave & 1) * 16 + (lane & 15)) * 4 + (lane >> 4)];
    xo = dup(G_XP + xcd * 2048 + (wave & 1) * 1024 + (blk * 64 + (lane & ~1)) * 2) + (lane & 1) * 8192;
    PIN(po); PIN(rdst); PIN(rsrc); PIN(xo);
  };
  u32 pso = (wave == 0 && (g & 1) == 0) ? pslot + (c >> 3) * 1024 + (c & 3) * 256 + (c >> 2 & 1) * 128 + (g >> 1) * 8192 : OOB;
  PIN(pso);
  const u32 nadd = wdef ? P.LW : (is_up ? P.LU : 0);
  u32 nto = tid < 2 ? dup(G_NT) + bid * 8 + tid * 8192 : OOB;
  PIN(nto);
  auto notices = [&]() -> int {
    u32 off = dup(G_NT) + near + lane * 16;
    PIN(off);
    until(due);
    int mx;
#pragma nounroll
    for (int spin = 0; spin < SPIN_MAX; ++spin) {
      CFENCE();
      const u32x4 ga_ = ld16<AUX_SC1>(wsr, off, 0), gb_ = ld16<AUX_SC1>(wsr, off, 1024);
      mx = max(max((int)(ga_[0] - t0), (int)(ga_[2] - t0)), max((int)(gb_[0] - t0), (int)(gb_[2] - t0)));
      if (all_ok((((ga_[1] ^ tag) | (ga_[3] ^ tag)) | ((gb_[1] ^ tag) | (gb_[3] ^ tag))) == 0u)) break;
      for (int k = 0; k < P.AG; ++k) __builtin_amdgcn_s_sleep(1);
    }
    if (all_ok(mx <= (t1 >> 8) - P.AC)) return t1;
    mx = max(mx, __builtin_amdgcn_mov_dpp(mx, XOR1, 0xf, 0xf, true)); mx = max(mx, __builtin_amdgcn_mov_dpp(mx, XOR2, 0xf, 0xf, true));
    mx = max(mx, __builtin_amdgcn_mov_dpp(mx, XOR7, 0xf, 0xf, true)); mx = max(mx, __builtin_amdgcn_mov_dpp(mx, XOR8, 0xf, 0xf, true));
    mx = max(max(__builtin_amdgcn_readlane(mx, 0), __builtin_amdgcn_readlane(mx, 16)), max(__builtin_amdgcn_readlane(mx, 32), __builtin_amdgcn_readlane(mx, 48)));
    return max(min(mx, 1 << 20) * 256, t1 + P.L0 * 256) + P.L1 * 256;
  };
  auto pv_row = [&](auto jc) {
    constexpr int i = jc.v;
    const u32 pv = __float_as_uint(bcast16(u2f(pb[i >> 4]), i & 15));
    acc = mfma16(u32x4{pv & pm[0], pv & pm[1], pv & pm[2], pv & pm[3]}, vr[i], acc);
    PIN(acc);
    return true;
  };
  sfor<0, NL - 1>([&](auto ic) {
    constexpr int v = ic.v;
    if constexpr (v >= SK0 && v < SK1) { if (left_out(v)) return true; }
    if constexpr (v > 0) {
      const u32 so = __builtin_amdgcn_readlane(sot, v);
      if constexpr (v >= SK1) { if (v == NL - P.NE) tau = (tau * P.FE) >> 4; }
      gate();
      if constexpr (v >= RVS && v <= RVP) sample();
      SB();
      if constexpr (v >= SK0 && v < SK1) ld16_into(vr[v], rv, lane * 16, so); else vr[v] = ld16<AUX_NT>(rv, lane * 16, so);
      SB();
      SB();
    }
    if constexpr (v == SK0 - 1) {
#pragma unroll
      for (int i = SK0; i < SK1; ++i) { vr[i] = u32x4{0u, 0u, 0u, 0u}; PIN(vr[i]); }
    }
    if constexpr (v == VQ - 4) { qg[0] = ld16<AUX_SC1>(wsr, qoff, 0); qg[1] = ld16<AUX_SC1>(wsr, qoff + 16, 0); }
    if constexpr (v == VSIDE) side_loads();
    if constexpr (v == CPF) {
      u64 pc; u32 dlt, len;
      asm volatile("s_getpc_b64 %0\n.Lcpf_here:\n\ts_mov_b32 %1, .Ltail_start-.Lcpf_here\n\ts_mov_b32 %2, .Lend_a-.Ltail_start\n\ts_max_i32 %2, %2, .Lend_b-.Ltail_start\n\ts_max_i32 %2, %2, .Lend_c-.Ltail_start\n\ts_max_i32 %2, %2, .Lend_d-.Ltail_start\n\ts_max_i32 %2, %2, .Lend_e-.Ltail_start" : "=s"(pc), "=s"(dlt), "=s"(len) :: "scc");
      pc += dlt;
      const rsrc_t rc = mk_rsrc((const void*)pc, (part == 3 && i8 == 1) ? len : 0u);
      cpf = __builtin_amdgcn_raw_buffer_load_b32(rc, tid * 64, 0, 0);
    }
    if constexpr (v == SK1 + 2) PIN(cpf);
    if constexpr (v == RVP) report(G_VT + 1024);
    if constexpr (v == RVL) tbg = ld8<AUX_SC1>(wsr, wave == 7 ? G_VT + 1024 + xcd * 128 + c * 8 : OOB, 0);
    if constexpr (v == SK1) mlg = ld16<AUX_SC1>(wsr, wave == 0 ? mlo : OOB, 0);
    SB();
    if constexpr (NL + v < 72) limit(IC<NL + v>{});
    if constexpr (v >= SK0 + WV) { WAIT_VM(v < SK1 ? WV : WV + 1); SB(); }
    if constexpr (v == VQ) {
      if (!all_ok((qg[0][1] == tag) & (qg[0][3] == tag) & (qg[1][1] == tag) & (qg[1][3] == tag))) { STAMP(20); gather16<2>(wsr, qoff, 16, tag, qg); }
      STAMP(1); WSTAMP(1);
      qq[0] = qg[0][0]; qq[1] = qg[0][2]; qq[2] = qg[1][0]; qq[3] = qg[1][2];
    }
    if constexpr (v >= VQ && v < VS) {
      constexpr int sl = v - VQ, bt = sl >> 1;
      float xs[8];
#pragma unroll
      for (int i = 0; i < 8; ++i) {
        float d = 0.f;
        if (sl * 8 + i < NL) {
#pragma unroll
          for (int k = 0; k < 4; ++k) d = dot2(kr[sl * 8 + i][k], qq[k], d);
        }
        xs[i] = d;
      }
#pragma unroll
      for (int h = 0; h < 4; ++h) xs[h] = sel_add<XOR7>(xs[h], xs[h + 4], c & 4);
#pragma unroll
      for (int h = 0; h < 2; ++h) xs[h] = sel_add<XOR2>(xs[h], xs[h + 2], c & 2);
      xs[0] = sel_add<XOR1>(xs[0], xs[1], c & 1);
      if constexpr ((sl & 1) || sl == NSL - 1) {
        const float t = (sl & 1) ? sel_add<XOR8>(xlo, xs[0], c & 8) : xs[0] + dpp<XOR8>(xs[0]);
        const float s = rbf(rbf(t) * 0.08838834764831845f);
        sc[bt] = (bt * 16 + c < SK0 || (bt * 16 + c >= first && bt * 16 + c < NL)) ? s : -__builtin_inff();
        PIN(sc[bt]);
      } else {
        xlo = xs[0];
        PIN(xlo);
      }
    }
    if constexpr (v == VS) {
      float mloc = sc[0];
#pragma unroll
      for (int bt = 1; bt < NB; ++bt) mloc = fmaxf(mloc, sc[bt]);
      m = fmaxf(wave_max(mloc), -3.0e38f);
      float ls = 0.f;
#pragma unroll
      for (int bt = 0; bt < NB; ++bt) { pr[bt] = __expf(sc[bt] - m); ls += pr[bt]; }
      const float l = wave_sum(ls);
      if (lane == 0) { sm.ml[wave][0] = m; sm.ml[wave][1] = l; }
      if (c == ((NL - 1) & 15)) sm.pl[g][wave] = pr[NB - 1];
      STAMP(2); WSTAMP(2);
    }
    if constexpr (v >= SK1) pv_row(IC<v - SK1 + SK0 - WV>{});
    if constexpr (v < SK0 || v >= SK0 + WV) sfor<cons_end(v - 1), cons_end(v)>(pv_row);
    if constexpr (v == RVU) {
      if (wave == 7) {
        const bool ok = tbg[1] == tag;
        float lg = ok ? (float)(tbg[0] & 0xffffu) : 0.f, nf = ok ? 8.f * (RVP - RVS + 1) : 0.f;
        lg += dpp<XOR1>(lg); nf += dpp<XOR1>(nf); lg += dpp<XOR2>(lg); nf += dpp<XOR2>(nf); lg += dpp<XOR7>(lg); nf += dpp<XOR7>(nf); lg += dpp<XOR8>(lg); nf += dpp<XOR8>(nf);
        lg *= __builtin_amdgcn_rcpf(fmaxf(nf, 1.f));
        if (lane == 0) sm.bc[1] = (u32)min(max((int)((lg - (float)P.F0) * (float)(P.FG * 32)), 0), 200 * 256);
      }
      LDS_BARRIER();
      const int st = __builtin_amdgcn_readfirstlane(sm.bc[1]), sp = (st * P.FF_) >> 3;
      t1 += st;
      due += sp;
      tau += __builtin_amdgcn_readfirstlane((int)((float)(st - sp) * __builtin_amdgcn_rcpf((float)(nl - RVU - 3))));
    }
    if constexpr (v == SK1 + 1) {
      float p[8];
#pragma unroll
      for (int w = 0; w < 8; ++w) p[w] = sm.pl[g][w] * __expf(sm.ml[w][0] - m);
#pragma unroll
      for (int w = 0; w < 8; ++w) pl8[w] = pack_bf(p[w], p[w]);
#pragma unroll
      for (int w = 0; w < 8; ++w) PIN(pl8[w]);
    }
    if constexpr (v == MLS - 1) { side_pin(); if (is_up) up_prep(); PIN(ga); PIN(xin); PIN(cc1); }
    if constexpr (v == UPREP - 1) wo_addr();
    if constexpr (v == UPREP) {
#pragma unroll
      for (int k = 0; k < 8; ++k) asm volatile("" : "=v"(bf[k]));
      if (part == 0) wo_prep(); else if (is_up) up_frags();
#pragma unroll
      for (int k = 0; k < 8; ++k) PIN(bf[k]);
    }
    if constexpr (v == UPREP + 1) {
      if (is_up) {
        const u32x4* a1 = ap + 128 + ((lane >> 1) & 1) * 128;
        f32x4 d0 = {0.f, 0.f, 0.f, 0.f}, d1 = {0.f, 0.f, 0.f, 0.f};
#pragma unroll
        for (int rb = 0; rb < 4; ++rb) { d0 = mfma16(a1[rb * 8], bf[rb], d0); d1 = mfma16(a1[(rb + 4) * 8], bf[rb + 4], d1); }
        Af = par ? d0[1] + d1[1] : d0[0] + d1[0]; Bf = par ? d0[3] + d1[3] : d0[2] + d1[2];
        Af += dpp<XOR8>(Af); Bf += dpp<XOR8>(Bf);
      }
      PIN(Af); PIN(Bf);
    }
    if (v == 10 || v == 16) STAMP(15 + v / 6);
    if (v == 20) STAMP(18);
    if (v == 32) STAMP(19);
    if constexpr (v == MLS) {
      LDS_BARRIER();
      float mm[8], ll[8];
#pragma unroll
      for (int w = 0; w < 8; ++w) { const f32x2 t = *reinterpret_cast<const f32x2*>(&sm.ml[w][0]); mm[w] = t[0]; ll[w] = t[1]; }
      float Mwg = mm[0], Lwg = 0.f;
#pragma unroll
      for (int w = 1; w < 8; ++w) Mwg = fmaxf(Mwg, mm[w]);
      if (wave == 0) {
#pragma unroll
        for (int w = 0; w < 8; ++w) Lwg = fmaf(__expf(mm[w] - Mwg), ll[w], Lwg);
        if (lane == 0) st16_sc1(wsr, G_ML + ((head * 2 + b) * 4 + part) * 16, __float_as_uint(Mwg), __float_as_uint(Lwg), tag);
      }
      const float fw = __expf(m - Mwg);
      m = Mwg;
#pragma unroll
      for (int bt = 0; bt < NB; ++bt) { pb[bt] = pack_bf(pr[bt] * fw, pr[bt] * fw); PIN(pb[bt]); }
#pragma unroll
      for (int k = 0; k < 4; ++k) { pm[k] = (c >> 1 & 3) == k ? ((c & 1) ? 0xffff0000u : 0x0000ffffu) : 0u; PIN(pm[k]); }
    }
    SB();
    return true;
  });
  asm volatile(".Ltail_start:");
  if (wave != 0) {
    pv_row(IC<SK0 - 1>{});
    WAIT_VM(WV); SB();
    sfor<NL - 1 - WV, NL - 1>(pv_row);
    *LDSP(f32x4, adst0) = acc;
    WSTAMP(14);
    LDS_BARRIER();
  } else {
    u32x4 xr[8], am[8];
#pragma unroll
    for (int w = 0; w < 8; ++w) am[w] = u32x4{pl8[w] & pm[0], pl8[w] & pm[1], pl8[w] & pm[2], pl8[w] & pm[3]};
#pragma unroll
    for (int w = 0; w < 8; ++w) PIN(am[w]);
    {
      const u32 so = __builtin_amdgcn_readlane(sot, NL - 1);
      gate();
      SB();
#pragma unroll
      for (int w = 0; w < 8; ++w) xr[w] = ld16<AUX_NT>(rv, lane * 16, so ^ ((w & 1) ? col(P, 7) : 0u) ^ ((w & 2) ? col(P, 8) : 0u) ^ ((w & 4) ? col(P, 9) : 0u));
      SB();
    }
    u32 rsum = lds_addr(&sm.red[0][c * 8 + (g & 1) * 4]);
    pv_row(IC<SK0 - 1>{});
    WAIT_VM(WV + 8); SB();
    sfor<NL - 1 - WV, NL - 3>(pv_row);
    if (__builtin_expect(!all_ok((mlg[1] == tag) & (mlg[3] == tag)), 0)) {
#pragma nounroll
      for (int spin = 0; spin < SPIN_MAX; ++spin) {
        CFENCE();
        mlg = ld16<AUX_SC1>(wsr, mlo, 0);
        if (all_ok((mlg[1] == tag) & (mlg[3] == tag))) break;
      }
    }
    merge_factor(mlg);
    sfor<NL - 3, NL - 1>(pv_row);
    STAMP(14); WSTAMP(14);
    LDS_BARRIER();
    STAMP(15);
    f32x4 t[8];
#pragma unroll
    for (int w = 1; w < 8; ++w) t[w] = LDSP(f32x4, rsum)[w * 32];
    acc = ((acc + t[1]) + (t[2] + t[3])) + ((t[4] + t[5]) + (t[6] + t[7]));
    PIN(acc);
    STAMP(3);
    SB();
#pragma unroll
    for (int w = 0; w < 8; ++w) acc = mfma16(am[w], xr[w], acc);
  }
  WAIT_VM(0);
  acc *= fmrg;
  {
    const u32 w0 = min(pack_bf(acc[0], acc[1]), SENT - 1u), w1 = min(pack_bf(acc[2], acc[3]), SENT - 1u);
    const auto e0 = __builtin_amdgcn_permlane16_swap(w0, w0, false, false), e1 = __builtin_amdgcn_permlane16_swap(w1, w1, false, false);
    __builtin_amdgcn_raw_buffer_store_b128(u32x4{e0[0], e1[0], e0[1], e1[1]}, wsr, pso, 0, AUX_SC1);
  }
  st8_sc1(wsr, nto, (u32)wall_clock64() + nadd, tag);
  SB();
  STAMP(4);

  if (part == 2) FINISH();
  if (part == 0) {
    __builtin_amdgcn_s_barrier();
#pragma nounroll
    while (wleft > 0) piece();
    bool late = false;
    u32x4 og[2];
    if (wdef) {
      late = true;
      while ((int)(((u32)wall_clock64() - t0) << 8) < t1 - 20 * 256) if (__builtin_amdgcn_s_getreg(7 | (3 << 11)) == 0) { late = false; break; }
      if (__builtin_expect(late, 0)) { until(t1); CFENCE(); og[0] = ld16<AUX_SC1>(wsr, po, 0); SB(); WAIT_VM(1); } else { WAIT_VM(0); }
      CFENCE();
      if (P.WD > 4) wo_prep(0, 1);
      wo_prep(1, 2);
#pragma unroll
      for (int k = 0; k < 8; ++k) PIN(bf[k]);
      SB();
    }
    if (__builtin_expect(!late, 1)) { until(t1); CFENCE(); og[0] = ld16<AUX_SC1>(wsr, po, 0); }
    STAMP(11);
    SB();
    f32x4 d[4];
    auto wo_mm = [&]() {
      const f32x4 z = {0.f, 0.f, 0.f, 0.f};
#pragma unroll
      for (int t = 0; t < 4; ++t) { d[t] = mfma16(og[0], bf[t], z); og[1][t] = __float_as_uint(dpp<XOR8>(u2f(og[0][t]))); }
      SB();
#pragma unroll
      for (int t = 0; t < 4; ++t) d[t] = mfma16(og[1], bf[4 + t], d[t]);
    };
    auto p_bad = [&]() { return !all_ok(max(max(og[0][0], og[0][1]), max(og[0][2], og[0][3])) != SENT); };
    wo_mm();
    if (__builtin_expect(p_bad(), 0)) {
#pragma nounroll
      for (int spin = 0; spin < SPIN_MAX; ++spin) {
        __builtin_amdgcn_s_sleep(1);
        CFENCE();
        og[0] = ld16<AUX_SC1>(wsr, po, 0);
        if (!p_bad()) break;
      }
      wo_mm();
      SB();
      asm volatile(".Lend_d:");
    }
    STAMP(5); WSTAMP(5);
    {
      SB();
      f32x4 y; f32x2 h[4];
#pragma unroll
      for (int t = 0; t < 4; ++t) { h[t] = d[t].xy + d[t].zw; PIN(h[t]); }
#pragma unroll
      for (int t = 0; t < 4; ++t) { y[t] = h[t][0] + h[t][1]; PIN(y[t]); }
      *LDSP(f32x4, rdst) = y;
    }
    STAMP(16);
    LDS_BARRIER();
    STAMP(17);
    if (wave < 2) {
      f32x2 v[4];
#pragma unroll
      for (int w = 0; w < 4; ++w) v[w] = f32x2{LDSP(float, rsrc)[w * 256], LDSP(float, rsrc)[w * 256 + 128]};
      const f32x2 h = (v[0] + v[1]) + (v[2] + v[3]);
      const float y = h[0] + h[1];
      __builtin_amdgcn_raw_buffer_store_b32(min(pack_bf(dpp<0xA0>(y), dpp<0xF5>(y)), SENT - 1u), wsr, xo, 0, AUX_SC1);
      asm volatile(".Lend_a:");
    }
    STAMP(6);
    FINISH();
  }
  if (part == 3) {
    if (!has_w) FINISH();
    u32x4 b2[4];
    {
      __builtin_amdgcn_s_barrier();
      const rsrc_t r2 = mk_rsrc(w2 + (size_t)(blk * 32 + wave * 4) * 512, 4096u);
      const u32 l2 = (u32)(uintptr_t)(&sm.w2b[wave * 4][0]);
#pragma unroll
      for (int n = 0; n < 4; ++n) dma16(r2, (lane ^ (n * 2) ^ ((wave & 2) << 2)) * 16, n * 1024, l2 + n * 1024);
    }
    const u32 tdst = lds_addr(&sm.hb[hpos]);
    const u32 sdst = lds_addr(&sm.stat[wave][lane]);
    const u32 ssrc = lds_addr(&sm.stat[(g >> 1) * 4 + ((lane >> 3) & 3)][(lane & 7) * 4]);
    const u32 adst = lds_addr((c < 4 && (g & 1) == 0) ? &sm.actl[g >> 1][wave * 4 + c] : reinterpret_cast<u16*>(&sm.dummy[lane]));
    const u32 asrc = lds_addr(&sm.actl[lane & 1][g * 8]);
    const u32 apa = lds_addr(&sm.hb[(c >> 3) * 512 + (c & 3) * 64 + ((c >> 2) & 1) * 32 + g * 8]);
    constexpr u32 xs2 = 16384;
    u32 xoff = dup(G_XP) + near + g * 2048 + ub * 1024 + (wave & 3) * 256 + c * 16;
    u32x4 one;
#pragma unroll
    for (int k = 0; k < 4; ++k) one[k] = ((c & 2) == 0 && (c >> 2) == k) ? ((c & 1) ? 0x3f800000u : 0x00003f80u) : 0u;
    PIN(one);
    u32 ooff = dup(G_OP + (blk * 512 + tid) * 8);
    float Afp = Af, Bfp = Bf;
    PIN(xoff); PIN(ooff); PIN(Afp); PIN(Bfp);
    f32x2 gc = ga * cc1, xc = xin * cc1;
    PIN(ga); PIN(xin); PIN(gc); PIN(xc);
    u32 w2a = (u32)(uintptr_t)(&sm.w2b[0][0]) + (g * 8 + ((lane >> 2) & 3)) * 1024 + (wave ^ (g & 1)) * 128 + (lane & 15) * 8;
    PIN(w2a);
    if (wave == 1) {
      due = t1 + P.N1 * 256;
      const int tn = notices();
      due = tn + P.X2 * 256;
      until(due);
    }
    asm volatile("s_waitcnt vmcnt(0)" ::: "memory");
    STAMP(9);
    __builtin_amdgcn_s_barrier();
    STAMP(11);
    CFENCE();
    u32x4 xg[2] = {ld16<AUX_SC1>(wsr, xoff, 0), ld16<AUX_SC1>(wsr, xoff, xs2)};
    SB();
#pragma unroll
    for (int t = 0; t < 4; ++t) {
      const u32x2 lo = tr_read(w2a ^ (t * 32)), hi = tr_read((w2a ^ (t * 32)) + 4096);
      b2[t] = u32x4{lo[0], lo[1], hi[0], hi[1]};
    }
#pragma unroll
    for (int t = 0; t < 4; ++t) PIN(b2[t]);
    SB();
    const f32x4 z4 = {0.f, 0.f, 0.f, 0.f};
    auto xp_bad = [&]() { return !all_ok(max(max(max(xg[0][0], xg[0][1]), max(xg[0][2], xg[0][3])), max(max(xg[1][0], xg[1][1]), max(xg[1][2], xg[1][3]))) != SENT); };
    f32x4 v = mfma16(one, xg[1], mfma16(one, xg[0], z4));
    if (__builtin_expect(xp_bad(), 0)) {
#pragma nounroll
      for (int spin = 0; spin < SPIN_MAX; ++spin) {
        __builtin_amdgcn_s_sleep(1);
        CFENCE();
        xg[0] = ld16<AUX_SC1>(wsr, xoff, 0); xg[1] = ld16<AUX_SC1>(wsr, xoff, xs2);
        if (!xp_bad()) break;
      }
      v = mfma16(one, xg[1], mfma16(one, xg[0], z4));
      SB();
      asm volatile(".Lend_e:");
    }
    STAMP(5); WSTAMP(5);
    {
      const f32x2 a = v.xy, hc = __builtin_elementwise_fma(gc, a, xc), x1 = __builtin_elementwise_fma(ga, a, xin);
      *LDSP(u32, tdst) = pack_bf(hc[0], hc[1]);
      *LDSP(float, sdst) = swap32_add(x1[0] + x1[1], fmaf(x1[0], x1[0], x1[1] * x1[1]));
    }
    STAMP(16);
    LDS_BARRIER();
    STAMP(17);
    {
      const f32x4 z = {0.f, 0.f, 0.f, 0.f};
      const u32x4 af0 = LDSP(u32x4, apa)[0];
      SB();
      const f32x4 s4 = LDSP(f32x4, ssrc)[0], q4 = LDSP(f32x4, ssrc)[8];
      const u32x4 af1 = LDSP(u32x4, apa)[32];
      SB();
      f32x4 d[4];
      d[0] = mfma16(af0, bf[0], z); d[1] = mfma16(af0, bf[1], z);
      SB();
      const f32x2 s2 = s4.xy + s4.zw, q2 = q4.xy + q4.zw;
      float s = s2[0] + s2[1], q = q2[0] + q2[1];
      SB();
      d[2] = mfma16(af0, bf[2], z);
      SB();
      s = swap16_add(s, q);
      SB();
      d[3] = mfma16(af0, bf[3], z);
      SB();
      s += dpp<XOR1>(s); s += dpp<XOR2>(s);
      SB();
      d[0] = mfma16(af1, bf[4], d[0]);
      SB();
      s += dpp<XOR7>(s); s += dpp<XOR8>(s);
      SB();
      d[1] = mfma16(af1, bf[5], d[1]);
      SB();
      const auto sq = __builtin_amdgcn_permlane16_swap(__float_as_uint(s), __float_as_uint(s), false, false);
      const float mean = u2f(sq[0]) * (1.f / 512.f);
      SB();
      d[2] = mfma16(af1, bf[6], d[2]);
      SB();
      const float var = fmaf(u2f(sq[1]), 1.f / 512.f, -mean * mean);
      SB();
      d[3] = mfma16(af1, bf[7], d[3]);
      SB();
      float ri = __builtin_amdgcn_rsqf(fmaxf(var, 0.f) + 1e-5f);
      float cf = fmaf(-ri * mean, Afp, Bfp);
      float rk = ri * -1.44269504f, ck = cf * -1.44269504f;
      PIN(ri); PIN(cf); PIN(rk); PIN(ck);
      SB();
      PIN(d[0]); PIN(d[1]);
      float y = d[0][0] + d[1][1];
      PIN(y); PIN(d[2]);
      y += d[2][2];
      PIN(y); PIN(d[3]);
      y += d[3][3];
      y = __builtin_bit_cast(float, __builtin_amdgcn_update_dpp(__builtin_bit_cast(int, y), __builtin_bit_cast(int, y), XOR8, 0xa, 0xf, false));
      y = swap16_add(y, y);
      const float ex = __builtin_amdgcn_exp2f(fmaf(y, rk, ck));
      y = fmaf(y, ri, cf);
      const float gu = y * dpp<0x104>(y);
      *LDSP(u16, adst) = (u16)pack_bf(gu * __builtin_amdgcn_rcpf(1.f + ex), 0.f);
    }
    STAMP(18);
    LDS_BARRIER();
    STAMP(10);
    {
      const u32x4 af = *LDSP(u32x4, asrc);
      const f32x4 z = {0.f, 0.f, 0.f, 0.f};
      f32x4 d0 = mfma16(af, b2[0], z), d1 = mfma16(af, b2[1], z), d2 = mfma16(af, b2[2], z), d3 = mfma16(af, b2[3], z);
      asm volatile("" : "+v"(d0), "+v"(d1), "+v"(d2), "+v"(d3));
      const bool g1 = g & 1, g2 = g & 2;
      const float e0 = g1 ? d1[0] : d0[0], e1 = g1 ? d1[1] : d0[1], f0 = g1 ? d3[0] : d2[0], f1 = g1 ? d3[1] : d2[1];
      const u32x2 ov = {pack_bf(g2 ? f0 : e0, g2 ? f1 : e1), tag};
      __builtin_amdgcn_raw_buffer_store_b64(ov, wsr, ooff, near ^ 8192u, AUX_SC1);
      __builtin_amdgcn_raw_buffer_store_b64(ov, wsr, ooff, near, AUX_SC1);
    }
    asm volatile(".Lend_b:");
    STAMP(6);
    FINISH();
  }
  if (wave == 0) {
    if (!all_ok((sg[0][1] == tag) & (sg[0][3] == tag) & (sg[1][1] == tag) & (sg[1][3] == tag))) {
      u32x4 t[2];
      gather16<2>(wsr, G_MOD + (1024 + fd) * 8, 1536 * 8, tag, t);
      sg[0] = t[0]; sg[1] = t[1];
    }
    u32 xa;
    {
      due = t1 + P.N2 * 256;
      const int tn = notices();
      due = tn + P.X3 * 256;
      until(due);
    }
    const bool hb = g & 1;
#pragma nounroll
    for (int spin = 0; spin < SPIN_MAX; ++spin) {
      CFENCE();
      xa = __builtin_amdgcn_raw_buffer_load_b32(wsr, dup(G_XP) + near + (g >> 1) * 16384 + (c >> 2) * 2048 + (g & 1) * 1024 + fd * 2, 0, AUX_SC1);
      if (all_ok(xa != SENT)) break;
      __builtin_amdgcn_s_sleep(1);
    }
    float a0 = lo_f(xa), a1 = hi_f(xa);
    a0 += dpp<0x124>(a0); a1 += dpp<0x124>(a1);
    a0 += dpp<XOR8>(a0); a1 += dpp<XOR8>(a1);
    a0 = swap32_add(a0, a0); a1 = swap32_add(a1, a1);
    f32x2 xv = {rbf(lo_f(sx2) + rbf((hb ? hi_f(sg[0][0]) : lo_f(sg[0][0])) * rbf(a0))), rbf(hi_f(sx2) + rbf((hb ? hi_f(sg[0][2]) : lo_f(sg[0][2])) * rbf(a1)))};
    f32x2 gv = {hb ? hi_f(sg[1][0]) : lo_f(sg[1][0]), hb ? hi_f(sg[1][2]) : lo_f(sg[1][2])};
    PIN(xv); PIN(gv);
    if (wi == 0 && lane == 0) __builtin_amdgcn_raw_buffer_store_b32(tag, wsr, WS_EPOCH, 0, AUX_SC1);
    STAMP(10);
    due = max(due + P.X4 * 256, (int)(((u32)wall_clock64() - t0) << 8) + P.GX * 256);
    const bool last = lane < (NUP - 32) * 4;
    u32 o0 = dup(G_OP + (lane >> 2) * 4096 + fd * 8) + near, o2 = last ? o0 : OOB, tg2 = last ? tag : 0u;
    const rsrc_t rout = mk_rsrc(out, 2048u);
    u32 oo = (lane & 44) == 0 ? (g * 512 + fd) * 2 : OOB;
    const u32 pk = (c & 10) ? 0u : ((c & 4) ? 0x3f800000u : 0x00003f80u);
    u32x4 one = {(c & 1) ? 0u : pk, 0u, (c & 1) ? pk : 0u, 0u};
    PIN(o0); PIN(o2); PIN(tg2); PIN(oo); PIN(one);
    __builtin_amdgcn_raw_buffer_store_b32(pack_bf(xv[0], xv[1]), rout, oo, 0, AUX_SC1);
    u32 s1_ = 131072, s2_ = 262144;
    asm volatile("" : "+s"(s1_), "+s"(s2_));
    until(due);
    f32x4 sv;
#pragma nounroll
    for (int spin = 0; spin < SPIN_MAX; ++spin) {
      CFENCE();
      const u32x4 og[3] = {ld16<AUX_SC1>(wsr, o0, 0), ld16<AUX_SC1>(wsr, o0, s1_), ld16<AUX_SC1>(wsr, o2, s2_)};
      sv = f32x4{0.f, 0.f, 0.f, 0.f};
#pragma unroll
      for (int k = 0; k < 3; ++k) sv = mfma16(one, og[k], sv);
      SB();
      u32 bad = ((og[0][1] ^ tag) | (og[0][3] ^ tag) | (og[1][1] ^ tag)) | ((og[1][3] ^ tag) | (og[2][1] ^ tg2) | (og[2][3] ^ tg2));
      PIN(bad);
      SB();
      float s0 = sv[0] + dpp<0x124>(sv[0]), s1 = sv[1] + dpp<0x124>(sv[1]);
      PIN(s0); PIN(s1);
      s0 += dpp<XOR8>(s0); s1 += dpp<XOR8>(s1);
      PIN(s0); PIN(s1);
      __builtin_amdgcn_raw_buffer_store_b32(pack_bf(fmaf(gv[0], s0, xv[0]), fmaf(gv[1], s1, xv[1])), rout, oo, 0, AUX_SC1);
      SB();
      if (all_ok(bad == 0u)) break;
      __builtin_amdgcn_s_sleep(1);
    }
    STAMP(7);
    asm volatile(".Lend_c:");
  }
  FINISH();
}

extern "C" void kernel_launch(void* const* d_in, const int* in_sizes, int n_in,
                              void* d_out, int out_size, void* d_ws, size_t ws_size,
                              hipStream_t stream) {
  fused_kernel<<<256, 512, 0, stream>>>((const u16*)d_in[1], (const u16*)d_in[2], (const u16*)d_in[4], (const u16*)d_in[3], (const u16*)d_in[10], (const u16*)d_in[6], (const u16*)d_in[7],
                                        (unsigned char*)d_ws, (const u16*)d_in[0], (const u16*)d_in[9], (const u16*)d_in[8], (u16*)d_out);
}
```

```cpp
#define PROBE_MUL 10
#include <hip/hip_runtime.h>
#include <stdint.h>

typedef unsigned int u32;
typedef unsigned short u16;
typedef unsigned long long u64;
typedef __attribute__((ext_vector_type(4))) unsigned u32x4;
typedef __attribute__((ext_vector_type(2))) unsigned u32x2;
typedef __attribute__((ext_vector_type(2))) __bf16 bf16x2;
typedef __attribute__((ext_vector_type(4))) float f32x4;
typedef __attribute__((ext_vector_type(2))) float f32x2;
typedef __amdgpu_buffer_rsrc_t rsrc_t;

constexpr int NH = 32, SK = 16384, DH = 128, WIN = 4096, FF = 1376;
struct Prm { int E0, ES, E1, E2, E2C, EU, TEND, DT_UP, L0, L1, X1, X2, X3, X4, NE, FE, M0, M1, M2, M3, M4, GS, GX, N1, N2, GA, WD, DT_WO, LW, LU, EW, PM, NF, NR, C0, A0, AG, AC, TS, TW, TK, TG, GB, B0, BG, BF, D0, DG, TC, LM, C1, F0, FG, FF_, W[9]; };
constexpr Prm PRM0 = {32, 20, 32, 32, 54, 56, 1976, 60, 12, 30, 86, 70, 100, 89, 3, 9, 0x2511f55b, 0x2b7425d5, 0x6017665, 0x17e0b9ec, 0x31944305, 62, 30, 36, 62, 30, 4, 100, 45, 15, 12, 0x0, 15, 14, 3, 30, 4, 4, 508, 220, 75, 6, 9, 74, 26, 8, 8, 3, 0, 120, 2, 67, 18, 4, 0x77777777, 0x77777777, 0x77777777, 0x77777777, 0x77777777, 0x44444477, 0x44444444, 0x44444444, 0x44444444};
constexpr int NL = 37;
constexpr int SK0 = 21, SK1 = 32;
constexpr int NCMIN = (NL - SK1 + SK0) * 8;
constexpr int WK = 8, WV = 5;
constexpr int WUSE = 12, LNS = 7;
constexpr int QP = 14, QD = 4;
constexpr int VQ = 6;
constexpr int NSL = (NL + 7) / 8;
constexpr int VS = VQ + NSL;
constexpr int MLS = VS + 2;
constexpr int VSIDE = MLS - 5, UPREP = MLS + 6;
constexpr int CPF = 17;
constexpr int SMO = 1;
constexpr int RVS = 5, RVP = 8, RVL = 12, RVU = 16;
constexpr int RPS = 24, RPP = 28;
constexpr int PRB = 12;
constexpr int TBS = 7, TBP = 10, TBL = 13, TBU = 17;
constexpr int window(int s, int w) {
  int n = w;
  for (int u = s - w + 1; u <= s; ++u) n += u == SMO ? 6 : (u == TBL || u == SK1 || u == NL + CPF || u == NL + RVL || u == NL + SK1 ? 1 : (u == QP || u == NL + VQ - 4 ? 2 : (u == NL + VSIDE ? 4 : 0)));
  return n;
}
constexpr int cons_end(int v) {
  const int a = v - WV + 1, c = (v - MLS) * 4;
  const int m = a < c ? a : c;
  return m < 0 ? 0 : (m > NL ? NL : m);
}
constexpr u32 col(const Prm& P, int j) { const int m[6] = {P.M0, P.M1, P.M2, P.M3, P.M4, P.PM}; return (u32)(m[j / 3] >> (j % 3 * 10) & 1023) << 10; }
constexpr u32 first_col(const Prm& P, int k) {
  if (k < 4) return col(P, 12 + k) | 1u << (24 + k);
  if (k < 6) return col(P, k - 4);
  if (k < 8) return col(P, 4 + k) | 1u << (16 + k);
  if (k < 11) return col(P, k - 1);
  return (col(P, 2) ^ col(P, 3) ^ col(P, 4) ^ col(P, 5) ^ col(P, 6) ^ col(P, 10) ^ col(P, 11)) | 0xf00000u;
}
constexpr bool one_to_one(const Prm& P) {
  u32 c[10] = {};
  for (int j = 0; j < 10; ++j) c[j] = col(P, j);
  for (int j = 0; j < 10; ++j) {
    if (c[j] == 0) return false;
    const u32 low = c[j] & (0u - c[j]);
    for (int k = j + 1; k < 10; ++k) if (c[k] & low) c[k] ^= c[j];
  }
  return true;
}
static_assert(one_to_one(PRM0), "every KB of the window exactly once");
struct Tab { u32 e[512]; };
constexpr Tab make_tab(const Prm& P) {
  Tab T{};
  for (int bid = 0; bid < 256; ++bid) {
    const int xcd = bid & 7, part = bid >> 4 & 3, i8 = (bid >> 3 & 1) | bid >> 6 << 1, wi = i8 * 8 + xcd;
    const bool has_mod = wi < 48, has_up = wi >= 21;
    const int cb0 = (i8 + P.C0) & 7, cb1 = (i8 + P.C1) & 7, ms = wi - 20;
    const int mi = wi < 8 ? wi : (wi < 16 ? 8 + ((xcd + 4) & 7) : (wi < 20 ? (wi - 14) * 8 + 3 : 16 + ms / 7 * 8 + ms % 7 + (ms % 7 >= 3 ? 1 : 0)));
    const int ui = xcd * 5 + (xcd > 5 ? xcd - 5 : 0) + (i8 == 6 ? 0 : (xcd >= 5 ? 1 : 0) + (i8 == 7 ? 4 : i8 - 2));
    const bool edge = i8 >= 6;
    const int wd = cb0 == 3 ? 0 : P.WD;
    const int e[4] = {P.E0 - 4 * wd + (cb0 == 3 ? P.ES : 0), P.E1 + (cb1 == 3 ? P.ES : 0), has_mod ? (wi < 16 ? P.E2C : P.E2) + ((mi & 7) == 3 ? P.ES : 0) : 0, has_up ? P.EU + (edge ? P.TC : 0) : 0};
    int tw[4] = {}, win[4] = {}, span[4] = {}, k0[4] = {};
    for (int k = 0; k < 4; ++k) {
      const int np = k == 0 ? 8 - wd : 8;
      tw[k] = np ? e[k] * P.TW / (16 * np) : 0;
      k0[k] = P.NF + tw[k] * np / 2 > P.TK ? P.NF + tw[k] * np / 2 : P.TK;
      span[k] = P.TEND - k0[k] - P.TG - (k == 0 ? (wd ? P.DT_WO : P.A0) : (k == 3 && has_up ? P.DT_UP : 0));
      win[k] = span[k] - (k == 0 && wd ? 0 : (k == 3 && has_up ? P.GA + (edge ? P.LM : 0) : 0));
    }
    int n[4] = {NCMIN / 8, NCMIN / 8, NCMIN / 8, NCMIN / 8};
    for (int left = 128 - NCMIN / 2; left > 0; --left) {
      int best = -1;
      for (int k = 3; k >= 0; --k)
        if (n[k] < NL && (best < 0 || n[k] * win[best] < n[best] * win[k])) best = k;
      ++n[best];
    }
    const int c[4] = {0, n[0] - 1, n[0] + n[1] - 2, n[0] + n[1] + n[2] - 3};
    const int nl = n[part];
    const bool has_w = part < 2 || (part == 2 ? has_mod : has_up);
    const int blk = !has_w ? 0 : (part == 0 ? cb0 : (part == 1 ? xcd * 8 + cb1 : (part == 2 ? mi : ui)));
    T.e[2 * bid] = (u32)(c[part] | nl << 7 | blk << 13 | (has_w ? 1 : 0) << 19 | (part == 0 ? wd : 0) << 20) | (u32)tw[part] << 24;
    T.e[2 * bid + 1] = (u32)(span[part] * P.TS / (4 * (nl - 1)) | span[part] * (1024 - P.TS) / (4 * (nl - 1)) << 16);
  }
  return T;
}
constexpr int step_range(const Prm& P, bool mx_) {
  int mx = 0, mn = 1 << 20;
  const Tab T = make_tab(P);
  for (int bid = 0; bid < 256; ++bid) { const int n = T.e[2 * bid] >> 7 & 63; mx = n > mx ? n : mx; mn = n < mn ? n : mn; }
  return mx_ ? mx : mn;
}
extern "C" __attribute__((used, visibility("protected"))) __constant__ const Tab wg_tab = make_tab(PRM0);
static_assert(step_range(PRM0, true) <= NL && step_range(PRM0, false) >= NCMIN / 8, "a wave's key range does not fit its load steps");
static_assert(UPREP + 1 < SK0 && QP + QD + 1 < SK0 && cons_end(SK0 - 1) == SK0 - WV && NL - SK1 == WV, "steps every wave runs");
constexpr int NUP = FF / 32;
constexpr int SPIN_MAX = 1 << 17;

constexpr u32 WS_EPOCH = 0;
constexpr u32 G_TB = 1024;
constexpr u32 G_MOD = 4096;
constexpr u32 G_Q = G_MOD + 3072 * 8;
constexpr u32 G_ML = G_Q + 4096 * 8;
constexpr u32 G_U = G_ML + 256 * 16;
constexpr u32 G_P = 0;
constexpr u32 G_XP = G_P + 256 * 256;
constexpr u32 G_OP = G_XP + 8 * 512 * 4;
constexpr u32 G_NT = G_OP + 43 * 512 * 8;
constexpr u32 G_PB = G_OP + 44 * 512 * 8;
constexpr u32 G_VT = G_U + 2 * (G_PB + 8192);
constexpr u32 WS_END = G_VT + 2048;
__device__ __forceinline__ u32 dup(u32 o) { return G_U + ((o >> 13) << 14) + (o & 8191u); }
constexpr u32 SENT = 0xffffffffu;
constexpr u32 OOB = 0x40000000u;
#define WST 0

constexpr int AUX_NT = 2, AUX_SC1 = 16;

__device__ __forceinline__ float lo_f(u32 v) { return __uint_as_float(v << 16); }
__device__ __forceinline__ float hi_f(u32 v) { return __uint_as_float(v & 0xffff0000u); }
__device__ __forceinline__ float rbf(float x) { return (float)(__bf16)x; }
__device__ __forceinline__ u32 pack_bf(float lo, float hi) {
  bf16x2 r; r[0] = (__bf16)lo; r[1] = (__bf16)hi; return __builtin_bit_cast(u32, r);
}
__device__ __forceinline__ float dot2(u32 k, u32 q, float acc) {
  return __builtin_amdgcn_fdot2_f32_bf16(__builtin_bit_cast(bf16x2, k), __builtin_bit_cast(bf16x2, q), acc, false);
}
template <int CTRL> __device__ __forceinline__ float dpp(float x) {
  return __builtin_bit_cast(float, __builtin_amdgcn_mov_dpp(__builtin_bit_cast(int, x), CTRL, 0xf, 0xf, true));
}
constexpr int XOR1 = 0xB1, XOR2 = 0x4E, XOR7 = 0x141, XOR8 = 0x128;
__device__ __forceinline__ float swap32_add(float a, float b) {
  auto t = __builtin_amdgcn_permlane32_swap(__float_as_uint(a), __float_as_uint(b), false, false);
  return __uint_as_float(t[0]) + __uint_as_float(t[1]);
}
__device__ __forceinline__ float swap16_add(float a, float b) {
  auto t = __builtin_amdgcn_permlane16_swap(__float_as_uint(a), __float_as_uint(b), false, false);
  return __uint_as_float(t[0]) + __uint_as_float(t[1]);
}
__device__ __forceinline__ float wave_sum(float x) {
  x += dpp<XOR1>(x); x += dpp<XOR2>(x); x += dpp<XOR7>(x); x += dpp<XOR8>(x);
  x = swap16_add(x, x);
  return swap32_add(x, x);
}
__device__ __forceinline__ float wave_max(float x) {
  x = fmaxf(x, dpp<XOR1>(x)); x = fmaxf(x, dpp<XOR2>(x)); x = fmaxf(x, dpp<XOR7>(x)); x = fmaxf(x, dpp<XOR8>(x));
  auto s = __builtin_amdgcn_permlane16_swap(__float_as_uint(x), __float_as_uint(x), false, false);
  x = fmaxf(__uint_as_float(s[0]), __uint_as_float(s[1]));
  auto t = __builtin_amdgcn_permlane32_swap(__float_as_uint(x), __float_as_uint(x), false, false);
  return fmaxf(__uint_as_float(t[0]), __uint_as_float(t[1]));
}
template <int CTRL> __device__ __forceinline__ float sel_add(float lo, float hi, bool up) {
  const float t1 = lo + dpp<CTRL>(lo), t2 = hi + dpp<CTRL>(hi);
  return up ? t2 : t1;
}
template <int I> __device__ __forceinline__ float bc16(float x) {
  return __builtin_bit_cast(float, __builtin_amdgcn_update_dpp(0, __builtin_bit_cast(int, x), 0x150 + I, 0xf, 0xf, false));
}
__device__ __forceinline__ float bcast16(float x, int i) {
  switch (i) {
    case 0: return bc16<0>(x); case 1: return bc16<1>(x); case 2: return bc16<2>(x); case 3: return bc16<3>(x);
    case 4: return bc16<4>(x); case 5: return bc16<5>(x); case 6: return bc16<6>(x); case 7: return bc16<7>(x);
    case 8: return bc16<8>(x); case 9: return bc16<9>(x); case 10: return bc16<10>(x); case 11: return bc16<11>(x);
    case 12: return bc16<12>(x); case 13: return bc16<13>(x); case 14: return bc16<14>(x); default: return bc16<15>(x);
  }
}
__device__ __forceinline__ float silu_f(float x) { return x * __builtin_amdgcn_rcpf(1.f + __expf(-x)); }

template <int AUX> __device__ __forceinline__ u32x4 ld16(rsrc_t r, u32 voff, u32 soff) {
  return __builtin_amdgcn_raw_buffer_load_b128(r, voff, soff, AUX);
}
template <int AUX> __device__ __forceinline__ u32x2 ld8(rsrc_t r, u32 voff, u32 soff) {
  return __builtin_amdgcn_raw_buffer_load_b64(r, voff, soff, AUX);
}
__device__ __forceinline__ void st8_sc1(rsrc_t r, u32 voff, u32 v, u32 tag) {
  __builtin_amdgcn_raw_buffer_store_b64(u32x2{v, tag}, r, voff, 0, AUX_SC1);
}
__device__ __forceinline__ void st16_sc1(rsrc_t r, u32 voff, u32 v0, u32 v1, u32 tag) {
  __builtin_amdgcn_raw_buffer_store_b128(u32x4{v0, tag, v1, tag}, r, voff, 0, AUX_SC1);
}
__device__ __forceinline__ rsrc_t mk_rsrc(const void* p, u32 bytes) {
  return __builtin_amdgcn_make_buffer_rsrc(const_cast<void*>(p), 0, bytes, 0x00020000);
}
#define WAIT_VM(n) __builtin_amdgcn_s_waitcnt(((n) & 15) | (((n) >> 4) << 14) | (7 << 4) | (15 << 8))
#define SB() __builtin_amdgcn_sched_barrier(0)
#define CFENCE() asm volatile("" ::: "memory")
__device__ __forceinline__ bool all_ok(bool ok) { return __builtin_amdgcn_ballot_w64(!ok) == 0ull; }

template <int N> __device__ __forceinline__ void gather16(rsrc_t ws, u32 voff, u32 stride, u32 tag, u32x4 (&g)[N]) {
#pragma nounroll
  for (int spin = 0; spin < SPIN_MAX; ++spin) {
    CFENCE();
    bool ok = true;
#pragma unroll
    for (int i = 0; i < N; ++i) g[i] = ld16<AUX_SC1>(ws, voff + i * stride, 0);
#pragma unroll
    for (int i = 0; i < N; ++i) ok &= (g[i][1] == tag) & (g[i][3] == tag);
    if (all_ok(ok)) break;
    __builtin_amdgcn_s_sleep(8);
  }
}
template <int N> __device__ __forceinline__ void gather8(rsrc_t ws, u32 voff, u32 stride, u32 tag, u32 (&g)[N], bool act = true) {
#pragma nounroll
  for (int spin = 0; spin < SPIN_MAX; ++spin) {
    CFENCE();
    bool ok = true;
    u32x2 t[N];
#pragma unroll
    for (int i = 0; i < N; ++i) t[i] = ld8<AUX_SC1>(ws, voff + i * stride, 0);
#pragma unroll
    for (int i = 0; i < N; ++i) { ok &= (t[i][1] == tag); g[i] = t[i][0]; }
    if (all_ok(ok | !act)) break;
    __builtin_amdgcn_s_sleep(8);
  }
}

struct Smem {
  float red[8][128];
  float ml[8][2];
  float r2[64 * 20];
  u16 hb[3 * 1024];
  float stat[8][64];
  float red2[8][128];
  u32 dummy[64];
  u32x2 bc;
  u32x4 zero[4];
  float pl[4][8];
  u32 mflag[8];
  u32x4 wbuf[8][8][64];
  __attribute__((aligned(256))) u16 w2b[32][512];
  u16 actl[2][32];
};

#define PROBE_K 8
#define PROBE_OP 0
#define PROBE_PART -1
#define STAMP(k) do { if ((k) == PROBE_K && tid == 0) pst = wall_clock64(); } while (0)
#define WSTAMP(k) do {} while (0)
#define FINISH() do { STAMP(8); if (tid == 0) { __hip_atomic_store(reinterpret_cast<u64*>(ws + (1u << 20)) + bid * 2, pt0, __ATOMIC_RELAXED, __HIP_MEMORY_SCOPE_AGENT); \
   __hip_atomic_store(reinterpret_cast<u64*>(ws + (1u << 20)) + bid * 2 + 1, (u64)(u16)((u32)pst - t0 - (t1 >> 8) + 1000) | (u64)(u16)((u32)wall_clock64() - t0 - (t1 >> 8) + 1000) << 16, __ATOMIC_RELAXED, __HIP_MEMORY_SCOPE_AGENT); } \
   if (bid == 8 && wave == 0) probe_end(ws, pt0, lane, sh0, sh1); return; } while (0)
#define PIN(x) asm volatile("" : "+v"(x))
#define LDS_BARRIER() do { __builtin_amdgcn_s_waitcnt(0xc07f); __builtin_amdgcn_s_barrier(); CFENCE(); } while (0)
#define WAVE_SYNC() do { __builtin_amdgcn_fence(__ATOMIC_RELEASE, "wavefront"); __builtin_amdgcn_wave_barrier(); __builtin_amdgcn_fence(__ATOMIC_ACQUIRE, "wavefront"); } while (0)

__device__ __forceinline__ void dma16(rsrc_t r, u32 voff, u32 soff, u32 lds_dst) {
  u32 keep;
  asm volatile("s_mov_b32 %0, m0\n\ts_mov_b32 m0, %4\n\ts_nop 4\n\tbuffer_load_dwordx4 %1, %2, %3 offen nt lds\n\ts_mov_b32 m0, %0"
               : "=&s"(keep) : "v"(voff), "s"(r), "s"(soff), "s"(lds_dst) : "memory");
}
__device__ __forceinline__ void ld16_into(u32x4& d, rsrc_t r, u32 voff, u32 soff) {
  asm volatile("s_nop 4\n\tbuffer_load_dwordx4 %0, %1, %2, %3 offen nt" : "+v"(d) : "v"(voff), "s"(r), "s"(soff) : "memory");
}
__device__ __forceinline__ u32 perm_lo(u32 a, u32 b) { return __builtin_amdgcn_perm(b, a, 0x05040100u); }
__device__ __forceinline__ u32 perm_hi(u32 a, u32 b) { return __builtin_amdgcn_perm(b, a, 0x07060302u); }
__device__ __forceinline__ float rdl(float x, int l) { return __int_as_float(__builtin_amdgcn_readlane(__float_as_int(x), l)); }

typedef __attribute__((ext_vector_type(8))) __bf16 bf16x8;
__device__ __forceinline__ f32x4 mfma16(u32x4 a, u32x4 b, f32x4 c) {
  return __builtin_amdgcn_mfma_f32_16x16x32_bf16(__builtin_bit_cast(bf16x8, a), __builtin_bit_cast(bf16x8, b), c, 0, 0, 0);
}
__device__ __forceinline__ float sum8(const float* p) {
  const f32x4 a = *reinterpret_cast<const f32x4*>(p), b = *reinterpret_cast<const f32x4*>(p + 4);
  return ((a[0] + b[0]) + (a[1] + b[1])) + ((a[2] + b[2]) + (a[3] + b[3]));
}
__device__ __forceinline__ float ln_ri(float var) {
  return __builtin_amdgcn_rcpf(rbf(__builtin_amdgcn_sqrtf(rbf(rbf(fmaxf(var, 0.f)) + 1e-5f))));
}
__device__ __forceinline__ void ln_stats2(const float (&x0)[8], const float (&x1)[8], float& mb0, float& ri0, float& mb1, float& ri1) {
  const float k0 = rdl(x0[0], 0), k1 = rdl(x1[0], 0);
  float s0 = 0.f, s1 = 0.f, q0 = 0.f, q1 = 0.f;
#pragma unroll
  for (int k = 0; k < 8; ++k) {
    const float d0 = x0[k] - k0, d1 = x1[k] - k1;
    s0 += d0; s1 += d1; q0 = fmaf(d0, d0, q0); q1 = fmaf(d1, d1, q1);
  }
  float t = swap16_add(swap32_add(s0, s1), swap32_add(q0, q1));
  t += dpp<XOR1>(t); t += dpp<XOR2>(t); t += dpp<XOR7>(t); t += dpp<XOR8>(t);
  auto u = __builtin_amdgcn_permlane16_swap(__float_as_uint(t), __float_as_uint(t), false, false);
  const float ms = __uint_as_float(u[0]) * (1.f / 512.f), mq = __uint_as_float(u[1]) * (1.f / 512.f);
  const float ri = ln_ri(fmaf(-ms, ms, mq));
  mb0 = rbf(k0 + rdl(ms, 0)); mb1 = rbf(k1 + rdl(ms, 32));
  ri0 = rdl(ri, 0); ri1 = rdl(ri, 32);
}
template <int I> struct IC { static constexpr int v = I; };
template <int I, int N, class F> __device__ __forceinline__ void sfor(F&& f) {
  if constexpr (I < N) { if (f(IC<I>{})) sfor<I + 1, N>(f); }
}
typedef __attribute__((ext_vector_type(4))) short s16x4;
typedef __attribute__((address_space(3))) s16x4* lds_s16x4p;
__device__ __forceinline__ u32x2 tr_read(u32 lds_addr) {
  return __builtin_bit_cast(u32x2, __builtin_amdgcn_ds_read_tr16_b64_v4i16((lds_s16x4p)lds_addr));
}
__device__ __forceinline__ u32 next_tag(u32 e) { return ((e | 0x40004000u) + 1u) & ~0x40004000u; }
__device__ __forceinline__ float u2f(u32 v) { return __uint_as_float(v); }
#define LDSP(T, a) ((__attribute__((address_space(3))) T*)(a))
template <class T> __device__ __forceinline__ u32 lds_addr(T* p) { u32 a = (u32)(uintptr_t)p; PIN(a); return a; }

#define TAB_LOAD "s_add_u32 s30, s28, wg_tab@rel32@lo+4\ns_addc_u32 s31, s29, wg_tab@rel32@hi+12\nglobal_load_dwordx2 %[te], %[va], s[30:31]\n"
#define FC(k) ".Lfc" #k
#define FC_REGS
#define FX(r, bit, k) "s_bitcmp1_b32 %[" #r "], " #bit "\ns_cselect_b32 %[sb], " FC(k) ", 0\ns_xor_b32 %[sa], %[sa], %[sb]\n"
__device__ __forceinline__ void probe_end(unsigned char* ws, u64 pt0, int lane, int sh0, int sh1) {
  while (wall_clock64() < pt0 + 5500ull) __builtin_amdgcn_s_sleep(8);
  u64 a[4], c2[4];
  for (int i = 0; i < 4; ++i) {
    a[i] = __hip_atomic_load(reinterpret_cast<u64*>(ws + (1u << 20)) + (i * 64 + lane) * 2, __ATOMIC_RELAXED, __HIP_MEMORY_SCOPE_AGENT);
    c2[i] = __hip_atomic_load(reinterpret_cast<u64*>(ws + (1u << 20)) + (i * 64 + lane) * 2 + 1, __ATOMIC_RELAXED, __HIP_MEMORY_SCOPE_AGENT);
  }
  int mn0 = 1 << 30, mx1 = -(1 << 30), mn1 = 1 << 30, sum = 0, cnt = 0;
  for (int i = 0; i < 4; ++i) {
    const int d0 = (int)(a[i] - pt0);
    mn0 = min(mn0, d0);
    if (c2[i] != 0ull) { const int d1 = (int)(c2[i] - pt0); mx1 = max(mx1, d1); mn1 = min(mn1, d1); sum += d1; cnt += 1; }
  }
  for (int o = 1; o < 64; o <<= 1) { mn0 = min(mn0, __shfl_xor(mn0, o)); mx1 = max(mx1, __shfl_xor(mx1, o)); mn1 = min(mn1, __shfl_xor(mn1, o)); sum += __shfl_xor(sum, o); cnt += __shfl_xor(cnt, o); }
  int m4 = 0, m8 = 0;
  for (int i = 0; i < 4; ++i) { m4 = max(m4, (int)(c2[i] & 0xffff)); m8 = max(m8, (int)(c2[i] >> 16 & 0xffff)); }
  for (int o = 1; o < 64; o <<= 1) { m4 = max(m4, __shfl_xor(m4, o)); m8 = max(m8, __shfl_xor(m8, o)); }
  int X = mn0 + 100 * (min(sh0 / 8, 7) + 8 * min((sh1 & 0xffff) / 16, 3) + 32 * min((sh1 >> 16) / 12, 3) + 128 * min(max((m4 - 976) / 12, 0), 3) + 512 * min(max((m8 - 1222) / 16, 0), 7));
  const u64 tg = pt0 + (u64)(long long)(X + 8000);
  for (int i = 0; i < 4000000 && wall_clock64() < tg; ++i) __builtin_amdgcn_s_sleep(1);
}
__global__ void __launch_bounds__(512, 2) fused_kernel(const u16* kc_, const u16* vc_, const u16* wo_, const u16* wq_, const u16* wmod_, const u16* w1_, const u16* w3_,
                                                       unsigned char* ws_, const u16* x_, const u16* cnd_, const u16* w2_, u16* out_) {
  __shared__ __attribute__((aligned(256))) Smem sm;
  const u64 a_ka = (u64)__builtin_amdgcn_kernarg_segment_ptr();
  const int bid = blockIdx.x, tid = threadIdx.x;
  u64 wa, t64; u32x2 te; int tid0;
  u32 ar[14];
  u32 tch2, sa_, sb_, va_;
  u32x4 kr[NL], vr[NL];
  asm volatile(".set .Lfc0, %0\n.set .Lfc1, %1\n.set .Lfc2, %2\n.set .Lfc3, %3\n.set .Lfc4, %4\n.set .Lfc5, %5\n.set .Lfc6, %6\n.set .Lfc7, %7\n.set .Lfc8, %8\n.set .Lfc9, %9\n.set .Lfc10, %10\n.set .Lfc11, %11"
    :: "n"(first_col(PRM0, 0)), "n"(first_col(PRM0, 1)), "n"(first_col(PRM0, 2)), "n"(first_col(PRM0, 3)), "n"(first_col(PRM0, 4)), "n"(first_col(PRM0, 5)),
       "n"(first_col(PRM0, 6)), "n"(first_col(PRM0, 7)), "n"(first_col(PRM0, 8)), "n"(first_col(PRM0, 9)), "n"(first_col(PRM0, 10)), "n"(first_col(PRM0, 11)));
  asm volatile(
    "s_load_dwordx16 s[4:19], %[ka], 0x0\ns_load_dwordx8 s[20:27], %[ka], 0x40\n"
    "s_memrealtime %[t]\n"
    "v_lshlrev_b32 %[va], 3, %[bid]\n"
    "s_getpc_b64 s[28:29]\n.Lcpf_first:\n"
    TAB_LOAD
    "s_cmp_gt_u32 %[bid], 15\ns_cbranch_scc1 .Lno_cpf\n"
    "s_mov_b32 %[sa], .Lend_a-.Lcpf_first\ns_max_i32 %[sa], %[sa], .Lend_b-.Lcpf_first\ns_max_i32 %[sa], %[sa], .Lend_c-.Lcpf_first\ns_max_i32 %[sa], %[sa], .Lend_d-.Lcpf_first\ns_max_i32 %[sa], %[sa], .Lend_e-.Lcpf_first\n"
    "v_lshlrev_b32 %[va], 6, %[tid]\nv_min_u32 %[va], %[sa], %[va]\nglobal_load_dword %[pf], %[va], s[28:29]\n"
    ".Lno_cpf:\n"
    "v_readfirstlane_b32 %[t0], %[tid]\nv_and_b32 %[va], 63, %[tid]\n"
    "s_mov_b32 %[sa], " FC(11) "\n"
    FX(bid, 0, 0) FX(bid, 1, 1) FX(bid, 2, 2) FX(bid, 3, 3) FX(bid, 4, 4) FX(bid, 5, 5) FX(bid, 6, 6) FX(bid, 7, 7) FX(t0, 6, 8) FX(t0, 7, 9) FX(t0, 8, 10)
    "v_lshl_add_u32 %[va], %[va], 4, %[sa]\n"
    "s_waitcnt lgkmcnt(0)\nglobal_load_dwordx4 %[k0], %[va], s[4:5] nt\nglobal_load_dwordx4 %[v0], %[va], s[6:7] nt\n"
    "s_bitcmp1_b32 %[t0], 6\ns_cselect_b64 s[14:15], s[16:17], s[14:15]\n"
    "s_bitcmp1_b32 %[bid], 4\ns_cselect_b64 s[8:9], s[10:11], s[8:9]\ns_cselect_b64 s[12:13], s[14:15], s[12:13]\ns_bitcmp1_b32 %[bid], 5\ns_cselect_b64 %[wa], s[12:13], s[8:9]"
    : "=&{s4}"(ar[0]), "=&{s5}"(ar[1]), "=&{s6}"(ar[2]), "=&{s7}"(ar[3]), "=&{s18}"(ar[4]), "=&{s19}"(ar[5]), "=&{s20}"(ar[6]), "=&{s21}"(ar[7]), "=&{s22}"(ar[8]), "=&{s23}"(ar[9]),
      "=&{s24}"(ar[10]), "=&{s25}"(ar[11]), "=&{s26}"(ar[12]), "=&{s27}"(ar[13]), [wa] "=&s"(wa), [t0] "=&s"(tid0), [t] "=&s"(t64), [te] "=&v"(te), [sa] "=&s"(sa_), [sb] "=&s"(sb_),
      [va] "=&v"(va_), [pf] "=&v"(tch2), [k0] "=&v"(kr[0]), [v0] "=&v"(vr[0])
    : [ka] "s"(a_ka), [bid] "s"(bid), [tid] "v"(tid) : "s8", "s9", "s10", "s11", "s12", "s13", "s14", "s15", "s16", "s17", "s28", "s29", "s30", "s31", "scc", "memory" FC_REGS);
  asm volatile("" : "+s"(ar[0]), "+s"(ar[1]), "+s"(ar[2]), "+s"(ar[3]), "+s"(ar[4]), "+s"(ar[5]), "+s"(ar[6]), "+s"(ar[7]), "+s"(ar[8]), "+s"(ar[9]), "+s"(ar[10]), "+s"(ar[11]), "+s"(ar[12]), "+s"(ar[13]));
  asm volatile("" : "+s"(wa), "+s"(t64), "+s"(tid0), "+s"(sa_));
  const u32 tka = (u32)wall_clock64();
  const u64 pt0 = t64; u64 pst = (PROBE_K == 100 || PROBE_K == 102) ? pt0 : (PROBE_K == 101 ? pt0 + (u32)(tka - (u32)t64) : 0);
  const u32 t0 = tka;
  __builtin_assume(tid >= 0 && tid < 512 && tid0 >= 0 && tid0 < 512 && bid >= 0 && bid < 256);
  constexpr Prm P = PRM0;
  const int TEND = P.TEND;
  const int xcd = bid & 7, part = bid >> 4 & 3;
  const int lane = tid & 63, wave = tid0 >> 6;
  SB();
  const bool p3 = part == 3;
  u32 pitch = (u32)(0x005600c001000020ull >> (part * 16) & 0xffff) * 32u;
  const int sh = p3 ? 2 : 3;
  u32 wv0 = (u32)((p3 ? (wave >> 1) * 128 : wave * 64) + (part == 0 ? xcd * 512 : 0) + (lane >> sh)) * pitch + ((lane & (p3 ? 3 : 7)) ^ (lane >> (p3 ? 5 : 3))) * 16u;
  const u32 wimg = (u32)(uintptr_t)(&sm.wbuf[0][0][0]);
  u32 wsz = (part == 0 ? 4096u : 512u) * pitch, wld = wimg + (p3 ? (wave & 1) * 32768 + (wave >> 1) * 8192 : wave * 8192);
  PIN(wv0);
  asm volatile("" : "+s"(wsz), "+s"(wld), "+s"(pitch));
  auto karg = [&](int i) { return ar[2 * i] | (u64)ar[2 * i + 1] << 32; };
  const u64 a_kc = karg(0), a_vc = karg(1);
  unsigned char* ws = (unsigned char*)karg(2);
  const u16 *x = (const u16*)karg(3), *cnd = (const u16*)karg(4), *w2 = (const u16*)karg(5);
  u16* out = (u16*)karg(6);
  const rsrc_t wsr = mk_rsrc(ws, WS_END);
  asm volatile("s_waitcnt vmcnt(2)" : "+v"(te));
  const u32 te0 = __builtin_amdgcn_readfirstlane(te[0]), te1 = __builtin_amdgcn_readfirstlane(te[1]);
  const int bl0 = te0 & 127, nl = te0 >> 7 & 63, blk = te0 >> 13 & 63;
  const bool has_w = te0 >> 19 & 1;
  const rsrc_t rw = mk_rsrc((const void*)wa, wsz);
  const u32 wstep = pitch << (6 - sh), wflip = p3 ? 32u : 0u;
  u32 wso = blk << (sh + 4);
  int wleft = 8;
  auto piece = [&]() {
    dma16(rw, wv0, wso, wld);
    wso += wstep; wld += 1024;
    wv0 ^= (wleft & 1) ? wflip : 0u;
    --wleft;
  };
  SB();
  STAMP(23);
  const int b = bid >> 3 & 1, head = xcd * 4 + (bid >> 6 ^ 3);
  const int g = lane >> 4, c = lane & 15;
  const int i8 = b | bid >> 6 << 1, wi = i8 * 8 + xcd;
  const bool is_up = p3 && has_w, modc = part == 2 && wi < 16;
  const size_t base = ((size_t)(b * NH + head) * SK + (SK - WIN)) * DH;
  const rsrc_t rk = mk_rsrc((const u16*)a_kc + base, WIN * DH * 2), rv = mk_rsrc((const u16*)a_vc + base, WIN * DH * 2);
  int tau = te1 & 0xffff;
  const int tauw = te0 >> 24 << 7, wdef = te0 >> 20 & 15;
  int due = P.NF * 256, now = 0;
  auto gate = [&]() {
    while (now < due) { now = (int)(((u32)wall_clock64() - t0) << 8); if (now < due) __builtin_amdgcn_s_sleep(1); }
    due += tau;
  };
  auto until = [&](int t) { while ((int)(((u32)wall_clock64() - t0) << 8) < t); };
  const u32 pslot = dup(G_P + head * 2048 + (b * 4 + part) * 16);
  const u32 die = __builtin_amdgcn_s_getreg(20 | (2 << 6));
  const bool voter = p3 && i8 == 1, timer = p3 && i8 < 2;
  int vote = 0, lag = 0, shift = 0;
  const u32 pbo = dup(G_PB) + (xcd * 8 + wave) * 128;
  u32 near = 0;
  u32x2 ep; u32 xs0, xs1, tch1; u32x4 xr0, xr1;
  const rsrc_t rx = mk_rsrc(part == 2 ? cnd : x, (part == 1 || (part == 2 && has_w)) ? 2048u : 0u);
  auto small_ops = [&]() {
    ep = ld8<AUX_SC1>(wsr, wave == 7 ? WS_EPOCH : OOB, 0);
    xs0 = __builtin_amdgcn_raw_buffer_load_b16(rx, tid * 2, 0, 0); xs1 = __builtin_amdgcn_raw_buffer_load_b16(rx, tid * 2, 1024, 0);
    xr0 = ld16<0>(rx, part == 1 ? lane * 16 : OOB, 0); xr1 = ld16<0>(rx, part == 1 ? lane * 16 : OOB, 1024);
    tch1 = __builtin_amdgcn_raw_buffer_load_b32(mk_rsrc(w2 + (size_t)blk * 32 * 512, is_up ? 64u : 0u), tid == 384 ? 0 : OOB, 0, AUX_NT);
  };
  if (has_w) {
    if (modc) now = 1 << 30;
#pragma nounroll
    while (wleft > wdef) { gate(); due += tauw - tau; piece(); }
    now = 0;
  }
  due = max(due, P.TK * 256);
  SB();
  u32 sot = sa_ & 0xffc00u;
  {
    const u32 s1 = (bl0 + lane - 1 - (lane >= SK0 ? NL - nl : 0)) ^ (124 + part);
#pragma unroll
    for (int k = 0; k < 7; ++k) sot ^= (u32)-(int)(s1 >> k & 1u) & col(P, k);
    PIN(sot);
  }
  u32 tag = 0;
  u32x4 bf[8];
  auto wo_prep = [&](int s0 = 0, int s1 = 2) {
    const u32 wi_ = wimg + wave * 8192;
    const int q = (lane >> 2) & 3, pp = lane & 3;
#pragma unroll
    for (int s = s0; s < s1; ++s) {
#pragma unroll
      for (int t = 0; t < 4; ++t) {
        const int r0 = s * 32 + g * 8 + q, r1 = r0 + 4, ch = t * 2 + (pp >> 1);
        const u32x2 lo = tr_read(wi_ + r0 * 128 + ((ch ^ (r0 & 7)) * 16) + (pp & 1) * 8), hi = tr_read(wi_ + r1 * 128 + ((ch ^ (r1 & 7)) * 16) + (pp & 1) * 8);
        bf[s * 4 + t] = u32x4{lo[0], lo[1], hi[0], hi[1]};
      }
    }
  };
  auto gemv_go = [&](u32 hh) {
    sm.hb[tid] = (u16)hh; sm.hb[512 + tid] = (u16)(hh >> 16);
    WAVE_SYNC();
    const u32x4* a = (c & 11) == 0 ? reinterpret_cast<const u32x4*>(&sm.hb[(c >> 2) * 512 + wave * 64 + g * 8]) : &sm.zero[0];
    const u32x4 a0 = a[0], a1 = a[(c & 11) == 0 ? 4 : 0];
    const f32x4 z = {0.f, 0.f, 0.f, 0.f};
    f32x4 d[4];
#pragma unroll
    for (int t = 0; t < 4; ++t) d[t] = mfma16(a0, bf[t], z);
#pragma unroll
    for (int t = 0; t < 4; ++t) d[t] = mfma16(a1, bf[4 + t], d[t]);
    if (lane < 32) {
#pragma unroll
      for (int t = 0; t < 4; ++t) sm.r2[(t * 16 + c) * 20 + g * 8 + wave] = d[t][0];
    }
  };
  auto mod_stage = [&]() {
    wo_prep();
    gemv_go(pack_bf(silu_f(lo_f(xs0)), silu_f(lo_f(xs1))));
    __builtin_amdgcn_s_waitcnt(0xc07f);
    if (lane == 0) *LDSP(volatile u32, (u32)(uintptr_t)&sm.mflag[wave]) = 1u;
    if (wave == 7) {
#pragma nounroll
      for (int spin = 0; spin < SPIN_MAX; ++spin) {
        const u32 f = *LDSP(volatile u32, (u32)(uintptr_t)&sm.mflag[lane & 7]);
        if (all_ok(f != 0u)) break;
        __builtin_amdgcn_s_sleep(1);
      }
      CFENCE();
      st8_sc1(wsr, G_MOD + (blk * 64 + lane) * 8, pack_bf(sum8(&sm.r2[lane * 20]), sum8(&sm.r2[lane * 20 + 8])), tag);
    }
    STAMP(9);
  };
  if (lane < 4) sm.zero[lane] = u32x4{0u, 0u, 0u, 0u};
  if (part == 2 && has_w) {
    if (tid < 8) sm.mflag[tid] = 0u;
    LDS_BARRIER();
    if (__builtin_expect(modc, 0)) {
      small_ops();
      __builtin_amdgcn_raw_buffer_store_b128(u32x4{SENT, SENT, SENT, SENT}, wsr, G_U + wi * 10240 + tid * 16, 0, AUX_SC1);
      __builtin_amdgcn_raw_buffer_store_b128(u32x4{SENT, SENT, SENT, SENT}, wsr, tid < 128 ? G_U + wi * 10240 + tid * 16 : OOB, 8192, AUX_SC1);
      asm volatile("s_waitcnt vmcnt(0)" ::: "memory");
      tag = next_tag(__builtin_amdgcn_readfirstlane(ep[0]));
      mod_stage();
      __builtin_amdgcn_s_barrier();
    }
  }
  SB();
  const int fd = wi * 8 + (lane & 3) * 2;
  const int ub = wave >> 2, ud = (wave & 3) * 128 + c * 8 + g * 2;
  u32x4 sg[3]; u32 sx2;
  auto side_loads = [&]() {
    const bool fin = part == 1 && wave == 0;
    sg[0] = ld16<AUX_SC1>(wsr, is_up ? G_MOD + (1024 + ud) * 8 : (fin ? G_MOD + (1024 + fd) * 8 : OOB), 0);
    sg[1] = ld16<AUX_SC1>(wsr, is_up ? G_MOD + (1536 + ud) * 8 : (fin ? G_MOD + (2560 + fd) * 8 : OOB), 0);
    sg[2] = ld16<AUX_SC1>(wsr, is_up ? G_MOD + (2048 + ud) * 8 : OOB, 0);
    sx2 = __builtin_amdgcn_raw_buffer_load_b32(mk_rsrc(x, (is_up || fin) ? 2048u : 0u), is_up ? ub * 1024 + ud * 2 : (g & 1) * 1024 + fd * 2, 0, 0);
  };
  auto side_pin = [&]() { PIN(sg[0]); PIN(sg[1]); PIN(sg[2]); PIN(sx2); };
  const u32 mlo = G_ML + (head * 2 + b) * 64 + (lane & 3) * 16;
  float fmrg = 0.f;
  auto merge_factor = [&](const u32x4& ml) {
    const float mi = u2f(ml[0]);
    float M = fmaxf(mi, dpp<XOR1>(mi)); M = fmaxf(M, dpp<XOR2>(M));
    const float e = __expf(mi - M);
    float L = e * u2f(ml[2]);
    L += dpp<XOR1>(L); L += dpp<XOR2>(L);
    fmrg = rdl(e * __builtin_amdgcn_rcpf(L), part);
  };
  f32x2 ga = {0.f, 0.f}, xin = {0.f, 0.f}, cc1 = {0.f, 0.f};
  float Af = 0.f, Bf = 0.f;
  const int hpos = ub * 512 + ud;
  auto up_prep = [&]() {
    if (!all_ok((sg[0][1] == tag) & (sg[0][3] == tag) & (sg[1][1] == tag) & (sg[1][3] == tag) & (sg[2][1] == tag) & (sg[2][3] == tag))) gather16<3>(wsr, G_MOD + (1024 + ud) * 8, 512 * 8, tag, sg);
    ga = f32x2{ub ? hi_f(sg[0][0]) : lo_f(sg[0][0]), ub ? hi_f(sg[0][2]) : lo_f(sg[0][2])};
    cc1 = f32x2{rbf(1.f + (ub ? hi_f(sg[2][0]) : lo_f(sg[2][0]))), rbf(1.f + (ub ? hi_f(sg[2][2]) : lo_f(sg[2][2])))};
    xin = f32x2{lo_f(sx2), hi_f(sx2)};
    u32* hp = reinterpret_cast<u32*>(&sm.hb[hpos]);
    hp[512] = pack_bf(cc1[0], cc1[1]);
    hp[1024] = ub ? perm_hi(sg[1][0], sg[1][2]) : perm_lo(sg[1][0], sg[1][2]);
  };
  auto up_frags = [&]() {
    const int pp = lane & 3;
    const u32 a = wimg + (pp & 1) * 32768 + ((pp >> 1) * 32 + g * 8 + ((lane >> 2) & 3)) * 64 + (((wave >> 1) ^ ((g & 1) | (pp & 2))) * 16) + (wave & 1) * 8;
#pragma unroll
    for (int rb = 0; rb < 8; ++rb) {
      const u32x2 lo = tr_read(a + rb * 4096), hi = tr_read(a + rb * 4096 + 256);
      bf[rb] = u32x4{lo[0], lo[1], hi[0], hi[1]};
    }
  };
  const u32x4* ap = reinterpret_cast<const u32x4*>(&sm.hb[((lane >> 3) & 1) * 512 + (lane & 1) * 32 + g * 8]);
  const bool par = lane & 8;
  auto limit = [&](auto sc) {
    constexpr int s = sc.v;
    WAIT_VM(window(s, 1 + (PRM0.W[s >> 3] >> (4 * (s & 7)) & 7)));
    SB();
  };
  auto sample = [&]() { if (timer) lag += (int)(((u32)wall_clock64() - t0) << 8) - due + ((int)__builtin_amdgcn_s_getreg(7 | (3 << 11)) + 1) * tau; };
  auto report = [&](u32 dst) {
    if (timer) {
      if (lane == 0) sm.mflag[wave] = vote * 65536 + min(lag >> 8, 4000);
      lag = 0;
      LDS_BARRIER();
      if (wave == 7) {
        int s = sm.mflag[lane & 7];
        s += __builtin_amdgcn_mov_dpp(s, XOR1, 0xf, 0xf, true); s += __builtin_amdgcn_mov_dpp(s, XOR2, 0xf, 0xf, true); s += __builtin_amdgcn_mov_dpp(s, XOR7, 0xf, 0xf, true);
        if (lane < 8) st8_sc1(wsr, dst + lane * 128 + wi * 8, s, tag);
      }
    }
    SB();
  };
  int sh0 = 0, sh1 = 0;
  float l0 = 0.f, l1 = 0.f;
  u32x2 gmd[2], tbg;
  int t1 = 0;
#pragma unroll
  for (int i = SK0; i < SK1; ++i) kr[i] = u32x4{0u, 0u, 0u, 0u};
  const int first = SK0 + NL - nl;
  auto left_out = [&](int i) { int f = first; asm volatile("" : "+s"(f)); return i < f; };
  sfor<1, NL>([&](auto ic) {
    constexpr int i = ic.v;
    if constexpr (i >= SK0 && i < SK1) { if (left_out(i)) return true; }
    const u32 so = __builtin_amdgcn_readlane(sot, i);
    gate();
    if constexpr ((i >= TBS && i <= TBP) || (i >= RPS && i <= RPP)) sample();
    SB();
    if constexpr (i >= SK0 && i < SK1) ld16_into(kr[i], rk, lane * 16, so); else kr[i] = ld16<AUX_NT>(rk, lane * 16, so);
    if constexpr (i == SMO) small_ops();
    if constexpr (i == SMO + 1) { if (voter) __builtin_amdgcn_raw_buffer_store_b32(0u, wsr, pbo + (lane >> 5) * 8192 + (lane & 31) * 4, 0, AUX_SC1); }
    if constexpr (i == SK1) tbg = ld8<AUX_SC1>(wsr, wave == 7 ? G_VT + xcd * 128 + c * 8 : OOB, 0);
    if constexpr (i == TBL) tbg = ld8<AUX_SC1>(wsr, wave == 7 ? G_TB + xcd * 128 + c * 8 : OOB, 0);
    if constexpr (i == QP) { gmd[0] = ld8<AUX_SC1>(wsr, part == 1 ? G_MOD + tid * 8 : OOB, 0); gmd[1] = ld8<AUX_SC1>(wsr, part == 1 ? G_MOD + tid * 8 : OOB, 4096); }
    SB();
    SB();
    if (i >= 4) limit(ic);
    if (i == 1) STAMP(25);
    if (i == 8) STAMP(12);
    if (i == 20) STAMP(21);
    if (i == 32) STAMP(22);
    if (i == TBP) {
      if (timer) {
        if (lane == 0) sm.mflag[wave] = lag;
        lag = 0;
        LDS_BARRIER();
        if (wave == 7) {
          int s = sm.mflag[lane & 7];
          s += __builtin_amdgcn_mov_dpp(s, XOR1, 0xf, 0xf, true); s += __builtin_amdgcn_mov_dpp(s, XOR2, 0xf, 0xf, true); s += __builtin_amdgcn_mov_dpp(s, XOR7, 0xf, 0xf, true);
          if (lane < 8) st8_sc1(wsr, G_TB + lane * 128 + wi * 8, t0 << 10 | (u32)min(s / (2048 * (TBP - TBS + 1)), 1023), tag);
        }
      }
      SB();
    }
    if (i == PRB) {
      if (voter) {
        u64 ta, tb, tc; u32 dm;
        const u32 pa = pbo + (wave & 1) * 8192;
        asm volatile("s_memtime %0\n\ts_waitcnt lgkmcnt(0)\n\ts_load_dword %3, %4, %5\n\ts_waitcnt lgkmcnt(0)\n\ts_memtime %1\n\ts_waitcnt lgkmcnt(0)\n\t"
                     "s_load_dword %3, %4, %6\n\ts_waitcnt lgkmcnt(0)\n\ts_memtime %2\n\ts_waitcnt lgkmcnt(0)"
                     : "=&s"(ta), "=&s"(tb), "=&s"(tc), "=&s"(dm) : "s"(ws), "s"(pa), "s"(pa ^ 8192u));
        const int d = min(max((int)(2 * (u32)tb - (u32)ta - (u32)tc), -500), 500);
        vote = ((wave ^ die) & 1) ? -d : d;
        STAMP(26);
      }
      SB();
    }
    if (i == TBU) {
      if (wave == 7) {
        const bool ok = tbg[1] == tag;
        float lf = ok ? (float)min(max((int)((t0 << 10 | 1023u) - tbg[0]) >> 10, -300), 300) : 0.f, nf = ok ? 1.f : 0.f, lg = ok ? (float)(tbg[0] & 1023u) : 0.f;
        lf += dpp<XOR1>(lf); nf += dpp<XOR1>(nf); lg += dpp<XOR1>(lg); lf += dpp<XOR2>(lf); nf += dpp<XOR2>(nf); lg += dpp<XOR2>(lg);
        lf += dpp<XOR7>(lf); nf += dpp<XOR7>(nf); lg += dpp<XOR7>(lg); lf += dpp<XOR8>(lf); nf += dpp<XOR8>(nf); lg += dpp<XOR8>(lg);
        const float rn = __builtin_amdgcn_rcpf(fmaxf(nf, 1.f));
        if (lane == 0) sm.pl[3][7] = __uint_as_float((u32)min(max((int)((lg * rn - (float)P.GS) * (float)P.GB * 0.125f), 0), 150));
        shift = __builtin_amdgcn_readfirstlane(min(max((int)((lg * rn - (float)P.GS) * (float)P.GB * 0.125f), 0), 150));
        if (lane == 0) sm.bc = u32x2{tag, t0 - (u32)(int)(lf * rn) + shift};
      }
      LDS_BARRIER();
      const u32x2 bc = sm.bc;
      tag = __builtin_amdgcn_readfirstlane(bc[0]);
      sh0 = __builtin_amdgcn_readfirstlane(__float_as_uint(sm.pl[3][7]));
      t1 = (__builtin_amdgcn_readfirstlane((int)(bc[1] - t0)) + TEND + P.X1) * 256;
      tau -= __builtin_amdgcn_readfirstlane((int)((float)(int)(t0 - bc[1]) * 256.f * __builtin_amdgcn_rcpf((float)(nl - TBU - 1))));
      SB();
    }
    if (i == RPP) report(G_VT);
    if (i == LNS) {
      tag = next_tag(__builtin_amdgcn_readfirstlane(ep[0]));
      if (part == 1) {
        float x0[8], x1[8], mb0, ri0, mb1, ri1;
#pragma unroll
        for (int k = 0; k < 4; ++k) { x0[2 * k] = lo_f(xr0[k]); x0[2 * k + 1] = hi_f(xr0[k]); x1[2 * k] = lo_f(xr1[k]); x1[2 * k + 1] = hi_f(xr1[k]); }
        ln_stats2(x0, x1, mb0, ri0, mb1, ri1);
        l0 = rbf(rbf(lo_f(xs0) - mb0) * ri0); l1 = rbf(rbf(lo_f(xs1) - mb1) * ri1);
      }
      PIN(l0); PIN(l1);
      SB();
    }
    if (i == WUSE) {
      PIN(tch1); PIN(tch2);
      if ((part == 1 || (part == 2 && has_w && !modc))) {
        WAIT_VM(WK); CFENCE();
        if (part == 2) mod_stage(); else wo_prep();
      }
#pragma unroll
      for (int k = 0; k < 8; ++k) PIN(bf[k]);
      SB();
    }
    if (i == QP + QD) {
      if (part == 1) {
        u32 gm[2] = {gmd[0][0], gmd[1][0]};
        if (!all_ok((gmd[0][1] == tag) & (gmd[1][1] == tag))) { STAMP(11); gather8<2>(wsr, G_MOD + tid * 8, 512 * 8, tag, gm); }
        STAMP(5);
        gemv_go(pack_bf(rbf(l0 * rbf(1.f + lo_f(gm[1]))) + lo_f(gm[0]), rbf(l1 * rbf(1.f + hi_f(gm[1]))) + hi_f(gm[0])));
      }
      SB();
    }
    if (i == QP + QD + 1) {
      if (part == 1) {
        LDS_BARRIER();
        if (wave == 0) {
          const int bb = lane >> 5, jq = lane & 31;
          st8_sc1(wsr, G_Q + (bb * 2048 + blk * 32 + jq) * 8, pack_bf(sum8(&sm.r2[2 * jq * 20 + bb * 8]), sum8(&sm.r2[(2 * jq + 1) * 20 + bb * 8])), tag);
        }
        STAMP(6);
      }
      SB();
    }
    return true;
  });
  PIN(sot);
  if (wave == 7) {
    const bool ok = tbg[1] == tag;
    float vt = ok ? (float)((int)tbg[0] >> 16) : 0.f, lg = ok ? (float)(tbg[0] & 0xffffu) : 0.f, nf = ok ? 8.f * (RPP - RPS + 1) : 0.f;
    vt += dpp<XOR1>(vt); lg += dpp<XOR1>(lg); nf += dpp<XOR1>(nf); vt += dpp<XOR2>(vt); lg += dpp<XOR2>(lg); nf += dpp<XOR2>(nf);
    vt += dpp<XOR7>(vt); lg += dpp<XOR7>(lg); nf += dpp<XOR7>(nf); vt += dpp<XOR8>(vt); lg += dpp<XOR8>(lg); nf += dpp<XOR8>(nf);
    lg *= __builtin_amdgcn_rcpf(fmaxf(nf, 1.f));
    if (lane == 0) sm.bc = u32x2{(__float_as_uint(vt) & 0x80000000u) | (u32)min(shift * P.DG * 32, P.EW * 256), (u32)min(max((int)((lg - (float)P.B0) * (float)(P.BG * 32)) - shift * P.D0 * 32, 0), 200 * 256)};
  }
  LDS_BARRIER();
  {
    const u32x2 bc = sm.bc;
    const int st = __builtin_amdgcn_readfirstlane(bc[1]), sp = (st * P.BF) >> 3;
    near = ((__builtin_amdgcn_readfirstlane(bc[0]) >> 31 ^ die ^ P.NR ^ 1) & 1) * 8192u;
    t1 += st + (__builtin_amdgcn_readfirstlane(bc[0]) & 0xffff);
    sh1 = st >> 8;
    due += P.TG * 256 + sp;
    tau = (te1 >> 16) + __builtin_amdgcn_readfirstlane((int)((float)(st - sp) * __builtin_amdgcn_rcpf((float)(nl - 3))));
  }
  const u32 qoff = G_Q + ((b * 32 + head) * 64 + c * 4) * 8;
  u32x4 qg[2], mlg;
  u32 qq[4];
  constexpr int NB = (NL + 15) / 16;
  float xlo, sc[NB], pr[NB], m = 0.f;
  f32x4 acc = {0.f, 0.f, 0.f, 0.f};
  u32 pb[NB], pm[4], pl8[8], cpf;
  const u32 adst0 = lds_addr(lane < 32 ? &sm.red[wave][c * 8 + g * 4] : &sm.r2[(lane - 32) * 4]);
  u32 po, rdst, rsrc, xo;
  auto wo_addr = [&]() {
    po = dup(G_P + xcd * 8192) + tid * 16 + near;
    rdst = lane < 32 ? (u32)(uintptr_t)&sm.red2[wave][0] + lane * 16 : (u32)(uintptr_t)&sm.r2[0] + (lane - 32) * 16;
    rsrc = (u32)(uintptr_t)&sm.red2[0][((wave & 1) * 16 + (lane & 15)) * 4 + (lane >> 4)];
    xo = dup(G_XP + xcd * 2048 + (wave & 1) * 1024 + (blk * 64 + (lane & ~1)) * 2) + (lane & 1) * 8192;
    PIN(po); PIN(rdst); PIN(rsrc); PIN(xo);
  };
  u32 pso = (wave == 0 && (g & 1) == 0) ? pslot + (c >> 3) * 1024 + (c & 3) * 256 + (c >> 2 & 1) * 128 + (g >> 1) * 8192 : OOB;
  PIN(pso);
  const u32 nadd = wdef ? P.LW : (is_up ? P.LU : 0);
  u32 nto = tid < 2 ? dup(G_NT) + bid * 8 + tid * 8192 : OOB;
  PIN(nto);
  auto notices = [&]() -> int {
    u32 off = dup(G_NT) + near + lane * 16;
    PIN(off);
    until(due);
    int mx;
#pragma nounroll
    for (int spin = 0; spin < SPIN_MAX; ++spin) {
      CFENCE();
      const u32x4 ga_ = ld16<AUX_SC1>(wsr, off, 0), gb_ = ld16<AUX_SC1>(wsr, off, 1024);
      mx = max(max((int)(ga_[0] - t0), (int)(ga_[2] - t0)), max((int)(gb_[0] - t0), (int)(gb_[2] - t0)));
      if (all_ok((((ga_[1] ^ tag) | (ga_[3] ^ tag)) | ((gb_[1] ^ tag) | (gb_[3] ^ tag))) == 0u)) break;
      for (int k = 0; k < P.AG; ++k) __builtin_amdgcn_s_sleep(1);
    }
    if (all_ok(mx <= (t1 >> 8) - P.AC)) return t1;
    mx = max(mx, __builtin_amdgcn_mov_dpp(mx, XOR1, 0xf, 0xf, true)); mx = max(mx, __builtin_amdgcn_mov_dpp(mx, XOR2, 0xf, 0xf, true));
    mx = max(mx, __builtin_amdgcn_mov_dpp(mx, XOR7, 0xf, 0xf, true)); mx = max(mx, __builtin_amdgcn_mov_dpp(mx, XOR8, 0xf, 0xf, true));
    mx = max(max(__builtin_amdgcn_readlane(mx, 0), __builtin_amdgcn_readlane(mx, 16)), max(__builtin_amdgcn_readlane(mx, 32), __builtin_amdgcn_readlane(mx, 48)));
    return max(min(mx, 1 << 20) * 256, t1 + P.L0 * 256) + P.L1 * 256;
  };
  auto pv_row = [&](auto jc) {
    constexpr int i = jc.v;
    const u32 pv = __float_as_uint(bcast16(u2f(pb[i >> 4]), i & 15));
    acc = mfma16(u32x4{pv & pm[0], pv & pm[1], pv & pm[2], pv & pm[3]}, vr[i], acc);
    PIN(acc);
    return true;
  };
  sfor<0, NL - 1>([&](auto ic) {
    constexpr int v = ic.v;
    if constexpr (v >= SK0 && v < SK1) { if (left_out(v)) return true; }
    if constexpr (v > 0) {
      const u32 so = __builtin_amdgcn_readlane(sot, v);
      if constexpr (v >= SK1) { if (v == NL - P.NE) tau = (tau * P.FE) >> 4; }
      gate();
      if constexpr (v >= RVS && v <= RVP) sample();
      SB();
      if constexpr (v >= SK0 && v < SK1) ld16_into(vr[v], rv, lane * 16, so); else vr[v] = ld16<AUX_NT>(rv, lane * 16, so);
      SB();
      SB();
    }
    if constexpr (v == SK0 - 1) {
#pragma unroll
      for (int i = SK0; i < SK1; ++i) { vr[i] = u32x4{0u, 0u, 0u, 0u}; PIN(vr[i]); }
    }
    if constexpr (v == VQ - 4) { qg[0] = ld16<AUX_SC1>(wsr, qoff, 0); qg[1] = ld16<AUX_SC1>(wsr, qoff + 16, 0); }
    if constexpr (v == VSIDE) side_loads();
    if constexpr (v == CPF) {
      u64 pc; u32 dlt, len;
      asm volatile("s_getpc_b64 %0\n.Lcpf_here:\n\ts_mov_b32 %1, .Ltail_start-.Lcpf_here\n\ts_mov_b32 %2, .Lend_a-.Ltail_start\n\ts_max_i32 %2, %2, .Lend_b-.Ltail_start\n\ts_max_i32 %2, %2, .Lend_c-.Ltail_start\n\ts_max_i32 %2, %2, .Lend_d-.Ltail_start\n\ts_max_i32 %2, %2, .Lend_e-.Ltail_start" : "=s"(pc), "=s"(dlt), "=s"(len) :: "scc");
      pc += dlt;
      const rsrc_t rc = mk_rsrc((const void*)pc, (part == 3 && i8 == 1) ? len : 0u);
      cpf = __builtin_amdgcn_raw_buffer_load_b32(rc, tid * 64, 0, 0);
    }
    if constexpr (v == SK1 + 2) PIN(cpf);
    if constexpr (v == RVP) report(G_VT + 1024);
    if constexpr (v == RVL) tbg = ld8<AUX_SC1>(wsr, wave == 7 ? G_VT + 1024 + xcd * 128 + c * 8 : OOB, 0);
    if constexpr (v == SK1) mlg = ld16<AUX_SC1>(wsr, wave == 0 ? mlo : OOB, 0);
    SB();
    if constexpr (NL + v < 72) limit(IC<NL + v>{});
    if constexpr (v >= SK0 + WV) { WAIT_VM(v < SK1 ? WV : WV + 1); SB(); }
    if constexpr (v == VQ) {
      if (!all_ok((qg[0][1] == tag) & (qg[0][3] == tag) & (qg[1][1] == tag) & (qg[1][3] == tag))) { STAMP(20); gather16<2>(wsr, qoff, 16, tag, qg); }
      STAMP(1); WSTAMP(1);
      qq[0] = qg[0][0]; qq[1] = qg[0][2]; qq[2] = qg[1][0]; qq[3] = qg[1][2];
    }
    if constexpr (v >= VQ && v < VS) {
      constexpr int sl = v - VQ, bt = sl >> 1;
      float xs[8];
#pragma unroll
      for (int i = 0; i < 8; ++i) {
        float d = 0.f;
        if (sl * 8 + i < NL) {
#pragma unroll
          for (int k = 0; k < 4; ++k) d = dot2(kr[sl * 8 + i][k], qq[k], d);
        }
        xs[i] = d;
      }
#pragma unroll
      for (int h = 0; h < 4; ++h) xs[h] = sel_add<XOR7>(xs[h], xs[h + 4], c & 4);
#pragma unroll
      for (int h = 0; h < 2; ++h) xs[h] = sel_add<XOR2>(xs[h], xs[h + 2], c & 2);
      xs[0] = sel_add<XOR1>(xs[0], xs[1], c & 1);
      if constexpr ((sl & 1) || sl == NSL - 1) {
        const float t = (sl & 1) ? sel_add<XOR8>(xlo, xs[0], c & 8) : xs[0] + dpp<XOR8>(xs[0]);
        const float s = rbf(rbf(t) * 0.08838834764831845f);
        sc[bt] = (bt * 16 + c < SK0 || (bt * 16 + c >= first && bt * 16 + c < NL)) ? s : -__builtin_inff();
        PIN(sc[bt]);
      } else {
        xlo = xs[0];
        PIN(xlo);
      }
    }
    if constexpr (v == VS) {
      float mloc = sc[0];
#pragma unroll
      for (int bt = 1; bt < NB; ++bt) mloc = fmaxf(mloc, sc[bt]);
      m = fmaxf(wave_max(mloc), -3.0e38f);
      float ls = 0.f;
#pragma unroll
      for (int bt = 0; bt < NB; ++bt) { pr[bt] = __expf(sc[bt] - m); ls += pr[bt]; }
      const float l = wave_sum(ls);
      if (lane == 0) { sm.ml[wave][0] = m; sm.ml[wave][1] = l; }
      if (c == ((NL - 1) & 15)) sm.pl[g][wave] = pr[NB - 1];
      STAMP(2); WSTAMP(2);
    }
    if constexpr (v >= SK1) pv_row(IC<v - SK1 + SK0 - WV>{});
    if constexpr (v < SK0 || v >= SK0 + WV) sfor<cons_end(v - 1), cons_end(v)>(pv_row);
    if constexpr (v == RVU) {
      if (wave == 7) {
        const bool ok = tbg[1] == tag;
        float lg = ok ? (float)(tbg[0] & 0xffffu) : 0.f, nf = ok ? 8.f * (RVP - RVS + 1) : 0.f;
        lg += dpp<XOR1>(lg); nf += dpp<XOR1>(nf); lg += dpp<XOR2>(lg); nf += dpp<XOR2>(nf); lg += dpp<XOR7>(lg); nf += dpp<XOR7>(nf); lg += dpp<XOR8>(lg); nf += dpp<XOR8>(nf);
        lg *= __builtin_amdgcn_rcpf(fmaxf(nf, 1.f));
        if (lane == 0) sm.bc[1] = (u32)min(max((int)((lg - (float)P.F0) * (float)(P.FG * 32)), 0), 200 * 256);
      }
      LDS_BARRIER();
      const int st = __builtin_amdgcn_readfirstlane(sm.bc[1]), sp = (st * P.FF_) >> 3;
      t1 += st; sh1 += st >> 8 << 16;
      due += sp;
      tau += __builtin_amdgcn_readfirstlane((int)((float)(st - sp) * __builtin_amdgcn_rcpf((float)(nl - RVU - 3))));
    }
    if constexpr (v == SK1 + 1) {
      float p[8];
#pragma unroll
      for (int w = 0; w < 8; ++w) p[w] = sm.pl[g][w] * __expf(sm.ml[w][0] - m);
#pragma unroll
      for (int w = 0; w < 8; ++w) pl8[w] = pack_bf(p[w], p[w]);
#pragma unroll
      for (int w = 0; w < 8; ++w) PIN(pl8[w]);
    }
    if constexpr (v == MLS - 1) { side_pin(); if (is_up) up_prep(); PIN(ga); PIN(xin); PIN(cc1); }
    if constexpr (v == UPREP - 1) wo_addr();
    if constexpr (v == UPREP) {
#pragma unroll
      for (int k = 0; k < 8; ++k) asm volatile("" : "=v"(bf[k]));
      if (part == 0) wo_prep(); else if (is_up) up_frags();
#pragma unroll
      for (int k = 0; k < 8; ++k) PIN(bf[k]);
    }
    if constexpr (v == UPREP + 1) {
      if (is_up) {
        const u32x4* a1 = ap + 128 + ((lane >> 1) & 1) * 128;
        f32x4 d0 = {0.f, 0.f, 0.f, 0.f}, d1 = {0.f, 0.f, 0.f, 0.f};
#pragma unroll
        for (int rb = 0; rb < 4; ++rb) { d0 = mfma16(a1[rb * 8], bf[rb], d0); d1 = mfma16(a1[(rb + 4) * 8], bf[rb + 4], d1); }
        Af = par ? d0[1] + d1[1] : d0[0] + d1[0]; Bf = par ? d0[3] + d1[3] : d0[2] + d1[2];
        Af += dpp<XOR8>(Af); Bf += dpp<XOR8>(Bf);
      }
      PIN(Af); PIN(Bf);
    }
    if (v == 10 || v == 16) STAMP(15 + v / 6);
    if (v == 20) STAMP(18);
    if (v == 32) STAMP(19);
    if constexpr (v == MLS) {
      LDS_BARRIER();
      float mm[8], ll[8];
#pragma unroll
      for (int w = 0; w < 8; ++w) { const f32x2 t = *reinterpret_cast<const f32x2*>(&sm.ml[w][0]); mm[w] = t[0]; ll[w] = t[1]; }
      float Mwg = mm[0], Lwg = 0.f;
#pragma unroll
      for (int w = 1; w < 8; ++w) Mwg = fmaxf(Mwg, mm[w]);
      if (wave == 0) {
#pragma unroll
        for (int w = 0; w < 8; ++w) Lwg = fmaf(__expf(mm[w] - Mwg), ll[w], Lwg);
        if (lane == 0) st16_sc1(wsr, G_ML + ((head * 2 + b) * 4 + part) * 16, __float_as_uint(Mwg), __float_as_uint(Lwg), tag);
      }
      const float fw = __expf(m - Mwg);
      m = Mwg;
#pragma unroll
      for (int bt = 0; bt < NB; ++bt) { pb[bt] = pack_bf(pr[bt] * fw, pr[bt] * fw); PIN(pb[bt]); }
#pragma unroll
      for (int k = 0; k < 4; ++k) { pm[k] = (c >> 1 & 3) == k ? ((c & 1) ? 0xffff0000u : 0x0000ffffu) : 0u; PIN(pm[k]); }
    }
    SB();
    return true;
  });
  asm volatile(".Ltail_start:");
  if (wave != 0) {
    pv_row(IC<SK0 - 1>{});
    WAIT_VM(WV); SB();
    sfor<NL - 1 - WV, NL - 1>(pv_row);
    *LDSP(f32x4, adst0) = acc;
    WSTAMP(14);
    LDS_BARRIER();
  } else {
    u32x4 xr[8], am[8];
#pragma unroll
    for (int w = 0; w < 8; ++w) am[w] = u32x4{pl8[w] & pm[0], pl8[w] & pm[1], pl8[w] & pm[2], pl8[w] & pm[3]};
#pragma unroll
    for (int w = 0; w < 8; ++w) PIN(am[w]);
    {
      const u32 so = __builtin_amdgcn_readlane(sot, NL - 1);
      gate();
      SB();
#pragma unroll
      for (int w = 0; w < 8; ++w) xr[w] = ld16<AUX_NT>(rv, lane * 16, so ^ ((w & 1) ? col(P, 7) : 0u) ^ ((w & 2) ? col(P, 8) : 0u) ^ ((w & 4) ? col(P, 9) : 0u));
      SB();
    }
    u32 rsum = lds_addr(&sm.red[0][c * 8 + (g & 1) * 4]);
    pv_row(IC<SK0 - 1>{});
    WAIT_VM(WV + 8); SB();
    sfor<NL - 1 - WV, NL - 3>(pv_row);
    if (__builtin_expect(!all_ok((mlg[1] == tag) & (mlg[3] == tag)), 0)) {
#pragma nounroll
      for (int spin = 0; spin < SPIN_MAX; ++spin) {
        CFENCE();
        mlg = ld16<AUX_SC1>(wsr, mlo, 0);
        if (all_ok((mlg[1] == tag) & (mlg[3] == tag))) break;
      }
    }
    merge_factor(mlg);
    sfor<NL - 3, NL - 1>(pv_row);
    STAMP(14); WSTAMP(14);
    LDS_BARRIER();
    STAMP(15);
    f32x4 t[8];
#pragma unroll
    for (int w = 1; w < 8; ++w) t[w] = LDSP(f32x4, rsum)[w * 32];
    acc = ((acc + t[1]) + (t[2] + t[3])) + ((t[4] + t[5]) + (t[6] + t[7]));
    PIN(acc);
    STAMP(3);
    SB();
#pragma unroll
    for (int w = 0; w < 8; ++w) acc = mfma16(am[w], xr[w], acc);
  }
  WAIT_VM(0);
  acc *= fmrg;
  {
    const u32 w0 = min(pack_bf(acc[0], acc[1]), SENT - 1u), w1 = min(pack_bf(acc[2], acc[3]), SENT - 1u);
    const auto e0 = __builtin_amdgcn_permlane16_swap(w0, w0, false, false), e1 = __builtin_amdgcn_permlane16_swap(w1, w1, false, false);
    __builtin_amdgcn_raw_buffer_store_b128(u32x4{e0[0], e1[0], e0[1], e1[1]}, wsr, pso, 0, AUX_SC1);
  }
  st8_sc1(wsr, nto, (u32)wall_clock64() + nadd, tag);
  SB();
  STAMP(4);

  if (part == 2) FINISH();
  if (part == 0) {
    __builtin_amdgcn_s_barrier();
#pragma nounroll
    while (wleft > 0) piece();
    bool late = false;
    u32x4 og[2];
    if (wdef) {
      late = true;
      while ((int)(((u32)wall_clock64() - t0) << 8) < t1 - 20 * 256) if (__builtin_amdgcn_s_getreg(7 | (3 << 11)) == 0) { late = false; break; }
      if (__builtin_expect(late, 0)) { until(t1); CFENCE(); og[0] = ld16<AUX_SC1>(wsr, po, 0); SB(); WAIT_VM(1); } else { WAIT_VM(0); }
      CFENCE();
      if (P.WD > 4) wo_prep(0, 1);
      wo_prep(1, 2);
#pragma unroll
      for (int k = 0; k < 8; ++k) PIN(bf[k]);
      SB();
    }
    if (__builtin_expect(!late, 1)) { until(t1); CFENCE(); og[0] = ld16<AUX_SC1>(wsr, po, 0); }
    STAMP(11);
    SB();
    f32x4 d[4];
    auto wo_mm = [&]() {
      const f32x4 z = {0.f, 0.f, 0.f, 0.f};
#pragma unroll
      for (int t = 0; t < 4; ++t) { d[t] = mfma16(og[0], bf[t], z); og[1][t] = __float_as_uint(dpp<XOR8>(u2f(og[0][t]))); }
      SB();
#pragma unroll
      for (int t = 0; t < 4; ++t) d[t] = mfma16(og[1], bf[4 + t], d[t]);
    };
    auto p_bad = [&]() { return !all_ok(max(max(og[0][0], og[0][1]), max(og[0][2], og[0][3])) != SENT); };
    wo_mm();
    if (__builtin_expect(p_bad(), 0)) {
#pragma nounroll
      for (int spin = 0; spin < SPIN_MAX; ++spin) {
        __builtin_amdgcn_s_sleep(1);
        CFENCE();
        og[0] = ld16<AUX_SC1>(wsr, po, 0);
        if (!p_bad()) break;
      }
      wo_mm();
      SB();
      asm volatile(".Lend_d:");
    }
    STAMP(5); WSTAMP(5);
    {
      SB();
      f32x4 y; f32x2 h[4];
#pragma unroll
      for (int t = 0; t < 4; ++t) { h[t] = d[t].xy + d[t].zw; PIN(h[t]); }
#pragma unroll
      for (int t = 0; t < 4; ++t) { y[t] = h[t][0] + h[t][1]; PIN(y[t]); }
      *LDSP(f32x4, rdst) = y;
    }
    STAMP(16);
    LDS_BARRIER();
    STAMP(17);
    if (wave < 2) {
      f32x2 v[4];
#pragma unroll
      for (int w = 0; w < 4; ++w) v[w] = f32x2{LDSP(float, rsrc)[w * 256], LDSP(float, rsrc)[w * 256 + 128]};
      const f32x2 h = (v[0] + v[1]) + (v[2] + v[3]);
      const float y = h[0] + h[1];
      __builtin_amdgcn_raw_buffer_store_b32(min(pack_bf(dpp<0xA0>(y), dpp<0xF5>(y)), SENT - 1u), wsr, xo, 0, AUX_SC1);
      asm volatile(".Lend_a:");
    }
    STAMP(6);
    FINISH();
  }
  if (part == 3) {
    if (!has_w) FINISH();
    u32x4 b2[4];
    {
      __builtin_amdgcn_s_barrier();
      const rsrc_t r2 = mk_rsrc(w2 + (size_t)(blk * 32 + wave * 4) * 512, 4096u);
      const u32 l2 = (u32)(uintptr_t)(&sm.w2b[wave * 4][0]);
#pragma unroll
      for (int n = 0; n < 4; ++n) dma16(r2, (lane ^ (n * 2) ^ ((wave & 2) << 2)) * 16, n * 1024, l2 + n * 1024);
    }
    const u32 tdst = lds_addr(&sm.hb[hpos]);
    const u32 sdst = lds_addr(&sm.stat[wave][lane]);
    const u32 ssrc = lds_addr(&sm.stat[(g >> 1) * 4 + ((lane >> 3) & 3)][(lane & 7) * 4]);
    const u32 adst = lds_addr((c < 4 && (g & 1) == 0) ? &sm.actl[g >> 1][wave * 4 + c] : reinterpret_cast<u16*>(&sm.dummy[lane]));
    const u32 asrc = lds_addr(&sm.actl[lane & 1][g * 8]);
    const u32 apa = lds_addr(&sm.hb[(c >> 3) * 512 + (c & 3) * 64 + ((c >> 2) & 1) * 32 + g * 8]);
    constexpr u32 xs2 = 16384;
    u32 xoff = dup(G_XP) + near + g * 2048 + ub * 1024 + (wave & 3) * 256 + c * 16;
    u32x4 one;
#pragma unroll
    for (int k = 0; k < 4; ++k) one[k] = ((c & 2) == 0 && (c >> 2) == k) ? ((c & 1) ? 0x3f800000u : 0x00003f80u) : 0u;
    PIN(one);
    u32 ooff = dup(G_OP + (blk * 512 + tid) * 8);
    float Afp = Af, Bfp = Bf;
    PIN(xoff); PIN(ooff); PIN(Afp); PIN(Bfp);
    f32x2 gc = ga * cc1, xc = xin * cc1;
    PIN(ga); PIN(xin); PIN(gc); PIN(xc);
    u32 w2a = (u32)(uintptr_t)(&sm.w2b[0][0]) + (g * 8 + ((lane >> 2) & 3)) * 1024 + (wave ^ (g & 1)) * 128 + (lane & 15) * 8;
    PIN(w2a);
    if (wave == 1) {
      due = t1 + P.N1 * 256;
      const int tn = notices();
      due = tn + P.X2 * 256;
      until(due);
    }
    asm volatile("s_waitcnt vmcnt(0)" ::: "memory");
    STAMP(9);
    __builtin_amdgcn_s_barrier();
    STAMP(11);
    CFENCE();
    u32x4 xg[2] = {ld16<AUX_SC1>(wsr, xoff, 0), ld16<AUX_SC1>(wsr, xoff, xs2)};
    SB();
#pragma unroll
    for (int t = 0; t < 4; ++t) {
      const u32x2 lo = tr_read(w2a ^ (t * 32)), hi = tr_read((w2a ^ (t * 32)) + 4096);
      b2[t] = u32x4{lo[0], lo[1], hi[0], hi[1]};
    }
#pragma unroll
    for (int t = 0; t < 4; ++t) PIN(b2[t]);
    SB();
    const f32x4 z4 = {0.f, 0.f, 0.f, 0.f};
    auto xp_bad = [&]() { return !all_ok(max(max(max(xg[0][0], xg[0][1]), max(xg[0][2], xg[0][3])), max(max(xg[1][0], xg[1][1]), max(xg[1][2], xg[1][3]))) != SENT); };
    f32x4 v = mfma16(one, xg[1], mfma16(one, xg[0], z4));
    if (__builtin_expect(xp_bad(), 0)) {
#pragma nounroll
      for (int spin = 0; spin < SPIN_MAX; ++spin) {
        __builtin_amdgcn_s_sleep(1);
        CFENCE();
        xg[0] = ld16<AUX_SC1>(wsr, xoff, 0); xg[1] = ld16<AUX_SC1>(wsr, xoff, xs2);
        if (!xp_bad()) break;
      }
      v = mfma16(one, xg[1], mfma16(one, xg[0], z4));
      SB();
      asm volatile(".Lend_e:");
    }
    STAMP(5); WSTAMP(5);
    {
      const f32x2 a = v.xy, hc = __builtin_elementwise_fma(gc, a, xc), x1 = __builtin_elementwise_fma(ga, a, xin);
      *LDSP(u32, tdst) = pack_bf(hc[0], hc[1]);
      *LDSP(float, sdst) = swap32_add(x1[0] + x1[1], fmaf(x1[0], x1[0], x1[1] * x1[1]));
    }
    STAMP(16);
    LDS_BARRIER();
    STAMP(17);
    {
      const f32x4 z = {0.f, 0.f, 0.f, 0.f};
      const u32x4 af0 = LDSP(u32x4, apa)[0];
      SB();
      const f32x4 s4 = LDSP(f32x4, ssrc)[0], q4 = LDSP(f32x4, ssrc)[8];
      const u32x4 af1 = LDSP(u32x4, apa)[32];
      SB();
      f32x4 d[4];
      d[0] = mfma16(af0, bf[0], z); d[1] = mfma16(af0, bf[1], z);
      SB();
      const f32x2 s2 = s4.xy + s4.zw, q2 = q4.xy + q4.zw;
      float s = s2[0] + s2[1], q = q2[0] + q2[1];
      SB();
      d[2] = mfma16(af0, bf[2], z);
      SB();
      s = swap16_add(s, q);
      SB();
      d[3] = mfma16(af0, bf[3], z);
      SB();
      s += dpp<XOR1>(s); s += dpp<XOR2>(s);
      SB();
      d[0] = mfma16(af1, bf[4], d[0]);
      SB();
      s += dpp<XOR7>(s); s += dpp<XOR8>(s);
      SB();
      d[1] = mfma16(af1, bf[5], d[1]);
      SB();
      const auto sq = __builtin_amdgcn_permlane16_swap(__float_as_uint(s), __float_as_uint(s), false, false);
      const float mean = u2f(sq[0]) * (1.f / 512.f);
      SB();
      d[2] = mfma16(af1, bf[6], d[2]);
      SB();
      const float var = fmaf(u2f(sq[1]), 1.f / 512.f, -mean * mean);
      SB();
      d[3] = mfma16(af1, bf[7], d[3]);
      SB();
      float ri = __builtin_amdgcn_rsqf(fmaxf(var, 0.f) + 1e-5f);
      float cf = fmaf(-ri * mean, Afp, Bfp);
      float rk = ri * -1.44269504f, ck = cf * -1.44269504f;
      PIN(ri); PIN(cf); PIN(rk); PIN(ck);
      SB();
      PIN(d[0]); PIN(d[1]);
      float y = d[0][0] + d[1][1];
      PIN(y); PIN(d[2]);
      y += d[2][2];
      PIN(y); PIN(d[3]);
      y += d[3][3];
      y = __builtin_bit_cast(float, __builtin_amdgcn_update_dpp(__builtin_bit_cast(int, y), __builtin_bit_cast(int, y), XOR8, 0xa, 0xf, false));
      y = swap16_add(y, y);
      const float ex = __builtin_amdgcn_exp2f(fmaf(y, rk, ck));
      y = fmaf(y, ri, cf);
      const float gu = y * dpp<0x104>(y);
      *LDSP(u16, adst) = (u16)pack_bf(gu * __builtin_amdgcn_rcpf(1.f + ex), 0.f);
    }
    STAMP(18);
    LDS_BARRIER();
    STAMP(10);
    {
      const u32x4 af = *LDSP(u32x4, asrc);
      const f32x4 z = {0.f, 0.f, 0.f, 0.f};
      f32x4 d0 = mfma16(af, b2[0], z), d1 = mfma16(af, b2[1], z), d2 = mfma16(af, b2[2], z), d3 = mfma16(af, b2[3], z);
      asm volatile("" : "+v"(d0), "+v"(d1), "+v"(d2), "+v"(d3));
      const bool g1 = g & 1, g2 = g & 2;
      const float e0 = g1 ? d1[0] : d0[0], e1 = g1 ? d1[1] : d0[1], f0 = g1 ? d3[0] : d2[0], f1 = g1 ? d3[1] : d2[1];
      const u32x2 ov = {pack_bf(g2 ? f0 : e0, g2 ? f1 : e1), tag};
      __builtin_amdgcn_raw_buffer_store_b64(ov, wsr, ooff, near ^ 8192u, AUX_SC1);
      __builtin_amdgcn_raw_buffer_store_b64(ov, wsr, ooff, near, AUX_SC1);
    }
    asm volatile(".Lend_b:");
    STAMP(6);
    FINISH();
  }
  if (wave == 0) {
    if (!all_ok((sg[0][1] == tag) & (sg[0][3] == tag) & (sg[1][1] == tag) & (sg[1][3] == tag))) {
      u32x4 t[2];
      gather16<2>(wsr, G_MOD + (1024 + fd) * 8, 1536 * 8, tag, t);
      sg[0] = t[0]; sg[1] = t[1];
    }
    u32 xa;
    {
      due = t1 + P.N2 * 256;
      const int tn = notices();
      due = tn + P.X3 * 256;
      until(due);
    }
    const bool hb = g & 1;
#pragma nounroll
    for (int spin = 0; spin < SPIN_MAX; ++spin) {
      CFENCE();
      xa = __builtin_amdgcn_raw_buffer_load_b32(wsr, dup(G_XP) + near + (g >> 1) * 16384 + (c >> 2) * 2048 + (g & 1) * 1024 + fd * 2, 0, AUX_SC1);
      if (all_ok(xa != SENT)) break;
      __builtin_amdgcn_s_sleep(1);
    }
    float a0 = lo_f(xa), a1 = hi_f(xa);
    a0 += dpp<0x124>(a0); a1 += dpp<0x124>(a1);
    a0 += dpp<XOR8>(a0); a1 += dpp<XOR8>(a1);
    a0 = swap32_add(a0, a0); a1 = swap32_add(a1, a1);
    f32x2 xv = {rbf(lo_f(sx2) + rbf((hb ? hi_f(sg[0][0]) : lo_f(sg[0][0])) * rbf(a0))), rbf(hi_f(sx2) + rbf((hb ? hi_f(sg[0][2]) : lo_f(sg[0][2])) * rbf(a1)))};
    f32x2 gv = {hb ? hi_f(sg[1][0]) : lo_f(sg[1][0]), hb ? hi_f(sg[1][2]) : lo_f(sg[1][2])};
    PIN(xv); PIN(gv);
    if (wi == 0 && lane == 0) __builtin_amdgcn_raw_buffer_store_b32(tag, wsr, WS_EPOCH, 0, AUX_SC1);
    STAMP(10);
    due = max(due + P.X4 * 256, (int)(((u32)wall_clock64() - t0) << 8) + P.GX * 256);
    const bool last = lane < (NUP - 32) * 4;
    u32 o0 = dup(G_OP + (lane >> 2) * 4096 + fd * 8) + near, o2 = last ? o0 : OOB, tg2 = last ? tag : 0u;
    const rsrc_t rout = mk_rsrc(out, 2048u);
    u32 oo = (lane & 44) == 0 ? (g * 512 + fd) * 2 : OOB;
    const u32 pk = (c & 10) ? 0u : ((c & 4) ? 0x3f800000u : 0x00003f80u);
    u32x4 one = {(c & 1) ? 0u : pk, 0u, (c & 1) ? pk : 0u, 0u};
    PIN(o0); PIN(o2); PIN(tg2); PIN(oo); PIN(one);
    __builtin_amdgcn_raw_buffer_store_b32(pack_bf(xv[0], xv[1]), rout, oo, 0, AUX_SC1);
    u32 s1_ = 131072, s2_ = 262144;
    asm volatile("" : "+s"(s1_), "+s"(s2_));
    until(due);
    f32x4 sv;
#pragma nounroll
    for (int spin = 0; spin < SPIN_MAX; ++spin) {
      CFENCE();
      const u32x4 og[3] = {ld16<AUX_SC1>(wsr, o0, 0), ld16<AUX_SC1>(wsr, o0, s1_), ld16<AUX_SC1>(wsr, o2, s2_)};
      sv = f32x4{0.f, 0.f, 0.f, 0.f};
#pragma unroll
      for (int k = 0; k < 3; ++k) sv = mfma16(one, og[k], sv);
      SB();
      u32 bad = ((og[0][1] ^ tag) | (og[0][3] ^ tag) | (og[1][1] ^ tag)) | ((og[1][3] ^ tag) | (og[2][1] ^ tg2) | (og[2][3] ^ tg2));
      PIN(bad);
      SB();
      float s0 = sv[0] + dpp<0x124>(sv[0]), s1 = sv[1] + dpp<0x124>(sv[1]);
      PIN(s0); PIN(s1);
      s0 += dpp<XOR8>(s0); s1 += dpp<XOR8>(s1);
      PIN(s0); PIN(s1);
      __builtin_amdgcn_raw_buffer_store_b32(pack_bf(fmaf(gv[0], s0, xv[0]), fmaf(gv[1], s1, xv[1])), rout, oo, 0, AUX_SC1);
      SB();
      if (all_ok(bad == 0u)) break;
      __builtin_amdgcn_s_sleep(1);
    }
    STAMP(7);
    asm volatile(".Lend_c:");
  }
  FINISH();
}

extern "C" void kernel_launch(void* const* d_in, const int* in_sizes, int n_in,
                              void* d_out, int out_size, void* d_ws, size_t ws_size,
                              hipStream_t stream) {
  fused_kernel<<<256, 512, 0, stream>>>((const u16*)d_in[1], (const u16*)d_in[2], (const u16*)d_in[4], (const u16*)d_in[3], (const u16*)d_in[10], (const u16*)d_in[6], (const u16*)d_in[7],
                                        (unsigned char*)d_ws, (const u16*)d_in[0], (const u16*)d_in[9], (const u16*)d_in[8], (u16*)d_out);
}
```

```cpp
#define PROBE_MUL 10
#include <hip/hip_runtime.h>
#include <stdint.h>

typedef unsigned int u32;
typedef unsigned short u16;
typedef unsigned long long u64;
typedef __attribute__((ext_vector_type(4))) unsigned u32x4;
typedef __attribute__((ext_vector_type(2))) unsigned u32x2;
typedef __attribute__((ext_vector_type(2))) __bf16 bf16x2;
typedef __attribute__((ext_vector_type(4))) float f32x4;
typedef __attribute__((ext_vector_type(2))) float f32x2;
typedef __amdgpu_buffer_rsrc_t rsrc_t;

constexpr int NH = 32, SK = 16384, DH = 128, WIN = 4096, FF = 1376;
struct Prm { int E0, ES, E1, E2, E2C, EU, TEND, DT_UP, L0, L1, X1, X2, X3, X4, NE, FE, M0, M1, M2, M3, M4, GS, GX, N1, N2, GA, WD, DT_WO, LW, LU, EW, PM, NF, NR, C0, A0, AG, AC, TS, TW, TK, TG, GB, B0, BG, BF, D0, DG, TC, LM, C1, F0, FG, FF_, PA, PD, QA, QD, RT, W[9]; };
constexpr Prm PRM0 = {32, 20, 32, 32, 54, 56, 1980, 64, 12, 30, 83, 70, 100, 89, 3, 9, 0x2511f55b, 0x2b7425d5, 0x6017665, 0x17e0b9ec, 0x31944305, 62, 30, 36, 62, 30, 4, 100, 45, 10, 10, 0x0, 15, 14, 3, 30, 4, 4, 508, 220, 75, 6, 6, 74, 26, 8, 8, 6, 0, 120, 2, 67, 18, 4, 62, 14, 2, 15, 0, 0x77777777, 0x77777777, 0x77777777, 0x77777777, 0x77777777, 0x44444477, 0x44444444, 0x44444444, 0x44444444};
constexpr int NL = 37;
constexpr int SK0 = 21, SK1 = 32;
constexpr int NCMIN = (NL - SK1 + SK0) * 8;
constexpr int WK = 8, WV = 5;
constexpr int WUSE = 12, LNS = 7;
constexpr int QP = 14, QD = 4;
constexpr int VQ = 6;
constexpr int NSL = (NL + 7) / 8;
constexpr int VS = VQ + NSL;
constexpr int MLS = VS + 2;
constexpr int VSIDE = MLS - 5, UPREP = MLS + 6;
constexpr int CPF = 17;
constexpr int SMO = 1;
constexpr int RVS = 5, RVP = 8, RVL = 12, RVU = 16;
constexpr int RPS = 24, RPP = 28;
constexpr int PRB = 12;
constexpr int TBS = 7, TBP = 10, TBL = 13, TBU = 17;
constexpr int window(int s, int w) {
  int n = w;
  for (int u = s - w + 1; u <= s; ++u) n += u == SMO ? 6 : (u == TBL || u == SK1 || u == NL + CPF || u == NL + RVL || u == NL + SK1 ? 1 : (u == QP || u == NL + VQ - 4 ? 2 : (u == NL + VSIDE ? 4 : 0)));
  return n;
}
constexpr int cons_end(int v) {
  const int a = v - WV + 1, c = (v - MLS) * 4;
  const int m = a < c ? a : c;
  return m < 0 ? 0 : (m > NL ? NL : m);
}
constexpr u32 col(const Prm& P, int j) { const int m[6] = {P.M0, P.M1, P.M2, P.M3, P.M4, P.PM}; return (u32)(m[j / 3] >> (j % 3 * 10) & 1023) << 10; }
constexpr u32 first_col(const Prm& P, int k) {
  if (k < 4) return col(P, 12 + k) | 1u << (24 + k);
  if (k < 6) return col(P, k - 4);
  if (k < 8) return col(P, 4 + k) | 1u << (16 + k);
  if (k < 11) return col(P, k - 1);
  return (col(P, 2) ^ col(P, 3) ^ col(P, 4) ^ col(P, 5) ^ col(P, 6) ^ col(P, 10) ^ col(P, 11)) | 0xf00000u;
}
constexpr bool one_to_one(const Prm& P) {
  u32 c[10] = {};
  for (int j = 0; j < 10; ++j) c[j] = col(P, j);
  for (int j = 0; j < 10; ++j) {
    if (c[j] == 0) return false;
    const u32 low = c[j] & (0u - c[j]);
    for (int k = j + 1; k < 10; ++k) if (c[k] & low) c[k] ^= c[j];
  }
  return true;
}
static_assert(one_to_one(PRM0), "every KB of the window exactly once");
struct Tab { u32 e[512]; };
constexpr Tab make_tab(const Prm& P) {
  Tab T{};
  for (int bid = 0; bid < 256; ++bid) {
    const int xcd = bid & 7, part = bid >> 4 & 3, i8 = (bid >> 3 & 1) | bid >> 6 << 1, wi = i8 * 8 + xcd;
    const bool has_mod = wi < 48, has_up = wi >= 21;
    const int cb0 = (i8 + P.C0) & 7, cb1 = (i8 + P.C1) & 7, ms = wi - 20;
    const int mi = wi < 8 ? wi : (wi < 16 ? 8 + ((xcd + 4) & 7) : (wi < 20 ? (wi - 14) * 8 + 3 : 16 + ms / 7 * 8 + ms % 7 + (ms % 7 >= 3 ? 1 : 0)));
    const int ui = xcd * 5 + (xcd > 5 ? xcd - 5 : 0) + (i8 == 6 ? 0 : (xcd >= 5 ? 1 : 0) + (i8 == 7 ? 4 : i8 - 2));
    const bool edge = i8 >= 6;
    const int wd = cb0 == 3 ? 0 : P.WD;
    const int e[4] = {P.E0 - 4 * wd + (cb0 == 3 ? P.ES : 0), P.E1 + (cb1 == 3 ? P.ES : 0), has_mod ? (wi < 16 ? P.E2C : P.E2) + ((mi & 7) == 3 ? P.ES : 0) : 0, has_up ? P.EU + (edge ? P.TC : 0) : 0};
    int tw[4] = {}, win[4] = {}, span[4] = {}, k0[4] = {};
    for (int k = 0; k < 4; ++k) {
      const int np = k == 0 ? 8 - wd : 8;
      tw[k] = np ? e[k] * P.TW / (16 * np) : 0;
      k0[k] = P.NF + tw[k] * np / 2 > P.TK ? P.NF + tw[k] * np / 2 : P.TK;
      span[k] = P.TEND - k0[k] - P.TG - (k == 0 ? (wd ? P.DT_WO : P.A0) : (k == 3 && has_up ? P.DT_UP : 0));
      win[k] = span[k] - (k == 0 && wd ? 0 : (k == 3 && has_up ? P.GA + (edge ? P.LM : 0) : 0));
    }
    int n[4] = {NCMIN / 8, NCMIN / 8, NCMIN / 8, NCMIN / 8};
    for (int left = 128 - NCMIN / 2; left > 0; --left) {
      int best = -1;
      for (int k = 3; k >= 0; --k)
        if (n[k] < NL && (best < 0 || n[k] * win[best] < n[best] * win[k])) best = k;
      ++n[best];
    }
    const int c[4] = {0, n[0] - 1, n[0] + n[1] - 2, n[0] + n[1] + n[2] - 3};
    const int nl = n[part];
    const bool has_w = part < 2 || (part == 2 ? has_mod : has_up);
    const int blk = !has_w ? 0 : (part == 0 ? cb0 : (part == 1 ? xcd * 8 + cb1 : (part == 2 ? mi : ui)));
    T.e[2 * bid] = (u32)(c[part] | nl << 7 | blk << 13 | (has_w ? 1 : 0) << 19 | (part == 0 ? wd : 0) << 20) | (u32)tw[part] << 24;
    T.e[2 * bid + 1] = (u32)(span[part] * P.TS / (4 * (nl - 1)) | span[part] * (1024 - P.TS) / (4 * (nl - 1)) << 16);
  }
  return T;
}
constexpr int step_range(const Prm& P, bool mx_) {
  int mx = 0, mn = 1 << 20;
  const Tab T = make_tab(P);
  for (int bid = 0; bid < 256; ++bid) { const int n = T.e[2 * bid] >> 7 & 63; mx = n > mx ? n : mx; mn = n < mn ? n : mn; }
  return mx_ ? mx : mn;
}
extern "C" __attribute__((used, visibility("protected"))) __constant__ const Tab wg_tab = make_tab(PRM0);
static_assert(step_range(PRM0, true) <= NL && step_range(PRM0, false) >= NCMIN / 8, "a wave's key range does not fit its load steps");
static_assert(UPREP + 1 < SK0 && QP + QD + 1 < SK0 && cons_end(SK0 - 1) == SK0 - WV && NL - SK1 == WV, "steps every wave runs");
constexpr int NUP = FF / 32;
constexpr int SPIN_MAX = 1 << 17;

constexpr u32 WS_EPOCH = 0;
constexpr u32 G_TB = 1024;
constexpr u32 G_MOD = 4096;
constexpr u32 G_Q = G_MOD + 3072 * 8;
constexpr u32 G_ML = G_Q + 4096 * 8;
constexpr u32 G_U = G_ML + 256 * 16;
constexpr u32 G_P = 0;
constexpr u32 G_XP = G_P + 256 * 256;
constexpr u32 G_OP = G_XP + 8 * 512 * 4;
constexpr u32 G_NT = G_OP + 43 * 512 * 8;
constexpr u32 G_PB = G_OP + 44 * 512 * 8;
constexpr u32 G_VT = G_U + 2 * (G_PB + 8192);
constexpr u32 WS_END = G_VT + 2048;
__device__ __forceinline__ u32 dup(u32 o) { return G_U + ((o >> 13) << 14) + (o & 8191u); }
constexpr u32 SENT = 0xffffffffu;
constexpr u32 OOB = 0x40000000u;
#define WST 0

constexpr int AUX_NT = 2, AUX_SC1 = 16;

__device__ __forceinline__ float lo_f(u32 v) { return __uint_as_float(v << 16); }
__device__ __forceinline__ float hi_f(u32 v) { return __uint_as_float(v & 0xffff0000u); }
__device__ __forceinline__ float rbf(float x) { return (float)(__bf16)x; }
__device__ __forceinline__ u32 pack_bf(float lo, float hi) {
  bf16x2 r; r[0] = (__bf16)lo; r[1] = (__bf16)hi; return __builtin_bit_cast(u32, r);
}
__device__ __forceinline__ float dot2(u32 k, u32 q, float acc) {
  return __builtin_amdgcn_fdot2_f32_bf16(__builtin_bit_cast(bf16x2, k), __builtin_bit_cast(bf16x2, q), acc, false);
}
template <int CTRL> __device__ __forceinline__ float dpp(float x) {
  return __builtin_bit_cast(float, __builtin_amdgcn_mov_dpp(__builtin_bit_cast(int, x), CTRL, 0xf, 0xf, true));
}
constexpr int XOR1 = 0xB1, XOR2 = 0x4E, XOR7 = 0x141, XOR8 = 0x128;
__device__ __forceinline__ float swap32_add(float a, float b) {
  auto t = __builtin_amdgcn_permlane32_swap(__float_as_uint(a), __float_as_uint(b), false, false);
  return __uint_as_float(t[0]) + __uint_as_float(t[1]);
}
__device__ __forceinline__ float swap16_add(float a, float b) {
  auto t = __builtin_amdgcn_permlane16_swap(__float_as_uint(a), __float_as_uint(b), false, false);
  return __uint_as_float(t[0]) + __uint_as_float(t[1]);
}
__device__ __forceinline__ float wave_sum(float x) {
  x += dpp<XOR1>(x); x += dpp<XOR2>(x); x += dpp<XOR7>(x); x += dpp<XOR8>(x);
  x = swap16_add(x, x);
  return swap32_add(x, x);
}
__device__ __forceinline__ float wave_max(float x) {
  x = fmaxf(x, dpp<XOR1>(x)); x = fmaxf(x, dpp<XOR2>(x)); x = fmaxf(x, dpp<XOR7>(x)); x = fmaxf(x, dpp<XOR8>(x));
  auto s = __builtin_amdgcn_permlane16_swap(__float_as_uint(x), __float_as_uint(x), false, false);
  x = fmaxf(__uint_as_float(s[0]), __uint_as_float(s[1]));
  auto t = __builtin_amdgcn_permlane32_swap(__float_as_uint(x), __float_as_uint(x), false, false);
  return fmaxf(__uint_as_float(t[0]), __uint_as_float(t[1]));
}
template <int CTRL> __device__ __forceinline__ float sel_add(float lo, float hi, bool up) {
  const float t1 = lo + dpp<CTRL>(lo), t2 = hi + dpp<CTRL>(hi);
  return up ? t2 : t1;
}
template <int I> __device__ __forceinline__ float bc16(float x) {
  return __builtin_bit_cast(float, __builtin_amdgcn_update_dpp(0, __builtin_bit_cast(int, x), 0x150 + I, 0xf, 0xf, false));
}
__device__ __forceinline__ float bcast16(float x, int i) {
  switch (i) {
    case 0: return bc16<0>(x); case 1: return bc16<1>(x); case 2: return bc16<2>(x); case 3: return bc16<3>(x);
    case 4: return bc16<4>(x); case 5: return bc16<5>(x); case 6: return bc16<6>(x); case 7: return bc16<7>(x);
    case 8: return bc16<8>(x); case 9: return bc16<9>(x); case 10: return bc16<10>(x); case 11: return bc16<11>(x);
    case 12: return bc16<12>(x); case 13: return bc16<13>(x); case 14: return bc16<14>(x); default: return bc16<15>(x);
  }
}
__device__ __forceinline__ float silu_f(float x) { return x * __builtin_amdgcn_rcpf(1.f + __expf(-x)); }

template <int AUX> __device__ __forceinline__ u32x4 ld16(rsrc_t r, u32 voff, u32 soff) {
  return __builtin_amdgcn_raw_buffer_load_b128(r, voff, soff, AUX);
}
template <int AUX> __device__ __forceinline__ u32x2 ld8(rsrc_t r, u32 voff, u32 soff) {
  return __builtin_amdgcn_raw_buffer_load_b64(r, voff, soff, AUX);
}
__device__ __forceinline__ void st8_sc1(rsrc_t r, u32 voff, u32 v, u32 tag) {
  __builtin_amdgcn_raw_buffer_store_b64(u32x2{v, tag}, r, voff, 0, AUX_SC1);
}
__device__ __forceinline__ void st16_sc1(rsrc_t r, u32 voff, u32 v0, u32 v1, u32 tag) {
  __builtin_amdgcn_raw_buffer_store_b128(u32x4{v0, tag, v1, tag}, r, voff, 0, AUX_SC1);
}
__device__ __forceinline__ rsrc_t mk_rsrc(const void* p, u32 bytes) {
  return __builtin_amdgcn_make_buffer_rsrc(const_cast<void*>(p), 0, bytes, 0x00020000);
}
#define WAIT_VM(n) __builtin_amdgcn_s_waitcnt(((n) & 15) | (((n) >> 4) << 14) | (7 << 4) | (15 << 8))
#define SB() __builtin_amdgcn_sched_barrier(0)
#define CFENCE() asm volatile("" ::: "memory")
__device__ __forceinline__ bool all_ok(bool ok) { return __builtin_amdgcn_ballot_w64(!ok) == 0ull; }

template <int N> __device__ __forceinline__ void gather16(rsrc_t ws, u32 voff, u32 stride, u32 tag, u32x4 (&g)[N]) {
#pragma nounroll
  for (int spin = 0; spin < SPIN_MAX; ++spin) {
    CFENCE();
    bool ok = true;
#pragma unroll
    for (int i = 0; i < N; ++i) g[i] = ld16<AUX_SC1>(ws, voff + i * stride, 0);
#pragma unroll
    for (int i = 0; i < N; ++i) ok &= (g[i][1] == tag) & (g[i][3] == tag);
    if (all_ok(ok)) break;
    __builtin_amdgcn_s_sleep(8);
  }
}
template <int N> __device__ __forceinline__ void gather8(rsrc_t ws, u32 voff, u32 stride, u32 tag, u32 (&g)[N], bool act = true) {
#pragma nounroll
  for (int spin = 0; spin < SPIN_MAX; ++spin) {
    CFENCE();
    bool ok = true;
    u32x2 t[N];
#pragma unroll
    for (int i = 0; i < N; ++i) t[i] = ld8<AUX_SC1>(ws, voff + i * stride, 0);
#pragma unroll
    for (int i = 0; i < N; ++i) { ok &= (t[i][1] == tag); g[i] = t[i][0]; }
    if (all_ok(ok | !act)) break;
    __builtin_amdgcn_s_sleep(8);
  }
}

struct Smem {
  float red[8][128];
  float ml[8][2];
  float r2[64 * 20];
  u16 hb[3 * 1024];
  float stat[8][64];
  float red2[8][128];
  u32 dummy[64];
  u32x2 bc;
  u32x4 zero[4];
  float pl[4][8];
  u32 mflag[8];
  u32x4 wbuf[8][8][64];
  __attribute__((aligned(256))) u16 w2b[32][512];
  u16 actl[2][32];
};

#define PROBE_K 8
#define PROBE_OP 0
#define PROBE_PART -1
#define STAMP(k) do { if ((k) == PROBE_K && tid == 0) pst = wall_clock64(); } while (0)
#define WSTAMP(k) do {} while (0)
#define FINISH() do { STAMP(8); if (tid == 0) { __hip_atomic_store(reinterpret_cast<u64*>(ws + (1u << 20)) + bid * 2, pt0, __ATOMIC_RELAXED, __HIP_MEMORY_SCOPE_AGENT); \
   __hip_atomic_store(reinterpret_cast<u64*>(ws + (1u << 20)) + bid * 2 + 1, (PROBE_PART < 0 || part == PROBE_PART) ? pst : 0ull, __ATOMIC_RELAXED, __HIP_MEMORY_SCOPE_AGENT); } \
   if (bid == 8 && wave == 0) probe_end(ws, pt0, lane); return; } while (0)
#define PIN(x) asm volatile("" : "+v"(x))
#define LDS_BARRIER() do { __builtin_amdgcn_s_waitcnt(0xc07f); __builtin_amdgcn_s_barrier(); CFENCE(); } while (0)
#define WAVE_SYNC() do { __builtin_amdgcn_fence(__ATOMIC_RELEASE, "wavefront"); __builtin_amdgcn_wave_barrier(); __builtin_amdgcn_fence(__ATOMIC_ACQUIRE, "wavefront"); } while (0)

__device__ __forceinline__ void dma16(rsrc_t r, u32 voff, u32 soff, u32 lds_dst) {
  u32 keep;
  asm volatile("s_mov_b32 %0, m0\n\ts_mov_b32 m0, %4\n\ts_nop 4\n\tbuffer_load_dwordx4 %1, %2, %3 offen nt lds\n\ts_mov_b32 m0, %0"
               : "=&s"(keep) : "v"(voff), "s"(r), "s"(soff), "s"(lds_dst) : "memory");
}
__device__ __forceinline__ void ld16_into(u32x4& d, rsrc_t r, u32 voff, u32 soff) {
  asm volatile("s_nop 4\n\tbuffer_load_dwordx4 %0, %1, %2, %3 offen nt" : "+v"(d) : "v"(voff), "s"(r), "s"(soff) : "memory");
}
__device__ __forceinline__ u32 perm_lo(u32 a, u32 b) { return __builtin_amdgcn_perm(b, a, 0x05040100u); }
__device__ __forceinline__ u32 perm_hi(u32 a, u32 b) { return __builtin_amdgcn_perm(b, a, 0x07060302u); }
__device__ __forceinline__ float rdl(float x, int l) { return __int_as_float(__builtin_amdgcn_readlane(__float_as_int(x), l)); }

typedef __attribute__((ext_vector_type(8))) __bf16 bf16x8;
__device__ __forceinline__ f32x4 mfma16(u32x4 a, u32x4 b, f32x4 c) {
  return __builtin_amdgcn_mfma_f32_16x16x32_bf16(__builtin_bit_cast(bf16x8, a), __builtin_bit_cast(bf16x8, b), c, 0, 0, 0);
}
__device__ __forceinline__ float sum8(const float* p) {
  const f32x4 a = *reinterpret_cast<const f32x4*>(p), b = *reinterpret_cast<const f32x4*>(p + 4);
  return ((a[0] + b[0]) + (a[1] + b[1])) + ((a[2] + b[2]) + (a[3] + b[3]));
}
__device__ __forceinline__ float ln_ri(float var) {
  return __builtin_amdgcn_rcpf(rbf(__builtin_amdgcn_sqrtf(rbf(rbf(fmaxf(var, 0.f)) + 1e-5f))));
}
__device__ __forceinline__ void ln_stats2(const float (&x0)[8], const float (&x1)[8], float& mb0, float& ri0, float& mb1, float& ri1) {
  const float k0 = rdl(x0[0], 0), k1 = rdl(x1[0], 0);
  float s0 = 0.f, s1 = 0.f, q0 = 0.f, q1 = 0.f;
#pragma unroll
  for (int k = 0; k < 8; ++k) {
    const float d0 = x0[k] - k0, d1 = x1[k] - k1;
    s0 += d0; s1 += d1; q0 = fmaf(d0, d0, q0); q1 = fmaf(d1, d1, q1);
  }
  float t = swap16_add(swap32_add(s0, s1), swap32_add(q0, q1));
  t += dpp<XOR1>(t); t += dpp<XOR2>(t); t += dpp<XOR7>(t); t += dpp<XOR8>(t);
  auto u = __builtin_amdgcn_permlane16_swap(__float_as_uint(t), __float_as_uint(t), false, false);
  const float ms = __uint_as_float(u[0]) * (1.f / 512.f), mq = __uint_as_float(u[1]) * (1.f / 512.f);
  const float ri = ln_ri(fmaf(-ms, ms, mq));
  mb0 = rbf(k0 + rdl(ms, 0)); mb1 = rbf(k1 + rdl(ms, 32));
  ri0 = rdl(ri, 0); ri1 = rdl(ri, 32);
}
template <int I> struct IC { static constexpr int v = I; };
template <int I, int N, class F> __device__ __forceinline__ void sfor(F&& f) {
  if constexpr (I < N) { if (f(IC<I>{})) sfor<I + 1, N>(f); }
}
typedef __attribute__((ext_vector_type(4))) short s16x4;
typedef __attribute__((address_space(3))) s16x4* lds_s16x4p;
__device__ __forceinline__ u32x2 tr_read(u32 lds_addr) {
  return __builtin_bit_cast(u32x2, __builtin_amdgcn_ds_read_tr16_b64_v4i16((lds_s16x4p)lds_addr));
}
__device__ __forceinline__ u32 next_tag(u32 e) { return ((e | 0x40004000u) + 1u) & ~0x40004000u; }
__device__ __forceinline__ float u2f(u32 v) { return __uint_as_float(v); }
#define LDSP(T, a) ((__attribute__((address_space(3))) T*)(a))
template <class T> __device__ __forceinline__ u32 lds_addr(T* p) { u32 a = (u32)(uintptr_t)p; PIN(a); return a; }

#define TAB_LOAD "s_add_u32 s30, s28, wg_tab@rel32@lo+4\ns_addc_u32 s31, s29, wg_tab@rel32@hi+12\nglobal_load_dwordx2 %[te], %[va], s[30:31]\n"
#define FC(k) ".Lfc" #k
#define FC_REGS
#define FX(r, bit, k) "s_bitcmp1_b32 %[" #r "], " #bit "\ns_cselect_b32 %[sb], " FC(k) ", 0\ns_xor_b32 %[sa], %[sa], %[sb]\n"
__device__ __forceinline__ void probe_end(unsigned char* ws, u64 pt0, int lane) {
  while (wall_clock64() < pt0 + 5500ull) __builtin_amdgcn_s_sleep(8);
  u64 a[4], c2[4];
  for (int i = 0; i < 4; ++i) {
    a[i] = __hip_atomic_load(reinterpret_cast<u64*>(ws + (1u << 20)) + (i * 64 + lane) * 2, __ATOMIC_RELAXED, __HIP_MEMORY_SCOPE_AGENT);
    c2[i] = __hip_atomic_load(reinterpret_cast<u64*>(ws + (1u << 20)) + (i * 64 + lane) * 2 + 1, __ATOMIC_RELAXED, __HIP_MEMORY_SCOPE_AGENT);
  }
  int mn0 = 1 << 30, mx1 = -(1 << 30), mn1 = 1 << 30, sum = 0, cnt = 0;
  for (int i = 0; i < 4; ++i) {
    const int d0 = (int)(a[i] - pt0);
    mn0 = min(mn0, d0);
    if (c2[i] != 0ull) { const int d1 = (int)(c2[i] - pt0); mx1 = max(mx1, d1); mn1 = min(mn1, d1); sum += d1; cnt += 1; }
  }
  for (int o = 1; o < 64; o <<= 1) { mn0 = min(mn0, __shfl_xor(mn0, o)); mx1 = max(mx1, __shfl_xor(mx1, o)); mn1 = min(mn1, __shfl_xor(mn1, o)); sum += __shfl_xor(sum, o); cnt += __shfl_xor(cnt, o); }
  int X = PROBE_OP == 0 ? mx1 : (PROBE_OP == 1 ? sum / max(cnt, 1) : mn1);
  if (PROBE_K == 102) X = mn0;
  X = mn0 + (X - mn0) * PROBE_MUL;
  const u64 tg = pt0 + (u64)(long long)(X + 8000);
  for (int i = 0; i < 400000 && wall_clock64() < tg; ++i) __builtin_amdgcn_s_sleep(1);
}
__global__ void __launch_bounds__(512, 2) fused_kernel(const u16* kc_, const u16* vc_, const u16* wo_, const u16* wq_, const u16* wmod_, const u16* w1_, const u16* w3_,
                                                       unsigned char* ws_, const u16* x_, const u16* cnd_, const u16* w2_, u16* out_) {
  __shared__ __attribute__((aligned(256))) Smem sm;
  const u64 a_ka = (u64)__builtin_amdgcn_kernarg_segment_ptr();
  const int bid = blockIdx.x, tid = threadIdx.x;
  u64 wa, t64; u32x2 te; int tid0;
  u32 ar[14];
  u32 tch2, sa_, sb_, va_;
  u32x4 kr[NL], vr[NL];
  asm volatile(".set .Lfc0, %0\n.set .Lfc1, %1\n.set .Lfc2, %2\n.set .Lfc3, %3\n.set .Lfc4, %4\n.set .Lfc5, %5\n.set .Lfc6, %6\n.set .Lfc7, %7\n.set .Lfc8, %8\n.set .Lfc9, %9\n.set .Lfc10, %10\n.set .Lfc11, %11"
    :: "n"(first_col(PRM0, 0)), "n"(first_col(PRM0, 1)), "n"(first_col(PRM0, 2)), "n"(first_col(PRM0, 3)), "n"(first_col(PRM0, 4)), "n"(first_col(PRM0, 5)),
       "n"(first_col(PRM0, 6)), "n"(first_col(PRM0, 7)), "n"(first_col(PRM0, 8)), "n"(first_col(PRM0, 9)), "n"(first_col(PRM0, 10)), "n"(first_col(PRM0, 11)));
  asm volatile(
    "s_load_dwordx16 s[4:19], %[ka], 0x0\ns_load_dwordx8 s[20:27], %[ka], 0x40\n"
    "s_memrealtime %[t]\n"
    "v_lshlrev_b32 %[va], 3, %[bid]\n"
    "s_getpc_b64 s[28:29]\n.Lcpf_first:\n"
    TAB_LOAD
    "s_cmp_gt_u32 %[bid], 15\ns_cbranch_scc1 .Lno_cpf\n"
    "s_mov_b32 %[sa], .Lend_a-.Lcpf_first\ns_max_i32 %[sa], %[sa], .Lend_b-.Lcpf_first\ns_max_i32 %[sa], %[sa], .Lend_c-.Lcpf_first\ns_max_i32 %[sa], %[sa], .Lend_d-.Lcpf_first\ns_max_i32 %[sa], %[sa], .Lend_e-.Lcpf_first\n"
    "v_lshlrev_b32 %[va], 6, %[tid]\nv_min_u32 %[va], %[sa], %[va]\nglobal_load_dword %[pf], %[va], s[28:29]\n"
    ".Lno_cpf:\n"
    "v_readfirstlane_b32 %[t0], %[tid]\nv_and_b32 %[va], 63, %[tid]\n"
    "s_mov_b32 %[sa], " FC(11) "\n"
    FX(bid, 0, 0) FX(bid, 1, 1) FX(bid, 2, 2) FX(bid, 3, 3) FX(bid, 4, 4) FX(bid, 5, 5) FX(bid, 6, 6) FX(bid, 7, 7) FX(t0, 6, 8) FX(t0, 7, 9) FX(t0, 8, 10)
    "v_lshl_add_u32 %[va], %[va], 4, %[sa]\n"
    "s_waitcnt lgkmcnt(0)\nglobal_load_dwordx4 %[k0], %[va], s[4:5] nt\nglobal_load_dwordx4 %[v0], %[va], s[6:7] nt\n"
    "s_bitcmp1_b32 %[t0], 6\ns_cselect_b64 s[14:15], s[16:17], s[14:15]\n"
    "s_bitcmp1_b32 %[bid], 4\ns_cselect_b64 s[8:9], s[10:11], s[8:9]\ns_cselect_b64 s[12:13], s[14:15], s[12:13]\ns_bitcmp1_b32 %[bid], 5\ns_cselect_b64 %[wa], s[12:13], s[8:9]"
    : "=&{s4}"(ar[0]), "=&{s5}"(ar[1]), "=&{s6}"(ar[2]), "=&{s7}"(ar[3]), "=&{s18}"(ar[4]), "=&{s19}"(ar[5]), "=&{s20}"(ar[6]), "=&{s21}"(ar[7]), "=&{s22}"(ar[8]), "=&{s23}"(ar[9]),
      "=&{s24}"(ar[10]), "=&{s25}"(ar[11]), "=&{s26}"(ar[12]), "=&{s27}"(ar[13]), [wa] "=&s"(wa), [t0] "=&s"(tid0), [t] "=&s"(t64), [te] "=&v"(te), [sa] "=&s"(sa_), [sb] "=&s"(sb_),
      [va] "=&v"(va_), [pf] "=&v"(tch2), [k0] "=&v"(kr[0]), [v0] "=&v"(vr[0])
    : [ka] "s"(a_ka), [bid] "s"(bid), [tid] "v"(tid) : "s8", "s9", "s10", "s11", "s12", "s13", "s14", "s15", "s16", "s17", "s28", "s29", "s30", "s31", "scc", "memory" FC_REGS);
  asm volatile("" : "+s"(ar[0]), "+s"(ar[1]), "+s"(ar[2]), "+s"(ar[3]), "+s"(ar[4]), "+s"(ar[5]), "+s"(ar[6]), "+s"(ar[7]), "+s"(ar[8]), "+s"(ar[9]), "+s"(ar[10]), "+s"(ar[11]), "+s"(ar[12]), "+s"(ar[13]));
  asm volatile("" : "+s"(wa), "+s"(t64), "+s"(tid0), "+s"(sa_));
  const u32 tka = (u32)wall_clock64();
  const u64 pt0 = t64; u64 pst = (PROBE_K == 100 || PROBE_K == 102) ? pt0 : (PROBE_K == 101 ? pt0 + (u32)(tka - (u32)t64) : 0);
  const u32 t0 = tka;
  __builtin_assume(tid >= 0 && tid < 512 && tid0 >= 0 && tid0 < 512 && bid >= 0 && bid < 256);
  constexpr Prm P = PRM0;
  const int TEND = P.TEND;
  const int xcd = bid & 7, part = bid >> 4 & 3;
  const int lane = tid & 63, wave = tid0 >> 6;
  SB();
  const bool p3 = part == 3;
  u32 pitch = (u32)(0x005600c001000020ull >> (part * 16) & 0xffff) * 32u;
  const int sh = p3 ? 2 : 3;
  u32 wv0 = (u32)((p3 ? (wave >> 1) * 128 : wave * 64) + (part == 0 ? xcd * 512 : 0) + (lane >> sh)) * pitch + ((lane & (p3 ? 3 : 7)) ^ (lane >> (p3 ? 5 : 3))) * 16u;
  const u32 wimg = (u32)(uintptr_t)(&sm.wbuf[0][0][0]);
  u32 wsz = (part == 0 ? 4096u : 512u) * pitch, wld = wimg + (p3 ? (wave & 1) * 32768 + (wave >> 1) * 8192 : wave * 8192);
  PIN(wv0);
  asm volatile("" : "+s"(wsz), "+s"(wld), "+s"(pitch));
  auto karg = [&](int i) { return ar[2 * i] | (u64)ar[2 * i + 1] << 32; };
  const u64 a_kc = karg(0), a_vc = karg(1);
  unsigned char* ws = (unsigned char*)karg(2);
  const u16 *x = (const u16*)karg(3), *cnd = (const u16*)karg(4), *w2 = (const u16*)karg(5);
  u16* out = (u16*)karg(6);
  const rsrc_t wsr = mk_rsrc(ws, WS_END);
  asm volatile("s_waitcnt vmcnt(2)" : "+v"(te));
  const u32 te0 = __builtin_amdgcn_readfirstlane(te[0]), te1 = __builtin_amdgcn_readfirstlane(te[1]);
  const int bl0 = te0 & 127, nl = te0 >> 7 & 63, blk = te0 >> 13 & 63;
  const bool has_w = te0 >> 19 & 1;
  const rsrc_t rw = mk_rsrc((const void*)wa, wsz);
  const u32 wstep = pitch << (6 - sh), wflip = p3 ? 32u : 0u;
  u32 wso = blk << (sh + 4);
  const u32 wrot = p3 ? 0 : (wave ^ xcd >> 1) & (part == 0 ? P.RT & 3 : P.RT >> 4 & 7);
  int wleft = 8;
  auto piece = [&]() {
    const u32 k = (8 - wleft) ^ wrot;
    dma16(rw, wv0, wso + k * wstep, wld + k * 1024);
    wv0 ^= (wleft & 1) ? wflip : 0u;
    --wleft;
  };
  SB();
  STAMP(23);
  const int b = bid >> 3 & 1, head = xcd * 4 + (bid >> 6 ^ 3);
  const int g = lane >> 4, c = lane & 15;
  const int i8 = b | bid >> 6 << 1, wi = i8 * 8 + xcd;
  const bool is_up = p3 && has_w, modc = part == 2 && wi < 16;
  const size_t base = ((size_t)(b * NH + head) * SK + (SK - WIN)) * DH;
  const rsrc_t rk = mk_rsrc((const u16*)a_kc + base, WIN * DH * 2), rv = mk_rsrc((const u16*)a_vc + base, WIN * DH * 2);
  int tau = te1 & 0xffff;
  const int tauw = te0 >> 24 << 7, wdef = te0 >> 20 & 15;
  int due = P.NF * 256, now = 0;
  auto gate = [&]() {
    while (now < due) { now = (int)(((u32)wall_clock64() - t0) << 8); if (now < due) __builtin_amdgcn_s_sleep(1); }
    due += tau;
  };
  auto until = [&](int t) { while ((int)(((u32)wall_clock64() - t0) << 8) < t); };
  const u32 pslot = dup(G_P + head * 2048 + (b * 4 + part) * 16);
  const u32 die = __builtin_amdgcn_s_getreg(20 | (2 << 6));
  const bool voter = p3 && i8 == 1, timer = p3 && i8 < 2;
  int vote = 0, lag = 0, shift = 0;
  const u32 pbo = dup(G_PB) + (xcd * 8 + wave) * 128;
  u32 near = 0;
  u32x2 ep; u32 xs0, xs1, tch1; u32x4 xr0, xr1;
  const rsrc_t rx = mk_rsrc(part == 2 ? cnd : x, (part == 1 || (part == 2 && has_w)) ? 2048u : 0u);
  auto small_ops = [&]() {
    ep = ld8<AUX_SC1>(wsr, wave == 7 ? WS_EPOCH : OOB, 0);
    xs0 = __builtin_amdgcn_raw_buffer_load_b16(rx, tid * 2, 0, 0); xs1 = __builtin_amdgcn_raw_buffer_load_b16(rx, tid * 2, 1024, 0);
    xr0 = ld16<0>(rx, part == 1 ? lane * 16 : OOB, 0); xr1 = ld16<0>(rx, part == 1 ? lane * 16 : OOB, 1024);
    tch1 = __builtin_amdgcn_raw_buffer_load_b32(mk_rsrc(w2 + (size_t)blk * 32 * 512, is_up ? 64u : 0u), tid == 384 ? 0 : OOB, 0, AUX_NT);
  };
  if (has_w) {
    if (modc) now = 1 << 30;
#pragma nounroll
    while (wleft > wdef) { gate(); due += tauw - tau; piece(); }
    now = 0;
  }
  due = max(due, P.TK * 256);
  SB();
  u32 sot = sa_ & 0xffc00u;
  {
    const u32 s1 = (bl0 + lane - 1 - (lane >= SK0 ? NL - nl : 0)) ^ (124 + part);
#pragma unroll
    for (int k = 0; k < 7; ++k) sot ^= (u32)-(int)(s1 >> k & 1u) & col(P, k);
    PIN(sot);
  }
  u32 tag = 0;
  u32x4 bf[8];
  auto wo_prep = [&](int s0 = 0, int s1 = 2) {
    const u32 wi_ = wimg + wave * 8192;
    const int q = (lane >> 2) & 3, pp = lane & 3;
#pragma unroll
    for (int s = s0; s < s1; ++s) {
#pragma unroll
      for (int t = 0; t < 4; ++t) {
        const int r0 = s * 32 + g * 8 + q, r1 = r0 + 4, ch = t * 2 + (pp >> 1);
        const u32x2 lo = tr_read(wi_ + r0 * 128 + ((ch ^ (r0 & 7)) * 16) + (pp & 1) * 8), hi = tr_read(wi_ + r1 * 128 + ((ch ^ (r1 & 7)) * 16) + (pp & 1) * 8);
        bf[s * 4 + t] = u32x4{lo[0], lo[1], hi[0], hi[1]};
      }
    }
  };
  auto gemv_go = [&](u32 hh) {
    sm.hb[tid] = (u16)hh; sm.hb[512 + tid] = (u16)(hh >> 16);
    WAVE_SYNC();
    const u32x4* a = (c & 11) == 0 ? reinterpret_cast<const u32x4*>(&sm.hb[(c >> 2) * 512 + wave * 64 + g * 8]) : &sm.zero[0];
    const u32x4 a0 = a[0], a1 = a[(c & 11) == 0 ? 4 : 0];
    const f32x4 z = {0.f, 0.f, 0.f, 0.f};
    f32x4 d[4];
#pragma unroll
    for (int t = 0; t < 4; ++t) d[t] = mfma16(a0, bf[t], z);
#pragma unroll
    for (int t = 0; t < 4; ++t) d[t] = mfma16(a1, bf[4 + t], d[t]);
    if (lane < 32) {
#pragma unroll
      for (int t = 0; t < 4; ++t) sm.r2[(t * 16 + c) * 20 + g * 8 + wave] = d[t][0];
    }
  };
  auto mod_stage = [&]() {
    wo_prep();
    gemv_go(pack_bf(silu_f(lo_f(xs0)), silu_f(lo_f(xs1))));
    __builtin_amdgcn_s_waitcnt(0xc07f);
    if (lane == 0) *LDSP(volatile u32, (u32)(uintptr_t)&sm.mflag[wave]) = 1u;
    if (wave == 7) {
#pragma nounroll
      for (int spin = 0; spin < SPIN_MAX; ++spin) {
        const u32 f = *LDSP(volatile u32, (u32)(uintptr_t)&sm.mflag[lane & 7]);
        if (all_ok(f != 0u)) break;
        __builtin_amdgcn_s_sleep(1);
      }
      CFENCE();
      st8_sc1(wsr, G_MOD + (blk * 64 + lane) * 8, pack_bf(sum8(&sm.r2[lane * 20]), sum8(&sm.r2[lane * 20 + 8])), tag);
    }
    STAMP(9);
  };
  if (lane < 4) sm.zero[lane] = u32x4{0u, 0u, 0u, 0u};
  if (part == 2 && has_w) {
    if (tid < 8) sm.mflag[tid] = 0u;
    LDS_BARRIER();
    if (__builtin_expect(modc, 0)) {
      small_ops();
      __builtin_amdgcn_raw_buffer_store_b128(u32x4{SENT, SENT, SENT, SENT}, wsr, G_U + wi * 10240 + tid * 16, 0, AUX_SC1);
      __builtin_amdgcn_raw_buffer_store_b128(u32x4{SENT, SENT, SENT, SENT}, wsr, tid < 128 ? G_U + wi * 10240 + tid * 16 : OOB, 8192, AUX_SC1);
      asm volatile("s_waitcnt vmcnt(0)" ::: "memory");
      tag = next_tag(__builtin_amdgcn_readfirstlane(ep[0]));
      mod_stage();
      __builtin_amdgcn_s_barrier();
    }
  }
  SB();
  const int fd = wi * 8 + (lane & 3) * 2;
  const int ub = wave >> 2, ud = (wave & 3) * 128 + c * 8 + g * 2;
  u32x4 sg[3]; u32 sx2;
  auto side_loads = [&]() {
    const bool fin = part == 1 && wave == 0;
    sg[0] = ld16<AUX_SC1>(wsr, is_up ? G_MOD + (1024 + ud) * 8 : (fin ? G_MOD + (1024 + fd) * 8 : OOB), 0);
    sg[1] = ld16<AUX_SC1>(wsr, is_up ? G_MOD + (1536 + ud) * 8 : (fin ? G_MOD + (2560 + fd) * 8 : OOB), 0);
    sg[2] = ld16<AUX_SC1>(wsr, is_up ? G_MOD + (2048 + ud) * 8 : OOB, 0);
    sx2 = __builtin_amdgcn_raw_buffer_load_b32(mk_rsrc(x, (is_up || fin) ? 2048u : 0u), is_up ? ub * 1024 + ud * 2 : (g & 1) * 1024 + fd * 2, 0, 0);
  };
  auto side_pin = [&]() { PIN(sg[0]); PIN(sg[1]); PIN(sg[2]); PIN(sx2); };
  const u32 mlo = G_ML + (head * 2 + b) * 64 + (lane & 3) * 16;
  float fmrg = 0.f;
  auto merge_factor = [&](const u32x4& ml) {
    const float mi = u2f(ml[0]);
    float M = fmaxf(mi, dpp<XOR1>(mi)); M = fmaxf(M, dpp<XOR2>(M));
    const float e = __expf(mi - M);
    float L = e * u2f(ml[2]);
    L += dpp<XOR1>(L); L += dpp<XOR2>(L);
    fmrg = rdl(e * __builtin_amdgcn_rcpf(L), part);
  };
  f32x2 ga = {0.f, 0.f}, xin = {0.f, 0.f}, cc1 = {0.f, 0.f};
  float Af = 0.f, Bf = 0.f;
  const int hpos = ub * 512 + ud;
  auto up_prep = [&]() {
    if (!all_ok((sg[0][1] == tag) & (sg[0][3] == tag) & (sg[1][1] == tag) & (sg[1][3] == tag) & (sg[2][1] == tag) & (sg[2][3] == tag))) gather16<3>(wsr, G_MOD + (1024 + ud) * 8, 512 * 8, tag, sg);
    ga = f32x2{ub ? hi_f(sg[0][0]) : lo_f(sg[0][0]), ub ? hi_f(sg[0][2]) : lo_f(sg[0][2])};
    cc1 = f32x2{rbf(1.f + (ub ? hi_f(sg[2][0]) : lo_f(sg[2][0]))), rbf(1.f + (ub ? hi_f(sg[2][2]) : lo_f(sg[2][2])))};
    xin = f32x2{lo_f(sx2), hi_f(sx2)};
    u32* hp = reinterpret_cast<u32*>(&sm.hb[hpos]);
    hp[512] = pack_bf(cc1[0], cc1[1]);
    hp[1024] = ub ? perm_hi(sg[1][0], sg[1][2]) : perm_lo(sg[1][0], sg[1][2]);
  };
  auto up_frags = [&]() {
    const int pp = lane & 3;
    const u32 a = wimg + (pp & 1) * 32768 + ((pp >> 1) * 32 + g * 8 + ((lane >> 2) & 3)) * 64 + (((wave >> 1) ^ ((g & 1) | (pp & 2))) * 16) + (wave & 1) * 8;
#pragma unroll
    for (int rb = 0; rb < 8; ++rb) {
      const u32x2 lo = tr_read(a + rb * 4096), hi = tr_read(a + rb * 4096 + 256);
      bf[rb] = u32x4{lo[0], lo[1], hi[0], hi[1]};
    }
  };
  const u32x4* ap = reinterpret_cast<const u32x4*>(&sm.hb[((lane >> 3) & 1) * 512 + (lane & 1) * 32 + g * 8]);
  const bool par = lane & 8;
  auto limit = [&](auto sc) {
    constexpr int s = sc.v;
    WAIT_VM(window(s, 1 + (PRM0.W[s >> 3] >> (4 * (s & 7)) & 7)));
    SB();
  };
  auto sample = [&]() { if (timer) lag += (int)(((u32)wall_clock64() - t0) << 8) - due + ((int)__builtin_amdgcn_s_getreg(7 | (3 << 11)) + 1) * tau; };
  auto report = [&](u32 dst) {
    if (timer) {
      if (lane == 0) sm.mflag[wave] = vote * 65536 + min(lag >> 8, 4000);
      lag = 0;
      LDS_BARRIER();
      if (wave == 7) {
        int s = sm.mflag[lane & 7];
        s += __builtin_amdgcn_mov_dpp(s, XOR1, 0xf, 0xf, true); s += __builtin_amdgcn_mov_dpp(s, XOR2, 0xf, 0xf, true); s += __builtin_amdgcn_mov_dpp(s, XOR7, 0xf, 0xf, true);
        if (lane < 8) st8_sc1(wsr, dst + lane * 128 + wi * 8, s, tag);
      }
    }
    SB();
  };
  float l0 = 0.f, l1 = 0.f;
  u32x2 gmd[2], tbg;
  int t1 = 0;
#pragma unroll
  for (int i = SK0; i < SK1; ++i) kr[i] = u32x4{0u, 0u, 0u, 0u};
  const int first = SK0 + NL - nl;
  auto left_out = [&](int i) { int f = first; asm volatile("" : "+s"(f)); return i < f; };
  sfor<1, NL>([&](auto ic) {
    constexpr int i = ic.v;
    if constexpr (i >= SK0 && i < SK1) { if (left_out(i)) return true; }
    const u32 so = __builtin_amdgcn_readlane(sot, i);
    gate();
    if constexpr ((i >= TBS && i <= TBP) || (i >= RPS && i <= RPP)) sample();
    SB();
    if constexpr (i >= SK0 && i < SK1) ld16_into(kr[i], rk, lane * 16, so); else kr[i] = ld16<AUX_NT>(rk, lane * 16, so);
    if constexpr (i == SMO) small_ops();
    if constexpr (i == SMO + 1) { if (voter) __builtin_amdgcn_raw_buffer_store_b32(0u, wsr, pbo + (lane >> 5) * 8192 + (lane & 31) * 4, 0, AUX_SC1); }
    if constexpr (i == SK1) tbg = ld8<AUX_SC1>(wsr, wave == 7 ? G_VT + xcd * 128 + c * 8 : OOB, 0);
    if constexpr (i == TBL) tbg = ld8<AUX_SC1>(wsr, wave == 7 ? G_TB + xcd * 128 + c * 8 : OOB, 0);
    if constexpr (i == QP) { gmd[0] = ld8<AUX_SC1>(wsr, part == 1 ? G_MOD + tid * 8 : OOB, 0); gmd[1] = ld8<AUX_SC1>(wsr, part == 1 ? G_MOD + tid * 8 : OOB, 4096); }
    SB();
    SB();
    if (i >= 4) limit(ic);
    if (i == 1) STAMP(25);
    if (i == 8) STAMP(12);
    if (i == 20) STAMP(21);
    if (i == 32) STAMP(22);
    if (i == TBP) {
      if (timer) {
        if (lane == 0) sm.mflag[wave] = lag;
        lag = 0;
        LDS_BARRIER();
        if (wave == 7) {
          int s = sm.mflag[lane & 7];
          s += __builtin_amdgcn_mov_dpp(s, XOR1, 0xf, 0xf, true); s += __builtin_amdgcn_mov_dpp(s, XOR2, 0xf, 0xf, true); s += __builtin_amdgcn_mov_dpp(s, XOR7, 0xf, 0xf, true);
          if (lane < 8) st8_sc1(wsr, G_TB + lane * 128 + wi * 8, t0 << 10 | (u32)min(s / (2048 * (TBP - TBS + 1)), 1023), tag);
        }
      }
      SB();
    }
    if (i == PRB) {
      if (voter) {
        u64 ta, tb, tc; u32 dm;
        const u32 pa = pbo + (wave & 1) * 8192;
        asm volatile("s_memtime %0\n\ts_waitcnt lgkmcnt(0)\n\ts_load_dword %3, %4, %5\n\ts_waitcnt lgkmcnt(0)\n\ts_memtime %1\n\ts_waitcnt lgkmcnt(0)\n\t"
                     "s_load_dword %3, %4, %6\n\ts_waitcnt lgkmcnt(0)\n\ts_memtime %2\n\ts_waitcnt lgkmcnt(0)"
                     : "=&s"(ta), "=&s"(tb), "=&s"(tc), "=&s"(dm) : "s"(ws), "s"(pa), "s"(pa ^ 8192u));
        const int d = min(max((int)(2 * (u32)tb - (u32)ta - (u32)tc), -500), 500);
        vote = ((wave ^ die) & 1) ? -d : d;
        STAMP(26);
      }
      SB();
    }
    if (i == TBU) {
      if (wave == 7) {
        const bool ok = tbg[1] == tag;
        float lf = ok ? (float)min(max((int)((t0 << 10 | 1023u) - tbg[0]) >> 10, -300), 300) : 0.f, nf = ok ? 1.f : 0.f, lg = ok ? (float)(tbg[0] & 1023u) : 0.f;
        lf += dpp<XOR1>(lf); nf += dpp<XOR1>(nf); lg += dpp<XOR1>(lg); lf += dpp<XOR2>(lf); nf += dpp<XOR2>(nf); lg += dpp<XOR2>(lg);
        lf += dpp<XOR7>(lf); nf += dpp<XOR7>(nf); lg += dpp<XOR7>(lg); lf += dpp<XOR8>(lf); nf += dpp<XOR8>(nf); lg += dpp<XOR8>(lg);
        const float rn = __builtin_amdgcn_rcpf(fmaxf(nf, 1.f));
        shift = __builtin_amdgcn_readfirstlane(min(max((int)((lg * rn - (float)P.GS) * (float)P.GB * 0.125f), 0), 150));
        if (lane == 0) sm.bc = u32x2{tag, t0 - (u32)(int)(lf * rn) + shift};
      }
      LDS_BARRIER();
      const u32x2 bc = sm.bc;
      tag = __builtin_amdgcn_readfirstlane(bc[0]);
      t1 = (__builtin_amdgcn_readfirstlane((int)(bc[1] - t0)) + TEND + P.X1) * 256;
      tau -= __builtin_amdgcn_readfirstlane((int)((float)(int)(t0 - bc[1]) * 256.f * __builtin_amdgcn_rcpf((float)(nl - TBU - 1))));
      SB();
    }
    if (i == RPP) report(G_VT);
    if (i == LNS) {
      tag = next_tag(__builtin_amdgcn_readfirstlane(ep[0]));
      if (part == 1) {
        float x0[8], x1[8], mb0, ri0, mb1, ri1;
#pragma unroll
        for (int k = 0; k < 4; ++k) { x0[2 * k] = lo_f(xr0[k]); x0[2 * k + 1] = hi_f(xr0[k]); x1[2 * k] = lo_f(xr1[k]); x1[2 * k + 1] = hi_f(xr1[k]); }
        ln_stats2(x0, x1, mb0, ri0, mb1, ri1);
        l0 = rbf(rbf(lo_f(xs0) - mb0) * ri0); l1 = rbf(rbf(lo_f(xs1) - mb1) * ri1);
      }
      PIN(l0); PIN(l1);
      SB();
    }
    if (i == WUSE) {
      PIN(tch1); PIN(tch2);
      if ((part == 1 || (part == 2 && has_w && !modc))) {
        WAIT_VM(WK); CFENCE();
        if (part == 2) mod_stage(); else wo_prep();
      }
#pragma unroll
      for (int k = 0; k < 8; ++k) PIN(bf[k]);
      SB();
    }
    if (i == QP + QD) {
      if (part == 1) {
        u32 gm[2] = {gmd[0][0], gmd[1][0]};
        if (!all_ok((gmd[0][1] == tag) & (gmd[1][1] == tag))) { STAMP(11); gather8<2>(wsr, G_MOD + tid * 8, 512 * 8, tag, gm); }
        STAMP(5);
        gemv_go(pack_bf(rbf(l0 * rbf(1.f + lo_f(gm[1]))) + lo_f(gm[0]), rbf(l1 * rbf(1.f + hi_f(gm[1]))) + hi_f(gm[0])));
      }
      SB();
    }
    if (i == QP + QD + 1) {
      if (part == 1) {
        LDS_BARRIER();
        if (wave == 0) {
          const int bb = lane >> 5, jq = lane & 31;
          st8_sc1(wsr, G_Q + (bb * 2048 + blk * 32 + jq) * 8, pack_bf(sum8(&sm.r2[2 * jq * 20 + bb * 8]), sum8(&sm.r2[(2 * jq + 1) * 20 + bb * 8])), tag);
        }
        STAMP(6);
      }
      SB();
    }
    return true;
  });
  PIN(sot);
  if (wave == 7) {
    const bool ok = tbg[1] == tag;
    float vt = ok ? (float)((int)tbg[0] >> 16) : 0.f, lg = ok ? (float)(tbg[0] & 0xffffu) : 0.f, nf = ok ? 8.f * (RPP - RPS + 1) : 0.f;
    vt += dpp<XOR1>(vt); lg += dpp<XOR1>(lg); nf += dpp<XOR1>(nf); vt += dpp<XOR2>(vt); lg += dpp<XOR2>(lg); nf += dpp<XOR2>(nf);
    vt += dpp<XOR7>(vt); lg += dpp<XOR7>(lg); nf += dpp<XOR7>(nf); vt += dpp<XOR8>(vt); lg += dpp<XOR8>(lg); nf += dpp<XOR8>(nf);
    lg *= __builtin_amdgcn_rcpf(fmaxf(nf, 1.f));
    if (lane == 0) sm.bc = u32x2{(__float_as_uint(vt) & 0x80000000u) | (u32)min(shift * P.DG * 32, P.EW * 256), (u32)min(max((int)((lg - (float)P.B0) * (float)(P.BG * 32)) - shift * P.D0 * 32, 0), 200 * 256)};
  }
  LDS_BARRIER();
  {
    const u32x2 bc = sm.bc;
    const int st = __builtin_amdgcn_readfirstlane(bc[1]), sp = (st * P.BF) >> 3;
    near = ((__builtin_amdgcn_readfirstlane(bc[0]) >> 31 ^ die ^ P.NR ^ 1) & 1) * 8192u;
    t1 += st + (__builtin_amdgcn_readfirstlane(bc[0]) & 0xffff);
    due += P.TG * 256 + sp;
    tau = (te1 >> 16) + __builtin_amdgcn_readfirstlane((int)((float)(st - sp) * __builtin_amdgcn_rcpf((float)(nl - 3))));
  }
  const u32 qoff = G_Q + ((b * 32 + head) * 64 + c * 4) * 8;
  u32x4 qg[2], mlg;
  u32 qq[4];
  constexpr int NB = (NL + 15) / 16;
  float xlo, sc[NB], pr[NB], m = 0.f;
  f32x4 acc = {0.f, 0.f, 0.f, 0.f};
  u32 pb[NB], pm[4], pl8[8], cpf;
  const u32 adst0 = lds_addr(lane < 32 ? &sm.red[wave][c * 8 + g * 4] : &sm.r2[(lane - 32) * 4]);
  u32 po, rdst, rsrc, xo;
  auto wo_addr = [&]() {
    po = dup(G_P + xcd * 8192) + tid * 16 + near;
    rdst = lane < 32 ? (u32)(uintptr_t)&sm.red2[wave][0] + lane * 16 : (u32)(uintptr_t)&sm.r2[0] + (lane - 32) * 16;
    rsrc = (u32)(uintptr_t)&sm.red2[0][((wave & 1) * 16 + (lane & 15)) * 4 + (lane >> 4)];
    xo = dup(G_XP + xcd * 2048 + (wave & 1) * 1024 + (blk * 64 + (lane & ~1)) * 2) + (lane & 1) * 8192;
    PIN(po); PIN(rdst); PIN(rsrc); PIN(xo);
  };
  u32 pso = (wave == 0 && (g & 1) == 0) ? pslot + (c >> 3) * 1024 + (c & 3) * 256 + (c >> 2 & 1) * 128 + (g >> 1) * 8192 : OOB;
  PIN(pso);
  const u32 nadd = wdef ? P.LW : (is_up ? P.LU : 0);
  u32 nto = tid < 2 ? dup(G_NT) + bid * 8 + tid * 8192 : OOB;
  PIN(nto);
  auto notices = [&]() -> int {
    u32 off = dup(G_NT) + near + lane * 16;
    PIN(off);
    until(due);
    int mx;
#pragma nounroll
    for (int spin = 0; spin < SPIN_MAX; ++spin) {
      CFENCE();
      const u32x4 ga_ = ld16<AUX_SC1>(wsr, off, 0), gb_ = ld16<AUX_SC1>(wsr, off, 1024);
      mx = max(max((int)(ga_[0] - t0), (int)(ga_[2] - t0)), max((int)(gb_[0] - t0), (int)(gb_[2] - t0)));
      if (all_ok((((ga_[1] ^ tag) | (ga_[3] ^ tag)) | ((gb_[1] ^ tag) | (gb_[3] ^ tag))) == 0u)) break;
      for (int k = 0; k < P.AG; ++k) __builtin_amdgcn_s_sleep(1);
    }
    if (all_ok(mx <= (t1 >> 8) - P.AC)) return t1;
    mx = max(mx, __builtin_amdgcn_mov_dpp(mx, XOR1, 0xf, 0xf, true)); mx = max(mx, __builtin_amdgcn_mov_dpp(mx, XOR2, 0xf, 0xf, true));
    mx = max(mx, __builtin_amdgcn_mov_dpp(mx, XOR7, 0xf, 0xf, true)); mx = max(mx, __builtin_amdgcn_mov_dpp(mx, XOR8, 0xf, 0xf, true));
    mx = max(max(__builtin_amdgcn_readlane(mx, 0), __builtin_amdgcn_readlane(mx, 16)), max(__builtin_amdgcn_readlane(mx, 32), __builtin_amdgcn_readlane(mx, 48)));
    return max(min(mx, 1 << 20) * 256, t1 + P.L0 * 256) + P.L1 * 256;
  };
  auto pv_row = [&](auto jc) {
    constexpr int i = jc.v;
    const u32 pv = __float_as_uint(bcast16(u2f(pb[i >> 4]), i & 15));
    acc = mfma16(u32x4{pv & pm[0], pv & pm[1], pv & pm[2], pv & pm[3]}, vr[i], acc);
    PIN(acc);
    return true;
  };
  sfor<0, NL - 1>([&](auto ic) {
    constexpr int v = ic.v;
    if constexpr (v >= SK0 && v < SK1) { if (left_out(v)) return true; }
    if constexpr (v > 0) {
      const u32 so = __builtin_amdgcn_readlane(sot, v);
      if constexpr (v >= SK1) { if (v == NL - P.NE) tau = (tau * P.FE) >> 4; }
      gate();
      if constexpr (v >= RVS && v <= RVP) sample();
      SB();
      if constexpr (v >= SK0 && v < SK1) ld16_into(vr[v], rv, lane * 16, so); else vr[v] = ld16<AUX_NT>(rv, lane * 16, so);
      SB();
      SB();
    }
    if constexpr (v == SK0 - 1) {
#pragma unroll
      for (int i = SK0; i < SK1; ++i) { vr[i] = u32x4{0u, 0u, 0u, 0u}; PIN(vr[i]); }
    }
    if constexpr (v == VQ - 4) { qg[0] = ld16<AUX_SC1>(wsr, qoff, 0); qg[1] = ld16<AUX_SC1>(wsr, qoff + 16, 0); }
    if constexpr (v == VSIDE) side_loads();
    if constexpr (v == CPF) {
      u64 pc; u32 dlt, len;
      asm volatile("s_getpc_b64 %0\n.Lcpf_here:\n\ts_mov_b32 %1, .Ltail_start-.Lcpf_here\n\ts_mov_b32 %2, .Lend_a-.Ltail_start\n\ts_max_i32 %2, %2, .Lend_b-.Ltail_start\n\ts_max_i32 %2, %2, .Lend_c-.Ltail_start\n\ts_max_i32 %2, %2, .Lend_d-.Ltail_start\n\ts_max_i32 %2, %2, .Lend_e-.Ltail_start" : "=s"(pc), "=s"(dlt), "=s"(len) :: "scc");
      pc += dlt;
      const rsrc_t rc = mk_rsrc((const void*)pc, (part == 3 && i8 == 1) ? len : 0u);
      cpf = __builtin_amdgcn_raw_buffer_load_b32(rc, tid * 64, 0, 0);
    }
    if constexpr (v == SK1 + 2) PIN(cpf);
    if constexpr (v == RVP) report(G_VT + 1024);
    if constexpr (v == RVL) tbg = ld8<AUX_SC1>(wsr, wave == 7 ? G_VT + 1024 + xcd * 128 + c * 8 : OOB, 0);
    if constexpr (v == SK1) mlg = ld16<AUX_SC1>(wsr, wave == 0 ? mlo : OOB, 0);
    SB();
    if constexpr (NL + v < 72) limit(IC<NL + v>{});
    if constexpr (v >= SK0 + WV) { WAIT_VM(v < SK1 ? WV : WV + 1); SB(); }
    if constexpr (v == VQ) {
      if (!all_ok((qg[0][1] == tag) & (qg[0][3] == tag) & (qg[1][1] == tag) & (qg[1][3] == tag))) { STAMP(20); gather16<2>(wsr, qoff, 16, tag, qg); }
      STAMP(1); WSTAMP(1);
      qq[0] = qg[0][0]; qq[1] = qg[0][2]; qq[2] = qg[1][0]; qq[3] = qg[1][2];
    }
    if constexpr (v >= VQ && v < VS) {
      constexpr int sl = v - VQ, bt = sl >> 1;
      float xs[8];
#pragma unroll
      for (int i = 0; i < 8; ++i) {
        float d = 0.f;
        if (sl * 8 + i < NL) {
#pragma unroll
          for (int k = 0; k < 4; ++k) d = dot2(kr[sl * 8 + i][k], qq[k], d);
        }
        xs[i] = d;
      }
#pragma unroll
      for (int h = 0; h < 4; ++h) xs[h] = sel_add<XOR7>(xs[h], xs[h + 4], c & 4);
#pragma unroll
      for (int h = 0; h < 2; ++h) xs[h] = sel_add<XOR2>(xs[h], xs[h + 2], c & 2);
      xs[0] = sel_add<XOR1>(xs[0], xs[1], c & 1);
      if constexpr ((sl & 1) || sl == NSL - 1) {
        const float t = (sl & 1) ? sel_add<XOR8>(xlo, xs[0], c & 8) : xs[0] + dpp<XOR8>(xs[0]);
        const float s = rbf(rbf(t) * 0.08838834764831845f);
        sc[bt] = (bt * 16 + c < SK0 || (bt * 16 + c >= first && bt * 16 + c < NL)) ? s : -__builtin_inff();
        PIN(sc[bt]);
      } else {
        xlo = xs[0];
        PIN(xlo);
      }
    }
    if constexpr (v == VS) {
      float mloc = sc[0];
#pragma unroll
      for (int bt = 1; bt < NB; ++bt) mloc = fmaxf(mloc, sc[bt]);
      m = fmaxf(wave_max(mloc), -3.0e38f);
      float ls = 0.f;
#pragma unroll
      for (int bt = 0; bt < NB; ++bt) { pr[bt] = __expf(sc[bt] - m); ls += pr[bt]; }
      const float l = wave_sum(ls);
      if (lane == 0) { sm.ml[wave][0] = m; sm.ml[wave][1] = l; }
      if (c == ((NL - 1) & 15)) sm.pl[g][wave] = pr[NB - 1];
      STAMP(2); WSTAMP(2);
    }
    if constexpr (v >= SK1) pv_row(IC<v - SK1 + SK0 - WV>{});
    if constexpr (v < SK0 || v >= SK0 + WV) sfor<cons_end(v - 1), cons_end(v)>(pv_row);
    if constexpr (v == RVU) {
      if (wave == 7) {
        const bool ok = tbg[1] == tag;
        float lg = ok ? (float)(tbg[0] & 0xffffu) : 0.f, nf = ok ? 8.f * (RVP - RVS + 1) : 0.f;
        lg += dpp<XOR1>(lg); nf += dpp<XOR1>(nf); lg += dpp<XOR2>(lg); nf += dpp<XOR2>(nf); lg += dpp<XOR7>(lg); nf += dpp<XOR7>(nf); lg += dpp<XOR8>(lg); nf += dpp<XOR8>(nf);
        lg *= __builtin_amdgcn_rcpf(fmaxf(nf, 1.f));
        if (lane == 0) sm.bc[1] = (u32)min(max((int)((lg - (float)P.F0) * (float)(P.FG * 32)), 0), 200 * 256);
      }
      LDS_BARRIER();
      const int st = __builtin_amdgcn_readfirstlane(sm.bc[1]), sp = (st * P.FF_) >> 3;
      t1 += st;
      due += sp;
      tau += __builtin_amdgcn_readfirstlane((int)((float)(st - sp) * __builtin_amdgcn_rcpf((float)(nl - RVU - 3))));
    }
    if constexpr (v == SK1 + 1) {
      float p[8];
#pragma unroll
      for (int w = 0; w < 8; ++w) p[w] = sm.pl[g][w] * __expf(sm.ml[w][0] - m);
#pragma unroll
      for (int w = 0; w < 8; ++w) pl8[w] = pack_bf(p[w], p[w]);
#pragma unroll
      for (int w = 0; w < 8; ++w) PIN(pl8[w]);
    }
    if constexpr (v == MLS - 1) { side_pin(); if (is_up) up_prep(); PIN(ga); PIN(xin); PIN(cc1); }
    if constexpr (v == UPREP - 1) wo_addr();
    if constexpr (v == UPREP) {
#pragma unroll
      for (int k = 0; k < 8; ++k) asm volatile("" : "=v"(bf[k]));
      if (part == 0) wo_prep(); else if (is_up) up_frags();
#pragma unroll
      for (int k = 0; k < 8; ++k) PIN(bf[k]);
    }
    if constexpr (v == UPREP + 1) {
      if (is_up) {
        const u32x4* a1 = ap + 128 + ((lane >> 1) & 1) * 128;
        f32x4 d0 = {0.f, 0.f, 0.f, 0.f}, d1 = {0.f, 0.f, 0.f, 0.f};
#pragma unroll
        for (int rb = 0; rb < 4; ++rb) { d0 = mfma16(a1[rb * 8], bf[rb], d0); d1 = mfma16(a1[(rb + 4) * 8], bf[rb + 4], d1); }
        Af = par ? d0[1] + d1[1] : d0[0] + d1[0]; Bf = par ? d0[3] + d1[3] : d0[2] + d1[2];
        Af += dpp<XOR8>(Af); Bf += dpp<XOR8>(Bf);
      }
      PIN(Af); PIN(Bf);
    }
    if (v == 10 || v == 16) STAMP(15 + v / 6);
    if (v == 20) STAMP(18);
    if (v == 32) STAMP(19);
    if constexpr (v == MLS) {
      LDS_BARRIER();
      float mm[8], ll[8];
#pragma unroll
      for (int w = 0; w < 8; ++w) { const f32x2 t = *reinterpret_cast<const f32x2*>(&sm.ml[w][0]); mm[w] = t[0]; ll[w] = t[1]; }
      float Mwg = mm[0], Lwg = 0.f;
#pragma unroll
      for (int w = 1; w < 8; ++w) Mwg = fmaxf(Mwg, mm[w]);
      if (wave == 0) {
#pragma unroll
        for (int w = 0; w < 8; ++w) Lwg = fmaf(__expf(mm[w] - Mwg), ll[w], Lwg);
        if (lane == 0) st16_sc1(wsr, G_ML + ((head * 2 + b) * 4 + part) * 16, __float_as_uint(Mwg), __float_as_uint(Lwg), tag);
      }
      const float fw = __expf(m - Mwg);
      m = Mwg;
#pragma unroll
      for (int bt = 0; bt < NB; ++bt) { pb[bt] = pack_bf(pr[bt] * fw, pr[bt] * fw); PIN(pb[bt]); }
#pragma unroll
      for (int k = 0; k < 4; ++k) { pm[k] = (c >> 1 & 3) == k ? ((c & 1) ? 0xffff0000u : 0x0000ffffu) : 0u; PIN(pm[k]); }
    }
    SB();
    return true;
  });
  asm volatile(".Ltail_start:");
  if (wave != 0) {
    pv_row(IC<SK0 - 1>{});
    WAIT_VM(WV); SB();
    sfor<NL - 1 - WV, NL - 1>(pv_row);
    *LDSP(f32x4, adst0) = acc;
    WSTAMP(14);
    LDS_BARRIER();
  } else {
    u32x4 xr[8], am[8];
#pragma unroll
    for (int w = 0; w < 8; ++w) am[w] = u32x4{pl8[w] & pm[0], pl8[w] & pm[1], pl8[w] & pm[2], pl8[w] & pm[3]};
#pragma unroll
    for (int w = 0; w < 8; ++w) PIN(am[w]);
    {
      const u32 so = __builtin_amdgcn_readlane(sot, NL - 1);
      gate();
      SB();
#pragma unroll
      for (int w = 0; w < 8; ++w) xr[w] = ld16<AUX_NT>(rv, lane * 16, so ^ ((w & 1) ? col(P, 7) : 0u) ^ ((w & 2) ? col(P, 8) : 0u) ^ ((w & 4) ? col(P, 9) : 0u));
      SB();
    }
    u32 rsum = lds_addr(&sm.red[0][c * 8 + (g & 1) * 4]);
    pv_row(IC<SK0 - 1>{});
    WAIT_VM(WV + 8); SB();
    sfor<NL - 1 - WV, NL - 3>(pv_row);
    if (__builtin_expect(!all_ok((mlg[1] == tag) & (mlg[3] == tag)), 0)) {
#pragma nounroll
      for (int spin = 0; spin < SPIN_MAX; ++spin) {
        CFENCE();
        mlg = ld16<AUX_SC1>(wsr, mlo, 0);
        if (all_ok((mlg[1] == tag) & (mlg[3] == tag))) break;
      }
    }
    merge_factor(mlg);
    sfor<NL - 3, NL - 1>(pv_row);
    STAMP(14); WSTAMP(14);
    LDS_BARRIER();
    STAMP(15);
    f32x4 t[8];
#pragma unroll
    for (int w = 1; w < 8; ++w) t[w] = LDSP(f32x4, rsum)[w * 32];
    acc = ((acc + t[1]) + (t[2] + t[3])) + ((t[4] + t[5]) + (t[6] + t[7]));
    PIN(acc);
    STAMP(3);
    SB();
#pragma unroll
    for (int w = 0; w < 8; ++w) acc = mfma16(am[w], xr[w], acc);
  }
  WAIT_VM(0);
  acc *= fmrg;
  {
    const u32 w0 = min(pack_bf(acc[0], acc[1]), SENT - 1u), w1 = min(pack_bf(acc[2], acc[3]), SENT - 1u);
    const auto e0 = __builtin_amdgcn_permlane16_swap(w0, w0, false, false), e1 = __builtin_amdgcn_permlane16_swap(w1, w1, false, false);
    __builtin_amdgcn_raw_buffer_store_b128(u32x4{e0[0], e1[0], e0[1], e1[1]}, wsr, pso, 0, AUX_SC1);
  }
  st8_sc1(wsr, nto, (u32)wall_clock64() + nadd, tag);
  SB();
  STAMP(4);

  if (part == 2) FINISH();
  if (part == 0) {
    __builtin_amdgcn_s_barrier();
#pragma nounroll
    while (wleft > 0) { until(t1 - (P.PA + wleft * P.PD) * 256); piece(); }
    bool late = false;
    u32x4 og[2];
    if (wdef) {
      late = true;
      while ((int)(((u32)wall_clock64() - t0) << 8) < t1 - 20 * 256) if (__builtin_amdgcn_s_getreg(7 | (3 << 11)) == 0) { late = false; break; }
      if (__builtin_expect(late, 0)) { until(t1); CFENCE(); og[0] = ld16<AUX_SC1>(wsr, po, 0); SB(); WAIT_VM(1); } else { WAIT_VM(0); }
      CFENCE();
      if (P.WD > 4) wo_prep(0, 1);
      wo_prep(1, 2);
#pragma unroll
      for (int k = 0; k < 8; ++k) PIN(bf[k]);
      SB();
    }
    if (__builtin_expect(!late, 1)) { until(t1); CFENCE(); og[0] = ld16<AUX_SC1>(wsr, po, 0); }
    STAMP(11);
    SB();
    f32x4 d[4];
    auto wo_mm = [&]() {
      const f32x4 z = {0.f, 0.f, 0.f, 0.f};
#pragma unroll
      for (int t = 0; t < 4; ++t) { d[t] = mfma16(og[0], bf[t], z); og[1][t] = __float_as_uint(dpp<XOR8>(u2f(og[0][t]))); }
      SB();
#pragma unroll
      for (int t = 0; t < 4; ++t) d[t] = mfma16(og[1], bf[4 + t], d[t]);
    };
    auto p_bad = [&]() { return !all_ok(max(max(og[0][0], og[0][1]), max(og[0][2], og[0][3])) != SENT); };
    wo_mm();
    if (__builtin_expect(p_bad(), 0)) {
#pragma nounroll
      for (int spin = 0; spin < SPIN_MAX; ++spin) {
        __builtin_amdgcn_s_sleep(1);
        CFENCE();
        og[0] = ld16<AUX_SC1>(wsr, po, 0);
        if (!p_bad()) break;
      }
      wo_mm();
      SB();
      asm volatile(".Lend_d:");
    }
    STAMP(5); WSTAMP(5);
    {
      SB();
      f32x4 y; f32x2 h[4];
#pragma unroll
      for (int t = 0; t < 4; ++t) { h[t] = d[t].xy + d[t].zw; PIN(h[t]); }
#pragma unroll
      for (int t = 0; t < 4; ++t) { y[t] = h[t][0] + h[t][1]; PIN(y[t]); }
      *LDSP(f32x4, rdst) = y;
    }
    STAMP(16);
    LDS_BARRIER();
    STAMP(17);
    if (wave < 2) {
      f32x2 v[4];
#pragma unroll
      for (int w = 0; w < 4; ++w) v[w] = f32x2{LDSP(float, rsrc)[w * 256], LDSP(float, rsrc)[w * 256 + 128]};
      const f32x2 h = (v[0] + v[1]) + (v[2] + v[3]);
      const float y = h[0] + h[1];
      __builtin_amdgcn_raw_buffer_store_b32(min(pack_bf(dpp<0xA0>(y), dpp<0xF5>(y)), SENT - 1u), wsr, xo, 0, AUX_SC1);
      asm volatile(".Lend_a:");
    }
    STAMP(6);
    FINISH();
  }
  if (part == 3) {
    if (!has_w) FINISH();
    u32x4 b2[4];
    {
      __builtin_amdgcn_s_barrier();
      const rsrc_t r2 = mk_rsrc(w2 + (size_t)(blk * 32 + wave * 4) * 512, 4096u);
      const u32 l2 = (u32)(uintptr_t)(&sm.w2b[wave * 4][0]);
#pragma unroll
      for (int j = 0; j < 4; ++j) { const u32 n = j ^ ((blk ^ wave) & P.RT >> 2 & 3); until(t1 - (P.QA + (4 - j) * P.QD) * 256); dma16(r2, (lane ^ (n * 2) ^ ((wave & 2) << 2)) * 16, n * 1024, l2 + n * 1024); }
    }
    const u32 tdst = lds_addr(&sm.hb[hpos]);
    const u32 sdst = lds_addr(&sm.stat[wave][lane]);
    const u32 ssrc = lds_addr(&sm.stat[(g >> 1) * 4 + ((lane >> 3) & 3)][(lane & 7) * 4]);
    const u32 adst = lds_addr((c < 4 && (g & 1) == 0) ? &sm.actl[g >> 1][wave * 4 + c] : reinterpret_cast<u16*>(&sm.dummy[lane]));
    const u32 asrc = lds_addr(&sm.actl[lane & 1][g * 8]);
    const u32 apa = lds_addr(&sm.hb[(c >> 3) * 512 + (c & 3) * 64 + ((c >> 2) & 1) * 32 + g * 8]);
    constexpr u32 xs2 = 16384;
    u32 xoff = dup(G_XP) + near + g * 2048 + ub * 1024 + (wave & 3) * 256 + c * 16;
    u32x4 one;
#pragma unroll
    for (int k = 0; k < 4; ++k) one[k] = ((c & 2) == 0 && (c >> 2) == k) ? ((c & 1) ? 0x3f800000u : 0x00003f80u) : 0u;
    PIN(one);
    u32 ooff = dup(G_OP + (blk * 512 + tid) * 8);
    float Afp = Af, Bfp = Bf;
    PIN(xoff); PIN(ooff); PIN(Afp); PIN(Bfp);
    f32x2 gc = ga * cc1, xc = xin * cc1;
    PIN(ga); PIN(xin); PIN(gc); PIN(xc);
    u32 w2a = (u32)(uintptr_t)(&sm.w2b[0][0]) + (g * 8 + ((lane >> 2) & 3)) * 1024 + (wave ^ (g & 1)) * 128 + (lane & 15) * 8;
    PIN(w2a);
    if (wave == 1) {
      due = t1 + P.N1 * 256;
      const int tn = notices();
      due = tn + P.X2 * 256;
      until(due);
    }
    asm volatile("s_waitcnt vmcnt(0)" ::: "memory");
    STAMP(9);
    __builtin_amdgcn_s_barrier();
    STAMP(11);
    CFENCE();
    u32x4 xg[2] = {ld16<AUX_SC1>(wsr, xoff, 0), ld16<AUX_SC1>(wsr, xoff, xs2)};
    SB();
#pragma unroll
    for (int t = 0; t < 4; ++t) {
      const u32x2 lo = tr_read(w2a ^ (t * 32)), hi = tr_read((w2a ^ (t * 32)) + 4096);
      b2[t] = u32x4{lo[0], lo[1], hi[0], hi[1]};
    }
#pragma unroll
    for (int t = 0; t < 4; ++t) PIN(b2[t]);
    SB();
    const f32x4 z4 = {0.f, 0.f, 0.f, 0.f};
    auto xp_bad = [&]() { return !all_ok(max(max(max(xg[0][0], xg[0][1]), max(xg[0][2], xg[0][3])), max(max(xg[1][0], xg[1][1]), max(xg[1][2], xg[1][3]))) != SENT); };
    f32x4 v = mfma16(one, xg[1], mfma16(one, xg[0], z4));
    if (__builtin_expect(xp_bad(), 0)) {
#pragma nounroll
      for (int spin = 0; spin < SPIN_MAX; ++spin) {
        __builtin_amdgcn_s_sleep(1);
        CFENCE();
        xg[0] = ld16<AUX_SC1>(wsr, xoff, 0); xg[1] = ld16<AUX_SC1>(wsr, xoff, xs2);
        if (!xp_bad()) break;
      }
      v = mfma16(one, xg[1], mfma16(one, xg[0], z4));
      SB();
      asm volatile(".Lend_e:");
    }
    STAMP(5); WSTAMP(5);
    {
      const f32x2 a = v.xy, hc = __builtin_elementwise_fma(gc, a, xc), x1 = __builtin_elementwise_fma(ga, a, xin);
      *LDSP(u32, tdst) = pack_bf(hc[0], hc[1]);
      *LDSP(float, sdst) = swap32_add(x1[0] + x1[1], fmaf(x1[0], x1[0], x1[1] * x1[1]));
    }
    STAMP(16);
    LDS_BARRIER();
    STAMP(17);
    {
      const f32x4 z = {0.f, 0.f, 0.f, 0.f};
      const u32x4 af0 = LDSP(u32x4, apa)[0];
      SB();
      const f32x4 s4 = LDSP(f32x4, ssrc)[0], q4 = LDSP(f32x4, ssrc)[8];
      const u32x4 af1 = LDSP(u32x4, apa)[32];
      SB();
      f32x4 d[4];
      d[0] = mfma16(af0, bf[0], z); d[1] = mfma16(af0, bf[1], z);
      SB();
      const f32x2 s2 = s4.xy + s4.zw, q2 = q4.xy + q4.zw;
      float s = s2[0] + s2[1], q = q2[0] + q2[1];
      SB();
      d[2] = mfma16(af0, bf[2], z);
      SB();
      s = swap16_add(s, q);
      SB();
      d[3] = mfma16(af0, bf[3], z);
      SB();
      s += dpp<XOR1>(s); s += dpp<XOR2>(s);
      SB();
      d[0] = mfma16(af1, bf[4], d[0]);
      SB();
      s += dpp<XOR7>(s); s += dpp<XOR8>(s);
      SB();
      d[1] = mfma16(af1, bf[5], d[1]);
      SB();
      const auto sq = __builtin_amdgcn_permlane16_swap(__float_as_uint(s), __float_as_uint(s), false, false);
      const float mean = u2f(sq[0]) * (1.f / 512.f);
      SB();
      d[2] = mfma16(af1, bf[6], d[2]);
      SB();
      const float var = fmaf(u2f(sq[1]), 1.f / 512.f, -mean * mean);
      SB();
      d[3] = mfma16(af1, bf[7], d[3]);
      SB();
      float ri = __builtin_amdgcn_rsqf(fmaxf(var, 0.f) + 1e-5f);
      float cf = fmaf(-ri * mean, Afp, Bfp);
      float rk = ri * -1.44269504f, ck = cf * -1.44269504f;
      PIN(ri); PIN(cf); PIN(rk); PIN(ck);
      SB();
      PIN(d[0]); PIN(d[1]);
      float y = d[0][0] + d[1][1];
      PIN(y); PIN(d[2]);
      y += d[2][2];
      PIN(y); PIN(d[3]);
      y += d[3][3];
      y = __builtin_bit_cast(float, __builtin_amdgcn_update_dpp(__builtin_bit_cast(int, y), __builtin_bit_cast(int, y), XOR8, 0xa, 0xf, false));
      y = swap16_add(y, y);
      const float ex = __builtin_amdgcn_exp2f(fmaf(y, rk, ck));
      y = fmaf(y, ri, cf);
      const float gu = y * dpp<0x104>(y);
      *LDSP(u16, adst) = (u16)pack_bf(gu * __builtin_amdgcn_rcpf(1.f + ex), 0.f);
    }
    STAMP(18);
    LDS_BARRIER();
    STAMP(10);
    {
      const u32x4 af = *LDSP(u32x4, asrc);
      const f32x4 z = {0.f, 0.f, 0.f, 0.f};
      f32x4 d0 = mfma16(af, b2[0], z), d1 = mfma16(af, b2[1], z), d2 = mfma16(af, b2[2], z), d3 = mfma16(af, b2[3], z);
      asm volatile("" : "+v"(d0), "+v"(d1), "+v"(d2), "+v"(d3));
      const bool g1 = g & 1, g2 = g & 2;
      const float e0 = g1 ? d1[0] : d0[0], e1 = g1 ? d1[1] : d0[1], f0 = g1 ? d3[0] : d2[0], f1 = g1 ? d3[1] : d2[1];
      const u32x2 ov = {pack_bf(g2 ? f0 : e0, g2 ? f1 : e1), tag};
      __builtin_amdgcn_raw_buffer_store_b64(ov, wsr, ooff, near ^ 8192u, AUX_SC1);
      __builtin_amdgcn_raw_buffer_store_b64(ov, wsr, ooff, near, AUX_SC1);
    }
    asm volatile(".Lend_b:");
    STAMP(6);
    FINISH();
  }
  if (wave == 0) {
    if (!all_ok((sg[0][1] == tag) & (sg[0][3] == tag) & (sg[1][1] == tag) & (sg[1][3] == tag))) {
      u32x4 t[2];
      gather16<2>(wsr, G_MOD + (1024 + fd) * 8, 1536 * 8, tag, t);
      sg[0] = t[0]; sg[1] = t[1];
    }
    u32 xa;
    {
      due = t1 + P.N2 * 256;
      const int tn = notices();
      due = tn + P.X3 * 256;
      until(due);
    }
    const bool hb = g & 1;
#pragma nounroll
    for (int spin = 0; spin < SPIN_MAX; ++spin) {
      CFENCE();
      xa = __builtin_amdgcn_raw_buffer_load_b32(wsr, dup(G_XP) + near + (g >> 1) * 16384 + (c >> 2) * 2048 + (g & 1) * 1024 + fd * 2, 0, AUX_SC1);
      if (all_ok(xa != SENT)) break;
      __builtin_amdgcn_s_sleep(1);
    }
    float a0 = lo_f(xa), a1 = hi_f(xa);
    a0 += dpp<0x124>(a0); a1 += dpp<0x124>(a1);
    a0 += dpp<XOR8>(a0); a1 += dpp<XOR8>(a1);
    a0 = swap32_add(a0, a0); a1 = swap32_add(a1, a1);
    f32x2 xv = {rbf(lo_f(sx2) + rbf((hb ? hi_f(sg[0][0]) : lo_f(sg[0][0])) * rbf(a0))), rbf(hi_f(sx2) + rbf((hb ? hi_f(sg[0][2]) : lo_f(sg[0][2])) * rbf(a1)))};
    f32x2 gv = {hb ? hi_f(sg[1][0]) : lo_f(sg[1][0]), hb ? hi_f(sg[1][2]) : lo_f(sg[1][2])};
    PIN(xv); PIN(gv);
    if (wi == 0 && lane == 0) __builtin_amdgcn_raw_buffer_store_b32(tag, wsr, WS_EPOCH, 0, AUX_SC1);
    STAMP(10);
    due = max(due + P.X4 * 256, (int)(((u32)wall_clock64() - t0) << 8) + P.GX * 256);
    const bool last = lane < (NUP - 32) * 4;
    u32 o0 = dup(G_OP + (lane >> 2) * 4096 + fd * 8) + near, o2 = last ? o0 : OOB, tg2 = last ? tag : 0u;
    const rsrc_t rout = mk_rsrc(out, 2048u);
    u32 oo = (lane & 44) == 0 ? (g * 512 + fd) * 2 : OOB;
    const u32 pk = (c & 10) ? 0u : ((c & 4) ? 0x3f800000u : 0x00003f80u);
    u32x4 one = {(c & 1) ? 0u : pk, 0u, (c & 1) ? pk : 0u, 0u};
    PIN(o0); PIN(o2); PIN(tg2); PIN(oo); PIN(one);
    __builtin_amdgcn_raw_buffer_store_b32(pack_bf(xv[0], xv[1]), rout, oo, 0, AUX_SC1);
    u32 s1_ = 131072, s2_ = 262144;
    asm volatile("" : "+s"(s1_), "+s"(s2_));
    until(due);
    f32x4 sv;
#pragma nounroll
    for (int spin = 0; spin < SPIN_MAX; ++spin) {
      CFENCE();
      const u32x4 og[3] = {ld16<AUX_SC1>(wsr, o0, 0), ld16<AUX_SC1>(wsr, o0, s1_), ld16<AUX_SC1>(wsr, o2, s2_)};
      sv = f32x4{0.f, 0.f, 0.f, 0.f};
#pragma unroll
      for (int k = 0; k < 3; ++k) sv = mfma16(one, og[k], sv);
      SB();
      u32 bad = ((og[0][1] ^ tag) | (og[0][3] ^ tag) | (og[1][1] ^ tag)) | ((og[1][3] ^ tag) | (og[2][1] ^ tg2) | (og[2][3] ^ tg2));
      PIN(bad);
      SB();
      float s0 = sv[0] + dpp<0x124>(sv[0]), s1 = sv[1] + dpp<0x124>(sv[1]);
      PIN(s0); PIN(s1);
      s0 += dpp<XOR8>(s0); s1 += dpp<XOR8>(s1);
      PIN(s0); PIN(s1);
      __builtin_amdgcn_raw_buffer_store_b32(pack_bf(fmaf(gv[0], s0, xv[0]), fmaf(gv[1], s1, xv[1])), rout, oo, 0, AUX_SC1);
      SB();
      if (all_ok(bad == 0u)) break;
      __builtin_amdgcn_s_sleep(1);
    }
    STAMP(7);
    asm volatile(".Lend_c:");
  }
  FINISH();
}

extern "C" void kernel_launch(void* const* d_in, const int* in_sizes, int n_in,
                              void* d_out, int out_size, void* d_ws, size_t ws_size,
                              hipStream_t stream) {
  fused_kernel<<<256, 512, 0, stream>>>((const u16*)d_in[1], (const u16*)d_in[2], (const u16*)d_in[4], (const u16*)d_in[3], (const u16*)d_in[10], (const u16*)d_in[6], (const u16*)d_in[7],
                                        (unsigned char*)d_ws, (const u16*)d_in[0], (const u16*)d_in[9], (const u16*)d_in[8], (u16*)d_out);
}
```

```cpp
#include <hip/hip_runtime.h>
#include <stdint.h>


typedef unsigned int u32;
typedef unsigned short u16;
typedef unsigned long long u64;
typedef __attribute__((ext_vector_type(4))) unsigned u32x4;
typedef __attribute__((ext_vector_type(2))) unsigned u32x2;
typedef __attribute__((ext_vector_type(2))) __bf16 bf16x2;
typedef __attribute__((ext_vector_type(4))) float f32x4;
typedef __attribute__((ext_vector_type(2))) float f32x2;
typedef __amdgpu_buffer_rsrc_t rsrc_t;

constexpr int NH = 32, SK = 16384, DH = 128, WIN = 4096, FF = 1376;
struct Prm { int E0, ES, E1, E2, E2C, EU, TEND, DT_UP, L0, L1, X1, X2, X3, X4, NE, FE, M0, M1, M2, M3, M4, GS, GX, N1, N2, GA, WD, DT_WO, LW, LU, EW, PM, NF, NR, C0, A0, AG, AC, TS, TW, TK, TG, GB, B0, BG, BF, D0, DG, TC, LM, C1, F0, FG, FF_, PA, PD, QA, QD, RT, W[9]; };
constexpr Prm PRM0 = {32, 20, 32, 32, 54, 56, 1980, 64, 12, 30, 83, 66, 100, 80, 3, 9, 0x2511f55b, 0x2b7425d5, 0x6017665, 0x17e0b9ec, 0x31944305, 62, 30, 32, 62, 30, 4, 100, 45, 10, 10, 0x0, 15, 14, 3, 30, 4, 4, 508, 220, 75, 6, 6, 74, 26, 8, 8, 6, 0, 120, 2, 67, 18, 4, 62, 14, 6, 15, 0, 0x77777777, 0x77777777, 0x77777777, 0x77777777, 0x77777777, 0x44444477, 0x44444444, 0x44444444, 0x44444444};
constexpr int NL = 37;
constexpr int SK0 = 21, SK1 = 32;
constexpr int NCMIN = (NL - SK1 + SK0) * 8;
constexpr int WK = 8, WV = 5;
constexpr int WUSE = 12, LNS = 7;
constexpr int QP = 14, QD = 4;
constexpr int VQ = 6;
constexpr int NSL = (NL + 7) / 8;
constexpr int VS = VQ + NSL;
constexpr int MLS = VS + 2;
constexpr int VSIDE = MLS - 5, UPREP = MLS + 6;
constexpr int CPF = 17;
constexpr int SMO = 1;
constexpr int RVS = 5, RVP = 8, RVL = 12, RVU = 16;
constexpr int RPS = 24, RPP = 28;
constexpr int PRB = 12;
constexpr int TBS = 7, TBP = 10, TBL = 13, TBU = 17;
constexpr int window(int s, int w) {
  int n = w;
  for (int u = s - w + 1; u <= s; ++u) n += u == SMO ? 6 : (u == TBL || u == SK1 || u == NL + CPF || u == NL + RVL || u == NL + SK1 ? 1 : (u == QP || u == NL + VQ - 4 ? 2 : (u == NL + VSIDE ? 4 : 0)));
  return n;
}
constexpr int cons_end(int v) {
  const int a = v - WV + 1, c = (v - MLS) * 4;
  const int m = a < c ? a : c;
  return m < 0 ? 0 : (m > NL ? NL : m);
}
constexpr u32 col(const Prm& P, int j) { const int m[6] = {P.M0, P.M1, P.M2, P.M3, P.M4, P.PM}; return (u32)(m[j / 3] >> (j % 3 * 10) & 1023) << 10; }
constexpr u32 first_col(const Prm& P, int k) {
  if (k < 4) return col(P, 12 + k) | 1u << (24 + k);
  if (k < 6) return col(P, k - 4);
  if (k < 8) return col(P, 4 + k) | 1u << (16 + k);
  if (k < 11) return col(P, k - 1);
  return (col(P, 2) ^ col(P, 3) ^ col(P, 4) ^ col(P, 5) ^ col(P, 6) ^ col(P, 10) ^ col(P, 11)) | 0xf00000u;
}
constexpr bool one_to_one(const Prm& P) {
  u32 c[10] = {};
  for (int j = 0; j < 10; ++j) c[j] = col(P, j);
  for (int j = 0; j < 10; ++j) {
    if (c[j] == 0) return false;
    const u32 low = c[j] & (0u - c[j]);
    for (int k = j + 1; k < 10; ++k) if (c[k] & low) c[k] ^= c[j];
  }
  return true;
}
static_assert(one_to_one(PRM0), "every KB of the window exactly once");
struct Tab { u32 e[512]; };
constexpr Tab make_tab(const Prm& P) {
  Tab T{};
  for (int bid = 0; bid < 256; ++bid) {
    const int xcd = bid & 7, part = bid >> 4 & 3, i8 = (bid >> 3 & 1) | bid >> 6 << 1, wi = i8 * 8 + xcd;
    const bool has_mod = wi < 48, has_up = wi >= 21;
    const int cb0 = (i8 + P.C0) & 7, cb1 = (i8 + P.C1) & 7, ms = wi - 20;
    const int mi = wi < 8 ? wi : (wi < 16 ? 8 + ((xcd + 4) & 7) : (wi < 20 ? (wi - 14) * 8 + 3 : 16 + ms / 7 * 8 + ms % 7 + (ms % 7 >= 3 ? 1 : 0)));
    const int ui = xcd * 5 + (xcd > 5 ? xcd - 5 : 0) + (i8 == 6 ? 0 : (xcd >= 5 ? 1 : 0) + (i8 == 7 ? 4 : i8 - 2));
    const bool edge = i8 >= 6;
    const int wd = cb0 == 3 ? 0 : P.WD;
    const int e[4] = {P.E0 - 4 * wd + (cb0 == 3 ? P.ES : 0), P.E1 + (cb1 == 3 ? P.ES : 0), has_mod ? (wi < 16 ? P.E2C : P.E2) + ((mi & 7) == 3 ? P.ES : 0) : 0, has_up ? P.EU + (edge ? P.TC : 0) : 0};
    int tw[4] = {}, win[4] = {}, span[4] = {}, k0[4] = {};
    for (int k = 0; k < 4; ++k) {
      const int np = k == 0 ? 8 - wd : 8;
      tw[k] = np ? e[k] * P.TW / (16 * np) : 0;
      k0[k] = P.NF + tw[k] * np / 2 > P.TK ? P.NF + tw[k] * np / 2 : P.TK;
      span[k] = P.TEND - k0[k] - P.TG - (k == 0 ? (wd ? P.DT_WO : P.A0) : (k == 3 && has_up ? P.DT_UP : 0));
      win[k] = span[k] - (k == 3 && has_up ? P.GA + (edge ? P.LM : 0) : 0);
    }
    int n[4] = {NCMIN / 8, NCMIN / 8, NCMIN / 8, NCMIN / 8};
    for (int left = 128 - NCMIN / 2; left > 0; --left) {
      int best = -1;
      for (int k = 3; k >= 0; --k)
        if (n[k] < NL && (best < 0 || n[k] * win[best] < n[best] * win[k])) best = k;
      ++n[best];
    }
    const int c[4] = {0, n[0] - 1, n[0] + n[1] - 2, n[0] + n[1] + n[2] - 3};
    const int nl = n[part];
    const bool has_w = part < 2 || (part == 2 ? has_mod : has_up);
    const int blk = !has_w ? 0 : (part == 0 ? cb0 : (part == 1 ? xcd * 8 + cb1 : (part == 2 ? mi : ui)));
    T.e[2 * bid] = (u32)(c[part] | nl << 7 | blk << 13 | (has_w ? 1 : 0) << 19 | (part == 0 ? wd : 0) << 20) | (u32)tw[part] << 24;
    T.e[2 * bid + 1] = (u32)(span[part] * P.TS / (4 * (nl - 1)) | span[part] * (1024 - P.TS) / (4 * (nl - 1)) << 16);
  }
  return T;
}
constexpr int step_range(const Prm& P, bool mx_) {
  int mx = 0, mn = 1 << 20;
  const Tab T = make_tab(P);
  for (int bid = 0; bid < 256; ++bid) { const int n = T.e[2 * bid] >> 7 & 63; mx = n > mx ? n : mx; mn = n < mn ? n : mn; }
  return mx_ ? mx : mn;
}
extern "C" __attribute__((used, visibility("protected"))) __constant__ const Tab wg_tab = make_tab(PRM0);
static_assert(step_range(PRM0, true) <= NL && step_range(PRM0, false) >= NCMIN / 8, "a wave's key range does not fit its load steps");
static_assert(UPREP + 1 < SK0 && QP + QD + 1 < SK0 && cons_end(SK0 - 1) == SK0 - WV && NL - SK1 == WV, "steps every wave runs");
constexpr int NUP = FF / 32;
constexpr int SPIN_MAX = 1 << 17;

constexpr u32 WS_EPOCH = 0;
constexpr u32 G_TB = 1024;
constexpr u32 G_MOD = 4096;
constexpr u32 G_Q = G_MOD + 3072 * 8;
constexpr u32 G_ML = G_Q + 4096 * 8;
constexpr u32 G_U = G_ML + 256 * 16;
constexpr u32 G_P = 0;
constexpr u32 G_XP = G_P + 256 * 256;
constexpr u32 G_OP = G_XP + 8 * 512 * 4;
constexpr u32 G_NT = G_OP + 43 * 512 * 8;
constexpr u32 G_PB = G_OP + 44 * 512 * 8;
constexpr u32 G_VT = G_U + 2 * (G_PB + 8192);
constexpr u32 WS_END = G_VT + 2048;
__device__ __forceinline__ u32 dup(u32 o) { return G_U + ((o >> 13) << 14) + (o & 8191u); }
constexpr u32 SENT = 0xffffffffu;
constexpr u32 OOB = 0x40000000u;

constexpr int AUX_NT = 2, AUX_SC1 = 16;

__device__ __forceinline__ float lo_f(u32 v) { return __uint_as_float(v << 16); }
__device__ __forceinline__ float hi_f(u32 v) { return __uint_as_float(v & 0xffff0000u); }
__device__ __forceinline__ float rbf(float x) { return (float)(__bf16)x; }
__device__ __forceinline__ u32 pack_bf(float lo, float hi) {
  bf16x2 r; r[0] = (__bf16)lo; r[1] = (__bf16)hi; return __builtin_bit_cast(u32, r);
}
__device__ __forceinline__ float dot2(u32 k, u32 q, float acc) {
  return __builtin_amdgcn_fdot2_f32_bf16(__builtin_bit_cast(bf16x2, k), __builtin_bit_cast(bf16x2, q), acc, false);
}
template <int CTRL> __device__ __forceinline__ float dpp(float x) {
  return __builtin_bit_cast(float, __builtin_amdgcn_mov_dpp(__builtin_bit_cast(int, x), CTRL, 0xf, 0xf, true));
}
constexpr int XOR1 = 0xB1, XOR2 = 0x4E, XOR7 = 0x141, XOR8 = 0x128;
__device__ __forceinline__ float swap32_add(float a, float b) {
  auto t = __builtin_amdgcn_permlane32_swap(__float_as_uint(a), __float_as_uint(b), false, false);
  return __uint_as_float(t[0]) + __uint_as_float(t[1]);
}
__device__ __forceinline__ float swap16_add(float a, float b) {
  auto t = __builtin_amdgcn_permlane16_swap(__float_as_uint(a), __float_as_uint(b), false, false);
  return __uint_as_float(t[0]) + __uint_as_float(t[1]);
}
__device__ __forceinline__ float wave_sum(float x) {
  x += dpp<XOR1>(x); x += dpp<XOR2>(x); x += dpp<XOR7>(x); x += dpp<XOR8>(x);
  x = swap16_add(x, x);
  return swap32_add(x, x);
}
__device__ __forceinline__ float wave_max(float x) {
  x = fmaxf(x, dpp<XOR1>(x)); x = fmaxf(x, dpp<XOR2>(x)); x = fmaxf(x, dpp<XOR7>(x)); x = fmaxf(x, dpp<XOR8>(x));
  auto s = __builtin_amdgcn_permlane16_swap(__float_as_uint(x), __float_as_uint(x), false, false);
  x = fmaxf(__uint_as_float(s[0]), __uint_as_float(s[1]));
  auto t = __builtin_amdgcn_permlane32_swap(__float_as_uint(x), __float_as_uint(x), false, false);
  return fmaxf(__uint_as_float(t[0]), __uint_as_float(t[1]));
}
template <int CTRL> __device__ __forceinline__ float sel_add(float lo, float hi, bool up) {
  const float t1 = lo + dpp<CTRL>(lo), t2 = hi + dpp<CTRL>(hi);
  return up ? t2 : t1;
}
template <int I> __device__ __forceinline__ float bc16(float x) {
  return __builtin_bit_cast(float, __builtin_amdgcn_update_dpp(0, __builtin_bit_cast(int, x), 0x150 + I, 0xf, 0xf, false));
}
__device__ __forceinline__ float bcast16(float x, int i) {
  switch (i) {
    case 0: return bc16<0>(x); case 1: return bc16<1>(x); case 2: return bc16<2>(x); case 3: return bc16<3>(x);
    case 4: return bc16<4>(x); case 5: return bc16<5>(x); case 6: return bc16<6>(x); case 7: return bc16<7>(x);
    case 8: return bc16<8>(x); case 9: return bc16<9>(x); case 10: return bc16<10>(x); case 11: return bc16<11>(x);
    case 12: return bc16<12>(x); case 13: return bc16<13>(x); case 14: return bc16<14>(x); default: return bc16<15>(x);
  }
}
__device__ __forceinline__ float silu_f(float x) { return x * __builtin_amdgcn_rcpf(1.f + __expf(-x)); }

template <int AUX> __device__ __forceinline__ u32x4 ld16(rsrc_t r, u32 voff, u32 soff) {
  return __builtin_amdgcn_raw_buffer_load_b128(r, voff, soff, AUX);
}
template <int AUX> __device__ __forceinline__ u32x2 ld8(rsrc_t r, u32 voff, u32 soff) {
  return __builtin_amdgcn_raw_buffer_load_b64(r, voff, soff, AUX);
}
__device__ __forceinline__ void st8_sc1(rsrc_t r, u32 voff, u32 v, u32 tag) {
  __builtin_amdgcn_raw_buffer_store_b64(u32x2{v, tag}, r, voff, 0, AUX_SC1);
}
__device__ __forceinline__ void st16_sc1(rsrc_t r, u32 voff, u32 v0, u32 v1, u32 tag) {
  __builtin_amdgcn_raw_buffer_store_b128(u32x4{v0, tag, v1, tag}, r, voff, 0, AUX_SC1);
}
__device__ __forceinline__ rsrc_t mk_rsrc(const void* p, u32 bytes) {
  return __builtin_amdgcn_make_buffer_rsrc(const_cast<void*>(p), 0, bytes, 0x00020000);
}
#define WAIT_VM(n) __builtin_amdgcn_s_waitcnt(((n) & 15) | (((n) >> 4) << 14) | (7 << 4) | (15 << 8))
#define SB() __builtin_amdgcn_sched_barrier(0)
#define CFENCE() asm volatile("" ::: "memory")
__device__ __forceinline__ bool all_ok(bool ok) { return __builtin_amdgcn_ballot_w64(!ok) == 0ull; }

template <int N> __device__ __forceinline__ void gather16(rsrc_t ws, u32 voff, u32 stride, u32 tag, u32x4 (&g)[N]) {
#pragma nounroll
  for (int spin = 0; spin < SPIN_MAX; ++spin) {
    CFENCE();
    bool ok = true;
#pragma unroll
    for (int i = 0; i < N; ++i) g[i] = ld16<AUX_SC1>(ws, voff + i * stride, 0);
#pragma unroll
    for (int i = 0; i < N; ++i) ok &= (g[i][1] == tag) & (g[i][3] == tag);
    if (all_ok(ok)) break;
    __builtin_amdgcn_s_sleep(8);
  }
}
template <int N> __device__ __forceinline__ void gather8(rsrc_t ws, u32 voff, u32 stride, u32 tag, u32 (&g)[N], bool act = true) {
#pragma nounroll
  for (int spin = 0; spin < SPIN_MAX; ++spin) {
    CFENCE();
    bool ok = true;
    u32x2 t[N];
#pragma unroll
    for (int i = 0; i < N; ++i) t[i] = ld8<AUX_SC1>(ws, voff + i * stride, 0);
#pragma unroll
    for (int i = 0; i < N; ++i) { ok &= (t[i][1] == tag); g[i] = t[i][0]; }
    if (all_ok(ok | !act)) break;
    __builtin_amdgcn_s_sleep(8);
  }
}

struct Smem {
  float red[8][128];
  float ml[8][2];
  float r2[64 * 20];
  u16 hb[3 * 1024];
  float stat[8][64];
  float red2[8][128];
  u32 dummy[64];
  u32x2 bc, bc2;
  u32 bc3, bc4;
  u32x4 zero[4];
  float pl[4][8];
  u32 mflag[8];
  u32x4 wbuf[8][8][64];
  __attribute__((aligned(256))) u16 w2b[32][512];
  u16 actl[2][32];
};

#define PIN(x) asm volatile("" : "+v"(x))
#define LDS_BARRIER() do { __builtin_amdgcn_s_waitcnt(0xc07f); __builtin_amdgcn_s_barrier(); CFENCE(); } while (0)
#define WAVE_SYNC() do { __builtin_amdgcn_fence(__ATOMIC_RELEASE, "wavefront"); __builtin_amdgcn_wave_barrier(); __builtin_amdgcn_fence(__ATOMIC_ACQUIRE, "wavefront"); } while (0)

__device__ __forceinline__ void dma16(rsrc_t r, u32 voff, u32 soff, u32 lds_dst) {
  u32 keep;
  asm volatile("s_mov_b32 %0, m0\n\ts_mov_b32 m0, %4\n\ts_nop 4\n\tbuffer_load_dwordx4 %1, %2, %3 offen nt lds\n\ts_mov_b32 m0, %0"
               : "=&s"(keep) : "v"(voff), "s"(r), "s"(soff), "s"(lds_dst) : "memory");
}
__device__ __forceinline__ void ld16_into(u32x4& d, rsrc_t r, u32 voff, u32 soff) {
  asm volatile("s_nop 4\n\tbuffer_load_dwordx4 %0, %1, %2, %3 offen nt" : "+v"(d) : "v"(voff), "s"(r), "s"(soff) : "memory");
}
__device__ __forceinline__ u32 perm_lo(u32 a, u32 b) { return __builtin_amdgcn_perm(b, a, 0x05040100u); }
__device__ __forceinline__ u32 perm_hi(u32 a, u32 b) { return __builtin_amdgcn_perm(b, a, 0x07060302u); }
__device__ __forceinline__ float rdl(float x, int l) { return __int_as_float(__builtin_amdgcn_readlane(__float_as_int(x), l)); }

typedef __attribute__((ext_vector_type(8))) __bf16 bf16x8;
__device__ __forceinline__ f32x4 mfma16(u32x4 a, u32x4 b, f32x4 c) {
  return __builtin_amdgcn_mfma_f32_16x16x32_bf16(__builtin_bit_cast(bf16x8, a), __builtin_bit_cast(bf16x8, b), c, 0, 0, 0);
}
__device__ __forceinline__ float sum8(const float* p) {
  const f32x4 a = *reinterpret_cast<const f32x4*>(p), b = *reinterpret_cast<const f32x4*>(p + 4);
  return ((a[0] + b[0]) + (a[1] + b[1])) + ((a[2] + b[2]) + (a[3] + b[3]));
}
__device__ __forceinline__ float ln_ri(float var) {
  return __builtin_amdgcn_rcpf(rbf(__builtin_amdgcn_sqrtf(rbf(rbf(fmaxf(var, 0.f)) + 1e-5f))));
}
__device__ __forceinline__ void ln_stats2(const float (&x0)[8], const float (&x1)[8], float& mb0, float& ri0, float& mb1, float& ri1) {
  const float k0 = rdl(x0[0], 0), k1 = rdl(x1[0], 0);
  float s0 = 0.f, s1 = 0.f, q0 = 0.f, q1 = 0.f;
#pragma unroll
  for (int k = 0; k < 8; ++k) {
    const float d0 = x0[k] - k0, d1 = x1[k] - k1;
    s0 += d0; s1 += d1; q0 = fmaf(d0, d0, q0); q1 = fmaf(d1, d1, q1);
  }
  float t = swap16_add(swap32_add(s0, s1), swap32_add(q0, q1));
  t += dpp<XOR1>(t); t += dpp<XOR2>(t); t += dpp<XOR7>(t); t += dpp<XOR8>(t);
  auto u = __builtin_amdgcn_permlane16_swap(__float_as_uint(t), __float_as_uint(t), false, false);
  const float ms = __uint_as_float(u[0]) * (1.f / 512.f), mq = __uint_as_float(u[1]) * (1.f / 512.f);
  const float ri = ln_ri(fmaf(-ms, ms, mq));
  mb0 = rbf(k0 + rdl(ms, 0)); mb1 = rbf(k1 + rdl(ms, 32));
  ri0 = rdl(ri, 0); ri1 = rdl(ri, 32);
}
template <int I> struct IC { static constexpr int v = I; };
template <int I, int N, class F> __device__ __forceinline__ void sfor(F&& f) {
  if constexpr (I < N) { if (f(IC<I>{})) sfor<I + 1, N>(f); }
}
typedef __attribute__((ext_vector_type(4))) short s16x4;
typedef __attribute__((address_space(3))) s16x4* lds_s16x4p;
__device__ __forceinline__ u32x2 tr_read(u32 lds_addr) {
  return __builtin_bit_cast(u32x2, __builtin_amdgcn_ds_read_tr16_b64_v4i16((lds_s16x4p)lds_addr));
}
__device__ __forceinline__ u32 next_tag(u32 e) { return ((e | 0x40004000u) + 1u) & ~0x40004000u; }
__device__ __forceinline__ float u2f(u32 v) { return __uint_as_float(v); }
#define LDSP(T, a) ((__attribute__((address_space(3))) T*)(a))
template <class T> __device__ __forceinline__ u32 lds_addr(T* p) { u32 a = (u32)(uintptr_t)p; PIN(a); return a; }

#define TAB_LOAD "s_add_u32 s30, s28, wg_tab@rel32@lo+4\ns_addc_u32 s31, s29, wg_tab@rel32@hi+12\nglobal_load_dwordx2 %[te], %[va], s[30:31]\n"
#define FC(k) ".Lfc" #k
#define FX(r, bit, k) "s_bitcmp1_b32 %[" #r "], " #bit "\ns_cselect_b32 %[sb], " FC(k) ", 0\ns_xor_b32 %[sa], %[sa], %[sb]\n"
__global__ void __launch_bounds__(512, 2) fused_kernel(const u16* kc_, const u16* vc_, const u16* wo_, const u16* wq_, const u16* wmod_, const u16* w1_, const u16* w3_,
                                                       unsigned char* ws_, const u16* x_, const u16* cnd_, const u16* w2_, u16* out_) {
  __shared__ __attribute__((aligned(256))) Smem sm;
  const u64 a_ka = (u64)__builtin_amdgcn_kernarg_segment_ptr();
  const int bid = blockIdx.x, tid = threadIdx.x;
  u64 wa, t64; u32x2 te; int tid0;
  u32 ar[14];
  u32 tch2, sa_, sb_, va_;
  u32x4 kr[NL], vr[NL];
  asm volatile(".set .Lfc0, %0\n.set .Lfc1, %1\n.set .Lfc2, %2\n.set .Lfc3, %3\n.set .Lfc4, %4\n.set .Lfc5, %5\n.set .Lfc6, %6\n.set .Lfc7, %7\n.set .Lfc8, %8\n.set .Lfc9, %9\n.set .Lfc10, %10\n.set .Lfc11, %11"
    :: "n"(first_col(PRM0, 0)), "n"(first_col(PRM0, 1)), "n"(first_col(PRM0, 2)), "n"(first_col(PRM0, 3)), "n"(first_col(PRM0, 4)), "n"(first_col(PRM0, 5)),
       "n"(first_col(PRM0, 6)), "n"(first_col(PRM0, 7)), "n"(first_col(PRM0, 8)), "n"(first_col(PRM0, 9)), "n"(first_col(PRM0, 10)), "n"(first_col(PRM0, 11)));
  asm volatile(
    "s_load_dwordx16 s[4:19], %[ka], 0x0\ns_load_dwordx8 s[20:27], %[ka], 0x40\n"
    "s_memrealtime %[t]\n"
    "v_lshlrev_b32 %[va], 3, %[bid]\n"
    "s_getpc_b64 s[28:29]\n.Lcpf_first:\n"
    TAB_LOAD
    "s_cmp_gt_u32 %[bid], 15\ns_cbranch_scc1 .Lno_cpf\n"
    "s_mov_b32 %[sa], .Lend_a-.Lcpf_first\ns_max_i32 %[sa], %[sa], .Lend_b-.Lcpf_first\ns_max_i32 %[sa], %[sa], .Lend_c-.Lcpf_first\ns_max_i32 %[sa], %[sa], .Lend_d-.Lcpf_first\ns_max_i32 %[sa], %[sa], .Lend_e-.Lcpf_first\n"
    "v_lshlrev_b32 %[va], 6, %[tid]\nv_min_u32 %[va], %[sa], %[va]\nglobal_load_dword %[pf], %[va], s[28:29]\n"
    ".Lno_cpf:\n"
    "v_readfirstlane_b32 %[t0], %[tid]\nv_and_b32 %[va], 63, %[tid]\n"
    "s_mov_b32 %[sa], " FC(11) "\n"
    FX(bid, 0, 0) FX(bid, 1, 1) FX(bid, 2, 2) FX(bid, 3, 3) FX(bid, 4, 4) FX(bid, 5, 5) FX(bid, 6, 6) FX(bid, 7, 7) FX(t0, 6, 8) FX(t0, 7, 9) FX(t0, 8, 10)
    "v_lshl_add_u32 %[va], %[va], 4, %[sa]\n"
    "s_waitcnt lgkmcnt(0)\nglobal_load_dwordx4 %[k0], %[va], s[4:5] nt\nglobal_load_dwordx4 %[v0], %[va], s[6:7] nt\n"
    "s_bitcmp1_b32 %[t0], 6\ns_cselect_b64 s[14:15], s[16:17], s[14:15]\n"
    "s_bitcmp1_b32 %[bid], 4\ns_cselect_b64 s[8:9], s[10:11], s[8:9]\ns_cselect_b64 s[12:13], s[14:15], s[12:13]\ns_bitcmp1_b32 %[bid], 5\ns_cselect_b64 %[wa], s[12:13], s[8:9]"
    : "=&{s4}"(ar[0]), "=&{s5}"(ar[1]), "=&{s6}"(ar[2]), "=&{s7}"(ar[3]), "=&{s18}"(ar[4]), "=&{s19}"(ar[5]), "=&{s20}"(ar[6]), "=&{s21}"(ar[7]), "=&{s22}"(ar[8]), "=&{s23}"(ar[9]),
      "=&{s24}"(ar[10]), "=&{s25}"(ar[11]), "=&{s26}"(ar[12]), "=&{s27}"(ar[13]), [wa] "=&s"(wa), [t0] "=&s"(tid0), [t] "=&s"(t64), [te] "=&v"(te), [sa] "=&s"(sa_), [sb] "=&s"(sb_),
      [va] "=&v"(va_), [pf] "=&v"(tch2), [k0] "=&v"(kr[0]), [v0] "=&v"(vr[0])
    : [ka] "s"(a_ka), [bid] "s"(bid), [tid] "v"(tid) : "s8", "s9", "s10", "s11", "s12", "s13", "s14", "s15", "s16", "s17", "s28", "s29", "s30", "s31", "scc", "memory");
  asm volatile("" : "+s"(ar[0]), "+s"(ar[1]), "+s"(ar[2]), "+s"(ar[3]), "+s"(ar[4]), "+s"(ar[5]), "+s"(ar[6]), "+s"(ar[7]), "+s"(ar[8]), "+s"(ar[9]), "+s"(ar[10]), "+s"(ar[11]), "+s"(ar[12]), "+s"(ar[13]));
  asm volatile("" : "+s"(wa), "+s"(t64), "+s"(tid0), "+s"(sa_));
  const u32 tka = (u32)wall_clock64();
  const u32 t0 = tka;
  __builtin_assume(tid >= 0 && tid < 512 && tid0 >= 0 && tid0 < 512 && bid >= 0 && bid < 256);
  constexpr Prm P = PRM0;
  const int TEND = P.TEND;
  const int xcd = bid & 7, part = bid >> 4 & 3;
  const int lane = tid & 63, wave = tid0 >> 6;
  SB();
  const bool p3 = part == 3;
  u32 pitch = (u32)(0x005600c001000020ull >> (part * 16) & 0xffff) * 32u;
  const int sh = p3 ? 2 : 3;
  u32 wv0 = (u32)((p3 ? (wave >> 1) * 128 : wave * 64) + (part == 0 ? xcd * 512 : 0) + (lane >> sh)) * pitch + ((lane & (p3 ? 3 : 7)) ^ (lane >> (p3 ? 5 : 3))) * 16u;
  const u32 wimg = (u32)(uintptr_t)(&sm.wbuf[0][0][0]);
  u32 wsz = (part == 0 ? 4096u : 512u) * pitch, wld = wimg + (p3 ? (wave & 1) * 32768 + (wave >> 1) * 8192 : wave * 8192);
  PIN(wv0);
  asm volatile("" : "+s"(wsz), "+s"(wld), "+s"(pitch));
  auto karg = [&](int i) { return ar[2 * i] | (u64)ar[2 * i + 1] << 32; };
  const u64 a_kc = karg(0), a_vc = karg(1);
  unsigned char* ws = (unsigned char*)karg(2);
  const u16 *x = (const u16*)karg(3), *cnd = (const u16*)karg(4), *w2 = (const u16*)karg(5);
  u16* out = (u16*)karg(6);
  const rsrc_t wsr = mk_rsrc(ws, WS_END);
  asm volatile("s_waitcnt vmcnt(2)" : "+v"(te));
  const u32 te0 = __builtin_amdgcn_readfirstlane(te[0]), te1 = __builtin_amdgcn_readfirstlane(te[1]);
  const int bl0 = te0 & 127, nl = te0 >> 7 & 63, blk = te0 >> 13 & 63;
  const bool has_w = te0 >> 19 & 1;
  const rsrc_t rw = mk_rsrc((const void*)wa, wsz);
  const u32 wstep = pitch << (6 - sh), wflip = p3 ? 32u : 0u;
  u32 wso = blk << (sh + 4);
  int wleft = 8;
  auto piece = [&]() {
    dma16(rw, wv0, wso, wld);
    wso += wstep; wld += 1024;
    wv0 ^= (wleft & 1) ? wflip : 0u;
    --wleft;
  };
  SB();
  const int b = bid >> 3 & 1, head = xcd * 4 + (bid >> 6 ^ 3);
  const int g = lane >> 4, c = lane & 15;
  const int i8 = b | bid >> 6 << 1, wi = i8 * 8 + xcd;
  const bool is_up = p3 && has_w, modc = part == 2 && wi < 16;
  const size_t base = ((size_t)(b * NH + head) * SK + (SK - WIN)) * DH;
  const rsrc_t rk = mk_rsrc((const u16*)a_kc + base, WIN * DH * 2), rv = mk_rsrc((const u16*)a_vc + base, WIN * DH * 2);
  int tau = te1 & 0xffff;
  const int tauw = te0 >> 24 << 7, wdef = te0 >> 20 & 15;
  int due = P.NF * 256, now = 0;
  auto gate = [&]() {
    while (now < due) { now = (int)(((u32)wall_clock64() - t0) << 8); if (now < due) __builtin_amdgcn_s_sleep(1); }
    due += tau;
  };
  auto until = [&](int t) { while ((int)(((u32)wall_clock64() - t0) << 8) < t); };
  const u32 pslot = dup(G_P + head * 2048 + (b * 4 + part) * 16);
  const u32 die = __builtin_amdgcn_s_getreg(20 | (2 << 6));
  const bool voter = p3 && i8 == 1, timer = p3 && i8 < 2;
  int vote = 0, lag = 0, shift = 0;
  const u32 pbo = dup(G_PB) + (xcd * 8 + wave) * 128;
  u32 near = 0;
  u32x2 ep; u32 xs0, xs1, tch1; u32x4 xr0, xr1;
  const rsrc_t rx = mk_rsrc(part == 2 ? cnd : x, (part == 1 || (part == 2 && has_w)) ? 2048u : 0u);
  auto small_ops = [&]() {
    ep = ld8<AUX_SC1>(wsr, wave == 7 ? WS_EPOCH : OOB, 0);
    xs0 = __builtin_amdgcn_raw_buffer_load_b16(rx, tid * 2, 0, 0); xs1 = __builtin_amdgcn_raw_buffer_load_b16(rx, tid * 2, 1024, 0);
    xr0 = ld16<0>(rx, part == 1 ? lane * 16 : OOB, 0); xr1 = ld16<0>(rx, part == 1 ? lane * 16 : OOB, 1024);
    tch1 = __builtin_amdgcn_raw_buffer_load_b32(mk_rsrc(w2 + (size_t)blk * 32 * 512, is_up ? 64u : 0u), tid == 384 ? 0 : OOB, 0, AUX_NT);
  };
  if (has_w) {
    if (modc) now = 1 << 30;
#pragma nounroll
    while (wleft > wdef) { gate(); due += tauw - tau; piece(); }
    now = 0;
  }
  due = max(due, P.TK * 256);
  SB();
  u32 sot = sa_ & 0xffc00u;
  {
    const u32 s1 = (bl0 + lane - 1 - (lane >= SK0 ? NL - nl : 0)) ^ (124 + part);
#pragma unroll
    for (int k = 0; k < 7; ++k) sot ^= (u32)-(int)(s1 >> k & 1u) & col(P, k);
    PIN(sot);
  }
  u32 tag = 0;
  u32x4 bf[8];
  auto wo_prep = [&](int s0 = 0, int s1 = 2) {
    const u32 wi_ = wimg + wave * 8192;
    const int q = (lane >> 2) & 3, pp = lane & 3;
#pragma unroll
    for (int s = s0; s < s1; ++s) {
#pragma unroll
      for (int t = 0; t < 4; ++t) {
        const int r0 = s * 32 + g * 8 + q, r1 = r0 + 4, ch = t * 2 + (pp >> 1);
        const u32x2 lo = tr_read(wi_ + r0 * 128 + ((ch ^ (r0 & 7)) * 16) + (pp & 1) * 8), hi = tr_read(wi_ + r1 * 128 + ((ch ^ (r1 & 7)) * 16) + (pp & 1) * 8);
        bf[s * 4 + t] = u32x4{lo[0], lo[1], hi[0], hi[1]};
      }
    }
  };
  auto gemv_go = [&](u32 hh) {
    sm.hb[tid] = (u16)hh; sm.hb[512 + tid] = (u16)(hh >> 16);
    WAVE_SYNC();
    const u32x4* a = (c & 11) == 0 ? reinterpret_cast<const u32x4*>(&sm.hb[(c >> 2) * 512 + wave * 64 + g * 8]) : &sm.zero[0];
    const u32x4 a0 = a[0], a1 = a[(c & 11) == 0 ? 4 : 0];
    const f32x4 z = {0.f, 0.f, 0.f, 0.f};
    f32x4 d[4];
#pragma unroll
    for (int t = 0; t < 4; ++t) d[t] = mfma16(a0, bf[t], z);
#pragma unroll
    for (int t = 0; t < 4; ++t) d[t] = mfma16(a1, bf[4 + t], d[t]);
    if (lane < 32) {
#pragma unroll
      for (int t = 0; t < 4; ++t) sm.r2[(t * 16 + c) * 20 + g * 8 + wave] = d[t][0];
    }
  };
  auto mod_stage = [&]() {
    wo_prep();
    gemv_go(pack_bf(silu_f(lo_f(xs0)), silu_f(lo_f(xs1))));
    __builtin_amdgcn_s_waitcnt(0xc07f);
    if (lane == 0) *LDSP(volatile u32, (u32)(uintptr_t)&sm.mflag[wave]) = 1u;
    if (wave == 7) {
#pragma nounroll
      for (int spin = 0; spin < SPIN_MAX; ++spin) {
        const u32 f = *LDSP(volatile u32, (u32)(uintptr_t)&sm.mflag[lane & 7]);
        if (all_ok(f != 0u)) break;
        __builtin_amdgcn_s_sleep(1);
      }
      CFENCE();
      st8_sc1(wsr, G_MOD + (blk * 64 + lane) * 8, pack_bf(sum8(&sm.r2[lane * 20]), sum8(&sm.r2[lane * 20 + 8])), tag);
    }
  };
  if (lane < 4) sm.zero[lane] = u32x4{0u, 0u, 0u, 0u};
  if (part == 2 && has_w) {
    if (tid < 8) sm.mflag[tid] = 0u;
    LDS_BARRIER();
    if (__builtin_expect(modc, 0)) {
      small_ops();
      __builtin_amdgcn_raw_buffer_store_b128(u32x4{SENT, SENT, SENT, SENT}, wsr, G_U + wi * 10240 + tid * 16, 0, AUX_SC1);
      __builtin_amdgcn_raw_buffer_store_b128(u32x4{SENT, SENT, SENT, SENT}, wsr, tid < 128 ? G_U + wi * 10240 + tid * 16 : OOB, 8192, AUX_SC1);
      asm volatile("s_waitcnt vmcnt(0)" ::: "memory");
      tag = next_tag(__builtin_amdgcn_readfirstlane(ep[0]));
      mod_stage();
      __builtin_amdgcn_s_barrier();
    }
  }
  SB();
  const int fd = wi * 8 + (lane & 3) * 2;
  const int ub = wave >> 2, ud = (wave & 3) * 128 + c * 8 + g * 2;
  u32x4 sg[3]; u32 sx2;
  auto side_loads = [&]() {
    const bool fin = part == 1 && wave == 0;
    sg[0] = ld16<AUX_SC1>(wsr, is_up ? G_MOD + (1024 + ud) * 8 : (fin ? G_MOD + (1024 + fd) * 8 : OOB), 0);
    sg[1] = ld16<AUX_SC1>(wsr, is_up ? G_MOD + (1536 + ud) * 8 : (fin ? G_MOD + (2560 + fd) * 8 : OOB), 0);
    sg[2] = ld16<AUX_SC1>(wsr, is_up ? G_MOD + (2048 + ud) * 8 : OOB, 0);
    sx2 = __builtin_amdgcn_raw_buffer_load_b32(mk_rsrc(x, (is_up || fin) ? 2048u : 0u), is_up ? ub * 1024 + ud * 2 : (g & 1) * 1024 + fd * 2, 0, 0);
  };
  auto side_pin = [&]() { PIN(sg[0]); PIN(sg[1]); PIN(sg[2]); PIN(sx2); };
  const u32 mlo = G_ML + (head * 2 + b) * 64 + (lane & 3) * 16;
  float fmrg = 0.f;
  auto merge_factor = [&](const u32x4& ml) {
    const float mi = u2f(ml[0]);
    float M = fmaxf(mi, dpp<XOR1>(mi)); M = fmaxf(M, dpp<XOR2>(M));
    const float e = __expf(mi - M);
    float L = e * u2f(ml[2]);
    L += dpp<XOR1>(L); L += dpp<XOR2>(L);
    fmrg = rdl(e * __builtin_amdgcn_rcpf(L), part);
  };
  f32x2 ga = {0.f, 0.f}, xin = {0.f, 0.f}, cc1 = {0.f, 0.f};
  float Af = 0.f, Bf = 0.f;
  const int hpos = ub * 512 + ud;
  auto up_prep = [&]() {
    if (!all_ok((sg[0][1] == tag) & (sg[0][3] == tag) & (sg[1][1] == tag) & (sg[1][3] == tag) & (sg[2][1] == tag) & (sg[2][3] == tag))) gather16<3>(wsr, G_MOD + (1024 + ud) * 8, 512 * 8, tag, sg);
    ga = f32x2{ub ? hi_f(sg[0][0]) : lo_f(sg[0][0]), ub ? hi_f(sg[0][2]) : lo_f(sg[0][2])};
    cc1 = f32x2{rbf(1.f + (ub ? hi_f(sg[2][0]) : lo_f(sg[2][0]))), rbf(1.f + (ub ? hi_f(sg[2][2]) : lo_f(sg[2][2])))};
    xin = f32x2{lo_f(sx2), hi_f(sx2)};
    u32* hp = reinterpret_cast<u32*>(&sm.hb[hpos]);
    hp[512] = pack_bf(cc1[0], cc1[1]);
    hp[1024] = ub ? perm_hi(sg[1][0], sg[1][2]) : perm_lo(sg[1][0], sg[1][2]);
  };
  auto up_frags = [&]() {
    const int pp = lane & 3;
    const u32 a = wimg + (pp & 1) * 32768 + ((pp >> 1) * 32 + g * 8 + ((lane >> 2) & 3)) * 64 + (((wave >> 1) ^ ((g & 1) | (pp & 2))) * 16) + (wave & 1) * 8;
#pragma unroll
    for (int rb = 0; rb < 8; ++rb) {
      const u32x2 lo = tr_read(a + rb * 4096), hi = tr_read(a + rb * 4096 + 256);
      bf[rb] = u32x4{lo[0], lo[1], hi[0], hi[1]};
    }
  };
  const u32x4* ap = reinterpret_cast<const u32x4*>(&sm.hb[((lane >> 3) & 1) * 512 + (lane & 1) * 32 + g * 8]);
  const bool par = lane & 8;
  auto limit = [&](auto sc) {
    constexpr int s = sc.v;
    WAIT_VM(window(s, 1 + (PRM0.W[s >> 3] >> (4 * (s & 7)) & 7)));
    SB();
  };
  auto sample = [&]() { if (timer) lag += (int)(((u32)wall_clock64() - t0) << 8) - due + ((int)__builtin_amdgcn_s_getreg(7 | (3 << 11)) + 1) * tau; };
  auto report = [&](u32 dst) {
    if (timer) {
      if (lane == 0) sm.mflag[wave] = vote * 65536 + min(lag >> 8, 4000);
      lag = 0;
      LDS_BARRIER();
      if (wave == 7) {
        int s = sm.mflag[lane & 7];
        s += __builtin_amdgcn_mov_dpp(s, XOR1, 0xf, 0xf, true); s += __builtin_amdgcn_mov_dpp(s, XOR2, 0xf, 0xf, true); s += __builtin_amdgcn_mov_dpp(s, XOR7, 0xf, 0xf, true);
        if (lane < 8) st8_sc1(wsr, dst + lane * 128 + wi * 8, s, tag);
      }
    }
    SB();
  };
  float l0 = 0.f, l1 = 0.f;
  u32x2 gmd[2], tbg;
  int t1 = 0;
#pragma unroll
  for (int i = SK0; i < SK1; ++i) kr[i] = u32x4{0u, 0u, 0u, 0u};
  const int first = SK0 + NL - nl;
  auto left_out = [&](int i) { int f = first; asm volatile("" : "+s"(f)); return i < f; };
  sfor<1, NL>([&](auto ic) {
    constexpr int i = ic.v;
    if constexpr (i >= SK0 && i < SK1) { if (left_out(i)) return true; }
    const u32 so = __builtin_amdgcn_readlane(sot, i);
    gate();
    if constexpr ((i >= TBS && i <= TBP) || (i >= RPS && i <= RPP)) sample();
    SB();
    if constexpr (i >= SK0 && i < SK1) ld16_into(kr[i], rk, lane * 16, so); else kr[i] = ld16<AUX_NT>(rk, lane * 16, so);
    if constexpr (i == SMO) small_ops();
    if constexpr (i == SMO + 1) { if (voter) __builtin_amdgcn_raw_buffer_store_b32(0u, wsr, pbo + (lane >> 5) * 8192 + (lane & 31) * 4, 0, AUX_SC1); }
    if constexpr (i == SK1) tbg = ld8<AUX_SC1>(wsr, wave == 7 ? G_VT + xcd * 128 + c * 8 : OOB, 0);
    if constexpr (i == TBL) tbg = ld8<AUX_SC1>(wsr, wave == 7 ? G_TB + xcd * 128 + c * 8 : OOB, 0);
    if constexpr (i == QP) { gmd[0] = ld8<AUX_SC1>(wsr, part == 1 ? G_MOD + tid * 8 : OOB, 0); gmd[1] = ld8<AUX_SC1>(wsr, part == 1 ? G_MOD + tid * 8 : OOB, 4096); }
    SB();
    SB();
    if (i >= 4) limit(ic);
    if (i == TBP) {
      if (timer) {
        if (lane == 0) sm.mflag[wave] = lag;
        lag = 0;
        LDS_BARRIER();
        if (wave == 7) {
          int s = sm.mflag[lane & 7];
          s += __builtin_amdgcn_mov_dpp(s, XOR1, 0xf, 0xf, true); s += __builtin_amdgcn_mov_dpp(s, XOR2, 0xf, 0xf, true); s += __builtin_amdgcn_mov_dpp(s, XOR7, 0xf, 0xf, true);
          if (lane < 8) st8_sc1(wsr, G_TB + lane * 128 + wi * 8, t0 << 10 | (u32)min(s / (2048 * (TBP - TBS + 1)), 1023), tag);
        }
      }
      SB();
    }
    if (i == PRB) {
      if (voter) {
        u64 ta, tb, tc; u32 dm;
        const u32 pa = pbo + (wave & 1) * 8192;
        asm volatile("s_memtime %0\n\ts_waitcnt lgkmcnt(0)\n\ts_load_dword %3, %4, %5\n\ts_waitcnt lgkmcnt(0)\n\ts_memtime %1\n\ts_waitcnt lgkmcnt(0)\n\t"
                     "s_load_dword %3, %4, %6\n\ts_waitcnt lgkmcnt(0)\n\ts_memtime %2\n\ts_waitcnt lgkmcnt(0)"
                     : "=&s"(ta), "=&s"(tb), "=&s"(tc), "=&s"(dm) : "s"(ws), "s"(pa), "s"(pa ^ 8192u));
        const int d = min(max((int)(2 * (u32)tb - (u32)ta - (u32)tc), -500), 500);
        vote = ((wave ^ die) & 1) ? -d : d;
      }
      SB();
    }
    if (i == TBU) {
      if (wave == 7) {
        const bool ok = tbg[1] == tag;
        float lf = ok ? (float)min(max((int)((t0 << 10 | 1023u) - tbg[0]) >> 10, -300), 300) : 0.f, nf = ok ? 1.f : 0.f, lg = ok ? (float)(tbg[0] & 1023u) : 0.f;
        lf += dpp<XOR1>(lf); nf += dpp<XOR1>(nf); lg += dpp<XOR1>(lg); lf += dpp<XOR2>(lf); nf += dpp<XOR2>(nf); lg += dpp<XOR2>(lg);
        lf += dpp<XOR7>(lf); nf += dpp<XOR7>(nf); lg += dpp<XOR7>(lg); lf += dpp<XOR8>(lf); nf += dpp<XOR8>(nf); lg += dpp<XOR8>(lg);
        const float rn = __builtin_amdgcn_rcpf(fmaxf(nf, 1.f));
        shift = __builtin_amdgcn_readfirstlane(min(max((int)((lg * rn - (float)P.GS) * (float)P.GB * 0.125f), 0), 150));
        if (lane == 0) sm.bc = u32x2{tag, t0 - (u32)(int)(lf * rn) + shift};
      }
      LDS_BARRIER();
      const u32x2 bc = sm.bc;
      tag = __builtin_amdgcn_readfirstlane(bc[0]);
      t1 = (__builtin_amdgcn_readfirstlane((int)(bc[1] - t0)) + TEND + P.X1) * 256;
      tau -= __builtin_amdgcn_readfirstlane((int)((float)(int)(t0 - bc[1]) * 256.f * __builtin_amdgcn_rcpf((float)(nl - TBU - 1))));
      SB();
    }
    if (i == RPP) report(G_VT);
    if (i == LNS) {
      tag = next_tag(__builtin_amdgcn_readfirstlane(ep[0]));
      if (part == 1) {
        float x0[8], x1[8], mb0, ri0, mb1, ri1;
#pragma unroll
        for (int k = 0; k < 4; ++k) { x0[2 * k] = lo_f(xr0[k]); x0[2 * k + 1] = hi_f(xr0[k]); x1[2 * k] = lo_f(xr1[k]); x1[2 * k + 1] = hi_f(xr1[k]); }
        ln_stats2(x0, x1, mb0, ri0, mb1, ri1);
        l0 = rbf(rbf(lo_f(xs0) - mb0) * ri0); l1 = rbf(rbf(lo_f(xs1) - mb1) * ri1);
      }
      PIN(l0); PIN(l1);
      SB();
    }
    if (i == WUSE) {
      PIN(tch1); PIN(tch2);
      if ((part == 1 || (part == 2 && has_w && !modc))) {
        WAIT_VM(WK); CFENCE();
        if (part == 2) mod_stage(); else wo_prep();
      }
#pragma unroll
      for (int k = 0; k < 8; ++k) PIN(bf[k]);
      SB();
    }
    if (i == QP + QD) {
      if (part == 1) {
        u32 gm[2] = {gmd[0][0], gmd[1][0]};
        if (!all_ok((gmd[0][1] == tag) & (gmd[1][1] == tag))) gather8<2>(wsr, G_MOD + tid * 8, 512 * 8, tag, gm);
        gemv_go(pack_bf(rbf(l0 * rbf(1.f + lo_f(gm[1]))) + lo_f(gm[0]), rbf(l1 * rbf(1.f + hi_f(gm[1]))) + hi_f(gm[0])));
      }
      SB();
    }
    if (i == QP + QD + 1) {
      if (part == 1) {
        LDS_BARRIER();
        if (wave == 0) {
          const int bb = lane >> 5, jq = lane & 31;
          st8_sc1(wsr, G_Q + (bb * 2048 + blk * 32 + jq) * 8, pack_bf(sum8(&sm.r2[2 * jq * 20 + bb * 8]), sum8(&sm.r2[(2 * jq + 1) * 20 + bb * 8])), tag);
        }
      }
      SB();
    }
    return true;
  });
  PIN(sot);
  if (wave == 7) {
    const bool ok = tbg[1] == tag;
    float vt = ok ? (float)((int)tbg[0] >> 16) : 0.f, lg = ok ? (float)(tbg[0] & 0xffffu) : 0.f, nf = ok ? 8.f * (RPP - RPS + 1) : 0.f;
    vt += dpp<XOR1>(vt); lg += dpp<XOR1>(lg); nf += dpp<XOR1>(nf); vt += dpp<XOR2>(vt); lg += dpp<XOR2>(lg); nf += dpp<XOR2>(nf);
    vt += dpp<XOR7>(vt); lg += dpp<XOR7>(lg); nf += dpp<XOR7>(nf); vt += dpp<XOR8>(vt); lg += dpp<XOR8>(lg); nf += dpp<XOR8>(nf);
    lg *= __builtin_amdgcn_rcpf(fmaxf(nf, 1.f));
    if (lane == 0) sm.bc2 = u32x2{(__float_as_uint(vt) & 0x80000000u) | (u32)min(shift * P.DG * 32, P.EW * 256), (u32)min(max((int)((lg - (float)P.B0) * (float)(P.BG * 32)) - shift * P.D0 * 32, 0), 200 * 256)};
  }
  LDS_BARRIER();
  {
    const u32x2 bc = sm.bc2;
    const int st = __builtin_amdgcn_readfirstlane(bc[1]), sp = (st * P.BF) >> 3;
    near = ((__builtin_amdgcn_readfirstlane(bc[0]) >> 31 ^ die ^ P.NR ^ 1) & 1) * 8192u;
    t1 += st + (__builtin_amdgcn_readfirstlane(bc[0]) & 0xffff);
    due += P.TG * 256 + sp;
    tau = (te1 >> 16) + __builtin_amdgcn_readfirstlane((int)((float)(st - sp) * __builtin_amdgcn_rcpf((float)(nl - 3))));
  }
  const u32 qoff = G_Q + ((b * 32 + head) * 64 + c * 4) * 8;
  u32x4 qg[2], mlg;
  u32 qq[4];
  constexpr int NB = (NL + 15) / 16;
  float xlo, sc[NB], pr[NB], m = 0.f;
  f32x4 acc = {0.f, 0.f, 0.f, 0.f};
  u32 pb[NB], pm[4], pl8[8], cpf;
  const u32 adst0 = lds_addr(lane < 32 ? &sm.red[wave][c * 8 + g * 4] : &sm.r2[(lane - 32) * 4]);
  u32 po, rdst, rsrc, xo;
  auto wo_addr = [&]() {
    po = dup(G_P + xcd * 8192) + tid * 16 + near;
    rdst = lane < 32 ? (u32)(uintptr_t)&sm.red2[wave][0] + lane * 16 : (u32)(uintptr_t)&sm.r2[0] + (lane - 32) * 16;
    rsrc = (u32)(uintptr_t)&sm.red2[0][((wave & 1) * 16 + (lane & 15)) * 4 + (lane >> 4)];
    xo = dup(G_XP + xcd * 2048 + (wave & 1) * 1024 + (blk * 64 + (lane & ~1)) * 2) + (lane & 1) * 8192;
    PIN(po); PIN(rdst); PIN(rsrc); PIN(xo);
  };
  u32 pso = (wave == 0 && (g & 1) == 0) ? pslot + (c >> 3) * 1024 + (c & 3) * 256 + (c >> 2 & 1) * 128 + (g >> 1) * 8192 : OOB;
  PIN(pso);
  const u32 nadd = wdef ? P.LW : (is_up ? P.LU : 0);
  u32 nto = tid < 2 ? dup(G_NT) + bid * 8 + tid * 8192 : OOB;
  PIN(nto);
  auto notices = [&]() -> int {
    u32 off = dup(G_NT) + near + lane * 16;
    PIN(off);
    until(due);
    int mx;
#pragma nounroll
    for (int spin = 0; spin < SPIN_MAX; ++spin) {
      CFENCE();
      const u32x4 ga_ = ld16<AUX_SC1>(wsr, off, 0), gb_ = ld16<AUX_SC1>(wsr, off, 1024);
      mx = max(max((int)(ga_[0] - t0), (int)(ga_[2] - t0)), max((int)(gb_[0] - t0), (int)(gb_[2] - t0)));
      if (all_ok((((ga_[1] ^ tag) | (ga_[3] ^ tag)) | ((gb_[1] ^ tag) | (gb_[3] ^ tag))) == 0u)) break;
      for (int k = 0; k < P.AG; ++k) __builtin_amdgcn_s_sleep(1);
    }
    if (all_ok(mx <= (t1 >> 8) - P.AC)) return t1;
    mx = max(mx, __builtin_amdgcn_mov_dpp(mx, XOR1, 0xf, 0xf, true)); mx = max(mx, __builtin_amdgcn_mov_dpp(mx, XOR2, 0xf, 0xf, true));
    mx = max(mx, __builtin_amdgcn_mov_dpp(mx, XOR7, 0xf, 0xf, true)); mx = max(mx, __builtin_amdgcn_mov_dpp(mx, XOR8, 0xf, 0xf, true));
    mx = max(max(__builtin_amdgcn_readlane(mx, 0), __builtin_amdgcn_readlane(mx, 16)), max(__builtin_amdgcn_readlane(mx, 32), __builtin_amdgcn_readlane(mx, 48)));
    return max(min(mx, 1 << 20) * 256, t1 + P.L0 * 256) + P.L1 * 256;
  };
  auto pv_row = [&](auto jc) {
    constexpr int i = jc.v;
    const u32 pv = __float_as_uint(bcast16(u2f(pb[i >> 4]), i & 15));
    acc = mfma16(u32x4{pv & pm[0], pv & pm[1], pv & pm[2], pv & pm[3]}, vr[i], acc);
    PIN(acc);
    return true;
  };
  sfor<0, NL - 1>([&](auto ic) {
    constexpr int v = ic.v;
    if constexpr (v >= SK0 && v < SK1) { if (left_out(v)) return true; }
    if constexpr (v > 0) {
      const u32 so = __builtin_amdgcn_readlane(sot, v);
      if constexpr (v >= SK1) { if (v == NL - P.NE) tau = (tau * P.FE) >> 4; }
      gate();
      if constexpr (v >= RVS && v <= RVP) sample();
      SB();
      if constexpr (v >= SK0 && v < SK1) ld16_into(vr[v], rv, lane * 16, so); else vr[v] = ld16<AUX_NT>(rv, lane * 16, so);
      SB();
      SB();
    }
    if constexpr (v == SK0 - 1) {
#pragma unroll
      for (int i = SK0; i < SK1; ++i) { vr[i] = u32x4{0u, 0u, 0u, 0u}; PIN(vr[i]); }
    }
    if constexpr (v == VQ - 4) { qg[0] = ld16<AUX_SC1>(wsr, qoff, 0); qg[1] = ld16<AUX_SC1>(wsr, qoff + 16, 0); }
    if constexpr (v == VSIDE) side_loads();
    if constexpr (v == CPF) {
      u64 pc; u32 dlt, len;
      asm volatile("s_getpc_b64 %0\n.Lcpf_here:\n\ts_mov_b32 %1, .Ltail_start-.Lcpf_here\n\ts_mov_b32 %2, .Lend_a-.Ltail_start\n\ts_max_i32 %2, %2, .Lend_b-.Ltail_start\n\ts_max_i32 %2, %2, .Lend_c-.Ltail_start\n\ts_max_i32 %2, %2, .Lend_d-.Ltail_start\n\ts_max_i32 %2, %2, .Lend_e-.Ltail_start" : "=s"(pc), "=s"(dlt), "=s"(len) :: "scc");
      pc += dlt;
      const rsrc_t rc = mk_rsrc((const void*)pc, (part == 3 && i8 == 1) ? len : 0u);
      cpf = __builtin_amdgcn_raw_buffer_load_b32(rc, tid * 64, 0, 0);
    }
    if constexpr (v == SK1 + 2) PIN(cpf);
    if constexpr (v == RVP) report(G_VT + 1024);
    if constexpr (v == RVL) tbg = ld8<AUX_SC1>(wsr, wave == 7 ? G_VT + 1024 + xcd * 128 + c * 8 : OOB, 0);
    if constexpr (v == SK1) mlg = ld16<AUX_SC1>(wsr, wave == 0 ? mlo : OOB, 0);
    SB();
    if constexpr (NL + v < 72) limit(IC<NL + v>{});
    if constexpr (v >= SK0 + WV) { WAIT_VM(v < SK1 ? WV : WV + 1); SB(); }
    if constexpr (v == VQ) {
      if (!all_ok((qg[0][1] == tag) & (qg[0][3] == tag) & (qg[1][1] == tag) & (qg[1][3] == tag))) gather16<2>(wsr, qoff, 16, tag, qg);
      qq[0] = qg[0][0]; qq[1] = qg[0][2]; qq[2] = qg[1][0]; qq[3] = qg[1][2];
    }
    if constexpr (v >= VQ && v < VS) {
      constexpr int sl = v - VQ, bt = sl >> 1;
      float xs[8];
#pragma unroll
      for (int i = 0; i < 8; ++i) {
        float d = 0.f;
        if (sl * 8 + i < NL) {
#pragma unroll
          for (int k = 0; k < 4; ++k) d = dot2(kr[sl * 8 + i][k], qq[k], d);
        }
        xs[i] = d;
      }
#pragma unroll
      for (int h = 0; h < 4; ++h) xs[h] = sel_add<XOR7>(xs[h], xs[h + 4], c & 4);
#pragma unroll
      for (int h = 0; h < 2; ++h) xs[h] = sel_add<XOR2>(xs[h], xs[h + 2], c & 2);
      xs[0] = sel_add<XOR1>(xs[0], xs[1], c & 1);
      if constexpr ((sl & 1) || sl == NSL - 1) {
        const float t = (sl & 1) ? sel_add<XOR8>(xlo, xs[0], c & 8) : xs[0] + dpp<XOR8>(xs[0]);
        const float s = rbf(rbf(t) * 0.08838834764831845f);
        sc[bt] = (bt * 16 + c < SK0 || (bt * 16 + c >= first && bt * 16 + c < NL)) ? s : -__builtin_inff();
        PIN(sc[bt]);
      } else {
        xlo = xs[0];
        PIN(xlo);
      }
    }
    if constexpr (v == VS) {
      float mloc = sc[0];
#pragma unroll
      for (int bt = 1; bt < NB; ++bt) mloc = fmaxf(mloc, sc[bt]);
      m = fmaxf(wave_max(mloc), -3.0e38f);
      float ls = 0.f;
#pragma unroll
      for (int bt = 0; bt < NB; ++bt) { pr[bt] = __expf(sc[bt] - m); ls += pr[bt]; }
      const float l = wave_sum(ls);
      if (lane == 0) { sm.ml[wave][0] = m; sm.ml[wave][1] = l; }
      if (c == ((NL - 1) & 15)) sm.pl[g][wave] = pr[NB - 1];
    }
    if constexpr (v >= SK1) pv_row(IC<v - SK1 + SK0 - WV>{});
    if constexpr (v < SK0 || v >= SK0 + WV) sfor<cons_end(v - 1), cons_end(v)>(pv_row);
    if constexpr (v == RVU) {
      if (wave == 7) {
        const bool ok = tbg[1] == tag;
        float lg = ok ? (float)(tbg[0] & 0xffffu) : 0.f, nf = ok ? 8.f * (RVP - RVS + 1) : 0.f;
        lg += dpp<XOR1>(lg); nf += dpp<XOR1>(nf); lg += dpp<XOR2>(lg); nf += dpp<XOR2>(nf); lg += dpp<XOR7>(lg); nf += dpp<XOR7>(nf); lg += dpp<XOR8>(lg); nf += dpp<XOR8>(nf);
        lg *= __builtin_amdgcn_rcpf(fmaxf(nf, 1.f));
        if (lane == 0) sm.bc3 = (u32)min(max((int)((lg - (float)P.F0) * (float)(P.FG * 32)), 0), 200 * 256);
      }
      LDS_BARRIER();
      const int st = __builtin_amdgcn_readfirstlane(sm.bc3), sp = (st * P.FF_) >> 3;
      t1 += st;
      due += sp;
      tau += __builtin_amdgcn_readfirstlane((int)((float)(st - sp) * __builtin_amdgcn_rcpf((float)(nl - RVU - 3))));
    }
    if constexpr (v == SK1 + 1) {
      float p[8];
#pragma unroll
      for (int w = 0; w < 8; ++w) p[w] = sm.pl[g][w] * __expf(sm.ml[w][0] - m);
#pragma unroll
      for (int w = 0; w < 8; ++w) pl8[w] = pack_bf(p[w], p[w]);
#pragma unroll
      for (int w = 0; w < 8; ++w) PIN(pl8[w]);
    }
    if constexpr (v == MLS - 1) { side_pin(); if (is_up) up_prep(); PIN(ga); PIN(xin); PIN(cc1); }
    if constexpr (v == UPREP - 1) wo_addr();
    if constexpr (v == UPREP) {
#pragma unroll
      for (int k = 0; k < 8; ++k) asm volatile("" : "=v"(bf[k]));
      if (part == 0) wo_prep(); else if (is_up) up_frags();
#pragma unroll
      for (int k = 0; k < 8; ++k) PIN(bf[k]);
    }
    if constexpr (v == UPREP + 1) {
      if (is_up) {
        const u32x4* a1 = ap + 128 + ((lane >> 1) & 1) * 128;
        f32x4 d0 = {0.f, 0.f, 0.f, 0.f}, d1 = {0.f, 0.f, 0.f, 0.f};
#pragma unroll
        for (int rb = 0; rb < 4; ++rb) { d0 = mfma16(a1[rb * 8], bf[rb], d0); d1 = mfma16(a1[(rb + 4) * 8], bf[rb + 4], d1); }
        Af = par ? d0[1] + d1[1] : d0[0] + d1[0]; Bf = par ? d0[3] + d1[3] : d0[2] + d1[2];
        Af += dpp<XOR8>(Af); Bf += dpp<XOR8>(Bf);
      }
      PIN(Af); PIN(Bf);
    }
    if constexpr (v == MLS) {
      LDS_BARRIER();
      float mm[8], ll[8];
#pragma unroll
      for (int w = 0; w < 8; ++w) { const f32x2 t = *reinterpret_cast<const f32x2*>(&sm.ml[w][0]); mm[w] = t[0]; ll[w] = t[1]; }
      float Mwg = mm[0], Lwg = 0.f;
#pragma unroll
      for (int w = 1; w < 8; ++w) Mwg = fmaxf(Mwg, mm[w]);
      if (wave == 0) {
#pragma unroll
        for (int w = 0; w < 8; ++w) Lwg = fmaf(__expf(mm[w] - Mwg), ll[w], Lwg);
        if (lane == 0) st16_sc1(wsr, G_ML + ((head * 2 + b) * 4 + part) * 16, __float_as_uint(Mwg), __float_as_uint(Lwg), tag);
      }
      const float fw = __expf(m - Mwg);
      m = Mwg;
#pragma unroll
      for (int bt = 0; bt < NB; ++bt) { pb[bt] = pack_bf(pr[bt] * fw, pr[bt] * fw); PIN(pb[bt]); }
#pragma unroll
      for (int k = 0; k < 4; ++k) { pm[k] = (c >> 1 & 3) == k ? ((c & 1) ? 0xffff0000u : 0x0000ffffu) : 0u; PIN(pm[k]); }
    }
    SB();
    return true;
  });
  asm volatile(".Ltail_start:");
  if (wave != 0) {
    pv_row(IC<SK0 - 1>{});
    WAIT_VM(WV); SB();
    sfor<NL - 1 - WV, NL - 1>(pv_row);
    *LDSP(f32x4, adst0) = acc;
    LDS_BARRIER();
  } else {
    u32x4 xr[8], am[8];
#pragma unroll
    for (int w = 0; w < 8; ++w) am[w] = u32x4{pl8[w] & pm[0], pl8[w] & pm[1], pl8[w] & pm[2], pl8[w] & pm[3]};
#pragma unroll
    for (int w = 0; w < 8; ++w) PIN(am[w]);
    {
      const u32 so = __builtin_amdgcn_readlane(sot, NL - 1);
      gate();
      SB();
#pragma unroll
      for (int w = 0; w < 8; ++w) xr[w] = ld16<AUX_NT>(rv, lane * 16, so ^ ((w & 1) ? col(P, 7) : 0u) ^ ((w & 2) ? col(P, 8) : 0u) ^ ((w & 4) ? col(P, 9) : 0u));
      SB();
    }
    u32 rsum = lds_addr(&sm.red[0][c * 8 + (g & 1) * 4]);
    pv_row(IC<SK0 - 1>{});
    WAIT_VM(WV + 8); SB();
    sfor<NL - 1 - WV, NL - 3>(pv_row);
    if (__builtin_expect(!all_ok((mlg[1] == tag) & (mlg[3] == tag)), 0)) {
#pragma nounroll
      for (int spin = 0; spin < SPIN_MAX; ++spin) {
        CFENCE();
        mlg = ld16<AUX_SC1>(wsr, mlo, 0);
        if (all_ok((mlg[1] == tag) & (mlg[3] == tag))) break;
      }
    }
    merge_factor(mlg);
    sfor<NL - 3, NL - 1>(pv_row);
    LDS_BARRIER();
    f32x4 t[8];
#pragma unroll
    for (int w = 1; w < 8; ++w) t[w] = LDSP(f32x4, rsum)[w * 32];
    acc = ((acc + t[1]) + (t[2] + t[3])) + ((t[4] + t[5]) + (t[6] + t[7]));
    PIN(acc);
    SB();
#pragma unroll
    for (int w = 0; w < 8; ++w) acc = mfma16(am[w], xr[w], acc);
  }
  WAIT_VM(0);
  acc *= fmrg;
  {
    const u32 w0 = min(pack_bf(acc[0], acc[1]), SENT - 1u), w1 = min(pack_bf(acc[2], acc[3]), SENT - 1u);
    const auto e0 = __builtin_amdgcn_permlane16_swap(w0, w0, false, false), e1 = __builtin_amdgcn_permlane16_swap(w1, w1, false, false);
    __builtin_amdgcn_raw_buffer_store_b128(u32x4{e0[0], e1[0], e0[1], e1[1]}, wsr, pso, 0, AUX_SC1);
  }
  st8_sc1(wsr, nto, (u32)wall_clock64() + nadd, tag);
  SB();

  if (part == 2) return;
  if (part == 0) {
    __builtin_amdgcn_s_barrier();
#pragma nounroll
    while (wleft > 0) { until(t1 - (P.PA + wleft * P.PD) * 256); piece(); }
    bool late = false;
    u32x4 og[2];
    if (wdef) {
      late = true;
      while ((int)(((u32)wall_clock64() - t0) << 8) < t1 - 20 * 256) if (__builtin_amdgcn_s_getreg(7 | (3 << 11)) == 0) { late = false; break; }
      if (__builtin_expect(late, 0)) { until(t1); CFENCE(); og[0] = ld16<AUX_SC1>(wsr, po, 0); SB(); WAIT_VM(1); } else { WAIT_VM(0); }
      CFENCE();
      if (P.WD > 4) wo_prep(0, 1);
      wo_prep(1, 2);
#pragma unroll
      for (int k = 0; k < 8; ++k) PIN(bf[k]);
      SB();
    }
    if (__builtin_expect(!late, 1)) { until(t1); CFENCE(); og[0] = ld16<AUX_SC1>(wsr, po, 0); }
    SB();
    f32x4 d[4];
    auto wo_mm = [&]() {
      const f32x4 z = {0.f, 0.f, 0.f, 0.f};
#pragma unroll
      for (int t = 0; t < 4; ++t) { d[t] = mfma16(og[0], bf[t], z); og[1][t] = __float_as_uint(dpp<XOR8>(u2f(og[0][t]))); }
      SB();
#pragma unroll
      for (int t = 0; t < 4; ++t) d[t] = mfma16(og[1], bf[4 + t], d[t]);
    };
    auto p_bad = [&]() { return !all_ok(max(max(og[0][0], og[0][1]), max(og[0][2], og[0][3])) != SENT); };
    wo_mm();
    if (__builtin_expect(p_bad(), 0)) {
#pragma nounroll
      for (int spin = 0; spin < SPIN_MAX; ++spin) {
        __builtin_amdgcn_s_sleep(1);
        CFENCE();
        og[0] = ld16<AUX_SC1>(wsr, po, 0);
        if (!p_bad()) break;
      }
      wo_mm();
      SB();
      asm volatile(".Lend_d:");
    }
    {
      SB();
      f32x4 y; f32x2 h[4];
#pragma unroll
      for (int t = 0; t < 4; ++t) { h[t] = d[t].xy + d[t].zw; PIN(h[t]); }
#pragma unroll
      for (int t = 0; t < 4; ++t) { y[t] = h[t][0] + h[t][1]; PIN(y[t]); }
      *LDSP(f32x4, rdst) = y;
    }
    LDS_BARRIER();
    if (wave < 2) {
      f32x2 v[4];
#pragma unroll
      for (int w = 0; w < 4; ++w) v[w] = f32x2{LDSP(float, rsrc)[w * 256], LDSP(float, rsrc)[w * 256 + 128]};
      const f32x2 h = (v[0] + v[1]) + (v[2] + v[3]);
      const float y = h[0] + h[1];
      __builtin_amdgcn_raw_buffer_store_b32(min(pack_bf(dpp<0xA0>(y), dpp<0xF5>(y)), SENT - 1u), wsr, xo, 0, AUX_SC1);
      asm volatile(".Lend_a:");
    }
    return;
  }
  if (part == 3) {
    if (!has_w) return;
    u32x4 b2[4];
    {
      __builtin_amdgcn_s_barrier();
      const rsrc_t r2 = mk_rsrc(w2 + (size_t)(blk * 32 + wave * 4) * 512, 4096u);
      const u32 l2 = (u32)(uintptr_t)(&sm.w2b[wave * 4][0]);
#pragma unroll
      for (int n = 0; n < 4; ++n) { until(t1 - (P.QA + (4 - n) * P.QD) * 256); dma16(r2, (lane ^ (n * 2) ^ ((wave & 2) << 2)) * 16, n * 1024, l2 + n * 1024); }
    }
    const u32 tdst = lds_addr(&sm.hb[hpos]);
    const u32 sdst = lds_addr(&sm.stat[wave][lane]);
    const u32 ssrc = lds_addr(&sm.stat[(g >> 1) * 4 + ((lane >> 3) & 3)][(lane & 7) * 4]);
    const u32 adst = lds_addr((c < 4 && (g & 1) == 0) ? &sm.actl[g >> 1][wave * 4 + c] : reinterpret_cast<u16*>(&sm.dummy[lane]));
    const u32 asrc = lds_addr(&sm.actl[lane & 1][g * 8]);
    const u32 apa = lds_addr(&sm.hb[(c >> 3) * 512 + (c & 3) * 64 + ((c >> 2) & 1) * 32 + g * 8]);
    constexpr u32 xs2 = 16384;
    u32 xoff = dup(G_XP) + near + g * 2048 + ub * 1024 + (wave & 3) * 256 + c * 16;
    u32x4 one;
#pragma unroll
    for (int k = 0; k < 4; ++k) one[k] = ((c & 2) == 0 && (c >> 2) == k) ? ((c & 1) ? 0x3f800000u : 0x00003f80u) : 0u;
    PIN(one);
    u32 ooff = dup(G_OP + (blk * 512 + tid) * 8);
    float Afp = Af, Bfp = Bf;
    PIN(xoff); PIN(ooff); PIN(Afp); PIN(Bfp);
    f32x2 gc = ga * cc1, xc = xin * cc1;
    PIN(ga); PIN(xin); PIN(gc); PIN(xc);
    u32 w2a = (u32)(uintptr_t)(&sm.w2b[0][0]) + (g * 8 + ((lane >> 2) & 3)) * 1024 + (wave ^ (g & 1)) * 128 + (lane & 15) * 8;
    PIN(w2a);
    if (wave == 1) {
      due = t1 + P.N1 * 256;
      const int tn = notices();
      due = tn + P.X2 * 256;
      until(due);
    }
    asm volatile("s_waitcnt vmcnt(0)" ::: "memory");
    __builtin_amdgcn_s_barrier();
    CFENCE();
    u32x4 xg[2] = {ld16<AUX_SC1>(wsr, xoff, 0), ld16<AUX_SC1>(wsr, xoff, xs2)};
    SB();
#pragma unroll
    for (int t = 0; t < 4; ++t) {
      const u32x2 lo = tr_read(w2a ^ (t * 32)), hi = tr_read((w2a ^ (t * 32)) + 4096);
      b2[t] = u32x4{lo[0], lo[1], hi[0], hi[1]};
    }
#pragma unroll
    for (int t = 0; t < 4; ++t) PIN(b2[t]);
    SB();
    const f32x4 z4 = {0.f, 0.f, 0.f, 0.f};
    auto xp_bad = [&]() { return !all_ok(max(max(max(xg[0][0], xg[0][1]), max(xg[0][2], xg[0][3])), max(max(xg[1][0], xg[1][1]), max(xg[1][2], xg[1][3]))) != SENT); };
    f32x4 v = mfma16(one, xg[1], mfma16(one, xg[0], z4));
    if (__builtin_expect(xp_bad(), 0)) {
#pragma nounroll
      for (int spin = 0; spin < SPIN_MAX; ++spin) {
        __builtin_amdgcn_s_sleep(1);
        CFENCE();
        xg[0] = ld16<AUX_SC1>(wsr, xoff, 0); xg[1] = ld16<AUX_SC1>(wsr, xoff, xs2);
        if (!xp_bad()) break;
      }
      v = mfma16(one, xg[1], mfma16(one, xg[0], z4));
      SB();
      asm volatile(".Lend_e:");
    }
    {
      const f32x2 a = v.xy, hc = __builtin_elementwise_fma(gc, a, xc), x1 = __builtin_elementwise_fma(ga, a, xin);
      *LDSP(u32, tdst) = pack_bf(hc[0], hc[1]);
      *LDSP(float, sdst) = swap32_add(x1[0] + x1[1], fmaf(x1[0], x1[0], x1[1] * x1[1]));
    }
    LDS_BARRIER();
    {
      const f32x4 z = {0.f, 0.f, 0.f, 0.f};
      const u32x4 af0 = LDSP(u32x4, apa)[0];
      SB();
      const f32x4 s4 = LDSP(f32x4, ssrc)[0], q4 = LDSP(f32x4, ssrc)[8];
      const u32x4 af1 = LDSP(u32x4, apa)[32];
      SB();
      f32x4 d[4];
      d[0] = mfma16(af0, bf[0], z); d[1] = mfma16(af0, bf[1], z);
      SB();
      const f32x2 s2 = s4.xy + s4.zw, q2 = q4.xy + q4.zw;
      float s = s2[0] + s2[1], q = q2[0] + q2[1];
      SB();
      d[2] = mfma16(af0, bf[2], z);
      SB();
      s = swap16_add(s, q);
      SB();
      d[3] = mfma16(af0, bf[3], z);
      SB();
      s += dpp<XOR1>(s); s += dpp<XOR2>(s);
      SB();
      d[0] = mfma16(af1, bf[4], d[0]);
      SB();
      s += dpp<XOR7>(s); s += dpp<XOR8>(s);
      SB();
      d[1] = mfma16(af1, bf[5], d[1]);
      SB();
      const auto sq = __builtin_amdgcn_permlane16_swap(__float_as_uint(s), __float_as_uint(s), false, false);
      const float mean = u2f(sq[0]) * (1.f / 512.f);
      SB();
      d[2] = mfma16(af1, bf[6], d[2]);
      SB();
      const float var = fmaf(u2f(sq[1]), 1.f / 512.f, -mean * mean);
      SB();
      d[3] = mfma16(af1, bf[7], d[3]);
      SB();
      float ri = __builtin_amdgcn_rsqf(fmaxf(var, 0.f) + 1e-5f);
      float cf = fmaf(-ri * mean, Afp, Bfp);
      float rk = ri * -1.44269504f, ck = cf * -1.44269504f;
      PIN(ri); PIN(cf); PIN(rk); PIN(ck);
      SB();
      PIN(d[0]); PIN(d[1]);
      float y = d[0][0] + d[1][1];
      PIN(y); PIN(d[2]);
      y += d[2][2];
      PIN(y); PIN(d[3]);
      y += d[3][3];
      y = __builtin_bit_cast(float, __builtin_amdgcn_update_dpp(__builtin_bit_cast(int, y), __builtin_bit_cast(int, y), XOR8, 0xa, 0xf, false));
      y = swap16_add(y, y);
      const float ex = __builtin_amdgcn_exp2f(fmaf(y, rk, ck));
      y = fmaf(y, ri, cf);
      const float gu = y * dpp<0x104>(y);
      *LDSP(u16, adst) = (u16)pack_bf(gu * __builtin_amdgcn_rcpf(1.f + ex), 0.f);
    }
    LDS_BARRIER();
    {
      const u32x4 af = *LDSP(u32x4, asrc);
      const f32x4 z = {0.f, 0.f, 0.f, 0.f};
      f32x4 d0 = mfma16(af, b2[0], z), d1 = mfma16(af, b2[1], z), d2 = mfma16(af, b2[2], z), d3 = mfma16(af, b2[3], z);
      asm volatile("" : "+v"(d0), "+v"(d1), "+v"(d2), "+v"(d3));
      const bool g1 = g & 1, g2 = g & 2;
      const float e0 = g1 ? d1[0] : d0[0], e1 = g1 ? d1[1] : d0[1], f0 = g1 ? d3[0] : d2[0], f1 = g1 ? d3[1] : d2[1];
      const u32x2 ov = {pack_bf(g2 ? f0 : e0, g2 ? f1 : e1), tag};
      __builtin_amdgcn_raw_buffer_store_b64(ov, wsr, ooff, near ^ 8192u, AUX_SC1);
      __builtin_amdgcn_raw_buffer_store_b64(ov, wsr, ooff, near, AUX_SC1);
    }
    asm volatile(".Lend_b:");
    return;
  }
  if (wave == 0) {
    if (!all_ok((sg[0][1] == tag) & (sg[0][3] == tag) & (sg[1][1] == tag) & (sg[1][3] == tag))) {
      u32x4 t[2];
      gather16<2>(wsr, G_MOD + (1024 + fd) * 8, 1536 * 8, tag, t);
      sg[0] = t[0]; sg[1] = t[1];
    }
    u32 xa;
    {
      due = t1 + P.N2 * 256;
      const int tn = notices();
      due = tn + P.X3 * 256;
      until(due);
    }
    const bool hb = g & 1;
#pragma nounroll
    for (int spin = 0; spin < SPIN_MAX; ++spin) {
      CFENCE();
      xa = __builtin_amdgcn_raw_buffer_load_b32(wsr, dup(G_XP) + near + (g >> 1) * 16384 + (c >> 2) * 2048 + (g & 1) * 1024 + fd * 2, 0, AUX_SC1);
      if (all_ok(xa != SENT)) break;
      __builtin_amdgcn_s_sleep(1);
    }
    float a0 = lo_f(xa), a1 = hi_f(xa);
    a0 += dpp<0x124>(a0); a1 += dpp<0x124>(a1);
    a0 += dpp<XOR8>(a0); a1 += dpp<XOR8>(a1);
    a0 = swap32_add(a0, a0); a1 = swap32_add(a1, a1);
    f32x2 xv = {rbf(lo_f(sx2) + rbf((hb ? hi_f(sg[0][0]) : lo_f(sg[0][0])) * rbf(a0))), rbf(hi_f(sx2) + rbf((hb ? hi_f(sg[0][2]) : lo_f(sg[0][2])) * rbf(a1)))};
    f32x2 gv = {hb ? hi_f(sg[1][0]) : lo_f(sg[1][0]), hb ? hi_f(sg[1][2]) : lo_f(sg[1][2])};
    PIN(xv); PIN(gv);
    if (wi == 0 && lane == 0) __builtin_amdgcn_raw_buffer_store_b32(tag, wsr, WS_EPOCH, 0, AUX_SC1);
    due = max(due + P.X4 * 256, (int)(((u32)wall_clock64() - t0) << 8) + P.GX * 256);
    const bool last = lane < (NUP - 32) * 4;
    u32 o0 = dup(G_OP + (lane >> 2) * 4096 + fd * 8) + near, o2 = last ? o0 : OOB, tg2 = last ? tag : 0u;
    const rsrc_t rout = mk_rsrc(out, 2048u);
    u32 oo = (lane & 44) == 0 ? (g * 512 + fd) * 2 : OOB;
    const u32 pk = (c & 10) ? 0u : ((c & 4) ? 0x3f800000u : 0x00003f80u);
    u32x4 one = {(c & 1) ? 0u : pk, 0u, (c & 1) ? pk : 0u, 0u};
    PIN(o0); PIN(o2); PIN(tg2); PIN(oo); PIN(one);
    __builtin_amdgcn_raw_buffer_store_b32(pack_bf(xv[0], xv[1]), rout, oo, 0, AUX_SC1);
    u32 s1_ = 131072, s2_ = 262144;
    asm volatile("" : "+s"(s1_), "+s"(s2_));
    until(due);
    f32x4 sv;
#pragma nounroll
    for (int spin = 0; spin < SPIN_MAX; ++spin) {
      CFENCE();
      const u32x4 og[3] = {ld16<AUX_SC1>(wsr, o0, 0), ld16<AUX_SC1>(wsr, o0, s1_), ld16<AUX_SC1>(wsr, o2, s2_)};
      sv = f32x4{0.f, 0.f, 0.f, 0.f};
#pragma unroll
      for (int k = 0; k < 3; ++k) sv = mfma16(one, og[k], sv);
      SB();
      u32 bad = ((og[0][1] ^ tag) | (og[0][3] ^ tag) | (og[1][1] ^ tag)) | ((og[1][3] ^ tag) | (og[2][1] ^ tg2) | (og[2][3] ^ tg2));
      PIN(bad);
      SB();
      float s0 = sv[0] + dpp<0x124>(sv[0]), s1 = sv[1] + dpp<0x124>(sv[1]);
      PIN(s0); PIN(s1);
      s0 += dpp<XOR8>(s0); s1 += dpp<XOR8>(s1);
      PIN(s0); PIN(s1);
      __builtin_amdgcn_raw_buffer_store_b32(pack_bf(fmaf(gv[0], s0, xv[0]), fmaf(gv[1], s1, xv[1])), rout, oo, 0, AUX_SC1);
      SB();
      if (all_ok(bad == 0u)) break;
      __builtin_amdgcn_s_sleep(1);
    }
    asm volatile(".Lend_c:");
  }
}

extern "C" void kernel_launch(void* const* d_in, const int* in_sizes, int n_in,
                              void* d_out, int out_size, void* d_ws, size_t ws_size,
                              hipStream_t stream) {
  fused_kernel<<<256, 512, 0, stream>>>((const u16*)d_in[1], (const u16*)d_in[2], (const u16*)d_in[4], (const u16*)d_in[3], (const u16*)d_in[10], (const u16*)d_in[6], (const u16*)d_in[7],
                                        (unsigned char*)d_ws, (const u16*)d_in[0], (const u16*)d_in[9], (const u16*)d_in[8], (u16*)d_out);
}
```

```cpp
#define PROBE_MUL 10
#include <hip/hip_runtime.h>
#include <stdint.h>

typedef unsigned int u32;
typedef unsigned short u16;
typedef unsigned long long u64;
typedef __attribute__((ext_vector_type(4))) unsigned u32x4;
typedef __attribute__((ext_vector_type(2))) unsigned u32x2;
typedef __attribute__((ext_vector_type(2))) __bf16 bf16x2;
typedef __attribute__((ext_vector_type(4))) float f32x4;
typedef __attribute__((ext_vector_type(2))) float f32x2;
typedef __amdgpu_buffer_rsrc_t rsrc_t;

constexpr int NH = 32, SK = 16384, DH = 128, WIN = 4096, FF = 1376;
struct Prm { int E0, ES, E1, E2, E2C, EU, TEND, DT_UP, L0, L1, X1, X2, X3, X4, NE, FE, M0, M1, M2, M3, M4, GS, GX, N1, N2, GA, WD, DT_WO, LW, LU, EW, PM, NF, NR, C0, A0, AG, AC, TS, TW, TK, TG, GB, B0, BG, BF, D0, DG, TC, LM, C1, F0, FG, FF_, PA, PD, QA, QD, RT, W[9]; };
constexpr Prm PRM0 = {32, 20, 32, 32, 54, 56, 1980, 64, 12, 30, 83, 66, 100, 80, 3, 9, 0x2511f55b, 0x2b7425d5, 0x6017665, 0x17e0b9ec, 0x31944305, 62, 30, 32, 62, 30, 4, 100, 45, 10, 10, 0x0, 15, 14, 3, 30, 4, 4, 508, 220, 75, 6, 6, 74, 26, 8, 8, 6, 0, 120, 2, 67, 18, 4, 62, 14, 6, 15, 0, 0x77777777, 0x77777777, 0x77777777, 0x77777777, 0x77777777, 0x44444477, 0x44444444, 0x44444444, 0x44444444};
constexpr int NL = 37;
constexpr int SK0 = 21, SK1 = 32;
constexpr int NCMIN = (NL - SK1 + SK0) * 8;
constexpr int WK = 8, WV = 5;
constexpr int WUSE = 12, LNS = 7;
constexpr int QP = 14, QD = 4;
constexpr int VQ = 6;
constexpr int NSL = (NL + 7) / 8;
constexpr int VS = VQ + NSL;
constexpr int MLS = VS + 2;
constexpr int VSIDE = MLS - 5, UPREP = MLS + 6;
constexpr int CPF = 17;
constexpr int SMO = 1;
constexpr int RVS = 5, RVP = 8, RVL = 12, RVU = 16;
constexpr int RPS = 24, RPP = 28;
constexpr int PRB = 12;
constexpr int TBS = 7, TBP = 10, TBL = 13, TBU = 17;
constexpr int window(int s, int w) {
  int n = w;
  for (int u = s - w + 1; u <= s; ++u) n += u == SMO ? 6 : (u == TBL || u == SK1 || u == NL + CPF || u == NL + RVL || u == NL + SK1 ? 1 : (u == QP || u == NL + VQ - 4 ? 2 : (u == NL + VSIDE ? 4 : 0)));
  return n;
}
constexpr int cons_end(int v) {
  const int a = v - WV + 1, c = (v - MLS) * 4;
  const int m = a < c ? a : c;
  return m < 0 ? 0 : (m > NL ? NL : m);
}
constexpr u32 col(const Prm& P, int j) { const int m[6] = {P.M0, P.M1, P.M2, P.M3, P.M4, P.PM}; return (u32)(m[j / 3] >> (j % 3 * 10) & 1023) << 10; }
constexpr u32 first_col(const Prm& P, int k) {
  if (k < 4) return col(P, 12 + k) | 1u << (24 + k);
  if (k < 6) return col(P, k - 4);
  if (k < 8) return col(P, 4 + k) | 1u << (16 + k);
  if (k < 11) return col(P, k - 1);
  return (col(P, 2) ^ col(P, 3) ^ col(P, 4) ^ col(P, 5) ^ col(P, 6) ^ col(P, 10) ^ col(P, 11)) | 0xf00000u;
}
constexpr bool one_to_one(const Prm& P) {
  u32 c[10] = {};
  for (int j = 0; j < 10; ++j) c[j] = col(P, j);
  for (int j = 0; j < 10; ++j) {
    if (c[j] == 0) return false;
    const u32 low = c[j] & (0u - c[j]);
    for (int k = j + 1; k < 10; ++k) if (c[k] & low) c[k] ^= c[j];
  }
  return true;
}
static_assert(one_to_one(PRM0), "every KB of the window exactly once");
struct Tab { u32 e[512]; };
constexpr Tab make_tab(const Prm& P) {
  Tab T{};
  for (int bid = 0; bid < 256; ++bid) {
    const int xcd = bid & 7, part = bid >> 4 & 3, i8 = (bid >> 3 & 1) | bid >> 6 << 1, wi = i8 * 8 + xcd;
    const bool has_mod = wi < 48, has_up = wi >= 21;
    const int cb0 = (i8 + P.C0) & 7, cb1 = (i8 + P.C1) & 7, ms = wi - 20;
    const int mi = wi < 8 ? wi : (wi < 16 ? 8 + ((xcd + 4) & 7) : (wi < 20 ? (wi - 14) * 8 + 3 : 16 + ms / 7 * 8 + ms % 7 + (ms % 7 >= 3 ? 1 : 0)));
    const int ui = xcd * 5 + (xcd > 5 ? xcd - 5 : 0) + (i8 == 6 ? 0 : (xcd >= 5 ? 1 : 0) + (i8 == 7 ? 4 : i8 - 2));
    const bool edge = i8 >= 6;
    const int wd = cb0 == 3 ? 0 : P.WD;
    const int e[4] = {P.E0 - 4 * wd + (cb0 == 3 ? P.ES : 0), P.E1 + (cb1 == 3 ? P.ES : 0), has_mod ? (wi < 16 ? P.E2C : P.E2) + ((mi & 7) == 3 ? P.ES : 0) : 0, has_up ? P.EU + (edge ? P.TC : 0) : 0};
    int tw[4] = {}, win[4] = {}, span[4] = {}, k0[4] = {};
    for (int k = 0; k < 4; ++k) {
      const int np = k == 0 ? 8 - wd : 8;
      tw[k] = np ? e[k] * P.TW / (16 * np) : 0;
      k0[k] = P.NF + tw[k] * np / 2 > P.TK ? P.NF + tw[k] * np / 2 : P.TK;
      span[k] = P.TEND - k0[k] - P.TG - (k == 0 ? (wd ? P.DT_WO : P.A0) : (k == 3 && has_up ? P.DT_UP : 0));
      win[k] = span[k] - (k == 3 && has_up ? P.GA + (edge ? P.LM : 0) : 0);
    }
    int n[4] = {NCMIN / 8, NCMIN / 8, NCMIN / 8, NCMIN / 8};
    for (int left = 128 - NCMIN / 2; left > 0; --left) {
      int best = -1;
      for (int k = 3; k >= 0; --k)
        if (n[k] < NL && (best < 0 || n[k] * win[best] < n[best] * win[k])) best = k;
      ++n[best];
    }
    const int c[4] = {0, n[0] - 1, n[0] + n[1] - 2, n[0] + n[1] + n[2] - 3};
    const int nl = n[part];
    const bool has_w = part < 2 || (part == 2 ? has_mod : has_up);
    const int blk = !has_w ? 0 : (part == 0 ? cb0 : (part == 1 ? xcd * 8 + cb1 : (part == 2 ? mi : ui)));
    T.e[2 * bid] = (u32)(c[part] | nl << 7 | blk << 13 | (has_w ? 1 : 0) << 19 | (part == 0 ? wd : 0) << 20) | (u32)tw[part] << 24;
    T.e[2 * bid + 1] = (u32)(span[part] * P.TS / (4 * (nl - 1)) | span[part] * (1024 - P.TS) / (4 * (nl - 1)) << 16);
  }
  return T;
}
constexpr int step_range(const Prm& P, bool mx_) {
  int mx = 0, mn = 1 << 20;
  const Tab T = make_tab(P);
  for (int bid = 0; bid < 256; ++bid) { const int n = T.e[2 * bid] >> 7 & 63; mx = n > mx ? n : mx; mn = n < mn ? n : mn; }
  return mx_ ? mx : mn;
}
extern "C" __attribute__((used, visibility("protected"))) __constant__ const Tab wg_tab = make_tab(PRM0);
static_assert(step_range(PRM0, true) <= NL && step_range(PRM0, false) >= NCMIN / 8, "a wave's key range does not fit its load steps");
static_assert(UPREP + 1 < SK0 && QP + QD + 1 < SK0 && cons_end(SK0 - 1) == SK0 - WV && NL - SK1 == WV, "steps every wave runs");
constexpr int NUP = FF / 32;
constexpr int SPIN_MAX = 1 << 17;

constexpr u32 WS_EPOCH = 0;
constexpr u32 G_TB = 1024;
constexpr u32 G_MOD = 4096;
constexpr u32 G_Q = G_MOD + 3072 * 8;
constexpr u32 G_ML = G_Q + 4096 * 8;
constexpr u32 G_U = G_ML + 256 * 16;
constexpr u32 G_P = 0;
constexpr u32 G_XP = G_P + 256 * 256;
constexpr u32 G_OP = G_XP + 8 * 512 * 4;
constexpr u32 G_NT = G_OP + 43 * 512 * 8;
constexpr u32 G_PB = G_OP + 44 * 512 * 8;
constexpr u32 G_VT = G_U + 2 * (G_PB + 8192);
constexpr u32 WS_END = G_VT + 2048;
__device__ __forceinline__ u32 dup(u32 o) { return G_U + ((o >> 13) << 14) + (o & 8191u); }
constexpr u32 SENT = 0xffffffffu;
constexpr u32 OOB = 0x40000000u;
#define WST 0

constexpr int AUX_NT = 2, AUX_SC1 = 16;

__device__ __forceinline__ float lo_f(u32 v) { return __uint_as_float(v << 16); }
__device__ __forceinline__ float hi_f(u32 v) { return __uint_as_float(v & 0xffff0000u); }
__device__ __forceinline__ float rbf(float x) { return (float)(__bf16)x; }
__device__ __forceinline__ u32 pack_bf(float lo, float hi) {
  bf16x2 r; r[0] = (__bf16)lo; r[1] = (__bf16)hi; return __builtin_bit_cast(u32, r);
}
__device__ __forceinline__ float dot2(u32 k, u32 q, float acc) {
  return __builtin_amdgcn_fdot2_f32_bf16(__builtin_bit_cast(bf16x2, k), __builtin_bit_cast(bf16x2, q), acc, false);
}
template <int CTRL> __device__ __forceinline__ float dpp(float x) {
  return __builtin_bit_cast(float, __builtin_amdgcn_mov_dpp(__builtin_bit_cast(int, x), CTRL, 0xf, 0xf, true));
}
constexpr int XOR1 = 0xB1, XOR2 = 0x4E, XOR7 = 0x141, XOR8 = 0x128;
__device__ __forceinline__ float swap32_add(float a, float b) {
  auto t = __builtin_amdgcn_permlane32_swap(__float_as_uint(a), __float_as_uint(b), false, false);
  return __uint_as_float(t[0]) + __uint_as_float(t[1]);
}
__device__ __forceinline__ float swap16_add(float a, float b) {
  auto t = __builtin_amdgcn_permlane16_swap(__float_as_uint(a), __float_as_uint(b), false, false);
  return __uint_as_float(t[0]) + __uint_as_float(t[1]);
}
__device__ __forceinline__ float wave_sum(float x) {
  x += dpp<XOR1>(x); x += dpp<XOR2>(x); x += dpp<XOR7>(x); x += dpp<XOR8>(x);
  x = swap16_add(x, x);
  return swap32_add(x, x);
}
__device__ __forceinline__ float wave_max(float x) {
  x = fmaxf(x, dpp<XOR1>(x)); x = fmaxf(x, dpp<XOR2>(x)); x = fmaxf(x, dpp<XOR7>(x)); x = fmaxf(x, dpp<XOR8>(x));
  auto s = __builtin_amdgcn_permlane16_swap(__float_as_uint(x), __float_as_uint(x), false, false);
  x = fmaxf(__uint_as_float(s[0]), __uint_as_float(s[1]));
  auto t = __builtin_amdgcn_permlane32_swap(__float_as_uint(x), __float_as_uint(x), false, false);
  return fmaxf(__uint_as_float(t[0]), __uint_as_float(t[1]));
}
template <int CTRL> __device__ __forceinline__ float sel_add(float lo, float hi, bool up) {
  const float t1 = lo + dpp<CTRL>(lo), t2 = hi + dpp<CTRL>(hi);
  return up ? t2 : t1;
}
template <int I> __device__ __forceinline__ float bc16(float x) {
  return __builtin_bit_cast(float, __builtin_amdgcn_update_dpp(0, __builtin_bit_cast(int, x), 0x150 + I, 0xf, 0xf, false));
}
__device__ __forceinline__ float bcast16(float x, int i) {
  switch (i) {
    case 0: return bc16<0>(x); case 1: return bc16<1>(x); case 2: return bc16<2>(x); case 3: return bc16<3>(x);
    case 4: return bc16<4>(x); case 5: return bc16<5>(x); case 6: return bc16<6>(x); case 7: return bc16<7>(x);
    case 8: return bc16<8>(x); case 9: return bc16<9>(x); case 10: return bc16<10>(x); case 11: return bc16<11>(x);
    case 12: return bc16<12>(x); case 13: return bc16<13>(x); case 14: return bc16<14>(x); default: return bc16<15>(x);
  }
}
__device__ __forceinline__ float silu_f(float x) { return x * __builtin_amdgcn_rcpf(1.f + __expf(-x)); }

template <int AUX> __device__ __forceinline__ u32x4 ld16(rsrc_t r, u32 voff, u32 soff) {
  return __builtin_amdgcn_raw_buffer_load_b128(r, voff, soff, AUX);
}
template <int AUX> __device__ __forceinline__ u32x2 ld8(rsrc_t r, u32 voff, u32 soff) {
  return __builtin_amdgcn_raw_buffer_load_b64(r, voff, soff, AUX);
}
__device__ __forceinline__ void st8_sc1(rsrc_t r, u32 voff, u32 v, u32 tag) {
  __builtin_amdgcn_raw_buffer_store_b64(u32x2{v, tag}, r, voff, 0, AUX_SC1);
}
__device__ __forceinline__ void st16_sc1(rsrc_t r, u32 voff, u32 v0, u32 v1, u32 tag) {
  __builtin_amdgcn_raw_buffer_store_b128(u32x4{v0, tag, v1, tag}, r, voff, 0, AUX_SC1);
}
__device__ __forceinline__ rsrc_t mk_rsrc(const void* p, u32 bytes) {
  return __builtin_amdgcn_make_buffer_rsrc(const_cast<void*>(p), 0, bytes, 0x00020000);
}
#define WAIT_VM(n) __builtin_amdgcn_s_waitcnt(((n) & 15) | (((n) >> 4) << 14) | (7 << 4) | (15 << 8))
#define SB() __builtin_amdgcn_sched_barrier(0)
#define CFENCE() asm volatile("" ::: "memory")
__device__ __forceinline__ bool all_ok(bool ok) { return __builtin_amdgcn_ballot_w64(!ok) == 0ull; }

template <int N> __device__ __forceinline__ void gather16(rsrc_t ws, u32 voff, u32 stride, u32 tag, u32x4 (&g)[N]) {
#pragma nounroll
  for (int spin = 0; spin < SPIN_MAX; ++spin) {
    CFENCE();
    bool ok = true;
#pragma unroll
    for (int i = 0; i < N; ++i) g[i] = ld16<AUX_SC1>(ws, voff + i * stride, 0);
#pragma unroll
    for (int i = 0; i < N; ++i) ok &= (g[i][1] == tag) & (g[i][3] == tag);
    if (all_ok(ok)) break;
    __builtin_amdgcn_s_sleep(8);
  }
}
template <int N> __device__ __forceinline__ void gather8(rsrc_t ws, u32 voff, u32 stride, u32 tag, u32 (&g)[N], bool act = true) {
#pragma nounroll
  for (int spin = 0; spin < SPIN_MAX; ++spin) {
    CFENCE();
    bool ok = true;
    u32x2 t[N];
#pragma unroll
    for (int i = 0; i < N; ++i) t[i] = ld8<AUX_SC1>(ws, voff + i * stride, 0);
#pragma unroll
    for (int i = 0; i < N; ++i) { ok &= (t[i][1] == tag); g[i] = t[i][0]; }
    if (all_ok(ok | !act)) break;
    __builtin_amdgcn_s_sleep(8);
  }
}

struct Smem {
  float red[8][128];
  float ml[8][2];
  float r2[64 * 20];
  u16 hb[3 * 1024];
  float stat[8][64];
  float red2[8][128];
  u32 dummy[64];
  u32x2 bc, bc2;
  u32 bc3, bc4;
  u32x4 zero[4];
  float pl[4][8];
  u32 mflag[8];
  u32x4 wbuf[8][8][64];
  __attribute__((aligned(256))) u16 w2b[32][512];
  u16 actl[2][32];
};

#define PROBE_K 8
#define PROBE_OP 0
#define PROBE_PART -1
#define STAMP(k) do { if ((k) == PROBE_K && tid == 0) pst = wall_clock64(); } while (0)
#define WSTAMP(k) do {} while (0)
#define FINISH() do { STAMP(8); if (tid == 0) { __hip_atomic_store(reinterpret_cast<u64*>(ws + (1u << 20)) + bid * 2, pt0, __ATOMIC_RELAXED, __HIP_MEMORY_SCOPE_AGENT); \
   __hip_atomic_store(reinterpret_cast<u64*>(ws + (1u << 20)) + bid * 2 + 1, (PROBE_PART < 0 || part == PROBE_PART) ? pst : 0ull, __ATOMIC_RELAXED, __HIP_MEMORY_SCOPE_AGENT); } \
   if (bid == 8 && wave == 0) probe_end(ws, pt0, lane); return; } while (0)
#define PIN(x) asm volatile("" : "+v"(x))
#define LDS_BARRIER() do { __builtin_amdgcn_s_waitcnt(0xc07f); __builtin_amdgcn_s_barrier(); CFENCE(); } while (0)
#define WAVE_SYNC() do { __builtin_amdgcn_fence(__ATOMIC_RELEASE, "wavefront"); __builtin_amdgcn_wave_barrier(); __builtin_amdgcn_fence(__ATOMIC_ACQUIRE, "wavefront"); } while (0)

__device__ __forceinline__ void dma16(rsrc_t r, u32 voff, u32 soff, u32 lds_dst) {
  u32 keep;
  asm volatile("s_mov_b32 %0, m0\n\ts_mov_b32 m0, %4\n\ts_nop 4\n\tbuffer_load_dwordx4 %1, %2, %3 offen nt lds\n\ts_mov_b32 m0, %0"
               : "=&s"(keep) : "v"(voff), "s"(r), "s"(soff), "s"(lds_dst) : "memory");
}
__device__ __forceinline__ void ld16_into(u32x4& d, rsrc_t r, u32 voff, u32 soff) {
  asm volatile("s_nop 4\n\tbuffer_load_dwordx4 %0, %1, %2, %3 offen nt" : "+v"(d) : "v"(voff), "s"(r), "s"(soff) : "memory");
}
__device__ __forceinline__ u32 perm_lo(u32 a, u32 b) { return __builtin_amdgcn_perm(b, a, 0x05040100u); }
__device__ __forceinline__ u32 perm_hi(u32 a, u32 b) { return __builtin_amdgcn_perm(b, a, 0x07060302u); }
__device__ __forceinline__ float rdl(float x, int l) { return __int_as_float(__builtin_amdgcn_readlane(__float_as_int(x), l)); }

typedef __attribute__((ext_vector_type(8))) __bf16 bf16x8;
__device__ __forceinline__ f32x4 mfma16(u32x4 a, u32x4 b, f32x4 c) {
  return __builtin_amdgcn_mfma_f32_16x16x32_bf16(__builtin_bit_cast(bf16x8, a), __builtin_bit_cast(bf16x8, b), c, 0, 0, 0);
}
__device__ __forceinline__ float sum8(const float* p) {
  const f32x4 a = *reinterpret_cast<const f32x4*>(p), b = *reinterpret_cast<const f32x4*>(p + 4);
  return ((a[0] + b[0]) + (a[1] + b[1])) + ((a[2] + b[2]) + (a[3] + b[3]));
}
__device__ __forceinline__ float ln_ri(float var) {
  return __builtin_amdgcn_rcpf(rbf(__builtin_amdgcn_sqrtf(rbf(rbf(fmaxf(var, 0.f)) + 1e-5f))));
}
__device__ __forceinline__ void ln_stats2(const float (&x0)[8], const float (&x1)[8], float& mb0, float& ri0, float& mb1, float& ri1) {
  const float k0 = rdl(x0[0], 0), k1 = rdl(x1[0], 0);
  float s0 = 0.f, s1 = 0.f, q0 = 0.f, q1 = 0.f;
#pragma unroll
  for (int k = 0; k < 8; ++k) {
    const float d0 = x0[k] - k0, d1 = x1[k] - k1;
    s0 += d0; s1 += d1; q0 = fmaf(d0, d0, q0); q1 = fmaf(d1, d1, q1);
  }
  float t = swap16_add(swap32_add(s0, s1), swap32_add(q0, q1));
  t += dpp<XOR1>(t); t += dpp<XOR2>(t); t += dpp<XOR7>(t); t += dpp<XOR8>(t);
  auto u = __builtin_amdgcn_permlane16_swap(__float_as_uint(t), __float_as_uint(t), false, false);
  const float ms = __uint_as_float(u[0]) * (1.f / 512.f), mq = __uint_as_float(u[1]) * (1.f / 512.f);
  const float ri = ln_ri(fmaf(-ms, ms, mq));
  mb0 = rbf(k0 + rdl(ms, 0)); mb1 = rbf(k1 + rdl(ms, 32));
  ri0 = rdl(ri, 0); ri1 = rdl(ri, 32);
}
template <int I> struct IC { static constexpr int v = I; };
template <int I, int N, class F> __device__ __forceinline__ void sfor(F&& f) {
  if constexpr (I < N) { if (f(IC<I>{})) sfor<I + 1, N>(f); }
}
typedef __attribute__((ext_vector_type(4))) short s16x4;
typedef __attribute__((address_space(3))) s16x4* lds_s16x4p;
__device__ __forceinline__ u32x2 tr_read(u32 lds_addr) {
  return __builtin_bit_cast(u32x2, __builtin_amdgcn_ds_read_tr16_b64_v4i16((lds_s16x4p)lds_addr));
}
__device__ __forceinline__ u32 next_tag(u32 e) { return ((e | 0x40004000u) + 1u) & ~0x40004000u; }
__device__ __forceinline__ float u2f(u32 v) { return __uint_as_float(v); }
#define LDSP(T, a) ((__attribute__((address_space(3))) T*)(a))
template <class T> __device__ __forceinline__ u32 lds_addr(T* p) { u32 a = (u32)(uintptr_t)p; PIN(a); return a; }

#define TAB_LOAD "s_add_u32 s30, s28, wg_tab@rel32@lo+4\ns_addc_u32 s31, s29, wg_tab@rel32@hi+12\nglobal_load_dwordx2 %[te], %[va], s[30:31]\n"
#define FC(k) ".Lfc" #k
#define FC_REGS
#define FX(r, bit, k) "s_bitcmp1_b32 %[" #r "], " #bit "\ns_cselect_b32 %[sb], " FC(k) ", 0\ns_xor_b32 %[sa], %[sa], %[sb]\n"
__device__ __forceinline__ void probe_end(unsigned char* ws, u64 pt0, int lane) {
  while (wall_clock64() < pt0 + 5500ull) __builtin_amdgcn_s_sleep(8);
  u64 a[4], c2[4];
  for (int i = 0; i < 4; ++i) {
    a[i] = __hip_atomic_load(reinterpret_cast<u64*>(ws + (1u << 20)) + (i * 64 + lane) * 2, __ATOMIC_RELAXED, __HIP_MEMORY_SCOPE_AGENT);
    c2[i] = __hip_atomic_load(reinterpret_cast<u64*>(ws + (1u << 20)) + (i * 64 + lane) * 2 + 1, __ATOMIC_RELAXED, __HIP_MEMORY_SCOPE_AGENT);
  }
  int mn0 = 1 << 30, mx1 = -(1 << 30), mn1 = 1 << 30, sum = 0, cnt = 0;
  for (int i = 0; i < 4; ++i) {
    const int d0 = (int)(a[i] - pt0);
    mn0 = min(mn0, d0);
    if (c2[i] != 0ull) { const int d1 = (int)(c2[i] - pt0); mx1 = max(mx1, d1); mn1 = min(mn1, d1); sum += d1; cnt += 1; }
  }
  for (int o = 1; o < 64; o <<= 1) { mn0 = min(mn0, __shfl_xor(mn0, o)); mx1 = max(mx1, __shfl_xor(mx1, o)); mn1 = min(mn1, __shfl_xor(mn1, o)); sum += __shfl_xor(sum, o); cnt += __shfl_xor(cnt, o); }
  int X = PROBE_OP == 0 ? mx1 : (PROBE_OP == 1 ? sum / max(cnt, 1) : mn1);
  if (PROBE_K == 102) X = mn0;
  X = mn0 + (X - mn0) * PROBE_MUL;
  const u64 tg = pt0 + (u64)(long long)(X + 8000);
  for (int i = 0; i < 400000 && wall_clock64() < tg; ++i) __builtin_amdgcn_s_sleep(1);
}
__global__ void __launch_bounds__(512, 2) fused_kernel(const u16* kc_, const u16* vc_, const u16* wo_, const u16* wq_, const u16* wmod_, const u16* w1_, const u16* w3_,
                                                       unsigned char* ws_, const u16* x_, const u16* cnd_, const u16* w2_, u16* out_) {
  __shared__ __attribute__((aligned(256))) Smem sm;
  const u64 a_ka = (u64)__builtin_amdgcn_kernarg_segment_ptr();
  const int bid = blockIdx.x, tid = threadIdx.x;
  u64 wa, t64; u32x2 te; int tid0;
  u32 ar[14];
  u32 tch2, sa_, sb_, va_;
  u32x4 kr[NL], vr[NL];
  asm volatile(".set .Lfc0, %0\n.set .Lfc1, %1\n.set .Lfc2, %2\n.set .Lfc3, %3\n.set .Lfc4, %4\n.set .Lfc5, %5\n.set .Lfc6, %6\n.set .Lfc7, %7\n.set .Lfc8, %8\n.set .Lfc9, %9\n.set .Lfc10, %10\n.set .Lfc11, %11"
    :: "n"(first_col(PRM0, 0)), "n"(first_col(PRM0, 1)), "n"(first_col(PRM0, 2)), "n"(first_col(PRM0, 3)), "n"(first_col(PRM0, 4)), "n"(first_col(PRM0, 5)),
       "n"(first_col(PRM0, 6)), "n"(first_col(PRM0, 7)), "n"(first_col(PRM0, 8)), "n"(first_col(PRM0, 9)), "n"(first_col(PRM0, 10)), "n"(first_col(PRM0, 11)));
  asm volatile(
    "s_load_dwordx16 s[4:19], %[ka], 0x0\ns_load_dwordx8 s[20:27], %[ka], 0x40\n"
    "s_memrealtime %[t]\n"
    "v_lshlrev_b32 %[va], 3, %[bid]\n"
    "s_getpc_b64 s[28:29]\n.Lcpf_first:\n"
    TAB_LOAD
    "s_cmp_gt_u32 %[bid], 15\ns_cbranch_scc1 .Lno_cpf\n"
    "s_mov_b32 %[sa], .Lend_a-.Lcpf_first\ns_max_i32 %[sa], %[sa], .Lend_b-.Lcpf_first\ns_max_i32 %[sa], %[sa], .Lend_c-.Lcpf_first\ns_max_i32 %[sa], %[sa], .Lend_d-.Lcpf_first\ns_max_i32 %[sa], %[sa], .Lend_e-.Lcpf_first\n"
    "v_lshlrev_b32 %[va], 6, %[tid]\nv_min_u32 %[va], %[sa], %[va]\nglobal_load_dword %[pf], %[va], s[28:29]\n"
    ".Lno_cpf:\n"
    "v_readfirstlane_b32 %[t0], %[tid]\nv_and_b32 %[va], 63, %[tid]\n"
    "s_mov_b32 %[sa], " FC(11) "\n"
    FX(bid, 0, 0) FX(bid, 1, 1) FX(bid, 2, 2) FX(bid, 3, 3) FX(bid, 4, 4) FX(bid, 5, 5) FX(bid, 6, 6) FX(bid, 7, 7) FX(t0, 6, 8) FX(t0, 7, 9) FX(t0, 8, 10)
    "v_lshl_add_u32 %[va], %[va], 4, %[sa]\n"
    "s_waitcnt lgkmcnt(0)\nglobal_load_dwordx4 %[k0], %[va], s[4:5] nt\nglobal_load_dwordx4 %[v0], %[va], s[6:7] nt\n"
    "s_bitcmp1_b32 %[t0], 6\ns_cselect_b64 s[14:15], s[16:17], s[14:15]\n"
    "s_bitcmp1_b32 %[bid], 4\ns_cselect_b64 s[8:9], s[10:11], s[8:9]\ns_cselect_b64 s[12:13], s[14:15], s[12:13]\ns_bitcmp1_b32 %[bid], 5\ns_cselect_b64 %[wa], s[12:13], s[8:9]"
    : "=&{s4}"(ar[0]), "=&{s5}"(ar[1]), "=&{s6}"(ar[2]), "=&{s7}"(ar[3]), "=&{s18}"(ar[4]), "=&{s19}"(ar[5]), "=&{s20}"(ar[6]), "=&{s21}"(ar[7]), "=&{s22}"(ar[8]), "=&{s23}"(ar[9]),
      "=&{s24}"(ar[10]), "=&{s25}"(ar[11]), "=&{s26}"(ar[12]), "=&{s27}"(ar[13]), [wa] "=&s"(wa), [t0] "=&s"(tid0), [t] "=&s"(t64), [te] "=&v"(te), [sa] "=&s"(sa_), [sb] "=&s"(sb_),
      [va] "=&v"(va_), [pf] "=&v"(tch2), [k0] "=&v"(kr[0]), [v0] "=&v"(vr[0])
    : [ka] "s"(a_ka), [bid] "s"(bid), [tid] "v"(tid) : "s8", "s9", "s10", "s11", "s12", "s13", "s14", "s15", "s16", "s17", "s28", "s29", "s30", "s31", "scc", "memory" FC_REGS);
  asm volatile("" : "+s"(ar[0]), "+s"(ar[1]), "+s"(ar[2]), "+s"(ar[3]), "+s"(ar[4]), "+s"(ar[5]), "+s"(ar[6]), "+s"(ar[7]), "+s"(ar[8]), "+s"(ar[9]), "+s"(ar[10]), "+s"(ar[11]), "+s"(ar[12]), "+s"(ar[13]));
  asm volatile("" : "+s"(wa), "+s"(t64), "+s"(tid0), "+s"(sa_));
  const u32 tka = (u32)wall_clock64();
  const u64 pt0 = t64; u64 pst = (PROBE_K == 100 || PROBE_K == 102) ? pt0 : (PROBE_K == 101 ? pt0 + (u32)(tka - (u32)t64) : 0);
  const u32 t0 = tka;
  __builtin_assume(tid >= 0 && tid < 512 && tid0 >= 0 && tid0 < 512 && bid >= 0 && bid < 256);
  constexpr Prm P = PRM0;
  const int TEND = P.TEND;
  const int xcd = bid & 7, part = bid >> 4 & 3;
  const int lane = tid & 63, wave = tid0 >> 6;
  SB();
  const bool p3 = part == 3;
  u32 pitch = (u32)(0x005600c001000020ull >> (part * 16) & 0xffff) * 32u;
  const int sh = p3 ? 2 : 3;
  u32 wv0 = (u32)((p3 ? (wave >> 1) * 128 : wave * 64) + (part == 0 ? xcd * 512 : 0) + (lane >> sh)) * pitch + ((lane & (p3 ? 3 : 7)) ^ (lane >> (p3 ? 5 : 3))) * 16u;
  const u32 wimg = (u32)(uintptr_t)(&sm.wbuf[0][0][0]);
  u32 wsz = (part == 0 ? 4096u : 512u) * pitch, wld = wimg + (p3 ? (wave & 1) * 32768 + (wave >> 1) * 8192 : wave * 8192);
  PIN(wv0);
  asm volatile("" : "+s"(wsz), "+s"(wld), "+s"(pitch));
  auto karg = [&](int i) { return ar[2 * i] | (u64)ar[2 * i + 1] << 32; };
  const u64 a_kc = karg(0), a_vc = karg(1);
  unsigned char* ws = (unsigned char*)karg(2);
  const u16 *x = (const u16*)karg(3), *cnd = (const u16*)karg(4), *w2 = (const u16*)karg(5);
  u16* out = (u16*)karg(6);
  const rsrc_t wsr = mk_rsrc(ws, WS_END);
  asm volatile("s_waitcnt vmcnt(2)" : "+v"(te));
  const u32 te0 = __builtin_amdgcn_readfirstlane(te[0]), te1 = __builtin_amdgcn_readfirstlane(te[1]);
  const int bl0 = te0 & 127, nl = te0 >> 7 & 63, blk = te0 >> 13 & 63;
  const bool has_w = te0 >> 19 & 1;
  const rsrc_t rw = mk_rsrc((const void*)wa, wsz);
  const u32 wstep = pitch << (6 - sh), wflip = p3 ? 32u : 0u;
  u32 wso = blk << (sh + 4);
  int wleft = 8;
  auto piece = [&]() {
    dma16(rw, wv0, wso, wld);
    wso += wstep; wld += 1024;
    wv0 ^= (wleft & 1) ? wflip : 0u;
    --wleft;
  };
  SB();
  STAMP(23);
  const int b = bid >> 3 & 1, head = xcd * 4 + (bid >> 6 ^ 3);
  const int g = lane >> 4, c = lane & 15;
  const int i8 = b | bid >> 6 << 1, wi = i8 * 8 + xcd;
  const bool is_up = p3 && has_w, modc = part == 2 && wi < 16;
  const size_t base = ((size_t)(b * NH + head) * SK + (SK - WIN)) * DH;
  const rsrc_t rk = mk_rsrc((const u16*)a_kc + base, WIN * DH * 2), rv = mk_rsrc((const u16*)a_vc + base, WIN * DH * 2);
  int tau = te1 & 0xffff;
  const int tauw = te0 >> 24 << 7, wdef = te0 >> 20 & 15;
  int due = P.NF * 256, now = 0;
  auto gate = [&]() {
    while (now < due) { now = (int)(((u32)wall_clock64() - t0) << 8); if (now < due) __builtin_amdgcn_s_sleep(1); }
    due += tau;
  };
  auto until = [&](int t) { while ((int)(((u32)wall_clock64() - t0) << 8) < t); };
  const u32 pslot = dup(G_P + head * 2048 + (b * 4 + part) * 16);
  const u32 die = __builtin_amdgcn_s_getreg(20 | (2 << 6));
  const bool voter = p3 && i8 == 1, timer = p3 && i8 < 2;
  int vote = 0, lag = 0, shift = 0;
  const u32 pbo = dup(G_PB) + (xcd * 8 + wave) * 128;
  u32 near = 0;
  u32x2 ep; u32 xs0, xs1, tch1; u32x4 xr0, xr1;
  const rsrc_t rx = mk_rsrc(part == 2 ? cnd : x, (part == 1 || (part == 2 && has_w)) ? 2048u : 0u);
  auto small_ops = [&]() {
    ep = ld8<AUX_SC1>(wsr, wave == 7 ? WS_EPOCH : OOB, 0);
    xs0 = __builtin_amdgcn_raw_buffer_load_b16(rx, tid * 2, 0, 0); xs1 = __builtin_amdgcn_raw_buffer_load_b16(rx, tid * 2, 1024, 0);
    xr0 = ld16<0>(rx, part == 1 ? lane * 16 : OOB, 0); xr1 = ld16<0>(rx, part == 1 ? lane * 16 : OOB, 1024);
    tch1 = __builtin_amdgcn_raw_buffer_load_b32(mk_rsrc(w2 + (size_t)blk * 32 * 512, is_up ? 64u : 0u), tid == 384 ? 0 : OOB, 0, AUX_NT);
  };
  if (has_w) {
    if (modc) now = 1 << 30;
#pragma nounroll
    while (wleft > wdef) { gate(); due += tauw - tau; piece(); }
    now = 0;
  }
  due = max(due, P.TK * 256);
  SB();
  u32 sot = sa_ & 0xffc00u;
  {
    const u32 s1 = (bl0 + lane - 1 - (lane >= SK0 ? NL - nl : 0)) ^ (124 + part);
#pragma unroll
    for (int k = 0; k < 7; ++k) sot ^= (u32)-(int)(s1 >> k & 1u) & col(P, k);
    PIN(sot);
  }
  u32 tag = 0;
  u32x4 bf[8];
  auto wo_prep = [&](int s0 = 0, int s1 = 2) {
    const u32 wi_ = wimg + wave * 8192;
    const int q = (lane >> 2) & 3, pp = lane & 3;
#pragma unroll
    for (int s = s0; s < s1; ++s) {
#pragma unroll
      for (int t = 0; t < 4; ++t) {
        const int r0 = s * 32 + g * 8 + q, r1 = r0 + 4, ch = t * 2 + (pp >> 1);
        const u32x2 lo = tr_read(wi_ + r0 * 128 + ((ch ^ (r0 & 7)) * 16) + (pp & 1) * 8), hi = tr_read(wi_ + r1 * 128 + ((ch ^ (r1 & 7)) * 16) + (pp & 1) * 8);
        bf[s * 4 + t] = u32x4{lo[0], lo[1], hi[0], hi[1]};
      }
    }
  };
  auto gemv_go = [&](u32 hh) {
    sm.hb[tid] = (u16)hh; sm.hb[512 + tid] = (u16)(hh >> 16);
    WAVE_SYNC();
    const u32x4* a = (c & 11) == 0 ? reinterpret_cast<const u32x4*>(&sm.hb[(c >> 2) * 512 + wave * 64 + g * 8]) : &sm.zero[0];
    const u32x4 a0 = a[0], a1 = a[(c & 11) == 0 ? 4 : 0];
    const f32x4 z = {0.f, 0.f, 0.f, 0.f};
    f32x4 d[4];
#pragma unroll
    for (int t = 0; t < 4; ++t) d[t] = mfma16(a0, bf[t], z);
#pragma unroll
    for (int t = 0; t < 4; ++t) d[t] = mfma16(a1, bf[4 + t], d[t]);
    if (lane < 32) {
#pragma unroll
      for (int t = 0; t < 4; ++t) sm.r2[(t * 16 + c) * 20 + g * 8 + wave] = d[t][0];
    }
  };
  auto mod_stage = [&]() {
    wo_prep();
    gemv_go(pack_bf(silu_f(lo_f(xs0)), silu_f(lo_f(xs1))));
    __builtin_amdgcn_s_waitcnt(0xc07f);
    if (lane == 0) *LDSP(volatile u32, (u32)(uintptr_t)&sm.mflag[wave]) = 1u;
    if (wave == 7) {
#pragma nounroll
      for (int spin = 0; spin < SPIN_MAX; ++spin) {
        const u32 f = *LDSP(volatile u32, (u32)(uintptr_t)&sm.mflag[lane & 7]);
        if (all_ok(f != 0u)) break;
        __builtin_amdgcn_s_sleep(1);
      }
      CFENCE();
      st8_sc1(wsr, G_MOD + (blk * 64 + lane) * 8, pack_bf(sum8(&sm.r2[lane * 20]), sum8(&sm.r2[lane * 20 + 8])), tag);
    }
    STAMP(9);
  };
  if (lane < 4) sm.zero[lane] = u32x4{0u, 0u, 0u, 0u};
  if (part == 2 && has_w) {
    if (tid < 8) sm.mflag[tid] = 0u;
    LDS_BARRIER();
    if (__builtin_expect(modc, 0)) {
      small_ops();
      __builtin_amdgcn_raw_buffer_store_b128(u32x4{SENT, SENT, SENT, SENT}, wsr, G_U + wi * 10240 + tid * 16, 0, AUX_SC1);
      __builtin_amdgcn_raw_buffer_store_b128(u32x4{SENT, SENT, SENT, SENT}, wsr, tid < 128 ? G_U + wi * 10240 + tid * 16 : OOB, 8192, AUX_SC1);
      asm volatile("s_waitcnt vmcnt(0)" ::: "memory");
      tag = next_tag(__builtin_amdgcn_readfirstlane(ep[0]));
      mod_stage();
      __builtin_amdgcn_s_barrier();
    }
  }
  SB();
  const int fd = wi * 8 + (lane & 3) * 2;
  const int ub = wave >> 2, ud = (wave & 3) * 128 + c * 8 + g * 2;
  u32x4 sg[3]; u32 sx2;
  auto side_loads = [&]() {
    const bool fin = part == 1 && wave == 0;
    sg[0] = ld16<AUX_SC1>(wsr, is_up ? G_MOD + (1024 + ud) * 8 : (fin ? G_MOD + (1024 + fd) * 8 : OOB), 0);
    sg[1] = ld16<AUX_SC1>(wsr, is_up ? G_MOD + (1536 + ud) * 8 : (fin ? G_MOD + (2560 + fd) * 8 : OOB), 0);
    sg[2] = ld16<AUX_SC1>(wsr, is_up ? G_MOD + (2048 + ud) * 8 : OOB, 0);
    sx2 = __builtin_amdgcn_raw_buffer_load_b32(mk_rsrc(x, (is_up || fin) ? 2048u : 0u), is_up ? ub * 1024 + ud * 2 : (g & 1) * 1024 + fd * 2, 0, 0);
  };
  auto side_pin = [&]() { PIN(sg[0]); PIN(sg[1]); PIN(sg[2]); PIN(sx2); };
  const u32 mlo = G_ML + (head * 2 + b) * 64 + (lane & 3) * 16;
  float fmrg = 0.f;
  auto merge_factor = [&](const u32x4& ml) {
    const float mi = u2f(ml[0]);
    float M = fmaxf(mi, dpp<XOR1>(mi)); M = fmaxf(M, dpp<XOR2>(M));
    const float e = __expf(mi - M);
    float L = e * u2f(ml[2]);
    L += dpp<XOR1>(L); L += dpp<XOR2>(L);
    fmrg = rdl(e * __builtin_amdgcn_rcpf(L), part);
  };
  f32x2 ga = {0.f, 0.f}, xin = {0.f, 0.f}, cc1 = {0.f, 0.f};
  float Af = 0.f, Bf = 0.f;
  const int hpos = ub * 512 + ud;
  auto up_prep = [&]() {
    if (!all_ok((sg[0][1] == tag) & (sg[0][3] == tag) & (sg[1][1] == tag) & (sg[1][3] == tag) & (sg[2][1] == tag) & (sg[2][3] == tag))) gather16<3>(wsr, G_MOD + (1024 + ud) * 8, 512 * 8, tag, sg);
    ga = f32x2{ub ? hi_f(sg[0][0]) : lo_f(sg[0][0]), ub ? hi_f(sg[0][2]) : lo_f(sg[0][2])};
    cc1 = f32x2{rbf(1.f + (ub ? hi_f(sg[2][0]) : lo_f(sg[2][0]))), rbf(1.f + (ub ? hi_f(sg[2][2]) : lo_f(sg[2][2])))};
    xin = f32x2{lo_f(sx2), hi_f(sx2)};
    u32* hp = reinterpret_cast<u32*>(&sm.hb[hpos]);
    hp[512] = pack_bf(cc1[0], cc1[1]);
    hp[1024] = ub ? perm_hi(sg[1][0], sg[1][2]) : perm_lo(sg[1][0], sg[1][2]);
  };
  auto up_frags = [&]() {
    const int pp = lane & 3;
    const u32 a = wimg + (pp & 1) * 32768 + ((pp >> 1) * 32 + g * 8 + ((lane >> 2) & 3)) * 64 + (((wave >> 1) ^ ((g & 1) | (pp & 2))) * 16) + (wave & 1) * 8;
#pragma unroll
    for (int rb = 0; rb < 8; ++rb) {
      const u32x2 lo = tr_read(a + rb * 4096), hi = tr_read(a + rb * 4096 + 256);
      bf[rb] = u32x4{lo[0], lo[1], hi[0], hi[1]};
    }
  };
  const u32x4* ap = reinterpret_cast<const u32x4*>(&sm.hb[((lane >> 3) & 1) * 512 + (lane & 1) * 32 + g * 8]);
  const bool par = lane & 8;
  auto limit = [&](auto sc) {
    constexpr int s = sc.v;
    WAIT_VM(window(s, 1 + (PRM0.W[s >> 3] >> (4 * (s & 7)) & 7)));
    SB();
  };
  auto sample = [&]() { if (timer) lag += (int)(((u32)wall_clock64() - t0) << 8) - due + ((int)__builtin_amdgcn_s_getreg(7 | (3 << 11)) + 1) * tau; };
  auto report = [&](u32 dst) {
    if (timer) {
      if (lane == 0) sm.mflag[wave] = vote * 65536 + min(lag >> 8, 4000);
      lag = 0;
      LDS_BARRIER();
      if (wave == 7) {
        int s = sm.mflag[lane & 7];
        s += __builtin_amdgcn_mov_dpp(s, XOR1, 0xf, 0xf, true); s += __builtin_amdgcn_mov_dpp(s, XOR2, 0xf, 0xf, true); s += __builtin_amdgcn_mov_dpp(s, XOR7, 0xf, 0xf, true);
        if (lane < 8) st8_sc1(wsr, dst + lane * 128 + wi * 8, s, tag);
      }
    }
    SB();
  };
  float l0 = 0.f, l1 = 0.f;
  u32x2 gmd[2], tbg;
  int t1 = 0;
#pragma unroll
  for (int i = SK0; i < SK1; ++i) kr[i] = u32x4{0u, 0u, 0u, 0u};
  const int first = SK0 + NL - nl;
  auto left_out = [&](int i) { int f = first; asm volatile("" : "+s"(f)); return i < f; };
  sfor<1, NL>([&](auto ic) {
    constexpr int i = ic.v;
    if constexpr (i >= SK0 && i < SK1) { if (left_out(i)) return true; }
    const u32 so = __builtin_amdgcn_readlane(sot, i);
    gate();
    if constexpr ((i >= TBS && i <= TBP) || (i >= RPS && i <= RPP)) sample();
    SB();
    if constexpr (i >= SK0 && i < SK1) ld16_into(kr[i], rk, lane * 16, so); else kr[i] = ld16<AUX_NT>(rk, lane * 16, so);
    if constexpr (i == SMO) small_ops();
    if constexpr (i == SMO + 1) { if (voter) __builtin_amdgcn_raw_buffer_store_b32(0u, wsr, pbo + (lane >> 5) * 8192 + (lane & 31) * 4, 0, AUX_SC1); }
    if constexpr (i == SK1) tbg = ld8<AUX_SC1>(wsr, wave == 7 ? G_VT + xcd * 128 + c * 8 : OOB, 0);
    if constexpr (i == TBL) tbg = ld8<AUX_SC1>(wsr, wave == 7 ? G_TB + xcd * 128 + c * 8 : OOB, 0);
    if constexpr (i == QP) { gmd[0] = ld8<AUX_SC1>(wsr, part == 1 ? G_MOD + tid * 8 : OOB, 0); gmd[1] = ld8<AUX_SC1>(wsr, part == 1 ? G_MOD + tid * 8 : OOB, 4096); }
    SB();
    SB();
    if (i >= 4) limit(ic);
    if (i == 1) STAMP(25);
    if (i == 8) STAMP(12);
    if (i == 20) STAMP(21);
    if (i == 32) STAMP(22);
    if (i == TBP) {
      if (timer) {
        if (lane == 0) sm.mflag[wave] = lag;
        lag = 0;
        LDS_BARRIER();
        if (wave == 7) {
          int s = sm.mflag[lane & 7];
          s += __builtin_amdgcn_mov_dpp(s, XOR1, 0xf, 0xf, true); s += __builtin_amdgcn_mov_dpp(s, XOR2, 0xf, 0xf, true); s += __builtin_amdgcn_mov_dpp(s, XOR7, 0xf, 0xf, true);
          if (lane < 8) st8_sc1(wsr, G_TB + lane * 128 + wi * 8, t0 << 10 | (u32)min(s / (2048 * (TBP - TBS + 1)), 1023), tag);
        }
      }
      SB();
    }
    if (i == PRB) {
      if (voter) {
        u64 ta, tb, tc; u32 dm;
        const u32 pa = pbo + (wave & 1) * 8192;
        asm volatile("s_memtime %0\n\ts_waitcnt lgkmcnt(0)\n\ts_load_dword %3, %4, %5\n\ts_waitcnt lgkmcnt(0)\n\ts_memtime %1\n\ts_waitcnt lgkmcnt(0)\n\t"
                     "s_load_dword %3, %4, %6\n\ts_waitcnt lgkmcnt(0)\n\ts_memtime %2\n\ts_waitcnt lgkmcnt(0)"
                     : "=&s"(ta), "=&s"(tb), "=&s"(tc), "=&s"(dm) : "s"(ws), "s"(pa), "s"(pa ^ 8192u));
        const int d = min(max((int)(2 * (u32)tb - (u32)ta - (u32)tc), -500), 500);
        vote = ((wave ^ die) & 1) ? -d : d;
        STAMP(26);
      }
      SB();
    }
    if (i == TBU) {
      if (wave == 7) {
        const bool ok = tbg[1] == tag;
        float lf = ok ? (float)min(max((int)((t0 << 10 | 1023u) - tbg[0]) >> 10, -300), 300) : 0.f, nf = ok ? 1.f : 0.f, lg = ok ? (float)(tbg[0] & 1023u) : 0.f;
        lf += dpp<XOR1>(lf); nf += dpp<XOR1>(nf); lg += dpp<XOR1>(lg); lf += dpp<XOR2>(lf); nf += dpp<XOR2>(nf); lg += dpp<XOR2>(lg);
        lf += dpp<XOR7>(lf); nf += dpp<XOR7>(nf); lg += dpp<XOR7>(lg); lf += dpp<XOR8>(lf); nf += dpp<XOR8>(nf); lg += dpp<XOR8>(lg);
        const float rn = __builtin_amdgcn_rcpf(fmaxf(nf, 1.f));
        shift = __builtin_amdgcn_readfirstlane(min(max((int)((lg * rn - (float)P.GS) * (float)P.GB * 0.125f), 0), 150));
        if (lane == 0) sm.bc = u32x2{tag, t0 - (u32)(int)(lf * rn) + shift};
      }
      LDS_BARRIER();
      const u32x2 bc = sm.bc;
      tag = __builtin_amdgcn_readfirstlane(bc[0]);
      t1 = (__builtin_amdgcn_readfirstlane((int)(bc[1] - t0)) + TEND + P.X1) * 256;
      tau -= __builtin_amdgcn_readfirstlane((int)((float)(int)(t0 - bc[1]) * 256.f * __builtin_amdgcn_rcpf((float)(nl - TBU - 1))));
      SB();
    }
    if (i == RPP) report(G_VT);
    if (i == LNS) {
      tag = next_tag(__builtin_amdgcn_readfirstlane(ep[0]));
      if (part == 1) {
        float x0[8], x1[8], mb0, ri0, mb1, ri1;
#pragma unroll
        for (int k = 0; k < 4; ++k) { x0[2 * k] = lo_f(xr0[k]); x0[2 * k + 1] = hi_f(xr0[k]); x1[2 * k] = lo_f(xr1[k]); x1[2 * k + 1] = hi_f(xr1[k]); }
        ln_stats2(x0, x1, mb0, ri0, mb1, ri1);
        l0 = rbf(rbf(lo_f(xs0) - mb0) * ri0); l1 = rbf(rbf(lo_f(xs1) - mb1) * ri1);
      }
      PIN(l0); PIN(l1);
      SB();
    }
    if (i == WUSE) {
      PIN(tch1); PIN(tch2);
      if ((part == 1 || (part == 2 && has_w && !modc))) {
        WAIT_VM(WK); CFENCE();
        if (part == 2) mod_stage(); else wo_prep();
      }
#pragma unroll
      for (int k = 0; k < 8; ++k) PIN(bf[k]);
      SB();
    }
    if (i == QP + QD) {
      if (part == 1) {
        u32 gm[2] = {gmd[0][0], gmd[1][0]};
        if (!all_ok((gmd[0][1] == tag) & (gmd[1][1] == tag))) { STAMP(11); gather8<2>(wsr, G_MOD + tid * 8, 512 * 8, tag, gm); }
        STAMP(5);
        gemv_go(pack_bf(rbf(l0 * rbf(1.f + lo_f(gm[1]))) + lo_f(gm[0]), rbf(l1 * rbf(1.f + hi_f(gm[1]))) + hi_f(gm[0])));
      }
      SB();
    }
    if (i == QP + QD + 1) {
      if (part == 1) {
        LDS_BARRIER();
        if (wave == 0) {
          const int bb = lane >> 5, jq = lane & 31;
          st8_sc1(wsr, G_Q + (bb * 2048 + blk * 32 + jq) * 8, pack_bf(sum8(&sm.r2[2 * jq * 20 + bb * 8]), sum8(&sm.r2[(2 * jq + 1) * 20 + bb * 8])), tag);
        }
        STAMP(6);
      }
      SB();
    }
    return true;
  });
  PIN(sot);
  if (wave == 7) {
    const bool ok = tbg[1] == tag;
    float vt = ok ? (float)((int)tbg[0] >> 16) : 0.f, lg = ok ? (float)(tbg[0] & 0xffffu) : 0.f, nf = ok ? 8.f * (RPP - RPS + 1) : 0.f;
    vt += dpp<XOR1>(vt); lg += dpp<XOR1>(lg); nf += dpp<XOR1>(nf); vt += dpp<XOR2>(vt); lg += dpp<XOR2>(lg); nf += dpp<XOR2>(nf);
    vt += dpp<XOR7>(vt); lg += dpp<XOR7>(lg); nf += dpp<XOR7>(nf); vt += dpp<XOR8>(vt); lg += dpp<XOR8>(lg); nf += dpp<XOR8>(nf);
    lg *= __builtin_amdgcn_rcpf(fmaxf(nf, 1.f));
    if (lane == 0) sm.bc2 = u32x2{(__float_as_uint(vt) & 0x80000000u) | (u32)min(shift * P.DG * 32, P.EW * 256), (u32)min(max((int)((lg - (float)P.B0) * (float)(P.BG * 32)) - shift * P.D0 * 32, 0), 200 * 256)};
  }
  LDS_BARRIER();
  {
    const u32x2 bc = sm.bc2;
    const int st = __builtin_amdgcn_readfirstlane(bc[1]), sp = (st * P.BF) >> 3;
    near = ((__builtin_amdgcn_readfirstlane(bc[0]) >> 31 ^ die ^ P.NR ^ 1) & 1) * 8192u;
    t1 += st + (__builtin_amdgcn_readfirstlane(bc[0]) & 0xffff);
    due += P.TG * 256 + sp;
    tau = (te1 >> 16) + __builtin_amdgcn_readfirstlane((int)((float)(st - sp) * __builtin_amdgcn_rcpf((float)(nl - 3))));
  }
  const u32 qoff = G_Q + ((b * 32 + head) * 64 + c * 4) * 8;
  u32x4 qg[2], mlg;
  u32 qq[4];
  constexpr int NB = (NL + 15) / 16;
  float xlo, sc[NB], pr[NB], m = 0.f;
  f32x4 acc = {0.f, 0.f, 0.f, 0.f};
  u32 pb[NB], pm[4], pl8[8], cpf;
  const u32 adst0 = lds_addr(lane < 32 ? &sm.red[wave][c * 8 + g * 4] : &sm.r2[(lane - 32) * 4]);
  u32 po, rdst, rsrc, xo;
  auto wo_addr = [&]() {
    po = dup(G_P + xcd * 8192) + tid * 16 + near;
    rdst = lane < 32 ? (u32)(uintptr_t)&sm.red2[wave][0] + lane * 16 : (u32)(uintptr_t)&sm.r2[0] + (lane - 32) * 16;
    rsrc = (u32)(uintptr_t)&sm.red2[0][((wave & 1) * 16 + (lane & 15)) * 4 + (lane >> 4)];
    xo = dup(G_XP + xcd * 2048 + (wave & 1) * 1024 + (blk * 64 + (lane & ~1)) * 2) + (lane & 1) * 8192;
    PIN(po); PIN(rdst); PIN(rsrc); PIN(xo);
  };
  u32 pso = (wave == 0 && (g & 1) == 0) ? pslot + (c >> 3) * 1024 + (c & 3) * 256 + (c >> 2 & 1) * 128 + (g >> 1) * 8192 : OOB;
  PIN(pso);
  const u32 nadd = wdef ? P.LW : (is_up ? P.LU : 0);
  u32 nto = tid < 2 ? dup(G_NT) + bid * 8 + tid * 8192 : OOB;
  PIN(nto);
  auto notices = [&]() -> int {
    u32 off = dup(G_NT) + near + lane * 16;
    PIN(off);
    until(due);
    int mx;
#pragma nounroll
    for (int spin = 0; spin < SPIN_MAX; ++spin) {
      CFENCE();
      const u32x4 ga_ = ld16<AUX_SC1>(wsr, off, 0), gb_ = ld16<AUX_SC1>(wsr, off, 1024);
      mx = max(max((int)(ga_[0] - t0), (int)(ga_[2] - t0)), max((int)(gb_[0] - t0), (int)(gb_[2] - t0)));
      if (all_ok((((ga_[1] ^ tag) | (ga_[3] ^ tag)) | ((gb_[1] ^ tag) | (gb_[3] ^ tag))) == 0u)) break;
      for (int k = 0; k < P.AG; ++k) __builtin_amdgcn_s_sleep(1);
    }
    if (all_ok(mx <= (t1 >> 8) - P.AC)) return t1;
    mx = max(mx, __builtin_amdgcn_mov_dpp(mx, XOR1, 0xf, 0xf, true)); mx = max(mx, __builtin_amdgcn_mov_dpp(mx, XOR2, 0xf, 0xf, true));
    mx = max(mx, __builtin_amdgcn_mov_dpp(mx, XOR7, 0xf, 0xf, true)); mx = max(mx, __builtin_amdgcn_mov_dpp(mx, XOR8, 0xf, 0xf, true));
    mx = max(max(__builtin_amdgcn_readlane(mx, 0), __builtin_amdgcn_readlane(mx, 16)), max(__builtin_amdgcn_readlane(mx, 32), __builtin_amdgcn_readlane(mx, 48)));
    return max(min(mx, 1 << 20) * 256, t1 + P.L0 * 256) + P.L1 * 256;
  };
  auto pv_row = [&](auto jc) {
    constexpr int i = jc.v;
    const u32 pv = __float_as_uint(bcast16(u2f(pb[i >> 4]), i & 15));
    acc = mfma16(u32x4{pv & pm[0], pv & pm[1], pv & pm[2], pv & pm[3]}, vr[i], acc);
    PIN(acc);
    return true;
  };
  sfor<0, NL - 1>([&](auto ic) {
    constexpr int v = ic.v;
    if constexpr (v >= SK0 && v < SK1) { if (left_out(v)) return true; }
    if constexpr (v > 0) {
      const u32 so = __builtin_amdgcn_readlane(sot, v);
      if constexpr (v >= SK1) { if (v == NL - P.NE) tau = (tau * P.FE) >> 4; }
      gate();
      if constexpr (v >= RVS && v <= RVP) sample();
      SB();
      if constexpr (v >= SK0 && v < SK1) ld16_into(vr[v], rv, lane * 16, so); else vr[v] = ld16<AUX_NT>(rv, lane * 16, so);
      SB();
      SB();
    }
    if constexpr (v == SK0 - 1) {
#pragma unroll
      for (int i = SK0; i < SK1; ++i) { vr[i] = u32x4{0u, 0u, 0u, 0u}; PIN(vr[i]); }
    }
    if constexpr (v == VQ - 4) { qg[0] = ld16<AUX_SC1>(wsr, qoff, 0); qg[1] = ld16<AUX_SC1>(wsr, qoff + 16, 0); }
    if constexpr (v == VSIDE) side_loads();
    if constexpr (v == CPF) {
      u64 pc; u32 dlt, len;
      asm volatile("s_getpc_b64 %0\n.Lcpf_here:\n\ts_mov_b32 %1, .Ltail_start-.Lcpf_here\n\ts_mov_b32 %2, .Lend_a-.Ltail_start\n\ts_max_i32 %2, %2, .Lend_b-.Ltail_start\n\ts_max_i32 %2, %2, .Lend_c-.Ltail_start\n\ts_max_i32 %2, %2, .Lend_d-.Ltail_start\n\ts_max_i32 %2, %2, .Lend_e-.Ltail_start" : "=s"(pc), "=s"(dlt), "=s"(len) :: "scc");
      pc += dlt;
      const rsrc_t rc = mk_rsrc((const void*)pc, (part == 3 && i8 == 1) ? len : 0u);
      cpf = __builtin_amdgcn_raw_buffer_load_b32(rc, tid * 64, 0, 0);
    }
    if constexpr (v == SK1 + 2) PIN(cpf);
    if constexpr (v == RVP) report(G_VT + 1024);
    if constexpr (v == RVL) tbg = ld8<AUX_SC1>(wsr, wave == 7 ? G_VT + 1024 + xcd * 128 + c * 8 : OOB, 0);
    if constexpr (v == SK1) mlg = ld16<AUX_SC1>(wsr, wave == 0 ? mlo : OOB, 0);
    SB();
    if constexpr (NL + v < 72) limit(IC<NL + v>{});
    if constexpr (v >= SK0 + WV) { WAIT_VM(v < SK1 ? WV : WV + 1); SB(); }
    if constexpr (v == VQ) {
      if (!all_ok((qg[0][1] == tag) & (qg[0][3] == tag) & (qg[1][1] == tag) & (qg[1][3] == tag))) { STAMP(20); gather16<2>(wsr, qoff, 16, tag, qg); }
      STAMP(1); WSTAMP(1);
      qq[0] = qg[0][0]; qq[1] = qg[0][2]; qq[2] = qg[1][0]; qq[3] = qg[1][2];
    }
    if constexpr (v >= VQ && v < VS) {
      constexpr int sl = v - VQ, bt = sl >> 1;
      float xs[8];
#pragma unroll
      for (int i = 0; i < 8; ++i) {
        float d = 0.f;
        if (sl * 8 + i < NL) {
#pragma unroll
          for (int k = 0; k < 4; ++k) d = dot2(kr[sl * 8 + i][k], qq[k], d);
        }
        xs[i] = d;
      }
#pragma unroll
      for (int h = 0; h < 4; ++h) xs[h] = sel_add<XOR7>(xs[h], xs[h + 4], c & 4);
#pragma unroll
      for (int h = 0; h < 2; ++h) xs[h] = sel_add<XOR2>(xs[h], xs[h + 2], c & 2);
      xs[0] = sel_add<XOR1>(xs[0], xs[1], c & 1);
      if constexpr ((sl & 1) || sl == NSL - 1) {
        const float t = (sl & 1) ? sel_add<XOR8>(xlo, xs[0], c & 8) : xs[0] + dpp<XOR8>(xs[0]);
        const float s = rbf(rbf(t) * 0.08838834764831845f);
        sc[bt] = (bt * 16 + c < SK0 || (bt * 16 + c >= first && bt * 16 + c < NL)) ? s : -__builtin_inff();
        PIN(sc[bt]);
      } else {
        xlo = xs[0];
        PIN(xlo);
      }
    }
    if constexpr (v == VS) {
      float mloc = sc[0];
#pragma unroll
      for (int bt = 1; bt < NB; ++bt) mloc = fmaxf(mloc, sc[bt]);
      m = fmaxf(wave_max(mloc), -3.0e38f);
      float ls = 0.f;
#pragma unroll
      for (int bt = 0; bt < NB; ++bt) { pr[bt] = __expf(sc[bt] - m); ls += pr[bt]; }
      const float l = wave_sum(ls);
      if (lane == 0) { sm.ml[wave][0] = m; sm.ml[wave][1] = l; }
      if (c == ((NL - 1) & 15)) sm.pl[g][wave] = pr[NB - 1];
      STAMP(2); WSTAMP(2);
    }
    if constexpr (v >= SK1) pv_row(IC<v - SK1 + SK0 - WV>{});
    if constexpr (v < SK0 || v >= SK0 + WV) sfor<cons_end(v - 1), cons_end(v)>(pv_row);
    if constexpr (v == RVU) {
      if (wave == 7) {
        const bool ok = tbg[1] == tag;
        float lg = ok ? (float)(tbg[0] & 0xffffu) : 0.f, nf = ok ? 8.f * (RVP - RVS + 1) : 0.f;
        lg += dpp<XOR1>(lg); nf += dpp<XOR1>(nf); lg += dpp<XOR2>(lg); nf += dpp<XOR2>(nf); lg += dpp<XOR7>(lg); nf += dpp<XOR7>(nf); lg += dpp<XOR8>(lg); nf += dpp<XOR8>(nf);
        lg *= __builtin_amdgcn_rcpf(fmaxf(nf, 1.f));
        if (lane == 0) sm.bc3 = (u32)min(max((int)((lg - (float)P.F0) * (float)(P.FG * 32)), 0), 200 * 256);
      }
      LDS_BARRIER();
      const int st = __builtin_amdgcn_readfirstlane(sm.bc3), sp = (st * P.FF_) >> 3;
      t1 += st;
      due += sp;
      tau += __builtin_amdgcn_readfirstlane((int)((float)(st - sp) * __builtin_amdgcn_rcpf((float)(nl - RVU - 3))));
    }
    if constexpr (v == SK1 + 1) {
      float p[8];
#pragma unroll
      for (int w = 0; w < 8; ++w) p[w] = sm.pl[g][w] * __expf(sm.ml[w][0] - m);
#pragma unroll
      for (int w = 0; w < 8; ++w) pl8[w] = pack_bf(p[w], p[w]);
#pragma unroll
      for (int w = 0; w < 8; ++w) PIN(pl8[w]);
    }
    if constexpr (v == MLS - 1) { side_pin(); if (is_up) up_prep(); PIN(ga); PIN(xin); PIN(cc1); }
    if constexpr (v == UPREP - 1) wo_addr();
    if constexpr (v == UPREP) {
#pragma unroll
      for (int k = 0; k < 8; ++k) asm volatile("" : "=v"(bf[k]));
      if (part == 0) wo_prep(); else if (is_up) up_frags();
#pragma unroll
      for (int k = 0; k < 8; ++k) PIN(bf[k]);
    }
    if constexpr (v == UPREP + 1) {
      if (is_up) {
        const u32x4* a1 = ap + 128 + ((lane >> 1) & 1) * 128;
        f32x4 d0 = {0.f, 0.f, 0.f, 0.f}, d1 = {0.f, 0.f, 0.f, 0.f};
#pragma unroll
        for (int rb = 0; rb < 4; ++rb) { d0 = mfma16(a1[rb * 8], bf[rb], d0); d1 = mfma16(a1[(rb + 4) * 8], bf[rb + 4], d1); }
        Af = par ? d0[1] + d1[1] : d0[0] + d1[0]; Bf = par ? d0[3] + d1[3] : d0[2] + d1[2];
        Af += dpp<XOR8>(Af); Bf += dpp<XOR8>(Bf);
      }
      PIN(Af); PIN(Bf);
    }
    if (v == 10 || v == 16) STAMP(15 + v / 6);
    if (v == 20) STAMP(18);
    if (v == 32) STAMP(19);
    if constexpr (v == MLS) {
      LDS_BARRIER();
      float mm[8], ll[8];
#pragma unroll
      for (int w = 0; w < 8; ++w) { const f32x2 t = *reinterpret_cast<const f32x2*>(&sm.ml[w][0]); mm[w] = t[0]; ll[w] = t[1]; }
      float Mwg = mm[0], Lwg = 0.f;
#pragma unroll
      for (int w = 1; w < 8; ++w) Mwg = fmaxf(Mwg, mm[w]);
      if (wave == 0) {
#pragma unroll
        for (int w = 0; w < 8; ++w) Lwg = fmaf(__expf(mm[w] - Mwg), ll[w], Lwg);
        if (lane == 0) st16_sc1(wsr, G_ML + ((head * 2 + b) * 4 + part) * 16, __float_as_uint(Mwg), __float_as_uint(Lwg), tag);
      }
      const float fw = __expf(m - Mwg);
      m = Mwg;
#pragma unroll
      for (int bt = 0; bt < NB; ++bt) { pb[bt] = pack_bf(pr[bt] * fw, pr[bt] * fw); PIN(pb[bt]); }
#pragma unroll
      for (int k = 0; k < 4; ++k) { pm[k] = (c >> 1 & 3) == k ? ((c & 1) ? 0xffff0000u : 0x0000ffffu) : 0u; PIN(pm[k]); }
    }
    SB();
    return true;
  });
  asm volatile(".Ltail_start:");
  if (wave != 0) {
    pv_row(IC<SK0 - 1>{});
    WAIT_VM(WV); SB();
    sfor<NL - 1 - WV, NL - 1>(pv_row);
    *LDSP(f32x4, adst0) = acc;
    WSTAMP(14);
    LDS_BARRIER();
  } else {
    u32x4 xr[8], am[8];
#pragma unroll
    for (int w = 0; w < 8; ++w) am[w] = u32x4{pl8[w] & pm[0], pl8[w] & pm[1], pl8[w] & pm[2], pl8[w] & pm[3]};
#pragma unroll
    for (int w = 0; w < 8; ++w) PIN(am[w]);
    {
      const u32 so = __builtin_amdgcn_readlane(sot, NL - 1);
      gate();
      SB();
#pragma unroll
      for (int w = 0; w < 8; ++w) xr[w] = ld16<AUX_NT>(rv, lane * 16, so ^ ((w & 1) ? col(P, 7) : 0u) ^ ((w & 2) ? col(P, 8) : 0u) ^ ((w & 4) ? col(P, 9) : 0u));
      SB();
    }
    u32 rsum = lds_addr(&sm.red[0][c * 8 + (g & 1) * 4]);
    pv_row(IC<SK0 - 1>{});
    WAIT_VM(WV + 8); SB();
    sfor<NL - 1 - WV, NL - 3>(pv_row);
    if (__builtin_expect(!all_ok((mlg[1] == tag) & (mlg[3] == tag)), 0)) {
#pragma nounroll
      for (int spin = 0; spin < SPIN_MAX; ++spin) {
        CFENCE();
        mlg = ld16<AUX_SC1>(wsr, mlo, 0);
        if (all_ok((mlg[1] == tag) & (mlg[3] == tag))) break;
      }
    }
    merge_factor(mlg);
    sfor<NL - 3, NL - 1>(pv_row);
    STAMP(14); WSTAMP(14);
    LDS_BARRIER();
    STAMP(15);
    f32x4 t[8];
#pragma unroll
    for (int w = 1; w < 8; ++w) t[w] = LDSP(f32x4, rsum)[w * 32];
    acc = ((acc + t[1]) + (t[2] + t[3])) + ((t[4] + t[5]) + (t[6] + t[7]));
    PIN(acc);
    STAMP(3);
    SB();
#pragma unroll
    for (int w = 0; w < 8; ++w) acc = mfma16(am[w], xr[w], acc);
  }
  WAIT_VM(0);
  acc *= fmrg;
  {
    const u32 w0 = min(pack_bf(acc[0], acc[1]), SENT - 1u), w1 = min(pack_bf(acc[2], acc[3]), SENT - 1u);
    const auto e0 = __builtin_amdgcn_permlane16_swap(w0, w0, false, false), e1 = __builtin_amdgcn_permlane16_swap(w1, w1, false, false);
    __builtin_amdgcn_raw_buffer_store_b128(u32x4{e0[0], e1[0], e0[1], e1[1]}, wsr, pso, 0, AUX_SC1);
  }
  st8_sc1(wsr, nto, (u32)wall_clock64() + nadd, tag);
  SB();
  STAMP(4);

  if (part == 2) FINISH();
  if (part == 0) {
    __builtin_amdgcn_s_barrier();
#pragma nounroll
    while (wleft > 0) { until(t1 - (P.PA + wleft * P.PD) * 256); piece(); }
    bool late = false;
    u32x4 og[2];
    if (wdef) {
      late = true;
      while ((int)(((u32)wall_clock64() - t0) << 8) < t1 - 20 * 256) if (__builtin_amdgcn_s_getreg(7 | (3 << 11)) == 0) { late = false; break; }
      if (__builtin_expect(late, 0)) { until(t1); CFENCE(); og[0] = ld16<AUX_SC1>(wsr, po, 0); SB(); WAIT_VM(1); } else { WAIT_VM(0); }
      CFENCE();
      if (P.WD > 4) wo_prep(0, 1);
      wo_prep(1, 2);
#pragma unroll
      for (int k = 0; k < 8; ++k) PIN(bf[k]);
      SB();
    }
    if (__builtin_expect(!late, 1)) { until(t1); CFENCE(); og[0] = ld16<AUX_SC1>(wsr, po, 0); }
    STAMP(11);
    SB();
    f32x4 d[4];
    auto wo_mm = [&]() {
      const f32x4 z = {0.f, 0.f, 0.f, 0.f};
#pragma unroll
      for (int t = 0; t < 4; ++t) { d[t] = mfma16(og[0], bf[t], z); og[1][t] = __float_as_uint(dpp<XOR8>(u2f(og[0][t]))); }
      SB();
#pragma unroll
      for (int t = 0; t < 4; ++t) d[t] = mfma16(og[1], bf[4 + t], d[t]);
    };
    auto p_bad = [&]() { return !all_ok(max(max(og[0][0], og[0][1]), max(og[0][2], og[0][3])) != SENT); };
    wo_mm();
    if (__builtin_expect(p_bad(), 0)) {
#pragma nounroll
      for (int spin = 0; spin < SPIN_MAX; ++spin) {
        __builtin_amdgcn_s_sleep(1);
        CFENCE();
        og[0] = ld16<AUX_SC1>(wsr, po, 0);
        if (!p_bad()) break;
      }
      wo_mm();
      SB();
      asm volatile(".Lend_d:");
    }
    STAMP(5); WSTAMP(5);
    {
      SB();
      f32x4 y; f32x2 h[4];
#pragma unroll
      for (int t = 0; t < 4; ++t) { h[t] = d[t].xy + d[t].zw; PIN(h[t]); }
#pragma unroll
      for (int t = 0; t < 4; ++t) { y[t] = h[t][0] + h[t][1]; PIN(y[t]); }
      *LDSP(f32x4, rdst) = y;
    }
    STAMP(16);
    LDS_BARRIER();
    STAMP(17);
    if (wave < 2) {
      f32x2 v[4];
#pragma unroll
      for (int w = 0; w < 4; ++w) v[w] = f32x2{LDSP(float, rsrc)[w * 256], LDSP(float, rsrc)[w * 256 + 128]};
      const f32x2 h = (v[0] + v[1]) + (v[2] + v[3]);
      const float y = h[0] + h[1];
      __builtin_amdgcn_raw_buffer_store_b32(min(pack_bf(dpp<0xA0>(y), dpp<0xF5>(y)), SENT - 1u), wsr, xo, 0, AUX_SC1);
      asm volatile(".Lend_a:");
    }
    STAMP(6);
    FINISH();
  }
  if (part == 3) {
    if (!has_w) FINISH();
    u32x4 b2[4];
    {
      __builtin_amdgcn_s_barrier();
      const rsrc_t r2 = mk_rsrc(w2 + (size_t)(blk * 32 + wave * 4) * 512, 4096u);
      const u32 l2 = (u32)(uintptr_t)(&sm.w2b[wave * 4][0]);
#pragma unroll
      for (int n = 0; n < 4; ++n) { until(t1 - (P.QA + (4 - n) * P.QD) * 256); dma16(r2, (lane ^ (n * 2) ^ ((wave & 2) << 2)) * 16, n * 1024, l2 + n * 1024); }
    }
    const u32 tdst = lds_addr(&sm.hb[hpos]);
    const u32 sdst = lds_addr(&sm.stat[wave][lane]);
    const u32 ssrc = lds_addr(&sm.stat[(g >> 1) * 4 + ((lane >> 3) & 3)][(lane & 7) * 4]);
    const u32 adst = lds_addr((c < 4 && (g & 1) == 0) ? &sm.actl[g >> 1][wave * 4 + c] : reinterpret_cast<u16*>(&sm.dummy[lane]));
    const u32 asrc = lds_addr(&sm.actl[lane & 1][g * 8]);
    const u32 apa = lds_addr(&sm.hb[(c >> 3) * 512 + (c & 3) * 64 + ((c >> 2) & 1) * 32 + g * 8]);
    constexpr u32 xs2 = 16384;
    u32 xoff = dup(G_XP) + near + g * 2048 + ub * 1024 + (wave & 3) * 256 + c * 16;
    u32x4 one;
#pragma unroll
    for (int k = 0; k < 4; ++k) one[k] = ((c & 2) == 0 && (c >> 2) == k) ? ((c & 1) ? 0x3f800000u : 0x00003f80u) : 0u;
    PIN(one);
    u32 ooff = dup(G_OP + (blk * 512 + tid) * 8);
    float Afp = Af, Bfp = Bf;
    PIN(xoff); PIN(ooff); PIN(Afp); PIN(Bfp);
    f32x2 gc = ga * cc1, xc = xin * cc1;
    PIN(ga); PIN(xin); PIN(gc); PIN(xc);
    u32 w2a = (u32)(uintptr_t)(&sm.w2b[0][0]) + (g * 8 + ((lane >> 2) & 3)) * 1024 + (wave ^ (g & 1)) * 128 + (lane & 15) * 8;
    PIN(w2a);
    if (wave == 1) {
      due = t1 + P.N1 * 256;
      const int tn = notices();
      due = tn + P.X2 * 256;
      until(due);
    }
    asm volatile("s_waitcnt vmcnt(0)" ::: "memory");
    STAMP(9);
    __builtin_amdgcn_s_barrier();
    STAMP(11);
    CFENCE();
    u32x4 xg[2] = {ld16<AUX_SC1>(wsr, xoff, 0), ld16<AUX_SC1>(wsr, xoff, xs2)};
    SB();
#pragma unroll
    for (int t = 0; t < 4; ++t) {
      const u32x2 lo = tr_read(w2a ^ (t * 32)), hi = tr_read((w2a ^ (t * 32)) + 4096);
      b2[t] = u32x4{lo[0], lo[1], hi[0], hi[1]};
    }
#pragma unroll
    for (int t = 0; t < 4; ++t) PIN(b2[t]);
    SB();
    const f32x4 z4 = {0.f, 0.f, 0.f, 0.f};
    auto xp_bad = [&]() { return !all_ok(max(max(max(xg[0][0], xg[0][1]), max(xg[0][2], xg[0][3])), max(max(xg[1][0], xg[1][1]), max(xg[1][2], xg[1][3]))) != SENT); };
    f32x4 v = mfma16(one, xg[1], mfma16(one, xg[0], z4));
    if (__builtin_expect(xp_bad(), 0)) {
#pragma nounroll
      for (int spin = 0; spin < SPIN_MAX; ++spin) {
        __builtin_amdgcn_s_sleep(1);
        CFENCE();
        xg[0] = ld16<AUX_SC1>(wsr, xoff, 0); xg[1] = ld16<AUX_SC1>(wsr, xoff, xs2);
        if (!xp_bad()) break;
      }
      v = mfma16(one, xg[1], mfma16(one, xg[0], z4));
      SB();
      asm volatile(".Lend_e:");
    }
    STAMP(5); WSTAMP(5);
    {
      const f32x2 a = v.xy, hc = __builtin_elementwise_fma(gc, a, xc), x1 = __builtin_elementwise_fma(ga, a, xin);
      *LDSP(u32, tdst) = pack_bf(hc[0], hc[1]);
      *LDSP(float, sdst) = swap32_add(x1[0] + x1[1], fmaf(x1[0], x1[0], x1[1] * x1[1]));
    }
    STAMP(16);
    LDS_BARRIER();
    STAMP(17);
    {
      const f32x4 z = {0.f, 0.f, 0.f, 0.f};
      const u32x4 af0 = LDSP(u32x4, apa)[0];
      SB();
      const f32x4 s4 = LDSP(f32x4, ssrc)[0], q4 = LDSP(f32x4, ssrc)[8];
      const u32x4 af1 = LDSP(u32x4, apa)[32];
      SB();
      f32x4 d[4];
      d[0] = mfma16(af0, bf[0], z); d[1] = mfma16(af0, bf[1], z);
      SB();
      const f32x2 s2 = s4.xy + s4.zw, q2 = q4.xy + q4.zw;
      float s = s2[0] + s2[1], q = q2[0] + q2[1];
      SB();
      d[2] = mfma16(af0, bf[2], z);
      SB();
      s = swap16_add(s, q);
      SB();
      d[3] = mfma16(af0, bf[3], z);
      SB();
      s += dpp<XOR1>(s); s += dpp<XOR2>(s);
      SB();
      d[0] = mfma16(af1, bf[4], d[0]);
      SB();
      s += dpp<XOR7>(s); s += dpp<XOR8>(s);
      SB();
      d[1] = mfma16(af1, bf[5], d[1]);
      SB();
      const auto sq = __builtin_amdgcn_permlane16_swap(__float_as_uint(s), __float_as_uint(s), false, false);
      const float mean = u2f(sq[0]) * (1.f / 512.f);
      SB();
      d[2] = mfma16(af1, bf[6], d[2]);
      SB();
      const float var = fmaf(u2f(sq[1]), 1.f / 512.f, -mean * mean);
      SB();
      d[3] = mfma16(af1, bf[7], d[3]);
      SB();
      float ri = __builtin_amdgcn_rsqf(fmaxf(var, 0.f) + 1e-5f);
      float cf = fmaf(-ri * mean, Afp, Bfp);
      float rk = ri * -1.44269504f, ck = cf * -1.44269504f;
      PIN(ri); PIN(cf); PIN(rk); PIN(ck);
      SB();
      PIN(d[0]); PIN(d[1]);
      float y = d[0][0] + d[1][1];
      PIN(y); PIN(d[2]);
      y += d[2][2];
      PIN(y); PIN(d[3]);
      y += d[3][3];
      y = __builtin_bit_cast(float, __builtin_amdgcn_update_dpp(__builtin_bit_cast(int, y), __builtin_bit_cast(int, y), XOR8, 0xa, 0xf, false));
      y = swap16_add(y, y);
      const float ex = __builtin_amdgcn_exp2f(fmaf(y, rk, ck));
      y = fmaf(y, ri, cf);
      const float gu = y * dpp<0x104>(y);
      *LDSP(u16, adst) = (u16)pack_bf(gu * __builtin_amdgcn_rcpf(1.f + ex), 0.f);
    }
    STAMP(18);
    LDS_BARRIER();
    STAMP(10);
    {
      const u32x4 af = *LDSP(u32x4, asrc);
      const f32x4 z = {0.f, 0.f, 0.f, 0.f};
      f32x4 d0 = mfma16(af, b2[0], z), d1 = mfma16(af, b2[1], z), d2 = mfma16(af, b2[2], z), d3 = mfma16(af, b2[3], z);
      asm volatile("" : "+v"(d0), "+v"(d1), "+v"(d2), "+v"(d3));
      const bool g1 = g & 1, g2 = g & 2;
      const float e0 = g1 ? d1[0] : d0[0], e1 = g1 ? d1[1] : d0[1], f0 = g1 ? d3[0] : d2[0], f1 = g1 ? d3[1] : d2[1];
      const u32x2 ov = {pack_bf(g2 ? f0 : e0, g2 ? f1 : e1), tag};
      __builtin_amdgcn_raw_buffer_store_b64(ov, wsr, ooff, near ^ 8192u, AUX_SC1);
      __builtin_amdgcn_raw_buffer_store_b64(ov, wsr, ooff, near, AUX_SC1);
    }
    asm volatile(".Lend_b:");
    STAMP(6);
    FINISH();
  }
  if (wave == 0) {
    if (!all_ok((sg[0][1] == tag) & (sg[0][3] == tag) & (sg[1][1] == tag) & (sg[1][3] == tag))) {
      u32x4 t[2];
      gather16<2>(wsr, G_MOD + (1024 + fd) * 8, 1536 * 8, tag, t);
      sg[0] = t[0]; sg[1] = t[1];
    }
    u32 xa;
    {
      due = t1 + P.N2 * 256;
      const int tn = notices();
      due = tn + P.X3 * 256;
      until(due);
    }
    const bool hb = g & 1;
#pragma nounroll
    for (int spin = 0; spin < SPIN_MAX; ++spin) {
      CFENCE();
      xa = __builtin_amdgcn_raw_buffer_load_b32(wsr, dup(G_XP) + near + (g >> 1) * 16384 + (c >> 2) * 2048 + (g & 1) * 1024 + fd * 2, 0, AUX_SC1);
      if (all_ok(xa != SENT)) break;
      __builtin_amdgcn_s_sleep(1);
    }
    float a0 = lo_f(xa), a1 = hi_f(xa);
    a0 += dpp<0x124>(a0); a1 += dpp<0x124>(a1);
    a0 += dpp<XOR8>(a0); a1 += dpp<XOR8>(a1);
    a0 = swap32_add(a0, a0); a1 = swap32_add(a1, a1);
    f32x2 xv = {rbf(lo_f(sx2) + rbf((hb ? hi_f(sg[0][0]) : lo_f(sg[0][0])) * rbf(a0))), rbf(hi_f(sx2) + rbf((hb ? hi_f(sg[0][2]) : lo_f(sg[0][2])) * rbf(a1)))};
    f32x2 gv = {hb ? hi_f(sg[1][0]) : lo_f(sg[1][0]), hb ? hi_f(sg[1][2]) : lo_f(sg[1][2])};
    PIN(xv); PIN(gv);
    if (wi == 0 && lane == 0) __builtin_amdgcn_raw_buffer_store_b32(tag, wsr, WS_EPOCH, 0, AUX_SC1);
    STAMP(10);
    due = max(due + P.X4 * 256, (int)(((u32)wall_clock64() - t0) << 8) + P.GX * 256);
    const bool last = lane < (NUP - 32) * 4;
    u32 o0 = dup(G_OP + (lane >> 2) * 4096 + fd * 8) + near, o2 = last ? o0 : OOB, tg2 = last ? tag : 0u;
    const rsrc_t rout = mk_rsrc(out, 2048u);
    u32 oo = (lane & 44) == 0 ? (g * 512 + fd) * 2 : OOB;
    const u32 pk = (c & 10) ? 0u : ((c & 4) ? 0x3f800000u : 0x00003f80u);
    u32x4 one = {(c & 1) ? 0u : pk, 0u, (c & 1) ? pk : 0u, 0u};
    PIN(o0); PIN(o2); PIN(tg2); PIN(oo); PIN(one);
    __builtin_amdgcn_raw_buffer_store_b32(pack_bf(xv[0], xv[1]), rout, oo, 0, AUX_SC1);
    u32 s1_ = 131072, s2_ = 262144;
    asm volatile("" : "+s"(s1_), "+s"(s2_));
    until(due);
    f32x4 sv;
#pragma nounroll
    for (int spin = 0; spin < SPIN_MAX; ++spin) {
      CFENCE();
      const u32x4 og[3] = {ld16<AUX_SC1>(wsr, o0, 0), ld16<AUX_SC1>(wsr, o0, s1_), ld16<AUX_SC1>(wsr, o2, s2_)};
      sv = f32x4{0.f, 0.f, 0.f, 0.f};
#pragma unroll
      for (int k = 0; k < 3; ++k) sv = mfma16(one, og[k], sv);
      SB();
      u32 bad = ((og[0][1] ^ tag) | (og[0][3] ^ tag) | (og[1][1] ^ tag)) | ((og[1][3] ^ tag) | (og[2][1] ^ tg2) | (og[2][3] ^ tg2));
      PIN(bad);
      SB();
      float s0 = sv[0] + dpp<0x124>(sv[0]), s1 = sv[1] + dpp<0x124>(sv[1]);
      PIN(s0); PIN(s1);
      s0 += dpp<XOR8>(s0); s1 += dpp<XOR8>(s1);
      PIN(s0); PIN(s1);
      __builtin_amdgcn_raw_buffer_store_b32(pack_bf(fmaf(gv[0], s0, xv[0]), fmaf(gv[1], s1, xv[1])), rout, oo, 0, AUX_SC1);
      SB();
      if (all_ok(bad == 0u)) break;
      __builtin_amdgcn_s_sleep(1);
    }
    STAMP(7);
    asm volatile(".Lend_c:");
  }
  FINISH();
}

extern "C" void kernel_launch(void* const* d_in, const int* in_sizes, int n_in,
                              void* d_out, int out_size, void* d_ws, size_t ws_size,
                              hipStream_t stream) {
  fused_kernel<<<256, 512, 0, stream>>>((const u16*)d_in[1], (const u16*)d_in[2], (const u16*)d_in[4], (const u16*)d_in[3], (const u16*)d_in[10], (const u16*)d_in[6], (const u16*)d_in[7],
                                        (unsigned char*)d_ws, (const u16*)d_in[0], (const u16*)d_in[9], (const u16*)d_in[8], (u16*)d_out);
}
```

```cpp
#include <hip/hip_runtime.h>
#include <stdint.h>


typedef unsigned int u32;
typedef unsigned short u16;
typedef unsigned long long u64;
typedef __attribute__((ext_vector_type(4))) unsigned u32x4;
typedef __attribute__((ext_vector_type(2))) unsigned u32x2;
typedef __attribute__((ext_vector_type(2))) __bf16 bf16x2;
typedef __attribute__((ext_vector_type(4))) float f32x4;
typedef __attribute__((ext_vector_type(2))) float f32x2;
typedef __amdgpu_buffer_rsrc_t rsrc_t;

constexpr int NH = 32, SK = 16384, DH = 128, WIN = 4096, FF = 1376;
struct Prm { int E0, ES, E1, E2, E2C, EU, TEND, DT_UP, L0, L1, X1, X2, X3, X4, NE, FE, M0, M1, M2, M3, M4, GS, GX, N1, N2, GA, WD, DT_WO, LW, LU, EW, PM, NF, NR, C0, A0, AG, AC, TS, TW, TK, TG, GB, B0, BG, BF, D0, DG, TC, LM, C1, F0, FG, FF_, PA, PD, QA, QD, RT, W[9]; };
constexpr Prm PRM0 = {32, 20, 32, 32, 60, 60, 1974, 70, 12, 30, 83, 66, 100, 80, 3, 9, 0x2511f55b, 0x2b7425d5, 0x6017665, 0x17e0b9ec, 0x31944305, 62, 30, 32, 62, 50, 4, 100, 45, 10, 10, 0x0, 12, 14, 3, 30, 4, 4, 511, 222, 90, 2, 6, 69, 10, 8, 8, 6, 87, 120, 2, 65, 6, 4, 62, 14, 6, 15, 24, 0x77777777, 0x77777777, 0x77777777, 0x77777777, 0x77777777, 0x44444477, 0x44444444, 0x44444444, 0x44444444};
constexpr int NL = 37;
constexpr int SK0 = 21, SK1 = 32;
constexpr int NCMIN = (NL - SK1 + SK0) * 8;
constexpr int WK = 8, WV = 5;
constexpr int WUSE = 12, LNS = 7;
constexpr int QP = 14, QD = 4;
constexpr int VQ = 6;
constexpr int NSL = (NL + 7) / 8;
constexpr int VS = VQ + NSL;
constexpr int MLS = VS + 2;
constexpr int VSIDE = MLS - 5, UPREP = MLS + 6;
constexpr int CPF = 17;
constexpr int SMO = 1;
constexpr int RVS = 5, RVP = 8, RVL = 12, RVU = 16;
constexpr int RPS = 24, RPP = 28;
constexpr int PRB = 12;
constexpr int TBS = 7, TBP = 10, TBL = 13, TBU = 17;
constexpr int window(int s, int w) {
  int n = w;
  for (int u = s - w + 1; u <= s; ++u) n += u == SMO ? 6 : (u == TBL || u == SK1 || u == NL + CPF || u == NL + RVL || u == NL + SK1 ? 1 : (u == QP || u == NL + VQ - 4 ? 2 : (u == NL + VSIDE ? 4 : 0)));
  return n;
}
constexpr int cons_end(int v) {
  const int a = v - WV + 1, c = (v - MLS) * 4;
  const int m = a < c ? a : c;
  return m < 0 ? 0 : (m > NL ? NL : m);
}
constexpr u32 col(const Prm& P, int j) { const int m[6] = {P.M0, P.M1, P.M2, P.M3, P.M4, P.PM}; return (u32)(m[j / 3] >> (j % 3 * 10) & 1023) << 10; }
constexpr u32 first_col(const Prm& P, int k) {
  if (k < 4) return col(P, 12 + k) | 1u << (24 + k);
  if (k < 6) return col(P, k - 4);
  if (k < 8) return col(P, 4 + k) | 1u << (16 + k);
  if (k < 11) return col(P, k - 1);
  return (col(P, 2) ^ col(P, 3) ^ col(P, 4) ^ col(P, 5) ^ col(P, 6) ^ col(P, 10) ^ col(P, 11)) | 0xf00000u;
}
constexpr bool one_to_one(const Prm& P) {
  u32 c[10] = {};
  for (int j = 0; j < 10; ++j) c[j] = col(P, j);
  for (int j = 0; j < 10; ++j) {
    if (c[j] == 0) return false;
    const u32 low = c[j] & (0u - c[j]);
    for (int k = j + 1; k < 10; ++k) if (c[k] & low) c[k] ^= c[j];
  }
  return true;
}
static_assert(one_to_one(PRM0), "every KB of the window exactly once");
struct Tab { u32 e[512]; };
constexpr Tab make_tab(const Prm& P) {
  Tab T{};
  for (int bid = 0; bid < 256; ++bid) {
    const int xcd = bid & 7, part = bid >> 4 & 3, i8 = (bid >> 3 & 1) | bid >> 6 << 1, wi = i8 * 8 + xcd;
    const bool has_mod = wi < 48, has_up = wi >= 21;
    const int cb0 = (i8 + P.C0) & 7, cb1 = (i8 + P.C1) & 7, ms = wi - 20;
    const int mi = wi < 8 ? wi : (wi < 16 ? 8 + ((xcd + 4) & 7) : (wi < 20 ? (wi - 14) * 8 + 3 : 16 + ms / 7 * 8 + ms % 7 + (ms % 7 >= 3 ? 1 : 0)));
    const int ui = xcd * 5 + (xcd > 5 ? xcd - 5 : 0) + (i8 == 6 ? 0 : (xcd >= 5 ? 1 : 0) + (i8 == 7 ? 4 : i8 - 2));
    const bool edge = i8 >= 6;
    const int wd = cb0 == 3 ? 0 : P.WD;
    const int e[4] = {P.E0 - 4 * wd + (cb0 == 3 ? P.ES : 0), P.E1 + (cb1 == 3 ? P.ES : 0), has_mod ? (wi < 16 ? P.E2C : P.E2) + ((mi & 7) == 3 ? P.ES : 0) : 0, has_up ? P.EU : 0};
    int tw[4] = {}, win[4] = {}, span[4] = {}, k0[4] = {};
    for (int k = 0; k < 4; ++k) {
      const int np = k == 0 ? 8 - wd : 8;
      tw[k] = np ? e[k] * P.TW / (16 * np) : 0;
      k0[k] = P.NF + tw[k] * np / 2 > P.TK ? P.NF + tw[k] * np / 2 : P.TK;
      span[k] = P.TEND - k0[k] - P.TG - (k == 0 ? (wd ? P.DT_WO : P.A0) : (k == 3 && has_up ? P.DT_UP : 0));
      win[k] = span[k] - (k == 3 && has_up ? P.GA + (edge ? P.LM : 0) : 0);
    }
    int n[4] = {NCMIN / 8, NCMIN / 8, NCMIN / 8, NCMIN / 8};
    for (int left = 128 - NCMIN / 2; left > 0; --left) {
      int best = -1;
      for (int k = 3; k >= 0; --k)
        if (n[k] < NL && (best < 0 || n[k] * win[best] < n[best] * win[k])) best = k;
      ++n[best];
    }
    const int c[4] = {0, n[0] - 1, n[0] + n[1] - 2, n[0] + n[1] + n[2] - 3};
    const int nl = n[part];
    const bool has_w = part < 2 || (part == 2 ? has_mod : has_up);
    const int blk = !has_w ? 0 : (part == 0 ? cb0 : (part == 1 ? xcd * 8 + cb1 : (part == 2 ? mi : ui)));
    T.e[2 * bid] = (u32)(c[part] | nl << 7 | blk << 13 | (has_w ? 1 : 0) << 19 | (part == 0 ? wd : 0) << 20) | (u32)tw[part] << 24;
    T.e[2 * bid + 1] = (u32)(span[part] * P.TS / (4 * (nl - 1)) | span[part] * (1024 - P.TS) / (4 * (nl - 1)) << 16);
  }
  return T;
}
constexpr int step_range(const Prm& P, bool mx_) {
  int mx = 0, mn = 1 << 20;
  const Tab T = make_tab(P);
  for (int bid = 0; bid < 256; ++bid) { const int n = T.e[2 * bid] >> 7 & 63; mx = n > mx ? n : mx; mn = n < mn ? n : mn; }
  return mx_ ? mx : mn;
}
extern "C" __attribute__((used, visibility("protected"))) __constant__ const Tab wg_tab = make_tab(PRM0);
static_assert(step_range(PRM0, true) <= NL && step_range(PRM0, false) >= NCMIN / 8, "a wave's key range does not fit its load steps");
static_assert(UPREP + 1 < SK0 && QP + QD + 1 < SK0 && cons_end(SK0 - 1) == SK0 - WV && NL - SK1 == WV, "steps every wave runs");
constexpr int NUP = FF / 32;
constexpr int SPIN_MAX = 1 << 17;

constexpr u32 WS_EPOCH = 0;
constexpr u32 G_TB = 1024;
constexpr u32 G_MOD = 4096;
constexpr u32 G_Q = G_MOD + 3072 * 8;
constexpr u32 G_ML = G_Q + 4096 * 8;
constexpr u32 G_U = G_ML + 256 * 16;
constexpr u32 G_P = 0;
constexpr u32 G_XP = G_P + 256 * 256;
constexpr u32 G_OP = G_XP + 8 * 512 * 4;
constexpr u32 G_NT = G_OP + 43 * 512 * 8;
constexpr u32 G_PB = G_OP + 44 * 512 * 8;
constexpr u32 G_VT = G_U + 2 * (G_PB + 8192);
constexpr u32 WS_END = G_VT + 2048;
__device__ __forceinline__ u32 dup(u32 o) { return G_U + ((o >> 13) << 14) + (o & 8191u); }
constexpr u32 SENT = 0xffffffffu;
constexpr u32 OOB = 0x40000000u;

constexpr int AUX_NT = 2, AUX_SC1 = 16;

__device__ __forceinline__ float lo_f(u32 v) { return __uint_as_float(v << 16); }
__device__ __forceinline__ float hi_f(u32 v) { return __uint_as_float(v & 0xffff0000u); }
__device__ __forceinline__ float rbf(float x) { return (float)(__bf16)x; }
__device__ __forceinline__ u32 pack_bf(float lo, float hi) {
  bf16x2 r; r[0] = (__bf16)lo; r[1] = (__bf16)hi; return __builtin_bit_cast(u32, r);
}
__device__ __forceinline__ float dot2(u32 k, u32 q, float acc) {
  return __builtin_amdgcn_fdot2_f32_bf16(__builtin_bit_cast(bf16x2, k), __builtin_bit_cast(bf16x2, q), acc, false);
}
template <int CTRL> __device__ __forceinline__ float dpp(float x) {
  return __builtin_bit_cast(float, __builtin_amdgcn_mov_dpp(__builtin_bit_cast(int, x), CTRL, 0xf, 0xf, true));
}
constexpr int XOR1 = 0xB1, XOR2 = 0x4E, XOR7 = 0x141, XOR8 = 0x128;
__device__ __forceinline__ float swap32_add(float a, float b) {
  auto t = __builtin_amdgcn_permlane32_swap(__float_as_uint(a), __float_as_uint(b), false, false);
  return __uint_as_float(t[0]) + __uint_as_float(t[1]);
}
__device__ __forceinline__ float swap16_add(float a, float b) {
  auto t = __builtin_amdgcn_permlane16_swap(__float_as_uint(a), __float_as_uint(b), false, false);
  return __uint_as_float(t[0]) + __uint_as_float(t[1]);
}
__device__ __forceinline__ float wave_sum(float x) {
  x += dpp<XOR1>(x); x += dpp<XOR2>(x); x += dpp<XOR7>(x); x += dpp<XOR8>(x);
  x = swap16_add(x, x);
  return swap32_add(x, x);
}
__device__ __forceinline__ float wave_max(float x) {
  x = fmaxf(x, dpp<XOR1>(x)); x = fmaxf(x, dpp<XOR2>(x)); x = fmaxf(x, dpp<XOR7>(x)); x = fmaxf(x, dpp<XOR8>(x));
  auto s = __builtin_amdgcn_permlane16_swap(__float_as_uint(x), __float_as_uint(x), false, false);
  x = fmaxf(__uint_as_float(s[0]), __uint_as_float(s[1]));
  auto t = __builtin_amdgcn_permlane32_swap(__float_as_uint(x), __float_as_uint(x), false, false);
  return fmaxf(__uint_as_float(t[0]), __uint_as_float(t[1]));
}
template <int CTRL> __device__ __forceinline__ float sel_add(float lo, float hi, bool up) {
  const float t1 = lo + dpp<CTRL>(lo), t2 = hi + dpp<CTRL>(hi);
  return up ? t2 : t1;
}
template <int I> __device__ __forceinline__ float bc16(float x) {
  return __builtin_bit_cast(float, __builtin_amdgcn_update_dpp(0, __builtin_bit_cast(int, x), 0x150 + I, 0xf, 0xf, false));
}
__device__ __forceinline__ float bcast16(float x, int i) {
  switch (i) {
    case 0: return bc16<0>(x); case 1: return bc16<1>(x); case 2: return bc16<2>(x); case 3: return bc16<3>(x);
    case 4: return bc16<4>(x); case 5: return bc16<5>(x); case 6: return bc16<6>(x); case 7: return bc16<7>(x);
    case 8: return bc16<8>(x); case 9: return bc16<9>(x); case 10: return bc16<10>(x); case 11: return bc16<11>(x);
    case 12: return bc16<12>(x); case 13: return bc16<13>(x); case 14: return bc16<14>(x); default: return bc16<15>(x);
  }
}
__device__ __forceinline__ float silu_f(float x) { return x * __builtin_amdgcn_rcpf(1.f + __expf(-x)); }

template <int AUX> __device__ __forceinline__ u32x4 ld16(rsrc_t r, u32 voff, u32 soff) {
  return __builtin_amdgcn_raw_buffer_load_b128(r, voff, soff, AUX);
}
template <int AUX> __device__ __forceinline__ u32x2 ld8(rsrc_t r, u32 voff, u32 soff) {
  return __builtin_amdgcn_raw_buffer_load_b64(r, voff, soff, AUX);
}
__device__ __forceinline__ void st8_sc1(rsrc_t r, u32 voff, u32 v, u32 tag) {
  __builtin_amdgcn_raw_buffer_store_b64(u32x2{v, tag}, r, voff, 0, AUX_SC1);
}
__device__ __forceinline__ void st16_sc1(rsrc_t r, u32 voff, u32 v0, u32 v1, u32 tag) {
  __builtin_amdgcn_raw_buffer_store_b128(u32x4{v0, tag, v1, tag}, r, voff, 0, AUX_SC1);
}
__device__ __forceinline__ rsrc_t mk_rsrc(const void* p, u32 bytes) {
  return __builtin_amdgcn_make_buffer_rsrc(const_cast<void*>(p), 0, bytes, 0x00020000);
}
#define WAIT_VM(n) __builtin_amdgcn_s_waitcnt(((n) & 15) | (((n) >> 4) << 14) | (7 << 4) | (15 << 8))
#define SB() __builtin_amdgcn_sched_barrier(0)
#define CFENCE() asm volatile("" ::: "memory")
__device__ __forceinline__ bool all_ok(bool ok) { return __builtin_amdgcn_ballot_w64(!ok) == 0ull; }

template <int N> __device__ __forceinline__ void gather16(rsrc_t ws, u32 voff, u32 stride, u32 tag, u32x4 (&g)[N]) {
#pragma nounroll
  for (int spin = 0; spin < SPIN_MAX; ++spin) {
    CFENCE();
    bool ok = true;
#pragma unroll
    for (int i = 0; i < N; ++i) g[i] = ld16<AUX_SC1>(ws, voff + i * stride, 0);
#pragma unroll
    for (int i = 0; i < N; ++i) ok &= (g[i][1] == tag) & (g[i][3] == tag);
    if (all_ok(ok)) break;
    __builtin_amdgcn_s_sleep(8);
  }
}
template <int N> __device__ __forceinline__ void gather8(rsrc_t ws, u32 voff, u32 stride, u32 tag, u32 (&g)[N], bool act = true) {
#pragma nounroll
  for (int spin = 0; spin < SPIN_MAX; ++spin) {
    CFENCE();
    bool ok = true;
    u32x2 t[N];
#pragma unroll
    for (int i = 0; i < N; ++i) t[i] = ld8<AUX_SC1>(ws, voff + i * stride, 0);
#pragma unroll
    for (int i = 0; i < N; ++i) { ok &= (t[i][1] == tag); g[i] = t[i][0]; }
    if (all_ok(ok | !act)) break;
    __builtin_amdgcn_s_sleep(8);
  }
}

struct Smem {
  float red[8][128];
  float ml[8][2];
  float r2[64 * 20];
  u16 hb[3 * 1024];
  float stat[8][64];
  float red2[8][128];
  u32 dummy[64];
  u32x2 bc, bc2;
  u32 bc3, bc4;
  u32x4 zero[4];
  float pl[4][8];
  u32 mflag[8];
  u32x4 wbuf[8][8][64];
  __attribute__((aligned(256))) u16 w2b[32][512];
  u16 actl[2][32];
};

#define PIN(x) asm volatile("" : "+v"(x))
#define LDS_BARRIER() do { __builtin_amdgcn_s_waitcnt(0xc07f); __builtin_amdgcn_s_barrier(); CFENCE(); } while (0)
#define WAVE_SYNC() do { __builtin_amdgcn_fence(__ATOMIC_RELEASE, "wavefront"); __builtin_amdgcn_wave_barrier(); __builtin_amdgcn_fence(__ATOMIC_ACQUIRE, "wavefront"); } while (0)

__device__ __forceinline__ void dma16(rsrc_t r, u32 voff, u32 soff, u32 lds_dst) {
  u32 keep;
  asm volatile("s_mov_b32 %0, m0\n\ts_mov_b32 m0, %4\n\ts_nop 4\n\tbuffer_load_dwordx4 %1, %2, %3 offen nt lds\n\ts_mov_b32 m0, %0"
               : "=&s"(keep) : "v"(voff), "s"(r), "s"(soff), "s"(lds_dst) : "memory");
}
__device__ __forceinline__ void ld16_into(u32x4& d, rsrc_t r, u32 voff, u32 soff) {
  asm volatile("s_nop 4\n\tbuffer_load_dwordx4 %0, %1, %2, %3 offen nt" : "+v"(d) : "v"(voff), "s"(r), "s"(soff) : "memory");
}
__device__ __forceinline__ u32 perm_lo(u32 a, u32 b) { return __builtin_amdgcn_perm(b, a, 0x05040100u); }
__device__ __forceinline__ u32 perm_hi(u32 a, u32 b) { return __builtin_amdgcn_perm(b, a, 0x07060302u); }
__device__ __forceinline__ float rdl(float x, int l) { return __int_as_float(__builtin_amdgcn_readlane(__float_as_int(x), l)); }

typedef __attribute__((ext_vector_type(8))) __bf16 bf16x8;
__device__ __forceinline__ f32x4 mfma16(u32x4 a, u32x4 b, f32x4 c) {
  return __builtin_amdgcn_mfma_f32_16x16x32_bf16(__builtin_bit_cast(bf16x8, a), __builtin_bit_cast(bf16x8, b), c, 0, 0, 0);
}
__device__ __forceinline__ float sum8(const float* p) {
  const f32x4 a = *reinterpret_cast<const f32x4*>(p), b = *reinterpret_cast<const f32x4*>(p + 4);
  return ((a[0] + b[0]) + (a[1] + b[1])) + ((a[2] + b[2]) + (a[3] + b[3]));
}
__device__ __forceinline__ float ln_ri(float var) {
  return __builtin_amdgcn_rcpf(rbf(__builtin_amdgcn_sqrtf(rbf(rbf(fmaxf(var, 0.f)) + 1e-5f))));
}
__device__ __forceinline__ void ln_stats2(const float (&x0)[8], const float (&x1)[8], float& mb0, float& ri0, float& mb1, float& ri1) {
  const float k0 = rdl(x0[0], 0), k1 = rdl(x1[0], 0);
  float s0 = 0.f, s1 = 0.f, q0 = 0.f, q1 = 0.f;
#pragma unroll
  for (int k = 0; k < 8; ++k) {
    const float d0 = x0[k] - k0, d1 = x1[k] - k1;
    s0 += d0; s1 += d1; q0 = fmaf(d0, d0, q0); q1 = fmaf(d1, d1, q1);
  }
  float t = swap16_add(swap32_add(s0, s1), swap32_add(q0, q1));
  t += dpp<XOR1>(t); t += dpp<XOR2>(t); t += dpp<XOR7>(t); t += dpp<XOR8>(t);
  auto u = __builtin_amdgcn_permlane16_swap(__float_as_uint(t), __float_as_uint(t), false, false);
  const float ms = __uint_as_float(u[0]) * (1.f / 512.f), mq = __uint_as_float(u[1]) * (1.f / 512.f);
  const float ri = ln_ri(fmaf(-ms, ms, mq));
  mb0 = rbf(k0 + rdl(ms, 0)); mb1 = rbf(k1 + rdl(ms, 32));
  ri0 = rdl(ri, 0); ri1 = rdl(ri, 32);
}
template <int I> struct IC { static constexpr int v = I; };
template <int I, int N, class F> __device__ __forceinline__ void sfor(F&& f) {
  if constexpr (I < N) { if (f(IC<I>{})) sfor<I + 1, N>(f); }
}
typedef __attribute__((ext_vector_type(4))) short s16x4;
typedef __attribute__((address_space(3))) s16x4* lds_s16x4p;
__device__ __forceinline__ u32x2 tr_read(u32 lds_addr) {
  return __builtin_bit_cast(u32x2, __builtin_amdgcn_ds_read_tr16_b64_v4i16((lds_s16x4p)lds_addr));
}
__device__ __forceinline__ u32 next_tag(u32 e) { return ((e | 0x40004000u) + 1u) & ~0x40004000u; }
__device__ __forceinline__ float u2f(u32 v) { return __uint_as_float(v); }
#define LDSP(T, a) ((__attribute__((address_space(3))) T*)(a))
template <class T> __device__ __forceinline__ u32 lds_addr(T* p) { u32 a = (u32)(uintptr_t)p; PIN(a); return a; }

#define TAB_LOAD "s_add_u32 s30, s28, wg_tab@rel32@lo+4\ns_addc_u32 s31, s29, wg_tab@rel32@hi+12\nglobal_load_dwordx2 %[te], %[va], s[30:31]\n"
#define FC(k) ".Lfc" #k
#define FX(r, bit, k) "s_bitcmp1_b32 %[" #r "], " #bit "\ns_cselect_b32 %[sb], " FC(k) ", 0\ns_xor_b32 %[sa], %[sa], %[sb]\n"
__global__ void __launch_bounds__(512, 2) fused_kernel(const u16* kc_, const u16* vc_, const u16* wo_, const u16* wq_, const u16* wmod_, const u16* w1_, const u16* w3_,
                                                       unsigned char* ws_, const u16* x_, const u16* cnd_, const u16* w2_, u16* out_) {
  __shared__ __attribute__((aligned(256))) Smem sm;
  const u64 a_ka = (u64)__builtin_amdgcn_kernarg_segment_ptr();
  const int bid = blockIdx.x, tid = threadIdx.x;
  u64 wa, t64; u32x2 te; int tid0;
  u32 ar[14];
  u32 tch2, sa_, sb_, va_;
  u32x4 kr[NL], vr[NL];
  asm volatile(".set .Lfc0, %0\n.set .Lfc1, %1\n.set .Lfc2, %2\n.set .Lfc3, %3\n.set .Lfc4, %4\n.set .Lfc5, %5\n.set .Lfc6, %6\n.set .Lfc7, %7\n.set .Lfc8, %8\n.set .Lfc9, %9\n.set .Lfc10, %10\n.set .Lfc11, %11"
    :: "n"(first_col(PRM0, 0)), "n"(first_col(PRM0, 1)), "n"(first_col(PRM0, 2)), "n"(first_col(PRM0, 3)), "n"(first_col(PRM0, 4)), "n"(first_col(PRM0, 5)),
       "n"(first_col(PRM0, 6)), "n"(first_col(PRM0, 7)), "n"(first_col(PRM0, 8)), "n"(first_col(PRM0, 9)), "n"(first_col(PRM0, 10)), "n"(first_col(PRM0, 11)));
  asm volatile(
    "s_load_dwordx16 s[4:19], %[ka], 0x0\ns_load_dwordx8 s[20:27], %[ka], 0x40\n"
    "s_memrealtime %[t]\n"
    "v_lshlrev_b32 %[va], 3, %[bid]\n"
    "s_getpc_b64 s[28:29]\n.Lcpf_first:\n"
    TAB_LOAD
    "s_cmp_gt_u32 %[bid], 15\ns_cbranch_scc1 .Lno_cpf\n"
    "s_mov_b32 %[sa], .Lend_a-.Lcpf_first\ns_max_i32 %[sa], %[sa], .Lend_b-.Lcpf_first\ns_max_i32 %[sa], %[sa], .Lend_c-.Lcpf_first\ns_max_i32 %[sa], %[sa], .Lend_d-.Lcpf_first\ns_max_i32 %[sa], %[sa], .Lend_e-.Lcpf_first\n"
    "v_lshlrev_b32 %[va], 6, %[tid]\nv_min_u32 %[va], %[sa], %[va]\nglobal_load_dword %[pf], %[va], s[28:29]\n"
    ".Lno_cpf:\n"
    "v_readfirstlane_b32 %[t0], %[tid]\nv_and_b32 %[va], 63, %[tid]\n"
    "s_mov_b32 %[sa], " FC(11) "\n"
    FX(bid, 0, 0) FX(bid, 1, 1) FX(bid, 2, 2) FX(bid, 3, 3) FX(bid, 4, 4) FX(bid, 5, 5) FX(bid, 6, 6) FX(bid, 7, 7) FX(t0, 6, 8) FX(t0, 7, 9) FX(t0, 8, 10)
    "v_lshl_add_u32 %[va], %[va], 4, %[sa]\n"
    "s_waitcnt lgkmcnt(0)\nglobal_load_dwordx4 %[k0], %[va], s[4:5] nt\nglobal_load_dwordx4 %[v0], %[va], s[6:7] nt\n"
    "s_bitcmp1_b32 %[t0], 6\ns_cselect_b64 s[14:15], s[16:17], s[14:15]\n"
    "s_bitcmp1_b32 %[bid], 4\ns_cselect_b64 s[8:9], s[10:11], s[8:9]\ns_cselect_b64 s[12:13], s[14:15], s[12:13]\ns_bitcmp1_b32 %[bid], 5\ns_cselect_b64 %[wa], s[12:13], s[8:9]"
    : "=&{s4}"(ar[0]), "=&{s5}"(ar[1]), "=&{s6}"(ar[2]), "=&{s7}"(ar[3]), "=&{s18}"(ar[4]), "=&{s19}"(ar[5]), "=&{s20}"(ar[6]), "=&{s21}"(ar[7]), "=&{s22}"(ar[8]), "=&{s23}"(ar[9]),
      "=&{s24}"(ar[10]), "=&{s25}"(ar[11]), "=&{s26}"(ar[12]), "=&{s27}"(ar[13]), [wa] "=&s"(wa), [t0] "=&s"(tid0), [t] "=&s"(t64), [te] "=&v"(te), [sa] "=&s"(sa_), [sb] "=&s"(sb_),
      [va] "=&v"(va_), [pf] "=&v"(tch2), [k0] "=&v"(kr[0]), [v0] "=&v"(vr[0])
    : [ka] "s"(a_ka), [bid] "s"(bid), [tid] "v"(tid) : "s8", "s9", "s10", "s11", "s12", "s13", "s14", "s15", "s16", "s17", "s28", "s29", "s30", "s31", "scc", "memory");
  asm volatile("" : "+s"(ar[0]), "+s"(ar[1]), "+s"(ar[2]), "+s"(ar[3]), "+s"(ar[4]), "+s"(ar[5]), "+s"(ar[6]), "+s"(ar[7]), "+s"(ar[8]), "+s"(ar[9]), "+s"(ar[10]), "+s"(ar[11]), "+s"(ar[12]), "+s"(ar[13]));
  asm volatile("" : "+s"(wa), "+s"(t64), "+s"(tid0), "+s"(sa_));
  const u32 tka = (u32)wall_clock64();
  const u32 t0 = tka;
  __builtin_assume(tid >= 0 && tid < 512 && tid0 >= 0 && tid0 < 512 && bid >= 0 && bid < 256);
  constexpr Prm P = PRM0;
  const int TEND = P.TEND;
  const int xcd = bid & 7, part = bid >> 4 & 3;
  const int lane = tid & 63, wave = tid0 >> 6;
  SB();
  const bool p3 = part == 3;
  u32 pitch = (u32)(0x005600c001000020ull >> (part * 16) & 0xffff) * 32u;
  const int sh = p3 ? 2 : 3;
  u32 wv0 = (u32)((p3 ? (wave >> 1) * 128 : wave * 64) + (part == 0 ? xcd * 512 : 0) + (lane >> sh)) * pitch + ((lane & (p3 ? 3 : 7)) ^ (lane >> (p3 ? 5 : 3))) * 16u;
  const u32 wimg = (u32)(uintptr_t)(&sm.wbuf[0][0][0]);
  u32 wsz = (part == 0 ? 4096u : 512u) * pitch, wld = wimg + (p3 ? (wave & 1) * 32768 + (wave >> 1) * 8192 : wave * 8192);
  PIN(wv0);
  asm volatile("" : "+s"(wsz), "+s"(wld), "+s"(pitch));
  auto karg = [&](int i) { return ar[2 * i] | (u64)ar[2 * i + 1] << 32; };
  const u64 a_kc = karg(0), a_vc = karg(1);
  unsigned char* ws = (unsigned char*)karg(2);
  const u16 *x = (const u16*)karg(3), *cnd = (const u16*)karg(4), *w2 = (const u16*)karg(5);
  u16* out = (u16*)karg(6);
  const rsrc_t wsr = mk_rsrc(ws, WS_END);
  asm volatile("s_waitcnt vmcnt(2)" : "+v"(te));
  const u32 te0 = __builtin_amdgcn_readfirstlane(te[0]), te1 = __builtin_amdgcn_readfirstlane(te[1]);
  const int bl0 = te0 & 127, nl = te0 >> 7 & 63, blk = te0 >> 13 & 63;
  const bool has_w = te0 >> 19 & 1;
  const rsrc_t rw = mk_rsrc((const void*)wa, wsz);
  const u32 wstep = pitch << (6 - sh), wflip = p3 ? 32u : 0u;
  u32 wso = blk << (sh + 4);
  int wleft = 8;
  auto piece = [&]() {
    dma16(rw, wv0, wso, wld);
    wso += wstep; wld += 1024;
    wv0 ^= (wleft & 1) ? wflip : 0u;
    --wleft;
  };
  SB();
  const int b = bid >> 3 & 1, head = xcd * 4 + (bid >> 6 ^ 3);
  const int g = lane >> 4, c = lane & 15;
  const int i8 = b | bid >> 6 << 1, wi = i8 * 8 + xcd;
  const bool is_up = p3 && has_w, modc = part == 2 && wi < 16;
  const size_t base = ((size_t)(b * NH + head) * SK + (SK - WIN)) * DH;
  const rsrc_t rk = mk_rsrc((const u16*)a_kc + base, WIN * DH * 2), rv = mk_rsrc((const u16*)a_vc + base, WIN * DH * 2);
  int tau = te1 & 0xffff;
  const int tauw = te0 >> 24 << 7, wdef = te0 >> 20 & 15;
  int due = P.NF * 256, now = 0;
  auto gate = [&]() {
    while (now < due) { now = (int)(((u32)wall_clock64() - t0) << 8); if (now < due) __builtin_amdgcn_s_sleep(1); }
    due += tau;
  };
  auto until = [&](int t) { while ((int)(((u32)wall_clock64() - t0) << 8) < t); };
  const u32 pslot = dup(G_P + head * 2048 + (b * 4 + part) * 16);
  const u32 die = __builtin_amdgcn_s_getreg(20 | (2 << 6));
  const bool voter = p3 && i8 == 1, timer = p3 && i8 < 2;
  int vote = 0, lag = 0, shift = 0;
  const u32 pbo = dup(G_PB) + (xcd * 8 + wave) * 128;
  u32 near = 0;
  u32x2 ep; u32 xs0, xs1, tch1; u32x4 xr0, xr1;
  const rsrc_t rx = mk_rsrc(part == 2 ? cnd : x, (part == 1 || (part == 2 && has_w)) ? 2048u : 0u);
  auto small_ops = [&]() {
    ep = ld8<AUX_SC1>(wsr, wave == 7 ? WS_EPOCH : OOB, 0);
    xs0 = __builtin_amdgcn_raw_buffer_load_b16(rx, tid * 2, 0, 0); xs1 = __builtin_amdgcn_raw_buffer_load_b16(rx, tid * 2, 1024, 0);
    xr0 = ld16<0>(rx, part == 1 ? lane * 16 : OOB, 0); xr1 = ld16<0>(rx, part == 1 ? lane * 16 : OOB, 1024);
    tch1 = __builtin_amdgcn_raw_buffer_load_b32(mk_rsrc(w2 + (size_t)blk * 32 * 512, is_up ? 64u : 0u), tid == 384 ? 0 : OOB, 0, AUX_NT);
  };
  if (has_w) {
    if (modc) now = 1 << 30;
#pragma nounroll
    while (wleft > wdef) { gate(); due += tauw - tau; piece(); }
    now = 0;
  }
  due = max(due, P.TK * 256);
  SB();
  u32 sot = sa_ & 0xffc00u;
  {
    const u32 s1 = (bl0 + lane - 1 - (lane >= SK0 ? NL - nl : 0)) ^ (124 + part);
#pragma unroll
    for (int k = 0; k < 7; ++k) sot ^= (u32)-(int)(s1 >> k & 1u) & col(P, k);
    PIN(sot);
  }
  u32 tag = 0;
  u32x4 bf[8];
  auto wo_prep = [&](int s0 = 0, int s1 = 2) {
    const u32 wi_ = wimg + wave * 8192;
    const int q = (lane >> 2) & 3, pp = lane & 3;
#pragma unroll
    for (int s = s0; s < s1; ++s) {
#pragma unroll
      for (int t = 0; t < 4; ++t) {
        const int r0 = s * 32 + g * 8 + q, r1 = r0 + 4, ch = t * 2 + (pp >> 1);
        const u32x2 lo = tr_read(wi_ + r0 * 128 + ((ch ^ (r0 & 7)) * 16) + (pp & 1) * 8), hi = tr_read(wi_ + r1 * 128 + ((ch ^ (r1 & 7)) * 16) + (pp & 1) * 8);
        bf[s * 4 + t] = u32x4{lo[0], lo[1], hi[0], hi[1]};
      }
    }
  };
  auto gemv_go = [&](u32 hh) {
    sm.hb[tid] = (u16)hh; sm.hb[512 + tid] = (u16)(hh >> 16);
    WAVE_SYNC();
    const u32x4* a = (c & 11) == 0 ? reinterpret_cast<const u32x4*>(&sm.hb[(c >> 2) * 512 + wave * 64 + g * 8]) : &sm.zero[0];
    const u32x4 a0 = a[0], a1 = a[(c & 11) == 0 ? 4 : 0];
    const f32x4 z = {0.f, 0.f, 0.f, 0.f};
    f32x4 d[4];
#pragma unroll
    for (int t = 0; t < 4; ++t) d[t] = mfma16(a0, bf[t], z);
#pragma unroll
    for (int t = 0; t < 4; ++t) d[t] = mfma16(a1, bf[4 + t], d[t]);
    if (lane < 32) {
#pragma unroll
      for (int t = 0; t < 4; ++t) sm.r2[(t * 16 + c) * 20 + g * 8 + wave] = d[t][0];
    }
  };
  auto mod_stage = [&]() {
    wo_prep();
    gemv_go(pack_bf(silu_f(lo_f(xs0)), silu_f(lo_f(xs1))));
    __builtin_amdgcn_s_waitcnt(0xc07f);
    if (lane == 0) *LDSP(volatile u32, (u32)(uintptr_t)&sm.mflag[wave]) = 1u;
    if (wave == 7) {
#pragma nounroll
      for (int spin = 0; spin < SPIN_MAX; ++spin) {
        const u32 f = *LDSP(volatile u32, (u32)(uintptr_t)&sm.mflag[lane & 7]);
        if (all_ok(f != 0u)) break;
        __builtin_amdgcn_s_sleep(1);
      }
      CFENCE();
      st8_sc1(wsr, G_MOD + (blk * 64 + lane) * 8, pack_bf(sum8(&sm.r2[lane * 20]), sum8(&sm.r2[lane * 20 + 8])), tag);
    }
  };
  if (lane < 4) sm.zero[lane] = u32x4{0u, 0u, 0u, 0u};
  if (part == 2 && has_w) {
    if (tid < 8) sm.mflag[tid] = 0u;
    LDS_BARRIER();
    if (__builtin_expect(modc, 0)) {
      small_ops();
      __builtin_amdgcn_raw_buffer_store_b128(u32x4{SENT, SENT, SENT, SENT}, wsr, G_U + wi * 10240 + tid * 16, 0, AUX_SC1);
      __builtin_amdgcn_raw_buffer_store_b128(u32x4{SENT, SENT, SENT, SENT}, wsr, tid < 128 ? G_U + wi * 10240 + tid * 16 : OOB, 8192, AUX_SC1);
      asm volatile("s_waitcnt vmcnt(0)" ::: "memory");
      tag = next_tag(__builtin_amdgcn_readfirstlane(ep[0]));
      mod_stage();
      __builtin_amdgcn_s_barrier();
    }
  }
  SB();
  const int fd = wi * 8 + (lane & 3) * 2;
  const int ub = wave >> 2, ud = (wave & 3) * 128 + c * 8 + g * 2;
  u32x4 sg[3]; u32 sx2;
  auto side_loads = [&]() {
    const bool fin = part == 1 && wave == 0;
    sg[0] = ld16<AUX_SC1>(wsr, is_up ? G_MOD + (1024 + ud) * 8 : (fin ? G_MOD + (1024 + fd) * 8 : OOB), 0);
    sg[1] = ld16<AUX_SC1>(wsr, is_up ? G_MOD + (1536 + ud) * 8 : (fin ? G_MOD + (2560 + fd) * 8 : OOB), 0);
    sg[2] = ld16<AUX_SC1>(wsr, is_up ? G_MOD + (2048 + ud) * 8 : OOB, 0);
    sx2 = __builtin_amdgcn_raw_buffer_load_b32(mk_rsrc(x, (is_up || fin) ? 2048u : 0u), is_up ? ub * 1024 + ud * 2 : (g & 1) * 1024 + fd * 2, 0, 0);
  };
  auto side_pin = [&]() { PIN(sg[0]); PIN(sg[1]); PIN(sg[2]); PIN(sx2); };
  const u32 mlo = G_ML + (head * 2 + b) * 64 + (lane & 3) * 16;
  float fmrg = 0.f;
  auto merge_factor = [&](const u32x4& ml) {
    const float mi = u2f(ml[0]);
    float M = fmaxf(mi, dpp<XOR1>(mi)); M = fmaxf(M, dpp<XOR2>(M));
    const float e = __expf(mi - M);
    float L = e * u2f(ml[2]);
    L += dpp<XOR1>(L); L += dpp<XOR2>(L);
    fmrg = rdl(e * __builtin_amdgcn_rcpf(L), part);
  };
  f32x2 ga = {0.f, 0.f}, xin = {0.f, 0.f}, cc1 = {0.f, 0.f};
  float Af = 0.f, Bf = 0.f;
  const int hpos = ub * 512 + ud;
  auto up_prep = [&]() {
    if (!all_ok((sg[0][1] == tag) & (sg[0][3] == tag) & (sg[1][1] == tag) & (sg[1][3] == tag) & (sg[2][1] == tag) & (sg[2][3] == tag))) gather16<3>(wsr, G_MOD + (1024 + ud) * 8, 512 * 8, tag, sg);
    ga = f32x2{ub ? hi_f(sg[0][0]) : lo_f(sg[0][0]), ub ? hi_f(sg[0][2]) : lo_f(sg[0][2])};
    cc1 = f32x2{rbf(1.f + (ub ? hi_f(sg[2][0]) : lo_f(sg[2][0]))), rbf(1.f + (ub ? hi_f(sg[2][2]) : lo_f(sg[2][2])))};
    xin = f32x2{lo_f(sx2), hi_f(sx2)};
    u32* hp = reinterpret_cast<u32*>(&sm.hb[hpos]);
    hp[512] = pack_bf(cc1[0], cc1[1]);
    hp[1024] = ub ? perm_hi(sg[1][0], sg[1][2]) : perm_lo(sg[1][0], sg[1][2]);
  };
  auto up_frags = [&]() {
    const int pp = lane & 3;
    const u32 a = wimg + (pp & 1) * 32768 + ((pp >> 1) * 32 + g * 8 + ((lane >> 2) & 3)) * 64 + (((wave >> 1) ^ ((g & 1) | (pp & 2))) * 16) + (wave & 1) * 8;
#pragma unroll
    for (int rb = 0; rb < 8; ++rb) {
      const u32x2 lo = tr_read(a + rb * 4096), hi = tr_read(a + rb * 4096 + 256);
      bf[rb] = u32x4{lo[0], lo[1], hi[0], hi[1]};
    }
  };
  const u32x4* ap = reinterpret_cast<const u32x4*>(&sm.hb[((lane >> 3) & 1) * 512 + (lane & 1) * 32 + g * 8]);
  const bool par = lane & 8;
  auto limit = [&](auto sc) {
    constexpr int s = sc.v;
    WAIT_VM(window(s, 1 + (PRM0.W[s >> 3] >> (4 * (s & 7)) & 7)));
    SB();
  };
  auto sample = [&]() { if (timer) lag += (int)(((u32)wall_clock64() - t0) << 8) - due + ((int)__builtin_amdgcn_s_getreg(7 | (3 << 11)) + 1) * tau; };
  auto report = [&](u32 dst) {
    if (timer) {
      if (lane == 0) sm.mflag[wave] = vote * 65536 + min(lag >> 8, 4000);
      lag = 0;
      LDS_BARRIER();
      if (wave == 7) {
        int s = sm.mflag[lane & 7];
        s += __builtin_amdgcn_mov_dpp(s, XOR1, 0xf, 0xf, true); s += __builtin_amdgcn_mov_dpp(s, XOR2, 0xf, 0xf, true); s += __builtin_amdgcn_mov_dpp(s, XOR7, 0xf, 0xf, true);
        if (lane < 8) st8_sc1(wsr, dst + lane * 128 + wi * 8, s, tag);
      }
    }
    SB();
  };
  float l0 = 0.f, l1 = 0.f;
  u32x2 gmd[2], tbg;
  int t1 = 0;
#pragma unroll
  for (int i = SK0; i < SK1; ++i) kr[i] = u32x4{0u, 0u, 0u, 0u};
  const int first = SK0 + NL - nl;
  auto left_out = [&](int i) { int f = first; asm volatile("" : "+s"(f)); return i < f; };
  sfor<1, NL>([&](auto ic) {
    constexpr int i = ic.v;
    if constexpr (i >= SK0 && i < SK1) { if (left_out(i)) return true; }
    const u32 so = __builtin_amdgcn_readlane(sot, i);
    gate();
    if constexpr ((i >= TBS && i <= TBP) || (i >= RPS && i <= RPP)) sample();
    SB();
    if constexpr (i >= SK0 && i < SK1) ld16_into(kr[i], rk, lane * 16, so); else kr[i] = ld16<AUX_NT>(rk, lane * 16, so);
    if constexpr (i == SMO) small_ops();
    if constexpr (i == SMO + 1) { if (voter) __builtin_amdgcn_raw_buffer_store_b32(0u, wsr, pbo + (lane >> 5) * 8192 + (lane & 31) * 4, 0, AUX_SC1); }
    if constexpr (i == SK1) tbg = ld8<AUX_SC1>(wsr, wave == 7 ? G_VT + xcd * 128 + c * 8 : OOB, 0);
    if constexpr (i == TBL) tbg = ld8<AUX_SC1>(wsr, wave == 7 ? G_TB + xcd * 128 + c * 8 : OOB, 0);
    if constexpr (i == QP) { gmd[0] = ld8<AUX_SC1>(wsr, part == 1 ? G_MOD + tid * 8 : OOB, 0); gmd[1] = ld8<AUX_SC1>(wsr, part == 1 ? G_MOD + tid * 8 : OOB, 4096); }
    SB();
    SB();
    if (i >= 4) limit(ic);
    if (i == TBP) {
      if (timer) {
        if (lane == 0) sm.mflag[wave] = lag;
        lag = 0;
        LDS_BARRIER();
        if (wave == 7) {
          int s = sm.mflag[lane & 7];
          s += __builtin_amdgcn_mov_dpp(s, XOR1, 0xf, 0xf, true); s += __builtin_amdgcn_mov_dpp(s, XOR2, 0xf, 0xf, true); s += __builtin_amdgcn_mov_dpp(s, XOR7, 0xf, 0xf, true);
          if (lane < 8) st8_sc1(wsr, G_TB + lane * 128 + wi * 8, t0 << 10 | (u32)min(s / (2048 * (TBP - TBS + 1)), 1023), tag);
        }
      }
      SB();
    }
    if (i == PRB) {
      if (voter) {
        u64 ta, tb, tc; u32 dm;
        const u32 pa = pbo + (wave & 1) * 8192;
        asm volatile("s_memtime %0\n\ts_waitcnt lgkmcnt(0)\n\ts_load_dword %3, %4, %5\n\ts_waitcnt lgkmcnt(0)\n\ts_memtime %1\n\ts_waitcnt lgkmcnt(0)\n\t"
                     "s_load_dword %3, %4, %6\n\ts_waitcnt lgkmcnt(0)\n\ts_memtime %2\n\ts_waitcnt lgkmcnt(0)"
                     : "=&s"(ta), "=&s"(tb), "=&s"(tc), "=&s"(dm) : "s"(ws), "s"(pa), "s"(pa ^ 8192u));
        const int d = min(max((int)(2 * (u32)tb - (u32)ta - (u32)tc), -500), 500);
        vote = ((wave ^ die) & 1) ? -d : d;
      }
      SB();
    }
    if (i == TBU) {
      if (wave == 7) {
        const bool ok = tbg[1] == tag;
        float lf = ok ? (float)min(max((int)((t0 << 10 | 1023u) - tbg[0]) >> 10, -300), 300) : 0.f, nf = ok ? 1.f : 0.f, lg = ok ? (float)(tbg[0] & 1023u) : 0.f;
        lf += dpp<XOR1>(lf); nf += dpp<XOR1>(nf); lg += dpp<XOR1>(lg); lf += dpp<XOR2>(lf); nf += dpp<XOR2>(nf); lg += dpp<XOR2>(lg);
        lf += dpp<XOR7>(lf); nf += dpp<XOR7>(nf); lg += dpp<XOR7>(lg); lf += dpp<XOR8>(lf); nf += dpp<XOR8>(nf); lg += dpp<XOR8>(lg);
        const float rn = __builtin_amdgcn_rcpf(fmaxf(nf, 1.f));
        shift = __builtin_amdgcn_readfirstlane(min(max((int)((lg * rn - (float)P.GS) * (float)P.GB * 0.125f), 0), 150));
        if (lane == 0) sm.bc = u32x2{tag, t0 - (u32)(int)(lf * rn) + shift};
      }
      LDS_BARRIER();
      const u32x2 bc = sm.bc;
      tag = __builtin_amdgcn_readfirstlane(bc[0]);
      t1 = (__builtin_amdgcn_readfirstlane((int)(bc[1] - t0)) + TEND + P.X1) * 256;
      tau -= __builtin_amdgcn_readfirstlane((int)((float)(int)(t0 - bc[1]) * 256.f * __builtin_amdgcn_rcpf((float)(nl - TBU - 1))));
      SB();
    }
    if (i == RPP) report(G_VT);
    if (i == LNS) {
      tag = next_tag(__builtin_amdgcn_readfirstlane(ep[0]));
      if (part == 1) {
        float x0[8], x1[8], mb0, ri0, mb1, ri1;
#pragma unroll
        for (int k = 0; k < 4; ++k) { x0[2 * k] = lo_f(xr0[k]); x0[2 * k + 1] = hi_f(xr0[k]); x1[2 * k] = lo_f(xr1[k]); x1[2 * k + 1] = hi_f(xr1[k]); }
        ln_stats2(x0, x1, mb0, ri0, mb1, ri1);
        l0 = rbf(rbf(lo_f(xs0) - mb0) * ri0); l1 = rbf(rbf(lo_f(xs1) - mb1) * ri1);
      }
      PIN(l0); PIN(l1);
      SB();
    }
    if (i == WUSE) {
      PIN(tch1); PIN(tch2);
      if ((part == 1 || (part == 2 && has_w && !modc))) {
        WAIT_VM(WK); CFENCE();
        if (part == 2) mod_stage(); else wo_prep();
      }
#pragma unroll
      for (int k = 0; k < 8; ++k) PIN(bf[k]);
      SB();
    }
    if (i == QP + QD) {
      if (part == 1) {
        u32 gm[2] = {gmd[0][0], gmd[1][0]};
        if (!all_ok((gmd[0][1] == tag) & (gmd[1][1] == tag))) gather8<2>(wsr, G_MOD + tid * 8, 512 * 8, tag, gm);
        gemv_go(pack_bf(rbf(l0 * rbf(1.f + lo_f(gm[1]))) + lo_f(gm[0]), rbf(l1 * rbf(1.f + hi_f(gm[1]))) + hi_f(gm[0])));
      }
      SB();
    }
    if (i == QP + QD + 1) {
      if (part == 1) {
        LDS_BARRIER();
        if (wave == 0) {
          const int bb = lane >> 5, jq = lane & 31;
          st8_sc1(wsr, G_Q + (bb * 2048 + blk * 32 + jq) * 8, pack_bf(sum8(&sm.r2[2 * jq * 20 + bb * 8]), sum8(&sm.r2[(2 * jq + 1) * 20 + bb * 8])), tag);
        }
      }
      SB();
    }
    return true;
  });
  PIN(sot);
  if (wave == 7) {
    const bool ok = tbg[1] == tag;
    float vt = ok ? (float)((int)tbg[0] >> 16) : 0.f, lg = ok ? (float)(tbg[0] & 0xffffu) : 0.f, nf = ok ? 8.f * (RPP - RPS + 1) : 0.f;
    vt += dpp<XOR1>(vt); lg += dpp<XOR1>(lg); nf += dpp<XOR1>(nf); vt += dpp<XOR2>(vt); lg += dpp<XOR2>(lg); nf += dpp<XOR2>(nf);
    vt += dpp<XOR7>(vt); lg += dpp<XOR7>(lg); nf += dpp<XOR7>(nf); vt += dpp<XOR8>(vt); lg += dpp<XOR8>(lg); nf += dpp<XOR8>(nf);
    lg *= __builtin_amdgcn_rcpf(fmaxf(nf, 1.f));
    if (lane == 0) sm.bc2 = u32x2{(__float_as_uint(vt) & 0x80000000u) | (u32)min(shift * P.DG * 32, P.EW * 256), (u32)min(max((int)((lg - (float)P.B0) * (float)(P.BG * 32) + fmaxf(lg - (float)P.TC, 0.f) * (float)(P.RT * 32)) - shift * P.D0 * 32, 0), 200 * 256)};
  }
  LDS_BARRIER();
  {
    const u32x2 bc = sm.bc2;
    const int st = __builtin_amdgcn_readfirstlane(bc[1]), sp = (st * P.BF) >> 3;
    near = ((__builtin_amdgcn_readfirstlane(bc[0]) >> 31 ^ die ^ P.NR ^ 1) & 1) * 8192u;
    t1 += st + (__builtin_amdgcn_readfirstlane(bc[0]) & 0xffff);
    due += P.TG * 256 + sp;
    tau = (te1 >> 16) + __builtin_amdgcn_readfirstlane((int)((float)(st - sp) * __builtin_amdgcn_rcpf((float)(nl - 3))));
  }
  const u32 qoff = G_Q + ((b * 32 + head) * 64 + c * 4) * 8;
  u32x4 qg[2], mlg;
  u32 qq[4];
  constexpr int NB = (NL + 15) / 16;
  float xlo, sc[NB], pr[NB], m = 0.f;
  f32x4 acc = {0.f, 0.f, 0.f, 0.f};
  u32 pb[NB], pm[4], pl8[8], cpf;
  const u32 adst0 = lds_addr(lane < 32 ? &sm.red[wave][c * 8 + g * 4] : &sm.r2[(lane - 32) * 4]);
  u32 po, rdst, rsrc, xo;
  auto wo_addr = [&]() {
    po = dup(G_P + xcd * 8192) + tid * 16 + near;
    rdst = lane < 32 ? (u32)(uintptr_t)&sm.red2[wave][0] + lane * 16 : (u32)(uintptr_t)&sm.r2[0] + (lane - 32) * 16;
    rsrc = (u32)(uintptr_t)&sm.red2[0][((wave & 1) * 16 + (lane & 15)) * 4 + (lane >> 4)];
    xo = dup(G_XP + xcd * 2048 + (wave & 1) * 1024 + (blk * 64 + (lane & ~1)) * 2) + (lane & 1) * 8192;
    PIN(po); PIN(rdst); PIN(rsrc); PIN(xo);
  };
  u32 pso = (wave == 0 && (g & 1) == 0) ? pslot + (c >> 3) * 1024 + (c & 3) * 256 + (c >> 2 & 1) * 128 + (g >> 1) * 8192 : OOB;
  PIN(pso);
  const u32 nadd = wdef ? P.LW : (is_up ? P.LU : 0);
  u32 nto = tid < 2 ? dup(G_NT) + bid * 8 + tid * 8192 : OOB;
  PIN(nto);
  auto notices = [&]() -> int {
    u32 off = dup(G_NT) + near + lane * 16;
    PIN(off);
    until(due);
    int mx;
#pragma nounroll
    for (int spin = 0; spin < SPIN_MAX; ++spin) {
      CFENCE();
      const u32x4 ga_ = ld16<AUX_SC1>(wsr, off, 0), gb_ = ld16<AUX_SC1>(wsr, off, 1024);
      mx = max(max((int)(ga_[0] - t0), (int)(ga_[2] - t0)), max((int)(gb_[0] - t0), (int)(gb_[2] - t0)));
      if (all_ok((((ga_[1] ^ tag) | (ga_[3] ^ tag)) | ((gb_[1] ^ tag) | (gb_[3] ^ tag))) == 0u)) break;
      for (int k = 0; k < P.AG; ++k) __builtin_amdgcn_s_sleep(1);
    }
    if (all_ok(mx <= (t1 >> 8) - P.AC)) return t1;
    mx = max(mx, __builtin_amdgcn_mov_dpp(mx, XOR1, 0xf, 0xf, true)); mx = max(mx, __builtin_amdgcn_mov_dpp(mx, XOR2, 0xf, 0xf, true));
    mx = max(mx, __builtin_amdgcn_mov_dpp(mx, XOR7, 0xf, 0xf, true)); mx = max(mx, __builtin_amdgcn_mov_dpp(mx, XOR8, 0xf, 0xf, true));
    mx = max(max(__builtin_amdgcn_readlane(mx, 0), __builtin_amdgcn_readlane(mx, 16)), max(__builtin_amdgcn_readlane(mx, 32), __builtin_amdgcn_readlane(mx, 48)));
    return max(min(mx, 1 << 20) * 256, t1 + P.L0 * 256) + P.L1 * 256;
  };
  auto pv_row = [&](auto jc) {
    constexpr int i = jc.v;
    const u32 pv = __float_as_uint(bcast16(u2f(pb[i >> 4]), i & 15));
    acc = mfma16(u32x4{pv & pm[0], pv & pm[1], pv & pm[2], pv & pm[3]}, vr[i], acc);
    PIN(acc);
    return true;
  };
  sfor<0, NL - 1>([&](auto ic) {
    constexpr int v = ic.v;
    if constexpr (v >= SK0 && v < SK1) { if (left_out(v)) return true; }
    if constexpr (v > 0) {
      const u32 so = __builtin_amdgcn_readlane(sot, v);
      if constexpr (v >= SK1) { if (v == NL - P.NE) tau = (tau * P.FE) >> 4; }
      gate();
      if constexpr (v >= RVS && v <= RVP) sample();
      SB();
      if constexpr (v >= SK0 && v < SK1) ld16_into(vr[v], rv, lane * 16, so); else vr[v] = ld16<AUX_NT>(rv, lane * 16, so);
      SB();
      SB();
    }
    if constexpr (v == SK0 - 1) {
#pragma unroll
      for (int i = SK0; i < SK1; ++i) { vr[i] = u32x4{0u, 0u, 0u, 0u}; PIN(vr[i]); }
    }
    if constexpr (v == VQ - 4) { qg[0] = ld16<AUX_SC1>(wsr, qoff, 0); qg[1] = ld16<AUX_SC1>(wsr, qoff + 16, 0); }
    if constexpr (v == VSIDE) side_loads();
    if constexpr (v == CPF) {
      u64 pc; u32 dlt, len;
      asm volatile("s_getpc_b64 %0\n.Lcpf_here:\n\ts_mov_b32 %1, .Ltail_start-.Lcpf_here\n\ts_mov_b32 %2, .Lend_a-.Ltail_start\n\ts_max_i32 %2, %2, .Lend_b-.Ltail_start\n\ts_max_i32 %2, %2, .Lend_c-.Ltail_start\n\ts_max_i32 %2, %2, .Lend_d-.Ltail_start\n\ts_max_i32 %2, %2, .Lend_e-.Ltail_start" : "=s"(pc), "=s"(dlt), "=s"(len) :: "scc");
      pc += dlt;
      const rsrc_t rc = mk_rsrc((const void*)pc, (part == 3 && i8 == 1) ? len : 0u);
      cpf = __builtin_amdgcn_raw_buffer_load_b32(rc, tid * 64, 0, 0);
    }
    if constexpr (v == SK1 + 2) PIN(cpf);
    if constexpr (v == RVP) report(G_VT + 1024);
    if constexpr (v == RVL) tbg = ld8<AUX_SC1>(wsr, wave == 7 ? G_VT + 1024 + xcd * 128 + c * 8 : OOB, 0);
    if constexpr (v == SK1) mlg = ld16<AUX_SC1>(wsr, wave == 0 ? mlo : OOB, 0);
    SB();
    if constexpr (NL + v < 72) limit(IC<NL + v>{});
    if constexpr (v >= SK0 + WV) { WAIT_VM(v < SK1 ? WV : WV + 1); SB(); }
    if constexpr (v == VQ) {
      if (!all_ok((qg[0][1] == tag) & (qg[0][3] == tag) & (qg[1][1] == tag) & (qg[1][3] == tag))) gather16<2>(wsr, qoff, 16, tag, qg);
      qq[0] = qg[0][0]; qq[1] = qg[0][2]; qq[2] = qg[1][0]; qq[3] = qg[1][2];
    }
    if constexpr (v >= VQ && v < VS) {
      constexpr int sl = v - VQ, bt = sl >> 1;
      float xs[8];
#pragma unroll
      for (int i = 0; i < 8; ++i) {
        float d = 0.f;
        if (sl * 8 + i < NL) {
#pragma unroll
          for (int k = 0; k < 4; ++k) d = dot2(kr[sl * 8 + i][k], qq[k], d);
        }
        xs[i] = d;
      }
#pragma unroll
      for (int h = 0; h < 4; ++h) xs[h] = sel_add<XOR7>(xs[h], xs[h + 4], c & 4);
#pragma unroll
      for (int h = 0; h < 2; ++h) xs[h] = sel_add<XOR2>(xs[h], xs[h + 2], c & 2);
      xs[0] = sel_add<XOR1>(xs[0], xs[1], c & 1);
      if constexpr ((sl & 1) || sl == NSL - 1) {
        const float t = (sl & 1) ? sel_add<XOR8>(xlo, xs[0], c & 8) : xs[0] + dpp<XOR8>(xs[0]);
        const float s = rbf(rbf(t) * 0.08838834764831845f);
        sc[bt] = (bt * 16 + c < SK0 || (bt * 16 + c >= first && bt * 16 + c < NL)) ? s : -__builtin_inff();
        PIN(sc[bt]);
      } else {
        xlo = xs[0];
        PIN(xlo);
      }
    }
    if constexpr (v == VS) {
      float mloc = sc[0];
#pragma unroll
      for (int bt = 1; bt < NB; ++bt) mloc = fmaxf(mloc, sc[bt]);
      m = fmaxf(wave_max(mloc), -3.0e38f);
      float ls = 0.f;
#pragma unroll
      for (int bt = 0; bt < NB; ++bt) { pr[bt] = __expf(sc[bt] - m); ls += pr[bt]; }
      const float l = wave_sum(ls);
      if (lane == 0) { sm.ml[wave][0] = m; sm.ml[wave][1] = l; }
      if (c == ((NL - 1) & 15)) sm.pl[g][wave] = pr[NB - 1];
    }
    if constexpr (v >= SK1) pv_row(IC<v - SK1 + SK0 - WV>{});
    if constexpr (v < SK0 || v >= SK0 + WV) sfor<cons_end(v - 1), cons_end(v)>(pv_row);
    if constexpr (v == RVU) {
      if (wave == 7) {
        const bool ok = tbg[1] == tag;
        float lg = ok ? (float)(tbg[0] & 0xffffu) : 0.f, nf = ok ? 8.f * (RVP - RVS + 1) : 0.f;
        lg += dpp<XOR1>(lg); nf += dpp<XOR1>(nf); lg += dpp<XOR2>(lg); nf += dpp<XOR2>(nf); lg += dpp<XOR7>(lg); nf += dpp<XOR7>(nf); lg += dpp<XOR8>(lg); nf += dpp<XOR8>(nf);
        lg *= __builtin_amdgcn_rcpf(fmaxf(nf, 1.f));
        if (lane == 0) sm.bc3 = (u32)min(max((int)((lg - (float)P.F0) * (float)(P.FG * 32)), 0), 200 * 256);
      }
      LDS_BARRIER();
      const int st = __builtin_amdgcn_readfirstlane(sm.bc3), sp = (st * P.FF_) >> 3;
      t1 += st;
      due += sp;
      tau += __builtin_amdgcn_readfirstlane((int)((float)(st - sp) * __builtin_amdgcn_rcpf((float)(nl - RVU - 3))));
    }
    if constexpr (v == SK1 + 1) {
      float p[8];
#pragma unroll
      for (int w = 0; w < 8; ++w) p[w] = sm.pl[g][w] * __expf(sm.ml[w][0] - m);
#pragma unroll
      for (int w = 0; w < 8; ++w) pl8[w] = pack_bf(p[w], p[w]);
#pragma unroll
      for (int w = 0; w < 8; ++w) PIN(pl8[w]);
    }
    if constexpr (v == MLS - 1) { side_pin(); if (is_up) up_prep(); PIN(ga); PIN(xin); PIN(cc1); }
    if constexpr (v == UPREP - 1) wo_addr();
    if constexpr (v == UPREP) {
#pragma unroll
      for (int k = 0; k < 8; ++k) asm volatile("" : "=v"(bf[k]));
      if (part == 0) wo_prep(); else if (is_up) up_frags();
#pragma unroll
      for (int k = 0; k < 8; ++k) PIN(bf[k]);
    }
    if constexpr (v == UPREP + 1) {
      if (is_up) {
        const u32x4* a1 = ap + 128 + ((lane >> 1) & 1) * 128;
        f32x4 d0 = {0.f, 0.f, 0.f, 0.f}, d1 = {0.f, 0.f, 0.f, 0.f};
#pragma unroll
        for (int rb = 0; rb < 4; ++rb) { d0 = mfma16(a1[rb * 8], bf[rb], d0); d1 = mfma16(a1[(rb + 4) * 8], bf[rb + 4], d1); }
        Af = par ? d0[1] + d1[1] : d0[0] + d1[0]; Bf = par ? d0[3] + d1[3] : d0[2] + d1[2];
        Af += dpp<XOR8>(Af); Bf += dpp<XOR8>(Bf);
      }
      PIN(Af); PIN(Bf);
    }
    if constexpr (v == MLS) {
      LDS_BARRIER();
      float mm[8], ll[8];
#pragma unroll
      for (int w = 0; w < 8; ++w) { const f32x2 t = *reinterpret_cast<const f32x2*>(&sm.ml[w][0]); mm[w] = t[0]; ll[w] = t[1]; }
      float Mwg = mm[0], Lwg = 0.f;
#pragma unroll
      for (int w = 1; w < 8; ++w) Mwg = fmaxf(Mwg, mm[w]);
      if (wave == 0) {
#pragma unroll
        for (int w = 0; w < 8; ++w) Lwg = fmaf(__expf(mm[w] - Mwg), ll[w], Lwg);
        if (lane == 0) st16_sc1(wsr, G_ML + ((head * 2 + b) * 4 + part) * 16, __float_as_uint(Mwg), __float_as_uint(Lwg), tag);
      }
      const float fw = __expf(m - Mwg);
      m = Mwg;
#pragma unroll
      for (int bt = 0; bt < NB; ++bt) { pb[bt] = pack_bf(pr[bt] * fw, pr[bt] * fw); PIN(pb[bt]); }
#pragma unroll
      for (int k = 0; k < 4; ++k) { pm[k] = (c >> 1 & 3) == k ? ((c & 1) ? 0xffff0000u : 0x0000ffffu) : 0u; PIN(pm[k]); }
    }
    SB();
    return true;
  });
  asm volatile(".Ltail_start:");
  if (wave != 0) {
    pv_row(IC<SK0 - 1>{});
    WAIT_VM(WV); SB();
    sfor<NL - 1 - WV, NL - 1>(pv_row);
    *LDSP(f32x4, adst0) = acc;
    LDS_BARRIER();
  } else {
    u32x4 xr[8], am[8];
#pragma unroll
    for (int w = 0; w < 8; ++w) am[w] = u32x4{pl8[w] & pm[0], pl8[w] & pm[1], pl8[w] & pm[2], pl8[w] & pm[3]};
#pragma unroll
    for (int w = 0; w < 8; ++w) PIN(am[w]);
    {
      const u32 so = __builtin_amdgcn_readlane(sot, NL - 1);
      gate();
      SB();
#pragma unroll
      for (int w = 0; w < 8; ++w) xr[w] = ld16<AUX_NT>(rv, lane * 16, so ^ ((w & 1) ? col(P, 7) : 0u) ^ ((w & 2) ? col(P, 8) : 0u) ^ ((w & 4) ? col(P, 9) : 0u));
      SB();
    }
    u32 rsum = lds_addr(&sm.red[0][c * 8 + (g & 1) * 4]);
    pv_row(IC<SK0 - 1>{});
    WAIT_VM(WV + 8); SB();
    sfor<NL - 1 - WV, NL - 3>(pv_row);
    if (__builtin_expect(!all_ok((mlg[1] == tag) & (mlg[3] == tag)), 0)) {
#pragma nounroll
      for (int spin = 0; spin < SPIN_MAX; ++spin) {
        CFENCE();
        mlg = ld16<AUX_SC1>(wsr, mlo, 0);
        if (all_ok((mlg[1] == tag) & (mlg[3] == tag))) break;
      }
    }
    merge_factor(mlg);
    sfor<NL - 3, NL - 1>(pv_row);
    LDS_BARRIER();
    f32x4 t[8];
#pragma unroll
    for (int w = 1; w < 8; ++w) t[w] = LDSP(f32x4, rsum)[w * 32];
    acc = ((acc + t[1]) + (t[2] + t[3])) + ((t[4] + t[5]) + (t[6] + t[7]));
    PIN(acc);
    SB();
#pragma unroll
    for (int w = 0; w < 8; ++w) acc = mfma16(am[w], xr[w], acc);
  }
  WAIT_VM(0);
  acc *= fmrg;
  {
    const u32 w0 = min(pack_bf(acc[0], acc[1]), SENT - 1u), w1 = min(pack_bf(acc[2], acc[3]), SENT - 1u);
    const auto e0 = __builtin_amdgcn_permlane16_swap(w0, w0, false, false), e1 = __builtin_amdgcn_permlane16_swap(w1, w1, false, false);
    __builtin_amdgcn_raw_buffer_store_b128(u32x4{e0[0], e1[0], e0[1], e1[1]}, wsr, pso, 0, AUX_SC1);
  }
  st8_sc1(wsr, nto, (u32)wall_clock64() + nadd, tag);
  SB();

  if (part == 2) return;
  if (part == 0) {
    __builtin_amdgcn_s_barrier();
#pragma nounroll
    while (wleft > 0) { until(t1 - (P.PA + wleft * P.PD) * 256); piece(); }
    bool late = false;
    u32x4 og[2];
    if (wdef) {
      late = true;
      while ((int)(((u32)wall_clock64() - t0) << 8) < t1 - 20 * 256) if (__builtin_amdgcn_s_getreg(7 | (3 << 11)) == 0) { late = false; break; }
      if (__builtin_expect(late, 0)) { until(t1); CFENCE(); og[0] = ld16<AUX_SC1>(wsr, po, 0); SB(); WAIT_VM(1); } else { WAIT_VM(0); }
      CFENCE();
      if (P.WD > 4) wo_prep(0, 1);
      wo_prep(1, 2);
#pragma unroll
      for (int k = 0; k < 8; ++k) PIN(bf[k]);
      SB();
    }
    if (__builtin_expect(!late, 1)) { until(t1); CFENCE(); og[0] = ld16<AUX_SC1>(wsr, po, 0); }
    SB();
    f32x4 d[4];
    auto wo_mm = [&]() {
      const f32x4 z = {0.f, 0.f, 0.f, 0.f};
#pragma unroll
      for (int t = 0; t < 4; ++t) { d[t] = mfma16(og[0], bf[t], z); og[1][t] = __float_as_uint(dpp<XOR8>(u2f(og[0][t]))); }
      SB();
#pragma unroll
      for (int t = 0; t < 4; ++t) d[t] = mfma16(og[1], bf[4 + t], d[t]);
    };
    auto p_bad = [&]() { return !all_ok(max(max(og[0][0], og[0][1]), max(og[0][2], og[0][3])) != SENT); };
    wo_mm();
    if (__builtin_expect(p_bad(), 0)) {
#pragma nounroll
      for (int spin = 0; spin < SPIN_MAX; ++spin) {
        __builtin_amdgcn_s_sleep(1);
        CFENCE();
        og[0] = ld16<AUX_SC1>(wsr, po, 0);
        if (!p_bad()) break;
      }
      wo_mm();
      SB();
      asm volatile(".Lend_d:");
    }
    {
      SB();
      f32x4 y; f32x2 h[4];
#pragma unroll
      for (int t = 0; t < 4; ++t) { h[t] = d[t].xy + d[t].zw; PIN(h[t]); }
#pragma unroll
      for (int t = 0; t < 4; ++t) { y[t] = h[t][0] + h[t][1]; PIN(y[t]); }
      *LDSP(f32x4, rdst) = y;
    }
    LDS_BARRIER();
    if (wave < 2) {
      f32x2 v[4];
#pragma unroll
      for (int w = 0; w < 4; ++w) v[w] = f32x2{LDSP(float, rsrc)[w * 256], LDSP(float, rsrc)[w * 256 + 128]};
      const f32x2 h = (v[0] + v[1]) + (v[2] + v[3]);
      const float y = h[0] + h[1];
      __builtin_amdgcn_raw_buffer_store_b32(min(pack_bf(dpp<0xA0>(y), dpp<0xF5>(y)), SENT - 1u), wsr, xo, 0, AUX_SC1);
      asm volatile(".Lend_a:");
    }
    return;
  }
  if (part == 3) {
    if (!has_w) return;
    u32x4 b2[4];
    {
      __builtin_amdgcn_s_barrier();
      const rsrc_t r2 = mk_rsrc(w2 + (size_t)(blk * 32 + wave * 4) * 512, 4096u);
      const u32 l2 = (u32)(uintptr_t)(&sm.w2b[wave * 4][0]);
#pragma unroll
      for (int n = 0; n < 4; ++n) { until(t1 - (P.QA + (4 - n) * P.QD) * 256); dma16(r2, (lane ^ (n * 2) ^ ((wave & 2) << 2)) * 16, n * 1024, l2 + n * 1024); }
    }
    const u32 tdst = lds_addr(&sm.hb[hpos]);
    const u32 sdst = lds_addr(&sm.stat[wave][lane]);
    const u32 ssrc = lds_addr(&sm.stat[(g >> 1) * 4 + ((lane >> 3) & 3)][(lane & 7) * 4]);
    const u32 adst = lds_addr((c < 4 && (g & 1) == 0) ? &sm.actl[g >> 1][wave * 4 + c] : reinterpret_cast<u16*>(&sm.dummy[lane]));
    const u32 asrc = lds_addr(&sm.actl[lane & 1][g * 8]);
    const u32 apa = lds_addr(&sm.hb[(c >> 3) * 512 + (c & 3) * 64 + ((c >> 2) & 1) * 32 + g * 8]);
    constexpr u32 xs2 = 16384;
    u32 xoff = dup(G_XP) + near + g * 2048 + ub * 1024 + (wave & 3) * 256 + c * 16;
    u32x4 one;
#pragma unroll
    for (int k = 0; k < 4; ++k) one[k] = ((c & 2) == 0 && (c >> 2) == k) ? ((c & 1) ? 0x3f800000u : 0x00003f80u) : 0u;
    PIN(one);
    u32 ooff = dup(G_OP + (blk * 512 + tid) * 8);
    float Afp = Af, Bfp = Bf;
    PIN(xoff); PIN(ooff); PIN(Afp); PIN(Bfp);
    f32x2 gc = ga * cc1, xc = xin * cc1;
    PIN(ga); PIN(xin); PIN(gc); PIN(xc);
    u32 w2a = (u32)(uintptr_t)(&sm.w2b[0][0]) + (g * 8 + ((lane >> 2) & 3)) * 1024 + (wave ^ (g & 1)) * 128 + (lane & 15) * 8;
    PIN(w2a);
    if (wave == 1) {
      due = t1 + P.N1 * 256;
      const int tn = notices();
      due = tn + P.X2 * 256;
      until(due);
    }
    asm volatile("s_waitcnt vmcnt(0)" ::: "memory");
    __builtin_amdgcn_s_barrier();
    CFENCE();
    u32x4 xg[2] = {ld16<AUX_SC1>(wsr, xoff, 0), ld16<AUX_SC1>(wsr, xoff, xs2)};
    SB();
#pragma unroll
    for (int t = 0; t < 4; ++t) {
      const u32x2 lo = tr_read(w2a ^ (t * 32)), hi = tr_read((w2a ^ (t * 32)) + 4096);
      b2[t] = u32x4{lo[0], lo[1], hi[0], hi[1]};
    }
#pragma unroll
    for (int t = 0; t < 4; ++t) PIN(b2[t]);
    SB();
    const f32x4 z4 = {0.f, 0.f, 0.f, 0.f};
    auto xp_bad = [&]() { return !all_ok(max(max(max(xg[0][0], xg[0][1]), max(xg[0][2], xg[0][3])), max(max(xg[1][0], xg[1][1]), max(xg[1][2], xg[1][3]))) != SENT); };
    f32x4 v = mfma16(one, xg[1], mfma16(one, xg[0], z4));
    if (__builtin_expect(xp_bad(), 0)) {
#pragma nounroll
      for (int spin = 0; spin < SPIN_MAX; ++spin) {
        __builtin_amdgcn_s_sleep(1);
        CFENCE();
        xg[0] = ld16<AUX_SC1>(wsr, xoff, 0); xg[1] = ld16<AUX_SC1>(wsr, xoff, xs2);
        if (!xp_bad()) break;
      }
      v = mfma16(one, xg[1], mfma16(one, xg[0], z4));
      SB();
      asm volatile(".Lend_e:");
    }
    {
      const f32x2 a = v.xy, hc = __builtin_elementwise_fma(gc, a, xc), x1 = __builtin_elementwise_fma(ga, a, xin);
      *LDSP(u32, tdst) = pack_bf(hc[0], hc[1]);
      *LDSP(float, sdst) = swap32_add(x1[0] + x1[1], fmaf(x1[0], x1[0], x1[1] * x1[1]));
    }
    LDS_BARRIER();
    {
      const f32x4 z = {0.f, 0.f, 0.f, 0.f};
      const u32x4 af0 = LDSP(u32x4, apa)[0];
      SB();
      const f32x4 s4 = LDSP(f32x4, ssrc)[0], q4 = LDSP(f32x4, ssrc)[8];
      const u32x4 af1 = LDSP(u32x4, apa)[32];
      SB();
      f32x4 d[4];
      d[0] = mfma16(af0, bf[0], z); d[1] = mfma16(af0, bf[1], z);
      SB();
      const f32x2 s2 = s4.xy + s4.zw, q2 = q4.xy + q4.zw;
      float s = s2[0] + s2[1], q = q2[0] + q2[1];
      SB();
      d[2] = mfma16(af0, bf[2], z);
      SB();
      s = swap16_add(s, q);
      SB();
      d[3] = mfma16(af0, bf[3], z);
      SB();
      s += dpp<XOR1>(s); s += dpp<XOR2>(s);
      SB();
      d[0] = mfma16(af1, bf[4], d[0]);
      SB();
      s += dpp<XOR7>(s); s += dpp<XOR8>(s);
      SB();
      d[1] = mfma16(af1, bf[5], d[1]);
      SB();
      const auto sq = __builtin_amdgcn_permlane16_swap(__float_as_uint(s), __float_as_uint(s), false, false);
      const float mean = u2f(sq[0]) * (1.f / 512.f);
      SB();
      d[2] = mfma16(af1, bf[6], d[2]);
      SB();
      const float var = fmaf(u2f(sq[1]), 1.f / 512.f, -mean * mean);
      SB();
      d[3] = mfma16(af1, bf[7], d[3]);
      SB();
      float ri = __builtin_amdgcn_rsqf(fmaxf(var, 0.f) + 1e-5f);
      float cf = fmaf(-ri * mean, Afp, Bfp);
      float rk = ri * -1.44269504f, ck = cf * -1.44269504f;
      PIN(ri); PIN(cf); PIN(rk); PIN(ck);
      SB();
      PIN(d[0]); PIN(d[1]);
      float y = d[0][0] + d[1][1];
      PIN(y); PIN(d[2]);
      y += d[2][2];
      PIN(y); PIN(d[3]);
      y += d[3][3];
      y = __builtin_bit_cast(float, __builtin_amdgcn_update_dpp(__builtin_bit_cast(int, y), __builtin_bit_cast(int, y), XOR8, 0xa, 0xf, false));
      y = swap16_add(y, y);
      const float ex = __builtin_amdgcn_exp2f(fmaf(y, rk, ck));
      y = fmaf(y, ri, cf);
      const float gu = y * dpp<0x104>(y);
      *LDSP(u16, adst) = (u16)pack_bf(gu * __builtin_amdgcn_rcpf(1.f + ex), 0.f);
    }
    LDS_BARRIER();
    {
      const u32x4 af = *LDSP(u32x4, asrc);
      const f32x4 z = {0.f, 0.f, 0.f, 0.f};
      f32x4 d0 = mfma16(af, b2[0], z), d1 = mfma16(af, b2[1], z), d2 = mfma16(af, b2[2], z), d3 = mfma16(af, b2[3], z);
      asm volatile("" : "+v"(d0), "+v"(d1), "+v"(d2), "+v"(d3));
      const bool g1 = g & 1, g2 = g & 2;
      const float e0 = g1 ? d1[0] : d0[0], e1 = g1 ? d1[1] : d0[1], f0 = g1 ? d3[0] : d2[0], f1 = g1 ? d3[1] : d2[1];
      const u32x2 ov = {pack_bf(g2 ? f0 : e0, g2 ? f1 : e1), tag};
      __builtin_amdgcn_raw_buffer_store_b64(ov, wsr, ooff, near ^ 8192u, AUX_SC1);
      __builtin_amdgcn_raw_buffer_store_b64(ov, wsr, ooff, near, AUX_SC1);
    }
    asm volatile(".Lend_b:");
    return;
  }
  if (wave == 0) {
    if (!all_ok((sg[0][1] == tag) & (sg[0][3] == tag) & (sg[1][1] == tag) & (sg[1][3] == tag))) {
      u32x4 t[2];
      gather16<2>(wsr, G_MOD + (1024 + fd) * 8, 1536 * 8, tag, t);
      sg[0] = t[0]; sg[1] = t[1];
    }
    u32 xa;
    {
      due = t1 + P.N2 * 256;
      const int tn = notices();
      due = tn + P.X3 * 256;
      until(due);
    }
    const bool hb = g & 1;
#pragma nounroll
    for (int spin = 0; spin < SPIN_MAX; ++spin) {
      CFENCE();
      xa = __builtin_amdgcn_raw_buffer_load_b32(wsr, dup(G_XP) + near + (g >> 1) * 16384 + (c >> 2) * 2048 + (g & 1) * 1024 + fd * 2, 0, AUX_SC1);
      if (all_ok(xa != SENT)) break;
      __builtin_amdgcn_s_sleep(1);
    }
    float a0 = lo_f(xa), a1 = hi_f(xa);
    a0 += dpp<0x124>(a0); a1 += dpp<0x124>(a1);
    a0 += dpp<XOR8>(a0); a1 += dpp<XOR8>(a1);
    a0 = swap32_add(a0, a0); a1 = swap32_add(a1, a1);
    f32x2 xv = {rbf(lo_f(sx2) + rbf((hb ? hi_f(sg[0][0]) : lo_f(sg[0][0])) * rbf(a0))), rbf(hi_f(sx2) + rbf((hb ? hi_f(sg[0][2]) : lo_f(sg[0][2])) * rbf(a1)))};
    f32x2 gv = {hb ? hi_f(sg[1][0]) : lo_f(sg[1][0]), hb ? hi_f(sg[1][2]) : lo_f(sg[1][2])};
    PIN(xv); PIN(gv);
    if (wi == 0 && lane == 0) __builtin_amdgcn_raw_buffer_store_b32(tag, wsr, WS_EPOCH, 0, AUX_SC1);
    due = max(due + P.X4 * 256, (int)(((u32)wall_clock64() - t0) << 8) + P.GX * 256);
    const bool last = lane < (NUP - 32) * 4;
    u32 o0 = dup(G_OP + (lane >> 2) * 4096 + fd * 8) + near, o2 = last ? o0 : OOB, tg2 = last ? tag : 0u;
    const rsrc_t rout = mk_rsrc(out, 2048u);
    u32 oo = (lane & 44) == 0 ? (g * 512 + fd) * 2 : OOB;
    const u32 pk = (c & 10) ? 0u : ((c & 4) ? 0x3f800000u : 0x00003f80u);
    u32x4 one = {(c & 1) ? 0u : pk, 0u, (c & 1) ? pk : 0u, 0u};
    PIN(o0); PIN(o2); PIN(tg2); PIN(oo); PIN(one);
    __builtin_amdgcn_raw_buffer_store_b32(pack_bf(xv[0], xv[1]), rout, oo, 0, AUX_SC1);
    u32 s1_ = 131072, s2_ = 262144;
    asm volatile("" : "+s"(s1_), "+s"(s2_));
    until(due);
    f32x4 sv;
#pragma nounroll
    for (int spin = 0; spin < SPIN_MAX; ++spin) {
      CFENCE();
      const u32x4 og[3] = {ld16<AUX_SC1>(wsr, o0, 0), ld16<AUX_SC1>(wsr, o0, s1_), ld16<AUX_SC1>(wsr, o2, s2_)};
      sv = f32x4{0.f, 0.f, 0.f, 0.f};
#pragma unroll
      for (int k = 0; k < 3; ++k) sv = mfma16(one, og[k], sv);
      SB();
      u32 bad = ((og[0][1] ^ tag) | (og[0][3] ^ tag) | (og[1][1] ^ tag)) | ((og[1][3] ^ tag) | (og[2][1] ^ tg2) | (og[2][3] ^ tg2));
      PIN(bad);
      SB();
      float s0 = sv[0] + dpp<0x124>(sv[0]), s1 = sv[1] + dpp<0x124>(sv[1]);
      PIN(s0); PIN(s1);
      s0 += dpp<XOR8>(s0); s1 += dpp<XOR8>(s1);
      PIN(s0); PIN(s1);
      __builtin_amdgcn_raw_buffer_store_b32(pack_bf(fmaf(gv[0], s0, xv[0]), fmaf(gv[1], s1, xv[1])), rout, oo, 0, AUX_SC1);
      SB();
      if (all_ok(bad == 0u)) break;
      __builtin_amdgcn_s_sleep(1);
    }
    asm volatile(".Lend_c:");
  }
}

extern "C" void kernel_launch(void* const* d_in, const int* in_sizes, int n_in,
                              void* d_out, int out_size, void* d_ws, size_t ws_size,
                              hipStream_t stream) {
  fused_kernel<<<256, 512, 0, stream>>>((const u16*)d_in[1], (const u16*)d_in[2], (const u16*)d_in[4], (const u16*)d_in[3], (const u16*)d_in[10], (const u16*)d_in[6], (const u16*)d_in[7],
                                        (unsigned char*)d_ws, (const u16*)d_in[0], (const u16*)d_in[9], (const u16*)d_in[8], (u16*)d_out);
}
```

```cpp
#define PROBE_MUL 10
#include <hip/hip_runtime.h>
#include <stdint.h>

typedef unsigned int u32;
typedef unsigned short u16;
typedef unsigned long long u64;
typedef __attribute__((ext_vector_type(4))) unsigned u32x4;
typedef __attribute__((ext_vector_type(2))) unsigned u32x2;
typedef __attribute__((ext_vector_type(2))) __bf16 bf16x2;
typedef __attribute__((ext_vector_type(4))) float f32x4;
typedef __attribute__((ext_vector_type(2))) float f32x2;
typedef __amdgpu_buffer_rsrc_t rsrc_t;

constexpr int NH = 32, SK = 16384, DH = 128, WIN = 4096, FF = 1376;
struct Prm { int E0, ES, E1, E2, E2C, EU, TEND, DT_UP, L0, L1, X1, X2, X3, X4, NE, FE, M0, M1, M2, M3, M4, GS, GX, N1, N2, GA, WD, DT_WO, LW, LU, EW, PM, NF, NR, C0, A0, AG, AC, TS, TW, TK, TG, GB, B0, BG, BF, D0, DG, TC, LM, C1, F0, FG, FF_, PA, PD, QA, QD, RT, W[9]; };
constexpr Prm PRM0 = {32, 20, 32, 32, 60, 60, 1980, 70, 12, 27, 81, 63, 100, 75, 3, 9, 0x2511f55b, 0x2b7425d5, 0x6017665, 0x17e0b9ec, 0x31944305, 62, 30, 29, 62, 50, 4, 100, 50, 28, 10, 0x0, 12, 14, 3, 30, 4, 4, 511, 222, 90, 2, 6, 69, 10, 8, 8, 6, 87, 120, 2, 65, 6, 4, 52, 14, 9, 18, 24, 0x77777777, 0x77777777, 0x77777777, 0x77777777, 0x77777777, 0x44444477, 0x44444444, 0x44444444, 0x44444444};
constexpr int NL = 37;
constexpr int SK0 = 21, SK1 = 32;
constexpr int NCMIN = (NL - SK1 + SK0) * 8;
constexpr int WK = 8, WV = 5;
constexpr int WUSE = 12, LNS = 7;
constexpr int QP = 14, QD = 4;
constexpr int VQ = 6;
constexpr int NSL = (NL + 7) / 8;
constexpr int VS = VQ + NSL;
constexpr int MLS = VS + 2;
constexpr int VSIDE = MLS - 5, UPREP = MLS + 6;
constexpr int CPF = 17;
constexpr int SMO = 1;
constexpr int RVS = 5, RVP = 8, RVL = 12, RVU = 16;
constexpr int RPS = 24, RPP = 28;
constexpr int PRB = 12;
constexpr int TBS = 7, TBP = 10, TBL = 13, TBU = 17;
constexpr int window(int s, int w) {
  int n = w;
  for (int u = s - w + 1; u <= s; ++u) n += u == SMO ? 6 : (u == TBL || u == SK1 || u == NL + CPF || u == NL + RVL || u == NL + SK1 ? 1 : (u == QP || u == NL + VQ - 4 ? 2 : (u == NL + VSIDE ? 4 : 0)));
  return n;
}
constexpr int cons_end(int v) {
  const int a = v - WV + 1, c = (v - MLS) * 4;
  const int m = a < c ? a : c;
  return m < 0 ? 0 : (m > NL ? NL : m);
}
constexpr u32 col(const Prm& P, int j) { const int m[6] = {P.M0, P.M1, P.M2, P.M3, P.M4, P.PM}; return (u32)(m[j / 3] >> (j % 3 * 10) & 1023) << 10; }
constexpr u32 first_col(const Prm& P, int k) {
  if (k < 4) return col(P, 12 + k) | 1u << (24 + k);
  if (k < 6) return col(P, k - 4);
  if (k < 8) return col(P, 4 + k) | 1u << (16 + k);
  if (k < 11) return col(P, k - 1);
  return (col(P, 2) ^ col(P, 3) ^ col(P, 4) ^ col(P, 5) ^ col(P, 6) ^ col(P, 10) ^ col(P, 11)) | 0xf00000u;
}
constexpr bool one_to_one(const Prm& P) {
  u32 c[10] = {};
  for (int j = 0; j < 10; ++j) c[j] = col(P, j);
  for (int j = 0; j < 10; ++j) {
    if (c[j] == 0) return false;
    const u32 low = c[j] & (0u - c[j]);
    for (int k = j + 1; k < 10; ++k) if (c[k] & low) c[k] ^= c[j];
  }
  return true;
}
static_assert(one_to_one(PRM0), "every KB of the window exactly once");
struct Tab { u32 e[512]; };
constexpr Tab make_tab(const Prm& P) {
  Tab T{};
  for (int bid = 0; bid < 256; ++bid) {
    const int xcd = bid & 7, part = bid >> 4 & 3, i8 = (bid >> 3 & 1) | bid >> 6 << 1, wi = i8 * 8 + xcd;
    const bool has_mod = wi < 48, has_up = wi >= 21;
    const int cb0 = (i8 + P.C0) & 7, cb1 = (i8 + P.C1) & 7, ms = wi - 20;
    const int mi = wi < 8 ? wi : (wi < 16 ? 8 + ((xcd + 4) & 7) : (wi < 20 ? (wi - 14) * 8 + 3 : 16 + ms / 7 * 8 + ms % 7 + (ms % 7 >= 3 ? 1 : 0)));
    const int ui = xcd * 5 + (xcd > 5 ? xcd - 5 : 0) + (i8 == 6 ? 0 : (xcd >= 5 ? 1 : 0) + (i8 == 7 ? 4 : i8 - 2));
    const bool edge = i8 >= 6;
    const int wd = cb0 == 3 ? 0 : P.WD;
    const int e[4] = {P.E0 - 4 * wd + (cb0 == 3 ? P.ES : 0), P.E1 + (cb1 == 3 ? P.ES : 0), has_mod ? (wi < 16 ? P.E2C : P.E2) + ((mi & 7) == 3 ? P.ES : 0) : 0, has_up ? P.EU : 0};
    int tw[4] = {}, win[4] = {}, span[4] = {}, k0[4] = {};
    for (int k = 0; k < 4; ++k) {
      const int np = k == 0 ? 8 - wd : 8;
      tw[k] = np ? e[k] * P.TW / (16 * np) : 0;
      k0[k] = P.NF + tw[k] * np / 2 > P.TK ? P.NF + tw[k] * np / 2 : P.TK;
      span[k] = P.TEND - k0[k] - P.TG - (k == 0 ? (wd ? P.DT_WO : P.A0) : (k == 3 && has_up ? P.DT_UP : 0));
      win[k] = span[k] - (k == 3 && has_up ? P.GA + (edge ? P.LM : 0) : 0);
    }
    int n[4] = {NCMIN / 8, NCMIN / 8, NCMIN / 8, NCMIN / 8};
    for (int left = 128 - NCMIN / 2; left > 0; --left) {
      int best = -1;
      for (int k = 3; k >= 0; --k)
        if (n[k] < NL && (best < 0 || n[k] * win[best] < n[best] * win[k])) best = k;
      ++n[best];
    }
    const int c[4] = {0, n[0] - 1, n[0] + n[1] - 2, n[0] + n[1] + n[2] - 3};
    const int nl = n[part];
    const bool has_w = part < 2 || (part == 2 ? has_mod : has_up);
    const int blk = !has_w ? 0 : (part == 0 ? cb0 : (part == 1 ? xcd * 8 + cb1 : (part == 2 ? mi : ui)));
    T.e[2 * bid] = (u32)(c[part] | nl << 7 | blk << 13 | (has_w ? 1 : 0) << 19 | (part == 0 ? wd : 0) << 20) | (u32)tw[part] << 24;
    T.e[2 * bid + 1] = (u32)(span[part] * P.TS / (4 * (nl - 1)) | span[part] * (1024 - P.TS) / (4 * (nl - 1)) << 16);
  }
  return T;
}
constexpr int step_range(const Prm& P, bool mx_) {
  int mx = 0, mn = 1 << 20;
  const Tab T = make_tab(P);
  for (int bid = 0; bid < 256; ++bid) { const int n = T.e[2 * bid] >> 7 & 63; mx = n > mx ? n : mx; mn = n < mn ? n : mn; }
  return mx_ ? mx : mn;
}
extern "C" __attribute__((used, visibility("protected"))) __constant__ const Tab wg_tab = make_tab(PRM0);
static_assert(step_range(PRM0, true) <= NL && step_range(PRM0, false) >= NCMIN / 8, "a wave's key range does not fit its load steps");
static_assert(UPREP + 1 < SK0 && QP + QD + 1 < SK0 && cons_end(SK0 - 1) == SK0 - WV && NL - SK1 == WV, "steps every wave runs");
constexpr int NUP = FF / 32;
constexpr int SPIN_MAX = 1 << 17;

constexpr u32 WS_EPOCH = 0;
constexpr u32 G_TB = 1024;
constexpr u32 G_MOD = 4096;
constexpr u32 G_Q = G_MOD + 3072 * 8;
constexpr u32 G_ML = G_Q + 4096 * 8;
constexpr u32 G_U = G_ML + 256 * 16;
constexpr u32 G_P = 0;
constexpr u32 G_XP = G_P + 256 * 256;
constexpr u32 G_OP = G_XP + 8 * 512 * 4;
constexpr u32 G_NT = G_OP + 43 * 512 * 8;
constexpr u32 G_PB = G_OP + 44 * 512 * 8;
constexpr u32 G_VT = G_U + 2 * (G_PB + 8192);
constexpr u32 WS_END = G_VT + 2048;
__device__ __forceinline__ u32 dup(u32 o) { return G_U + ((o >> 13) << 14) + (o & 8191u); }
constexpr u32 SENT = 0xffffffffu;
constexpr u32 OOB = 0x40000000u;
#define WST 0

constexpr int AUX_NT = 2, AUX_SC1 = 16;

__device__ __forceinline__ float lo_f(u32 v) { return __uint_as_float(v << 16); }
__device__ __forceinline__ float hi_f(u32 v) { return __uint_as_float(v & 0xffff0000u); }
__device__ __forceinline__ float rbf(float x) { return (float)(__bf16)x; }
__device__ __forceinline__ u32 pack_bf(float lo, float hi) {
  bf16x2 r; r[0] = (__bf16)lo; r[1] = (__bf16)hi; return __builtin_bit_cast(u32, r);
}
__device__ __forceinline__ float dot2(u32 k, u32 q, float acc) {
  return __builtin_amdgcn_fdot2_f32_bf16(__builtin_bit_cast(bf16x2, k), __builtin_bit_cast(bf16x2, q), acc, false);
}
template <int CTRL> __device__ __forceinline__ float dpp(float x) {
  return __builtin_bit_cast(float, __builtin_amdgcn_mov_dpp(__builtin_bit_cast(int, x), CTRL, 0xf, 0xf, true));
}
constexpr int XOR1 = 0xB1, XOR2 = 0x4E, XOR7 = 0x141, XOR8 = 0x128;
__device__ __forceinline__ float swap32_add(float a, float b) {
  auto t = __builtin_amdgcn_permlane32_swap(__float_as_uint(a), __float_as_uint(b), false, false);
  return __uint_as_float(t[0]) + __uint_as_float(t[1]);
}
__device__ __forceinline__ float swap16_add(float a, float b) {
  auto t = __builtin_amdgcn_permlane16_swap(__float_as_uint(a), __float_as_uint(b), false, false);
  return __uint_as_float(t[0]) + __uint_as_float(t[1]);
}
__device__ __forceinline__ float wave_sum(float x) {
  x += dpp<XOR1>(x); x += dpp<XOR2>(x); x += dpp<XOR7>(x); x += dpp<XOR8>(x);
  x = swap16_add(x, x);
  return swap32_add(x, x);
}
__device__ __forceinline__ float wave_max(float x) {
  x = fmaxf(x, dpp<XOR1>(x)); x = fmaxf(x, dpp<XOR2>(x)); x = fmaxf(x, dpp<XOR7>(x)); x = fmaxf(x, dpp<XOR8>(x));
  auto s = __builtin_amdgcn_permlane16_swap(__float_as_uint(x), __float_as_uint(x), false, false);
  x = fmaxf(__uint_as_float(s[0]), __uint_as_float(s[1]));
  auto t = __builtin_amdgcn_permlane32_swap(__float_as_uint(x), __float_as_uint(x), false, false);
  return fmaxf(__uint_as_float(t[0]), __uint_as_float(t[1]));
}
template <int CTRL> __device__ __forceinline__ float sel_add(float lo, float hi, bool up) {
  const float t1 = lo + dpp<CTRL>(lo), t2 = hi + dpp<CTRL>(hi);
  return up ? t2 : t1;
}
template <int I> __device__ __forceinline__ float bc16(float x) {
  return __builtin_bit_cast(float, __builtin_amdgcn_update_dpp(0, __builtin_bit_cast(int, x), 0x150 + I, 0xf, 0xf, false));
}
__device__ __forceinline__ float bcast16(float x, int i) {
  switch (i) {
    case 0: return bc16<0>(x); case 1: return bc16<1>(x); case 2: return bc16<2>(x); case 3: return bc16<3>(x);
    case 4: return bc16<4>(x); case 5: return bc16<5>(x); case 6: return bc16<6>(x); case 7: return bc16<7>(x);
    case 8: return bc16<8>(x); case 9: return bc16<9>(x); case 10: return bc16<10>(x); case 11: return bc16<11>(x);
    case 12: return bc16<12>(x); case 13: return bc16<13>(x); case 14: return bc16<14>(x); default: return bc16<15>(x);
  }
}
__device__ __forceinline__ float silu_f(float x) { return x * __builtin_amdgcn_rcpf(1.f + __expf(-x)); }

template <int AUX> __device__ __forceinline__ u32x4 ld16(rsrc_t r, u32 voff, u32 soff) {
  return __builtin_amdgcn_raw_buffer_load_b128(r, voff, soff, AUX);
}
template <int AUX> __device__ __forceinline__ u32x2 ld8(rsrc_t r, u32 voff, u32 soff) {
  return __builtin_amdgcn_raw_buffer_load_b64(r, voff, soff, AUX);
}
__device__ __forceinline__ void st8_sc1(rsrc_t r, u32 voff, u32 v, u32 tag) {
  __builtin_amdgcn_raw_buffer_store_b64(u32x2{v, tag}, r, voff, 0, AUX_SC1);
}
__device__ __forceinline__ void st16_sc1(rsrc_t r, u32 voff, u32 v0, u32 v1, u32 tag) {
  __builtin_amdgcn_raw_buffer_store_b128(u32x4{v0, tag, v1, tag}, r, voff, 0, AUX_SC1);
}
__device__ __forceinline__ rsrc_t mk_rsrc(const void* p, u32 bytes) {
  return __builtin_amdgcn_make_buffer_rsrc(const_cast<void*>(p), 0, bytes, 0x00020000);
}
#define WAIT_VM(n) __builtin_amdgcn_s_waitcnt(((n) & 15) | (((n) >> 4) << 14) | (7 << 4) | (15 << 8))
#define SB() __builtin_amdgcn_sched_barrier(0)
#define CFENCE() asm volatile("" ::: "memory")
__device__ __forceinline__ bool all_ok(bool ok) { return __builtin_amdgcn_ballot_w64(!ok) == 0ull; }

template <int N> __device__ __forceinline__ void gather16(rsrc_t ws, u32 voff, u32 stride, u32 tag, u32x4 (&g)[N]) {
#pragma nounroll
  for (int spin = 0; spin < SPIN_MAX; ++spin) {
    CFENCE();
    bool ok = true;
#pragma unroll
    for (int i = 0; i < N; ++i) g[i] = ld16<AUX_SC1>(ws, voff + i * stride, 0);
#pragma unroll
    for (int i = 0; i < N; ++i) ok &= (g[i][1] == tag) & (g[i][3] == tag);
    if (all_ok(ok)) break;
    __builtin_amdgcn_s_sleep(8);
  }
}
template <int N> __device__ __forceinline__ void gather8(rsrc_t ws, u32 voff, u32 stride, u32 tag, u32 (&g)[N], bool act = true) {
#pragma nounroll
  for (int spin = 0; spin < SPIN_MAX; ++spin) {
    CFENCE();
    bool ok = true;
    u32x2 t[N];
#pragma unroll
    for (int i = 0; i < N; ++i) t[i] = ld8<AUX_SC1>(ws, voff + i * stride, 0);
#pragma unroll
    for (int i = 0; i < N; ++i) { ok &= (t[i][1] == tag); g[i] = t[i][0]; }
    if (all_ok(ok | !act)) break;
    __builtin_amdgcn_s_sleep(8);
  }
}

struct Smem {
  float red[8][128];
  float ml[8][2];
  float r2[64 * 20];
  u16 hb[3 * 1024];
  float stat[8][64];
  float red2[8][128];
  u32 dummy[64];
  u32x2 bc, bc2;
  u32 bc3, bc4;
  u32x4 zero[4];
  float pl[4][8];
  u32 mflag[8];
  u32x4 wbuf[8][8][64];
  __attribute__((aligned(256))) u16 w2b[32][512];
  u16 actl[2][32];
};

#define PROBE_K 8
#define PROBE_OP 0
#define PROBE_PART -1
#define STAMP(k) do { if ((k) == PROBE_K && tid == 0) pst = wall_clock64(); } while (0)
#define WSTAMP(k) do {} while (0)
#define FINISH() do { STAMP(8); if (tid == 0) { __hip_atomic_store(reinterpret_cast<u64*>(ws + (1u << 20)) + bid * 2, pt0, __ATOMIC_RELAXED, __HIP_MEMORY_SCOPE_AGENT); \
   __hip_atomic_store(reinterpret_cast<u64*>(ws + (1u << 20)) + bid * 2 + 1, (PROBE_PART < 0 || part == PROBE_PART) ? pst : 0ull, __ATOMIC_RELAXED, __HIP_MEMORY_SCOPE_AGENT); } \
   if (bid == 8 && wave == 0) probe_end(ws, pt0, lane); return; } while (0)
#define PIN(x) asm volatile("" : "+v"(x))
#define LDS_BARRIER() do { __builtin_amdgcn_s_waitcnt(0xc07f); __builtin_amdgcn_s_barrier(); CFENCE(); } while (0)
#define WAVE_SYNC() do { __builtin_amdgcn_fence(__ATOMIC_RELEASE, "wavefront"); __builtin_amdgcn_wave_barrier(); __builtin_amdgcn_fence(__ATOMIC_ACQUIRE, "wavefront"); } while (0)

__device__ __forceinline__ void dma16(rsrc_t r, u32 voff, u32 soff, u32 lds_dst) {
  u32 keep;
  asm volatile("s_mov_b32 %0, m0\n\ts_mov_b32 m0, %4\n\ts_nop 4\n\tbuffer_load_dwordx4 %1, %2, %3 offen nt lds\n\ts_mov_b32 m0, %0"
               : "=&s"(keep) : "v"(voff), "s"(r), "s"(soff), "s"(lds_dst) : "memory");
}
__device__ __forceinline__ void ld16_into(u32x4& d, rsrc_t r, u32 voff, u32 soff) {
  asm volatile("s_nop 4\n\tbuffer_load_dwordx4 %0, %1, %2, %3 offen nt" : "+v"(d) : "v"(voff), "s"(r), "s"(soff) : "memory");
}
__device__ __forceinline__ u32 perm_lo(u32 a, u32 b) { return __builtin_amdgcn_perm(b, a, 0x05040100u); }
__device__ __forceinline__ u32 perm_hi(u32 a, u32 b) { return __builtin_amdgcn_perm(b, a, 0x07060302u); }
__device__ __forceinline__ float rdl(float x, int l) { return __int_as_float(__builtin_amdgcn_readlane(__float_as_int(x), l)); }

typedef __attribute__((ext_vector_type(8))) __bf16 bf16x8;
__device__ __forceinline__ f32x4 mfma16(u32x4 a, u32x4 b, f32x4 c) {
  return __builtin_amdgcn_mfma_f32_16x16x32_bf16(__builtin_bit_cast(bf16x8, a), __builtin_bit_cast(bf16x8, b), c, 0, 0, 0);
}
typedef __attribute__((ext_vector_type(8))) unsigned u32x8;
typedef __attribute__((ext_vector_type(16))) __bf16 bf16x16;
__device__ __forceinline__ f32x4 mfma64(u32x4 a, u32x8 b, f32x4 c, u32 idx) {
  return __builtin_amdgcn_smfmac_f32_16x16x64_bf16(__builtin_bit_cast(bf16x8, a), __builtin_bit_cast(bf16x16, b), c, idx, 0, 0);
}
__device__ __forceinline__ float sum8(const float* p) {
  const f32x4 a = *reinterpret_cast<const f32x4*>(p), b = *reinterpret_cast<const f32x4*>(p + 4);
  return ((a[0] + b[0]) + (a[1] + b[1])) + ((a[2] + b[2]) + (a[3] + b[3]));
}
__device__ __forceinline__ float ln_ri(float var) {
  return __builtin_amdgcn_rcpf(rbf(__builtin_amdgcn_sqrtf(rbf(rbf(fmaxf(var, 0.f)) + 1e-5f))));
}
__device__ __forceinline__ void ln_stats2(const float (&x0)[8], const float (&x1)[8], float& mb0, float& ri0, float& mb1, float& ri1) {
  const float k0 = rdl(x0[0], 0), k1 = rdl(x1[0], 0);
  float s0 = 0.f, s1 = 0.f, q0 = 0.f, q1 = 0.f;
#pragma unroll
  for (int k = 0; k < 8; ++k) {
    const float d0 = x0[k] - k0, d1 = x1[k] - k1;
    s0 += d0; s1 += d1; q0 = fmaf(d0, d0, q0); q1 = fmaf(d1, d1, q1);
  }
  float t = swap16_add(swap32_add(s0, s1), swap32_add(q0, q1));
  t += dpp<XOR1>(t); t += dpp<XOR2>(t); t += dpp<XOR7>(t); t += dpp<XOR8>(t);
  auto u = __builtin_amdgcn_permlane16_swap(__float_as_uint(t), __float_as_uint(t), false, false);
  const float ms = __uint_as_float(u[0]) * (1.f / 512.f), mq = __uint_as_float(u[1]) * (1.f / 512.f);
  const float ri = ln_ri(fmaf(-ms, ms, mq));
  mb0 = rbf(k0 + rdl(ms, 0)); mb1 = rbf(k1 + rdl(ms, 32));
  ri0 = rdl(ri, 0); ri1 = rdl(ri, 32);
}
template <int I> struct IC { static constexpr int v = I; };
template <int I, int N, class F> __device__ __forceinline__ void sfor(F&& f) {
  if constexpr (I < N) { if (f(IC<I>{})) sfor<I + 1, N>(f); }
}
typedef __attribute__((ext_vector_type(4))) short s16x4;
typedef __attribute__((address_space(3))) s16x4* lds_s16x4p;
__device__ __forceinline__ u32x2 tr_read(u32 lds_addr) {
  return __builtin_bit_cast(u32x2, __builtin_amdgcn_ds_read_tr16_b64_v4i16((lds_s16x4p)lds_addr));
}
__device__ __forceinline__ u32 next_tag(u32 e) { return ((e | 0x40004000u) + 1u) & ~0x40004000u; }
__device__ __forceinline__ float u2f(u32 v) { return __uint_as_float(v); }
#define LDSP(T, a) ((__attribute__((address_space(3))) T*)(a))
template <class T> __device__ __forceinline__ u32 lds_addr(T* p) { u32 a = (u32)(uintptr_t)p; PIN(a); return a; }

#define TAB_LOAD "s_add_u32 s30, s28, wg_tab@rel32@lo+4\ns_addc_u32 s31, s29, wg_tab@rel32@hi+12\nglobal_load_dwordx2 %[te], %[va], s[30:31]\n"
#define FC(k) ".Lfc" #k
#define FC_REGS
#define FX(r, bit, k) "s_bitcmp1_b32 %[" #r "], " #bit "\ns_cselect_b32 %[sb], " FC(k) ", 0\ns_xor_b32 %[sa], %[sa], %[sb]\n"
__device__ __forceinline__ void probe_end(unsigned char* ws, u64 pt0, int lane) {
  while (wall_clock64() < pt0 + 5500ull) __builtin_amdgcn_s_sleep(8);
  u64 a[4], c2[4];
  for (int i = 0; i < 4; ++i) {
    a[i] = __hip_atomic_load(reinterpret_cast<u64*>(ws + (1u << 20)) + (i * 64 + lane) * 2, __ATOMIC_RELAXED, __HIP_MEMORY_SCOPE_AGENT);
    c2[i] = __hip_atomic_load(reinterpret_cast<u64*>(ws + (1u << 20)) + (i * 64 + lane) * 2 + 1, __ATOMIC_RELAXED, __HIP_MEMORY_SCOPE_AGENT);
  }
  int mn0 = 1 << 30, mx1 = -(1 << 30), mn1 = 1 << 30, sum = 0, cnt = 0;
  for (int i = 0; i < 4; ++i) {
    const int d0 = (int)(a[i] - pt0);
    mn0 = min(mn0, d0);
    if (c2[i] != 0ull) { const int d1 = (int)(c2[i] - pt0); mx1 = max(mx1, d1); mn1 = min(mn1, d1); sum += d1; cnt += 1; }
  }
  for (int o = 1; o < 64; o <<= 1) { mn0 = min(mn0, __shfl_xor(mn0, o)); mx1 = max(mx1, __shfl_xor(mx1, o)); mn1 = min(mn1, __shfl_xor(mn1, o)); sum += __shfl_xor(sum, o); cnt += __shfl_xor(cnt, o); }
  int X = PROBE_OP == 0 ? mx1 : (PROBE_OP == 1 ? sum / max(cnt, 1) : mn1);
  if (PROBE_K == 102) X = mn0;
  X = mn0 + (X - mn0) * PROBE_MUL;
  const u64 tg = pt0 + (u64)(long long)(X + 8000);
  for (int i = 0; i < 400000 && wall_clock64() < tg; ++i) __builtin_amdgcn_s_sleep(1);
}
__global__ void __launch_bounds__(512, 2) fused_kernel(const u16* kc_, const u16* vc_, const u16* wo_, const u16* wq_, const u16* wmod_, const u16* w1_, const u16* w3_,
                                                       unsigned char* ws_, const u16* x_, const u16* cnd_, const u16* w2_, u16* out_) {
  __shared__ __attribute__((aligned(256))) Smem sm;
  const u64 a_ka = (u64)__builtin_amdgcn_kernarg_segment_ptr();
  const int bid = blockIdx.x, tid = threadIdx.x;
  u64 wa, t64; u32x2 te; int tid0;
  u32 ar[14];
  u32 tch2, sa_, sb_, va_;
  u32x4 kr[NL], vr[NL];
  asm volatile(".set .Lfc0, %0\n.set .Lfc1, %1\n.set .Lfc2, %2\n.set .Lfc3, %3\n.set .Lfc4, %4\n.set .Lfc5, %5\n.set .Lfc6, %6\n.set .Lfc7, %7\n.set .Lfc8, %8\n.set .Lfc9, %9\n.set .Lfc10, %10\n.set .Lfc11, %11"
    :: "n"(first_col(PRM0, 0)), "n"(first_col(PRM0, 1)), "n"(first_col(PRM0, 2)), "n"(first_col(PRM0, 3)), "n"(first_col(PRM0, 4)), "n"(first_col(PRM0, 5)),
       "n"(first_col(PRM0, 6)), "n"(first_col(PRM0, 7)), "n"(first_col(PRM0, 8)), "n"(first_col(PRM0, 9)), "n"(first_col(PRM0, 10)), "n"(first_col(PRM0, 11)));
  asm volatile(
    "s_load_dwordx16 s[4:19], %[ka], 0x0\ns_load_dwordx8 s[20:27], %[ka], 0x40\n"
    "s_memrealtime %[t]\n"
    "v_lshlrev_b32 %[va], 3, %[bid]\n"
    "s_getpc_b64 s[28:29]\n.Lcpf_first:\n"
    TAB_LOAD
    "s_cmp_gt_u32 %[bid], 15\ns_cbranch_scc1 .Lno_cpf\n"
    "s_mov_b32 %[sa], .Lend_a-.Lcpf_first\ns_max_i32 %[sa], %[sa], .Lend_b-.Lcpf_first\ns_max_i32 %[sa], %[sa], .Lend_c-.Lcpf_first\ns_max_i32 %[sa], %[sa], .Lend_d-.Lcpf_first\ns_max_i32 %[sa], %[sa], .Lend_e-.Lcpf_first\n"
    "v_lshlrev_b32 %[va], 6, %[tid]\nv_min_u32 %[va], %[sa], %[va]\nglobal_load_dword %[pf], %[va], s[28:29]\n"
    ".Lno_cpf:\n"
    "v_readfirstlane_b32 %[t0], %[tid]\nv_and_b32 %[va], 63, %[tid]\n"
    "s_mov_b32 %[sa], " FC(11) "\n"
    FX(bid, 0, 0) FX(bid, 1, 1) FX(bid, 2, 2) FX(bid, 3, 3) FX(bid, 4, 4) FX(bid, 5, 5) FX(bid, 6, 6) FX(bid, 7, 7) FX(t0, 6, 8) FX(t0, 7, 9) FX(t0, 8, 10)
    "v_lshl_add_u32 %[va], %[va], 4, %[sa]\n"
    "s_waitcnt lgkmcnt(0)\nglobal_load_dwordx4 %[k0], %[va], s[4:5] nt\nglobal_load_dwordx4 %[v0], %[va], s[6:7] nt\n"
    "s_bitcmp1_b32 %[t0], 6\ns_cselect_b64 s[14:15], s[16:17], s[14:15]\n"
    "s_bitcmp1_b32 %[bid], 4\ns_cselect_b64 s[8:9], s[10:11], s[8:9]\ns_cselect_b64 s[12:13], s[14:15], s[12:13]\ns_bitcmp1_b32 %[bid], 5\ns_cselect_b64 %[wa], s[12:13], s[8:9]"
    : "=&{s4}"(ar[0]), "=&{s5}"(ar[1]), "=&{s6}"(ar[2]), "=&{s7}"(ar[3]), "=&{s18}"(ar[4]), "=&{s19}"(ar[5]), "=&{s20}"(ar[6]), "=&{s21}"(ar[7]), "=&{s22}"(ar[8]), "=&{s23}"(ar[9]),
      "=&{s24}"(ar[10]), "=&{s25}"(ar[11]), "=&{s26}"(ar[12]), "=&{s27}"(ar[13]), [wa] "=&s"(wa), [t0] "=&s"(tid0), [t] "=&s"(t64), [te] "=&v"(te), [sa] "=&s"(sa_), [sb] "=&s"(sb_),
      [va] "=&v"(va_), [pf] "=&v"(tch2), [k0] "=&v"(kr[0]), [v0] "=&v"(vr[0])
    : [ka] "s"(a_ka), [bid] "s"(bid), [tid] "v"(tid) : "s8", "s9", "s10", "s11", "s12", "s13", "s14", "s15", "s16", "s17", "s28", "s29", "s30", "s31", "scc", "memory" FC_REGS);
  asm volatile("" : "+s"(ar[0]), "+s"(ar[1]), "+s"(ar[2]), "+s"(ar[3]), "+s"(ar[4]), "+s"(ar[5]), "+s"(ar[6]), "+s"(ar[7]), "+s"(ar[8]), "+s"(ar[9]), "+s"(ar[10]), "+s"(ar[11]), "+s"(ar[12]), "+s"(ar[13]));
  asm volatile("" : "+s"(wa), "+s"(t64), "+s"(tid0), "+s"(sa_));
  const u32 tka = (u32)wall_clock64();
  const u64 pt0 = t64; u64 pst = (PROBE_K == 100 || PROBE_K == 102) ? pt0 : (PROBE_K == 101 ? pt0 + (u32)(tka - (u32)t64) : 0);
  const u32 t0 = tka;
  __builtin_assume(tid >= 0 && tid < 512 && tid0 >= 0 && tid0 < 512 && bid >= 0 && bid < 256);
  constexpr Prm P = PRM0;
  const int TEND = P.TEND;
  const int xcd = bid & 7, part = bid >> 4 & 3;
  const int lane = tid & 63, wave = tid0 >> 6;
  SB();
  const bool p3 = part == 3;
  u32 pitch = (u32)(0x005600c001000020ull >> (part * 16) & 0xffff) * 32u;
  const int sh = p3 ? 2 : 3;
  u32 wv0 = (u32)((p3 ? (wave >> 1) * 128 : wave * 64) + (part == 0 ? xcd * 512 : 0) + (lane >> sh)) * pitch + ((lane & (p3 ? 3 : 7)) ^ (lane >> (p3 ? 5 : 3))) * 16u;
  const u32 wimg = (u32)(uintptr_t)(&sm.wbuf[0][0][0]);
  u32 wsz = (part == 0 ? 4096u : 512u) * pitch, wld = wimg + (p3 ? (wave & 1) * 32768 + (wave >> 1) * 8192 : wave * 8192);
  PIN(wv0);
  asm volatile("" : "+s"(wsz), "+s"(wld), "+s"(pitch));
  auto karg = [&](int i) { return ar[2 * i] | (u64)ar[2 * i + 1] << 32; };
  const u64 a_kc = karg(0), a_vc = karg(1);
  unsigned char* ws = (unsigned char*)karg(2);
  const u16 *x = (const u16*)karg(3), *cnd = (const u16*)karg(4), *w2 = (const u16*)karg(5);
  u16* out = (u16*)karg(6);
  const rsrc_t wsr = mk_rsrc(ws, WS_END);
  asm volatile("s_waitcnt vmcnt(2)" : "+v"(te));
  const u32 te0 = __builtin_amdgcn_readfirstlane(te[0]), te1 = __builtin_amdgcn_readfirstlane(te[1]);
  const int bl0 = te0 & 127, nl = te0 >> 7 & 63, blk = te0 >> 13 & 63;
  const bool has_w = te0 >> 19 & 1;
  const rsrc_t rw = mk_rsrc((const void*)wa, wsz);
  const u32 wstep = pitch << (6 - sh), wflip = (p3 || part == 0) ? 32u : 0u, wflip0 = part == 0 ? 32u : 0u;
  u32 wso = blk << (sh + 4);
  int wleft = 8;
  auto piece = [&]() {
    dma16(rw, wv0, wso, wld);
    wso += wstep; wld += 1024;
    wv0 ^= (wleft & 1) ? wflip : wflip0;
    --wleft;
  };
  SB();
  STAMP(23);
  const int b = bid >> 3 & 1, head = xcd * 4 + (bid >> 6 ^ 3);
  const int g = lane >> 4, c = lane & 15;
  const int i8 = b | bid >> 6 << 1, wi = i8 * 8 + xcd;
  const bool is_up = p3 && has_w, modc = part == 2 && wi < 16;
  const size_t base = ((size_t)(b * NH + head) * SK + (SK - WIN)) * DH;
  const rsrc_t rk = mk_rsrc((const u16*)a_kc + base, WIN * DH * 2), rv = mk_rsrc((const u16*)a_vc + base, WIN * DH * 2);
  int tau = te1 & 0xffff;
  const int tauw = te0 >> 24 << 7, wdef = te0 >> 20 & 15;
  int due = P.NF * 256, now = 0;
  auto gate = [&]() {
    while (now < due) { now = (int)(((u32)wall_clock64() - t0) << 8); if (now < due) __builtin_amdgcn_s_sleep(1); }
    due += tau;
  };
  auto until = [&](int t) { while ((int)(((u32)wall_clock64() - t0) << 8) < t); };
  const u32 pslot = dup(G_P + head * 2048 + (b * 4 + part) * 16);
  const u32 die = __builtin_amdgcn_s_getreg(20 | (2 << 6));
  const bool voter = p3 && i8 == 1, timer = p3 && i8 < 2;
  int vote = 0, lag = 0, shift = 0;
  const u32 pbo = dup(G_PB) + (xcd * 8 + wave) * 128;
  u32 near = 0;
  u32x2 ep; u32 xs0, xs1, tch1; u32x4 xr0, xr1;
  const rsrc_t rx = mk_rsrc(part == 2 ? cnd : x, (part == 1 || (part == 2 && has_w)) ? 2048u : 0u);
  auto small_ops = [&]() {
    ep = ld8<AUX_SC1>(wsr, wave == 7 ? WS_EPOCH : OOB, 0);
    xs0 = __builtin_amdgcn_raw_buffer_load_b16(rx, tid * 2, 0, 0); xs1 = __builtin_amdgcn_raw_buffer_load_b16(rx, tid * 2, 1024, 0);
    xr0 = ld16<0>(rx, part == 1 ? lane * 16 : OOB, 0); xr1 = ld16<0>(rx, part == 1 ? lane * 16 : OOB, 1024);
    tch1 = __builtin_amdgcn_raw_buffer_load_b32(mk_rsrc(w2 + (size_t)blk * 32 * 512, is_up ? 64u : 0u), tid == 384 ? 0 : OOB, 0, AUX_NT);
  };
  if (has_w) {
    if (modc) now = 1 << 30;
#pragma nounroll
    while (wleft > wdef) { gate(); due += tauw - tau; piece(); }
    now = 0;
  }
  due = max(due, P.TK * 256);
  SB();
  u32 sot = sa_ & 0xffc00u;
  {
    const u32 s1 = (bl0 + lane - 1 - (lane >= SK0 ? NL - nl : 0)) ^ (124 + part);
#pragma unroll
    for (int k = 0; k < 7; ++k) sot ^= (u32)-(int)(s1 >> k & 1u) & col(P, k);
    PIN(sot);
  }
  u32 tag = 0;
  u32x4 bf[8];
  auto wo_prep = [&]() {
    const u32 wi_ = wimg + wave * 8192;
    const int q = (lane >> 2) & 3, pp = lane & 3;
#pragma unroll
    for (int s = 0; s < 2; ++s) {
#pragma unroll
      for (int t = 0; t < 4; ++t) {
        const int r0 = s * 32 + g * 8 + q, r1 = r0 + 4, ch = t * 2 + (pp >> 1);
        const u32x2 lo = tr_read(wi_ + r0 * 128 + ((ch ^ (r0 & 7)) * 16) + (pp & 1) * 8), hi = tr_read(wi_ + r1 * 128 + ((ch ^ (r1 & 7)) * 16) + (pp & 1) * 8);
        bf[s * 4 + t] = u32x4{lo[0], lo[1], hi[0], hi[1]};
      }
    }
  };
  auto gemv_go = [&](u32 hh) {
    sm.hb[tid] = (u16)hh; sm.hb[512 + tid] = (u16)(hh >> 16);
    WAVE_SYNC();
    const u32x4* a = (c & 11) == 0 ? reinterpret_cast<const u32x4*>(&sm.hb[(c >> 2) * 512 + wave * 64 + g * 8]) : &sm.zero[0];
    const u32x4 a0 = a[0], a1 = a[(c & 11) == 0 ? 4 : 0];
    const f32x4 z = {0.f, 0.f, 0.f, 0.f};
    f32x4 d[4];
#pragma unroll
    for (int t = 0; t < 4; ++t) d[t] = mfma16(a0, bf[t], z);
#pragma unroll
    for (int t = 0; t < 4; ++t) d[t] = mfma16(a1, bf[4 + t], d[t]);
    if (lane < 32) {
#pragma unroll
      for (int t = 0; t < 4; ++t) sm.r2[(t * 16 + c) * 20 + g * 8 + wave] = d[t][0];
    }
  };
  auto mod_stage = [&]() {
    wo_prep();
    gemv_go(pack_bf(silu_f(lo_f(xs0)), silu_f(lo_f(xs1))));
    __builtin_amdgcn_s_waitcnt(0xc07f);
    if (lane == 0) *LDSP(volatile u32, (u32)(uintptr_t)&sm.mflag[wave]) = 1u;
    if (wave == 7) {
#pragma nounroll
      for (int spin = 0; spin < SPIN_MAX; ++spin) {
        const u32 f = *LDSP(volatile u32, (u32)(uintptr_t)&sm.mflag[lane & 7]);
        if (all_ok(f != 0u)) break;
        __builtin_amdgcn_s_sleep(1);
      }
      CFENCE();
      st8_sc1(wsr, G_MOD + (blk * 64 + lane) * 8, pack_bf(sum8(&sm.r2[lane * 20]), sum8(&sm.r2[lane * 20 + 8])), tag);
    }
    STAMP(9);
  };
  if (lane < 4) sm.zero[lane] = u32x4{0u, 0u, 0u, 0u};
  if (part == 2 && has_w) {
    if (tid < 8) sm.mflag[tid] = 0u;
    LDS_BARRIER();
    if (__builtin_expect(modc, 0)) {
      small_ops();
      __builtin_amdgcn_raw_buffer_store_b128(u32x4{SENT, SENT, SENT, SENT}, wsr, G_U + wi * 10240 + tid * 16, 0, AUX_SC1);
      __builtin_amdgcn_raw_buffer_store_b128(u32x4{SENT, SENT, SENT, SENT}, wsr, tid < 128 ? G_U + wi * 10240 + tid * 16 : OOB, 8192, AUX_SC1);
      asm volatile("s_waitcnt vmcnt(0)" ::: "memory");
      tag = next_tag(__builtin_amdgcn_readfirstlane(ep[0]));
      mod_stage();
      __builtin_amdgcn_s_barrier();
    }
  }
  SB();
  const int fd = wi * 8 + (lane & 3) * 2;
  const int ub = wave >> 2, ud = (wave & 3) * 128 + c * 8 + g * 2;
  u32x4 sg[3]; u32 sx2;
  auto side_loads = [&]() {
    const bool fin = part == 1 && wave == 0;
    sg[0] = ld16<AUX_SC1>(wsr, is_up ? G_MOD + (1024 + ud) * 8 : (fin ? G_MOD + (1024 + fd) * 8 : OOB), 0);
    sg[1] = ld16<AUX_SC1>(wsr, is_up ? G_MOD + (1536 + ud) * 8 : (fin ? G_MOD + (2560 + fd) * 8 : OOB), 0);
    sg[2] = ld16<AUX_SC1>(wsr, is_up ? G_MOD + (2048 + ud) * 8 : OOB, 0);
    sx2 = __builtin_amdgcn_raw_buffer_load_b32(mk_rsrc(x, (is_up || fin) ? 2048u : 0u), is_up ? ub * 1024 + ud * 2 : (g & 1) * 1024 + fd * 2, 0, 0);
  };
  auto side_pin = [&]() { PIN(sg[0]); PIN(sg[1]); PIN(sg[2]); PIN(sx2); };
  const u32 mlo = G_ML + (head * 2 + b) * 64 + (lane & 3) * 16;
  float fmrg = 0.f;
  auto merge_factor = [&](const u32x4& ml) {
    const float mi = u2f(ml[0]);
    float M = fmaxf(mi, dpp<XOR1>(mi)); M = fmaxf(M, dpp<XOR2>(M));
    const float e = __expf(mi - M);
    float L = e * u2f(ml[2]);
    L += dpp<XOR1>(L); L += dpp<XOR2>(L);
    fmrg = rdl(e * __builtin_amdgcn_rcpf(L), part);
  };
  f32x2 ga = {0.f, 0.f}, xin = {0.f, 0.f}, cc1 = {0.f, 0.f};
  float Af = 0.f, Bf = 0.f;
  const int hpos = ub * 512 + (ud & ~15) + (g & 1) * 8 + (c & 1) * 4 + (g >> 1) * 2;
  auto up_prep = [&]() {
    if (!all_ok((sg[0][1] == tag) & (sg[0][3] == tag) & (sg[1][1] == tag) & (sg[1][3] == tag) & (sg[2][1] == tag) & (sg[2][3] == tag))) gather16<3>(wsr, G_MOD + (1024 + ud) * 8, 512 * 8, tag, sg);
    ga = f32x2{ub ? hi_f(sg[0][0]) : lo_f(sg[0][0]), ub ? hi_f(sg[0][2]) : lo_f(sg[0][2])};
    cc1 = f32x2{rbf(1.f + (ub ? hi_f(sg[2][0]) : lo_f(sg[2][0]))), rbf(1.f + (ub ? hi_f(sg[2][2]) : lo_f(sg[2][2])))};
    xin = f32x2{lo_f(sx2), hi_f(sx2)};
    u32* hp = reinterpret_cast<u32*>(&sm.hb[hpos]);
    hp[512] = pack_bf(cc1[0], cc1[1]);
    hp[1024] = ub ? perm_hi(sg[1][0], sg[1][2]) : perm_lo(sg[1][0], sg[1][2]);
  };
  u32x8 bw[4];
  auto up_frags = [&]() {
    const int pp = lane & 3;
    const u32 a = wimg + (pp & 1) * 32768 + ((pp >> 1) * 32 + g * 8 + ((lane >> 2) & 3)) * 64 + (((wave >> 1) ^ ((g & 1) | (pp & 2))) * 16) + (wave & 1) * 8;
#pragma unroll
    for (int j = 0; j < 4; ++j) {
      const u32x2 r0 = tr_read(a + j * 4096), r1 = tr_read(a + j * 4096 + 256), r2 = tr_read(a + j * 4096 + 16384), r3 = tr_read(a + j * 4096 + 16640);
      bw[j] = u32x8{r0[0], r0[1], r1[0], r1[1], r2[0], r2[1], r3[0], r3[1]};
    }
  };
  u32 wfa[4];
  auto wo_frags = [&](int s0) {
#pragma unroll
    for (int t = 0; t < 4; ++t) {
#pragma unroll
      for (int s = s0; s < 2; ++s) {
        const u32x2 lo = tr_read(wfa[t] + s * 4096), hi = tr_read(wfa[t ^ 1] + s * 4096 + 256);
        bw[t][s * 4] = lo[0]; bw[t][s * 4 + 1] = lo[1]; bw[t][s * 4 + 2] = hi[0]; bw[t][s * 4 + 3] = hi[1];
      }
    }
  };
  const bool par = lane & 8;
  auto limit = [&](auto sc) {
    constexpr int s = sc.v;
    WAIT_VM(window(s, 1 + (PRM0.W[s >> 3] >> (4 * (s & 7)) & 7)));
    SB();
  };
  auto sample = [&]() { if (timer) lag += (int)(((u32)wall_clock64() - t0) << 8) - due + ((int)__builtin_amdgcn_s_getreg(7 | (3 << 11)) + 1) * tau; };
  auto report = [&](u32 dst) {
    if (timer) {
      if (lane == 0) sm.mflag[wave] = vote * 65536 + min(lag >> 8, 4000);
      lag = 0;
      LDS_BARRIER();
      if (wave == 7) {
        int s = sm.mflag[lane & 7];
        s += __builtin_amdgcn_mov_dpp(s, XOR1, 0xf, 0xf, true); s += __builtin_amdgcn_mov_dpp(s, XOR2, 0xf, 0xf, true); s += __builtin_amdgcn_mov_dpp(s, XOR7, 0xf, 0xf, true);
        if (lane < 8) st8_sc1(wsr, dst + lane * 128 + wi * 8, s, tag);
      }
    }
    SB();
  };
  float l0 = 0.f, l1 = 0.f;
  u32x2 gmd[2], tbg;
  int t1 = 0;
#pragma unroll
  for (int i = SK0; i < SK1; ++i) kr[i] = u32x4{0u, 0u, 0u, 0u};
  const int first = SK0 + NL - nl;
  auto left_out = [&](int i) { int f = first; asm volatile("" : "+s"(f)); return i < f; };
  sfor<1, NL>([&](auto ic) {
    constexpr int i = ic.v;
    if constexpr (i >= SK0 && i < SK1) { if (left_out(i)) return true; }
    const u32 so = __builtin_amdgcn_readlane(sot, i);
    gate();
    if constexpr ((i >= TBS && i <= TBP) || (i >= RPS && i <= RPP)) sample();
    SB();
    if constexpr (i >= SK0 && i < SK1) ld16_into(kr[i], rk, lane * 16, so); else kr[i] = ld16<AUX_NT>(rk, lane * 16, so);
    if constexpr (i == SMO) small_ops();
    if constexpr (i == SMO + 1) { if (voter) __builtin_amdgcn_raw_buffer_store_b32(0u, wsr, pbo + (lane >> 5) * 8192 + (lane & 31) * 4, 0, AUX_SC1); }
    if constexpr (i == SK1) tbg = ld8<AUX_SC1>(wsr, wave == 7 ? G_VT + xcd * 128 + c * 8 : OOB, 0);
    if constexpr (i == TBL) tbg = ld8<AUX_SC1>(wsr, wave == 7 ? G_TB + xcd * 128 + c * 8 : OOB, 0);
    if constexpr (i == QP) { gmd[0] = ld8<AUX_SC1>(wsr, part == 1 ? G_MOD + tid * 8 : OOB, 0); gmd[1] = ld8<AUX_SC1>(wsr, part == 1 ? G_MOD + tid * 8 : OOB, 4096); }
    SB();
    SB();
    if (i >= 4) limit(ic);
    if (i == 1) STAMP(25);
    if (i == 8) STAMP(12);
    if (i == 20) STAMP(21);
    if (i == 32) STAMP(22);
    if (i == TBP) {
      if (timer) {
        if (lane == 0) sm.mflag[wave] = lag;
        lag = 0;
        LDS_BARRIER();
        if (wave == 7) {
          int s = sm.mflag[lane & 7];
          s += __builtin_amdgcn_mov_dpp(s, XOR1, 0xf, 0xf, true); s += __builtin_amdgcn_mov_dpp(s, XOR2, 0xf, 0xf, true); s += __builtin_amdgcn_mov_dpp(s, XOR7, 0xf, 0xf, true);
          if (lane < 8) st8_sc1(wsr, G_TB + lane * 128 + wi * 8, t0 << 10 | (u32)min(s / (2048 * (TBP - TBS + 1)), 1023), tag);
        }
      }
      SB();
    }
    if (i == PRB) {
      if (voter) {
        u64 ta, tb, tc; u32 dm;
        const u32 pa = pbo + (wave & 1) * 8192;
        asm volatile("s_memtime %0\n\ts_waitcnt lgkmcnt(0)\n\ts_load_dword %3, %4, %5\n\ts_waitcnt lgkmcnt(0)\n\ts_memtime %1\n\ts_waitcnt lgkmcnt(0)\n\t"
                     "s_load_dword %3, %4, %6\n\ts_waitcnt lgkmcnt(0)\n\ts_memtime %2\n\ts_waitcnt lgkmcnt(0)"
                     : "=&s"(ta), "=&s"(tb), "=&s"(tc), "=&s"(dm) : "s"(ws), "s"(pa), "s"(pa ^ 8192u));
        const int d = min(max((int)(2 * (u32)tb - (u32)ta - (u32)tc), -500), 500);
        vote = ((wave ^ die) & 1) ? -d : d;
        STAMP(26);
      }
      SB();
    }
    if (i == TBU) {
      if (wave == 7) {
        const bool ok = tbg[1] == tag;
        float lf = ok ? (float)min(max((int)((t0 << 10 | 1023u) - tbg[0]) >> 10, -300), 300) : 0.f, nf = ok ? 1.f : 0.f, lg = ok ? (float)(tbg[0] & 1023u) : 0.f;
        lf += dpp<XOR1>(lf); nf += dpp<XOR1>(nf); lg += dpp<XOR1>(lg); lf += dpp<XOR2>(lf); nf += dpp<XOR2>(nf); lg += dpp<XOR2>(lg);
        lf += dpp<XOR7>(lf); nf += dpp<XOR7>(nf); lg += dpp<XOR7>(lg); lf += dpp<XOR8>(lf); nf += dpp<XOR8>(nf); lg += dpp<XOR8>(lg);
        const float rn = __builtin_amdgcn_rcpf(fmaxf(nf, 1.f));
        shift = __builtin_amdgcn_readfirstlane(min(max((int)((lg * rn - (float)P.GS) * (float)P.GB * 0.125f), 0), 150));
        if (lane == 0) sm.bc = u32x2{tag, t0 - (u32)(int)(lf * rn) + shift};
      }
      LDS_BARRIER();
      const u32x2 bc = sm.bc;
      tag = __builtin_amdgcn_readfirstlane(bc[0]);
      t1 = (__builtin_amdgcn_readfirstlane((int)(bc[1] - t0)) + TEND + P.X1) * 256;
      tau -= __builtin_amdgcn_readfirstlane((int)((float)(int)(t0 - bc[1]) * 256.f * __builtin_amdgcn_rcpf((float)(nl - TBU - 1))));
      SB();
    }
    if (i == RPP) report(G_VT);
    if (i == LNS) {
      tag = next_tag(__builtin_amdgcn_readfirstlane(ep[0]));
      if (part == 1) {
        float x0[8], x1[8], mb0, ri0, mb1, ri1;
#pragma unroll
        for (int k = 0; k < 4; ++k) { x0[2 * k] = lo_f(xr0[k]); x0[2 * k + 1] = hi_f(xr0[k]); x1[2 * k] = lo_f(xr1[k]); x1[2 * k + 1] = hi_f(xr1[k]); }
        ln_stats2(x0, x1, mb0, ri0, mb1, ri1);
        l0 = rbf(rbf(lo_f(xs0) - mb0) * ri0); l1 = rbf(rbf(lo_f(xs1) - mb1) * ri1);
      }
      PIN(l0); PIN(l1);
      SB();
    }
    if (i == WUSE) {
      PIN(tch1); PIN(tch2);
      if ((part == 1 || (part == 2 && has_w && !modc))) {
        WAIT_VM(WK); CFENCE();
        if (part == 2) mod_stage(); else wo_prep();
      }
#pragma unroll
      for (int k = 0; k < 8; ++k) PIN(bf[k]);
      SB();
    }
    if (i == QP + QD) {
      if (part == 1) {
        u32 gm[2] = {gmd[0][0], gmd[1][0]};
        if (!all_ok((gmd[0][1] == tag) & (gmd[1][1] == tag))) { STAMP(11); gather8<2>(wsr, G_MOD + tid * 8, 512 * 8, tag, gm); }
        STAMP(5);
        gemv_go(pack_bf(rbf(l0 * rbf(1.f + lo_f(gm[1]))) + lo_f(gm[0]), rbf(l1 * rbf(1.f + hi_f(gm[1]))) + hi_f(gm[0])));
      }
      SB();
    }
    if (i == QP + QD + 1) {
      if (part == 1) {
        LDS_BARRIER();
        if (wave == 0) {
          const int bb = lane >> 5, jq = lane & 31;
          st8_sc1(wsr, G_Q + (bb * 2048 + blk * 32 + jq) * 8, pack_bf(sum8(&sm.r2[2 * jq * 20 + bb * 8]), sum8(&sm.r2[(2 * jq + 1) * 20 + bb * 8])), tag);
        }
        STAMP(6);
      }
      SB();
    }
    return true;
  });
  PIN(sot);
  if (wave == 7) {
    const bool ok = tbg[1] == tag;
    float vt = ok ? (float)((int)tbg[0] >> 16) : 0.f, lg = ok ? (float)(tbg[0] & 0xffffu) : 0.f, nf = ok ? 8.f * (RPP - RPS + 1) : 0.f;
    vt += dpp<XOR1>(vt); lg += dpp<XOR1>(lg); nf += dpp<XOR1>(nf); vt += dpp<XOR2>(vt); lg += dpp<XOR2>(lg); nf += dpp<XOR2>(nf);
    vt += dpp<XOR7>(vt); lg += dpp<XOR7>(lg); nf += dpp<XOR7>(nf); vt += dpp<XOR8>(vt); lg += dpp<XOR8>(lg); nf += dpp<XOR8>(nf);
    lg *= __builtin_amdgcn_rcpf(fmaxf(nf, 1.f));
    if (lane == 0) sm.bc2 = u32x2{(__float_as_uint(vt) & 0x80000000u) | (u32)min(shift * P.DG * 32, P.EW * 256), (u32)min(max((int)((lg - (float)P.B0) * (float)(P.BG * 32) + fmaxf(lg - (float)P.TC, 0.f) * (float)(P.RT * 32)) - shift * P.D0 * 32, 0), 200 * 256)};
  }
  LDS_BARRIER();
  {
    const u32x2 bc = sm.bc2;
    const int st = __builtin_amdgcn_readfirstlane(bc[1]), sp = (st * P.BF) >> 3;
    near = ((__builtin_amdgcn_readfirstlane(bc[0]) >> 31 ^ die ^ P.NR ^ 1) & 1) * 8192u;
    t1 += st + (__builtin_amdgcn_readfirstlane(bc[0]) & 0xffff);
    due += P.TG * 256 + sp;
    tau = (te1 >> 16) + __builtin_amdgcn_readfirstlane((int)((float)(st - sp) * __builtin_amdgcn_rcpf((float)(nl - 3))));
  }
  const u32 qoff = G_Q + ((b * 32 + head) * 64 + c * 4) * 8;
  u32x4 qg[2], mlg;
  u32 qq[4];
  constexpr int NB = (NL + 15) / 16;
  float xlo, sc[NB], pr[NB], m = 0.f;
  f32x4 acc = {0.f, 0.f, 0.f, 0.f};
  u32 pb[NB], pm[4], pl8[8], cpf;
  const u32 adst0 = lds_addr(lane < 32 ? &sm.red[wave][c * 8 + g * 4] : &sm.r2[(lane - 32) * 4]);
  u32 po, rdst, rsrc, xo;
  auto wo_addr = [&]() {
    po = dup(G_P + xcd * 8192) + tid * 16 + near;
    rdst = (u32)(uintptr_t)&sm.red2[wave][(lane & 31) * 4 + (lane >> 5) * 2];
    rsrc = (u32)(uintptr_t)&sm.red2[0][((wave & 1) * 16 + c) * 4 + (g & 1) * 2 + (g >> 1)];
    xo = dup(G_XP + xcd * 2048 + (wave & 1) * 1024 + (blk * 64 + (lane & ~1)) * 2) + (lane & 1) * 8192;
    PIN(po); PIN(rdst); PIN(rsrc); PIN(xo);
    const int q = (lane >> 2) & 3, pp = lane & 3, r = (g & 1) * 4 + (q & 1);
#pragma unroll
    for (int t = 0; t < 4; ++t) { wfa[t] = wimg + wave * 8192 + ((g >> 1) * 16 + (q >> 1) * 8 + r) * 128 + (pp & 1) * 8 + ((t * 2 + (pp >> 1)) ^ r ^ (q & 2)) * 16; PIN(wfa[t]); }
  };
  u32 pso = (wave == 0 && (g & 1) == 0) ? pslot + c * 128 + (g >> 1) * 8192 : OOB;
  PIN(pso);
  const u32 nadd = wdef ? P.LW : (is_up ? P.LU : 0);
  u32 nto = tid < 2 ? dup(G_NT) + bid * 8 + tid * 8192 : OOB;
  PIN(nto);
  auto notices = [&]() -> int {
    u32 off = dup(G_NT) + near + lane * 16;
    PIN(off);
    until(due);
    int mx;
#pragma nounroll
    for (int spin = 0; spin < SPIN_MAX; ++spin) {
      CFENCE();
      const u32x4 ga_ = ld16<AUX_SC1>(wsr, off, 0), gb_ = ld16<AUX_SC1>(wsr, off, 1024);
      mx = max(max((int)(ga_[0] - t0), (int)(ga_[2] - t0)), max((int)(gb_[0] - t0), (int)(gb_[2] - t0)));
      if (all_ok((((ga_[1] ^ tag) | (ga_[3] ^ tag)) | ((gb_[1] ^ tag) | (gb_[3] ^ tag))) == 0u)) break;
      for (int k = 0; k < P.AG; ++k) __builtin_amdgcn_s_sleep(1);
    }
    if (all_ok(mx <= (t1 >> 8) - P.AC)) return t1;
    mx = max(mx, __builtin_amdgcn_mov_dpp(mx, XOR1, 0xf, 0xf, true)); mx = max(mx, __builtin_amdgcn_mov_dpp(mx, XOR2, 0xf, 0xf, true));
    mx = max(mx, __builtin_amdgcn_mov_dpp(mx, XOR7, 0xf, 0xf, true)); mx = max(mx, __builtin_amdgcn_mov_dpp(mx, XOR8, 0xf, 0xf, true));
    mx = max(max(__builtin_amdgcn_readlane(mx, 0), __builtin_amdgcn_readlane(mx, 16)), max(__builtin_amdgcn_readlane(mx, 32), __builtin_amdgcn_readlane(mx, 48)));
    return max(min(mx, 1 << 20) * 256, t1 + P.L0 * 256) + P.L1 * 256;
  };
  auto pv_row = [&](auto jc) {
    constexpr int i = jc.v;
    const u32 pv = __float_as_uint(bcast16(u2f(pb[i >> 4]), i & 15));
    acc = mfma16(u32x4{pv & pm[0], pv & pm[1], pv & pm[2], pv & pm[3]}, vr[i], acc);
    PIN(acc);
    return true;
  };
  sfor<0, NL - 1>([&](auto ic) {
    constexpr int v = ic.v;
    if constexpr (v >= SK0 && v < SK1) { if (left_out(v)) return true; }
    if constexpr (v > 0) {
      const u32 so = __builtin_amdgcn_readlane(sot, v);
      if constexpr (v >= SK1) { if (v == NL - P.NE) tau = (tau * P.FE) >> 4; }
      gate();
      if constexpr (v >= RVS && v <= RVP) sample();
      SB();
      if constexpr (v >= SK0 && v < SK1) ld16_into(vr[v], rv, lane * 16, so); else vr[v] = ld16<AUX_NT>(rv, lane * 16, so);
      SB();
      SB();
    }
    if constexpr (v == SK0 - 1) {
#pragma unroll
      for (int i = SK0; i < SK1; ++i) { vr[i] = u32x4{0u, 0u, 0u, 0u}; PIN(vr[i]); }
    }
    if constexpr (v == VQ - 4) { qg[0] = ld16<AUX_SC1>(wsr, qoff, 0); qg[1] = ld16<AUX_SC1>(wsr, qoff + 16, 0); }
    if constexpr (v == VSIDE) side_loads();
    if constexpr (v == CPF) {
      u64 pc; u32 dlt, len;
      asm volatile("s_getpc_b64 %0\n.Lcpf_here:\n\ts_mov_b32 %1, .Ltail_start-.Lcpf_here\n\ts_mov_b32 %2, .Lend_a-.Ltail_start\n\ts_max_i32 %2, %2, .Lend_b-.Ltail_start\n\ts_max_i32 %2, %2, .Lend_c-.Ltail_start\n\ts_max_i32 %2, %2, .Lend_d-.Ltail_start\n\ts_max_i32 %2, %2, .Lend_e-.Ltail_start" : "=s"(pc), "=s"(dlt), "=s"(len) :: "scc");
      pc += dlt;
      const rsrc_t rc = mk_rsrc((const void*)pc, (part == 3 && i8 == 1) ? len : 0u);
      cpf = __builtin_amdgcn_raw_buffer_load_b32(rc, tid * 64, 0, 0);
    }
    if constexpr (v == SK1 + 2) PIN(cpf);
    if constexpr (v == RVP) report(G_VT + 1024);
    if constexpr (v == RVL) tbg = ld8<AUX_SC1>(wsr, wave == 7 ? G_VT + 1024 + xcd * 128 + c * 8 : OOB, 0);
    if constexpr (v == SK1) mlg = ld16<AUX_SC1>(wsr, wave == 0 ? mlo : OOB, 0);
    SB();
    if constexpr (NL + v < 72) limit(IC<NL + v>{});
    if constexpr (v >= SK0 + WV) { WAIT_VM(v < SK1 ? WV : WV + 1); SB(); }
    if constexpr (v == VQ) {
      if (!all_ok((qg[0][1] == tag) & (qg[0][3] == tag) & (qg[1][1] == tag) & (qg[1][3] == tag))) { STAMP(20); gather16<2>(wsr, qoff, 16, tag, qg); }
      STAMP(1); WSTAMP(1);
      qq[0] = qg[0][0]; qq[1] = qg[0][2]; qq[2] = qg[1][0]; qq[3] = qg[1][2];
    }
    if constexpr (v >= VQ && v < VS) {
      constexpr int sl = v - VQ, bt = sl >> 1;
      float xs[8];
#pragma unroll
      for (int i = 0; i < 8; ++i) {
        float d = 0.f;
        if (sl * 8 + i < NL) {
#pragma unroll
          for (int k = 0; k < 4; ++k) d = dot2(kr[sl * 8 + i][k], qq[k], d);
        }
        xs[i] = d;
      }
#pragma unroll
      for (int h = 0; h < 4; ++h) xs[h] = sel_add<XOR7>(xs[h], xs[h + 4], c & 4);
#pragma unroll
      for (int h = 0; h < 2; ++h) xs[h] = sel_add<XOR2>(xs[h], xs[h + 2], c & 2);
      xs[0] = sel_add<XOR1>(xs[0], xs[1], c & 1);
      if constexpr ((sl & 1) || sl == NSL - 1) {
        const float t = (sl & 1) ? sel_add<XOR8>(xlo, xs[0], c & 8) : xs[0] + dpp<XOR8>(xs[0]);
        const float s = rbf(rbf(t) * 0.08838834764831845f);
        sc[bt] = (bt * 16 + c < SK0 || (bt * 16 + c >= first && bt * 16 + c < NL)) ? s : -__builtin_inff();
        PIN(sc[bt]);
      } else {
        xlo = xs[0];
        PIN(xlo);
      }
    }
    if constexpr (v == VS) {
      float mloc = sc[0];
#pragma unroll
      for (int bt = 1; bt < NB; ++bt) mloc = fmaxf(mloc, sc[bt]);
      m = fmaxf(wave_max(mloc), -3.0e38f);
      float ls = 0.f;
#pragma unroll
      for (int bt = 0; bt < NB; ++bt) { pr[bt] = __expf(sc[bt] - m); ls += pr[bt]; }
      const float l = wave_sum(ls);
      if (lane == 0) { sm.ml[wave][0] = m; sm.ml[wave][1] = l; }
      if (c == ((NL - 1) & 15)) sm.pl[g][wave] = pr[NB - 1];
      STAMP(2); WSTAMP(2);
    }
    if constexpr (v >= SK1) pv_row(IC<v - SK1 + SK0 - WV>{});
    if constexpr (v < SK0 || v >= SK0 + WV) sfor<cons_end(v - 1), cons_end(v)>(pv_row);
    if constexpr (v == RVU) {
      if (wave == 7) {
        const bool ok = tbg[1] == tag;
        float lg = ok ? (float)(tbg[0] & 0xffffu) : 0.f, nf = ok ? 8.f * (RVP - RVS + 1) : 0.f;
        lg += dpp<XOR1>(lg); nf += dpp<XOR1>(nf); lg += dpp<XOR2>(lg); nf += dpp<XOR2>(nf); lg += dpp<XOR7>(lg); nf += dpp<XOR7>(nf); lg += dpp<XOR8>(lg); nf += dpp<XOR8>(nf);
        lg *= __builtin_amdgcn_rcpf(fmaxf(nf, 1.f));
        if (lane == 0) sm.bc3 = (u32)min(max((int)((lg - (float)P.F0) * (float)(P.FG * 32)), 0), 200 * 256);
      }
      LDS_BARRIER();
      const int st = __builtin_amdgcn_readfirstlane(sm.bc3), sp = (st * P.FF_) >> 3;
      t1 += st;
      due += sp;
      tau += __builtin_amdgcn_readfirstlane((int)((float)(st - sp) * __builtin_amdgcn_rcpf((float)(nl - RVU - 3))));
    }
    if constexpr (v == SK1 + 1) {
      float p[8];
#pragma unroll
      for (int w = 0; w < 8; ++w) p[w] = sm.pl[g][w] * __expf(sm.ml[w][0] - m);
#pragma unroll
      for (int w = 0; w < 8; ++w) pl8[w] = pack_bf(p[w], p[w]);
#pragma unroll
      for (int w = 0; w < 8; ++w) PIN(pl8[w]);
    }
    if constexpr (v == MLS - 1) { side_pin(); if (is_up) up_prep(); PIN(ga); PIN(xin); PIN(cc1); }
    if constexpr (v == UPREP - 1) wo_addr();
    if constexpr (v == UPREP) {
#pragma unroll
      for (int k = 0; k < 4; ++k) asm volatile("" : "=v"(bw[k]));
      if (part == 0) wo_frags(0); else if (is_up) up_frags();
#pragma unroll
      for (int k = 0; k < 4; ++k) PIN(bw[k]);
    }
    if constexpr (v == UPREP + 1) {
      if (is_up) {
        const u32x4* a1 = reinterpret_cast<const u32x4*>(&sm.hb[1024 + (c >> 2 & 1) * 1024 + (c >> 3) * 512 + (g >> 1) * 256 + (c >> 1 & 1) * 32 + (g & 1) * 16 + (c & 1) * 8]);
        f32x4 d = {0.f, 0.f, 0.f, 0.f};
#pragma unroll
        for (int j = 0; j < 4; ++j) d = mfma64(a1[j * 8], bw[j], d, (c & 1) ? 0xeeeeu : 0x4444u);
        float t = par ? d[2] + d[3] : d[0] + d[1];
        t += dpp<XOR8>(t);
        const auto ab = __builtin_amdgcn_permlane16_swap(__float_as_uint(t), __float_as_uint(t), false, false);
        Af = u2f(ab[0]); Bf = u2f(ab[1]);
      }
      PIN(Af); PIN(Bf);
    }
    if (v == 10 || v == 16) STAMP(15 + v / 6);
    if (v == 20) STAMP(18);
    if (v == 32) STAMP(19);
    if constexpr (v == MLS) {
      LDS_BARRIER();
      float mm[8], ll[8];
#pragma unroll
      for (int w = 0; w < 8; ++w) { const f32x2 t = *reinterpret_cast<const f32x2*>(&sm.ml[w][0]); mm[w] = t[0]; ll[w] = t[1]; }
      float Mwg = mm[0], Lwg = 0.f;
#pragma unroll
      for (int w = 1; w < 8; ++w) Mwg = fmaxf(Mwg, mm[w]);
      if (wave == 0) {
#pragma unroll
        for (int w = 0; w < 8; ++w) Lwg = fmaf(__expf(mm[w] - Mwg), ll[w], Lwg);
        if (lane == 0) st16_sc1(wsr, G_ML + ((head * 2 + b) * 4 + part) * 16, __float_as_uint(Mwg), __float_as_uint(Lwg), tag);
      }
      const float fw = __expf(m - Mwg);
      m = Mwg;
#pragma unroll
      for (int bt = 0; bt < NB; ++bt) { pb[bt] = pack_bf(pr[bt] * fw, pr[bt] * fw); PIN(pb[bt]); }
#pragma unroll
      for (int k = 0; k < 4; ++k) { pm[k] = (c >> 1 & 3) == k ? ((c & 1) ? 0xffff0000u : 0x0000ffffu) : 0u; PIN(pm[k]); }
    }
    SB();
    return true;
  });
  asm volatile(".Ltail_start:");
  if (wave != 0) {
    pv_row(IC<SK0 - 1>{});
    WAIT_VM(WV); SB();
    sfor<NL - 1 - WV, NL - 1>(pv_row);
    *LDSP(f32x4, adst0) = acc;
    WSTAMP(14);
    LDS_BARRIER();
  } else {
    u32x4 xr[8], am[8];
#pragma unroll
    for (int w = 0; w < 8; ++w) am[w] = u32x4{pl8[w] & pm[0], pl8[w] & pm[1], pl8[w] & pm[2], pl8[w] & pm[3]};
#pragma unroll
    for (int w = 0; w < 8; ++w) PIN(am[w]);
    {
      const u32 so = __builtin_amdgcn_readlane(sot, NL - 1);
      gate();
      SB();
#pragma unroll
      for (int w = 0; w < 8; ++w) xr[w] = ld16<AUX_NT>(rv, lane * 16, so ^ ((w & 1) ? col(P, 7) : 0u) ^ ((w & 2) ? col(P, 8) : 0u) ^ ((w & 4) ? col(P, 9) : 0u));
      SB();
    }
    u32 rsum = lds_addr(&sm.red[0][c * 8 + (g & 1) * 4]);
    pv_row(IC<SK0 - 1>{});
    WAIT_VM(WV + 8); SB();
    sfor<NL - 1 - WV, NL - 3>(pv_row);
    if (__builtin_expect(!all_ok((mlg[1] == tag) & (mlg[3] == tag)), 0)) {
#pragma nounroll
      for (int spin = 0; spin < SPIN_MAX; ++spin) {
        CFENCE();
        mlg = ld16<AUX_SC1>(wsr, mlo, 0);
        if (all_ok((mlg[1] == tag) & (mlg[3] == tag))) break;
      }
    }
    merge_factor(mlg);
    sfor<NL - 3, NL - 1>(pv_row);
    STAMP(14); WSTAMP(14);
    LDS_BARRIER();
    STAMP(15);
    f32x4 t[8];
#pragma unroll
    for (int w = 1; w < 8; ++w) t[w] = LDSP(f32x4, rsum)[w * 32];
    acc = ((acc + t[1]) + (t[2] + t[3])) + ((t[4] + t[5]) + (t[6] + t[7]));
    PIN(acc);
    STAMP(3);
    SB();
#pragma unroll
    for (int w = 0; w < 8; ++w) acc = mfma16(am[w], xr[w], acc);
  }
  WAIT_VM(0);
  acc *= fmrg;
  {
    const u32 w0 = min(pack_bf(acc[0], acc[1]), SENT - 1u), w1 = min(pack_bf(acc[2], acc[3]), SENT - 1u);
    const auto e0 = __builtin_amdgcn_permlane16_swap(w0, w0, false, false), e1 = __builtin_amdgcn_permlane16_swap(w1, w1, false, false);
    __builtin_amdgcn_raw_buffer_store_b128(u32x4{e0[0], e1[0], e0[1], e1[1]}, wsr, pso, 0, AUX_SC1);
  }
  st8_sc1(wsr, nto, (u32)wall_clock64() + nadd, tag);
  SB();
  STAMP(4);

  if (part == 2) FINISH();
  if (part == 0) {
    __builtin_amdgcn_s_barrier();
#pragma nounroll
    while (wleft > 0) { until(t1 - (P.PA + wleft * P.PD) * 256); piece(); }
    bool late = false;
    u32x4 og[1];
    f32x4 d[4];
    u32 six = (c & 8) ? 0xeeeeu : 0x4444u;
    PIN(six);
#pragma unroll
    for (int t = 0; t < 4; ++t) { d[t] = f32x4{0.f, 0.f, 0.f, 0.f}; PIN(d[t]); }
    if (wdef) {
      late = true;
      while ((int)(((u32)wall_clock64() - t0) << 8) < t1 - 10 * 256) if (__builtin_amdgcn_s_getreg(7 | (3 << 11)) == 0) { late = false; break; }
      if (__builtin_expect(late, 0)) { until(t1); CFENCE(); og[0] = ld16<AUX_SC1>(wsr, po, 0); SB(); WAIT_VM(1); } else { WAIT_VM(0); }
      CFENCE();
      wo_frags(1);
#pragma unroll
      for (int k = 0; k < 4; ++k) PIN(bw[k]);
      SB();
    }
    if (__builtin_expect(!late, 1)) { until(t1); CFENCE(); og[0] = ld16<AUX_SC1>(wsr, po, 0); }
    STAMP(11);
    SB();
    auto wo_mm = [&]() {
#pragma unroll
      for (int t = 0; t < 4; ++t) d[t] = mfma64(og[0], bw[t], d[t], six);
    };
    auto p_bad = [&]() { return !all_ok(max(max(og[0][0], og[0][1]), max(og[0][2], og[0][3])) != SENT); };
    wo_mm();
    if (__builtin_expect(p_bad(), 0)) {
#pragma nounroll
      for (int spin = 0; spin < SPIN_MAX; ++spin) {
        __builtin_amdgcn_s_sleep(1);
        CFENCE();
        og[0] = ld16<AUX_SC1>(wsr, po, 0);
        if (!p_bad()) break;
      }
#pragma unroll
      for (int t = 0; t < 4; ++t) d[t] = f32x4{0.f, 0.f, 0.f, 0.f};
      wo_mm();
      SB();
      asm volatile(".Lend_d:");
    }
    STAMP(5); WSTAMP(5);
    {
      SB();
      f32x4 y; f32x2 h[4];
#pragma unroll
      for (int t = 0; t < 4; ++t) { h[t] = d[t].xy + d[t].zw; PIN(h[t]); }
#pragma unroll
      for (int t = 0; t < 4; ++t) { y[t] = h[t][0] + h[t][1]; PIN(y[t]); }
      *LDSP(f32x2, rdst) = f32x2{swap32_add(y[0], y[1]), swap32_add(y[2], y[3])};
    }
    STAMP(16);
    LDS_BARRIER();
    STAMP(17);
    if (wave < 2) {
      f32x2 v[4];
#pragma unroll
      for (int w = 0; w < 4; ++w) v[w] = f32x2{LDSP(float, rsrc)[w * 256], LDSP(float, rsrc)[w * 256 + 128]};
      const f32x2 h = (v[0] + v[1]) + (v[2] + v[3]);
      const float y = h[0] + h[1];
      __builtin_amdgcn_raw_buffer_store_b32(min(pack_bf(dpp<0xA0>(y), dpp<0xF5>(y)), SENT - 1u), wsr, xo, 0, AUX_SC1);
      asm volatile(".Lend_a:");
    }
    STAMP(6);
    FINISH();
  }
  if (part == 3) {
    if (!has_w) FINISH();
    u32x4 b2[4];
    {
      __builtin_amdgcn_s_barrier();
      const rsrc_t r2 = mk_rsrc(w2 + (size_t)(blk * 32 + wave * 4) * 512, 4096u);
      const u32 l2 = (u32)(uintptr_t)(&sm.w2b[wave * 4][0]);
#pragma unroll
      for (int n = 0; n < 4; ++n) { until(t1 - (P.QA + (4 - n) * P.QD) * 256); dma16(r2, (lane ^ (n * 2) ^ ((wave & 2) << 2)) * 16, n * 1024, l2 + n * 1024); }
    }
    const u32 tdst = lds_addr(&sm.hb[hpos]);
    const u32 sdst = lds_addr(&sm.stat[wave][lane]);
    const u32 ssrc = lds_addr(&sm.stat[(g >> 1) * 4 + ((lane >> 3) & 3)][(lane & 7) * 4]);
    const u32 adst = lds_addr((c < 4 && (g & 1) == 0) ? &sm.actl[g >> 1][wave * 4 + c] : reinterpret_cast<u16*>(&sm.dummy[lane]));
    const u32 asrc = lds_addr(&sm.actl[lane & 1][g * 8]);
    const u32 apa = lds_addr(&sm.hb[(c >> 3) * 512 + (g >> 1) * 256 + (c >> 1 & 1) * 64 + ((c >> 2) & 1) * 32 + (g & 1) * 16 + (c & 1) * 8]);
    u32 six = (c & 1) ? 0xeeeeu : 0x4444u;
    f32x4 d[2] = {{0.f, 0.f, 0.f, 0.f}, {0.f, 0.f, 0.f, 0.f}};
    PIN(six); PIN(d[0]); PIN(d[1]);
    constexpr u32 xs2 = 16384;
    u32 xoff = dup(G_XP) + near + g * 2048 + ub * 1024 + (wave & 3) * 256 + c * 16;
    u32x4 one;
#pragma unroll
    for (int k = 0; k < 4; ++k) one[k] = ((c & 2) == 0 && (c >> 2) == k) ? ((c & 1) ? 0x3f800000u : 0x00003f80u) : 0u;
    PIN(one);
    u32 ooff = dup(G_OP + (blk * 512 + tid) * 8);
    float Afp = Af, Bfp = Bf;
    PIN(xoff); PIN(ooff); PIN(Afp); PIN(Bfp);
    f32x2 gc = ga * cc1, xc = xin * cc1;
    PIN(ga); PIN(xin); PIN(gc); PIN(xc);
    u32 w2a = (u32)(uintptr_t)(&sm.w2b[0][0]) + (g * 8 + ((lane >> 2) & 3)) * 1024 + (wave ^ (g & 1)) * 128 + (lane & 15) * 8;
    PIN(w2a);
    if (wave == 1) {
      due = t1 + P.N1 * 256;
      const int tn = notices();
      due = tn + P.X2 * 256;
      until(due);
    }
    asm volatile("s_waitcnt vmcnt(0)" ::: "memory");
    STAMP(9);
    __builtin_amdgcn_s_barrier();
    STAMP(11);
    CFENCE();
    u32x4 xg[2] = {ld16<AUX_SC1>(wsr, xoff, 0), ld16<AUX_SC1>(wsr, xoff, xs2)};
    SB();
#pragma unroll
    for (int t = 0; t < 4; ++t) {
      const u32x2 lo = tr_read(w2a ^ (t * 32)), hi = tr_read((w2a ^ (t * 32)) + 4096);
      b2[t] = u32x4{lo[0], lo[1], hi[0], hi[1]};
    }
#pragma unroll
    for (int t = 0; t < 4; ++t) PIN(b2[t]);
    SB();
    const f32x4 z4 = {0.f, 0.f, 0.f, 0.f};
    auto xp_bad = [&]() { return !all_ok(max(max(max(xg[0][0], xg[0][1]), max(xg[0][2], xg[0][3])), max(max(xg[1][0], xg[1][1]), max(xg[1][2], xg[1][3]))) != SENT); };
    f32x4 v = mfma16(one, xg[1], mfma16(one, xg[0], z4));
    if (__builtin_expect(xp_bad(), 0)) {
#pragma nounroll
      for (int spin = 0; spin < SPIN_MAX; ++spin) {
        __builtin_amdgcn_s_sleep(1);
        CFENCE();
        xg[0] = ld16<AUX_SC1>(wsr, xoff, 0); xg[1] = ld16<AUX_SC1>(wsr, xoff, xs2);
        if (!xp_bad()) break;
      }
      v = mfma16(one, xg[1], mfma16(one, xg[0], z4));
      SB();
      asm volatile(".Lend_e:");
    }
    STAMP(5); WSTAMP(5);
    {
      const f32x2 a = v.xy, hc = __builtin_elementwise_fma(gc, a, xc), x1 = __builtin_elementwise_fma(ga, a, xin);
      *LDSP(u32, tdst) = pack_bf(hc[0], hc[1]);
      *LDSP(float, sdst) = swap32_add(x1[0] + x1[1], fmaf(x1[0], x1[0], x1[1] * x1[1]));
    }
    STAMP(16);
    LDS_BARRIER();
    STAMP(17);
    {
      const f32x4 s4 = LDSP(f32x4, ssrc)[0], q4 = LDSP(f32x4, ssrc)[8];
      SB();
      const u32x4 af0 = LDSP(u32x4, apa)[0], af1 = LDSP(u32x4, apa)[16];
      SB();
      const f32x2 s2 = s4.xy + s4.zw, q2 = q4.xy + q4.zw;
      float s = s2[0] + s2[1], q = q2[0] + q2[1];
      SB();
      d[0] = mfma64(af0, bw[0], d[0], six);
      SB();
      s = swap16_add(s, q);
      SB();
      d[1] = mfma64(af0, bw[1], d[1], six);
      SB();
      s += dpp<XOR1>(s); s += dpp<XOR2>(s);
      SB();
      d[0] = mfma64(af1, bw[2], d[0], six);
      SB();
      s += dpp<XOR7>(s); s += dpp<XOR8>(s);
      SB();
      d[1] = mfma64(af1, bw[3], d[1], six);
      SB();
      const auto sq = __builtin_amdgcn_permlane16_swap(__float_as_uint(s), __float_as_uint(s), false, false);
      const float mean = u2f(sq[0]) * (1.f / 512.f);
      const float var = fmaf(u2f(sq[1]), 1.f / 512.f, -mean * mean);
      float ri = __builtin_amdgcn_rsqf(fmaxf(var, 0.f) + 1e-5f);
      PIN(ri); PIN(d[0]); PIN(d[1]);
      SB();
      float y0 = d[0][0] + d[0][1], y1 = d[1][2] + d[1][3], mr = -ri * mean;
      PIN(y0); PIN(y1); PIN(mr);
      float y = y0 + y1, cf = fmaf(mr, Afp, Bfp), rk = ri * -1.44269504f;
      PIN(y); PIN(cf); PIN(rk);
      y = __builtin_bit_cast(float, __builtin_amdgcn_update_dpp(__builtin_bit_cast(int, y), __builtin_bit_cast(int, y), XOR8, 0xa, 0xf, false));
      const float ck = cf * -1.44269504f;
      y = swap16_add(y, y);
      const float ex = __builtin_amdgcn_exp2f(fmaf(y, rk, ck));
      y = fmaf(y, ri, cf);
      const float gu = y * dpp<0x104>(y);
      *LDSP(u16, adst) = (u16)pack_bf(gu * __builtin_amdgcn_rcpf(1.f + ex), 0.f);
    }
    STAMP(18);
    LDS_BARRIER();
    STAMP(10);
    {
      const u32x4 af = *LDSP(u32x4, asrc);
      const f32x4 z = {0.f, 0.f, 0.f, 0.f};
      f32x4 d0 = mfma16(af, b2[0], z), d1 = mfma16(af, b2[1], z), d2 = mfma16(af, b2[2], z), d3 = mfma16(af, b2[3], z);
      asm volatile("" : "+v"(d0), "+v"(d1), "+v"(d2), "+v"(d3));
      const bool g1 = g & 1, g2 = g & 2;
      const float e0 = g1 ? d1[0] : d0[0], e1 = g1 ? d1[1] : d0[1], f0 = g1 ? d3[0] : d2[0], f1 = g1 ? d3[1] : d2[1];
      const u32x2 ov = {pack_bf(g2 ? f0 : e0, g2 ? f1 : e1), tag};
      __builtin_amdgcn_raw_buffer_store_b64(ov, wsr, ooff, near ^ 8192u, AUX_SC1);
      __builtin_amdgcn_raw_buffer_store_b64(ov, wsr, ooff, near, AUX_SC1);
    }
    asm volatile(".Lend_b:");
    STAMP(6);
    FINISH();
  }
  if (wave == 0) {
    if (!all_ok((sg[0][1] == tag) & (sg[0][3] == tag) & (sg[1][1] == tag) & (sg[1][3] == tag))) {
      u32x4 t[2];
      gather16<2>(wsr, G_MOD + (1024 + fd) * 8, 1536 * 8, tag, t);
      sg[0] = t[0]; sg[1] = t[1];
    }
    u32 xa;
    {
      due = t1 + P.N2 * 256;
      const int tn = notices();
      due = tn + P.X3 * 256;
      until(due);
    }
    const bool hb = g & 1;
#pragma nounroll
    for (int spin = 0; spin < SPIN_MAX; ++spin) {
      CFENCE();
      xa = __builtin_amdgcn_raw_buffer_load_b32(wsr, dup(G_XP) + near + (g >> 1) * 16384 + (c >> 2) * 2048 + (g & 1) * 1024 + fd * 2, 0, AUX_SC1);
      if (all_ok(xa != SENT)) break;
      __builtin_amdgcn_s_sleep(1);
    }
    float a0 = lo_f(xa), a1 = hi_f(xa);
    a0 += dpp<0x124>(a0); a1 += dpp<0x124>(a1);
    a0 += dpp<XOR8>(a0); a1 += dpp<XOR8>(a1);
    a0 = swap32_add(a0, a0); a1 = swap32_add(a1, a1);
    f32x2 xv = {rbf(lo_f(sx2) + rbf((hb ? hi_f(sg[0][0]) : lo_f(sg[0][0])) * rbf(a0))), rbf(hi_f(sx2) + rbf((hb ? hi_f(sg[0][2]) : lo_f(sg[0][2])) * rbf(a1)))};
    f32x2 gv = {hb ? hi_f(sg[1][0]) : lo_f(sg[1][0]), hb ? hi_f(sg[1][2]) : lo_f(sg[1][2])};
    PIN(xv); PIN(gv);
    if (wi == 0 && lane == 0) __builtin_amdgcn_raw_buffer_store_b32(tag, wsr, WS_EPOCH, 0, AUX_SC1);
    STAMP(10);
    due = max(due + P.X4 * 256, (int)(((u32)wall_clock64() - t0) << 8) + P.GX * 256);
    const bool last = lane < (NUP - 32) * 4;
    u32 o0 = dup(G_OP + (lane >> 2) * 4096 + fd * 8) + near, o2 = last ? o0 : OOB, tg2 = last ? tag : 0u;
    const rsrc_t rout = mk_rsrc(out, 2048u);
    u32 oo = (lane & 44) == 0 ? (g * 512 + fd) * 2 : OOB;
    const u32 pk = (c & 10) ? 0u : ((c & 4) ? 0x3f800000u : 0x00003f80u);
    u32x4 one = {(c & 1) ? 0u : pk, 0u, (c & 1) ? pk : 0u, 0u};
    PIN(o0); PIN(o2); PIN(tg2); PIN(oo); PIN(one);
    __builtin_amdgcn_raw_buffer_store_b32(pack_bf(xv[0], xv[1]), rout, oo, 0, AUX_SC1);
    u32 s1_ = 131072, s2_ = 262144;
    asm volatile("" : "+s"(s1_), "+s"(s2_));
    until(due);
    f32x4 sv;
#pragma nounroll
    for (int spin = 0; spin < SPIN_MAX; ++spin) {
      CFENCE();
      const u32x4 og[3] = {ld16<AUX_SC1>(wsr, o0, 0), ld16<AUX_SC1>(wsr, o0, s1_), ld16<AUX_SC1>(wsr, o2, s2_)};
      sv = f32x4{0.f, 0.f, 0.f, 0.f};
#pragma unroll
      for (int k = 0; k < 3; ++k) sv = mfma16(one, og[k], sv);
      SB();
      u32 bad = ((og[0][1] ^ tag) | (og[0][3] ^ tag) | (og[1][1] ^ tag)) | ((og[1][3] ^ tag) | (og[2][1] ^ tg2) | (og[2][3] ^ tg2));
      PIN(bad);
      SB();
      float s0 = sv[0] + dpp<0x124>(sv[0]), s1 = sv[1] + dpp<0x124>(sv[1]);
      PIN(s0); PIN(s1);
      s0 += dpp<XOR8>(s0); s1 += dpp<XOR8>(s1);
      PIN(s0); PIN(s1);
      __builtin_amdgcn_raw_buffer_store_b32(pack_bf(fmaf(gv[0], s0, xv[0]), fmaf(gv[1], s1, xv[1])), rout, oo, 0, AUX_SC1);
      SB();
      if (all_ok(bad == 0u)) break;
      __builtin_amdgcn_s_sleep(1);
    }
    STAMP(7);
    asm volatile(".Lend_c:");
  }
  FINISH();
}

extern "C" void kernel_launch(void* const* d_in, const int* in_sizes, int n_in,
                              void* d_out, int out_size, void* d_ws, size_t ws_size,
                              hipStream_t stream) {
  fused_kernel<<<256, 512, 0, stream>>>((const u16*)d_in[1], (const u16*)d_in[2], (const u16*)d_in[4], (const u16*)d_in[3], (const u16*)d_in[10], (const u16*)d_in[6], (const u16*)d_in[7],
                                        (unsigned char*)d_ws, (const u16*)d_in[0], (const u16*)d_in[9], (const u16*)d_in[8], (u16*)d_out);
}
```

```cpp
#include <hip/hip_runtime.h>
#include <stdint.h>


typedef unsigned int u32;
typedef unsigned short u16;
typedef unsigned long long u64;
typedef __attribute__((ext_vector_type(4))) unsigned u32x4;
typedef __attribute__((ext_vector_type(2))) unsigned u32x2;
typedef __attribute__((ext_vector_type(2))) __bf16 bf16x2;
typedef __attribute__((ext_vector_type(4))) float f32x4;
typedef __attribute__((ext_vector_type(2))) float f32x2;
typedef __amdgpu_buffer_rsrc_t rsrc_t;

constexpr int NH = 32, SK = 16384, DH = 128, WIN = 4096, FF = 1376;
struct Prm { int E0, ES, E1, E2, E2C, EU, TEND, DT_UP, L0, L1, X1, X2, X3, X4, NE, FE, M0, M1, M2, M3, M4, GS, GX, N1, N2, GA, WD, DT_WO, LW, LU, EW, PM, NF, NR, C0, A0, AG, AC, TS, TW, TK, TG, GB, B0, BG, BF, D0, DG, TC, LM, C1, F0, FG, FF_, PA, PD, QA, QD, RT, W[9]; };
constexpr Prm PRM0 = {32, 20, 32, 32, 60, 60, 1976, 76, 12, 27, 75, 63, 100, 75, 3, 8, 0x2511f55b, 0x2b7425d5, 0x6017665, 0x17e0b9ec, 0x31944305, 62, 30, 29, 62, 50, 4, 106, 50, 28, 10, 0x0, 12, 14, 3, 36, 4, 4, 509, 219, 80, -2, 6, 69, 10, 8, 8, 6, 87, 120, 2, 65, 6, 4, 52, 14, 9, 18, 12, 0x77777777, 0x77777777, 0x77777777, 0x77777777, 0x77777777, 0x44444477, 0x44444444, 0x44444444, 0x44444444};
constexpr int NL = 37;
constexpr int SK0 = 21, SK1 = 32;
constexpr int NCMIN = (NL - SK1 + SK0) * 8;
constexpr int WK = 8, WV = 5;
constexpr int WUSE = 12, LNS = 7;
constexpr int QP = 14, QD = 4;
constexpr int VQ = 6;
constexpr int NSL = (NL + 7) / 8;
constexpr int VS = VQ + NSL;
constexpr int MLS = VS + 2;
constexpr int VSIDE = MLS - 5, UPREP = MLS + 6;
constexpr int CPF = 17;
constexpr int SMO = 1;
constexpr int RVS = 5, RVP = 8, RVL = 14, RVU = 18;
constexpr int RPS = 23, RPP = 27;
constexpr int PRB = 12;
constexpr int TBS = 7, TBP = 10, TBL = 13, TBU = 17;
constexpr int window(int s, int w) {
  int n = w;
  for (int u = s - w + 1; u <= s; ++u) n += u == SMO ? 6 : (u == TBL || u == SK1 || u == NL + CPF || u == NL + RVL || u == NL + SK1 ? 1 : (u == QP || u == NL + VQ - 4 ? 2 : (u == NL + VSIDE ? 4 : 0)));
  return n;
}
constexpr int cons_end(int v) {
  const int a = v - WV + 1, c = (v - MLS) * 4;
  const int m = a < c ? a : c;
  return m < 0 ? 0 : (m > NL ? NL : m);
}
constexpr u32 col(const Prm& P, int j) { const int m[6] = {P.M0, P.M1, P.M2, P.M3, P.M4, P.PM}; return (u32)(m[j / 3] >> (j % 3 * 10) & 1023) << 10; }
constexpr u32 first_col(const Prm& P, int k) {
  if (k < 4) return col(P, 12 + k) | 1u << (24 + k);
  if (k < 6) return col(P, k - 4);
  if (k < 8) return col(P, 4 + k) | 1u << (16 + k);
  if (k < 11) return col(P, k - 1);
  return (col(P, 2) ^ col(P, 3) ^ col(P, 4) ^ col(P, 5) ^ col(P, 6) ^ col(P, 10) ^ col(P, 11)) | 0xf00000u;
}
constexpr bool one_to_one(const Prm& P) {
  u32 c[10] = {};
  for (int j = 0; j < 10; ++j) c[j] = col(P, j);
  for (int j = 0; j < 10; ++j) {
    if (c[j] == 0) return false;
    const u32 low = c[j] & (0u - c[j]);
    for (int k = j + 1; k < 10; ++k) if (c[k] & low) c[k] ^= c[j];
  }
  return true;
}
static_assert(one_to_one(PRM0), "every KB of the window exactly once");
struct Tab { u32 e[512]; };
constexpr Tab make_tab(const Prm& P) {
  Tab T{};
  for (int bid = 0; bid < 256; ++bid) {
    const int xcd = bid & 7, part = bid >> 4 & 3, i8 = (bid >> 3 & 1) | bid >> 6 << 1, wi = i8 * 8 + xcd;
    const bool has_mod = wi < 48, has_up = wi >= 21;
    const int cb0 = (i8 + P.C0) & 7, cb1 = (i8 + P.C1) & 7, ms = wi - 20;
    const int mi = wi < 8 ? wi : (wi < 16 ? 8 + ((xcd + 4) & 7) : (wi < 20 ? (wi - 14) * 8 + 3 : 16 + ms / 7 * 8 + ms % 7 + (ms % 7 >= 3 ? 1 : 0)));
    const int ui = xcd * 5 + (xcd > 5 ? xcd - 5 : 0) + (i8 == 6 ? 0 : (xcd >= 5 ? 1 : 0) + (i8 == 7 ? 4 : i8 - 2));
    const bool edge = i8 >= 6;
    const int wd = cb0 == 3 ? 0 : P.WD;
    const int e[4] = {P.E0 - 4 * wd + (cb0 == 3 ? P.ES : 0), P.E1 + (cb1 == 3 ? P.ES : 0), has_mod ? (wi < 16 ? P.E2C : P.E2) + ((mi & 7) == 3 ? P.ES : 0) : 0, has_up ? P.EU : 0};
    int tw[4] = {}, win[4] = {}, span[4] = {}, k0[4] = {};
    for (int k = 0; k < 4; ++k) {
      const int np = k == 0 ? 8 - wd : 8;
      tw[k] = np ? e[k] * P.TW / (16 * np) : 0;
      k0[k] = P.NF + tw[k] * np / 2 > P.TK ? P.NF + tw[k] * np / 2 : P.TK;
      span[k] = P.TEND - k0[k] - P.TG - (k == 0 ? (wd ? P.DT_WO : P.A0) : (k == 3 && has_up ? P.DT_UP : 0));
      win[k] = span[k] - (k == 3 && has_up ? P.GA + (edge ? P.LM : 0) : 0);
    }
    int n[4] = {NCMIN / 8, NCMIN / 8, NCMIN / 8, NCMIN / 8};
    for (int left = 128 - NCMIN / 2; left > 0; --left) {
      int best = -1;
      for (int k = 3; k >= 0; --k)
        if (n[k] < NL && (best < 0 || n[k] * win[best] < n[best] * win[k])) best = k;
      ++n[best];
    }
    const int c[4] = {0, n[0] - 1, n[0] + n[1] - 2, n[0] + n[1] + n[2] - 3};
    const int nl = n[part];
    const bool has_w = part < 2 || (part == 2 ? has_mod : has_up);
    const int blk = !has_w ? 0 : (part == 0 ? cb0 : (part == 1 ? xcd * 8 + cb1 : (part == 2 ? mi : ui)));
    T.e[2 * bid] = (u32)(c[part] | nl << 7 | blk << 13 | (has_w ? 1 : 0) << 19 | (part == 0 ? wd : 0) << 20) | (u32)tw[part] << 24;
    T.e[2 * bid + 1] = (u32)(span[part] * P.TS / (4 * (nl - 1)) | span[part] * (1024 - P.TS) / (4 * (nl - 1)) << 16);
  }
  return T;
}
constexpr int step_range(const Prm& P, bool mx_) {
  int mx = 0, mn = 1 << 20;
  const Tab T = make_tab(P);
  for (int bid = 0; bid < 256; ++bid) { const int n = T.e[2 * bid] >> 7 & 63; mx = n > mx ? n : mx; mn = n < mn ? n : mn; }
  return mx_ ? mx : mn;
}
extern "C" __attribute__((used, visibility("protected"))) __constant__ const Tab wg_tab = make_tab(PRM0);
static_assert(step_range(PRM0, true) <= NL && step_range(PRM0, false) >= NCMIN / 8, "a wave's key range does not fit its load steps");
static_assert(RVU < UPREP && UPREP + 1 < SK0 && QP + QD + 1 < SK0 && cons_end(SK0 - 1) == SK0 - WV && NL - SK1 == WV, "steps every wave runs");
constexpr bool timers_long(const Prm& P) {
  const Tab T = make_tab(P);
  for (int bid = 48; bid < 64; ++bid) if (SK0 + NL - (int)(T.e[2 * bid] >> 7 & 63) > RPS) return false;
  return true;
}
static_assert(timers_long(PRM0), "steps of the second report");
constexpr int NUP = FF / 32;
constexpr int SPIN_MAX = 1 << 17;

constexpr u32 WS_EPOCH = 0;
constexpr u32 G_TB = 1024;
constexpr u32 G_MOD = 4096;
constexpr u32 G_Q = G_MOD + 3072 * 8;
constexpr u32 G_ML = G_Q + 4096 * 8;
constexpr u32 G_U = G_ML + 256 * 16;
constexpr u32 G_P = 0;
constexpr u32 G_XP = G_P + 256 * 256;
constexpr u32 G_OP = G_XP + 8 * 512 * 4;
constexpr u32 G_NT = G_OP + 43 * 512 * 8;
constexpr u32 G_PB = G_OP + 44 * 512 * 8;
constexpr u32 G_VT = G_U + 2 * (G_PB + 8192);
constexpr u32 WS_END = G_VT + 2048;
__device__ __forceinline__ u32 dup(u32 o) { return G_U + ((o >> 13) << 14) + (o & 8191u); }
constexpr u32 SENT = 0xffffffffu;
constexpr u32 OOB = 0x40000000u;

constexpr int AUX_NT = 2, AUX_SC1 = 16;

__device__ __forceinline__ float lo_f(u32 v) { return __uint_as_float(v << 16); }
__device__ __forceinline__ float hi_f(u32 v) { return __uint_as_float(v & 0xffff0000u); }
__device__ __forceinline__ float rbf(float x) { return (float)(__bf16)x; }
__device__ __forceinline__ u32 pack_bf(float lo, float hi) {
  bf16x2 r; r[0] = (__bf16)lo; r[1] = (__bf16)hi; return __builtin_bit_cast(u32, r);
}
__device__ __forceinline__ float dot2(u32 k, u32 q, float acc) {
  return __builtin_amdgcn_fdot2_f32_bf16(__builtin_bit_cast(bf16x2, k), __builtin_bit_cast(bf16x2, q), acc, false);
}
template <int CTRL> __device__ __forceinline__ float dpp(float x) {
  return __builtin_bit_cast(float, __builtin_amdgcn_mov_dpp(__builtin_bit_cast(int, x), CTRL, 0xf, 0xf, true));
}
constexpr int XOR1 = 0xB1, XOR2 = 0x4E, XOR7 = 0x141, XOR8 = 0x128;
__device__ __forceinline__ float swap32_add(float a, float b) {
  auto t = __builtin_amdgcn_permlane32_swap(__float_as_uint(a), __float_as_uint(b), false, false);
  return __uint_as_float(t[0]) + __uint_as_float(t[1]);
}
__device__ __forceinline__ float swap16_add(float a, float b) {
  auto t = __builtin_amdgcn_permlane16_swap(__float_as_uint(a), __float_as_uint(b), false, false);
  return __uint_as_float(t[0]) + __uint_as_float(t[1]);
}
__device__ __forceinline__ float wave_sum(float x) {
  x += dpp<XOR1>(x); x += dpp<XOR2>(x); x += dpp<XOR7>(x); x += dpp<XOR8>(x);
  x = swap16_add(x, x);
  return swap32_add(x, x);
}
__device__ __forceinline__ float wave_max(float x) {
  x = fmaxf(x, dpp<XOR1>(x)); x = fmaxf(x, dpp<XOR2>(x)); x = fmaxf(x, dpp<XOR7>(x)); x = fmaxf(x, dpp<XOR8>(x));
  auto s = __builtin_amdgcn_permlane16_swap(__float_as_uint(x), __float_as_uint(x), false, false);
  x = fmaxf(__uint_as_float(s[0]), __uint_as_float(s[1]));
  auto t = __builtin_amdgcn_permlane32_swap(__float_as_uint(x), __float_as_uint(x), false, false);
  return fmaxf(__uint_as_float(t[0]), __uint_as_float(t[1]));
}
template <int CTRL> __device__ __forceinline__ float sel_add(float lo, float hi, bool up) {
  const float t1 = lo + dpp<CTRL>(lo), t2 = hi + dpp<CTRL>(hi);
  return up ? t2 : t1;
}
template <int I> __device__ __forceinline__ float bc16(float x) {
  return __builtin_bit_cast(float, __builtin_amdgcn_update_dpp(0, __builtin_bit_cast(int, x), 0x150 + I, 0xf, 0xf, false));
}
__device__ __forceinline__ float bcast16(float x, int i) {
  switch (i) {
    case 0: return bc16<0>(x); case 1: return bc16<1>(x); case 2: return bc16<2>(x); case 3: return bc16<3>(x);
    case 4: return bc16<4>(x); case 5: return bc16<5>(x); case 6: return bc16<6>(x); case 7: return bc16<7>(x);
    case 8: return bc16<8>(x); case 9: return bc16<9>(x); case 10: return bc16<10>(x); case 11: return bc16<11>(x);
    case 12: return bc16<12>(x); case 13: return bc16<13>(x); case 14: return bc16<14>(x); default: return bc16<15>(x);
  }
}
__device__ __forceinline__ float silu_f(float x) { return x * __builtin_amdgcn_rcpf(1.f + __expf(-x)); }

template <int AUX> __device__ __forceinline__ u32x4 ld16(rsrc_t r, u32 voff, u32 soff) {
  return __builtin_amdgcn_raw_buffer_load_b128(r, voff, soff, AUX);
}
template <int AUX> __device__ __forceinline__ u32x2 ld8(rsrc_t r, u32 voff, u32 soff) {
  return __builtin_amdgcn_raw_buffer_load_b64(r, voff, soff, AUX);
}
__device__ __forceinline__ void st8_sc1(rsrc_t r, u32 voff, u32 v, u32 tag) {
  __builtin_amdgcn_raw_buffer_store_b64(u32x2{v, tag}, r, voff, 0, AUX_SC1);
}
__device__ __forceinline__ void st16_sc1(rsrc_t r, u32 voff, u32 v0, u32 v1, u32 tag) {
  __builtin_amdgcn_raw_buffer_store_b128(u32x4{v0, tag, v1, tag}, r, voff, 0, AUX_SC1);
}
__device__ __forceinline__ rsrc_t mk_rsrc(const void* p, u32 bytes) {
  return __builtin_amdgcn_make_buffer_rsrc(const_cast<void*>(p), 0, bytes, 0x00020000);
}
#define WAIT_VM(n) __builtin_amdgcn_s_waitcnt(((n) & 15) | (((n) >> 4) << 14) | (7 << 4) | (15 << 8))
#define SB() __builtin_amdgcn_sched_barrier(0)
#define CFENCE() asm volatile("" ::: "memory")
__device__ __forceinline__ bool all_ok(bool ok) { return __builtin_amdgcn_ballot_w64(!ok) == 0ull; }

template <int N> __device__ __forceinline__ void gather16(rsrc_t ws, u32 voff, u32 stride, u32 tag, u32x4 (&g)[N]) {
#pragma nounroll
  for (int spin = 0; spin < SPIN_MAX; ++spin) {
    CFENCE();
    bool ok = true;
#pragma unroll
    for (int i = 0; i < N; ++i) g[i] = ld16<AUX_SC1>(ws, voff + i * stride, 0);
#pragma unroll
    for (int i = 0; i < N; ++i) ok &= (g[i][1] == tag) & (g[i][3] == tag);
    if (all_ok(ok)) break;
    __builtin_amdgcn_s_sleep(8);
  }
}
template <int N> __device__ __forceinline__ void gather8(rsrc_t ws, u32 voff, u32 stride, u32 tag, u32 (&g)[N], bool act = true) {
#pragma nounroll
  for (int spin = 0; spin < SPIN_MAX; ++spin) {
    CFENCE();
    bool ok = true;
    u32x2 t[N];
#pragma unroll
    for (int i = 0; i < N; ++i) t[i] = ld8<AUX_SC1>(ws, voff + i * stride, 0);
#pragma unroll
    for (int i = 0; i < N; ++i) { ok &= (t[i][1] == tag); g[i] = t[i][0]; }
    if (all_ok(ok | !act)) break;
    __builtin_amdgcn_s_sleep(8);
  }
}

struct Smem {
  float red[8][128];
  float ml[8][2];
  float r2[64 * 20];
  u16 hb[3 * 1024];
  float stat[8][64];
  float red2[8][128];
  u32 dummy[64];
  u32x2 bc, bc2;
  u32 bc3, bc4;
  u32x4 zero[4];
  float pl[4][8];
  u32 mflag[8];
  u32x4 wbuf[8][8][64];
  __attribute__((aligned(256))) u16 w2b[32][512];
  u16 actl[2][32];
};

#define PIN(x) asm volatile("" : "+v"(x))
#define LDS_BARRIER() do { __builtin_amdgcn_s_waitcnt(0xc07f); __builtin_amdgcn_s_barrier(); CFENCE(); } while (0)
#define WAVE_SYNC() do { __builtin_amdgcn_fence(__ATOMIC_RELEASE, "wavefront"); __builtin_amdgcn_wave_barrier(); __builtin_amdgcn_fence(__ATOMIC_ACQUIRE, "wavefront"); } while (0)

__device__ __forceinline__ void dma16(rsrc_t r, u32 voff, u32 soff, u32 lds_dst) {
  u32 keep;
  asm volatile("s_mov_b32 %0, m0\n\ts_mov_b32 m0, %4\n\ts_nop 4\n\tbuffer_load_dwordx4 %1, %2, %3 offen nt lds\n\ts_mov_b32 m0, %0"
               : "=&s"(keep) : "v"(voff), "s"(r), "s"(soff), "s"(lds_dst) : "memory");
}
__device__ __forceinline__ void ld16_into(u32x4& d, rsrc_t r, u32 voff, u32 soff) {
  asm volatile("s_nop 4\n\tbuffer_load_dwordx4 %0, %1, %2, %3 offen nt" : "+v"(d) : "v"(voff), "s"(r), "s"(soff) : "memory");
}
__device__ __forceinline__ u32 perm_lo(u32 a, u32 b) { return __builtin_amdgcn_perm(b, a, 0x05040100u); }
__device__ __forceinline__ u32 perm_hi(u32 a, u32 b) { return __builtin_amdgcn_perm(b, a, 0x07060302u); }
__device__ __forceinline__ float rdl(float x, int l) { return __int_as_float(__builtin_amdgcn_readlane(__float_as_int(x), l)); }

typedef __attribute__((ext_vector_type(8))) __bf16 bf16x8;
__device__ __forceinline__ f32x4 mfma16(u32x4 a, u32x4 b, f32x4 c) {
  return __builtin_amdgcn_mfma_f32_16x16x32_bf16(__builtin_bit_cast(bf16x8, a), __builtin_bit_cast(bf16x8, b), c, 0, 0, 0);
}
typedef __attribute__((ext_vector_type(8))) unsigned u32x8;
typedef __attribute__((ext_vector_type(16))) __bf16 bf16x16;
__device__ __forceinline__ f32x4 mfma64(u32x4 a, u32x8 b, f32x4 c, u32 idx) {
  return __builtin_amdgcn_smfmac_f32_16x16x64_bf16(__builtin_bit_cast(bf16x8, a), __builtin_bit_cast(bf16x16, b), c, idx, 0, 0);
}
__device__ __forceinline__ float sum8(const float* p) {
  const f32x4 a = *reinterpret_cast<const f32x4*>(p), b = *reinterpret_cast<const f32x4*>(p + 4);
  return ((a[0] + b[0]) + (a[1] + b[1])) + ((a[2] + b[2]) + (a[3] + b[3]));
}
__device__ __forceinline__ float ln_ri(float var) {
  return __builtin_amdgcn_rcpf(rbf(__builtin_amdgcn_sqrtf(rbf(rbf(fmaxf(var, 0.f)) + 1e-5f))));
}
__device__ __forceinline__ void ln_stats2(const float (&x0)[8], const float (&x1)[8], float& mb0, float& ri0, float& mb1, float& ri1) {
  const float k0 = rdl(x0[0], 0), k1 = rdl(x1[0], 0);
  float s0 = 0.f, s1 = 0.f, q0 = 0.f, q1 = 0.f;
#pragma unroll
  for (int k = 0; k < 8; ++k) {
    const float d0 = x0[k] - k0, d1 = x1[k] - k1;
    s0 += d0; s1 += d1; q0 = fmaf(d0, d0, q0); q1 = fmaf(d1, d1, q1);
  }
  float t = swap16_add(swap32_add(s0, s1), swap32_add(q0, q1));
  t += dpp<XOR1>(t); t += dpp<XOR2>(t); t += dpp<XOR7>(t); t += dpp<XOR8>(t);
  auto u = __builtin_amdgcn_permlane16_swap(__float_as_uint(t), __float_as_uint(t), false, false);
  const float ms = __uint_as_float(u[0]) * (1.f / 512.f), mq = __uint_as_float(u[1]) * (1.f / 512.f);
  const float ri = ln_ri(fmaf(-ms, ms, mq));
  mb0 = rbf(k0 + rdl(ms, 0)); mb1 = rbf(k1 + rdl(ms, 32));
  ri0 = rdl(ri, 0); ri1 = rdl(ri, 32);
}
template <int I> struct IC { static constexpr int v = I; };
template <int I, int N, class F> __device__ __forceinline__ void sfor(F&& f) {
  if constexpr (I < N) { if (f(IC<I>{})) sfor<I + 1, N>(f); }
}
typedef __attribute__((ext_vector_type(4))) short s16x4;
typedef __attribute__((address_space(3))) s16x4* lds_s16x4p;
__device__ __forceinline__ u32x2 tr_read(u32 lds_addr) {
  return __builtin_bit_cast(u32x2, __builtin_amdgcn_ds_read_tr16_b64_v4i16((lds_s16x4p)lds_addr));
}
__device__ __forceinline__ u32 next_tag(u32 e) { return ((e | 0x40004000u) + 1u) & ~0x40004000u; }
__device__ __forceinline__ float u2f(u32 v) { return __uint_as_float(v); }
#define LDSP(T, a) ((__attribute__((address_space(3))) T*)(a))
template <class T> __device__ __forceinline__ u32 lds_addr(T* p) { u32 a = (u32)(uintptr_t)p; PIN(a); return a; }

#define TAB_LOAD "s_add_u32 s30, s28, wg_tab@rel32@lo+4\ns_addc_u32 s31, s29, wg_tab@rel32@hi+12\nglobal_load_dwordx2 %[te], %[va], s[30:31]\n"
#define FC(k) ".Lfc" #k
#define FX(r, bit, k) "s_bitcmp1_b32 %[" #r "], " #bit "\ns_cselect_b32 %[sb], " FC(k) ", 0\ns_xor_b32 %[sa], %[sa], %[sb]\n"
__global__ void __launch_bounds__(512, 2) fused_kernel(const u16* kc_, const u16* vc_, const u16* wo_, const u16* wq_, const u16* wmod_, const u16* w1_, const u16* w3_,
                                                       unsigned char* ws_, const u16* x_, const u16* cnd_, const u16* w2_, u16* out_) {
  __shared__ __attribute__((aligned(256))) Smem sm;
  const u64 a_ka = (u64)__builtin_amdgcn_kernarg_segment_ptr();
  const int bid = blockIdx.x, tid = threadIdx.x;
  u64 wa, t64; u32x2 te; int tid0;
  u32 ar[14];
  u32 tch2, sa_, sb_, va_;
  u32x4 kr[NL], vr[NL];
  asm volatile(".set .Lfc0, %0\n.set .Lfc1, %1\n.set .Lfc2, %2\n.set .Lfc3, %3\n.set .Lfc4, %4\n.set .Lfc5, %5\n.set .Lfc6, %6\n.set .Lfc7, %7\n.set .Lfc8, %8\n.set .Lfc9, %9\n.set .Lfc10, %10\n.set .Lfc11, %11"
    :: "n"(first_col(PRM0, 0)), "n"(first_col(PRM0, 1)), "n"(first_col(PRM0, 2)), "n"(first_col(PRM0, 3)), "n"(first_col(PRM0, 4)), "n"(first_col(PRM0, 5)),
       "n"(first_col(PRM0, 6)), "n"(first_col(PRM0, 7)), "n"(first_col(PRM0, 8)), "n"(first_col(PRM0, 9)), "n"(first_col(PRM0, 10)), "n"(first_col(PRM0, 11)));
  asm volatile(
    "s_load_dwordx16 s[4:19], %[ka], 0x0\ns_load_dwordx8 s[20:27], %[ka], 0x40\n"
    "s_memrealtime %[t]\n"
    "v_lshlrev_b32 %[va], 3, %[bid]\n"
    "s_getpc_b64 s[28:29]\n.Lcpf_first:\n"
    TAB_LOAD
    "s_cmp_gt_u32 %[bid], 15\ns_cbranch_scc1 .Lno_cpf\n"
    "s_mov_b32 %[sa], .Lend_a-.Lcpf_first\ns_max_i32 %[sa], %[sa], .Lend_b-.Lcpf_first\ns_max_i32 %[sa], %[sa], .Lend_c-.Lcpf_first\ns_max_i32 %[sa], %[sa], .Lend_d-.Lcpf_first\ns_max_i32 %[sa], %[sa], .Lend_e-.Lcpf_first\n"
    "v_lshlrev_b32 %[va], 6, %[tid]\nv_min_u32 %[va], %[sa], %[va]\nglobal_load_dword %[pf], %[va], s[28:29]\n"
    ".Lno_cpf:\n"
    "v_readfirstlane_b32 %[t0], %[tid]\nv_and_b32 %[va], 63, %[tid]\n"
    "s_mov_b32 %[sa], " FC(11) "\n"
    FX(bid, 0, 0) FX(bid, 1, 1) FX(bid, 2, 2) FX(bid, 3, 3) FX(bid, 4, 4) FX(bid, 5, 5) FX(bid, 6, 6) FX(bid, 7, 7) FX(t0, 6, 8) FX(t0, 7, 9) FX(t0, 8, 10)
    "v_lshl_add_u32 %[va], %[va], 4, %[sa]\n"
    "s_waitcnt lgkmcnt(0)\nglobal_load_dwordx4 %[k0], %[va], s[4:5] nt\nglobal_load_dwordx4 %[v0], %[va], s[6:7] nt\n"
    "s_bitcmp1_b32 %[t0], 6\ns_cselect_b64 s[14:15], s[16:17], s[14:15]\n"
    "s_bitcmp1_b32 %[bid], 4\ns_cselect_b64 s[8:9], s[10:11], s[8:9]\ns_cselect_b64 s[12:13], s[14:15], s[12:13]\ns_bitcmp1_b32 %[bid], 5\ns_cselect_b64 %[wa], s[12:13], s[8:9]"
    : "=&{s4}"(ar[0]), "=&{s5}"(ar[1]), "=&{s6}"(ar[2]), "=&{s7}"(ar[3]), "=&{s18}"(ar[4]), "=&{s19}"(ar[5]), "=&{s20}"(ar[6]), "=&{s21}"(ar[7]), "=&{s22}"(ar[8]), "=&{s23}"(ar[9]),
      "=&{s24}"(ar[10]), "=&{s25}"(ar[11]), "=&{s26}"(ar[12]), "=&{s27}"(ar[13]), [wa] "=&s"(wa), [t0] "=&s"(tid0), [t] "=&s"(t64), [te] "=&v"(te), [sa] "=&s"(sa_), [sb] "=&s"(sb_),
      [va] "=&v"(va_), [pf] "=&v"(tch2), [k0] "=&v"(kr[0]), [v0] "=&v"(vr[0])
    : [ka] "s"(a_ka), [bid] "s"(bid), [tid] "v"(tid) : "s8", "s9", "s10", "s11", "s12", "s13", "s14", "s15", "s16", "s17", "s28", "s29", "s30", "s31", "scc", "memory");
  asm volatile("" : "+s"(ar[0]), "+s"(ar[1]), "+s"(ar[2]), "+s"(ar[3]), "+s"(ar[4]), "+s"(ar[5]), "+s"(ar[6]), "+s"(ar[7]), "+s"(ar[8]), "+s"(ar[9]), "+s"(ar[10]), "+s"(ar[11]), "+s"(ar[12]), "+s"(ar[13]));
  asm volatile("" : "+s"(wa), "+s"(t64), "+s"(tid0), "+s"(sa_));
  const u32 tka = (u32)wall_clock64();
  const u32 t0 = tka;
  __builtin_assume(tid >= 0 && tid < 512 && tid0 >= 0 && tid0 < 512 && bid >= 0 && bid < 256);
  constexpr Prm P = PRM0;
  const int TEND = P.TEND;
  const int xcd = bid & 7, part = bid >> 4 & 3;
  const int lane = tid & 63, wave = tid0 >> 6;
  SB();
  const bool p3 = part == 3;
  u32 pitch = (u32)(0x005600c001000020ull >> (part * 16) & 0xffff) * 32u;
  const int sh = p3 ? 2 : 3;
  u32 wv0 = (u32)((p3 ? (wave >> 1) * 128 : wave * 64) + (part == 0 ? xcd * 512 : 0) + (lane >> sh)) * pitch + ((lane & (p3 ? 3 : 7)) ^ (lane >> (p3 ? 5 : 3))) * 16u;
  const u32 wimg = (u32)(uintptr_t)(&sm.wbuf[0][0][0]);
  u32 wsz = (part == 0 ? 4096u : 512u) * pitch, wld = wimg + (p3 ? (wave & 1) * 32768 + (wave >> 1) * 8192 : wave * 8192);
  PIN(wv0);
  asm volatile("" : "+s"(wsz), "+s"(wld), "+s"(pitch));
  auto karg = [&](int i) { return ar[2 * i] | (u64)ar[2 * i + 1] << 32; };
  const u64 a_kc = karg(0), a_vc = karg(1);
  unsigned char* ws = (unsigned char*)karg(2);
  const u16 *x = (const u16*)karg(3), *cnd = (const u16*)karg(4), *w2 = (const u16*)karg(5);
  u16* out = (u16*)karg(6);
  const rsrc_t wsr = mk_rsrc(ws, WS_END);
  asm volatile("s_waitcnt vmcnt(2)" : "+v"(te));
  const u32 te0 = __builtin_amdgcn_readfirstlane(te[0]), te1 = __builtin_amdgcn_readfirstlane(te[1]);
  const int bl0 = te0 & 127, nl = te0 >> 7 & 63, blk = te0 >> 13 & 63;
  const bool has_w = te0 >> 19 & 1;
  const rsrc_t rw = mk_rsrc((const void*)wa, wsz);
  const u32 wstep = pitch << (6 - sh), wflip = (p3 || part == 0) ? 32u : 0u, wflip0 = part == 0 ? 32u : 0u;
  u32 wso = blk << (sh + 4);
  int wleft = 8;
  auto piece = [&]() {
    dma16(rw, wv0, wso, wld);
    wso += wstep; wld += 1024;
    wv0 ^= (wleft & 1) ? wflip : wflip0;
    --wleft;
  };
  SB();
  const int b = bid >> 3 & 1, head = xcd * 4 + (bid >> 6 ^ 3);
  const int g = lane >> 4, c = lane & 15;
  const int i8 = b | bid >> 6 << 1, wi = i8 * 8 + xcd;
  const bool is_up = p3 && has_w, modc = part == 2 && wi < 16;
  const size_t base = ((size_t)(b * NH + head) * SK + (SK - WIN)) * DH;
  const rsrc_t rk = mk_rsrc((const u16*)a_kc + base, WIN * DH * 2), rv = mk_rsrc((const u16*)a_vc + base, WIN * DH * 2);
  int tau = te1 & 0xffff;
  const int tauw = te0 >> 24 << 7, wdef = te0 >> 20 & 15;
  int due = P.NF * 256, now = 0;
  auto gate = [&]() {
    while (now < due) { now = (int)(((u32)wall_clock64() - t0) << 8); if (now < due) __builtin_amdgcn_s_sleep(1); }
    due += tau;
  };
  auto until = [&](int t) { while ((int)(((u32)wall_clock64() - t0) << 8) < t); };
  const u32 pslot = dup(G_P + head * 2048 + (b * 4 + part) * 16);
  const u32 die = __builtin_amdgcn_s_getreg(20 | (2 << 6));
  const bool voter = p3 && i8 == 1, timer = p3 && i8 < 2;
  int vote = 0, lag = 0, shift = 0;
  const u32 pbo = dup(G_PB) + (xcd * 8 + wave) * 128;
  u32 near = 0;
  u32x2 ep; u32 xs0, xs1, tch1; u32x4 xr0, xr1;
  const rsrc_t rx = mk_rsrc(part == 2 ? cnd : x, (part == 1 || (part == 2 && has_w)) ? 2048u : 0u);
  auto small_ops = [&]() {
    ep = ld8<AUX_SC1>(wsr, wave == 7 ? WS_EPOCH : OOB, 0);
    xs0 = __builtin_amdgcn_raw_buffer_load_b16(rx, tid * 2, 0, 0); xs1 = __builtin_amdgcn_raw_buffer_load_b16(rx, tid * 2, 1024, 0);
    xr0 = ld16<0>(rx, part == 1 ? lane * 16 : OOB, 0); xr1 = ld16<0>(rx, part == 1 ? lane * 16 : OOB, 1024);
    tch1 = __builtin_amdgcn_raw_buffer_load_b32(mk_rsrc(w2 + (size_t)blk * 32 * 512, is_up ? 64u : 0u), tid == 384 ? 0 : OOB, 0, AUX_NT);
  };
  if (has_w) {
    if (modc) now = 1 << 30;
#pragma nounroll
    while (wleft > wdef) { gate(); due += tauw - tau; piece(); }
    now = 0;
  }
  due = max(due, P.TK * 256);
  SB();
  u32 sot = sa_ & 0xffc00u;
  {
    const u32 s1 = (bl0 + lane - 1 - (lane >= SK0 ? NL - nl : 0)) ^ (124 + part);
#pragma unroll
    for (int k = 0; k < 7; ++k) sot ^= (u32)-(int)(s1 >> k & 1u) & col(P, k);
    PIN(sot);
  }
  u32 tag = 0;
  u32x4 bf[8];
  auto wo_prep = [&]() {
    const u32 wi_ = wimg + wave * 8192;
    const int q = (lane >> 2) & 3, pp = lane & 3;
#pragma unroll
    for (int s = 0; s < 2; ++s) {
#pragma unroll
      for (int t = 0; t < 4; ++t) {
        const int r0 = s * 32 + g * 8 + q, r1 = r0 + 4, ch = t * 2 + (pp >> 1);
        const u32x2 lo = tr_read(wi_ + r0 * 128 + ((ch ^ (r0 & 7)) * 16) + (pp & 1) * 8), hi = tr_read(wi_ + r1 * 128 + ((ch ^ (r1 & 7)) * 16) + (pp & 1) * 8);
        bf[s * 4 + t] = u32x4{lo[0], lo[1], hi[0], hi[1]};
      }
    }
  };
  auto gemv_go = [&](u32 hh) {
    sm.hb[tid] = (u16)hh; sm.hb[512 + tid] = (u16)(hh >> 16);
    WAVE_SYNC();
    const u32x4* a = (c & 11) == 0 ? reinterpret_cast<const u32x4*>(&sm.hb[(c >> 2) * 512 + wave * 64 + g * 8]) : &sm.zero[0];
    const u32x4 a0 = a[0], a1 = a[(c & 11) == 0 ? 4 : 0];
    const f32x4 z = {0.f, 0.f, 0.f, 0.f};
    f32x4 d[4];
#pragma unroll
    for (int t = 0; t < 4; ++t) d[t] = mfma16(a0, bf[t], z);
#pragma unroll
    for (int t = 0; t < 4; ++t) d[t] = mfma16(a1, bf[4 + t], d[t]);
    if (lane < 32) {
#pragma unroll
      for (int t = 0; t < 4; ++t) sm.r2[(t * 16 + c) * 20 + g * 8 + wave] = d[t][0];
    }
  };
  auto mod_stage = [&]() {
    wo_prep();
    gemv_go(pack_bf(silu_f(lo_f(xs0)), silu_f(lo_f(xs1))));
    __builtin_amdgcn_s_waitcnt(0xc07f);
    if (lane == 0) *LDSP(volatile u32, (u32)(uintptr_t)&sm.mflag[wave]) = 1u;
    if (wave == 7) {
#pragma nounroll
      for (int spin = 0; spin < SPIN_MAX; ++spin) {
        const u32 f = *LDSP(volatile u32, (u32)(uintptr_t)&sm.mflag[lane & 7]);
        if (all_ok(f != 0u)) break;
        __builtin_amdgcn_s_sleep(1);
      }
      CFENCE();
      st8_sc1(wsr, G_MOD + (blk * 64 + lane) * 8, pack_bf(sum8(&sm.r2[lane * 20]), sum8(&sm.r2[lane * 20 + 8])), tag);
    }
  };
  if (lane < 4) sm.zero[lane] = u32x4{0u, 0u, 0u, 0u};
  if (part == 2 && has_w) {
    if (tid < 8) sm.mflag[tid] = 0u;
    LDS_BARRIER();
    if (__builtin_expect(modc, 0)) {
      small_ops();
      __builtin_amdgcn_raw_buffer_store_b128(u32x4{SENT, SENT, SENT, SENT}, wsr, G_U + wi * 10240 + tid * 16, 0, AUX_SC1);
      __builtin_amdgcn_raw_buffer_store_b128(u32x4{SENT, SENT, SENT, SENT}, wsr, tid < 128 ? G_U + wi * 10240 + tid * 16 : OOB, 8192, AUX_SC1);
      asm volatile("s_waitcnt vmcnt(0)" ::: "memory");
      tag = next_tag(__builtin_amdgcn_readfirstlane(ep[0]));
      mod_stage();
      __builtin_amdgcn_s_barrier();
    }
  }
  SB();
  const int fd = (xcd * 8 + i8) * 8 + (lane & 3) * 2;
  const int ub = wave >> 2, ud = (wave & 3) * 128 + c * 8 + g * 2;
  u32x4 sg[3]; u32 sx2;
  auto side_loads = [&]() {
    const bool fin = part == 1 && wave == 0;
    sg[0] = ld16<AUX_SC1>(wsr, is_up ? G_MOD + (1024 + ud) * 8 : (fin ? G_MOD + (1024 + fd) * 8 : OOB), 0);
    sg[1] = ld16<AUX_SC1>(wsr, is_up ? G_MOD + (1536 + ud) * 8 : (fin ? G_MOD + (2560 + fd) * 8 : OOB), 0);
    sg[2] = ld16<AUX_SC1>(wsr, is_up ? G_MOD + (2048 + ud) * 8 : OOB, 0);
    sx2 = __builtin_amdgcn_raw_buffer_load_b32(mk_rsrc(x, (is_up || fin) ? 2048u : 0u), is_up ? ub * 1024 + ud * 2 : (g & 1) * 1024 + fd * 2, 0, 0);
  };
  auto side_pin = [&]() { PIN(sg[0]); PIN(sg[1]); PIN(sg[2]); PIN(sx2); };
  const u32 mlo = G_ML + (head * 2 + b) * 64 + (lane & 3) * 16;
  float fmrg = 0.f;
  auto merge_factor = [&](const u32x4& ml) {
    const float mi = u2f(ml[0]);
    float M = fmaxf(mi, dpp<XOR1>(mi)); M = fmaxf(M, dpp<XOR2>(M));
    const float e = __expf(mi - M);
    float L = e * u2f(ml[2]);
    L += dpp<XOR1>(L); L += dpp<XOR2>(L);
    fmrg = rdl(e * __builtin_amdgcn_rcpf(L), part);
  };
  f32x2 ga = {0.f, 0.f}, xin = {0.f, 0.f}, cc1 = {0.f, 0.f};
  float Af = 0.f, Bf = 0.f;
  const int hpos = ub * 512 + (ud & ~15) + (g & 1) * 8 + (c & 1) * 4 + (g >> 1) * 2;
  auto up_prep = [&]() {
    if (!all_ok((sg[0][1] == tag) & (sg[0][3] == tag) & (sg[1][1] == tag) & (sg[1][3] == tag) & (sg[2][1] == tag) & (sg[2][3] == tag))) gather16<3>(wsr, G_MOD + (1024 + ud) * 8, 512 * 8, tag, sg);
    ga = f32x2{ub ? hi_f(sg[0][0]) : lo_f(sg[0][0]), ub ? hi_f(sg[0][2]) : lo_f(sg[0][2])};
    cc1 = f32x2{rbf(1.f + (ub ? hi_f(sg[2][0]) : lo_f(sg[2][0]))), rbf(1.f + (ub ? hi_f(sg[2][2]) : lo_f(sg[2][2])))};
    xin = f32x2{lo_f(sx2), hi_f(sx2)};
    u32* hp = reinterpret_cast<u32*>(&sm.hb[hpos]);
    hp[512] = pack_bf(cc1[0], cc1[1]);
    hp[1024] = ub ? perm_hi(sg[1][0], sg[1][2]) : perm_lo(sg[1][0], sg[1][2]);
  };
  u32x8 bw[4];
  auto up_frags = [&]() {
    const int pp = lane & 3;
    const u32 a = wimg + (pp & 1) * 32768 + ((pp >> 1) * 32 + g * 8 + ((lane >> 2) & 3)) * 64 + (((wave >> 1) ^ ((g & 1) | (pp & 2))) * 16) + (wave & 1) * 8;
#pragma unroll
    for (int j = 0; j < 4; ++j) {
      const u32x2 r0 = tr_read(a + j * 4096), r1 = tr_read(a + j * 4096 + 256), r2 = tr_read(a + j * 4096 + 16384), r3 = tr_read(a + j * 4096 + 16640);
      bw[j] = u32x8{r0[0], r0[1], r1[0], r1[1], r2[0], r2[1], r3[0], r3[1]};
    }
  };
  u32 wfa[4];
  auto wo_frags = [&](int s0) {
#pragma unroll
    for (int t = 0; t < 4; ++t) {
#pragma unroll
      for (int s = s0; s < 2; ++s) {
        const u32x2 lo = tr_read(wfa[t] + s * 4096), hi = tr_read(wfa[t ^ 1] + s * 4096 + 256);
        bw[t][s * 4] = lo[0]; bw[t][s * 4 + 1] = lo[1]; bw[t][s * 4 + 2] = hi[0]; bw[t][s * 4 + 3] = hi[1];
      }
    }
  };
  const bool par = lane & 8;
  auto limit = [&](auto sc) {
    constexpr int s = sc.v;
    WAIT_VM(window(s, 1 + (PRM0.W[s >> 3] >> (4 * (s & 7)) & 7)));
    SB();
  };
  auto sample = [&]() { if (timer) lag += (int)(((u32)wall_clock64() - t0) << 8) - due + ((int)__builtin_amdgcn_s_getreg(7 | (3 << 11)) + 1) * tau; };
  auto report = [&](u32 dst) {
    if (timer) {
      if (lane == 0) sm.mflag[wave] = vote * 65536 + min(max(lag >> 8, 0), 4000);
      lag = 0;
      LDS_BARRIER();
      if (wave == 7) {
        int s = sm.mflag[lane & 7];
        s += __builtin_amdgcn_mov_dpp(s, XOR1, 0xf, 0xf, true); s += __builtin_amdgcn_mov_dpp(s, XOR2, 0xf, 0xf, true); s += __builtin_amdgcn_mov_dpp(s, XOR7, 0xf, 0xf, true);
        if (lane < 8) st8_sc1(wsr, dst + lane * 128 + wi * 8, s, tag);
      }
    }
    SB();
  };
  float l0 = 0.f, l1 = 0.f;
  u32x2 gmd[2], tbg;
  int t1 = 0;
#pragma unroll
  for (int i = SK0; i < SK1; ++i) kr[i] = u32x4{0u, 0u, 0u, 0u};
  const int first = SK0 + NL - nl;
  auto left_out = [&](int i) { int f = first; asm volatile("" : "+s"(f)); return i < f; };
  sfor<1, NL>([&](auto ic) {
    constexpr int i = ic.v;
    if constexpr (i >= SK0 && i < SK1) { if (left_out(i)) return true; }
    const u32 so = __builtin_amdgcn_readlane(sot, i);
    gate();
    if constexpr ((i >= TBS && i <= TBP) || (i >= RPS && i <= RPP)) sample();
    SB();
    if constexpr (i >= SK0 && i < SK1) ld16_into(kr[i], rk, lane * 16, so); else kr[i] = ld16<AUX_NT>(rk, lane * 16, so);
    if constexpr (i == SMO) small_ops();
    if constexpr (i == SMO + 1) { if (voter) __builtin_amdgcn_raw_buffer_store_b32(0u, wsr, pbo + (lane >> 5) * 8192 + (lane & 31) * 4, 0, AUX_SC1); }
    if constexpr (i == SK1) tbg = ld8<AUX_SC1>(wsr, wave == 7 ? G_VT + xcd * 128 + c * 8 : OOB, 0);
    if constexpr (i == TBL) tbg = ld8<AUX_SC1>(wsr, wave == 7 ? G_TB + xcd * 128 + c * 8 : OOB, 0);
    if constexpr (i == QP) { gmd[0] = ld8<AUX_SC1>(wsr, part == 1 ? G_MOD + tid * 8 : OOB, 0); gmd[1] = ld8<AUX_SC1>(wsr, part == 1 ? G_MOD + tid * 8 : OOB, 4096); }
    SB();
    SB();
    if (i >= 4) limit(ic);
    if (i == TBP) {
      if (timer) {
        if (lane == 0) sm.mflag[wave] = lag;
        lag = 0;
        LDS_BARRIER();
        if (wave == 7) {
          int s = sm.mflag[lane & 7];
          s += __builtin_amdgcn_mov_dpp(s, XOR1, 0xf, 0xf, true); s += __builtin_amdgcn_mov_dpp(s, XOR2, 0xf, 0xf, true); s += __builtin_amdgcn_mov_dpp(s, XOR7, 0xf, 0xf, true);
          if (lane < 8) st8_sc1(wsr, G_TB + lane * 128 + wi * 8, t0 << 10 | (u32)min(s / (2048 * (TBP - TBS + 1)), 1023), tag);
        }
      }
      SB();
    }
    if (i == PRB) {
      if (voter) {
        u64 ta, tb, tc; u32 dm;
        const u32 pa = pbo + (wave & 1) * 8192;
        asm volatile("s_memtime %0\n\ts_waitcnt lgkmcnt(0)\n\ts_load_dword %3, %4, %5\n\ts_waitcnt lgkmcnt(0)\n\ts_memtime %1\n\ts_waitcnt lgkmcnt(0)\n\t"
                     "s_load_dword %3, %4, %6\n\ts_waitcnt lgkmcnt(0)\n\ts_memtime %2\n\ts_waitcnt lgkmcnt(0)"
                     : "=&s"(ta), "=&s"(tb), "=&s"(tc), "=&s"(dm) : "s"(ws), "s"(pa), "s"(pa ^ 8192u));
        const int d = min(max((int)(2 * (u32)tb - (u32)ta - (u32)tc), -500), 500);
        vote = ((wave ^ die) & 1) ? -d : d;
      }
      SB();
    }
    if (i == TBU) {
      if (wave == 7) {
        const bool ok = tbg[1] == tag;
        float lf = ok ? (float)min(max((int)((t0 << 10 | 1023u) - tbg[0]) >> 10, -300), 300) : 0.f, nf = ok ? 1.f : 0.f, lg = ok ? (float)(tbg[0] & 1023u) : 0.f;
        lf += dpp<XOR1>(lf); nf += dpp<XOR1>(nf); lg += dpp<XOR1>(lg); lf += dpp<XOR2>(lf); nf += dpp<XOR2>(nf); lg += dpp<XOR2>(lg);
        lf += dpp<XOR7>(lf); nf += dpp<XOR7>(nf); lg += dpp<XOR7>(lg); lf += dpp<XOR8>(lf); nf += dpp<XOR8>(nf); lg += dpp<XOR8>(lg);
        const float rn = __builtin_amdgcn_rcpf(fmaxf(nf, 1.f));
        shift = __builtin_amdgcn_readfirstlane(min(max((int)((lg * rn - (float)P.GS) * (float)P.GB * 0.125f), 0), 150));
        if (lane == 0) sm.bc = u32x2{tag, t0 - (u32)(int)(lf * rn) + shift};
      }
      LDS_BARRIER();
      const u32x2 bc = sm.bc;
      tag = __builtin_amdgcn_readfirstlane(bc[0]);
      t1 = (__builtin_amdgcn_readfirstlane((int)(bc[1] - t0)) + TEND + P.X1) * 256;
      tau -= __builtin_amdgcn_readfirstlane((int)((float)(int)(t0 - bc[1]) * 256.f * __builtin_amdgcn_rcpf((float)(nl - TBU - 1))));
      SB();
    }
    if (i == RPP) report(G_VT);
    if (i == LNS) {
      tag = next_tag(__builtin_amdgcn_readfirstlane(ep[0]));
      if (part == 1) {
        float x0[8], x1[8], mb0, ri0, mb1, ri1;
#pragma unroll
        for (int k = 0; k < 4; ++k) { x0[2 * k] = lo_f(xr0[k]); x0[2 * k + 1] = hi_f(xr0[k]); x1[2 * k] = lo_f(xr1[k]); x1[2 * k + 1] = hi_f(xr1[k]); }
        ln_stats2(x0, x1, mb0, ri0, mb1, ri1);
        l0 = rbf(rbf(lo_f(xs0) - mb0) * ri0); l1 = rbf(rbf(lo_f(xs1) - mb1) * ri1);
      }
      PIN(l0); PIN(l1);
      SB();
    }
    if (i == WUSE) {
      PIN(tch1); PIN(tch2);
      if ((part == 1 || (part == 2 && has_w && !modc))) {
        WAIT_VM(WK); CFENCE();
        if (part == 2) mod_stage(); else wo_prep();
      }
#pragma unroll
      for (int k = 0; k < 8; ++k) PIN(bf[k]);
      SB();
    }
    if (i == QP + QD) {
      if (part == 1) {
        u32 gm[2] = {gmd[0][0], gmd[1][0]};
        if (!all_ok((gmd[0][1] == tag) & (gmd[1][1] == tag))) gather8<2>(wsr, G_MOD + tid * 8, 512 * 8, tag, gm);
        gemv_go(pack_bf(rbf(l0 * rbf(1.f + lo_f(gm[1]))) + lo_f(gm[0]), rbf(l1 * rbf(1.f + hi_f(gm[1]))) + hi_f(gm[0])));
      }
      SB();
    }
    if (i == QP + QD + 1) {
      if (part == 1) {
        LDS_BARRIER();
        if (wave == 0) {
          const int bb = lane >> 5, jq = lane & 31;
          st8_sc1(wsr, G_Q + (bb * 2048 + blk * 32 + jq) * 8, pack_bf(sum8(&sm.r2[2 * jq * 20 + bb * 8]), sum8(&sm.r2[(2 * jq + 1) * 20 + bb * 8])), tag);
        }
      }
      SB();
    }
    return true;
  });
  PIN(sot);
  if (wave == 7) {
    const bool ok = tbg[1] == tag;
    float lg = ok ? (float)(tbg[0] & 0xffffu) : 0.f, nf = ok ? 8.f * (RPP - RPS + 1) : 0.f;
    lg += dpp<XOR1>(lg); nf += dpp<XOR1>(nf); lg += dpp<XOR2>(lg); nf += dpp<XOR2>(nf); lg += dpp<XOR7>(lg); nf += dpp<XOR7>(nf); lg += dpp<XOR8>(lg); nf += dpp<XOR8>(nf);
    lg *= __builtin_amdgcn_rcpf(fmaxf(nf, 1.f));
    if (lane == 0) sm.bc2 = u32x2{(u32)min(shift * P.DG * 32, P.EW * 256), (u32)min(max((int)((lg - (float)P.B0) * (float)(P.BG * 32) + fmaxf(lg - (float)P.TC, 0.f) * (float)(P.RT * 32)) - shift * P.D0 * 32, 0), 200 * 256)};
  }
  LDS_BARRIER();
  {
    const u32x2 bc = sm.bc2;
    const int st = __builtin_amdgcn_readfirstlane(bc[1]), sp = (st * P.BF) >> 3;
    t1 += st + __builtin_amdgcn_readfirstlane(bc[0]);
    due += P.TG * 256 + sp;
    tau = (te1 >> 16) + __builtin_amdgcn_readfirstlane((int)((float)(st - sp) * __builtin_amdgcn_rcpf((float)(nl - 3))));
  }
  const u32 qoff = G_Q + ((b * 32 + head) * 64 + c * 4) * 8;
  u32x4 qg[2], mlg;
  u32 qq[4];
  constexpr int NB = (NL + 15) / 16;
  float xlo, sc[NB], pr[NB], m = 0.f;
  f32x4 acc = {0.f, 0.f, 0.f, 0.f};
  u32 pb[NB], pm[4], pl8[8], cpf;
  const u32 adst0 = lds_addr(lane < 32 ? &sm.red[wave][c * 8 + g * 4] : &sm.r2[(lane - 32) * 4]);
  u32 po, rdst, rsrc, xo;
  auto wo_addr = [&]() {
    po = dup(G_P + xcd * 8192) + tid * 16 + near;
    rdst = (u32)(uintptr_t)&sm.red2[wave][(lane & 31) * 4 + (lane >> 5) * 2];
    rsrc = (u32)(uintptr_t)&sm.red2[0][((wave & 1) * 16 + c) * 4 + (g & 1) * 2 + (g >> 1)];
    xo = dup(G_XP + xcd * 2048 + (wave & 1) * 1024 + (blk * 64 + (lane & ~1)) * 2) + (lane & 1) * 8192;
    PIN(po); PIN(rdst); PIN(rsrc); PIN(xo);
    const int q = (lane >> 2) & 3, pp = lane & 3, r = (g & 1) * 4 + (q & 1);
#pragma unroll
    for (int t = 0; t < 4; ++t) { wfa[t] = wimg + wave * 8192 + ((g >> 1) * 16 + (q >> 1) * 8 + r) * 128 + (pp & 1) * 8 + ((t * 2 + (pp >> 1)) ^ r ^ (q & 2)) * 16; PIN(wfa[t]); }
  };
  u32 pso = (wave == 0 && (g & 1) == 0) ? pslot + c * 128 + (g >> 1) * 8192 : OOB;
  PIN(pso);
  const u32 nadd = wdef ? P.LW : (is_up ? P.LU : 0);
  u32 nto = tid < 2 ? dup(G_NT) + bid * 8 + tid * 8192 : OOB;
  PIN(nto);
  auto notices = [&]() -> int {
    u32 off = dup(G_NT) + near + lane * 16;
    PIN(off);
    until(due);
    int mx;
#pragma nounroll
    for (int spin = 0; spin < SPIN_MAX; ++spin) {
      CFENCE();
      const u32x4 ga_ = ld16<AUX_SC1>(wsr, off, 0), gb_ = ld16<AUX_SC1>(wsr, off, 1024);
      mx = max(max((int)(ga_[0] - t0), (int)(ga_[2] - t0)), max((int)(gb_[0] - t0), (int)(gb_[2] - t0)));
      if (all_ok((((ga_[1] ^ tag) | (ga_[3] ^ tag)) | ((gb_[1] ^ tag) | (gb_[3] ^ tag))) == 0u)) break;
      for (int k = 0; k < P.AG; ++k) __builtin_amdgcn_s_sleep(1);
    }
    if (all_ok(mx <= (t1 >> 8) - P.AC)) return t1;
    mx = max(mx, __builtin_amdgcn_mov_dpp(mx, XOR1, 0xf, 0xf, true)); mx = max(mx, __builtin_amdgcn_mov_dpp(mx, XOR2, 0xf, 0xf, true));
    mx = max(mx, __builtin_amdgcn_mov_dpp(mx, XOR7, 0xf, 0xf, true)); mx = max(mx, __builtin_amdgcn_mov_dpp(mx, XOR8, 0xf, 0xf, true));
    mx = max(max(__builtin_amdgcn_readlane(mx, 0), __builtin_amdgcn_readlane(mx, 16)), max(__builtin_amdgcn_readlane(mx, 32), __builtin_amdgcn_readlane(mx, 48)));
    return max(min(mx, 1 << 20) * 256, t1 + P.L0 * 256) + P.L1 * 256;
  };
  auto pv_row = [&](auto jc) {
    constexpr int i = jc.v;
    const u32 pv = __float_as_uint(bcast16(u2f(pb[i >> 4]), i & 15));
    acc = mfma16(u32x4{pv & pm[0], pv & pm[1], pv & pm[2], pv & pm[3]}, vr[i], acc);
    PIN(acc);
    return true;
  };
  sfor<0, NL - 1>([&](auto ic) {
    constexpr int v = ic.v;
    if constexpr (v >= SK0 && v < SK1) { if (left_out(v)) return true; }
    if constexpr (v > 0) {
      const u32 so = __builtin_amdgcn_readlane(sot, v);
      if constexpr (v >= SK1) { if (v == NL - P.NE) tau = (tau * P.FE) >> 4; }
      gate();
      if constexpr (v >= RVS && v <= RVP) sample();
      SB();
      if constexpr (v >= SK0 && v < SK1) ld16_into(vr[v], rv, lane * 16, so); else vr[v] = ld16<AUX_NT>(rv, lane * 16, so);
      SB();
      SB();
    }
    if constexpr (v == SK0 - 1) {
#pragma unroll
      for (int i = SK0; i < SK1; ++i) { vr[i] = u32x4{0u, 0u, 0u, 0u}; PIN(vr[i]); }
    }
    if constexpr (v == VQ - 4) { qg[0] = ld16<AUX_SC1>(wsr, qoff, 0); qg[1] = ld16<AUX_SC1>(wsr, qoff + 16, 0); }
    if constexpr (v == VSIDE) side_loads();
    if constexpr (v == CPF) {
      u64 pc; u32 dlt, len;
      asm volatile("s_getpc_b64 %0\n.Lcpf_here:\n\ts_mov_b32 %1, .Ltail_start-.Lcpf_here\n\ts_mov_b32 %2, .Lend_a-.Ltail_start\n\ts_max_i32 %2, %2, .Lend_b-.Ltail_start\n\ts_max_i32 %2, %2, .Lend_c-.Ltail_start\n\ts_max_i32 %2, %2, .Lend_d-.Ltail_start\n\ts_max_i32 %2, %2, .Lend_e-.Ltail_start" : "=s"(pc), "=s"(dlt), "=s"(len) :: "scc");
      pc += dlt;
      const rsrc_t rc = mk_rsrc((const void*)pc, (part == 3 && i8 == 1) ? len : 0u);
      cpf = __builtin_amdgcn_raw_buffer_load_b32(rc, tid * 64, 0, 0);
    }
    if constexpr (v == SK1 + 2) PIN(cpf);
    if constexpr (v == RVP) report(G_VT + 1024);
    if constexpr (v == RVL) tbg = ld8<AUX_SC1>(wsr, wave == 7 ? G_VT + (g ? 0 : 1024) + xcd * 128 + c * 8 : OOB, 0);
    if constexpr (v == SK1) mlg = ld16<AUX_SC1>(wsr, wave == 0 ? mlo : OOB, 0);
    SB();
    if constexpr (NL + v < 72) limit(IC<NL + v>{});
    if constexpr (v >= SK0 + WV) { WAIT_VM(v < SK1 ? WV : WV + 1); SB(); }
    if constexpr (v == VQ) {
      if (!all_ok((qg[0][1] == tag) & (qg[0][3] == tag) & (qg[1][1] == tag) & (qg[1][3] == tag))) gather16<2>(wsr, qoff, 16, tag, qg);
      qq[0] = qg[0][0]; qq[1] = qg[0][2]; qq[2] = qg[1][0]; qq[3] = qg[1][2];
    }
    if constexpr (v >= VQ && v < VS) {
      constexpr int sl = v - VQ, bt = sl >> 1;
      float xs[8];
#pragma unroll
      for (int i = 0; i < 8; ++i) {
        float d = 0.f;
        if (sl * 8 + i < NL) {
#pragma unroll
          for (int k = 0; k < 4; ++k) d = dot2(kr[sl * 8 + i][k], qq[k], d);
        }
        xs[i] = d;
      }
#pragma unroll
      for (int h = 0; h < 4; ++h) xs[h] = sel_add<XOR7>(xs[h], xs[h + 4], c & 4);
#pragma unroll
      for (int h = 0; h < 2; ++h) xs[h] = sel_add<XOR2>(xs[h], xs[h + 2], c & 2);
      xs[0] = sel_add<XOR1>(xs[0], xs[1], c & 1);
      if constexpr ((sl & 1) || sl == NSL - 1) {
        const float t = (sl & 1) ? sel_add<XOR8>(xlo, xs[0], c & 8) : xs[0] + dpp<XOR8>(xs[0]);
        const float s = rbf(rbf(t) * 0.08838834764831845f);
        sc[bt] = (bt * 16 + c < SK0 || (bt * 16 + c >= first && bt * 16 + c < NL)) ? s : -__builtin_inff();
        PIN(sc[bt]);
      } else {
        xlo = xs[0];
        PIN(xlo);
      }
    }
    if constexpr (v == VS) {
      float mloc = sc[0];
#pragma unroll
      for (int bt = 1; bt < NB; ++bt) mloc = fmaxf(mloc, sc[bt]);
      m = fmaxf(wave_max(mloc), -3.0e38f);
      float ls = 0.f;
#pragma unroll
      for (int bt = 0; bt < NB; ++bt) { pr[bt] = __expf(sc[bt] - m); ls += pr[bt]; }
      const float l = wave_sum(ls);
      if (lane == 0) { sm.ml[wave][0] = m; sm.ml[wave][1] = l; }
      if (c == ((NL - 1) & 15)) sm.pl[g][wave] = pr[NB - 1];
    }
    if constexpr (v >= SK1) pv_row(IC<v - SK1 + SK0 - WV>{});
    if constexpr (v < SK0 || v >= SK0 + WV) sfor<cons_end(v - 1), cons_end(v)>(pv_row);
    if constexpr (v == RVU) {
      if (wave == 7) {
        const bool ok = tbg[1] == tag;
        float lg = ok ? (float)(g ? (int)tbg[0] >> 16 : (int)(tbg[0] & 0xffffu)) : 0.f, nf = ok ? 8.f * (RVP - RVS + 1) : 0.f;
        lg += dpp<XOR1>(lg); nf += dpp<XOR1>(nf); lg += dpp<XOR2>(lg); nf += dpp<XOR2>(nf); lg += dpp<XOR7>(lg); nf += dpp<XOR7>(nf); lg += dpp<XOR8>(lg); nf += dpp<XOR8>(nf);
        lg *= __builtin_amdgcn_rcpf(fmaxf(nf, 1.f));
        if (lane == 16) sm.bc4 = __float_as_uint(lg);
        if (lane == 0) sm.bc3 = (u32)min(max((int)((lg - (float)P.F0) * (float)(P.FG * 32)), 0), 200 * 256);
      }
      LDS_BARRIER();
      const int st = __builtin_amdgcn_readfirstlane(sm.bc3), sp = (st * P.FF_) >> 3;
      near = ((__builtin_amdgcn_readfirstlane(sm.bc4) >> 31 ^ die ^ P.NR ^ 1) & 1) * 8192u;
      t1 += st;
      due += sp;
      tau += __builtin_amdgcn_readfirstlane((int)((float)(st - sp) * __builtin_amdgcn_rcpf((float)(nl - RVU - 3))));
    }
    if constexpr (v == SK1 + 1) {
      float p[8];
#pragma unroll
      for (int w = 0; w < 8; ++w) p[w] = sm.pl[g][w] * __expf(sm.ml[w][0] - m);
#pragma unroll
      for (int w = 0; w < 8; ++w) pl8[w] = pack_bf(p[w], p[w]);
#pragma unroll
      for (int w = 0; w < 8; ++w) PIN(pl8[w]);
    }
    if constexpr (v == MLS - 1) { side_pin(); if (is_up) up_prep(); PIN(ga); PIN(xin); PIN(cc1); }
    if constexpr (v == UPREP - 1) wo_addr();
    if constexpr (v == UPREP) {
#pragma unroll
      for (int k = 0; k < 4; ++k) asm volatile("" : "=v"(bw[k]));
      if (part == 0) wo_frags(0); else if (is_up) up_frags();
#pragma unroll
      for (int k = 0; k < 4; ++k) PIN(bw[k]);
    }
    if constexpr (v == UPREP + 1) {
      if (is_up) {
        const u32x4* a1 = reinterpret_cast<const u32x4*>(&sm.hb[1024 + (c >> 2 & 1) * 1024 + (c >> 3) * 512 + (g >> 1) * 256 + (c >> 1 & 1) * 32 + (g & 1) * 16 + (c & 1) * 8]);
        f32x4 d = {0.f, 0.f, 0.f, 0.f};
#pragma unroll
        for (int j = 0; j < 4; ++j) d = mfma64(a1[j * 8], bw[j], d, (c & 1) ? 0xeeeeu : 0x4444u);
        float t = par ? d[2] + d[3] : d[0] + d[1];
        t += dpp<XOR8>(t);
        const auto ab = __builtin_amdgcn_permlane16_swap(__float_as_uint(t), __float_as_uint(t), false, false);
        Af = u2f(ab[0]); Bf = u2f(ab[1]);
      }
      PIN(Af); PIN(Bf);
    }
    if constexpr (v == MLS) {
      LDS_BARRIER();
      float mm[8], ll[8];
#pragma unroll
      for (int w = 0; w < 8; ++w) { const f32x2 t = *reinterpret_cast<const f32x2*>(&sm.ml[w][0]); mm[w] = t[0]; ll[w] = t[1]; }
      float Mwg = mm[0], Lwg = 0.f;
#pragma unroll
      for (int w = 1; w < 8; ++w) Mwg = fmaxf(Mwg, mm[w]);
      if (wave == 0) {
#pragma unroll
        for (int w = 0; w < 8; ++w) Lwg = fmaf(__expf(mm[w] - Mwg), ll[w], Lwg);
        if (lane == 0) st16_sc1(wsr, G_ML + ((head * 2 + b) * 4 + part) * 16, __float_as_uint(Mwg), __float_as_uint(Lwg), tag);
      }
      const float fw = __expf(m - Mwg);
      m = Mwg;
#pragma unroll
      for (int bt = 0; bt < NB; ++bt) { pb[bt] = pack_bf(pr[bt] * fw, pr[bt] * fw); PIN(pb[bt]); }
#pragma unroll
      for (int k = 0; k < 4; ++k) { pm[k] = (c >> 1 & 3) == k ? ((c & 1) ? 0xffff0000u : 0x0000ffffu) : 0u; PIN(pm[k]); }
    }
    SB();
    return true;
  });
  asm volatile(".Ltail_start:");
  if (wave != 0) {
    pv_row(IC<SK0 - 1>{});
    WAIT_VM(WV); SB();
    sfor<NL - 1 - WV, NL - 1>(pv_row);
    *LDSP(f32x4, adst0) = acc;
    LDS_BARRIER();
  } else {
    u32x4 xr[8], am[8];
#pragma unroll
    for (int w = 0; w < 8; ++w) am[w] = u32x4{pl8[w] & pm[0], pl8[w] & pm[1], pl8[w] & pm[2], pl8[w] & pm[3]};
#pragma unroll
    for (int w = 0; w < 8; ++w) PIN(am[w]);
    {
      const u32 so = __builtin_amdgcn_readlane(sot, NL - 1);
      gate();
      SB();
#pragma unroll
      for (int w = 0; w < 8; ++w) xr[w] = ld16<AUX_NT>(rv, lane * 16, so ^ ((w & 1) ? col(P, 7) : 0u) ^ ((w & 2) ? col(P, 8) : 0u) ^ ((w & 4) ? col(P, 9) : 0u));
      SB();
    }
    u32 rsum = lds_addr(&sm.red[0][c * 8 + (g & 1) * 4]);
    pv_row(IC<SK0 - 1>{});
    WAIT_VM(WV + 8); SB();
    sfor<NL - 1 - WV, NL - 3>(pv_row);
    if (__builtin_expect(!all_ok((mlg[1] == tag) & (mlg[3] == tag)), 0)) {
#pragma nounroll
      for (int spin = 0; spin < SPIN_MAX; ++spin) {
        CFENCE();
        mlg = ld16<AUX_SC1>(wsr, mlo, 0);
        if (all_ok((mlg[1] == tag) & (mlg[3] == tag))) break;
      }
    }
    merge_factor(mlg);
    sfor<NL - 3, NL - 1>(pv_row);
    LDS_BARRIER();
    f32x4 t[8];
#pragma unroll
    for (int w = 1; w < 8; ++w) t[w] = LDSP(f32x4, rsum)[w * 32];
    acc = ((acc + t[1]) + (t[2] + t[3])) + ((t[4] + t[5]) + (t[6] + t[7]));
    PIN(acc);
    SB();
#pragma unroll
    for (int w = 0; w < 8; ++w) acc = mfma16(am[w], xr[w], acc);
  }
  WAIT_VM(0);
  acc *= fmrg;
  {
    const u32 w0 = min(pack_bf(acc[0], acc[1]), SENT - 1u), w1 = min(pack_bf(acc[2], acc[3]), SENT - 1u);
    const auto e0 = __builtin_amdgcn_permlane16_swap(w0, w0, false, false), e1 = __builtin_amdgcn_permlane16_swap(w1, w1, false, false);
    __builtin_amdgcn_raw_buffer_store_b128(u32x4{e0[0], e1[0], e0[1], e1[1]}, wsr, pso, 0, AUX_SC1);
  }
  st8_sc1(wsr, nto, (u32)wall_clock64() + nadd, tag);
  SB();

  if (part == 2) return;
  if (part == 0) {
    __builtin_amdgcn_s_barrier();
#pragma nounroll
    while (wleft > 0) { until(t1 - (P.PA + wleft * P.PD) * 256); piece(); }
    bool late = false;
    u32x4 og[1];
    f32x4 d[4];
    u32 six = (c & 8) ? 0xeeeeu : 0x4444u;
    PIN(six);
#pragma unroll
    for (int t = 0; t < 4; ++t) { d[t] = f32x4{0.f, 0.f, 0.f, 0.f}; PIN(d[t]); }
    if (wdef) {
      late = true;
      while ((int)(((u32)wall_clock64() - t0) << 8) < t1 - 10 * 256) if (__builtin_amdgcn_s_getreg(7 | (3 << 11)) == 0) { late = false; break; }
      if (__builtin_expect(late, 0)) { until(t1); CFENCE(); og[0] = ld16<AUX_SC1>(wsr, po, 0); SB(); WAIT_VM(1); } else { WAIT_VM(0); }
      CFENCE();
      wo_frags(1);
#pragma unroll
      for (int k = 0; k < 4; ++k) PIN(bw[k]);
      SB();
    }
    if (__builtin_expect(!late, 1)) { until(t1); CFENCE(); og[0] = ld16<AUX_SC1>(wsr, po, 0); }
    SB();
    auto wo_mm = [&]() {
#pragma unroll
      for (int t = 0; t < 4; ++t) d[t] = mfma64(og[0], bw[t], d[t], six);
    };
    auto p_bad = [&]() { return !all_ok(max(max(og[0][0], og[0][1]), max(og[0][2], og[0][3])) != SENT); };
    wo_mm();
    if (__builtin_expect(p_bad(), 0)) {
#pragma nounroll
      for (int spin = 0; spin < SPIN_MAX; ++spin) {
        __builtin_amdgcn_s_sleep(1);
        CFENCE();
        og[0] = ld16<AUX_SC1>(wsr, po, 0);
        if (!p_bad()) break;
      }
#pragma unroll
      for (int t = 0; t < 4; ++t) d[t] = f32x4{0.f, 0.f, 0.f, 0.f};
      wo_mm();
      SB();
      asm volatile(".Lend_d:");
    }
    {
      SB();
      f32x4 y; f32x2 h[4];
#pragma unroll
      for (int t = 0; t < 4; ++t) { h[t] = d[t].xy + d[t].zw; PIN(h[t]); }
#pragma unroll
      for (int t = 0; t < 4; ++t) { y[t] = h[t][0] + h[t][1]; PIN(y[t]); }
      *LDSP(f32x2, rdst) = f32x2{swap32_add(y[0], y[1]), swap32_add(y[2], y[3])};
    }
    LDS_BARRIER();
    if (wave < 2) {
      f32x2 v[4];
#pragma unroll
      for (int w = 0; w < 4; ++w) v[w] = f32x2{LDSP(float, rsrc)[w * 256], LDSP(float, rsrc)[w * 256 + 128]};
      const f32x2 h = (v[0] + v[1]) + (v[2] + v[3]);
      const float y = h[0] + h[1];
      __builtin_amdgcn_raw_buffer_store_b32(min(pack_bf(dpp<0xA0>(y), dpp<0xF5>(y)), SENT - 1u), wsr, xo, 0, AUX_SC1);
      asm volatile(".Lend_a:");
    }
    return;
  }
  if (part == 3) {
    if (!has_w) return;
    u32x4 b2[4];
    {
      __builtin_amdgcn_s_barrier();
      const rsrc_t r2 = mk_rsrc(w2 + (size_t)(blk * 32 + wave * 4) * 512, 4096u);
      const u32 l2 = (u32)(uintptr_t)(&sm.w2b[wave * 4][0]);
#pragma unroll
      for (int n = 0; n < 4; ++n) { until(t1 - (P.QA + (4 - n) * P.QD) * 256); dma16(r2, (lane ^ (n * 2) ^ ((wave & 2) << 2)) * 16, n * 1024, l2 + n * 1024); }
    }
    const u32 tdst = lds_addr(&sm.hb[hpos]);
    const u32 sdst = lds_addr(&sm.stat[wave][lane]);
    const u32 ssrc = lds_addr(&sm.stat[(g >> 1) * 4 + ((lane >> 3) & 3)][(lane & 7) * 4]);
    const u32 adst = lds_addr((c < 4 && (g & 1) == 0) ? &sm.actl[g >> 1][wave * 4 + c] : reinterpret_cast<u16*>(&sm.dummy[lane]));
    const u32 asrc = lds_addr(&sm.actl[lane & 1][g * 8]);
    const u32 apa = lds_addr(&sm.hb[(c >> 3) * 512 + (g >> 1) * 256 + (c >> 1 & 1) * 64 + ((c >> 2) & 1) * 32 + (g & 1) * 16 + (c & 1) * 8]);
    u32 six = (c & 1) ? 0xeeeeu : 0x4444u;
    f32x4 d[2] = {{0.f, 0.f, 0.f, 0.f}, {0.f, 0.f, 0.f, 0.f}};
    PIN(six); PIN(d[0]); PIN(d[1]);
    constexpr u32 xs2 = 16384;
    u32 xoff = dup(G_XP) + near + g * 2048 + ub * 1024 + (wave & 3) * 256 + c * 16;
    u32x4 one;
#pragma unroll
    for (int k = 0; k < 4; ++k) one[k] = ((c & 2) == 0 && (c >> 2) == k) ? ((c & 1) ? 0x3f800000u : 0x00003f80u) : 0u;
    PIN(one);
    u32 ooff = dup(G_OP + (blk * 512 + tid) * 8);
    float Afp = Af, Bfp = Bf;
    PIN(xoff); PIN(ooff); PIN(Afp); PIN(Bfp);
    f32x2 gc = ga * cc1, xc = xin * cc1;
    PIN(ga); PIN(xin); PIN(gc); PIN(xc);
    u32 w2a = (u32)(uintptr_t)(&sm.w2b[0][0]) + (g * 8 + ((lane >> 2) & 3)) * 1024 + (wave ^ (g & 1)) * 128 + (lane & 15) * 8;
    PIN(w2a);
    if (wave == 1) {
      due = t1 + P.N1 * 256;
      const int tn = notices();
      due = tn + P.X2 * 256;
      until(due);
    }
    asm volatile("s_waitcnt vmcnt(0)" ::: "memory");
    __builtin_amdgcn_s_barrier();
    CFENCE();
    u32x4 xg[2] = {ld16<AUX_SC1>(wsr, xoff, 0), ld16<AUX_SC1>(wsr, xoff, xs2)};
    SB();
#pragma unroll
    for (int t = 0; t < 4; ++t) {
      const u32x2 lo = tr_read(w2a ^ (t * 32)), hi = tr_read((w2a ^ (t * 32)) + 4096);
      b2[t] = u32x4{lo[0], lo[1], hi[0], hi[1]};
    }
#pragma unroll
    for (int t = 0; t < 4; ++t) PIN(b2[t]);
    SB();
    const f32x4 z4 = {0.f, 0.f, 0.f, 0.f};
    auto xp_bad = [&]() { return !all_ok(max(max(max(xg[0][0], xg[0][1]), max(xg[0][2], xg[0][3])), max(max(xg[1][0], xg[1][1]), max(xg[1][2], xg[1][3]))) != SENT); };
    f32x4 v = mfma16(one, xg[1], mfma16(one, xg[0], z4));
    if (__builtin_expect(xp_bad(), 0)) {
#pragma nounroll
      for (int spin = 0; spin < SPIN_MAX; ++spin) {
        __builtin_amdgcn_s_sleep(1);
        CFENCE();
        xg[0] = ld16<AUX_SC1>(wsr, xoff, 0); xg[1] = ld16<AUX_SC1>(wsr, xoff, xs2);
        if (!xp_bad()) break;
      }
      v = mfma16(one, xg[1], mfma16(one, xg[0], z4));
      SB();
      asm volatile(".Lend_e:");
    }
    {
      const f32x2 a = v.xy, hc = __builtin_elementwise_fma(gc, a, xc), x1 = __builtin_elementwise_fma(ga, a, xin);
      *LDSP(u32, tdst) = pack_bf(hc[0], hc[1]);
      *LDSP(float, sdst) = swap32_add(x1[0] + x1[1], fmaf(x1[0], x1[0], x1[1] * x1[1]));
    }
    LDS_BARRIER();
    {
      const f32x4 s4 = LDSP(f32x4, ssrc)[0], q4 = LDSP(f32x4, ssrc)[8];
      SB();
      const u32x4 af0 = LDSP(u32x4, apa)[0], af1 = LDSP(u32x4, apa)[16];
      SB();
      const f32x2 s2 = s4.xy + s4.zw, q2 = q4.xy + q4.zw;
      float s = s2[0] + s2[1], q = q2[0] + q2[1];
      SB();
      d[0] = mfma64(af0, bw[0], d[0], six);
      SB();
      s = swap16_add(s, q);
      SB();
      d[1] = mfma64(af0, bw[1], d[1], six);
      SB();
      s += dpp<XOR1>(s); s += dpp<XOR2>(s);
      SB();
      d[0] = mfma64(af1, bw[2], d[0], six);
      SB();
      s += dpp<XOR7>(s); s += dpp<XOR8>(s);
      SB();
      d[1] = mfma64(af1, bw[3], d[1], six);
      SB();
      const auto sq = __builtin_amdgcn_permlane16_swap(__float_as_uint(s), __float_as_uint(s), false, false);
      const float mean = u2f(sq[0]) * (1.f / 512.f);
      const float var = fmaf(u2f(sq[1]), 1.f / 512.f, -mean * mean);
      float ri = __builtin_amdgcn_rsqf(fmaxf(var, 0.f) + 1e-5f);
      PIN(ri); PIN(d[0]); PIN(d[1]);
      SB();
      float y0 = d[0][0] + d[0][1], y1 = d[1][2] + d[1][3], mr = -ri * mean;
      PIN(y0); PIN(y1); PIN(mr);
      float y = y0 + y1, cf = fmaf(mr, Afp, Bfp), rk = ri * -1.44269504f;
      PIN(y); PIN(cf); PIN(rk);
      y = __builtin_bit_cast(float, __builtin_amdgcn_update_dpp(__builtin_bit_cast(int, y), __builtin_bit_cast(int, y), XOR8, 0xa, 0xf, false));
      const float ck = cf * -1.44269504f;
      y = swap16_add(y, y);
      const float ex = __builtin_amdgcn_exp2f(fmaf(y, rk, ck));
      y = fmaf(y, ri, cf);
      const float gu = y * dpp<0x104>(y);
      *LDSP(u16, adst) = (u16)pack_bf(gu * __builtin_amdgcn_rcpf(1.f + ex), 0.f);
    }
    LDS_BARRIER();
    {
      const u32x4 af = *LDSP(u32x4, asrc);
      const f32x4 z = {0.f, 0.f, 0.f, 0.f};
      f32x4 d0 = mfma16(af, b2[0], z), d1 = mfma16(af, b2[1], z), d2 = mfma16(af, b2[2], z), d3 = mfma16(af, b2[3], z);
      asm volatile("" : "+v"(d0), "+v"(d1), "+v"(d2), "+v"(d3));
      const bool g1 = g & 1, g2 = g & 2;
      const float e0 = g1 ? d1[0] : d0[0], e1 = g1 ? d1[1] : d0[1], f0 = g1 ? d3[0] : d2[0], f1 = g1 ? d3[1] : d2[1];
      const u32x2 ov = {pack_bf(g2 ? f0 : e0, g2 ? f1 : e1), tag};
      __builtin_amdgcn_raw_buffer_store_b64(ov, wsr, ooff, near ^ 8192u, AUX_SC1);
      __builtin_amdgcn_raw_buffer_store_b64(ov, wsr, ooff, near, AUX_SC1);
    }
    asm volatile(".Lend_b:");
    return;
  }
  if (wave == 0) {
    if (!all_ok((sg[0][1] == tag) & (sg[0][3] == tag) & (sg[1][1] == tag) & (sg[1][3] == tag))) {
      u32x4 t[2];
      gather16<2>(wsr, G_MOD + (1024 + fd) * 8, 1536 * 8, tag, t);
      sg[0] = t[0]; sg[1] = t[1];
    }
    u32 xa;
    {
      due = t1 + P.N2 * 256;
      const int tn = notices();
      due = tn + P.X3 * 256;
      until(due);
    }
    const bool hb = g & 1;
#pragma nounroll
    for (int spin = 0; spin < SPIN_MAX; ++spin) {
      CFENCE();
      xa = __builtin_amdgcn_raw_buffer_load_b32(wsr, dup(G_XP) + near + (g >> 1) * 16384 + (c >> 2) * 2048 + (g & 1) * 1024 + fd * 2, 0, AUX_SC1);
      if (all_ok(xa != SENT)) break;
      __builtin_amdgcn_s_sleep(1);
    }
    float a0 = lo_f(xa), a1 = hi_f(xa);
    a0 += dpp<0x124>(a0); a1 += dpp<0x124>(a1);
    a0 += dpp<XOR8>(a0); a1 += dpp<XOR8>(a1);
    a0 = swap32_add(a0, a0); a1 = swap32_add(a1, a1);
    f32x2 xv = {rbf(lo_f(sx2) + rbf((hb ? hi_f(sg[0][0]) : lo_f(sg[0][0])) * rbf(a0))), rbf(hi_f(sx2) + rbf((hb ? hi_f(sg[0][2]) : lo_f(sg[0][2])) * rbf(a1)))};
    f32x2 gv = {hb ? hi_f(sg[1][0]) : lo_f(sg[1][0]), hb ? hi_f(sg[1][2]) : lo_f(sg[1][2])};
    PIN(xv); PIN(gv);
    if (wi == 0 && lane == 0) __builtin_amdgcn_raw_buffer_store_b32(tag, wsr, WS_EPOCH, 0, AUX_SC1);
    due = max(due + P.X4 * 256, (int)(((u32)wall_clock64() - t0) << 8) + P.GX * 256);
    const bool last = lane < (NUP - 32) * 4;
    u32 o0 = dup(G_OP + (lane >> 2) * 4096 + fd * 8) + near, o2 = last ? o0 : OOB, tg2 = last ? tag : 0u;
    const rsrc_t rout = mk_rsrc(out, 2048u);
    u32 oo = (lane & 44) == 0 ? (g * 512 + fd) * 2 : OOB;
    const u32 pk = (c & 10) ? 0u : ((c & 4) ? 0x3f800000u : 0x00003f80u);
    u32x4 one = {(c & 1) ? 0u : pk, 0u, (c & 1) ? pk : 0u, 0u};
    PIN(o0); PIN(o2); PIN(tg2); PIN(oo); PIN(one);
    __builtin_amdgcn_raw_buffer_store_b32(pack_bf(xv[0], xv[1]), rout, oo, 0, AUX_SC1);
    u32 s1_ = 131072, s2_ = 262144;
    asm volatile("" : "+s"(s1_), "+s"(s2_));
    until(due);
    f32x4 sv;
#pragma nounroll
    for (int spin = 0; spin < SPIN_MAX; ++spin) {
      CFENCE();
      const u32x4 og[3] = {ld16<AUX_SC1>(wsr, o0, 0), ld16<AUX_SC1>(wsr, o0, s1_), ld16<AUX_SC1>(wsr, o2, s2_)};
      sv = f32x4{0.f, 0.f, 0.f, 0.f};
#pragma unroll
      for (int k = 0; k < 3; ++k) sv = mfma16(one, og[k], sv);
      SB();
      u32 bad = ((og[0][1] ^ tag) | (og[0][3] ^ tag) | (og[1][1] ^ tag)) | ((og[1][3] ^ tag) | (og[2][1] ^ tg2) | (og[2][3] ^ tg2));
      PIN(bad);
      SB();
      float s0 = sv[0] + dpp<0x124>(sv[0]), s1 = sv[1] + dpp<0x124>(sv[1]);
      PIN(s0); PIN(s1);
      s0 += dpp<XOR8>(s0); s1 += dpp<XOR8>(s1);
      PIN(s0); PIN(s1);
      __builtin_amdgcn_raw_buffer_store_b32(pack_bf(fmaf(gv[0], s0, xv[0]), fmaf(gv[1], s1, xv[1])), rout, oo, 0, AUX_SC1);
      SB();
      if (all_ok(bad == 0u)) break;
      __builtin_amdgcn_s_sleep(1);
    }
    asm volatile(".Lend_c:");
  }
}

extern "C" void kernel_launch(void* const* d_in, const int* in_sizes, int n_in,
                              void* d_out, int out_size, void* d_ws, size_t ws_size,
                              hipStream_t stream) {
  fused_kernel<<<256, 512, 0, stream>>>((const u16*)d_in[1], (const u16*)d_in[2], (const u16*)d_in[4], (const u16*)d_in[3], (const u16*)d_in[10], (const u16*)d_in[6], (const u16*)d_in[7],
                                        (unsigned char*)d_ws, (const u16*)d_in[0], (const u16*)d_in[9], (const u16*)d_in[8], (u16*)d_out);
}
```

```cpp
#include <hip/hip_runtime.h>
#include <stdint.h>


typedef unsigned int u32;
typedef unsigned short u16;
typedef unsigned long long u64;
typedef __attribute__((ext_vector_type(4))) unsigned u32x4;
typedef __attribute__((ext_vector_type(2))) unsigned u32x2;
typedef __attribute__((ext_vector_type(2))) __bf16 bf16x2;
typedef __attribute__((ext_vector_type(4))) float f32x4;
typedef __attribute__((ext_vector_type(2))) float f32x2;
typedef __amdgpu_buffer_rsrc_t rsrc_t;

constexpr int NH = 32, SK = 16384, DH = 128, WIN = 4096, FF = 1376;
struct Prm { int E0, ES, E1, E2, E2C, EU, TEND, DT_UP, L0, L1, X1, X2, X3, X4, NE, FE, M0, M1, M2, M3, M4, GS, GX, N1, N2, GA, WD, DT_WO, LW, LU, EW, PM, NF, NR, C0, A0, AG, AC, TS, TW, TK, TG, GB, B0, BG, BF, D0, DG, TC, LM, C1, F0, FG, FF_, PA, PD, QA, QD, RT, W[9]; };
constexpr Prm PRM0 = {32, 20, 32, 32, 60, 60, 1968, 76, 12, 27, 75, 63, 100, 75, 3, 8, 0x2511f55b, 0x2b7425d5, 0x6017665, 0x17e0b9ec, 0x31944305, 62, 30, 29, 62, 50, 4, 106, 50, 28, 10, 0x0, 12, 14, 3, 36, 4, 4, 509, 215, 80, -2, 6, 69, 10, 8, 8, 6, 87, 120, 2, 65, 6, 4, 52, 14, 9, 18, 12, 0x77777777, 0x77777777, 0x77777777, 0x77777777, 0x77777777, 0x44444477, 0x44444444, 0x44444444, 0x44444444};
constexpr int NL = 37;
constexpr int SK0 = 21, SK1 = 32;
constexpr int NCMIN = (NL - SK1 + SK0) * 8;
constexpr int WK = 8, WV = 5;
constexpr int WUSE = 12, LNS = 7;
constexpr int QP = 14, QD = 4;
constexpr int VQ = 6;
constexpr int NSL = (NL + 7) / 8;
constexpr int VS = VQ + NSL;
constexpr int MLS = VS + 2;
constexpr int VSIDE = MLS - 5, UPREP = MLS + 6;
constexpr int CPF = 17;
constexpr int SMO = 1;
constexpr int RVS = 5, RVP = 8, RVL = 14, RVU = 18;
constexpr int RPS = 23, RPP = 27;
constexpr int PRB = 12;
constexpr int TBS = 7, TBP = 10, TBL = 13, TBU = 17;
constexpr int window(int s, int w) {
  int n = w;
  for (int u = s - w + 1; u <= s; ++u) n += u == SMO ? 6 : (u == TBL || u == SK1 || u == NL + CPF || u == NL + RVL || u == NL + SK1 ? 1 : (u == QP || u == NL + VQ - 4 ? 2 : (u == NL + VSIDE ? 4 : 0)));
  return n;
}
constexpr int cons_end(int v) {
  const int a = v - WV + 1, c = (v - MLS) * 4;
  const int m = a < c ? a : c;
  return m < 0 ? 0 : (m > NL ? NL : m);
}
constexpr u32 col(const Prm& P, int j) { const int m[6] = {P.M0, P.M1, P.M2, P.M3, P.M4, P.PM}; return (u32)(m[j / 3] >> (j % 3 * 10) & 1023) << 10; }
constexpr u32 first_col(const Prm& P, int k) {
  if (k < 4) return col(P, 12 + k) | 1u << (24 + k);
  if (k < 6) return col(P, k - 4);
  if (k < 8) return col(P, 4 + k) | 1u << (16 + k);
  if (k < 11) return col(P, k - 1);
  return (col(P, 2) ^ col(P, 3) ^ col(P, 4) ^ col(P, 5) ^ col(P, 6) ^ col(P, 10) ^ col(P, 11)) | 0xf00000u;
}
constexpr bool one_to_one(const Prm& P) {
  u32 c[10] = {};
  for (int j = 0; j < 10; ++j) c[j] = col(P, j);
  for (int j = 0; j < 10; ++j) {
    if (c[j] == 0) return false;
    const u32 low = c[j] & (0u - c[j]);
    for (int k = j + 1; k < 10; ++k) if (c[k] & low) c[k] ^= c[j];
  }
  return true;
}
static_assert(one_to_one(PRM0), "every KB of the window exactly once");
struct Tab { u32 e[512]; };
constexpr Tab make_tab(const Prm& P) {
  Tab T{};
  for (int bid = 0; bid < 256; ++bid) {
    const int xcd = bid & 7, part = bid >> 4 & 3, i8 = (bid >> 3 & 1) | bid >> 6 << 1, wi = i8 * 8 + xcd;
    const bool has_mod = wi < 48, has_up = wi >= 21;
    const int cb0 = (i8 + P.C0) & 7, cb1 = (i8 + P.C1) & 7, ms = wi - 20;
    const int mi = wi < 8 ? wi : (wi < 16 ? 8 + ((xcd + 4) & 7) : (wi < 20 ? (wi - 14) * 8 + 3 : 16 + ms / 7 * 8 + ms % 7 + (ms % 7 >= 3 ? 1 : 0)));
    const int ui = xcd * 5 + (xcd > 5 ? xcd - 5 : 0) + (i8 == 6 ? 0 : (xcd >= 5 ? 1 : 0) + (i8 == 7 ? 4 : i8 - 2));
    const bool edge = i8 >= 6;
    const int wd = cb0 == 3 ? 0 : P.WD;
    const int e[4] = {P.E0 - 4 * wd + (cb0 == 3 ? P.ES : 0), P.E1 + (cb1 == 3 ? P.ES : 0), has_mod ? (wi < 16 ? P.E2C : P.E2) + ((mi & 7) == 3 ? P.ES : 0) : 0, has_up ? P.EU : 0};
    int tw[4] = {}, win[4] = {}, span[4] = {}, k0[4] = {};
    for (int k = 0; k < 4; ++k) {
      const int np = k == 0 ? 8 - wd : 8;
      tw[k] = np ? e[k] * P.TW / (16 * np) : 0;
      k0[k] = P.NF + tw[k] * np / 2 > P.TK ? P.NF + tw[k] * np / 2 : P.TK;
      span[k] = P.TEND - k0[k] - P.TG - (k == 0 ? (wd ? P.DT_WO : P.A0) : (k == 3 && has_up ? P.DT_UP : 0));
      win[k] = span[k] - (k == 3 && has_up ? P.GA + (edge ? P.LM : 0) : 0);
    }
    int n[4] = {NCMIN / 8, NCMIN / 8, NCMIN / 8, NCMIN / 8};
    for (int left = 128 - NCMIN / 2; left > 0; --left) {
      int best = -1;
      for (int k = 3; k >= 0; --k)
        if (n[k] < NL && (best < 0 || n[k] * win[best] < n[best] * win[k])) best = k;
      ++n[best];
    }
    const int c[4] = {0, n[0] - 1, n[0] + n[1] - 2, n[0] + n[1] + n[2] - 3};
    const int nl = n[part];
    const bool has_w = part < 2 || (part == 2 ? has_mod : has_up);
    const int blk = !has_w ? 0 : (part == 0 ? cb0 : (part == 1 ? xcd * 8 + cb1 : (part == 2 ? mi : ui)));
    T.e[2 * bid] = (u32)(c[part] | nl << 7 | blk << 13 | (has_w ? 1 : 0) << 19 | (part == 0 ? wd : 0) << 20) | (u32)tw[part] << 24;
    T.e[2 * bid + 1] = (u32)(span[part] * P.TS / (4 * (nl - 1)) | span[part] * (1024 - P.TS) / (4 * (nl - 1)) << 16);
  }
  return T;
}
constexpr int step_range(const Prm& P, bool mx_) {
  int mx = 0, mn = 1 << 20;
  const Tab T = make_tab(P);
  for (int bid = 0; bid < 256; ++bid) { const int n = T.e[2 * bid] >> 7 & 63; mx = n > mx ? n : mx; mn = n < mn ? n : mn; }
  return mx_ ? mx : mn;
}
extern "C" __attribute__((used, visibility("protected"))) __constant__ const Tab wg_tab = make_tab(PRM0);
static_assert(step_range(PRM0, true) <= NL && step_range(PRM0, false) >= NCMIN / 8, "a wave's key range does not fit its load steps");
static_assert(RVU < UPREP && UPREP + 1 < SK0 && QP + QD + 1 < SK0 && cons_end(SK0 - 1) == SK0 - WV && NL - SK1 == WV, "steps every wave runs");
constexpr bool timers_long(const Prm& P) {
  const Tab T = make_tab(P);
  for (int bid = 48; bid < 64; ++bid) if (SK0 + NL - (int)(T.e[2 * bid] >> 7 & 63) > RPS) return false;
  return true;
}
static_assert(timers_long(PRM0), "steps of the second report");
constexpr int NUP = FF / 32;
constexpr int SPIN_MAX = 1 << 17;

constexpr u32 WS_EPOCH = 0;
constexpr u32 G_TB = 1024;
constexpr u32 G_MOD = 4096;
constexpr u32 G_Q = G_MOD + 3072 * 8;
constexpr u32 G_ML = G_Q + 4096 * 8;
constexpr u32 G_U = G_ML + 256 * 16;
constexpr u32 G_P = 0;
constexpr u32 G_XP = G_P + 128 * 256;
constexpr u32 G_OP = G_XP + 8 * 512 * 4;
constexpr u32 G_NT = G_OP + 43 * 512 * 8;
constexpr u32 G_PB = G_OP + 44 * 512 * 8;
constexpr u32 G_VT = G_U + 2 * (G_PB + 8192);
constexpr u32 WS_END = G_VT + 2048;
__device__ __forceinline__ u32 dup(u32 o) { return G_U + ((o >> 13) << 14) + (o & 8191u); }
constexpr u32 SENT = 0xffffffffu;
constexpr u32 OOB = 0x40000000u;

constexpr int AUX_NT = 2, AUX_SC1 = 16;

__device__ __forceinline__ float lo_f(u32 v) { return __uint_as_float(v << 16); }
__device__ __forceinline__ float hi_f(u32 v) { return __uint_as_float(v & 0xffff0000u); }
__device__ __forceinline__ float rbf(float x) { return (float)(__bf16)x; }
__device__ __forceinline__ u32 pack_bf(float lo, float hi) {
  bf16x2 r; r[0] = (__bf16)lo; r[1] = (__bf16)hi; return __builtin_bit_cast(u32, r);
}
__device__ __forceinline__ float dot2(u32 k, u32 q, float acc) {
  return __builtin_amdgcn_fdot2_f32_bf16(__builtin_bit_cast(bf16x2, k), __builtin_bit_cast(bf16x2, q), acc, false);
}
template <int CTRL> __device__ __forceinline__ float dpp(float x) {
  return __builtin_bit_cast(float, __builtin_amdgcn_mov_dpp(__builtin_bit_cast(int, x), CTRL, 0xf, 0xf, true));
}
constexpr int XOR1 = 0xB1, XOR2 = 0x4E, XOR7 = 0x141, XOR8 = 0x128;
__device__ __forceinline__ float swap32_add(float a, float b) {
  auto t = __builtin_amdgcn_permlane32_swap(__float_as_uint(a), __float_as_uint(b), false, false);
  return __uint_as_float(t[0]) + __uint_as_float(t[1]);
}
__device__ __forceinline__ float swap16_add(float a, float b) {
  auto t = __builtin_amdgcn_permlane16_swap(__float_as_uint(a), __float_as_uint(b), false, false);
  return __uint_as_float(t[0]) + __uint_as_float(t[1]);
}
__device__ __forceinline__ float wave_sum(float x) {
  x += dpp<XOR1>(x); x += dpp<XOR2>(x); x += dpp<XOR7>(x); x += dpp<XOR8>(x);
  x = swap16_add(x, x);
  return swap32_add(x, x);
}
__device__ __forceinline__ float wave_max(float x) {
  x = fmaxf(x, dpp<XOR1>(x)); x = fmaxf(x, dpp<XOR2>(x)); x = fmaxf(x, dpp<XOR7>(x)); x = fmaxf(x, dpp<XOR8>(x));
  auto s = __builtin_amdgcn_permlane16_swap(__float_as_uint(x), __float_as_uint(x), false, false);
  x = fmaxf(__uint_as_float(s[0]), __uint_as_float(s[1]));
  auto t = __builtin_amdgcn_permlane32_swap(__float_as_uint(x), __float_as_uint(x), false, false);
  return fmaxf(__uint_as_float(t[0]), __uint_as_float(t[1]));
}
template <int CTRL> __device__ __forceinline__ float sel_add(float lo, float hi, bool up) {
  const float t1 = lo + dpp<CTRL>(lo), t2 = hi + dpp<CTRL>(hi);
  return up ? t2 : t1;
}
template <int I> __device__ __forceinline__ float bc16(float x) {
  return __builtin_bit_cast(float, __builtin_amdgcn_update_dpp(0, __builtin_bit_cast(int, x), 0x150 + I, 0xf, 0xf, false));
}
__device__ __forceinline__ float bcast16(float x, int i) {
  switch (i) {
    case 0: return bc16<0>(x); case 1: return bc16<1>(x); case 2: return bc16<2>(x); case 3: return bc16<3>(x);
    case 4: return bc16<4>(x); case 5: return bc16<5>(x); case 6: return bc16<6>(x); case 7: return bc16<7>(x);
    case 8: return bc16<8>(x); case 9: return bc16<9>(x); case 10: return bc16<10>(x); case 11: return bc16<11>(x);
    case 12: return bc16<12>(x); case 13: return bc16<13>(x); case 14: return bc16<14>(x); default: return bc16<15>(x);
  }
}
__device__ __forceinline__ float silu_f(float x) { return x * __builtin_amdgcn_rcpf(1.f + __expf(-x)); }

template <int AUX> __device__ __forceinline__ u32x4 ld16(rsrc_t r, u32 voff, u32 soff) {
  return __builtin_amdgcn_raw_buffer_load_b128(r, voff, soff, AUX);
}
template <int AUX> __device__ __forceinline__ u32x2 ld8(rsrc_t r, u32 voff, u32 soff) {
  return __builtin_amdgcn_raw_buffer_load_b64(r, voff, soff, AUX);
}
__device__ __forceinline__ void st8_sc1(rsrc_t r, u32 voff, u32 v, u32 tag) {
  __builtin_amdgcn_raw_buffer_store_b64(u32x2{v, tag}, r, voff, 0, AUX_SC1);
}
__device__ __forceinline__ void st16_sc1(rsrc_t r, u32 voff, u32 v0, u32 v1, u32 tag) {
  __builtin_amdgcn_raw_buffer_store_b128(u32x4{v0, tag, v1, tag}, r, voff, 0, AUX_SC1);
}
__device__ __forceinline__ rsrc_t mk_rsrc(const void* p, u32 bytes) {
  return __builtin_amdgcn_make_buffer_rsrc(const_cast<void*>(p), 0, bytes, 0x00020000);
}
#define WAIT_VM(n) __builtin_amdgcn_s_waitcnt(((n) & 15) | (((n) >> 4) << 14) | (7 << 4) | (15 << 8))
#define SB() __builtin_amdgcn_sched_barrier(0)
#define CFENCE() asm volatile("" ::: "memory")
__device__ __forceinline__ bool all_ok(bool ok) { return __builtin_amdgcn_ballot_w64(!ok) == 0ull; }

template <int N> __device__ __forceinline__ void gather16(rsrc_t ws, u32 voff, u32 stride, u32 tag, u32x4 (&g)[N]) {
#pragma nounroll
  for (int spin = 0; spin < SPIN_MAX; ++spin) {
    CFENCE();
    bool ok = true;
#pragma unroll
    for (int i = 0; i < N; ++i) g[i] = ld16<AUX_SC1>(ws, voff + i * stride, 0);
#pragma unroll
    for (int i = 0; i < N; ++i) ok &= (g[i][1] == tag) & (g[i][3] == tag);
    if (all_ok(ok)) break;
    __builtin_amdgcn_s_sleep(8);
  }
}
template <int N> __device__ __forceinline__ void gather8(rsrc_t ws, u32 voff, u32 stride, u32 tag, u32 (&g)[N], bool act = true) {
#pragma nounroll
  for (int spin = 0; spin < SPIN_MAX; ++spin) {
    CFENCE();
    bool ok = true;
    u32x2 t[N];
#pragma unroll
    for (int i = 0; i < N; ++i) t[i] = ld8<AUX_SC1>(ws, voff + i * stride, 0);
#pragma unroll
    for (int i = 0; i < N; ++i) { ok &= (t[i][1] == tag); g[i] = t[i][0]; }
    if (all_ok(ok | !act)) break;
    __builtin_amdgcn_s_sleep(8);
  }
}

struct Smem {
  float red[8][128];
  float ml[8][2];
  float r2[64 * 20];
  u16 hb[3 * 1024];
  float stat[8][64];
  float red2[8][128];
  u32 dummy[64];
  u32x2 bc, bc2;
  u32 bc3, bc4;
  u32x4 zero[4];
  float pl[4][8];
  u32 mflag[8];
  u32x4 wbuf[8][8][64];
  __attribute__((aligned(256))) u16 w2b[32][512];
  u16 actl[2][32];
};

#define PIN(x) asm volatile("" : "+v"(x))
#define LDS_BARRIER() do { __builtin_amdgcn_s_waitcnt(0xc07f); __builtin_amdgcn_s_barrier(); CFENCE(); } while (0)
#define WAVE_SYNC() do { __builtin_amdgcn_fence(__ATOMIC_RELEASE, "wavefront"); __builtin_amdgcn_wave_barrier(); __builtin_amdgcn_fence(__ATOMIC_ACQUIRE, "wavefront"); } while (0)

__device__ __forceinline__ void dma16(rsrc_t r, u32 voff, u32 soff, u32 lds_dst) {
  u32 keep;
  asm volatile("s_mov_b32 %0, m0\n\ts_mov_b32 m0, %4\n\ts_nop 4\n\tbuffer_load_dwordx4 %1, %2, %3 offen nt lds\n\ts_mov_b32 m0, %0"
               : "=&s"(keep) : "v"(voff), "s"(r), "s"(soff), "s"(lds_dst) : "memory");
}
__device__ __forceinline__ void ld16_into(u32x4& d, rsrc_t r, u32 voff, u32 soff) {
  asm volatile("s_nop 4\n\tbuffer_load_dwordx4 %0, %1, %2, %3 offen nt" : "+v"(d) : "v"(voff), "s"(r), "s"(soff) : "memory");
}
__device__ __forceinline__ u32 perm_lo(u32 a, u32 b) { return __builtin_amdgcn_perm(b, a, 0x05040100u); }
__device__ __forceinline__ u32 perm_hi(u32 a, u32 b) { return __builtin_amdgcn_perm(b, a, 0x07060302u); }
__device__ __forceinline__ float rdl(float x, int l) { return __int_as_float(__builtin_amdgcn_readlane(__float_as_int(x), l)); }

typedef __attribute__((ext_vector_type(8))) __bf16 bf16x8;
__device__ __forceinline__ f32x4 mfma16(u32x4 a, u32x4 b, f32x4 c) {
  return __builtin_amdgcn_mfma_f32_16x16x32_bf16(__builtin_bit_cast(bf16x8, a), __builtin_bit_cast(bf16x8, b), c, 0, 0, 0);
}
typedef __attribute__((ext_vector_type(8))) unsigned u32x8;
typedef __attribute__((ext_vector_type(16))) __bf16 bf16x16;
__device__ __forceinline__ f32x4 mfma64(u32x4 a, u32x8 b, f32x4 c, u32 idx) {
  return __builtin_amdgcn_smfmac_f32_16x16x64_bf16(__builtin_bit_cast(bf16x8, a), __builtin_bit_cast(bf16x16, b), c, idx, 0, 0);
}
__device__ __forceinline__ float sum8(const float* p) {
  const f32x4 a = *reinterpret_cast<const f32x4*>(p), b = *reinterpret_cast<const f32x4*>(p + 4);
  return ((a[0] + b[0]) + (a[1] + b[1])) + ((a[2] + b[2]) + (a[3] + b[3]));
}
__device__ __forceinline__ float ln_ri(float var) {
  return __builtin_amdgcn_rcpf(rbf(__builtin_amdgcn_sqrtf(rbf(rbf(fmaxf(var, 0.f)) + 1e-5f))));
}
__device__ __forceinline__ void ln_stats2(const float (&x0)[8], const float (&x1)[8], float& mb0, float& ri0, float& mb1, float& ri1) {
  const float k0 = rdl(x0[0], 0), k1 = rdl(x1[0], 0);
  float s0 = 0.f, s1 = 0.f, q0 = 0.f, q1 = 0.f;
#pragma unroll
  for (int k = 0; k < 8; ++k) {
    const float d0 = x0[k] - k0, d1 = x1[k] - k1;
    s0 += d0; s1 += d1; q0 = fmaf(d0, d0, q0); q1 = fmaf(d1, d1, q1);
  }
  float t = swap16_add(swap32_add(s0, s1), swap32_add(q0, q1));
  t += dpp<XOR1>(t); t += dpp<XOR2>(t); t += dpp<XOR7>(t); t += dpp<XOR8>(t);
  auto u = __builtin_amdgcn_permlane16_swap(__float_as_uint(t), __float_as_uint(t), false, false);
  const float ms = __uint_as_float(u[0]) * (1.f / 512.f), mq = __uint_as_float(u[1]) * (1.f / 512.f);
  const float ri = ln_ri(fmaf(-ms, ms, mq));
  mb0 = rbf(k0 + rdl(ms, 0)); mb1 = rbf(k1 + rdl(ms, 32));
  ri0 = rdl(ri, 0); ri1 = rdl(ri, 32);
}
template <int I> struct IC { static constexpr int v = I; };
template <int I, int N, class F> __device__ __forceinline__ void sfor(F&& f) {
  if constexpr (I < N) { if (f(IC<I>{})) sfor<I + 1, N>(f); }
}
typedef __attribute__((ext_vector_type(4))) short s16x4;
typedef __attribute__((address_space(3))) s16x4* lds_s16x4p;
__device__ __forceinline__ u32x2 tr_read(u32 lds_addr) {
  return __builtin_bit_cast(u32x2, __builtin_amdgcn_ds_read_tr16_b64_v4i16((lds_s16x4p)lds_addr));
}
__device__ __forceinline__ u32 next_tag(u32 e) { return ((e | 0x40004000u) + 1u) & ~0x40004000u; }
__device__ __forceinline__ float u2f(u32 v) { return __uint_as_float(v); }
#define LDSP(T, a) ((__attribute__((address_space(3))) T*)(a))
template <class T> __device__ __forceinline__ u32 lds_addr(T* p) { u32 a = (u32)(uintptr_t)p; PIN(a); return a; }

#define TAB_LOAD "s_add_u32 s30, s28, wg_tab@rel32@lo+4\ns_addc_u32 s31, s29, wg_tab@rel32@hi+12\nglobal_load_dwordx2 %[te], %[va], s[30:31]\n"
#define FC(k) ".Lfc" #k
#define FX(r, bit, k) "s_bitcmp1_b32 %[" #r "], " #bit "\ns_cselect_b32 %[sb], " FC(k) ", 0\ns_xor_b32 %[sa], %[sa], %[sb]\n"
__global__ void __launch_bounds__(512, 2) fused_kernel(const u16* kc_, const u16* vc_, const u16* wo_, const u16* wq_, const u16* wmod_, const u16* w1_, const u16* w3_,
                                                       unsigned char* ws_, const u16* x_, const u16* cnd_, const u16* w2_, u16* out_) {
  __shared__ __attribute__((aligned(256))) Smem sm;
  const u64 a_ka = (u64)__builtin_amdgcn_kernarg_segment_ptr();
  const int bid = blockIdx.x, tid = threadIdx.x;
  u64 wa, t64; u32x2 te; int tid0;
  u32 ar[14];
  u32 tch2, sa_, sb_, va_;
  u32x4 kr[NL], vr[NL];
  asm volatile(".set .Lfc0, %0\n.set .Lfc1, %1\n.set .Lfc2, %2\n.set .Lfc3, %3\n.set .Lfc4, %4\n.set .Lfc5, %5\n.set .Lfc6, %6\n.set .Lfc7, %7\n.set .Lfc8, %8\n.set .Lfc9, %9\n.set .Lfc10, %10\n.set .Lfc11, %11"
    :: "n"(first_col(PRM0, 0)), "n"(first_col(PRM0, 1)), "n"(first_col(PRM0, 2)), "n"(first_col(PRM0, 3)), "n"(first_col(PRM0, 4)), "n"(first_col(PRM0, 5)),
       "n"(first_col(PRM0, 6)), "n"(first_col(PRM0, 7)), "n"(first_col(PRM0, 8)), "n"(first_col(PRM0, 9)), "n"(first_col(PRM0, 10)), "n"(first_col(PRM0, 11)));
  asm volatile(
    "s_load_dwordx16 s[4:19], %[ka], 0x0\ns_load_dwordx8 s[20:27], %[ka], 0x40\n"
    "s_memrealtime %[t]\n"
    "v_lshlrev_b32 %[va], 3, %[bid]\n"
    "s_getpc_b64 s[28:29]\n.Lcpf_first:\n"
    TAB_LOAD
    "s_cmp_gt_u32 %[bid], 15\ns_cbranch_scc1 .Lno_cpf\n"
    "s_mov_b32 %[sa], .Lend_a-.Lcpf_first\ns_max_i32 %[sa], %[sa], .Lend_b-.Lcpf_first\ns_max_i32 %[sa], %[sa], .Lend_c-.Lcpf_first\ns_max_i32 %[sa], %[sa], .Lend_d-.Lcpf_first\ns_max_i32 %[sa], %[sa], .Lend_e-.Lcpf_first\n"
    "v_lshlrev_b32 %[va], 6, %[tid]\nv_min_u32 %[va], %[sa], %[va]\nglobal_load_dword %[pf], %[va], s[28:29]\n"
    ".Lno_cpf:\n"
    "v_readfirstlane_b32 %[t0], %[tid]\nv_and_b32 %[va], 63, %[tid]\n"
    "s_mov_b32 %[sa], " FC(11) "\n"
    FX(bid, 0, 0) FX(bid, 1, 1) FX(bid, 2, 2) FX(bid, 3, 3) FX(bid, 4, 4) FX(bid, 5, 5) FX(bid, 6, 6) FX(bid, 7, 7) FX(t0, 6, 8) FX(t0, 7, 9) FX(t0, 8, 10)
    "v_lshl_add_u32 %[va], %[va], 4, %[sa]\n"
    "s_waitcnt lgkmcnt(0)\nglobal_load_dwordx4 %[k0], %[va], s[4:5] nt\nglobal_load_dwordx4 %[v0], %[va], s[6:7] nt\n"
    "s_bitcmp1_b32 %[t0], 6\ns_cselect_b64 s[14:15], s[16:17], s[14:15]\n"
    "s_bitcmp1_b32 %[bid], 4\ns_cselect_b64 s[8:9], s[10:11], s[8:9]\ns_cselect_b64 s[12:13], s[14:15], s[12:13]\ns_bitcmp1_b32 %[bid], 5\ns_cselect_b64 %[wa], s[12:13], s[8:9]"
    : "=&{s4}"(ar[0]), "=&{s5}"(ar[1]), "=&{s6}"(ar[2]), "=&{s7}"(ar[3]), "=&{s18}"(ar[4]), "=&{s19}"(ar[5]), "=&{s20}"(ar[6]), "=&{s21}"(ar[7]), "=&{s22}"(ar[8]), "=&{s23}"(ar[9]),
      "=&{s24}"(ar[10]), "=&{s25}"(ar[11]), "=&{s26}"(ar[12]), "=&{s27}"(ar[13]), [wa] "=&s"(wa), [t0] "=&s"(tid0), [t] "=&s"(t64), [te] "=&v"(te), [sa] "=&s"(sa_), [sb] "=&s"(sb_),
      [va] "=&v"(va_), [pf] "=&v"(tch2), [k0] "=&v"(kr[0]), [v0] "=&v"(vr[0])
    : [ka] "s"(a_ka), [bid] "s"(bid), [tid] "v"(tid) : "s8", "s9", "s10", "s11", "s12", "s13", "s14", "s15", "s16", "s17", "s28", "s29", "s30", "s31", "scc", "memory");
  asm volatile("" : "+s"(ar[0]), "+s"(ar[1]), "+s"(ar[2]), "+s"(ar[3]), "+s"(ar[4]), "+s"(ar[5]), "+s"(ar[6]), "+s"(ar[7]), "+s"(ar[8]), "+s"(ar[9]), "+s"(ar[10]), "+s"(ar[11]), "+s"(ar[12]), "+s"(ar[13]));
  asm volatile("" : "+s"(wa), "+s"(t64), "+s"(tid0), "+s"(sa_));
  const u32 tka = (u32)wall_clock64();
  const u32 t0 = tka;
  __builtin_assume(tid >= 0 && tid < 512 && tid0 >= 0 && tid0 < 512 && bid >= 0 && bid < 256);
  constexpr Prm P = PRM0;
  const int TEND = P.TEND;
  const int xcd = bid & 7, part = bid >> 4 & 3;
  const int lane = tid & 63, wave = tid0 >> 6;
  SB();
  if ((bid >> 4) == 2) {
    const u32 o = tid < 384 ? G_U + (bid & 15) * 6144 + tid * 16 : OOB;
    __builtin_amdgcn_raw_buffer_store_b128(u32x4{SENT, SENT, SENT, SENT}, mk_rsrc((void*)(ar[4] | (u64)ar[5] << 32), WS_END), o, 0, AUX_SC1);
  }
  SB();
  const bool p3 = part == 3;
  u32 pitch = (u32)(0x005600c001000020ull >> (part * 16) & 0xffff) * 32u;
  const int sh = p3 ? 2 : 3;
  u32 wv0 = (u32)((p3 ? (wave >> 1) * 128 : wave * 64) + (part == 0 ? xcd * 512 : 0) + (lane >> sh)) * pitch + ((lane & (p3 ? 3 : 7)) ^ (lane >> (p3 ? 5 : 3))) * 16u;
  const u32 wimg = (u32)(uintptr_t)(&sm.wbuf[0][0][0]);
  u32 wsz = (part == 0 ? 4096u : 512u) * pitch, wld = wimg + (p3 ? (wave & 1) * 32768 + (wave >> 1) * 8192 : wave * 8192);
  PIN(wv0);
  asm volatile("" : "+s"(wsz), "+s"(wld), "+s"(pitch));
  auto karg = [&](int i) { return ar[2 * i] | (u64)ar[2 * i + 1] << 32; };
  const u64 a_kc = karg(0), a_vc = karg(1);
  unsigned char* ws = (unsigned char*)karg(2);
  const u16 *x = (const u16*)karg(3), *cnd = (const u16*)karg(4), *w2 = (const u16*)karg(5);
  u16* out = (u16*)karg(6);
  const rsrc_t wsr = mk_rsrc(ws, WS_END);
  asm volatile("s_waitcnt vmcnt(3)\n\ts_cmp_eq_u32 %1, 2\n\ts_cbranch_scc1 1f\n\ts_waitcnt vmcnt(2)\n1:" : "+v"(te) : "s"(bid >> 4) : "scc");
  const u32 te0 = __builtin_amdgcn_readfirstlane(te[0]), te1 = __builtin_amdgcn_readfirstlane(te[1]);
  const int bl0 = te0 & 127, nl = te0 >> 7 & 63, blk = te0 >> 13 & 63;
  const bool has_w = te0 >> 19 & 1;
  const rsrc_t rw = mk_rsrc((const void*)wa, wsz);
  const u32 wstep = pitch << (6 - sh), wflip = (p3 || part == 0) ? 32u : 0u, wflip0 = part == 0 ? 32u : 0u;
  u32 wso = blk << (sh + 4);
  int wleft = 8;
  auto piece = [&]() {
    dma16(rw, wv0, wso, wld);
    wso += wstep; wld += 1024;
    wv0 ^= (wleft & 1) ? wflip : wflip0;
    --wleft;
  };
  SB();
  const int b = bid >> 3 & 1, head = xcd * 4 + (bid >> 6 ^ 3);
  const int g = lane >> 4, c = lane & 15;
  const int i8 = b | bid >> 6 << 1, wi = i8 * 8 + xcd;
  const bool is_up = p3 && has_w, modc = part == 2 && wi < 16;
  const size_t base = ((size_t)(b * NH + head) * SK + (SK - WIN)) * DH;
  const rsrc_t rk = mk_rsrc((const u16*)a_kc + base, WIN * DH * 2), rv = mk_rsrc((const u16*)a_vc + base, WIN * DH * 2);
  int tau = te1 & 0xffff;
  const int tauw = te0 >> 24 << 7, wdef = te0 >> 20 & 15;
  int due = P.NF * 256, now = 0;
  auto gate = [&]() {
    while (now < due) { now = (int)(((u32)wall_clock64() - t0) << 8); if (now < due) __builtin_amdgcn_s_sleep(1); }
    due += tau;
  };
  auto until = [&](int t) { while ((int)(((u32)wall_clock64() - t0) << 8) < t); };
  const u32 pslot = dup(G_P + (head & 15) * 2048 + (b * 4 + part) * 16);
  const u32 die = xcd >> 2;
  const bool voter = p3 && i8 == 1, timer = p3 && i8 < 2;
  int vote = 0, lag = 0, shift = 0;
  const u32 pbo = dup(G_PB) + wave * 1152;
  u32 near = 0;
  u32x2 ep; u32 xs0, xs1, tch1; u32x4 xr0, xr1;
  const rsrc_t rx = mk_rsrc(part == 2 ? cnd : x, (part == 1 || (part == 2 && has_w)) ? 2048u : 0u);
  auto small_ops = [&]() {
    ep = ld8<AUX_SC1>(wsr, wave == 7 ? WS_EPOCH : OOB, 0);
    xs0 = __builtin_amdgcn_raw_buffer_load_b16(rx, tid * 2, 0, 0); xs1 = __builtin_amdgcn_raw_buffer_load_b16(rx, tid * 2, 1024, 0);
    xr0 = ld16<0>(rx, part == 1 ? lane * 16 : OOB, 0); xr1 = ld16<0>(rx, part == 1 ? lane * 16 : OOB, 1024);
    tch1 = __builtin_amdgcn_raw_buffer_load_b32(mk_rsrc(w2 + (size_t)blk * 32 * 512, is_up ? 64u : 0u), tid == 384 ? 0 : OOB, 0, AUX_NT);
  };
  if (has_w) {
    if (modc) now = 1 << 30;
#pragma nounroll
    while (wleft > wdef) { gate(); due += tauw - tau; piece(); }
    now = 0;
  }
  due = max(due, P.TK * 256);
  SB();
  u32 sot = sa_ & 0xffc00u;
  {
    const u32 s1 = (bl0 + lane - 1 - (lane >= SK0 ? NL - nl : 0)) ^ (124 + part);
#pragma unroll
    for (int k = 0; k < 7; ++k) sot ^= (u32)-(int)(s1 >> k & 1u) & col(P, k);
    PIN(sot);
  }
  u32 tag = 0;
  u32x4 bf[8];
  auto wo_prep = [&]() {
    const u32 wi_ = wimg + wave * 8192;
    const int q = (lane >> 2) & 3, pp = lane & 3;
#pragma unroll
    for (int s = 0; s < 2; ++s) {
#pragma unroll
      for (int t = 0; t < 4; ++t) {
        const int r0 = s * 32 + g * 8 + q, r1 = r0 + 4, ch = t * 2 + (pp >> 1);
        const u32x2 lo = tr_read(wi_ + r0 * 128 + ((ch ^ (r0 & 7)) * 16) + (pp & 1) * 8), hi = tr_read(wi_ + r1 * 128 + ((ch ^ (r1 & 7)) * 16) + (pp & 1) * 8);
        bf[s * 4 + t] = u32x4{lo[0], lo[1], hi[0], hi[1]};
      }
    }
  };
  auto gemv_go = [&](u32 hh) {
    sm.hb[tid] = (u16)hh; sm.hb[512 + tid] = (u16)(hh >> 16);
    WAVE_SYNC();
    const u32x4* a = (c & 11) == 0 ? reinterpret_cast<const u32x4*>(&sm.hb[(c >> 2) * 512 + wave * 64 + g * 8]) : &sm.zero[0];
    const u32x4 a0 = a[0], a1 = a[(c & 11) == 0 ? 4 : 0];
    const f32x4 z = {0.f, 0.f, 0.f, 0.f};
    f32x4 d[4];
#pragma unroll
    for (int t = 0; t < 4; ++t) d[t] = mfma16(a0, bf[t], z);
#pragma unroll
    for (int t = 0; t < 4; ++t) d[t] = mfma16(a1, bf[4 + t], d[t]);
    if (lane < 32) {
#pragma unroll
      for (int t = 0; t < 4; ++t) sm.r2[(t * 16 + c) * 20 + g * 8 + wave] = d[t][0];
    }
  };
  auto mod_stage = [&]() {
    wo_prep();
    gemv_go(pack_bf(silu_f(lo_f(xs0)), silu_f(lo_f(xs1))));
    __builtin_amdgcn_s_waitcnt(0xc07f);
    if (lane == 0) *LDSP(volatile u32, (u32)(uintptr_t)&sm.mflag[wave]) = 1u;
    if (wave == 7) {
#pragma nounroll
      for (int spin = 0; spin < SPIN_MAX; ++spin) {
        const u32 f = *LDSP(volatile u32, (u32)(uintptr_t)&sm.mflag[lane & 7]);
        if (all_ok(f != 0u)) break;
        __builtin_amdgcn_s_sleep(1);
      }
      CFENCE();
      st8_sc1(wsr, G_MOD + (blk * 64 + lane) * 8, pack_bf(sum8(&sm.r2[lane * 20]), sum8(&sm.r2[lane * 20 + 8])), tag);
    }
  };
  if (lane < 4) sm.zero[lane] = u32x4{0u, 0u, 0u, 0u};
  if (part == 2 && has_w) {
    if (tid < 8) sm.mflag[tid] = 0u;
    LDS_BARRIER();
    if (__builtin_expect(modc, 0)) {
      small_ops();
      asm volatile("s_waitcnt vmcnt(0)" ::: "memory");
      tag = next_tag(__builtin_amdgcn_readfirstlane(ep[0]));
      mod_stage();
      __builtin_amdgcn_s_barrier();
    }
  }
  SB();
  const int fd = (xcd * 8 + i8) * 8 + (lane & 3) * 2;
  const int ub = wave >> 2, ud = (wave & 3) * 128 + c * 8 + g * 2;
  u32x4 sg[3]; u32 sx2;
  auto side_loads = [&]() {
    const bool fin = part == 1 && wave == 0;
    sg[0] = ld16<AUX_SC1>(wsr, is_up ? G_MOD + (1024 + ud) * 8 : (fin ? G_MOD + (1024 + fd) * 8 : OOB), 0);
    sg[1] = ld16<AUX_SC1>(wsr, is_up ? G_MOD + (1536 + ud) * 8 : (fin ? G_MOD + (2560 + fd) * 8 : OOB), 0);
    sg[2] = ld16<AUX_SC1>(wsr, is_up ? G_MOD + (2048 + ud) * 8 : OOB, 0);
    sx2 = __builtin_amdgcn_raw_buffer_load_b32(mk_rsrc(x, (is_up || fin) ? 2048u : 0u), is_up ? ub * 1024 + ud * 2 : (g & 1) * 1024 + fd * 2, 0, 0);
  };
  auto side_pin = [&]() { PIN(sg[0]); PIN(sg[1]); PIN(sg[2]); PIN(sx2); };
  const u32 mlo = G_ML + (head * 2 + b) * 64 + (lane & 3) * 16;
  float fmrg = 0.f;
  auto merge_factor = [&](const u32x4& ml) {
    const float mi = u2f(ml[0]);
    float M = fmaxf(mi, dpp<XOR1>(mi)); M = fmaxf(M, dpp<XOR2>(M));
    const float e = __expf(mi - M);
    float L = e * u2f(ml[2]);
    L += dpp<XOR1>(L); L += dpp<XOR2>(L);
    fmrg = rdl(e * __builtin_amdgcn_rcpf(L), part);
  };
  f32x2 ga = {0.f, 0.f}, xin = {0.f, 0.f}, cc1 = {0.f, 0.f};
  float Af = 0.f, Bf = 0.f;
  const int hpos = ub * 512 + (ud & ~15) + (g & 1) * 8 + (c & 1) * 4 + (g >> 1) * 2;
  auto up_prep = [&]() {
    if (!all_ok((sg[0][1] == tag) & (sg[0][3] == tag) & (sg[1][1] == tag) & (sg[1][3] == tag) & (sg[2][1] == tag) & (sg[2][3] == tag))) gather16<3>(wsr, G_MOD + (1024 + ud) * 8, 512 * 8, tag, sg);
    ga = f32x2{ub ? hi_f(sg[0][0]) : lo_f(sg[0][0]), ub ? hi_f(sg[0][2]) : lo_f(sg[0][2])};
    cc1 = f32x2{rbf(1.f + (ub ? hi_f(sg[2][0]) : lo_f(sg[2][0]))), rbf(1.f + (ub ? hi_f(sg[2][2]) : lo_f(sg[2][2])))};
    xin = f32x2{lo_f(sx2), hi_f(sx2)};
    u32* hp = reinterpret_cast<u32*>(&sm.hb[hpos]);
    hp[512] = pack_bf(cc1[0], cc1[1]);
    hp[1024] = ub ? perm_hi(sg[1][0], sg[1][2]) : perm_lo(sg[1][0], sg[1][2]);
  };
  u32x8 bw[4];
  auto up_frags = [&]() {
    const int pp = lane & 3;
    const u32 a = wimg + (pp & 1) * 32768 + ((pp >> 1) * 32 + g * 8 + ((lane >> 2) & 3)) * 64 + (((wave >> 1) ^ ((g & 1) | (pp & 2))) * 16) + (wave & 1) * 8;
#pragma unroll
    for (int j = 0; j < 4; ++j) {
      const u32x2 r0 = tr_read(a + j * 4096), r1 = tr_read(a + j * 4096 + 256), r2 = tr_read(a + j * 4096 + 16384), r3 = tr_read(a + j * 4096 + 16640);
      bw[j] = u32x8{r0[0], r0[1], r1[0], r1[1], r2[0], r2[1], r3[0], r3[1]};
    }
  };
  u32 wfa[4];
  auto wo_frags = [&](int s0) {
#pragma unroll
    for (int t = 0; t < 4; ++t) {
#pragma unroll
      for (int s = s0; s < 2; ++s) {
        const u32x2 lo = tr_read(wfa[t] + s * 4096), hi = tr_read(wfa[t ^ 1] + s * 4096 + 256);
        bw[t][s * 4] = lo[0]; bw[t][s * 4 + 1] = lo[1]; bw[t][s * 4 + 2] = hi[0]; bw[t][s * 4 + 3] = hi[1];
      }
    }
  };
  const bool par = lane & 8;
  auto limit = [&](auto sc) {
    constexpr int s = sc.v;
    WAIT_VM(window(s, 1 + (PRM0.W[s >> 3] >> (4 * (s & 7)) & 7)));
    SB();
  };
  auto sample = [&]() { if (timer) lag += (int)(((u32)wall_clock64() - t0) << 8) - due + ((int)__builtin_amdgcn_s_getreg(7 | (3 << 11)) + 1) * tau; };
  auto report = [&](u32 dst) {
    if (timer) {
      if (lane == 0) sm.mflag[wave] = vote * 65536 + min(max(lag >> 8, 0), 4000);
      lag = 0;
      LDS_BARRIER();
      if (wave == 7) {
        int s = sm.mflag[lane & 7];
        s += __builtin_amdgcn_mov_dpp(s, XOR1, 0xf, 0xf, true); s += __builtin_amdgcn_mov_dpp(s, XOR2, 0xf, 0xf, true); s += __builtin_amdgcn_mov_dpp(s, XOR7, 0xf, 0xf, true);
        if (lane < 8) st8_sc1(wsr, dst + lane * 128 + wi * 8, s, tag);
      }
    }
    SB();
  };
  float l0 = 0.f, l1 = 0.f;
  u32x2 gmd[2], tbg;
  int t1 = 0;
#pragma unroll
  for (int i = SK0; i < SK1; ++i) kr[i] = u32x4{0u, 0u, 0u, 0u};
  const int first = SK0 + NL - nl;
  auto left_out = [&](int i) { int f = first; asm volatile("" : "+s"(f)); return i < f; };
  sfor<1, NL>([&](auto ic) {
    constexpr int i = ic.v;
    if constexpr (i >= SK0 && i < SK1) { if (left_out(i)) return true; }
    const u32 so = __builtin_amdgcn_readlane(sot, i);
    gate();
    if constexpr ((i >= TBS && i <= TBP) || (i >= RPS && i <= RPP)) sample();
    SB();
    if constexpr (i >= SK0 && i < SK1) ld16_into(kr[i], rk, lane * 16, so); else kr[i] = ld16<AUX_NT>(rk, lane * 16, so);
    if constexpr (i == SMO) small_ops();
    if constexpr (i == SMO + 1) { if (voter && (xcd & 3) == 0) __builtin_amdgcn_raw_buffer_store_b32(0u, wsr, pbo + (lane >> 5) * 8192 + (lane & 31) * 4, 0, AUX_SC1); }
    if constexpr (i == SK1) tbg = ld8<AUX_SC1>(wsr, wave == 7 ? G_VT + xcd * 128 + c * 8 : OOB, 0);
    if constexpr (i == TBL) tbg = ld8<AUX_SC1>(wsr, wave == 7 ? G_TB + xcd * 128 + c * 8 : OOB, 0);
    if constexpr (i == QP) { gmd[0] = ld8<AUX_SC1>(wsr, part == 1 ? G_MOD + tid * 8 : OOB, 0); gmd[1] = ld8<AUX_SC1>(wsr, part == 1 ? G_MOD + tid * 8 : OOB, 4096); }
    SB();
    SB();
    if (i >= 4) limit(ic);
    if (i == TBP) {
      if (timer) {
        if (lane == 0) sm.mflag[wave] = lag;
        lag = 0;
        LDS_BARRIER();
        if (wave == 7) {
          int s = sm.mflag[lane & 7];
          s += __builtin_amdgcn_mov_dpp(s, XOR1, 0xf, 0xf, true); s += __builtin_amdgcn_mov_dpp(s, XOR2, 0xf, 0xf, true); s += __builtin_amdgcn_mov_dpp(s, XOR7, 0xf, 0xf, true);
          if (lane < 8) st8_sc1(wsr, G_TB + lane * 128 + wi * 8, t0 << 10 | (u32)min(s / (2048 * (TBP - TBS + 1)), 1023), tag);
        }
      }
      SB();
    }
    if (i == PRB) {
      if (voter) {
        u64 ta, tb, tc; u32 dm;
        const u32 pa = pbo + (wave & 1) * 8192;
        asm volatile("s_memtime %0\n\ts_waitcnt lgkmcnt(0)\n\ts_load_dword %3, %4, %5\n\ts_waitcnt lgkmcnt(0)\n\ts_memtime %1\n\ts_waitcnt lgkmcnt(0)\n\t"
                     "s_load_dword %3, %4, %6\n\ts_waitcnt lgkmcnt(0)\n\ts_memtime %2\n\ts_waitcnt lgkmcnt(0)"
                     : "=&s"(ta), "=&s"(tb), "=&s"(tc), "=&s"(dm) : "s"(ws), "s"(pa), "s"(pa ^ 8192u));
        const int d = min(max((int)(2 * (u32)tb - (u32)ta - (u32)tc), -500), 500);
        vote = ((wave ^ die) & 1) ? -d : d;
      }
      SB();
    }
    if (i == TBU) {
      if (wave == 7) {
        const bool ok = tbg[1] == tag;
        float lf = ok ? (float)min(max((int)((t0 << 10 | 1023u) - tbg[0]) >> 10, -300), 300) : 0.f, nf = ok ? 1.f : 0.f, lg = ok ? (float)(tbg[0] & 1023u) : 0.f;
        lf += dpp<XOR1>(lf); nf += dpp<XOR1>(nf); lg += dpp<XOR1>(lg); lf += dpp<XOR2>(lf); nf += dpp<XOR2>(nf); lg += dpp<XOR2>(lg);
        lf += dpp<XOR7>(lf); nf += dpp<XOR7>(nf); lg += dpp<XOR7>(lg); lf += dpp<XOR8>(lf); nf += dpp<XOR8>(nf); lg += dpp<XOR8>(lg);
        const float rn = __builtin_amdgcn_rcpf(fmaxf(nf, 1.f));
        shift = __builtin_amdgcn_readfirstlane(min(max((int)((lg * rn - (float)P.GS) * (float)P.GB * 0.125f), 0), 150));
        if (lane == 0) sm.bc = u32x2{tag, t0 - (u32)(int)(lf * rn) + shift};
      }
      LDS_BARRIER();
      const u32x2 bc = sm.bc;
      tag = __builtin_amdgcn_readfirstlane(bc[0]);
      t1 = (__builtin_amdgcn_readfirstlane((int)(bc[1] - t0)) + TEND + P.X1) * 256;
      tau -= __builtin_amdgcn_readfirstlane((int)((float)(int)(t0 - bc[1]) * 256.f * __builtin_amdgcn_rcpf((float)(nl - TBU - 1))));
      SB();
    }
    if (i == RPP) report(G_VT);
    if (i == LNS) {
      tag = next_tag(__builtin_amdgcn_readfirstlane(ep[0]));
      if (part == 1) {
        float x0[8], x1[8], mb0, ri0, mb1, ri1;
#pragma unroll
        for (int k = 0; k < 4; ++k) { x0[2 * k] = lo_f(xr0[k]); x0[2 * k + 1] = hi_f(xr0[k]); x1[2 * k] = lo_f(xr1[k]); x1[2 * k + 1] = hi_f(xr1[k]); }
        ln_stats2(x0, x1, mb0, ri0, mb1, ri1);
        l0 = rbf(rbf(lo_f(xs0) - mb0) * ri0); l1 = rbf(rbf(lo_f(xs1) - mb1) * ri1);
      }
      PIN(l0); PIN(l1);
      SB();
    }
    if (i == WUSE) {
      PIN(tch1); PIN(tch2);
      if ((part == 1 || (part == 2 && has_w && !modc))) {
        WAIT_VM(WK); CFENCE();
        if (part == 2) mod_stage(); else wo_prep();
      }
#pragma unroll
      for (int k = 0; k < 8; ++k) PIN(bf[k]);
      SB();
    }
    if (i == QP + QD) {
      if (part == 1) {
        u32 gm[2] = {gmd[0][0], gmd[1][0]};
        if (!all_ok((gmd[0][1] == tag) & (gmd[1][1] == tag))) gather8<2>(wsr, G_MOD + tid * 8, 512 * 8, tag, gm);
        gemv_go(pack_bf(rbf(l0 * rbf(1.f + lo_f(gm[1]))) + lo_f(gm[0]), rbf(l1 * rbf(1.f + hi_f(gm[1]))) + hi_f(gm[0])));
      }
      SB();
    }
    if (i == QP + QD + 1) {
      if (part == 1) {
        LDS_BARRIER();
        if (wave == 0) {
          const int bb = lane >> 5, jq = lane & 31;
          st8_sc1(wsr, G_Q + (bb * 2048 + blk * 32 + jq) * 8, pack_bf(sum8(&sm.r2[2 * jq * 20 + bb * 8]), sum8(&sm.r2[(2 * jq + 1) * 20 + bb * 8])), tag);
        }
      }
      SB();
    }
    return true;
  });
  PIN(sot);
  if (wave == 7) {
    const bool ok = tbg[1] == tag;
    float lg = ok ? (float)(tbg[0] & 0xffffu) : 0.f, nf = ok ? 8.f * (RPP - RPS + 1) : 0.f;
    lg += dpp<XOR1>(lg); nf += dpp<XOR1>(nf); lg += dpp<XOR2>(lg); nf += dpp<XOR2>(nf); lg += dpp<XOR7>(lg); nf += dpp<XOR7>(nf); lg += dpp<XOR8>(lg); nf += dpp<XOR8>(nf);
    lg *= __builtin_amdgcn_rcpf(fmaxf(nf, 1.f));
    if (lane == 0) sm.bc2 = u32x2{(u32)min(shift * P.DG * 32, P.EW * 256), (u32)min(max((int)((lg - (float)P.B0) * (float)(P.BG * 32) + fmaxf(lg - (float)P.TC, 0.f) * (float)(P.RT * 32)) - shift * P.D0 * 32, 0), 200 * 256)};
  }
  LDS_BARRIER();
  {
    const u32x2 bc = sm.bc2;
    const int st = __builtin_amdgcn_readfirstlane(bc[1]), sp = (st * P.BF) >> 3;
    t1 += st + __builtin_amdgcn_readfirstlane(bc[0]);
    due += P.TG * 256 + sp;
    tau = (te1 >> 16) + __builtin_amdgcn_readfirstlane((int)((float)(st - sp) * __builtin_amdgcn_rcpf((float)(nl - 3))));
  }
  const u32 qoff = G_Q + ((b * 32 + head) * 64 + c * 4) * 8;
  u32x4 qg[2], mlg;
  u32 qq[4];
  constexpr int NB = (NL + 15) / 16;
  float xlo, sc[NB], pr[NB], m = 0.f;
  f32x4 acc = {0.f, 0.f, 0.f, 0.f};
  u32 pb[NB], pm[4], pl8[8], cpf;
  const u32 adst0 = lds_addr(lane < 32 ? &sm.red[wave][c * 8 + g * 4] : &sm.r2[(lane - 32) * 4]);
  u32 po, pso, rdst, rsrc, xo;
  auto wo_addr = [&]() {
    po = dup(G_P + (xcd & 3) * 8192) + tid * 16 + near;
    pso = tid < 16 ? pslot + c * 128 + near : OOB;
    PIN(pso);
    rdst = (u32)(uintptr_t)&sm.red2[wave][(lane & 31) * 4 + (lane >> 5) * 2];
    rsrc = (u32)(uintptr_t)&sm.red2[0][((wave & 1) * 16 + c) * 4 + (g & 1) * 2 + (g >> 1)];
    xo = dup(G_XP + xcd * 2048 + (wave & 1) * 1024 + (blk * 64 + (lane & ~1)) * 2) + (lane & 1) * 8192;
    PIN(po); PIN(rdst); PIN(rsrc); PIN(xo);
    const int q = (lane >> 2) & 3, pp = lane & 3, r = (g & 1) * 4 + (q & 1);
#pragma unroll
    for (int t = 0; t < 4; ++t) { wfa[t] = wimg + wave * 8192 + ((g >> 1) * 16 + (q >> 1) * 8 + r) * 128 + (pp & 1) * 8 + ((t * 2 + (pp >> 1)) ^ r ^ (q & 2)) * 16; PIN(wfa[t]); }
  };
  const u32 nadd = wdef ? P.LW : (is_up ? P.LU : 0);
  u32 nto = tid < 2 ? dup(G_NT) + bid * 8 + tid * 8192 : OOB;
  PIN(nto);
  auto notices = [&]() -> int {
    u32 off = dup(G_NT) + near + lane * 16;
    PIN(off);
    until(due);
    int mx;
#pragma nounroll
    for (int spin = 0; spin < SPIN_MAX; ++spin) {
      CFENCE();
      const u32x4 ga_ = ld16<AUX_SC1>(wsr, off, 0), gb_ = ld16<AUX_SC1>(wsr, off, 1024);
      mx = max(max((int)(ga_[0] - t0), (int)(ga_[2] - t0)), max((int)(gb_[0] - t0), (int)(gb_[2] - t0)));
      if (all_ok((((ga_[1] ^ tag) | (ga_[3] ^ tag)) | ((gb_[1] ^ tag) | (gb_[3] ^ tag))) == 0u)) break;
      for (int k = 0; k < P.AG; ++k) __builtin_amdgcn_s_sleep(1);
    }
    if (all_ok(mx <= (t1 >> 8) - P.AC)) return t1;
    mx = max(mx, __builtin_amdgcn_mov_dpp(mx, XOR1, 0xf, 0xf, true)); mx = max(mx, __builtin_amdgcn_mov_dpp(mx, XOR2, 0xf, 0xf, true));
    mx = max(mx, __builtin_amdgcn_mov_dpp(mx, XOR7, 0xf, 0xf, true)); mx = max(mx, __builtin_amdgcn_mov_dpp(mx, XOR8, 0xf, 0xf, true));
    mx = max(max(__builtin_amdgcn_readlane(mx, 0), __builtin_amdgcn_readlane(mx, 16)), max(__builtin_amdgcn_readlane(mx, 32), __builtin_amdgcn_readlane(mx, 48)));
    return max(min(mx, 1 << 20) * 256, t1 + P.L0 * 256) + P.L1 * 256;
  };
  auto pv_row = [&](auto jc) {
    constexpr int i = jc.v;
    const u32 pv = __float_as_uint(bcast16(u2f(pb[i >> 4]), i & 15));
    acc = mfma16(u32x4{pv & pm[0], pv & pm[1], pv & pm[2], pv & pm[3]}, vr[i], acc);
    PIN(acc);
    return true;
  };
  sfor<0, NL - 1>([&](auto ic) {
    constexpr int v = ic.v;
    if constexpr (v >= SK0 && v < SK1) { if (left_out(v)) return true; }
    if constexpr (v > 0) {
      const u32 so = __builtin_amdgcn_readlane(sot, v);
      if constexpr (v >= SK1) { if (v == NL - P.NE) tau = (tau * P.FE) >> 4; }
      gate();
      if constexpr (v >= RVS && v <= RVP) sample();
      SB();
      if constexpr (v >= SK0 && v < SK1) ld16_into(vr[v], rv, lane * 16, so); else vr[v] = ld16<AUX_NT>(rv, lane * 16, so);
      SB();
      SB();
    }
    if constexpr (v == SK0 - 1) {
#pragma unroll
      for (int i = SK0; i < SK1; ++i) { vr[i] = u32x4{0u, 0u, 0u, 0u}; PIN(vr[i]); }
    }
    if constexpr (v == VQ - 4) { qg[0] = ld16<AUX_SC1>(wsr, qoff, 0); qg[1] = ld16<AUX_SC1>(wsr, qoff + 16, 0); }
    if constexpr (v == VSIDE) side_loads();
    if constexpr (v == CPF) {
      u64 pc; u32 dlt, len;
      asm volatile("s_getpc_b64 %0\n.Lcpf_here:\n\ts_mov_b32 %1, .Ltail_start-.Lcpf_here\n\ts_mov_b32 %2, .Lend_a-.Ltail_start\n\ts_max_i32 %2, %2, .Lend_b-.Ltail_start\n\ts_max_i32 %2, %2, .Lend_c-.Ltail_start\n\ts_max_i32 %2, %2, .Lend_d-.Ltail_start\n\ts_max_i32 %2, %2, .Lend_e-.Ltail_start" : "=s"(pc), "=s"(dlt), "=s"(len) :: "scc");
      pc += dlt;
      const rsrc_t rc = mk_rsrc((const void*)pc, (part == 3 && i8 == 1) ? len : 0u);
      cpf = __builtin_amdgcn_raw_buffer_load_b32(rc, tid * 64, 0, 0);
    }
    if constexpr (v == SK1 + 2) PIN(cpf);
    if constexpr (v == RVP) report(G_VT + 1024);
    if constexpr (v == RVL) tbg = ld8<AUX_SC1>(wsr, wave == 7 ? G_VT + (g ? 0 : 1024) + xcd * 128 + c * 8 : OOB, 0);
    if constexpr (v == SK1) mlg = ld16<AUX_SC1>(wsr, wave == 0 ? mlo : OOB, 0);
    SB();
    if constexpr (NL + v < 72) limit(IC<NL + v>{});
    if constexpr (v >= SK0 + WV) { WAIT_VM(v < SK1 ? WV : WV + 1); SB(); }
    if constexpr (v == VQ) {
      if (!all_ok((qg[0][1] == tag) & (qg[0][3] == tag) & (qg[1][1] == tag) & (qg[1][3] == tag))) gather16<2>(wsr, qoff, 16, tag, qg);
      qq[0] = qg[0][0]; qq[1] = qg[0][2]; qq[2] = qg[1][0]; qq[3] = qg[1][2];
    }
    if constexpr (v >= VQ && v < VS) {
      constexpr int sl = v - VQ, bt = sl >> 1;
      float xs[8];
#pragma unroll
      for (int i = 0; i < 8; ++i) {
        float d = 0.f;
        if (sl * 8 + i < NL) {
#pragma unroll
          for (int k = 0; k < 4; ++k) d = dot2(kr[sl * 8 + i][k], qq[k], d);
        }
        xs[i] = d;
      }
#pragma unroll
      for (int h = 0; h < 4; ++h) xs[h] = sel_add<XOR7>(xs[h], xs[h + 4], c & 4);
#pragma unroll
      for (int h = 0; h < 2; ++h) xs[h] = sel_add<XOR2>(xs[h], xs[h + 2], c & 2);
      xs[0] = sel_add<XOR1>(xs[0], xs[1], c & 1);
      if constexpr ((sl & 1) || sl == NSL - 1) {
        const float t = (sl & 1) ? sel_add<XOR8>(xlo, xs[0], c & 8) : xs[0] + dpp<XOR8>(xs[0]);
        const float s = rbf(rbf(t) * 0.08838834764831845f);
        sc[bt] = (bt * 16 + c < SK0 || (bt * 16 + c >= first && bt * 16 + c < NL)) ? s : -__builtin_inff();
        PIN(sc[bt]);
      } else {
        xlo = xs[0];
        PIN(xlo);
      }
    }
    if constexpr (v == VS) {
      float mloc = sc[0];
#pragma unroll
      for (int bt = 1; bt < NB; ++bt) mloc = fmaxf(mloc, sc[bt]);
      m = fmaxf(wave_max(mloc), -3.0e38f);
      float ls = 0.f;
#pragma unroll
      for (int bt = 0; bt < NB; ++bt) { pr[bt] = __expf(sc[bt] - m); ls += pr[bt]; }
      const float l = wave_sum(ls);
      if (lane == 0) { sm.ml[wave][0] = m; sm.ml[wave][1] = l; }
      if (c == ((NL - 1) & 15)) sm.pl[g][wave] = pr[NB - 1];
    }
    if constexpr (v >= SK1) pv_row(IC<v - SK1 + SK0 - WV>{});
    if constexpr (v < SK0 || v >= SK0 + WV) sfor<cons_end(v - 1), cons_end(v)>(pv_row);
    if constexpr (v == RVU) {
      if (wave == 7) {
        if (__builtin_expect(!all_ok(tbg[1] == tag || g == 0), 0)) {
          u32 vg[1];
          gather8<1>(wsr, G_VT + xcd * 128 + c * 8, 0, tag, vg);
          if (g) tbg = u32x2{vg[0], tag};
        }
        const bool ok = tbg[1] == tag;
        float lg = ok ? (float)(g ? (int)tbg[0] >> 16 : (int)(tbg[0] & 0xffffu)) : 0.f, nf = ok ? 8.f * (RVP - RVS + 1) : 0.f;
        lg += dpp<XOR1>(lg); nf += dpp<XOR1>(nf); lg += dpp<XOR2>(lg); nf += dpp<XOR2>(nf); lg += dpp<XOR7>(lg); nf += dpp<XOR7>(nf); lg += dpp<XOR8>(lg); nf += dpp<XOR8>(nf);
        lg *= __builtin_amdgcn_rcpf(fmaxf(nf, 1.f));
        if (lane == 16) sm.bc4 = __float_as_uint(lg);
        if (lane == 0) sm.bc3 = (u32)min(max((int)((lg - (float)P.F0) * (float)(P.FG * 32)), 0), 200 * 256);
      }
      LDS_BARRIER();
      const int st = __builtin_amdgcn_readfirstlane(sm.bc3), sp = (st * P.FF_) >> 3;
      near = ((__builtin_amdgcn_readfirstlane(sm.bc4) >> 31 ^ die ^ P.NR ^ 1) & 1) * 8192u;
      t1 += st;
      due += sp;
      tau += __builtin_amdgcn_readfirstlane((int)((float)(st - sp) * __builtin_amdgcn_rcpf((float)(nl - RVU - 3))));
    }
    if constexpr (v == SK1 + 1) {
      float p[8];
#pragma unroll
      for (int w = 0; w < 8; ++w) p[w] = sm.pl[g][w] * __expf(sm.ml[w][0] - m);
#pragma unroll
      for (int w = 0; w < 8; ++w) pl8[w] = pack_bf(p[w], p[w]);
#pragma unroll
      for (int w = 0; w < 8; ++w) PIN(pl8[w]);
    }
    if constexpr (v == MLS - 1) { side_pin(); if (is_up) up_prep(); PIN(ga); PIN(xin); PIN(cc1); }
    if constexpr (v == UPREP - 1) wo_addr();
    if constexpr (v == UPREP) {
#pragma unroll
      for (int k = 0; k < 4; ++k) asm volatile("" : "=v"(bw[k]));
      if (part == 0) wo_frags(0); else if (is_up) up_frags();
#pragma unroll
      for (int k = 0; k < 4; ++k) PIN(bw[k]);
    }
    if constexpr (v == UPREP + 1) {
      if (is_up) {
        const u32x4* a1 = reinterpret_cast<const u32x4*>(&sm.hb[1024 + (c >> 2 & 1) * 1024 + (c >> 3) * 512 + (g >> 1) * 256 + (c >> 1 & 1) * 32 + (g & 1) * 16 + (c & 1) * 8]);
        f32x4 d = {0.f, 0.f, 0.f, 0.f};
#pragma unroll
        for (int j = 0; j < 4; ++j) d = mfma64(a1[j * 8], bw[j], d, (c & 1) ? 0xeeeeu : 0x4444u);
        float t = par ? d[2] + d[3] : d[0] + d[1];
        t += dpp<XOR8>(t);
        const auto ab = __builtin_amdgcn_permlane16_swap(__float_as_uint(t), __float_as_uint(t), false, false);
        Af = u2f(ab[0]); Bf = u2f(ab[1]);
      }
      PIN(Af); PIN(Bf);
    }
    if constexpr (v == MLS) {
      LDS_BARRIER();
      float mm[8], ll[8];
#pragma unroll
      for (int w = 0; w < 8; ++w) { const f32x2 t = *reinterpret_cast<const f32x2*>(&sm.ml[w][0]); mm[w] = t[0]; ll[w] = t[1]; }
      float Mwg = mm[0], Lwg = 0.f;
#pragma unroll
      for (int w = 1; w < 8; ++w) Mwg = fmaxf(Mwg, mm[w]);
      if (wave == 0) {
#pragma unroll
        for (int w = 0; w < 8; ++w) Lwg = fmaf(__expf(mm[w] - Mwg), ll[w], Lwg);
        if (lane == 0) st16_sc1(wsr, G_ML + ((head * 2 + b) * 4 + part) * 16, __float_as_uint(Mwg), __float_as_uint(Lwg), tag);
      }
      const float fw = __expf(m - Mwg);
      m = Mwg;
#pragma unroll
      for (int bt = 0; bt < NB; ++bt) { pb[bt] = pack_bf(pr[bt] * fw, pr[bt] * fw); PIN(pb[bt]); }
#pragma unroll
      for (int k = 0; k < 4; ++k) { pm[k] = (c >> 1 & 3) == k ? ((c & 1) ? 0xffff0000u : 0x0000ffffu) : 0u; PIN(pm[k]); }
    }
    SB();
    return true;
  });
  asm volatile(".Ltail_start:");
  if (wave != 0) {
    pv_row(IC<SK0 - 1>{});
    WAIT_VM(WV); SB();
    sfor<NL - 1 - WV, NL - 1>(pv_row);
    *LDSP(f32x4, adst0) = acc;
    LDS_BARRIER();
  } else {
    u32x4 xr[8], am[8];
#pragma unroll
    for (int w = 0; w < 8; ++w) am[w] = u32x4{pl8[w] & pm[0], pl8[w] & pm[1], pl8[w] & pm[2], pl8[w] & pm[3]};
#pragma unroll
    for (int w = 0; w < 8; ++w) PIN(am[w]);
    {
      const u32 so = __builtin_amdgcn_readlane(sot, NL - 1);
      gate();
      SB();
#pragma unroll
      for (int w = 0; w < 8; ++w) xr[w] = ld16<AUX_NT>(rv, lane * 16, so ^ ((w & 1) ? col(P, 7) : 0u) ^ ((w & 2) ? col(P, 8) : 0u) ^ ((w & 4) ? col(P, 9) : 0u));
      SB();
    }
    u32 rsum = lds_addr(&sm.red[0][c * 8 + (g & 1) * 4]);
    pv_row(IC<SK0 - 1>{});
    WAIT_VM(WV + 8); SB();
    sfor<NL - 1 - WV, NL - 3>(pv_row);
    if (__builtin_expect(!all_ok((mlg[1] == tag) & (mlg[3] == tag)), 0)) {
#pragma nounroll
      for (int spin = 0; spin < SPIN_MAX; ++spin) {
        CFENCE();
        mlg = ld16<AUX_SC1>(wsr, mlo, 0);
        if (all_ok((mlg[1] == tag) & (mlg[3] == tag))) break;
      }
    }
    merge_factor(mlg);
    sfor<NL - 3, NL - 1>(pv_row);
    LDS_BARRIER();
    f32x4 t[8];
#pragma unroll
    for (int w = 1; w < 8; ++w) t[w] = LDSP(f32x4, rsum)[w * 32];
    acc = ((acc + t[1]) + (t[2] + t[3])) + ((t[4] + t[5]) + (t[6] + t[7]));
    PIN(acc);
    SB();
#pragma unroll
    for (int w = 0; w < 8; ++w) acc = mfma16(am[w], xr[w], acc);
  }
  WAIT_VM(0);
  acc *= fmrg;
  {
    const u32 w0 = min(pack_bf(acc[0], acc[1]), SENT - 1u), w1 = min(pack_bf(acc[2], acc[3]), SENT - 1u);
    const auto e0 = __builtin_amdgcn_permlane16_swap(w0, w0, false, false), e1 = __builtin_amdgcn_permlane16_swap(w1, w1, false, false);
    __builtin_amdgcn_raw_buffer_store_b128(u32x4{e0[0], e1[0], e0[1], e1[1]}, wsr, pso, 0, AUX_SC1);
  }
  st8_sc1(wsr, nto, (u32)wall_clock64() + nadd, tag);
  SB();

  if (part == 2) return;
  if (part == 0) {
    __builtin_amdgcn_s_barrier();
#pragma nounroll
    while (wleft > 0) { until(t1 - (P.PA + wleft * P.PD) * 256); piece(); }
    bool late = false;
    u32x4 og[1];
    f32x4 d[4];
    u32 six = (c & 8) ? 0xeeeeu : 0x4444u;
    PIN(six);
#pragma unroll
    for (int t = 0; t < 4; ++t) { d[t] = f32x4{0.f, 0.f, 0.f, 0.f}; PIN(d[t]); }
    if (wdef) {
      late = true;
      while ((int)(((u32)wall_clock64() - t0) << 8) < t1 - 10 * 256) if (__builtin_amdgcn_s_getreg(7 | (3 << 11)) == 0) { late = false; break; }
      if (__builtin_expect(late, 0)) { until(t1); CFENCE(); og[0] = ld16<AUX_SC1>(wsr, po, 0); SB(); WAIT_VM(1); } else { WAIT_VM(0); }
      CFENCE();
      wo_frags(1);
#pragma unroll
      for (int k = 0; k < 4; ++k) PIN(bw[k]);
      SB();
    }
    if (__builtin_expect(!late, 1)) { until(t1); CFENCE(); og[0] = ld16<AUX_SC1>(wsr, po, 0); }
    SB();
    auto wo_mm = [&]() {
#pragma unroll
      for (int t = 0; t < 4; ++t) d[t] = mfma64(og[0], bw[t], d[t], six);
    };
    auto p_bad = [&]() { return !all_ok(max(max(og[0][0], og[0][1]), max(og[0][2], og[0][3])) != SENT); };
    wo_mm();
    if (__builtin_expect(p_bad(), 0)) {
#pragma nounroll
      for (int spin = 0; spin < SPIN_MAX; ++spin) {
        __builtin_amdgcn_s_sleep(1);
        CFENCE();
        og[0] = ld16<AUX_SC1>(wsr, po, 0);
        if (!p_bad()) break;
      }
#pragma unroll
      for (int t = 0; t < 4; ++t) d[t] = f32x4{0.f, 0.f, 0.f, 0.f};
      wo_mm();
      SB();
      asm volatile(".Lend_d:");
    }
    {
      SB();
      f32x4 y; f32x2 h[4];
#pragma unroll
      for (int t = 0; t < 4; ++t) { h[t] = d[t].xy + d[t].zw; PIN(h[t]); }
#pragma unroll
      for (int t = 0; t < 4; ++t) { y[t] = h[t][0] + h[t][1]; PIN(y[t]); }
      *LDSP(f32x2, rdst) = f32x2{swap32_add(y[0], y[1]), swap32_add(y[2], y[3])};
    }
    LDS_BARRIER();
    if (wave < 2) {
      f32x2 v[4];
#pragma unroll
      for (int w = 0; w < 4; ++w) v[w] = f32x2{LDSP(float, rsrc)[w * 256], LDSP(float, rsrc)[w * 256 + 128]};
      const f32x2 h = (v[0] + v[1]) + (v[2] + v[3]);
      const float y = h[0] + h[1];
      __builtin_amdgcn_raw_buffer_store_b32(min(pack_bf(dpp<0xA0>(y), dpp<0xF5>(y)), SENT - 1u), wsr, xo, 0, AUX_SC1);
      asm volatile(".Lend_a:");
    }
    return;
  }
  if (part == 3) {
    if (!has_w) return;
    u32x4 b2[4];
    {
      __builtin_amdgcn_s_barrier();
      const rsrc_t r2 = mk_rsrc(w2 + (size_t)(blk * 32 + wave * 4) * 512, 4096u);
      const u32 l2 = (u32)(uintptr_t)(&sm.w2b[wave * 4][0]);
#pragma unroll
      for (int n = 0; n < 4; ++n) { until(t1 - (P.QA + (4 - n) * P.QD) * 256); dma16(r2, (lane ^ (n * 2) ^ ((wave & 2) << 2)) * 16, n * 1024, l2 + n * 1024); }
    }
    const u32 tdst = lds_addr(&sm.hb[hpos]);
    const u32 sdst = lds_addr(&sm.stat[wave][lane]);
    const u32 ssrc = lds_addr(&sm.stat[(g >> 1) * 4 + ((lane >> 3) & 3)][(lane & 7) * 4]);
    const u32 adst = lds_addr((c < 4 && (g & 1) == 0) ? &sm.actl[g >> 1][wave * 4 + c] : reinterpret_cast<u16*>(&sm.dummy[lane]));
    const u32 asrc = lds_addr(&sm.actl[lane & 1][g * 8]);
    const u32 apa = lds_addr(&sm.hb[(c >> 3) * 512 + (g >> 1) * 256 + (c >> 1 & 1) * 64 + ((c >> 2) & 1) * 32 + (g & 1) * 16 + (c & 1) * 8]);
    u32 six = (c & 1) ? 0xeeeeu : 0x4444u;
    f32x4 d[2] = {{0.f, 0.f, 0.f, 0.f}, {0.f, 0.f, 0.f, 0.f}};
    PIN(six); PIN(d[0]); PIN(d[1]);
    constexpr u32 xs2 = 16384;
    u32 xoff = dup(G_XP) + near + g * 2048 + ub * 1024 + (wave & 3) * 256 + c * 16;
    u32x4 one;
#pragma unroll
    for (int k = 0; k < 4; ++k) one[k] = ((c & 2) == 0 && (c >> 2) == k) ? ((c & 1) ? 0x3f800000u : 0x00003f80u) : 0u;
    PIN(one);
    u32 ooff = dup(G_OP + (blk * 512 + tid) * 8);
    float Afp = Af, Bfp = Bf;
    PIN(xoff); PIN(ooff); PIN(Afp); PIN(Bfp);
    f32x2 gc = ga * cc1, xc = xin * cc1;
    PIN(ga); PIN(xin); PIN(gc); PIN(xc);
    u32 w2a = (u32)(uintptr_t)(&sm.w2b[0][0]) + (g * 8 + ((lane >> 2) & 3)) * 1024 + (wave ^ (g & 1)) * 128 + (lane & 15) * 8;
    PIN(w2a);
    if (wave == 1) {
      due = t1 + P.N1 * 256;
      const int tn = notices();
      due = tn + P.X2 * 256;
      until(due);
    }
    asm volatile("s_waitcnt vmcnt(0)" ::: "memory");
    __builtin_amdgcn_s_barrier();
    CFENCE();
    u32x4 xg[2] = {ld16<AUX_SC1>(wsr, xoff, 0), ld16<AUX_SC1>(wsr, xoff, xs2)};
    SB();
#pragma unroll
    for (int t = 0; t < 4; ++t) {
      const u32x2 lo = tr_read(w2a ^ (t * 32)), hi = tr_read((w2a ^ (t * 32)) + 4096);
      b2[t] = u32x4{lo[0], lo[1], hi[0], hi[1]};
    }
#pragma unroll
    for (int t = 0; t < 4; ++t) PIN(b2[t]);
    SB();
    const f32x4 z4 = {0.f, 0.f, 0.f, 0.f};
    auto xp_bad = [&]() { return !all_ok(max(max(max(xg[0][0], xg[0][1]), max(xg[0][2], xg[0][3])), max(max(xg[1][0], xg[1][1]), max(xg[1][2], xg[1][3]))) != SENT); };
    f32x4 v = mfma16(one, xg[1], mfma16(one, xg[0], z4));
    if (__builtin_expect(xp_bad(), 0)) {
#pragma nounroll
      for (int spin = 0; spin < SPIN_MAX; ++spin) {
        __builtin_amdgcn_s_sleep(1);
        CFENCE();
        xg[0] = ld16<AUX_SC1>(wsr, xoff, 0); xg[1] = ld16<AUX_SC1>(wsr, xoff, xs2);
        if (!xp_bad()) break;
      }
      v = mfma16(one, xg[1], mfma16(one, xg[0], z4));
      SB();
      asm volatile(".Lend_e:");
    }
    {
      const f32x2 a = v.xy, hc = __builtin_elementwise_fma(gc, a, xc), x1 = __builtin_elementwise_fma(ga, a, xin);
      *LDSP(u32, tdst) = pack_bf(hc[0], hc[1]);
      *LDSP(float, sdst) = swap32_add(x1[0] + x1[1], fmaf(x1[0], x1[0], x1[1] * x1[1]));
    }
    LDS_BARRIER();
    {
      const f32x4 s4 = LDSP(f32x4, ssrc)[0], q4 = LDSP(f32x4, ssrc)[8];
      SB();
      const u32x4 af0 = LDSP(u32x4, apa)[0], af1 = LDSP(u32x4, apa)[16];
      SB();
      const f32x2 s2 = s4.xy + s4.zw, q2 = q4.xy + q4.zw;
      float s = s2[0] + s2[1], q = q2[0] + q2[1];
      SB();
      d[0] = mfma64(af0, bw[0], d[0], six);
      SB();
      s = swap16_add(s, q);
      SB();
      d[1] = mfma64(af0, bw[1], d[1], six);
      SB();
      s += dpp<XOR1>(s); s += dpp<XOR2>(s);
      SB();
      d[0] = mfma64(af1, bw[2], d[0], six);
      SB();
      s += dpp<XOR7>(s); s += dpp<XOR8>(s);
      SB();
      d[1] = mfma64(af1, bw[3], d[1], six);
      SB();
      const auto sq = __builtin_amdgcn_permlane16_swap(__float_as_uint(s), __float_as_uint(s), false, false);
      const float mean = u2f(sq[0]) * (1.f / 512.f);
      const float var = fmaf(u2f(sq[1]), 1.f / 512.f, -mean * mean);
      float ri = __builtin_amdgcn_rsqf(fmaxf(var, 0.f) + 1e-5f);
      PIN(ri); PIN(d[0]); PIN(d[1]);
      SB();
      float y0 = d[0][0] + d[0][1], y1 = d[1][2] + d[1][3], mr = -ri * mean;
      PIN(y0); PIN(y1); PIN(mr);
      float y = y0 + y1, cf = fmaf(mr, Afp, Bfp), rk = ri * -1.44269504f;
      PIN(y); PIN(cf); PIN(rk);
      y = __builtin_bit_cast(float, __builtin_amdgcn_update_dpp(__builtin_bit_cast(int, y), __builtin_bit_cast(int, y), XOR8, 0xa, 0xf, false));
      const float ck = cf * -1.44269504f;
      y = swap16_add(y, y);
      const float ex = __builtin_amdgcn_exp2f(fmaf(y, rk, ck));
      y = fmaf(y, ri, cf);
      const float gu = y * dpp<0x104>(y);
      *LDSP(u16, adst) = (u16)pack_bf(gu * __builtin_amdgcn_rcpf(1.f + ex), 0.f);
    }
    LDS_BARRIER();
    {
      const u32x4 af = *LDSP(u32x4, asrc);
      const f32x4 z = {0.f, 0.f, 0.f, 0.f};
      f32x4 d0 = mfma16(af, b2[0], z), d1 = mfma16(af, b2[1], z), d2 = mfma16(af, b2[2], z), d3 = mfma16(af, b2[3], z);
      asm volatile("" : "+v"(d0), "+v"(d1), "+v"(d2), "+v"(d3));
      const bool g1 = g & 1, g2 = g & 2;
      const float e0 = g1 ? d1[0] : d0[0], e1 = g1 ? d1[1] : d0[1], f0 = g1 ? d3[0] : d2[0], f1 = g1 ? d3[1] : d2[1];
      const u32x2 ov = {pack_bf(g2 ? f0 : e0, g2 ? f1 : e1), tag};
      __builtin_amdgcn_raw_buffer_store_b64(ov, wsr, ooff, near ^ 8192u, AUX_SC1);
      __builtin_amdgcn_raw_buffer_store_b64(ov, wsr, ooff, near, AUX_SC1);
    }
    asm volatile(".Lend_b:");
    return;
  }
  if (wave == 0) {
    if (!all_ok((sg[0][1] == tag) & (sg[0][3] == tag) & (sg[1][1] == tag) & (sg[1][3] == tag))) {
      u32x4 t[2];
      gather16<2>(wsr, G_MOD + (1024 + fd) * 8, 1536 * 8, tag, t);
      sg[0] = t[0]; sg[1] = t[1];
    }
    u32 xa;
    {
      due = t1 + P.N2 * 256;
      const int tn = notices();
      due = tn + P.X3 * 256;
      until(due);
    }
    const bool hb = g & 1;
#pragma nounroll
    for (int spin = 0; spin < SPIN_MAX; ++spin) {
      CFENCE();
      xa = __builtin_amdgcn_raw_buffer_load_b32(wsr, dup(G_XP) + near + (g >> 1) * 16384 + (c >> 2) * 2048 + (g & 1) * 1024 + fd * 2, 0, AUX_SC1);
      if (all_ok(xa != SENT)) break;
      __builtin_amdgcn_s_sleep(1);
    }
    float a0 = lo_f(xa), a1 = hi_f(xa);
    a0 += dpp<0x124>(a0); a1 += dpp<0x124>(a1);
    a0 += dpp<XOR8>(a0); a1 += dpp<XOR8>(a1);
    a0 = swap32_add(a0, a0); a1 = swap32_add(a1, a1);
    f32x2 xv = {rbf(lo_f(sx2) + rbf((hb ? hi_f(sg[0][0]) : lo_f(sg[0][0])) * rbf(a0))), rbf(hi_f(sx2) + rbf((hb ? hi_f(sg[0][2]) : lo_f(sg[0][2])) * rbf(a1)))};
    f32x2 gv = {hb ? hi_f(sg[1][0]) : lo_f(sg[1][0]), hb ? hi_f(sg[1][2]) : lo_f(sg[1][2])};
    PIN(xv); PIN(gv);
    if (wi == 0 && lane == 0) __builtin_amdgcn_raw_buffer_store_b32(tag, wsr, WS_EPOCH, 0, AUX_SC1);
    due = max(due + P.X4 * 256, (int)(((u32)wall_clock64() - t0) << 8) + P.GX * 256);
    const bool last = lane < (NUP - 32) * 4;
    u32 o0 = dup(G_OP + (lane >> 2) * 4096 + fd * 8) + near, o2 = last ? o0 : OOB, tg2 = last ? tag : 0u;
    const rsrc_t rout = mk_rsrc(out, 2048u);
    u32 oo = (lane & 44) == 0 ? (g * 512 + fd) * 2 : OOB;
    const u32 pk = (c & 10) ? 0u : ((c & 4) ? 0x3f800000u : 0x00003f80u);
    u32x4 one = {(c & 1) ? 0u : pk, 0u, (c & 1) ? pk : 0u, 0u};
    PIN(o0); PIN(o2); PIN(tg2); PIN(oo); PIN(one);
    __builtin_amdgcn_raw_buffer_store_b32(pack_bf(xv[0], xv[1]), rout, oo, 0, AUX_SC1);
    u32 s1_ = 131072, s2_ = 262144;
    asm volatile("" : "+s"(s1_), "+s"(s2_));
    until(due);
    f32x4 sv;
#pragma nounroll
    for (int spin = 0; spin < SPIN_MAX; ++spin) {
      CFENCE();
      const u32x4 og[3] = {ld16<AUX_SC1>(wsr, o0, 0), ld16<AUX_SC1>(wsr, o0, s1_), ld16<AUX_SC1>(wsr, o2, s2_)};
      sv = f32x4{0.f, 0.f, 0.f, 0.f};
#pragma unroll
      for (int k = 0; k < 3; ++k) sv = mfma16(one, og[k], sv);
      SB();
      u32 bad = ((og[0][1] ^ tag) | (og[0][3] ^ tag) | (og[1][1] ^ tag)) | ((og[1][3] ^ tag) | (og[2][1] ^ tg2) | (og[2][3] ^ tg2));
      PIN(bad);
      SB();
      float s0 = sv[0] + dpp<0x124>(sv[0]), s1 = sv[1] + dpp<0x124>(sv[1]);
      PIN(s0); PIN(s1);
      s0 += dpp<XOR8>(s0); s1 += dpp<XOR8>(s1);
      PIN(s0); PIN(s1);
      __builtin_amdgcn_raw_buffer_store_b32(pack_bf(fmaf(gv[0], s0, xv[0]), fmaf(gv[1], s1, xv[1])), rout, oo, 0, AUX_SC1);
      SB();
      if (all_ok(bad == 0u)) break;
      __builtin_amdgcn_s_sleep(1);
    }
    asm volatile(".Lend_c:");
  }
}

extern "C" void kernel_launch(void* const* d_in, const int* in_sizes, int n_in,
                              void* d_out, int out_size, void* d_ws, size_t ws_size,
                              hipStream_t stream) {
  fused_kernel<<<256, 512, 0, stream>>>((const u16*)d_in[1], (const u16*)d_in[2], (const u16*)d_in[4], (const u16*)d_in[3], (const u16*)d_in[10], (const u16*)d_in[6], (const u16*)d_in[7],
                                        (unsigned char*)d_ws, (const u16*)d_in[0], (const u16*)d_in[9], (const u16*)d_in[8], (u16*)d_out);
}
```

```cpp
#define PROBE_MUL 10
#include <hip/hip_runtime.h>
#include <stdint.h>

typedef unsigned int u32;
typedef unsigned short u16;
typedef unsigned long long u64;
typedef __attribute__((ext_vector_type(4))) unsigned u32x4;
typedef __attribute__((ext_vector_type(2))) unsigned u32x2;
typedef __attribute__((ext_vector_type(2))) __bf16 bf16x2;
typedef __attribute__((ext_vector_type(4))) float f32x4;
typedef __attribute__((ext_vector_type(2))) float f32x2;
typedef __amdgpu_buffer_rsrc_t rsrc_t;

constexpr int NH = 32, SK = 16384, DH = 128, WIN = 4096, FF = 1376;
struct Prm { int E0, ES, E1, E2, E2C, EU, TEND, DT_UP, L0, L1, X1, X2, X3, X4, NE, FE, M0, M1, M2, M3, M4, GS, GX, N1, N2, GA, WD, DT_WO, LW, LU, EW, PM, NF, NR, C0, A0, AG, AC, TS, TW, TK, TG, GB, B0, BG, BF, D0, DG, TC, LM, C1, F0, FG, FF_, PA, PD, QA, QD, RT, W[9]; };
constexpr Prm PRM0 = {32, 20, 32, 32, 60, 60, 1968, 76, 12, 27, 75, 63, 100, 75, 3, 8, 0x2511f55b, 0x2b7425d5, 0x6017665, 0x17e0b9ec, 0x31944305, 62, 30, 29, 62, 50, 4, 106, 50, 28, 10, 0x0, 12, 14, 3, 36, 4, 4, 509, 215, 80, -2, 6, 69, 10, 8, 8, 6, 87, 120, 2, 65, 6, 4, 52, 14, 9, 18, 12, 0x77777777, 0x77777777, 0x77777777, 0x77777777, 0x77777777, 0x44444477, 0x44444444, 0x44444444, 0x44444444};
constexpr int NL = 37;
constexpr int SK0 = 21, SK1 = 32;
constexpr int NCMIN = (NL - SK1 + SK0) * 8;
constexpr int WK = 8, WV = 5;
constexpr int WUSE = 12, LNS = 7;
constexpr int QP = 14, QD = 4;
constexpr int VQ = 6;
constexpr int NSL = (NL + 7) / 8;
constexpr int VS = VQ + NSL;
constexpr int MLS = VS + 2;
constexpr int VSIDE = MLS - 5, UPREP = MLS + 6;
constexpr int CPF = 17;
constexpr int SMO = 1;
constexpr int RVS = 5, RVP = 8, RVL = 14, RVU = 18;
constexpr int RPS = 23, RPP = 27;
constexpr int PRB = 12;
constexpr int TBS = 7, TBP = 10, TBL = 13, TBU = 17;
constexpr int window(int s, int w) {
  int n = w;
  for (int u = s - w + 1; u <= s; ++u) n += u == SMO ? 6 : (u == TBL || u == SK1 || u == NL + CPF || u == NL + RVL || u == NL + SK1 ? 1 : (u == QP || u == NL + VQ - 4 ? 2 : (u == NL + VSIDE ? 4 : 0)));
  return n;
}
constexpr int cons_end(int v) {
  const int a = v - WV + 1, c = (v - MLS) * 4;
  const int m = a < c ? a : c;
  return m < 0 ? 0 : (m > NL ? NL : m);
}
constexpr u32 col(const Prm& P, int j) { const int m[6] = {P.M0, P.M1, P.M2, P.M3, P.M4, P.PM}; return (u32)(m[j / 3] >> (j % 3 * 10) & 1023) << 10; }
constexpr u32 first_col(const Prm& P, int k) {
  if (k < 4) return col(P, 12 + k) | 1u << (24 + k);
  if (k < 6) return col(P, k - 4);
  if (k < 8) return col(P, 4 + k) | 1u << (16 + k);
  if (k < 11) return col(P, k - 1);
  return (col(P, 2) ^ col(P, 3) ^ col(P, 4) ^ col(P, 5) ^ col(P, 6) ^ col(P, 10) ^ col(P, 11)) | 0xf00000u;
}
constexpr bool one_to_one(const Prm& P) {
  u32 c[10] = {};
  for (int j = 0; j < 10; ++j) c[j] = col(P, j);
  for (int j = 0; j < 10; ++j) {
    if (c[j] == 0) return false;
    const u32 low = c[j] & (0u - c[j]);
    for (int k = j + 1; k < 10; ++k) if (c[k] & low) c[k] ^= c[j];
  }
  return true;
}
static_assert(one_to_one(PRM0), "every KB of the window exactly once");
struct Tab { u32 e[512]; };
constexpr Tab make_tab(const Prm& P) {
  Tab T{};
  for (int bid = 0; bid < 256; ++bid) {
    const int xcd = bid & 7, part = bid >> 4 & 3, i8 = (bid >> 3 & 1) | bid >> 6 << 1, wi = i8 * 8 + xcd;
    const bool has_mod = wi < 48, has_up = wi >= 21;
    const int cb0 = (i8 + P.C0) & 7, cb1 = (i8 + P.C1) & 7, ms = wi - 20;
    const int mi = wi < 8 ? wi : (wi < 16 ? 8 + ((xcd + 4) & 7) : (wi < 20 ? (wi - 14) * 8 + 3 : 16 + ms / 7 * 8 + ms % 7 + (ms % 7 >= 3 ? 1 : 0)));
    const int ui = xcd * 5 + (xcd > 5 ? xcd - 5 : 0) + (i8 == 6 ? 0 : (xcd >= 5 ? 1 : 0) + (i8 == 7 ? 4 : i8 - 2));
    const bool edge = i8 >= 6;
    const int wd = cb0 == 3 ? 0 : P.WD;
    const int e[4] = {P.E0 - 4 * wd + (cb0 == 3 ? P.ES : 0), P.E1 + (cb1 == 3 ? P.ES : 0), has_mod ? (wi < 16 ? P.E2C : P.E2) + ((mi & 7) == 3 ? P.ES : 0) : 0, has_up ? P.EU : 0};
    int tw[4] = {}, win[4] = {}, span[4] = {}, k0[4] = {};
    for (int k = 0; k < 4; ++k) {
      const int np = k == 0 ? 8 - wd : 8;
      tw[k] = np ? e[k] * P.TW / (16 * np) : 0;
      k0[k] = P.NF + tw[k] * np / 2 > P.TK ? P.NF + tw[k] * np / 2 : P.TK;
      span[k] = P.TEND - k0[k] - P.TG - (k == 0 ? (wd ? P.DT_WO : P.A0) : (k == 3 && has_up ? P.DT_UP : 0));
      win[k] = span[k] - (k == 3 && has_up ? P.GA + (edge ? P.LM : 0) : 0);
    }
    int n[4] = {NCMIN / 8, NCMIN / 8, NCMIN / 8, NCMIN / 8};
    for (int left = 128 - NCMIN / 2; left > 0; --left) {
      int best = -1;
      for (int k = 3; k >= 0; --k)
        if (n[k] < NL && (best < 0 || n[k] * win[best] < n[best] * win[k])) best = k;
      ++n[best];
    }
    const int c[4] = {0, n[0] - 1, n[0] + n[1] - 2, n[0] + n[1] + n[2] - 3};
    const int nl = n[part];
    const bool has_w = part < 2 || (part == 2 ? has_mod : has_up);
    const int blk = !has_w ? 0 : (part == 0 ? cb0 : (part == 1 ? xcd * 8 + cb1 : (part == 2 ? mi : ui)));
    T.e[2 * bid] = (u32)(c[part] | nl << 7 | blk << 13 | (has_w ? 1 : 0) << 19 | (part == 0 ? wd : 0) << 20) | (u32)tw[part] << 24;
    T.e[2 * bid + 1] = (u32)(span[part] * P.TS / (4 * (nl - 1)) | span[part] * (1024 - P.TS) / (4 * (nl - 1)) << 16);
  }
  return T;
}
constexpr int step_range(const Prm& P, bool mx_) {
  int mx = 0, mn = 1 << 20;
  const Tab T = make_tab(P);
  for (int bid = 0; bid < 256; ++bid) { const int n = T.e[2 * bid] >> 7 & 63; mx = n > mx ? n : mx; mn = n < mn ? n : mn; }
  return mx_ ? mx : mn;
}
extern "C" __attribute__((used, visibility("protected"))) __constant__ const Tab wg_tab = make_tab(PRM0);
static_assert(step_range(PRM0, true) <= NL && step_range(PRM0, false) >= NCMIN / 8, "a wave's key range does not fit its load steps");
static_assert(RVU < UPREP && UPREP + 1 < SK0 && QP + QD + 1 < SK0 && cons_end(SK0 - 1) == SK0 - WV && NL - SK1 == WV, "steps every wave runs");
constexpr bool timers_long(const Prm& P) {
  const Tab T = make_tab(P);
  for (int bid = 48; bid < 64; ++bid) if (SK0 + NL - (int)(T.e[2 * bid] >> 7 & 63) > RPS) return false;
  return true;
}
static_assert(timers_long(PRM0), "steps of the second report");
constexpr int NUP = FF / 32;
constexpr int SPIN_MAX = 1 << 17;

constexpr u32 WS_EPOCH = 0;
constexpr u32 G_TB = 1024;
constexpr u32 G_MOD = 4096;
constexpr u32 G_Q = G_MOD + 3072 * 8;
constexpr u32 G_ML = G_Q + 4096 * 8;
constexpr u32 G_U = G_ML + 256 * 16;
constexpr u32 G_P = 0;
constexpr u32 G_XP = G_P + 128 * 256;
constexpr u32 G_OP = G_XP + 8 * 512 * 4;
constexpr u32 G_NT = G_OP + 43 * 512 * 8;
constexpr u32 G_PB = G_OP + 44 * 512 * 8;
constexpr u32 G_VT = G_U + 2 * (G_PB + 8192);
constexpr u32 WS_END = G_VT + 2048;
__device__ __forceinline__ u32 dup(u32 o) { return G_U + ((o >> 13) << 14) + (o & 8191u); }
constexpr u32 SENT = 0xffffffffu;
constexpr u32 OOB = 0x40000000u;
#define WST 0

constexpr int AUX_NT = 2, AUX_SC1 = 16;

__device__ __forceinline__ float lo_f(u32 v) { return __uint_as_float(v << 16); }
__device__ __forceinline__ float hi_f(u32 v) { return __uint_as_float(v & 0xffff0000u); }
__device__ __forceinline__ float rbf(float x) { return (float)(__bf16)x; }
__device__ __forceinline__ u32 pack_bf(float lo, float hi) {
  bf16x2 r; r[0] = (__bf16)lo; r[1] = (__bf16)hi; return __builtin_bit_cast(u32, r);
}
__device__ __forceinline__ float dot2(u32 k, u32 q, float acc) {
  return __builtin_amdgcn_fdot2_f32_bf16(__builtin_bit_cast(bf16x2, k), __builtin_bit_cast(bf16x2, q), acc, false);
}
template <int CTRL> __device__ __forceinline__ float dpp(float x) {
  return __builtin_bit_cast(float, __builtin_amdgcn_mov_dpp(__builtin_bit_cast(int, x), CTRL, 0xf, 0xf, true));
}
constexpr int XOR1 = 0xB1, XOR2 = 0x4E, XOR7 = 0x141, XOR8 = 0x128;
__device__ __forceinline__ float swap32_add(float a, float b) {
  auto t = __builtin_amdgcn_permlane32_swap(__float_as_uint(a), __float_as_uint(b), false, false);
  return __uint_as_float(t[0]) + __uint_as_float(t[1]);
}
__device__ __forceinline__ float swap16_add(float a, float b) {
  auto t = __builtin_amdgcn_permlane16_swap(__float_as_uint(a), __float_as_uint(b), false, false);
  return __uint_as_float(t[0]) + __uint_as_float(t[1]);
}
__device__ __forceinline__ float wave_sum(float x) {
  x += dpp<XOR1>(x); x += dpp<XOR2>(x); x += dpp<XOR7>(x); x += dpp<XOR8>(x);
  x = swap16_add(x, x);
  return swap32_add(x, x);
}
__device__ __forceinline__ float wave_max(float x) {
  x = fmaxf(x, dpp<XOR1>(x)); x = fmaxf(x, dpp<XOR2>(x)); x = fmaxf(x, dpp<XOR7>(x)); x = fmaxf(x, dpp<XOR8>(x));
  auto s = __builtin_amdgcn_permlane16_swap(__float_as_uint(x), __float_as_uint(x), false, false);
  x = fmaxf(__uint_as_float(s[0]), __uint_as_float(s[1]));
  auto t = __builtin_amdgcn_permlane32_swap(__float_as_uint(x), __float_as_uint(x), false, false);
  return fmaxf(__uint_as_float(t[0]), __uint_as_float(t[1]));
}
template <int CTRL> __device__ __forceinline__ float sel_add(float lo, float hi, bool up) {
  const float t1 = lo + dpp<CTRL>(lo), t2 = hi + dpp<CTRL>(hi);
  return up ? t2 : t1;
}
template <int I> __device__ __forceinline__ float bc16(float x) {
  return __builtin_bit_cast(float, __builtin_amdgcn_update_dpp(0, __builtin_bit_cast(int, x), 0x150 + I, 0xf, 0xf, false));
}
__device__ __forceinline__ float bcast16(float x, int i) {
  switch (i) {
    case 0: return bc16<0>(x); case 1: return bc16<1>(x); case 2: return bc16<2>(x); case 3: return bc16<3>(x);
    case 4: return bc16<4>(x); case 5: return bc16<5>(x); case 6: return bc16<6>(x); case 7: return bc16<7>(x);
    case 8: return bc16<8>(x); case 9: return bc16<9>(x); case 10: return bc16<10>(x); case 11: return bc16<11>(x);
    case 12: return bc16<12>(x); case 13: return bc16<13>(x); case 14: return bc16<14>(x); default: return bc16<15>(x);
  }
}
__device__ __forceinline__ float silu_f(float x) { return x * __builtin_amdgcn_rcpf(1.f + __expf(-x)); }

template <int AUX> __device__ __forceinline__ u32x4 ld16(rsrc_t r, u32 voff, u32 soff) {
  return __builtin_amdgcn_raw_buffer_load_b128(r, voff, soff, AUX);
}
template <int AUX> __device__ __forceinline__ u32x2 ld8(rsrc_t r, u32 voff, u32 soff) {
  return __builtin_amdgcn_raw_buffer_load_b64(r, voff, soff, AUX);
}
__device__ __forceinline__ void st8_sc1(rsrc_t r, u32 voff, u32 v, u32 tag) {
  __builtin_amdgcn_raw_buffer_store_b64(u32x2{v, tag}, r, voff, 0, AUX_SC1);
}
__device__ __forceinline__ void st16_sc1(rsrc_t r, u32 voff, u32 v0, u32 v1, u32 tag) {
  __builtin_amdgcn_raw_buffer_store_b128(u32x4{v0, tag, v1, tag}, r, voff, 0, AUX_SC1);
}
__device__ __forceinline__ rsrc_t mk_rsrc(const void* p, u32 bytes) {
  return __builtin_amdgcn_make_buffer_rsrc(const_cast<void*>(p), 0, bytes, 0x00020000);
}
#define WAIT_VM(n) __builtin_amdgcn_s_waitcnt(((n) & 15) | (((n) >> 4) << 14) | (7 << 4) | (15 << 8))
#define SB() __builtin_amdgcn_sched_barrier(0)
#define CFENCE() asm volatile("" ::: "memory")
__device__ __forceinline__ bool all_ok(bool ok) { return __builtin_amdgcn_ballot_w64(!ok) == 0ull; }

template <int N> __device__ __forceinline__ void gather16(rsrc_t ws, u32 voff, u32 stride, u32 tag, u32x4 (&g)[N]) {
#pragma nounroll
  for (int spin = 0; spin < SPIN_MAX; ++spin) {
    CFENCE();
    bool ok = true;
#pragma unroll
    for (int i = 0; i < N; ++i) g[i] = ld16<AUX_SC1>(ws, voff + i * stride, 0);
#pragma unroll
    for (int i = 0; i < N; ++i) ok &= (g[i][1] == tag) & (g[i][3] == tag);
    if (all_ok(ok)) break;
    __builtin_amdgcn_s_sleep(8);
  }
}
template <int N> __device__ __forceinline__ void gather8(rsrc_t ws, u32 voff, u32 stride, u32 tag, u32 (&g)[N], bool act = true) {
#pragma nounroll
  for (int spin = 0; spin < SPIN_MAX; ++spin) {
    CFENCE();
    bool ok = true;
    u32x2 t[N];
#pragma unroll
    for (int i = 0; i < N; ++i) t[i] = ld8<AUX_SC1>(ws, voff + i * stride, 0);
#pragma unroll
    for (int i = 0; i < N; ++i) { ok &= (t[i][1] == tag); g[i] = t[i][0]; }
    if (all_ok(ok | !act)) break;
    __builtin_amdgcn_s_sleep(8);
  }
}

struct Smem {
  float red[8][128];
  float ml[8][2];
  float r2[64 * 20];
  u16 hb[3 * 1024];
  float stat[8][64];
  float red2[8][128];
  u32 dummy[64];
  u32x2 bc, bc2;
  u32 bc3, bc4;
  u32x4 zero[4];
  float pl[4][8];
  u32 mflag[8];
  u32x4 wbuf[8][8][64];
  __attribute__((aligned(256))) u16 w2b[32][512];
  u16 actl[2][32];
};

#define PROBE_K 8
#define PROBE_OP 0
#define PROBE_PART -1
#define STAMP(k) do { if ((k) == PROBE_K && tid == 0) pst = wall_clock64(); } while (0)
#define WSTAMP(k) do {} while (0)
#define FINISH() do { STAMP(8); if (tid == 0) { __hip_atomic_store(reinterpret_cast<u64*>(ws + (1u << 20)) + bid * 2, pt0, __ATOMIC_RELAXED, __HIP_MEMORY_SCOPE_AGENT); \
   __hip_atomic_store(reinterpret_cast<u64*>(ws + (1u << 20)) + bid * 2 + 1, (PROBE_PART < 0 || part == PROBE_PART) ? pst : 0ull, __ATOMIC_RELAXED, __HIP_MEMORY_SCOPE_AGENT); } \
   if (bid == 8 && wave == 0) probe_end(ws, pt0, lane); return; } while (0)
#define PIN(x) asm volatile("" : "+v"(x))
#define LDS_BARRIER() do { __builtin_amdgcn_s_waitcnt(0xc07f); __builtin_amdgcn_s_barrier(); CFENCE(); } while (0)
#define WAVE_SYNC() do { __builtin_amdgcn_fence(__ATOMIC_RELEASE, "wavefront"); __builtin_amdgcn_wave_barrier(); __builtin_amdgcn_fence(__ATOMIC_ACQUIRE, "wavefront"); } while (0)

__device__ __forceinline__ void dma16(rsrc_t r, u32 voff, u32 soff, u32 lds_dst) {
  u32 keep;
  asm volatile("s_mov_b32 %0, m0\n\ts_mov_b32 m0, %4\n\ts_nop 4\n\tbuffer_load_dwordx4 %1, %2, %3 offen nt lds\n\ts_mov_b32 m0, %0"
               : "=&s"(keep) : "v"(voff), "s"(r), "s"(soff), "s"(lds_dst) : "memory");
}
__device__ __forceinline__ void ld16_into(u32x4& d, rsrc_t r, u32 voff, u32 soff) {
  asm volatile("s_nop 4\n\tbuffer_load_dwordx4 %0, %1, %2, %3 offen nt" : "+v"(d) : "v"(voff), "s"(r), "s"(soff) : "memory");
}
__device__ __forceinline__ u32 perm_lo(u32 a, u32 b) { return __builtin_amdgcn_perm(b, a, 0x05040100u); }
__device__ __forceinline__ u32 perm_hi(u32 a, u32 b) { return __builtin_amdgcn_perm(b, a, 0x07060302u); }
__device__ __forceinline__ float rdl(float x, int l) { return __int_as_float(__builtin_amdgcn_readlane(__float_as_int(x), l)); }

typedef __attribute__((ext_vector_type(8))) __bf16 bf16x8;
__device__ __forceinline__ f32x4 mfma16(u32x4 a, u32x4 b, f32x4 c) {
  return __builtin_amdgcn_mfma_f32_16x16x32_bf16(__builtin_bit_cast(bf16x8, a), __builtin_bit_cast(bf16x8, b), c, 0, 0, 0);
}
typedef __attribute__((ext_vector_type(8))) unsigned u32x8;
typedef __attribute__((ext_vector_type(16))) __bf16 bf16x16;
__device__ __forceinline__ f32x4 mfma64(u32x4 a, u32x8 b, f32x4 c, u32 idx) {
  return __builtin_amdgcn_smfmac_f32_16x16x64_bf16(__builtin_bit_cast(bf16x8, a), __builtin_bit_cast(bf16x16, b), c, idx, 0, 0);
}
__device__ __forceinline__ float sum8(const float* p) {
  const f32x4 a = *reinterpret_cast<const f32x4*>(p), b = *reinterpret_cast<const f32x4*>(p + 4);
  return ((a[0] + b[0]) + (a[1] + b[1])) + ((a[2] + b[2]) + (a[3] + b[3]));
}
__device__ __forceinline__ float ln_ri(float var) {
  return __builtin_amdgcn_rcpf(rbf(__builtin_amdgcn_sqrtf(rbf(rbf(fmaxf(var, 0.f)) + 1e-5f))));
}
__device__ __forceinline__ void ln_stats2(const float (&x0)[8], const float (&x1)[8], float& mb0, float& ri0, float& mb1, float& ri1) {
  const float k0 = rdl(x0[0], 0), k1 = rdl(x1[0], 0);
  float s0 = 0.f, s1 = 0.f, q0 = 0.f, q1 = 0.f;
#pragma unroll
  for (int k = 0; k < 8; ++k) {
    const float d0 = x0[k] - k0, d1 = x1[k] - k1;
    s0 += d0; s1 += d1; q0 = fmaf(d0, d0, q0); q1 = fmaf(d1, d1, q1);
  }
  float t = swap16_add(swap32_add(s0, s1), swap32_add(q0, q1));
  t += dpp<XOR1>(t); t += dpp<XOR2>(t); t += dpp<XOR7>(t); t += dpp<XOR8>(t);
  auto u = __builtin_amdgcn_permlane16_swap(__float_as_uint(t), __float_as_uint(t), false, false);
  const float ms = __uint_as_float(u[0]) * (1.f / 512.f), mq = __uint_as_float(u[1]) * (1.f / 512.f);
  const float ri = ln_ri(fmaf(-ms, ms, mq));
  mb0 = rbf(k0 + rdl(ms, 0)); mb1 = rbf(k1 + rdl(ms, 32));
  ri0 = rdl(ri, 0); ri1 = rdl(ri, 32);
}
template <int I> struct IC { static constexpr int v = I; };
template <int I, int N, class F> __device__ __forceinline__ void sfor(F&& f) {
  if constexpr (I < N) { if (f(IC<I>{})) sfor<I + 1, N>(f); }
}
typedef __attribute__((ext_vector_type(4))) short s16x4;
typedef __attribute__((address_space(3))) s16x4* lds_s16x4p;
__device__ __forceinline__ u32x2 tr_read(u32 lds_addr) {
  return __builtin_bit_cast(u32x2, __builtin_amdgcn_ds_read_tr16_b64_v4i16((lds_s16x4p)lds_addr));
}
__device__ __forceinline__ u32 next_tag(u32 e) { return ((e | 0x40004000u) + 1u) & ~0x40004000u; }
__device__ __forceinline__ float u2f(u32 v) { return __uint_as_float(v); }
#define LDSP(T, a) ((__attribute__((address_space(3))) T*)(a))
template <class T> __device__ __forceinline__ u32 lds_addr(T* p) { u32 a = (u32)(uintptr_t)p; PIN(a); return a; }

#define TAB_LOAD "s_add_u32 s30, s28, wg_tab@rel32@lo+4\ns_addc_u32 s31, s29, wg_tab@rel32@hi+12\nglobal_load_dwordx2 %[te], %[va], s[30:31]\n"
#define FC(k) ".Lfc" #k
#define FC_REGS
#define FX(r, bit, k) "s_bitcmp1_b32 %[" #r "], " #bit "\ns_cselect_b32 %[sb], " FC(k) ", 0\ns_xor_b32 %[sa], %[sa], %[sb]\n"
__device__ __forceinline__ void probe_end(unsigned char* ws, u64 pt0, int lane) {
  while (wall_clock64() < pt0 + 5500ull) __builtin_amdgcn_s_sleep(8);
  u64 a[4], c2[4];
  for (int i = 0; i < 4; ++i) {
    a[i] = __hip_atomic_load(reinterpret_cast<u64*>(ws + (1u << 20)) + (i * 64 + lane) * 2, __ATOMIC_RELAXED, __HIP_MEMORY_SCOPE_AGENT);
    c2[i] = __hip_atomic_load(reinterpret_cast<u64*>(ws + (1u << 20)) + (i * 64 + lane) * 2 + 1, __ATOMIC_RELAXED, __HIP_MEMORY_SCOPE_AGENT);
  }
  int mn0 = 1 << 30, mx1 = -(1 << 30), mn1 = 1 << 30, sum = 0, cnt = 0;
  for (int i = 0; i < 4; ++i) {
    const int d0 = (int)(a[i] - pt0);
    mn0 = min(mn0, d0);
    if (c2[i] != 0ull) { const int d1 = (int)(c2[i] - pt0); mx1 = max(mx1, d1); mn1 = min(mn1, d1); sum += d1; cnt += 1; }
  }
  for (int o = 1; o < 64; o <<= 1) { mn0 = min(mn0, __shfl_xor(mn0, o)); mx1 = max(mx1, __shfl_xor(mx1, o)); mn1 = min(mn1, __shfl_xor(mn1, o)); sum += __shfl_xor(sum, o); cnt += __shfl_xor(cnt, o); }
  int X = PROBE_OP == 0 ? mx1 : (PROBE_OP == 1 ? sum / max(cnt, 1) : mn1);
  if (PROBE_K == 102) X = mn0;
  X = mn0 + (X - mn0) * PROBE_MUL;
  const u64 tg = pt0 + (u64)(long long)(X + 8000);
  for (int i = 0; i < 400000 && wall_clock64() < tg; ++i) __builtin_amdgcn_s_sleep(1);
}
__global__ void __launch_bounds__(512, 2) fused_kernel(const u16* kc_, const u16* vc_, const u16* wo_, const u16* wq_, const u16* wmod_, const u16* w1_, const u16* w3_,
                                                       unsigned char* ws_, const u16* x_, const u16* cnd_, const u16* w2_, u16* out_) {
  __shared__ __attribute__((aligned(256))) Smem sm;
  const u64 a_ka = (u64)__builtin_amdgcn_kernarg_segment_ptr();
  const int bid = blockIdx.x, tid = threadIdx.x;
  u64 wa, t64; u32x2 te; int tid0;
  u32 ar[14];
  u32 tch2, sa_, sb_, va_;
  u32x4 kr[NL], vr[NL];
  asm volatile(".set .Lfc0, %0\n.set .Lfc1, %1\n.set .Lfc2, %2\n.set .Lfc3, %3\n.set .Lfc4, %4\n.set .Lfc5, %5\n.set .Lfc6, %6\n.set .Lfc7, %7\n.set .Lfc8, %8\n.set .Lfc9, %9\n.set .Lfc10, %10\n.set .Lfc11, %11"
    :: "n"(first_col(PRM0, 0)), "n"(first_col(PRM0, 1)), "n"(first_col(PRM0, 2)), "n"(first_col(PRM0, 3)), "n"(first_col(PRM0, 4)), "n"(first_col(PRM0, 5)),
       "n"(first_col(PRM0, 6)), "n"(first_col(PRM0, 7)), "n"(first_col(PRM0, 8)), "n"(first_col(PRM0, 9)), "n"(first_col(PRM0, 10)), "n"(first_col(PRM0, 11)));
  asm volatile(
    "s_load_dwordx16 s[4:19], %[ka], 0x0\ns_load_dwordx8 s[20:27], %[ka], 0x40\n"
    "s_memrealtime %[t]\n"
    "v_lshlrev_b32 %[va], 3, %[bid]\n"
    "s_getpc_b64 s[28:29]\n.Lcpf_first:\n"
    TAB_LOAD
    "s_cmp_gt_u32 %[bid], 15\ns_cbranch_scc1 .Lno_cpf\n"
    "s_mov_b32 %[sa], .Lend_a-.Lcpf_first\ns_max_i32 %[sa], %[sa], .Lend_b-.Lcpf_first\ns_max_i32 %[sa], %[sa], .Lend_c-.Lcpf_first\ns_max_i32 %[sa], %[sa], .Lend_d-.Lcpf_first\ns_max_i32 %[sa], %[sa], .Lend_e-.Lcpf_first\n"
    "v_lshlrev_b32 %[va], 6, %[tid]\nv_min_u32 %[va], %[sa], %[va]\nglobal_load_dword %[pf], %[va], s[28:29]\n"
    ".Lno_cpf:\n"
    "v_readfirstlane_b32 %[t0], %[tid]\nv_and_b32 %[va], 63, %[tid]\n"
    "s_mov_b32 %[sa], " FC(11) "\n"
    FX(bid, 0, 0) FX(bid, 1, 1) FX(bid, 2, 2) FX(bid, 3, 3) FX(bid, 4, 4) FX(bid, 5, 5) FX(bid, 6, 6) FX(bid, 7, 7) FX(t0, 6, 8) FX(t0, 7, 9) FX(t0, 8, 10)
    "v_lshl_add_u32 %[va], %[va], 4, %[sa]\n"
    "s_waitcnt lgkmcnt(0)\nglobal_load_dwordx4 %[k0], %[va], s[4:5] nt\nglobal_load_dwordx4 %[v0], %[va], s[6:7] nt\n"
    "s_bitcmp1_b32 %[t0], 6\ns_cselect_b64 s[14:15], s[16:17], s[14:15]\n"
    "s_bitcmp1_b32 %[bid], 4\ns_cselect_b64 s[8:9], s[10:11], s[8:9]\ns_cselect_b64 s[12:13], s[14:15], s[12:13]\ns_bitcmp1_b32 %[bid], 5\ns_cselect_b64 %[wa], s[12:13], s[8:9]"
    : "=&{s4}"(ar[0]), "=&{s5}"(ar[1]), "=&{s6}"(ar[2]), "=&{s7}"(ar[3]), "=&{s18}"(ar[4]), "=&{s19}"(ar[5]), "=&{s20}"(ar[6]), "=&{s21}"(ar[7]), "=&{s22}"(ar[8]), "=&{s23}"(ar[9]),
      "=&{s24}"(ar[10]), "=&{s25}"(ar[11]), "=&{s26}"(ar[12]), "=&{s27}"(ar[13]), [wa] "=&s"(wa), [t0] "=&s"(tid0), [t] "=&s"(t64), [te] "=&v"(te), [sa] "=&s"(sa_), [sb] "=&s"(sb_),
      [va] "=&v"(va_), [pf] "=&v"(tch2), [k0] "=&v"(kr[0]), [v0] "=&v"(vr[0])
    : [ka] "s"(a_ka), [bid] "s"(bid), [tid] "v"(tid) : "s8", "s9", "s10", "s11", "s12", "s13", "s14", "s15", "s16", "s17", "s28", "s29", "s30", "s31", "scc", "memory" FC_REGS);
  asm volatile("" : "+s"(ar[0]), "+s"(ar[1]), "+s"(ar[2]), "+s"(ar[3]), "+s"(ar[4]), "+s"(ar[5]), "+s"(ar[6]), "+s"(ar[7]), "+s"(ar[8]), "+s"(ar[9]), "+s"(ar[10]), "+s"(ar[11]), "+s"(ar[12]), "+s"(ar[13]));
  asm volatile("" : "+s"(wa), "+s"(t64), "+s"(tid0), "+s"(sa_));
  const u32 tka = (u32)wall_clock64();
  const u64 pt0 = t64; u64 pst = (PROBE_K == 100 || PROBE_K == 102) ? pt0 : (PROBE_K == 101 ? pt0 + (u32)(tka - (u32)t64) : 0);
  const u32 t0 = tka;
  __builtin_assume(tid >= 0 && tid < 512 && tid0 >= 0 && tid0 < 512 && bid >= 0 && bid < 256);
  constexpr Prm P = PRM0;
  const int TEND = P.TEND;
  const int xcd = bid & 7, part = bid >> 4 & 3;
  const int lane = tid & 63, wave = tid0 >> 6;
  SB();
  if ((bid >> 4) == 2) {
    const u32 o = tid < 384 ? G_U + (bid & 15) * 6144 + tid * 16 : OOB;
    __builtin_amdgcn_raw_buffer_store_b128(u32x4{SENT, SENT, SENT, SENT}, mk_rsrc((void*)(ar[4] | (u64)ar[5] << 32), WS_END), o, 0, AUX_SC1);
  }
  SB();
  const bool p3 = part == 3;
  u32 pitch = (u32)(0x005600c001000020ull >> (part * 16) & 0xffff) * 32u;
  const int sh = p3 ? 2 : 3;
  u32 wv0 = (u32)((p3 ? (wave >> 1) * 128 : wave * 64) + (part == 0 ? xcd * 512 : 0) + (lane >> sh)) * pitch + ((lane & (p3 ? 3 : 7)) ^ (lane >> (p3 ? 5 : 3))) * 16u;
  const u32 wimg = (u32)(uintptr_t)(&sm.wbuf[0][0][0]);
  u32 wsz = (part == 0 ? 4096u : 512u) * pitch, wld = wimg + (p3 ? (wave & 1) * 32768 + (wave >> 1) * 8192 : wave * 8192);
  PIN(wv0);
  asm volatile("" : "+s"(wsz), "+s"(wld), "+s"(pitch));
  auto karg = [&](int i) { return ar[2 * i] | (u64)ar[2 * i + 1] << 32; };
  const u64 a_kc = karg(0), a_vc = karg(1);
  unsigned char* ws = (unsigned char*)karg(2);
  const u16 *x = (const u16*)karg(3), *cnd = (const u16*)karg(4), *w2 = (const u16*)karg(5);
  u16* out = (u16*)karg(6);
  const rsrc_t wsr = mk_rsrc(ws, WS_END);
  asm volatile("s_waitcnt vmcnt(3)\n\ts_cmp_eq_u32 %1, 2\n\ts_cbranch_scc1 1f\n\ts_waitcnt vmcnt(2)\n1:" : "+v"(te) : "s"(bid >> 4) : "scc");
  const u32 te0 = __builtin_amdgcn_readfirstlane(te[0]), te1 = __builtin_amdgcn_readfirstlane(te[1]);
  const int bl0 = te0 & 127, nl = te0 >> 7 & 63, blk = te0 >> 13 & 63;
  const bool has_w = te0 >> 19 & 1;
  const rsrc_t rw = mk_rsrc((const void*)wa, wsz);
  const u32 wstep = pitch << (6 - sh), wflip = (p3 || part == 0) ? 32u : 0u, wflip0 = part == 0 ? 32u : 0u;
  u32 wso = blk << (sh + 4);
  int wleft = 8;
  auto piece = [&]() {
    dma16(rw, wv0, wso, wld);
    wso += wstep; wld += 1024;
    wv0 ^= (wleft & 1) ? wflip : wflip0;
    --wleft;
  };
  SB();
  STAMP(23);
  const int b = bid >> 3 & 1, head = xcd * 4 + (bid >> 6 ^ 3);
  const int g = lane >> 4, c = lane & 15;
  const int i8 = b | bid >> 6 << 1, wi = i8 * 8 + xcd;
  const bool is_up = p3 && has_w, modc = part == 2 && wi < 16;
  const size_t base = ((size_t)(b * NH + head) * SK + (SK - WIN)) * DH;
  const rsrc_t rk = mk_rsrc((const u16*)a_kc + base, WIN * DH * 2), rv = mk_rsrc((const u16*)a_vc + base, WIN * DH * 2);
  int tau = te1 & 0xffff;
  const int tauw = te0 >> 24 << 7, wdef = te0 >> 20 & 15;
  int due = P.NF * 256, now = 0;
  auto gate = [&]() {
    while (now < due) { now = (int)(((u32)wall_clock64() - t0) << 8); if (now < due) __builtin_amdgcn_s_sleep(1); }
    due += tau;
  };
  auto until = [&](int t) { while ((int)(((u32)wall_clock64() - t0) << 8) < t); };
  const u32 pslot = dup(G_P + (head & 15) * 2048 + (b * 4 + part) * 16);
  const u32 die = xcd >> 2;
  const bool voter = p3 && i8 == 1, timer = p3 && i8 < 2;
  int vote = 0, lag = 0, shift = 0;
  const u32 pbo = dup(G_PB) + wave * 1152;
  u32 near = 0;
  u32x2 ep; u32 xs0, xs1, tch1; u32x4 xr0, xr1;
  const rsrc_t rx = mk_rsrc(part == 2 ? cnd : x, (part == 1 || (part == 2 && has_w)) ? 2048u : 0u);
  auto small_ops = [&]() {
    ep = ld8<AUX_SC1>(wsr, wave == 7 ? WS_EPOCH : OOB, 0);
    xs0 = __builtin_amdgcn_raw_buffer_load_b16(rx, tid * 2, 0, 0); xs1 = __builtin_amdgcn_raw_buffer_load_b16(rx, tid * 2, 1024, 0);
    xr0 = ld16<0>(rx, part == 1 ? lane * 16 : OOB, 0); xr1 = ld16<0>(rx, part == 1 ? lane * 16 : OOB, 1024);
    tch1 = __builtin_amdgcn_raw_buffer_load_b32(mk_rsrc(w2 + (size_t)blk * 32 * 512, is_up ? 64u : 0u), tid == 384 ? 0 : OOB, 0, AUX_NT);
  };
  if (has_w) {
    if (modc) now = 1 << 30;
#pragma nounroll
    while (wleft > wdef) { gate(); due += tauw - tau; piece(); }
    now = 0;
  }
  due = max(due, P.TK * 256);
  SB();
  u32 sot = sa_ & 0xffc00u;
  {
    const u32 s1 = (bl0 + lane - 1 - (lane >= SK0 ? NL - nl : 0)) ^ (124 + part);
#pragma unroll
    for (int k = 0; k < 7; ++k) sot ^= (u32)-(int)(s1 >> k & 1u) & col(P, k);
    PIN(sot);
  }
  u32 tag = 0;
  u32x4 bf[8];
  auto wo_prep = [&]() {
    const u32 wi_ = wimg + wave * 8192;
    const int q = (lane >> 2) & 3, pp = lane & 3;
#pragma unroll
    for (int s = 0; s < 2; ++s) {
#pragma unroll
      for (int t = 0; t < 4; ++t) {
        const int r0 = s * 32 + g * 8 + q, r1 = r0 + 4, ch = t * 2 + (pp >> 1);
        const u32x2 lo = tr_read(wi_ + r0 * 128 + ((ch ^ (r0 & 7)) * 16) + (pp & 1) * 8), hi = tr_read(wi_ + r1 * 128 + ((ch ^ (r1 & 7)) * 16) + (pp & 1) * 8);
        bf[s * 4 + t] = u32x4{lo[0], lo[1], hi[0], hi[1]};
      }
    }
  };
  auto gemv_go = [&](u32 hh) {
    sm.hb[tid] = (u16)hh; sm.hb[512 + tid] = (u16)(hh >> 16);
    WAVE_SYNC();
    const u32x4* a = (c & 11) == 0 ? reinterpret_cast<const u32x4*>(&sm.hb[(c >> 2) * 512 + wave * 64 + g * 8]) : &sm.zero[0];
    const u32x4 a0 = a[0], a1 = a[(c & 11) == 0 ? 4 : 0];
    const f32x4 z = {0.f, 0.f, 0.f, 0.f};
    f32x4 d[4];
#pragma unroll
    for (int t = 0; t < 4; ++t) d[t] = mfma16(a0, bf[t], z);
#pragma unroll
    for (int t = 0; t < 4; ++t) d[t] = mfma16(a1, bf[4 + t], d[t]);
    if (lane < 32) {
#pragma unroll
      for (int t = 0; t < 4; ++t) sm.r2[(t * 16 + c) * 20 + g * 8 + wave] = d[t][0];
    }
  };
  auto mod_stage = [&]() {
    wo_prep();
    gemv_go(pack_bf(silu_f(lo_f(xs0)), silu_f(lo_f(xs1))));
    __builtin_amdgcn_s_waitcnt(0xc07f);
    if (lane == 0) *LDSP(volatile u32, (u32)(uintptr_t)&sm.mflag[wave]) = 1u;
    if (wave == 7) {
#pragma nounroll
      for (int spin = 0; spin < SPIN_MAX; ++spin) {
        const u32 f = *LDSP(volatile u32, (u32)(uintptr_t)&sm.mflag[lane & 7]);
        if (all_ok(f != 0u)) break;
        __builtin_amdgcn_s_sleep(1);
      }
      CFENCE();
      st8_sc1(wsr, G_MOD + (blk * 64 + lane) * 8, pack_bf(sum8(&sm.r2[lane * 20]), sum8(&sm.r2[lane * 20 + 8])), tag);
    }
    STAMP(9);
  };
  if (lane < 4) sm.zero[lane] = u32x4{0u, 0u, 0u, 0u};
  if (part == 2 && has_w) {
    if (tid < 8) sm.mflag[tid] = 0u;
    LDS_BARRIER();
    if (__builtin_expect(modc, 0)) {
      small_ops();
      asm volatile("s_waitcnt vmcnt(0)" ::: "memory");
      tag = next_tag(__builtin_amdgcn_readfirstlane(ep[0]));
      mod_stage();
      __builtin_amdgcn_s_barrier();
    }
  }
  SB();
  const int fd = (xcd * 8 + i8) * 8 + (lane & 3) * 2;
  const int ub = wave >> 2, ud = (wave & 3) * 128 + c * 8 + g * 2;
  u32x4 sg[3]; u32 sx2;
  auto side_loads = [&]() {
    const bool fin = part == 1 && wave == 0;
    sg[0] = ld16<AUX_SC1>(wsr, is_up ? G_MOD + (1024 + ud) * 8 : (fin ? G_MOD + (1024 + fd) * 8 : OOB), 0);
    sg[1] = ld16<AUX_SC1>(wsr, is_up ? G_MOD + (1536 + ud) * 8 : (fin ? G_MOD + (2560 + fd) * 8 : OOB), 0);
    sg[2] = ld16<AUX_SC1>(wsr, is_up ? G_MOD + (2048 + ud) * 8 : OOB, 0);
    sx2 = __builtin_amdgcn_raw_buffer_load_b32(mk_rsrc(x, (is_up || fin) ? 2048u : 0u), is_up ? ub * 1024 + ud * 2 : (g & 1) * 1024 + fd * 2, 0, 0);
  };
  auto side_pin = [&]() { PIN(sg[0]); PIN(sg[1]); PIN(sg[2]); PIN(sx2); };
  const u32 mlo = G_ML + (head * 2 + b) * 64 + (lane & 3) * 16;
  float fmrg = 0.f;
  auto merge_factor = [&](const u32x4& ml) {
    const float mi = u2f(ml[0]);
    float M = fmaxf(mi, dpp<XOR1>(mi)); M = fmaxf(M, dpp<XOR2>(M));
    const float e = __expf(mi - M);
    float L = e * u2f(ml[2]);
    L += dpp<XOR1>(L); L += dpp<XOR2>(L);
    fmrg = rdl(e * __builtin_amdgcn_rcpf(L), part);
  };
  f32x2 ga = {0.f, 0.f}, xin = {0.f, 0.f}, cc1 = {0.f, 0.f};
  float Af = 0.f, Bf = 0.f;
  const int hpos = ub * 512 + (ud & ~15) + (g & 1) * 8 + (c & 1) * 4 + (g >> 1) * 2;
  auto up_prep = [&]() {
    if (!all_ok((sg[0][1] == tag) & (sg[0][3] == tag) & (sg[1][1] == tag) & (sg[1][3] == tag) & (sg[2][1] == tag) & (sg[2][3] == tag))) gather16<3>(wsr, G_MOD + (1024 + ud) * 8, 512 * 8, tag, sg);
    ga = f32x2{ub ? hi_f(sg[0][0]) : lo_f(sg[0][0]), ub ? hi_f(sg[0][2]) : lo_f(sg[0][2])};
    cc1 = f32x2{rbf(1.f + (ub ? hi_f(sg[2][0]) : lo_f(sg[2][0]))), rbf(1.f + (ub ? hi_f(sg[2][2]) : lo_f(sg[2][2])))};
    xin = f32x2{lo_f(sx2), hi_f(sx2)};
    u32* hp = reinterpret_cast<u32*>(&sm.hb[hpos]);
    hp[512] = pack_bf(cc1[0], cc1[1]);
    hp[1024] = ub ? perm_hi(sg[1][0], sg[1][2]) : perm_lo(sg[1][0], sg[1][2]);
  };
  u32x8 bw[4];
  auto up_frags = [&]() {
    const int pp = lane & 3;
    const u32 a = wimg + (pp & 1) * 32768 + ((pp >> 1) * 32 + g * 8 + ((lane >> 2) & 3)) * 64 + (((wave >> 1) ^ ((g & 1) | (pp & 2))) * 16) + (wave & 1) * 8;
#pragma unroll
    for (int j = 0; j < 4; ++j) {
      const u32x2 r0 = tr_read(a + j * 4096), r1 = tr_read(a + j * 4096 + 256), r2 = tr_read(a + j * 4096 + 16384), r3 = tr_read(a + j * 4096 + 16640);
      bw[j] = u32x8{r0[0], r0[1], r1[0], r1[1], r2[0], r2[1], r3[0], r3[1]};
    }
  };
  u32 wfa[4];
  auto wo_frags = [&](int s0) {
#pragma unroll
    for (int t = 0; t < 4; ++t) {
#pragma unroll
      for (int s = s0; s < 2; ++s) {
        const u32x2 lo = tr_read(wfa[t] + s * 4096), hi = tr_read(wfa[t ^ 1] + s * 4096 + 256);
        bw[t][s * 4] = lo[0]; bw[t][s * 4 + 1] = lo[1]; bw[t][s * 4 + 2] = hi[0]; bw[t][s * 4 + 3] = hi[1];
      }
    }
  };
  const bool par = lane & 8;
  auto limit = [&](auto sc) {
    constexpr int s = sc.v;
    WAIT_VM(window(s, 1 + (PRM0.W[s >> 3] >> (4 * (s & 7)) & 7)));
    SB();
  };
  auto sample = [&]() { if (timer) lag += (int)(((u32)wall_clock64() - t0) << 8) - due + ((int)__builtin_amdgcn_s_getreg(7 | (3 << 11)) + 1) * tau; };
  auto report = [&](u32 dst) {
    if (timer) {
      if (lane == 0) sm.mflag[wave] = vote * 65536 + min(max(lag >> 8, 0), 4000);
      lag = 0;
      LDS_BARRIER();
      if (wave == 7) {
        int s = sm.mflag[lane & 7];
        s += __builtin_amdgcn_mov_dpp(s, XOR1, 0xf, 0xf, true); s += __builtin_amdgcn_mov_dpp(s, XOR2, 0xf, 0xf, true); s += __builtin_amdgcn_mov_dpp(s, XOR7, 0xf, 0xf, true);
        if (lane < 8) st8_sc1(wsr, dst + lane * 128 + wi * 8, s, tag);
      }
    }
    SB();
  };
  float l0 = 0.f, l1 = 0.f;
  u32x2 gmd[2], tbg;
  int t1 = 0;
#pragma unroll
  for (int i = SK0; i < SK1; ++i) kr[i] = u32x4{0u, 0u, 0u, 0u};
  const int first = SK0 + NL - nl;
  auto left_out = [&](int i) { int f = first; asm volatile("" : "+s"(f)); return i < f; };
  sfor<1, NL>([&](auto ic) {
    constexpr int i = ic.v;
    if constexpr (i >= SK0 && i < SK1) { if (left_out(i)) return true; }
    const u32 so = __builtin_amdgcn_readlane(sot, i);
    gate();
    if constexpr ((i >= TBS && i <= TBP) || (i >= RPS && i <= RPP)) sample();
    SB();
    if constexpr (i >= SK0 && i < SK1) ld16_into(kr[i], rk, lane * 16, so); else kr[i] = ld16<AUX_NT>(rk, lane * 16, so);
    if constexpr (i == SMO) small_ops();
    if constexpr (i == SMO + 1) { if (voter && (xcd & 3) == 0) __builtin_amdgcn_raw_buffer_store_b32(0u, wsr, pbo + (lane >> 5) * 8192 + (lane & 31) * 4, 0, AUX_SC1); }
    if constexpr (i == SK1) tbg = ld8<AUX_SC1>(wsr, wave == 7 ? G_VT + xcd * 128 + c * 8 : OOB, 0);
    if constexpr (i == TBL) tbg = ld8<AUX_SC1>(wsr, wave == 7 ? G_TB + xcd * 128 + c * 8 : OOB, 0);
    if constexpr (i == QP) { gmd[0] = ld8<AUX_SC1>(wsr, part == 1 ? G_MOD + tid * 8 : OOB, 0); gmd[1] = ld8<AUX_SC1>(wsr, part == 1 ? G_MOD + tid * 8 : OOB, 4096); }
    SB();
    SB();
    if (i >= 4) limit(ic);
    if (i == 1) STAMP(25);
    if (i == 8) STAMP(12);
    if (i == 20) STAMP(21);
    if (i == 32) STAMP(22);
    if (i == TBP) {
      if (timer) {
        if (lane == 0) sm.mflag[wave] = lag;
        lag = 0;
        LDS_BARRIER();
        if (wave == 7) {
          int s = sm.mflag[lane & 7];
          s += __builtin_amdgcn_mov_dpp(s, XOR1, 0xf, 0xf, true); s += __builtin_amdgcn_mov_dpp(s, XOR2, 0xf, 0xf, true); s += __builtin_amdgcn_mov_dpp(s, XOR7, 0xf, 0xf, true);
          if (lane < 8) st8_sc1(wsr, G_TB + lane * 128 + wi * 8, t0 << 10 | (u32)min(s / (2048 * (TBP - TBS + 1)), 1023), tag);
        }
      }
      SB();
    }
    if (i == PRB) {
      if (voter) {
        u64 ta, tb, tc; u32 dm;
        const u32 pa = pbo + (wave & 1) * 8192;
        asm volatile("s_memtime %0\n\ts_waitcnt lgkmcnt(0)\n\ts_load_dword %3, %4, %5\n\ts_waitcnt lgkmcnt(0)\n\ts_memtime %1\n\ts_waitcnt lgkmcnt(0)\n\t"
                     "s_load_dword %3, %4, %6\n\ts_waitcnt lgkmcnt(0)\n\ts_memtime %2\n\ts_waitcnt lgkmcnt(0)"
                     : "=&s"(ta), "=&s"(tb), "=&s"(tc), "=&s"(dm) : "s"(ws), "s"(pa), "s"(pa ^ 8192u));
        const int d = min(max((int)(2 * (u32)tb - (u32)ta - (u32)tc), -500), 500);
        vote = ((wave ^ die) & 1) ? -d : d;
        STAMP(26);
      }
      SB();
    }
    if (i == TBU) {
      if (wave == 7) {
        const bool ok = tbg[1] == tag;
        float lf = ok ? (float)min(max((int)((t0 << 10 | 1023u) - tbg[0]) >> 10, -300), 300) : 0.f, nf = ok ? 1.f : 0.f, lg = ok ? (float)(tbg[0] & 1023u) : 0.f;
        lf += dpp<XOR1>(lf); nf += dpp<XOR1>(nf); lg += dpp<XOR1>(lg); lf += dpp<XOR2>(lf); nf += dpp<XOR2>(nf); lg += dpp<XOR2>(lg);
        lf += dpp<XOR7>(lf); nf += dpp<XOR7>(nf); lg += dpp<XOR7>(lg); lf += dpp<XOR8>(lf); nf += dpp<XOR8>(nf); lg += dpp<XOR8>(lg);
        const float rn = __builtin_amdgcn_rcpf(fmaxf(nf, 1.f));
        shift = __builtin_amdgcn_readfirstlane(min(max((int)((lg * rn - (float)P.GS) * (float)P.GB * 0.125f), 0), 150));
        if (lane == 0) sm.bc = u32x2{tag, t0 - (u32)(int)(lf * rn) + shift};
      }
      LDS_BARRIER();
      const u32x2 bc = sm.bc;
      tag = __builtin_amdgcn_readfirstlane(bc[0]);
      t1 = (__builtin_amdgcn_readfirstlane((int)(bc[1] - t0)) + TEND + P.X1) * 256;
      tau -= __builtin_amdgcn_readfirstlane((int)((float)(int)(t0 - bc[1]) * 256.f * __builtin_amdgcn_rcpf((float)(nl - TBU - 1))));
      SB();
    }
    if (i == RPP) report(G_VT);
    if (i == LNS) {
      tag = next_tag(__builtin_amdgcn_readfirstlane(ep[0]));
      if (part == 1) {
        float x0[8], x1[8], mb0, ri0, mb1, ri1;
#pragma unroll
        for (int k = 0; k < 4; ++k) { x0[2 * k] = lo_f(xr0[k]); x0[2 * k + 1] = hi_f(xr0[k]); x1[2 * k] = lo_f(xr1[k]); x1[2 * k + 1] = hi_f(xr1[k]); }
        ln_stats2(x0, x1, mb0, ri0, mb1, ri1);
        l0 = rbf(rbf(lo_f(xs0) - mb0) * ri0); l1 = rbf(rbf(lo_f(xs1) - mb1) * ri1);
      }
      PIN(l0); PIN(l1);
      SB();
    }
    if (i == WUSE) {
      PIN(tch1); PIN(tch2);
      if ((part == 1 || (part == 2 && has_w && !modc))) {
        WAIT_VM(WK); CFENCE();
        if (part == 2) mod_stage(); else wo_prep();
      }
#pragma unroll
      for (int k = 0; k < 8; ++k) PIN(bf[k]);
      SB();
    }
    if (i == QP + QD) {
      if (part == 1) {
        u32 gm[2] = {gmd[0][0], gmd[1][0]};
        if (!all_ok((gmd[0][1] == tag) & (gmd[1][1] == tag))) { STAMP(11); gather8<2>(wsr, G_MOD + tid * 8, 512 * 8, tag, gm); }
        STAMP(5);
        gemv_go(pack_bf(rbf(l0 * rbf(1.f + lo_f(gm[1]))) + lo_f(gm[0]), rbf(l1 * rbf(1.f + hi_f(gm[1]))) + hi_f(gm[0])));
      }
      SB();
    }
    if (i == QP + QD + 1) {
      if (part == 1) {
        LDS_BARRIER();
        if (wave == 0) {
          const int bb = lane >> 5, jq = lane & 31;
          st8_sc1(wsr, G_Q + (bb * 2048 + blk * 32 + jq) * 8, pack_bf(sum8(&sm.r2[2 * jq * 20 + bb * 8]), sum8(&sm.r2[(2 * jq + 1) * 20 + bb * 8])), tag);
        }
        STAMP(6);
      }
      SB();
    }
    return true;
  });
  PIN(sot);
  if (wave == 7) {
    const bool ok = tbg[1] == tag;
    float lg = ok ? (float)(tbg[0] & 0xffffu) : 0.f, nf = ok ? 8.f * (RPP - RPS + 1) : 0.f;
    lg += dpp<XOR1>(lg); nf += dpp<XOR1>(nf); lg += dpp<XOR2>(lg); nf += dpp<XOR2>(nf); lg += dpp<XOR7>(lg); nf += dpp<XOR7>(nf); lg += dpp<XOR8>(lg); nf += dpp<XOR8>(nf);
    lg *= __builtin_amdgcn_rcpf(fmaxf(nf, 1.f));
    if (lane == 0) sm.bc2 = u32x2{(u32)min(shift * P.DG * 32, P.EW * 256), (u32)min(max((int)((lg - (float)P.B0) * (float)(P.BG * 32) + fmaxf(lg - (float)P.TC, 0.f) * (float)(P.RT * 32)) - shift * P.D0 * 32, 0), 200 * 256)};
  }
  LDS_BARRIER();
  {
    const u32x2 bc = sm.bc2;
    const int st = __builtin_amdgcn_readfirstlane(bc[1]), sp = (st * P.BF) >> 3;
    t1 += st + __builtin_amdgcn_readfirstlane(bc[0]);
    due += P.TG * 256 + sp;
    tau = (te1 >> 16) + __builtin_amdgcn_readfirstlane((int)((float)(st - sp) * __builtin_amdgcn_rcpf((float)(nl - 3))));
  }
  const u32 qoff = G_Q + ((b * 32 + head) * 64 + c * 4) * 8;
  u32x4 qg[2], mlg;
  u32 qq[4];
  constexpr int NB = (NL + 15) / 16;
  float xlo, sc[NB], pr[NB], m = 0.f;
  f32x4 acc = {0.f, 0.f, 0.f, 0.f};
  u32 pb[NB], pm[4], pl8[8], cpf;
  const u32 adst0 = lds_addr(lane < 32 ? &sm.red[wave][c * 8 + g * 4] : &sm.r2[(lane - 32) * 4]);
  u32 po, pso, rdst, rsrc, xo;
  auto wo_addr = [&]() {
    po = dup(G_P + (xcd & 3) * 8192) + tid * 16 + near;
    pso = tid < 16 ? pslot + c * 128 + near : OOB;
    PIN(pso);
    rdst = (u32)(uintptr_t)&sm.red2[wave][(lane & 31) * 4 + (lane >> 5) * 2];
    rsrc = (u32)(uintptr_t)&sm.red2[0][((wave & 1) * 16 + c) * 4 + (g & 1) * 2 + (g >> 1)];
    xo = dup(G_XP + xcd * 2048 + (wave & 1) * 1024 + (blk * 64 + (lane & ~1)) * 2) + (lane & 1) * 8192;
    PIN(po); PIN(rdst); PIN(rsrc); PIN(xo);
    const int q = (lane >> 2) & 3, pp = lane & 3, r = (g & 1) * 4 + (q & 1);
#pragma unroll
    for (int t = 0; t < 4; ++t) { wfa[t] = wimg + wave * 8192 + ((g >> 1) * 16 + (q >> 1) * 8 + r) * 128 + (pp & 1) * 8 + ((t * 2 + (pp >> 1)) ^ r ^ (q & 2)) * 16; PIN(wfa[t]); }
  };
  const u32 nadd = wdef ? P.LW : (is_up ? P.LU : 0);
  u32 nto = tid < 2 ? dup(G_NT) + bid * 8 + tid * 8192 : OOB;
  PIN(nto);
  auto notices = [&]() -> int {
    u32 off = dup(G_NT) + near + lane * 16;
    PIN(off);
    until(due);
    int mx;
#pragma nounroll
    for (int spin = 0; spin < SPIN_MAX; ++spin) {
      CFENCE();
      const u32x4 ga_ = ld16<AUX_SC1>(wsr, off, 0), gb_ = ld16<AUX_SC1>(wsr, off, 1024);
      mx = max(max((int)(ga_[0] - t0), (int)(ga_[2] - t0)), max((int)(gb_[0] - t0), (int)(gb_[2] - t0)));
      if (all_ok((((ga_[1] ^ tag) | (ga_[3] ^ tag)) | ((gb_[1] ^ tag) | (gb_[3] ^ tag))) == 0u)) break;
      for (int k = 0; k < P.AG; ++k) __builtin_amdgcn_s_sleep(1);
    }
    if (all_ok(mx <= (t1 >> 8) - P.AC)) return t1;
    mx = max(mx, __builtin_amdgcn_mov_dpp(mx, XOR1, 0xf, 0xf, true)); mx = max(mx, __builtin_amdgcn_mov_dpp(mx, XOR2, 0xf, 0xf, true));
    mx = max(mx, __builtin_amdgcn_mov_dpp(mx, XOR7, 0xf, 0xf, true)); mx = max(mx, __builtin_amdgcn_mov_dpp(mx, XOR8, 0xf, 0xf, true));
    mx = max(max(__builtin_amdgcn_readlane(mx, 0), __builtin_amdgcn_readlane(mx, 16)), max(__builtin_amdgcn_readlane(mx, 32), __builtin_amdgcn_readlane(mx, 48)));
    return max(min(mx, 1 << 20) * 256, t1 + P.L0 * 256) + P.L1 * 256;
  };
  auto pv_row = [&](auto jc) {
    constexpr int i = jc.v;
    const u32 pv = __float_as_uint(bcast16(u2f(pb[i >> 4]), i & 15));
    acc = mfma16(u32x4{pv & pm[0], pv & pm[1], pv & pm[2], pv & pm[3]}, vr[i], acc);
    PIN(acc);
    return true;
  };
  sfor<0, NL - 1>([&](auto ic) {
    constexpr int v = ic.v;
    if constexpr (v >= SK0 && v < SK1) { if (left_out(v)) return true; }
    if constexpr (v > 0) {
      const u32 so = __builtin_amdgcn_readlane(sot, v);
      if constexpr (v >= SK1) { if (v == NL - P.NE) tau = (tau * P.FE) >> 4; }
      gate();
      if constexpr (v >= RVS && v <= RVP) sample();
      SB();
      if constexpr (v >= SK0 && v < SK1) ld16_into(vr[v], rv, lane * 16, so); else vr[v] = ld16<AUX_NT>(rv, lane * 16, so);
      SB();
      SB();
    }
    if constexpr (v == SK0 - 1) {
#pragma unroll
      for (int i = SK0; i < SK1; ++i) { vr[i] = u32x4{0u, 0u, 0u, 0u}; PIN(vr[i]); }
    }
    if constexpr (v == VQ - 4) { qg[0] = ld16<AUX_SC1>(wsr, qoff, 0); qg[1] = ld16<AUX_SC1>(wsr, qoff + 16, 0); }
    if constexpr (v == VSIDE) side_loads();
    if constexpr (v == CPF) {
      u64 pc; u32 dlt, len;
      asm volatile("s_getpc_b64 %0\n.Lcpf_here:\n\ts_mov_b32 %1, .Ltail_start-.Lcpf_here\n\ts_mov_b32 %2, .Lend_a-.Ltail_start\n\ts_max_i32 %2, %2, .Lend_b-.Ltail_start\n\ts_max_i32 %2, %2, .Lend_c-.Ltail_start\n\ts_max_i32 %2, %2, .Lend_d-.Ltail_start\n\ts_max_i32 %2, %2, .Lend_e-.Ltail_start" : "=s"(pc), "=s"(dlt), "=s"(len) :: "scc");
      pc += dlt;
      const rsrc_t rc = mk_rsrc((const void*)pc, (part == 3 && i8 == 1) ? len : 0u);
      cpf = __builtin_amdgcn_raw_buffer_load_b32(rc, tid * 64, 0, 0);
    }
    if constexpr (v == SK1 + 2) PIN(cpf);
    if constexpr (v == RVP) report(G_VT + 1024);
    if constexpr (v == RVL) tbg = ld8<AUX_SC1>(wsr, wave == 7 ? G_VT + (g ? 0 : 1024) + xcd * 128 + c * 8 : OOB, 0);
    if constexpr (v == SK1) mlg = ld16<AUX_SC1>(wsr, wave == 0 ? mlo : OOB, 0);
    SB();
    if constexpr (NL + v < 72) limit(IC<NL + v>{});
    if constexpr (v >= SK0 + WV) { WAIT_VM(v < SK1 ? WV : WV + 1); SB(); }
    if constexpr (v == VQ) {
      if (!all_ok((qg[0][1] == tag) & (qg[0][3] == tag) & (qg[1][1] == tag) & (qg[1][3] == tag))) { STAMP(20); gather16<2>(wsr, qoff, 16, tag, qg); }
      STAMP(1); WSTAMP(1);
      qq[0] = qg[0][0]; qq[1] = qg[0][2]; qq[2] = qg[1][0]; qq[3] = qg[1][2];
    }
    if constexpr (v >= VQ && v < VS) {
      constexpr int sl = v - VQ, bt = sl >> 1;
      float xs[8];
#pragma unroll
      for (int i = 0; i < 8; ++i) {
        float d = 0.f;
        if (sl * 8 + i < NL) {
#pragma unroll
          for (int k = 0; k < 4; ++k) d = dot2(kr[sl * 8 + i][k], qq[k], d);
        }
        xs[i] = d;
      }
#pragma unroll
      for (int h = 0; h < 4; ++h) xs[h] = sel_add<XOR7>(xs[h], xs[h + 4], c & 4);
#pragma unroll
      for (int h = 0; h < 2; ++h) xs[h] = sel_add<XOR2>(xs[h], xs[h + 2], c & 2);
      xs[0] = sel_add<XOR1>(xs[0], xs[1], c & 1);
      if constexpr ((sl & 1) || sl == NSL - 1) {
        const float t = (sl & 1) ? sel_add<XOR8>(xlo, xs[0], c & 8) : xs[0] + dpp<XOR8>(xs[0]);
        const float s = rbf(rbf(t) * 0.08838834764831845f);
        sc[bt] = (bt * 16 + c < SK0 || (bt * 16 + c >= first && bt * 16 + c < NL)) ? s : -__builtin_inff();
        PIN(sc[bt]);
      } else {
        xlo = xs[0];
        PIN(xlo);
      }
    }
    if constexpr (v == VS) {
      float mloc = sc[0];
#pragma unroll
      for (int bt = 1; bt < NB; ++bt) mloc = fmaxf(mloc, sc[bt]);
      m = fmaxf(wave_max(mloc), -3.0e38f);
      float ls = 0.f;
#pragma unroll
      for (int bt = 0; bt < NB; ++bt) { pr[bt] = __expf(sc[bt] - m); ls += pr[bt]; }
      const float l = wave_sum(ls);
      if (lane == 0) { sm.ml[wave][0] = m; sm.ml[wave][1] = l; }
      if (c == ((NL - 1) & 15)) sm.pl[g][wave] = pr[NB - 1];
      STAMP(2); WSTAMP(2);
    }
    if constexpr (v >= SK1) pv_row(IC<v - SK1 + SK0 - WV>{});
    if constexpr (v < SK0 || v >= SK0 + WV) sfor<cons_end(v - 1), cons_end(v)>(pv_row);
    if constexpr (v == RVU) {
      if (wave == 7) {
        if (__builtin_expect(!all_ok(tbg[1] == tag || g == 0), 0)) {
          u32 vg[1];
          gather8<1>(wsr, G_VT + xcd * 128 + c * 8, 0, tag, vg);
          if (g) tbg = u32x2{vg[0], tag};
        }
        const bool ok = tbg[1] == tag;
        float lg = ok ? (float)(g ? (int)tbg[0] >> 16 : (int)(tbg[0] & 0xffffu)) : 0.f, nf = ok ? 8.f * (RVP - RVS + 1) : 0.f;
        lg += dpp<XOR1>(lg); nf += dpp<XOR1>(nf); lg += dpp<XOR2>(lg); nf += dpp<XOR2>(nf); lg += dpp<XOR7>(lg); nf += dpp<XOR7>(nf); lg += dpp<XOR8>(lg); nf += dpp<XOR8>(nf);
        lg *= __builtin_amdgcn_rcpf(fmaxf(nf, 1.f));
        if (lane == 16) sm.bc4 = __float_as_uint(lg);
        if (lane == 0) sm.bc3 = (u32)min(max((int)((lg - (float)P.F0) * (float)(P.FG * 32)), 0), 200 * 256);
      }
      LDS_BARRIER();
      const int st = __builtin_amdgcn_readfirstlane(sm.bc3), sp = (st * P.FF_) >> 3;
      near = ((__builtin_amdgcn_readfirstlane(sm.bc4) >> 31 ^ die ^ P.NR ^ 1) & 1) * 8192u;
      t1 += st;
      due += sp;
      tau += __builtin_amdgcn_readfirstlane((int)((float)(st - sp) * __builtin_amdgcn_rcpf((float)(nl - RVU - 3))));
    }
    if constexpr (v == SK1 + 1) {
      float p[8];
#pragma unroll
      for (int w = 0; w < 8; ++w) p[w] = sm.pl[g][w] * __expf(sm.ml[w][0] - m);
#pragma unroll
      for (int w = 0; w < 8; ++w) pl8[w] = pack_bf(p[w], p[w]);
#pragma unroll
      for (int w = 0; w < 8; ++w) PIN(pl8[w]);
    }
    if constexpr (v == MLS - 1) { side_pin(); if (is_up) up_prep(); PIN(ga); PIN(xin); PIN(cc1); }
    if constexpr (v == UPREP - 1) wo_addr();
    if constexpr (v == UPREP) {
#pragma unroll
      for (int k = 0; k < 4; ++k) asm volatile("" : "=v"(bw[k]));
      if (part == 0) wo_frags(0); else if (is_up) up_frags();
#pragma unroll
      for (int k = 0; k < 4; ++k) PIN(bw[k]);
    }
    if constexpr (v == UPREP + 1) {
      if (is_up) {
        const u32x4* a1 = reinterpret_cast<const u32x4*>(&sm.hb[1024 + (c >> 2 & 1) * 1024 + (c >> 3) * 512 + (g >> 1) * 256 + (c >> 1 & 1) * 32 + (g & 1) * 16 + (c & 1) * 8]);
        f32x4 d = {0.f, 0.f, 0.f, 0.f};
#pragma unroll
        for (int j = 0; j < 4; ++j) d = mfma64(a1[j * 8], bw[j], d, (c & 1) ? 0xeeeeu : 0x4444u);
        float t = par ? d[2] + d[3] : d[0] + d[1];
        t += dpp<XOR8>(t);
        const auto ab = __builtin_amdgcn_permlane16_swap(__float_as_uint(t), __float_as_uint(t), false, false);
        Af = u2f(ab[0]); Bf = u2f(ab[1]);
      }
      PIN(Af); PIN(Bf);
    }
    if (v == 10 || v == 16) STAMP(15 + v / 6);
    if (v == 20) STAMP(18);
    if (v == 32) STAMP(19);
    if constexpr (v == MLS) {
      LDS_BARRIER();
      float mm[8], ll[8];
#pragma unroll
      for (int w = 0; w < 8; ++w) { const f32x2 t = *reinterpret_cast<const f32x2*>(&sm.ml[w][0]); mm[w] = t[0]; ll[w] = t[1]; }
      float Mwg = mm[0], Lwg = 0.f;
#pragma unroll
      for (int w = 1; w < 8; ++w) Mwg = fmaxf(Mwg, mm[w]);
      if (wave == 0) {
#pragma unroll
        for (int w = 0; w < 8; ++w) Lwg = fmaf(__expf(mm[w] - Mwg), ll[w], Lwg);
        if (lane == 0) st16_sc1(wsr, G_ML + ((head * 2 + b) * 4 + part) * 16, __float_as_uint(Mwg), __float_as_uint(Lwg), tag);
      }
      const float fw = __expf(m - Mwg);
      m = Mwg;
#pragma unroll
      for (int bt = 0; bt < NB; ++bt) { pb[bt] = pack_bf(pr[bt] * fw, pr[bt] * fw); PIN(pb[bt]); }
#pragma unroll
      for (int k = 0; k < 4; ++k) { pm[k] = (c >> 1 & 3) == k ? ((c & 1) ? 0xffff0000u : 0x0000ffffu) : 0u; PIN(pm[k]); }
    }
    SB();
    return true;
  });
  asm volatile(".Ltail_start:");
  if (wave != 0) {
    pv_row(IC<SK0 - 1>{});
    WAIT_VM(WV); SB();
    sfor<NL - 1 - WV, NL - 1>(pv_row);
    *LDSP(f32x4, adst0) = acc;
    WSTAMP(14);
    LDS_BARRIER();
  } else {
    u32x4 xr[8], am[8];
#pragma unroll
    for (int w = 0; w < 8; ++w) am[w] = u32x4{pl8[w] & pm[0], pl8[w] & pm[1], pl8[w] & pm[2], pl8[w] & pm[3]};
#pragma unroll
    for (int w = 0; w < 8; ++w) PIN(am[w]);
    {
      const u32 so = __builtin_amdgcn_readlane(sot, NL - 1);
      gate();
      SB();
#pragma unroll
      for (int w = 0; w < 8; ++w) xr[w] = ld16<AUX_NT>(rv, lane * 16, so ^ ((w & 1) ? col(P, 7) : 0u) ^ ((w & 2) ? col(P, 8) : 0u) ^ ((w & 4) ? col(P, 9) : 0u));
      SB();
    }
    u32 rsum = lds_addr(&sm.red[0][c * 8 + (g & 1) * 4]);
    pv_row(IC<SK0 - 1>{});
    WAIT_VM(WV + 8); SB();
    sfor<NL - 1 - WV, NL - 3>(pv_row);
    if (__builtin_expect(!all_ok((mlg[1] == tag) & (mlg[3] == tag)), 0)) {
#pragma nounroll
      for (int spin = 0; spin < SPIN_MAX; ++spin) {
        CFENCE();
        mlg = ld16<AUX_SC1>(wsr, mlo, 0);
        if (all_ok((mlg[1] == tag) & (mlg[3] == tag))) break;
      }
    }
    merge_factor(mlg);
    sfor<NL - 3, NL - 1>(pv_row);
    STAMP(14); WSTAMP(14);
    LDS_BARRIER();
    STAMP(15);
    f32x4 t[8];
#pragma unroll
    for (int w = 1; w < 8; ++w) t[w] = LDSP(f32x4, rsum)[w * 32];
    acc = ((acc + t[1]) + (t[2] + t[3])) + ((t[4] + t[5]) + (t[6] + t[7]));
    PIN(acc);
    STAMP(3);
    SB();
#pragma unroll
    for (int w = 0; w < 8; ++w) acc = mfma16(am[w], xr[w], acc);
  }
  WAIT_VM(0);
  acc *= fmrg;
  {
    const u32 w0 = min(pack_bf(acc[0], acc[1]), SENT - 1u), w1 = min(pack_bf(acc[2], acc[3]), SENT - 1u);
    const auto e0 = __builtin_amdgcn_permlane16_swap(w0, w0, false, false), e1 = __builtin_amdgcn_permlane16_swap(w1, w1, false, false);
    __builtin_amdgcn_raw_buffer_store_b128(u32x4{e0[0], e1[0], e0[1], e1[1]}, wsr, pso, 0, AUX_SC1);
  }
  st8_sc1(wsr, nto, (u32)wall_clock64() + nadd, tag);
  SB();
  STAMP(4);

  if (part == 2) FINISH();
  if (part == 0) {
    __builtin_amdgcn_s_barrier();
#pragma nounroll
    while (wleft > 0) { until(t1 - (P.PA + wleft * P.PD) * 256); piece(); }
    bool late = false;
    u32x4 og[1];
    f32x4 d[4];
    u32 six = (c & 8) ? 0xeeeeu : 0x4444u;
    PIN(six);
#pragma unroll
    for (int t = 0; t < 4; ++t) { d[t] = f32x4{0.f, 0.f, 0.f, 0.f}; PIN(d[t]); }
    if (wdef) {
      late = true;
      while ((int)(((u32)wall_clock64() - t0) << 8) < t1 - 10 * 256) if (__builtin_amdgcn_s_getreg(7 | (3 << 11)) == 0) { late = false; break; }
      if (__builtin_expect(late, 0)) { until(t1); CFENCE(); og[0] = ld16<AUX_SC1>(wsr, po, 0); SB(); WAIT_VM(1); } else { WAIT_VM(0); }
      CFENCE();
      wo_frags(1);
#pragma unroll
      for (int k = 0; k < 4; ++k) PIN(bw[k]);
      SB();
    }
    if (__builtin_expect(!late, 1)) { until(t1); CFENCE(); og[0] = ld16<AUX_SC1>(wsr, po, 0); }
    STAMP(11);
    SB();
    auto wo_mm = [&]() {
#pragma unroll
      for (int t = 0; t < 4; ++t) d[t] = mfma64(og[0], bw[t], d[t], six);
    };
    auto p_bad = [&]() { return !all_ok(max(max(og[0][0], og[0][1]), max(og[0][2], og[0][3])) != SENT); };
    wo_mm();
    if (__builtin_expect(p_bad(), 0)) {
#pragma nounroll
      for (int spin = 0; spin < SPIN_MAX; ++spin) {
        __builtin_amdgcn_s_sleep(1);
        CFENCE();
        og[0] = ld16<AUX_SC1>(wsr, po, 0);
        if (!p_bad()) break;
      }
#pragma unroll
      for (int t = 0; t < 4; ++t) d[t] = f32x4{0.f, 0.f, 0.f, 0.f};
      wo_mm();
      SB();
      asm volatile(".Lend_d:");
    }
    STAMP(5); WSTAMP(5);
    {
      SB();
      f32x4 y; f32x2 h[4];
#pragma unroll
      for (int t = 0; t < 4; ++t) { h[t] = d[t].xy + d[t].zw; PIN(h[t]); }
#pragma unroll
      for (int t = 0; t < 4; ++t) { y[t] = h[t][0] + h[t][1]; PIN(y[t]); }
      *LDSP(f32x2, rdst) = f32x2{swap32_add(y[0], y[1]), swap32_add(y[2], y[3])};
    }
    STAMP(16);
    LDS_BARRIER();
    STAMP(17);
    if (wave < 2) {
      f32x2 v[4];
#pragma unroll
      for (int w = 0; w < 4; ++w) v[w] = f32x2{LDSP(float, rsrc)[w * 256], LDSP(float, rsrc)[w * 256 + 128]};
      const f32x2 h = (v[0] + v[1]) + (v[2] + v[3]);
      const float y = h[0] + h[1];
      __builtin_amdgcn_raw_buffer_store_b32(min(pack_bf(dpp<0xA0>(y), dpp<0xF5>(y)), SENT - 1u), wsr, xo, 0, AUX_SC1);
      asm volatile(".Lend_a:");
    }
    STAMP(6);
    FINISH();
  }
  if (part == 3) {
    if (!has_w) FINISH();
    u32x4 b2[4];
    {
      __builtin_amdgcn_s_barrier();
      const rsrc_t r2 = mk_rsrc(w2 + (size_t)(blk * 32 + wave * 4) * 512, 4096u);
      const u32 l2 = (u32)(uintptr_t)(&sm.w2b[wave * 4][0]);
#pragma unroll
      for (int n = 0; n < 4; ++n) { until(t1 - (P.QA + (4 - n) * P.QD) * 256); dma16(r2, (lane ^ (n * 2) ^ ((wave & 2) << 2)) * 16, n * 1024, l2 + n * 1024); }
    }
    const u32 tdst = lds_addr(&sm.hb[hpos]);
    const u32 sdst = lds_addr(&sm.stat[wave][lane]);
    const u32 ssrc = lds_addr(&sm.stat[(g >> 1) * 4 + ((lane >> 3) & 3)][(lane & 7) * 4]);
    const u32 adst = lds_addr((c < 4 && (g & 1) == 0) ? &sm.actl[g >> 1][wave * 4 + c] : reinterpret_cast<u16*>(&sm.dummy[lane]));
    const u32 asrc = lds_addr(&sm.actl[lane & 1][g * 8]);
    const u32 apa = lds_addr(&sm.hb[(c >> 3) * 512 + (g >> 1) * 256 + (c >> 1 & 1) * 64 + ((c >> 2) & 1) * 32 + (g & 1) * 16 + (c & 1) * 8]);
    u32 six = (c & 1) ? 0xeeeeu : 0x4444u;
    f32x4 d[2] = {{0.f, 0.f, 0.f, 0.f}, {0.f, 0.f, 0.f, 0.f}};
    PIN(six); PIN(d[0]); PIN(d[1]);
    constexpr u32 xs2 = 16384;
    u32 xoff = dup(G_XP) + near + g * 2048 + ub * 1024 + (wave & 3) * 256 + c * 16;
    u32x4 one;
#pragma unroll
    for (int k = 0; k < 4; ++k) one[k] = ((c & 2) == 0 && (c >> 2) == k) ? ((c & 1) ? 0x3f800000u : 0x00003f80u) : 0u;
    PIN(one);
    u32 ooff = dup(G_OP + (blk * 512 + tid) * 8);
    float Afp = Af, Bfp = Bf;
    PIN(xoff); PIN(ooff); PIN(Afp); PIN(Bfp);
    f32x2 gc = ga * cc1, xc = xin * cc1;
    PIN(ga); PIN(xin); PIN(gc); PIN(xc);
    u32 w2a = (u32)(uintptr_t)(&sm.w2b[0][0]) + (g * 8 + ((lane >> 2) & 3)) * 1024 + (wave ^ (g & 1)) * 128 + (lane & 15) * 8;
    PIN(w2a);
    if (wave == 1) {
      due = t1 + P.N1 * 256;
      const int tn = notices();
      due = tn + P.X2 * 256;
      until(due);
    }
    asm volatile("s_waitcnt vmcnt(0)" ::: "memory");
    STAMP(9);
    __builtin_amdgcn_s_barrier();
    STAMP(11);
    CFENCE();
    u32x4 xg[2] = {ld16<AUX_SC1>(wsr, xoff, 0), ld16<AUX_SC1>(wsr, xoff, xs2)};
    SB();
#pragma unroll
    for (int t = 0; t < 4; ++t) {
      const u32x2 lo = tr_read(w2a ^ (t * 32)), hi = tr_read((w2a ^ (t * 32)) + 4096);
      b2[t] = u32x4{lo[0], lo[1], hi[0], hi[1]};
    }
#pragma unroll
    for (int t = 0; t < 4; ++t) PIN(b2[t]);
    SB();
    const f32x4 z4 = {0.f, 0.f, 0.f, 0.f};
    auto xp_bad = [&]() { return !all_ok(max(max(max(xg[0][0], xg[0][1]), max(xg[0][2], xg[0][3])), max(max(xg[1][0], xg[1][1]), max(xg[1][2], xg[1][3]))) != SENT); };
    f32x4 v = mfma16(one, xg[1], mfma16(one, xg[0], z4));
    if (__builtin_expect(xp_bad(), 0)) {
#pragma nounroll
      for (int spin = 0; spin < SPIN_MAX; ++spin) {
        __builtin_amdgcn_s_sleep(1);
        CFENCE();
        xg[0] = ld16<AUX_SC1>(wsr, xoff, 0); xg[1] = ld16<AUX_SC1>(wsr, xoff, xs2);
        if (!xp_bad()) break;
      }
      v = mfma16(one, xg[1], mfma16(one, xg[0], z4));
      SB();
      asm volatile(".Lend_e:");
    }
    STAMP(5); WSTAMP(5);
    {
      const f32x2 a = v.xy, hc = __builtin_elementwise_fma(gc, a, xc), x1 = __builtin_elementwise_fma(ga, a, xin);
      *LDSP(u32, tdst) = pack_bf(hc[0], hc[1]);
      *LDSP(float, sdst) = swap32_add(x1[0] + x1[1], fmaf(x1[0], x1[0], x1[1] * x1[1]));
    }
    STAMP(16);
    LDS_BARRIER();
    STAMP(17);
    {
      const f32x4 s4 = LDSP(f32x4, ssrc)[0], q4 = LDSP(f32x4, ssrc)[8];
      SB();
      const u32x4 af0 = LDSP(u32x4, apa)[0], af1 = LDSP(u32x4, apa)[16];
      SB();
      const f32x2 s2 = s4.xy + s4.zw, q2 = q4.xy + q4.zw;
      float s = s2[0] + s2[1], q = q2[0] + q2[1];
      SB();
      d[0] = mfma64(af0, bw[0], d[0], six);
      SB();
      s = swap16_add(s, q);
      SB();
      d[1] = mfma64(af0, bw[1], d[1], six);
      SB();
      s += dpp<XOR1>(s); s += dpp<XOR2>(s);
      SB();
      d[0] = mfma64(af1, bw[2], d[0], six);
      SB();
      s += dpp<XOR7>(s); s += dpp<XOR8>(s);
      SB();
      d[1] = mfma64(af1, bw[3], d[1], six);
      SB();
      const auto sq = __builtin_amdgcn_permlane16_swap(__float_as_uint(s), __float_as_uint(s), false, false);
      const float mean = u2f(sq[0]) * (1.f / 512.f);
      const float var = fmaf(u2f(sq[1]), 1.f / 512.f, -mean * mean);
      float ri = __builtin_amdgcn_rsqf(fmaxf(var, 0.f) + 1e-5f);
      PIN(ri); PIN(d[0]); PIN(d[1]);
      SB();
      float y0 = d[0][0] + d[0][1], y1 = d[1][2] + d[1][3], mr = -ri * mean;
      PIN(y0); PIN(y1); PIN(mr);
      float y = y0 + y1, cf = fmaf(mr, Afp, Bfp), rk = ri * -1.44269504f;
      PIN(y); PIN(cf); PIN(rk);
      y = __builtin_bit_cast(float, __builtin_amdgcn_update_dpp(__builtin_bit_cast(int, y), __builtin_bit_cast(int, y), XOR8, 0xa, 0xf, false));
      const float ck = cf * -1.44269504f;
      y = swap16_add(y, y);
      const float ex = __builtin_amdgcn_exp2f(fmaf(y, rk, ck));
      y = fmaf(y, ri, cf);
      const float gu = y * dpp<0x104>(y);
      *LDSP(u16, adst) = (u16)pack_bf(gu * __builtin_amdgcn_rcpf(1.f + ex), 0.f);
    }
    STAMP(18);
    LDS_BARRIER();
    STAMP(10);
    {
      const u32x4 af = *LDSP(u32x4, asrc);
      const f32x4 z = {0.f, 0.f, 0.f, 0.f};
      f32x4 d0 = mfma16(af, b2[0], z), d1 = mfma16(af, b2[1], z), d2 = mfma16(af, b2[2], z), d3 = mfma16(af, b2[3], z);
      asm volatile("" : "+v"(d0), "+v"(d1), "+v"(d2), "+v"(d3));
      const bool g1 = g & 1, g2 = g & 2;
      const float e0 = g1 ? d1[0] : d0[0], e1 = g1 ? d1[1] : d0[1], f0 = g1 ? d3[0] : d2[0], f1 = g1 ? d3[1] : d2[1];
      const u32x2 ov = {pack_bf(g2 ? f0 : e0, g2 ? f1 : e1), tag};
      __builtin_amdgcn_raw_buffer_store_b64(ov, wsr, ooff, near ^ 8192u, AUX_SC1);
      __builtin_amdgcn_raw_buffer_store_b64(ov, wsr, ooff, near, AUX_SC1);
    }
    asm volatile(".Lend_b:");
    STAMP(6);
    FINISH();
  }
  if (wave == 0) {
    if (!all_ok((sg[0][1] == tag) & (sg[0][3] == tag) & (sg[1][1] == tag) & (sg[1][3] == tag))) {
      u32x4 t[2];
      gather16<2>(wsr, G_MOD + (1024 + fd) * 8, 1536 * 8, tag, t);
      sg[0] = t[0]; sg[1] = t[1];
    }
    u32 xa;
    {
      due = t1 + P.N2 * 256;
      const int tn = notices();
      due = tn + P.X3 * 256;
      until(due);
    }
    const bool hb = g & 1;
#pragma nounroll
    for (int spin = 0; spin < SPIN_MAX; ++spin) {
      CFENCE();
      xa = __builtin_amdgcn_raw_buffer_load_b32(wsr, dup(G_XP) + near + (g >> 1) * 16384 + (c >> 2) * 2048 + (g & 1) * 1024 + fd * 2, 0, AUX_SC1);
      if (all_ok(xa != SENT)) break;
      __builtin_amdgcn_s_sleep(1);
    }
    float a0 = lo_f(xa), a1 = hi_f(xa);
    a0 += dpp<0x124>(a0); a1 += dpp<0x124>(a1);
    a0 += dpp<XOR8>(a0); a1 += dpp<XOR8>(a1);
    a0 = swap32_add(a0, a0); a1 = swap32_add(a1, a1);
    f32x2 xv = {rbf(lo_f(sx2) + rbf((hb ? hi_f(sg[0][0]) : lo_f(sg[0][0])) * rbf(a0))), rbf(hi_f(sx2) + rbf((hb ? hi_f(sg[0][2]) : lo_f(sg[0][2])) * rbf(a1)))};
    f32x2 gv = {hb ? hi_f(sg[1][0]) : lo_f(sg[1][0]), hb ? hi_f(sg[1][2]) : lo_f(sg[1][2])};
    PIN(xv); PIN(gv);
    if (wi == 0 && lane == 0) __builtin_amdgcn_raw_buffer_store_b32(tag, wsr, WS_EPOCH, 0, AUX_SC1);
    STAMP(10);
    due = max(due + P.X4 * 256, (int)(((u32)wall_clock64() - t0) << 8) + P.GX * 256);
    const bool last = lane < (NUP - 32) * 4;
    u32 o0 = dup(G_OP + (lane >> 2) * 4096 + fd * 8) + near, o2 = last ? o0 : OOB, tg2 = last ? tag : 0u;
    const rsrc_t rout = mk_rsrc(out, 2048u);
    u32 oo = (lane & 44) == 0 ? (g * 512 + fd) * 2 : OOB;
    const u32 pk = (c & 10) ? 0u : ((c & 4) ? 0x3f800000u : 0x00003f80u);
    u32x4 one = {(c & 1) ? 0u : pk, 0u, (c & 1) ? pk : 0u, 0u};
    PIN(o0); PIN(o2); PIN(tg2); PIN(oo); PIN(one);
    __builtin_amdgcn_raw_buffer_store_b32(pack_bf(xv[0], xv[1]), rout, oo, 0, AUX_SC1);
    u32 s1_ = 131072, s2_ = 262144;
    asm volatile("" : "+s"(s1_), "+s"(s2_));
    until(due);
    f32x4 sv;
#pragma nounroll
    for (int spin = 0; spin < SPIN_MAX; ++spin) {
      CFENCE();
      const u32x4 og[3] = {ld16<AUX_SC1>(wsr, o0, 0), ld16<AUX_SC1>(wsr, o0, s1_), ld16<AUX_SC1>(wsr, o2, s2_)};
      sv = f32x4{0.f, 0.f, 0.f, 0.f};
#pragma unroll
      for (int k = 0; k < 3; ++k) sv = mfma16(one, og[k], sv);
      SB();
      u32 bad = ((og[0][1] ^ tag) | (og[0][3] ^ tag) | (og[1][1] ^ tag)) | ((og[1][3] ^ tag) | (og[2][1] ^ tg2) | (og[2][3] ^ tg2));
      PIN(bad);
      SB();
      float s0 = sv[0] + dpp<0x124>(sv[0]), s1 = sv[1] + dpp<0x124>(sv[1]);
      PIN(s0); PIN(s1);
      s0 += dpp<XOR8>(s0); s1 += dpp<XOR8>(s1);
      PIN(s0); PIN(s1);
      __builtin_amdgcn_raw_buffer_store_b32(pack_bf(fmaf(gv[0], s0, xv[0]), fmaf(gv[1], s1, xv[1])), rout, oo, 0, AUX_SC1);
      SB();
      if (all_ok(bad == 0u)) break;
      __builtin_amdgcn_s_sleep(1);
    }
    STAMP(7);
    asm volatile(".Lend_c:");
  }
  FINISH();
}

extern "C" void kernel_launch(void* const* d_in, const int* in_sizes, int n_in,
                              void* d_out, int out_size, void* d_ws, size_t ws_size,
                              hipStream_t stream) {
  fused_kernel<<<256, 512, 0, stream>>>((const u16*)d_in[1], (const u16*)d_in[2], (const u16*)d_in[4], (const u16*)d_in[3], (const u16*)d_in[10], (const u16*)d_in[6], (const u16*)d_in[7],
                                        (unsigned char*)d_ws, (const u16*)d_in[0], (const u16*)d_in[9], (const u16*)d_in[8], (u16*)d_out);
}
```

```cpp
#include <hip/hip_runtime.h>
#include <stdint.h>


typedef unsigned int u32;
typedef unsigned short u16;
typedef unsigned long long u64;
typedef __attribute__((ext_vector_type(4))) unsigned u32x4;
typedef __attribute__((ext_vector_type(2))) unsigned u32x2;
typedef __attribute__((ext_vector_type(2))) __bf16 bf16x2;
typedef __attribute__((ext_vector_type(4))) float f32x4;
typedef __attribute__((ext_vector_type(2))) float f32x2;
typedef __amdgpu_buffer_rsrc_t rsrc_t;

constexpr int NH = 32, SK = 16384, DH = 128, WIN = 4096, FF = 1376;
struct Prm { int E0, ES, E1, E2, E2C, EU, TEND, DT_UP, L0, L1, X1, X2, X3, X4, NE, FE, M0, M1, M2, M3, M4, GS, GX, N1, N2, GA, WD, DT_WO, LW, LU, EW, PM, NF, NR, C0, A0, AG, AC, TS, TW, TK, TG, GB, B0, BG, BF, D0, DG, TC, LM, C1, F0, FG, FF_, PA, PD, QA, QD, RT, SA, SG, SC, W[9]; };
constexpr Prm PRM0 = {32, 20, 32, 32, 60, 60, 1968, 76, 12, 27, 75, 63, 100, 75, 3, 8, 0x2511f55b, 0x2b7425d5, 0x6017665, 0x17e0b9ec, 0x31944305, 62, 30, 29, 62, 50, 4, 106, 50, 28, 10, 0x0, 12, 14, 3, 36, 4, 4, 509, 215, 80, -2, 6, 69, 10, 8, 8, 6, 87, 120, 2, 65, 6, 4, 52, 14, 9, 18, 12, 45, 10, 22, 0x77777777, 0x77777777, 0x77777777, 0x77777777, 0x77777777, 0x44444477, 0x44444444, 0x44444444, 0x44444444};
constexpr int NL = 37;
constexpr int SK0 = 21, SK1 = 32;
constexpr int NCMIN = (NL - SK1 + SK0) * 8;
constexpr int WK = 8, WV = 5;
constexpr int WUSE = 12, LNS = 7;
constexpr int QP = 14, QD = 4;
constexpr int VQ = 6;
constexpr int NSL = (NL + 7) / 8;
constexpr int VS = VQ + NSL;
constexpr int MLS = VS + 2;
constexpr int VSIDE = MLS - 5, UPREP = MLS + 6;
constexpr int CPF = 17;
constexpr int SMO = 1;
constexpr int RVS = 5, RVP = 8, RVL = 14, RVU = 18;
constexpr int RPS = 23, RPP = 27;
constexpr int PRB = 12;
constexpr int TBS = 7, TBP = 10, TBL = 13, TBU = 17;
constexpr int window(int s, int w) {
  int n = w;
  for (int u = s - w + 1; u <= s; ++u) n += u == SMO ? 6 : (u == TBL || u == SK1 || u == NL + CPF || u == NL + RVL || u == NL + SK1 ? 1 : (u == QP || u == NL + VQ - 4 ? 2 : (u == NL + VSIDE ? 4 : 0)));
  return n;
}
constexpr int cons_end(int v) {
  const int a = v - WV + 1, c = (v - MLS) * 4;
  const int m = a < c ? a : c;
  return m < 0 ? 0 : (m > NL ? NL : m);
}
constexpr u32 col(const Prm& P, int j) { const int m[6] = {P.M0, P.M1, P.M2, P.M3, P.M4, P.PM}; return (u32)(m[j / 3] >> (j % 3 * 10) & 1023) << 10; }
constexpr u32 first_col(const Prm& P, int k) {
  if (k < 4) return col(P, 12 + k) | 1u << (24 + k);
  if (k < 6) return col(P, k - 4);
  if (k < 8) return col(P, 4 + k) | 1u << (16 + k);
  if (k < 11) return col(P, k - 1);
  return (col(P, 2) ^ col(P, 3) ^ col(P, 4) ^ col(P, 5) ^ col(P, 6) ^ col(P, 10) ^ col(P, 11)) | 0xf00000u;
}
constexpr bool one_to_one(const Prm& P) {
  u32 c[10] = {};
  for (int j = 0; j < 10; ++j) c[j] = col(P, j);
  for (int j = 0; j < 10; ++j) {
    if (c[j] == 0) return false;
    const u32 low = c[j] & (0u - c[j]);
    for (int k = j + 1; k < 10; ++k) if (c[k] & low) c[k] ^= c[j];
  }
  return true;
}
static_assert(one_to_one(PRM0), "every KB of the window exactly once");
struct Tab { u32 e[512]; };
constexpr Tab make_tab(const Prm& P) {
  Tab T{};
  for (int bid = 0; bid < 256; ++bid) {
    const int xcd = bid & 7, part = bid >> 4 & 3, i8 = (bid >> 3 & 1) | bid >> 6 << 1, wi = i8 * 8 + xcd;
    const bool has_mod = wi < 48, has_up = wi >= 21;
    const int cb0 = (i8 + P.C0) & 7, cb1 = (i8 + P.C1) & 7, ms = wi - 20;
    const int mi = wi < 8 ? wi : (wi < 16 ? 8 + ((xcd + 4) & 7) : (wi < 20 ? (wi - 14) * 8 + 3 : 16 + ms / 7 * 8 + ms % 7 + (ms % 7 >= 3 ? 1 : 0)));
    const int ui = xcd * 5 + (xcd > 5 ? xcd - 5 : 0) + (i8 == 6 ? 0 : (xcd >= 5 ? 1 : 0) + (i8 == 7 ? 4 : i8 - 2));
    const bool edge = i8 >= 6;
    const int wd = cb0 == 3 ? 0 : P.WD;
    const int e[4] = {P.E0 - 4 * wd + (cb0 == 3 ? P.ES : 0), P.E1 + (cb1 == 3 ? P.ES : 0), has_mod ? (wi < 16 ? P.E2C : P.E2) + ((mi & 7) == 3 ? P.ES : 0) : 0, has_up ? P.EU : 0};
    int tw[4] = {}, win[4] = {}, span[4] = {}, k0[4] = {};
    for (int k = 0; k < 4; ++k) {
      const int np = k == 0 ? 8 - wd : 8;
      tw[k] = np ? e[k] * P.TW / (16 * np) : 0;
      k0[k] = P.NF + tw[k] * np / 2 > P.TK ? P.NF + tw[k] * np / 2 : P.TK;
      span[k] = P.TEND - k0[k] - P.TG - (k == 0 ? (wd ? P.DT_WO : P.A0) : (k == 3 && has_up ? P.DT_UP : 0));
      win[k] = span[k] - (k == 3 && has_up ? P.GA + (edge ? P.LM : 0) : 0);
    }
    int n[4] = {NCMIN / 8, NCMIN / 8, NCMIN / 8, NCMIN / 8};
    for (int left = 128 - NCMIN / 2; left > 0; --left) {
      int best = -1;
      for (int k = 3; k >= 0; --k)
        if (n[k] < NL && (best < 0 || n[k] * win[best] < n[best] * win[k])) best = k;
      ++n[best];
    }
    const int c[4] = {0, n[0] - 1, n[0] + n[1] - 2, n[0] + n[1] + n[2] - 3};
    const int nl = n[part];
    const bool has_w = part < 2 || (part == 2 ? has_mod : has_up);
    const int blk = !has_w ? 0 : (part == 0 ? cb0 : (part == 1 ? xcd * 8 + cb1 : (part == 2 ? mi : ui)));
    T.e[2 * bid] = (u32)(c[part] | nl << 7 | blk << 13 | (has_w ? 1 : 0) << 19 | (part == 0 ? wd : 0) << 20) | (u32)tw[part] << 24;
    T.e[2 * bid + 1] = (u32)(span[part] * P.TS / (4 * (nl - 1)) | span[part] * (1024 - P.TS) / (4 * (nl - 1)) << 16);
  }
  return T;
}
constexpr int step_range(const Prm& P, bool mx_) {
  int mx = 0, mn = 1 << 20;
  const Tab T = make_tab(P);
  for (int bid = 0; bid < 256; ++bid) { const int n = T.e[2 * bid] >> 7 & 63; mx = n > mx ? n : mx; mn = n < mn ? n : mn; }
  return mx_ ? mx : mn;
}
extern "C" __attribute__((used, visibility("protected"))) __constant__ const Tab wg_tab = make_tab(PRM0);
static_assert(step_range(PRM0, true) <= NL && step_range(PRM0, false) >= NCMIN / 8, "a wave's key range does not fit its load steps");
static_assert(RVU < UPREP && UPREP + 1 < SK0 && QP + QD + 1 < SK0 && cons_end(SK0 - 1) == SK0 - WV && NL - SK1 == WV, "steps every wave runs");
constexpr bool timers_long(const Prm& P) {
  const Tab T = make_tab(P);
  for (int bid = 48; bid < 64; ++bid) if (SK0 + NL - (int)(T.e[2 * bid] >> 7 & 63) > RPS) return false;
  return true;
}
static_assert(timers_long(PRM0), "steps of the second report");
constexpr int NUP = FF / 32;
constexpr int SPIN_MAX = 1 << 17;

constexpr u32 WS_EPOCH = 0;
constexpr u32 G_TB = 1024;
constexpr u32 G_MOD = 4096;
constexpr u32 G_Q = G_MOD + 3072 * 8;
constexpr u32 G_ML = G_Q + 4096 * 8;
constexpr u32 G_U = G_ML + 256 * 16;
constexpr u32 G_P = 0;
constexpr u32 G_XP = G_P + 128 * 256;
constexpr u32 G_OP = G_XP + 8 * 512 * 4;
constexpr u32 G_NT = G_OP + 43 * 512 * 8;
constexpr u32 G_PB = G_OP + 44 * 512 * 8;
constexpr u32 G_VT = G_U + 2 * (G_PB + 8192);
constexpr u32 WS_END = G_VT + 2048;
__device__ __forceinline__ u32 dup(u32 o) { return G_U + ((o >> 13) << 14) + (o & 8191u); }
constexpr u32 SENT = 0xffffffffu;
constexpr u32 OOB = 0x40000000u;

constexpr int AUX_NT = 2, AUX_SC1 = 16;

__device__ __forceinline__ float lo_f(u32 v) { return __uint_as_float(v << 16); }
__device__ __forceinline__ float hi_f(u32 v) { return __uint_as_float(v & 0xffff0000u); }
__device__ __forceinline__ float rbf(float x) { return (float)(__bf16)x; }
__device__ __forceinline__ u32 pack_bf(float lo, float hi) {
  bf16x2 r; r[0] = (__bf16)lo; r[1] = (__bf16)hi; return __builtin_bit_cast(u32, r);
}
__device__ __forceinline__ float dot2(u32 k, u32 q, float acc) {
  return __builtin_amdgcn_fdot2_f32_bf16(__builtin_bit_cast(bf16x2, k), __builtin_bit_cast(bf16x2, q), acc, false);
}
template <int CTRL> __device__ __forceinline__ float dpp(float x) {
  return __builtin_bit_cast(float, __builtin_amdgcn_mov_dpp(__builtin_bit_cast(int, x), CTRL, 0xf, 0xf, true));
}
constexpr int XOR1 = 0xB1, XOR2 = 0x4E, XOR7 = 0x141, XOR8 = 0x128;
__device__ __forceinline__ float swap32_add(float a, float b) {
  auto t = __builtin_amdgcn_permlane32_swap(__float_as_uint(a), __float_as_uint(b), false, false);
  return __uint_as_float(t[0]) + __uint_as_float(t[1]);
}
__device__ __forceinline__ float swap16_add(float a, float b) {
  auto t = __builtin_amdgcn_permlane16_swap(__float_as_uint(a), __float_as_uint(b), false, false);
  return __uint_as_float(t[0]) + __uint_as_float(t[1]);
}
__device__ __forceinline__ float wave_sum(float x) {
  x += dpp<XOR1>(x); x += dpp<XOR2>(x); x += dpp<XOR7>(x); x += dpp<XOR8>(x);
  x = swap16_add(x, x);
  return swap32_add(x, x);
}
__device__ __forceinline__ float wave_max(float x) {
  x = fmaxf(x, dpp<XOR1>(x)); x = fmaxf(x, dpp<XOR2>(x)); x = fmaxf(x, dpp<XOR7>(x)); x = fmaxf(x, dpp<XOR8>(x));
  auto s = __builtin_amdgcn_permlane16_swap(__float_as_uint(x), __float_as_uint(x), false, false);
  x = fmaxf(__uint_as_float(s[0]), __uint_as_float(s[1]));
  auto t = __builtin_amdgcn_permlane32_swap(__float_as_uint(x), __float_as_uint(x), false, false);
  return fmaxf(__uint_as_float(t[0]), __uint_as_float(t[1]));
}
template <int CTRL> __device__ __forceinline__ float sel_add(float lo, float hi, bool up) {
  const float t1 = lo + dpp<CTRL>(lo), t2 = hi + dpp<CTRL>(hi);
  return up ? t2 : t1;
}
template <int I> __device__ __forceinline__ float bc16(float x) {
  return __builtin_bit_cast(float, __builtin_amdgcn_update_dpp(0, __builtin_bit_cast(int, x), 0x150 + I, 0xf, 0xf, false));
}
__device__ __forceinline__ float bcast16(float x, int i) {
  switch (i) {
    case 0: return bc16<0>(x); case 1: return bc16<1>(x); case 2: return bc16<2>(x); case 3: return bc16<3>(x);
    case 4: return bc16<4>(x); case 5: return bc16<5>(x); case 6: return bc16<6>(x); case 7: return bc16<7>(x);
    case 8: return bc16<8>(x); case 9: return bc16<9>(x); case 10: return bc16<10>(x); case 11: return bc16<11>(x);
    case 12: return bc16<12>(x); case 13: return bc16<13>(x); case 14: return bc16<14>(x); default: return bc16<15>(x);
  }
}
__device__ __forceinline__ float silu_f(float x) { return x * __builtin_amdgcn_rcpf(1.f + __expf(-x)); }

template <int AUX> __device__ __forceinline__ u32x4 ld16(rsrc_t r, u32 voff, u32 soff) {
  return __builtin_amdgcn_raw_buffer_load_b128(r, voff, soff, AUX);
}
template <int AUX> __device__ __forceinline__ u32x2 ld8(rsrc_t r, u32 voff, u32 soff) {
  return __builtin_amdgcn_raw_buffer_load_b64(r, voff, soff, AUX);
}
__device__ __forceinline__ void st8_sc1(rsrc_t r, u32 voff, u32 v, u32 tag) {
  __builtin_amdgcn_raw_buffer_store_b64(u32x2{v, tag}, r, voff, 0, AUX_SC1);
}
__device__ __forceinline__ void st16_sc1(rsrc_t r, u32 voff, u32 v0, u32 v1, u32 tag) {
  __builtin_amdgcn_raw_buffer_store_b128(u32x4{v0, tag, v1, tag}, r, voff, 0, AUX_SC1);
}
__device__ __forceinline__ rsrc_t mk_rsrc(const void* p, u32 bytes) {
  return __builtin_amdgcn_make_buffer_rsrc(const_cast<void*>(p), 0, bytes, 0x00020000);
}
#define WAIT_VM(n) __builtin_amdgcn_s_waitcnt(((n) & 15) | (((n) >> 4) << 14) | (7 << 4) | (15 << 8))
#define SB() __builtin_amdgcn_sched_barrier(0)
#define CFENCE() asm volatile("" ::: "memory")
__device__ __forceinline__ bool all_ok(bool ok) { return __builtin_amdgcn_ballot_w64(!ok) == 0ull; }

template <int N> __device__ __forceinline__ void gather16(rsrc_t ws, u32 voff, u32 stride, u32 tag, u32x4 (&g)[N]) {
#pragma nounroll
  for (int spin = 0; spin < SPIN_MAX; ++spin) {
    CFENCE();
    bool ok = true;
#pragma unroll
    for (int i = 0; i < N; ++i) g[i] = ld16<AUX_SC1>(ws, voff + i * stride, 0);
#pragma unroll
    for (int i = 0; i < N; ++i) ok &= (g[i][1] == tag) & (g[i][3] == tag);
    if (all_ok(ok)) break;
    __builtin_amdgcn_s_sleep(8);
  }
}
template <int N> __device__ __forceinline__ void gather8(rsrc_t ws, u32 voff, u32 stride, u32 tag, u32 (&g)[N], bool act = true) {
#pragma nounroll
  for (int spin = 0; spin < SPIN_MAX; ++spin) {
    CFENCE();
    bool ok = true;
    u32x2 t[N];
#pragma unroll
    for (int i = 0; i < N; ++i) t[i] = ld8<AUX_SC1>(ws, voff + i * stride, 0);
#pragma unroll
    for (int i = 0; i < N; ++i) { ok &= (t[i][1] == tag); g[i] = t[i][0]; }
    if (all_ok(ok | !act)) break;
    __builtin_amdgcn_s_sleep(8);
  }
}

struct Smem {
  float red[8][128];
  float ml[8][2];
  float r2[64 * 20];
  u16 hb[3 * 1024];
  float stat[8][64];
  float red2[8][128];
  u32 dummy[64];
  u32x2 bc, bc2;
  u32 bc3, bc4;
  u32x4 zero[4];
  float pl[4][8];
  u32 mflag[8];
  u32x4 wbuf[8][8][64];
  __attribute__((aligned(256))) u16 w2b[32][512];
  u16 actl[2][32];
};

#define PIN(x) asm volatile("" : "+v"(x))
#define LDS_BARRIER() do { __builtin_amdgcn_s_waitcnt(0xc07f); __builtin_amdgcn_s_barrier(); CFENCE(); } while (0)
#define WAVE_SYNC() do { __builtin_amdgcn_fence(__ATOMIC_RELEASE, "wavefront"); __builtin_amdgcn_wave_barrier(); __builtin_amdgcn_fence(__ATOMIC_ACQUIRE, "wavefront"); } while (0)

__device__ __forceinline__ void dma16(rsrc_t r, u32 voff, u32 soff, u32 lds_dst) {
  u32 keep;
  asm volatile("s_mov_b32 %0, m0\n\ts_mov_b32 m0, %4\n\ts_nop 4\n\tbuffer_load_dwordx4 %1, %2, %3 offen nt lds\n\ts_mov_b32 m0, %0"
               : "=&s"(keep) : "v"(voff), "s"(r), "s"(soff), "s"(lds_dst) : "memory");
}
__device__ __forceinline__ void ld16_into(u32x4& d, rsrc_t r, u32 voff, u32 soff) {
  asm volatile("s_nop 4\n\tbuffer_load_dwordx4 %0, %1, %2, %3 offen nt" : "+v"(d) : "v"(voff), "s"(r), "s"(soff) : "memory");
}
__device__ __forceinline__ u32 perm_lo(u32 a, u32 b) { return __builtin_amdgcn_perm(b, a, 0x05040100u); }
__device__ __forceinline__ u32 perm_hi(u32 a, u32 b) { return __builtin_amdgcn_perm(b, a, 0x07060302u); }
__device__ __forceinline__ float rdl(float x, int l) { return __int_as_float(__builtin_amdgcn_readlane(__float_as_int(x), l)); }

typedef __attribute__((ext_vector_type(8))) __bf16 bf16x8;
__device__ __forceinline__ f32x4 mfma16(u32x4 a, u32x4 b, f32x4 c) {
  return __builtin_amdgcn_mfma_f32_16x16x32_bf16(__builtin_bit_cast(bf16x8, a), __builtin_bit_cast(bf16x8, b), c, 0, 0, 0);
}
typedef __attribute__((ext_vector_type(8))) unsigned u32x8;
typedef __attribute__((ext_vector_type(16))) __bf16 bf16x16;
__device__ __forceinline__ f32x4 mfma64(u32x4 a, u32x8 b, f32x4 c, u32 idx) {
  return __builtin_amdgcn_smfmac_f32_16x16x64_bf16(__builtin_bit_cast(bf16x8, a), __builtin_bit_cast(bf16x16, b), c, idx, 0, 0);
}
__device__ __forceinline__ float sum8(const float* p) {
  const f32x4 a = *reinterpret_cast<const f32x4*>(p), b = *reinterpret_cast<const f32x4*>(p + 4);
  return ((a[0] + b[0]) + (a[1] + b[1])) + ((a[2] + b[2]) + (a[3] + b[3]));
}
__device__ __forceinline__ float ln_ri(float var) {
  return __builtin_amdgcn_rcpf(rbf(__builtin_amdgcn_sqrtf(rbf(rbf(fmaxf(var, 0.f)) + 1e-5f))));
}
__device__ __forceinline__ void ln_stats2(const float (&x0)[8], const float (&x1)[8], float& mb0, float& ri0, float& mb1, float& ri1) {
  const float k0 = rdl(x0[0], 0), k1 = rdl(x1[0], 0);
  float s0 = 0.f, s1 = 0.f, q0 = 0.f, q1 = 0.f;
#pragma unroll
  for (int k = 0; k < 8; ++k) {
    const float d0 = x0[k] - k0, d1 = x1[k] - k1;
    s0 += d0; s1 += d1; q0 = fmaf(d0, d0, q0); q1 = fmaf(d1, d1, q1);
  }
  float t = swap16_add(swap32_add(s0, s1), swap32_add(q0, q1));
  t += dpp<XOR1>(t); t += dpp<XOR2>(t); t += dpp<XOR7>(t); t += dpp<XOR8>(t);
  auto u = __builtin_amdgcn_permlane16_swap(__float_as_uint(t), __float_as_uint(t), false, false);
  const float ms = __uint_as_float(u[0]) * (1.f / 512.f), mq = __uint_as_float(u[1]) * (1.f / 512.f);
  const float ri = ln_ri(fmaf(-ms, ms, mq));
  mb0 = rbf(k0 + rdl(ms, 0)); mb1 = rbf(k1 + rdl(ms, 32));
  ri0 = rdl(ri, 0); ri1 = rdl(ri, 32);
}
template <int I> struct IC { static constexpr int v = I; };
template <int I, int N, class F> __device__ __forceinline__ void sfor(F&& f) {
  if constexpr (I < N) { if (f(IC<I>{})) sfor<I + 1, N>(f); }
}
typedef __attribute__((ext_vector_type(4))) short s16x4;
typedef __attribute__((address_space(3))) s16x4* lds_s16x4p;
__device__ __forceinline__ u32x2 tr_read(u32 lds_addr) {
  return __builtin_bit_cast(u32x2, __builtin_amdgcn_ds_read_tr16_b64_v4i16((lds_s16x4p)lds_addr));
}
__device__ __forceinline__ u32 next_tag(u32 e) { return ((e | 0x40004000u) + 1u) & ~0x40004000u; }
__device__ __forceinline__ float u2f(u32 v) { return __uint_as_float(v); }
#define LDSP(T, a) ((__attribute__((address_space(3))) T*)(a))
template <class T> __device__ __forceinline__ u32 lds_addr(T* p) { u32 a = (u32)(uintptr_t)p; PIN(a); return a; }

#define TAB_LOAD "s_add_u32 s30, s28, wg_tab@rel32@lo+4\ns_addc_u32 s31, s29, wg_tab@rel32@hi+12\nglobal_load_dwordx2 %[te], %[va], s[30:31]\n"
#define FC(k) ".Lfc" #k
#define FX(r, bit, k) "s_bitcmp1_b32 %[" #r "], " #bit "\ns_cselect_b32 %[sb], " FC(k) ", 0\ns_xor_b32 %[sa], %[sa], %[sb]\n"
__global__ void __launch_bounds__(512, 2) fused_kernel(const u16* kc_, const u16* vc_, const u16* wo_, const u16* wq_, const u16* wmod_, const u16* w1_, const u16* w3_,
                                                       unsigned char* ws_, const u16* x_, const u16* cnd_, const u16* w2_, u16* out_) {
  __shared__ __attribute__((aligned(256))) Smem sm;
  const u64 a_ka = (u64)__builtin_amdgcn_kernarg_segment_ptr();
  const int bid = blockIdx.x, tid = threadIdx.x;
  u64 wa, t64; u32x2 te; int tid0;
  u32 ar[14];
  u32 tch2, sa_, sb_, va_;
  u32x4 kr[NL], vr[NL];
  asm volatile(".set .Lfc0, %0\n.set .Lfc1, %1\n.set .Lfc2, %2\n.set .Lfc3, %3\n.set .Lfc4, %4\n.set .Lfc5, %5\n.set .Lfc6, %6\n.set .Lfc7, %7\n.set .Lfc8, %8\n.set .Lfc9, %9\n.set .Lfc10, %10\n.set .Lfc11, %11"
    :: "n"(first_col(PRM0, 0)), "n"(first_col(PRM0, 1)), "n"(first_col(PRM0, 2)), "n"(first_col(PRM0, 3)), "n"(first_col(PRM0, 4)), "n"(first_col(PRM0, 5)),
       "n"(first_col(PRM0, 6)), "n"(first_col(PRM0, 7)), "n"(first_col(PRM0, 8)), "n"(first_col(PRM0, 9)), "n"(first_col(PRM0, 10)), "n"(first_col(PRM0, 11)));
  asm volatile(
    "s_load_dwordx16 s[4:19], %[ka], 0x0\ns_load_dwordx8 s[20:27], %[ka], 0x40\n"
    "s_memrealtime %[t]\n"
    "v_lshlrev_b32 %[va], 3, %[bid]\n"
    "s_getpc_b64 s[28:29]\n.Lcpf_first:\n"
    TAB_LOAD
    "s_cmp_gt_u32 %[bid], 15\ns_cbranch_scc1 .Lno_cpf\n"
    "s_mov_b32 %[sa], .Lend_a-.Lcpf_first\ns_max_i32 %[sa], %[sa], .Lend_b-.Lcpf_first\ns_max_i32 %[sa], %[sa], .Lend_c-.Lcpf_first\ns_max_i32 %[sa], %[sa], .Lend_d-.Lcpf_first\ns_max_i32 %[sa], %[sa], .Lend_e-.Lcpf_first\n"
    "v_lshlrev_b32 %[va], 6, %[tid]\nv_min_u32 %[va], %[sa], %[va]\nglobal_load_dword %[pf], %[va], s[28:29]\n"
    ".Lno_cpf:\n"
    "v_readfirstlane_b32 %[t0], %[tid]\nv_and_b32 %[va], 63, %[tid]\n"
    "s_mov_b32 %[sa], " FC(11) "\n"
    FX(bid, 0, 0) FX(bid, 1, 1) FX(bid, 2, 2) FX(bid, 3, 3) FX(bid, 4, 4) FX(bid, 5, 5) FX(bid, 6, 6) FX(bid, 7, 7) FX(t0, 6, 8) FX(t0, 7, 9) FX(t0, 8, 10)
    "v_lshl_add_u32 %[va], %[va], 4, %[sa]\n"
    "s_waitcnt lgkmcnt(0)\nglobal_load_dwordx4 %[k0], %[va], s[4:5] nt\nglobal_load_dwordx4 %[v0], %[va], s[6:7] nt\n"
    "s_bitcmp1_b32 %[t0], 6\ns_cselect_b64 s[14:15], s[16:17], s[14:15]\n"
    "s_bitcmp1_b32 %[bid], 4\ns_cselect_b64 s[8:9], s[10:11], s[8:9]\ns_cselect_b64 s[12:13], s[14:15], s[12:13]\ns_bitcmp1_b32 %[bid], 5\ns_cselect_b64 %[wa], s[12:13], s[8:9]"
    : "=&{s4}"(ar[0]), "=&{s5}"(ar[1]), "=&{s6}"(ar[2]), "=&{s7}"(ar[3]), "=&{s18}"(ar[4]), "=&{s19}"(ar[5]), "=&{s20}"(ar[6]), "=&{s21}"(ar[7]), "=&{s22}"(ar[8]), "=&{s23}"(ar[9]),
      "=&{s24}"(ar[10]), "=&{s25}"(ar[11]), "=&{s26}"(ar[12]), "=&{s27}"(ar[13]), [wa] "=&s"(wa), [t0] "=&s"(tid0), [t] "=&s"(t64), [te] "=&v"(te), [sa] "=&s"(sa_), [sb] "=&s"(sb_),
      [va] "=&v"(va_), [pf] "=&v"(tch2), [k0] "=&v"(kr[0]), [v0] "=&v"(vr[0])
    : [ka] "s"(a_ka), [bid] "s"(bid), [tid] "v"(tid) : "s8", "s9", "s10", "s11", "s12", "s13", "s14", "s15", "s16", "s17", "s28", "s29", "s30", "s31", "scc", "memory");
  asm volatile("" : "+s"(ar[0]), "+s"(ar[1]), "+s"(ar[2]), "+s"(ar[3]), "+s"(ar[4]), "+s"(ar[5]), "+s"(ar[6]), "+s"(ar[7]), "+s"(ar[8]), "+s"(ar[9]), "+s"(ar[10]), "+s"(ar[11]), "+s"(ar[12]), "+s"(ar[13]));
  asm volatile("" : "+s"(wa), "+s"(t64), "+s"(tid0), "+s"(sa_));
  const u32 tka = (u32)wall_clock64();
  __builtin_assume(tid >= 0 && tid < 512 && tid0 >= 0 && tid0 < 512 && bid >= 0 && bid < 256);
  constexpr Prm P = PRM0;
  const u32 t0 = tka + min(max((int)(tka - (u32)t64) - P.SA, 0) * P.SG >> 4, P.SC);
  const int TEND = P.TEND;
  const int xcd = bid & 7, part = bid >> 4 & 3;
  const int lane = tid & 63, wave = tid0 >> 6;
  SB();
  if ((bid >> 4) == 2) {
    const u32 o = tid < 384 ? G_U + (bid & 15) * 6144 + tid * 16 : OOB;
    __builtin_amdgcn_raw_buffer_store_b128(u32x4{SENT, SENT, SENT, SENT}, mk_rsrc((void*)(ar[4] | (u64)ar[5] << 32), WS_END), o, 0, AUX_SC1);
  }
  SB();
  const bool p3 = part == 3;
  u32 pitch = (u32)(0x005600c001000020ull >> (part * 16) & 0xffff) * 32u;
  const int sh = p3 ? 2 : 3;
  u32 wv0 = (u32)((p3 ? (wave >> 1) * 128 : wave * 64) + (part == 0 ? xcd * 512 : 0) + (lane >> sh)) * pitch + ((lane & (p3 ? 3 : 7)) ^ (lane >> (p3 ? 5 : 3))) * 16u;
  const u32 wimg = (u32)(uintptr_t)(&sm.wbuf[0][0][0]);
  u32 wsz = (part == 0 ? 4096u : 512u) * pitch, wld = wimg + (p3 ? (wave & 1) * 32768 + (wave >> 1) * 8192 : wave * 8192);
  PIN(wv0);
  asm volatile("" : "+s"(wsz), "+s"(wld), "+s"(pitch));
  auto karg = [&](int i) { return ar[2 * i] | (u64)ar[2 * i + 1] << 32; };
  const u64 a_kc = karg(0), a_vc = karg(1);
  unsigned char* ws = (unsigned char*)karg(2);
  const u16 *x = (const u16*)karg(3), *cnd = (const u16*)karg(4), *w2 = (const u16*)karg(5);
  u16* out = (u16*)karg(6);
  const rsrc_t wsr = mk_rsrc(ws, WS_END);
  asm volatile("s_waitcnt vmcnt(3)\n\ts_cmp_eq_u32 %1, 2\n\ts_cbranch_scc1 1f\n\ts_waitcnt vmcnt(2)\n1:" : "+v"(te) : "s"(bid >> 4) : "scc");
  const u32 te0 = __builtin_amdgcn_readfirstlane(te[0]), te1 = __builtin_amdgcn_readfirstlane(te[1]);
  const int bl0 = te0 & 127, nl = te0 >> 7 & 63, blk = te0 >> 13 & 63;
  const bool has_w = te0 >> 19 & 1;
  const rsrc_t rw = mk_rsrc((const void*)wa, wsz);
  const u32 wstep = pitch << (6 - sh), wflip = (p3 || part == 0) ? 32u : 0u, wflip0 = part == 0 ? 32u : 0u;
  u32 wso = blk << (sh + 4);
  int wleft = 8;
  auto piece = [&]() {
    dma16(rw, wv0, wso, wld);
    wso += wstep; wld += 1024;
    wv0 ^= (wleft & 1) ? wflip : wflip0;
    --wleft;
  };
  SB();
  const int b = bid >> 3 & 1, head = xcd * 4 + (bid >> 6 ^ 3);
  const int g = lane >> 4, c = lane & 15;
  const int i8 = b | bid >> 6 << 1, wi = i8 * 8 + xcd;
  const bool is_up = p3 && has_w, modc = part == 2 && wi < 16;
  const size_t base = ((size_t)(b * NH + head) * SK + (SK - WIN)) * DH;
  const rsrc_t rk = mk_rsrc((const u16*)a_kc + base, WIN * DH * 2), rv = mk_rsrc((const u16*)a_vc + base, WIN * DH * 2);
  int tau = te1 & 0xffff;
  const int tauw = te0 >> 24 << 7, wdef = te0 >> 20 & 15;
  int due = P.NF * 256, now = 0;
  auto gate = [&]() {
    while (now < due) { now = (int)(((u32)wall_clock64() - t0) << 8); if (now < due) __builtin_amdgcn_s_sleep(1); }
    due += tau;
  };
  auto until = [&](int t) { while ((int)(((u32)wall_clock64() - t0) << 8) < t); };
  const u32 pslot = dup(G_P + (head & 15) * 2048 + (b * 4 + part) * 16);
  const u32 die = xcd >> 2;
  const bool voter = p3 && i8 == 1, timer = p3 && i8 < 2;
  int vote = 0, lag = 0, shift = 0;
  const u32 pbo = dup(G_PB) + wave * 1152;
  u32 near = 0;
  u32x2 ep; u32 xs0, xs1, tch1; u32x4 xr0, xr1;
  const rsrc_t rx = mk_rsrc(part == 2 ? cnd : x, (part == 1 || (part == 2 && has_w)) ? 2048u : 0u);
  auto small_ops = [&]() {
    ep = ld8<AUX_SC1>(wsr, wave == 7 ? WS_EPOCH : OOB, 0);
    xs0 = __builtin_amdgcn_raw_buffer_load_b16(rx, tid * 2, 0, 0); xs1 = __builtin_amdgcn_raw_buffer_load_b16(rx, tid * 2, 1024, 0);
    xr0 = ld16<0>(rx, part == 1 ? lane * 16 : OOB, 0); xr1 = ld16<0>(rx, part == 1 ? lane * 16 : OOB, 1024);
    tch1 = __builtin_amdgcn_raw_buffer_load_b32(mk_rsrc(w2 + (size_t)blk * 32 * 512, is_up ? 64u : 0u), tid == 384 ? 0 : OOB, 0, AUX_NT);
  };
  if (has_w) {
    if (modc) now = 1 << 30;
#pragma nounroll
    while (wleft > wdef) { gate(); due += tauw - tau; piece(); }
    now = 0;
  }
  due = max(due, P.TK * 256);
  SB();
  u32 sot = sa_ & 0xffc00u;
  {
    const u32 s1 = (bl0 + lane - 1 - (lane >= SK0 ? NL - nl : 0)) ^ (124 + part);
#pragma unroll
    for (int k = 0; k < 7; ++k) sot ^= (u32)-(int)(s1 >> k & 1u) & col(P, k);
    PIN(sot);
  }
  u32 tag = 0;
  u32x4 bf[8];
  auto wo_prep = [&]() {
    const u32 wi_ = wimg + wave * 8192;
    const int q = (lane >> 2) & 3, pp = lane & 3;
#pragma unroll
    for (int s = 0; s < 2; ++s) {
#pragma unroll
      for (int t = 0; t < 4; ++t) {
        const int r0 = s * 32 + g * 8 + q, r1 = r0 + 4, ch = t * 2 + (pp >> 1);
        const u32x2 lo = tr_read(wi_ + r0 * 128 + ((ch ^ (r0 & 7)) * 16) + (pp & 1) * 8), hi = tr_read(wi_ + r1 * 128 + ((ch ^ (r1 & 7)) * 16) + (pp & 1) * 8);
        bf[s * 4 + t] = u32x4{lo[0], lo[1], hi[0], hi[1]};
      }
    }
  };
  auto gemv_go = [&](u32 hh) {
    sm.hb[tid] = (u16)hh; sm.hb[512 + tid] = (u16)(hh >> 16);
    WAVE_SYNC();
    const u32x4* a = (c & 11) == 0 ? reinterpret_cast<const u32x4*>(&sm.hb[(c >> 2) * 512 + wave * 64 + g * 8]) : &sm.zero[0];
    const u32x4 a0 = a[0], a1 = a[(c & 11) == 0 ? 4 : 0];
    const f32x4 z = {0.f, 0.f, 0.f, 0.f};
    f32x4 d[4];
#pragma unroll
    for (int t = 0; t < 4; ++t) d[t] = mfma16(a0, bf[t], z);
#pragma unroll
    for (int t = 0; t < 4; ++t) d[t] = mfma16(a1, bf[4 + t], d[t]);
    if (lane < 32) {
#pragma unroll
      for (int t = 0; t < 4; ++t) sm.r2[(t * 16 + c) * 20 + g * 8 + wave] = d[t][0];
    }
  };
  auto mod_stage = [&]() {
    wo_prep();
    gemv_go(pack_bf(silu_f(lo_f(xs0)), silu_f(lo_f(xs1))));
    __builtin_amdgcn_s_waitcnt(0xc07f);
    if (lane == 0) *LDSP(volatile u32, (u32)(uintptr_t)&sm.mflag[wave]) = 1u;
    if (wave == 7) {
#pragma nounroll
      for (int spin = 0; spin < SPIN_MAX; ++spin) {
        const u32 f = *LDSP(volatile u32, (u32)(uintptr_t)&sm.mflag[lane & 7]);
        if (all_ok(f != 0u)) break;
        __builtin_amdgcn_s_sleep(1);
      }
      CFENCE();
      st8_sc1(wsr, G_MOD + (blk * 64 + lane) * 8, pack_bf(sum8(&sm.r2[lane * 20]), sum8(&sm.r2[lane * 20 + 8])), tag);
    }
  };
  if (lane < 4) sm.zero[lane] = u32x4{0u, 0u, 0u, 0u};
  if (part == 2 && has_w) {
    if (tid < 8) sm.mflag[tid] = 0u;
    LDS_BARRIER();
    if (__builtin_expect(modc, 0)) {
      small_ops();
      asm volatile("s_waitcnt vmcnt(0)" ::: "memory");
      tag = next_tag(__builtin_amdgcn_readfirstlane(ep[0]));
      mod_stage();
      __builtin_amdgcn_s_barrier();
    }
  }
  SB();
  const int fd = (xcd * 8 + i8) * 8 + (lane & 3) * 2;
  const int ub = wave >> 2, ud = (wave & 3) * 128 + c * 8 + g * 2;
  u32x4 sg[3]; u32 sx2;
  auto side_loads = [&]() {
    const bool fin = part == 1 && wave == 0;
    sg[0] = ld16<AUX_SC1>(wsr, is_up ? G_MOD + (1024 + ud) * 8 : (fin ? G_MOD + (1024 + fd) * 8 : OOB), 0);
    sg[1] = ld16<AUX_SC1>(wsr, is_up ? G_MOD + (1536 + ud) * 8 : (fin ? G_MOD + (2560 + fd) * 8 : OOB), 0);
    sg[2] = ld16<AUX_SC1>(wsr, is_up ? G_MOD + (2048 + ud) * 8 : OOB, 0);
    sx2 = __builtin_amdgcn_raw_buffer_load_b32(mk_rsrc(x, (is_up || fin) ? 2048u : 0u), is_up ? ub * 1024 + ud * 2 : (g & 1) * 1024 + fd * 2, 0, 0);
  };
  auto side_pin = [&]() { PIN(sg[0]); PIN(sg[1]); PIN(sg[2]); PIN(sx2); };
  const u32 mlo = G_ML + (head * 2 + b) * 64 + (lane & 3) * 16;
  float fmrg = 0.f;
  auto merge_factor = [&](const u32x4& ml) {
    const float mi = u2f(ml[0]);
    float M = fmaxf(mi, dpp<XOR1>(mi)); M = fmaxf(M, dpp<XOR2>(M));
    const float e = __expf(mi - M);
    float L = e * u2f(ml[2]);
    L += dpp<XOR1>(L); L += dpp<XOR2>(L);
    fmrg = rdl(e * __builtin_amdgcn_rcpf(L), part);
  };
  f32x2 ga = {0.f, 0.f}, xin = {0.f, 0.f}, cc1 = {0.f, 0.f};
  float Af = 0.f, Bf = 0.f;
  const int hpos = ub * 512 + (ud & ~15) + (g & 1) * 8 + (c & 1) * 4 + (g >> 1) * 2;
  auto up_prep = [&]() {
    if (!all_ok((sg[0][1] == tag) & (sg[0][3] == tag) & (sg[1][1] == tag) & (sg[1][3] == tag) & (sg[2][1] == tag) & (sg[2][3] == tag))) gather16<3>(wsr, G_MOD + (1024 + ud) * 8, 512 * 8, tag, sg);
    ga = f32x2{ub ? hi_f(sg[0][0]) : lo_f(sg[0][0]), ub ? hi_f(sg[0][2]) : lo_f(sg[0][2])};
    cc1 = f32x2{rbf(1.f + (ub ? hi_f(sg[2][0]) : lo_f(sg[2][0]))), rbf(1.f + (ub ? hi_f(sg[2][2]) : lo_f(sg[2][2])))};
    xin = f32x2{lo_f(sx2), hi_f(sx2)};
    u32* hp = reinterpret_cast<u32*>(&sm.hb[hpos]);
    hp[512] = pack_bf(cc1[0], cc1[1]);
    hp[1024] = ub ? perm_hi(sg[1][0], sg[1][2]) : perm_lo(sg[1][0], sg[1][2]);
  };
  u32x8 bw[4];
  auto up_frags = [&]() {
    const int pp = lane & 3;
    const u32 a = wimg + (pp & 1) * 32768 + ((pp >> 1) * 32 + g * 8 + ((lane >> 2) & 3)) * 64 + (((wave >> 1) ^ ((g & 1) | (pp & 2))) * 16) + (wave & 1) * 8;
#pragma unroll
    for (int j = 0; j < 4; ++j) {
      const u32x2 r0 = tr_read(a + j * 4096), r1 = tr_read(a + j * 4096 + 256), r2 = tr_read(a + j * 4096 + 16384), r3 = tr_read(a + j * 4096 + 16640);
      bw[j] = u32x8{r0[0], r0[1], r1[0], r1[1], r2[0], r2[1], r3[0], r3[1]};
    }
  };
  u32 wfa[4];
  auto wo_frags = [&](int s0) {
#pragma unroll
    for (int t = 0; t < 4; ++t) {
#pragma unroll
      for (int s = s0; s < 2; ++s) {
        const u32x2 lo = tr_read(wfa[t] + s * 4096), hi = tr_read(wfa[t ^ 1] + s * 4096 + 256);
        bw[t][s * 4] = lo[0]; bw[t][s * 4 + 1] = lo[1]; bw[t][s * 4 + 2] = hi[0]; bw[t][s * 4 + 3] = hi[1];
      }
    }
  };
  const bool par = lane & 8;
  auto limit = [&](auto sc) {
    constexpr int s = sc.v;
    WAIT_VM(window(s, 1 + (PRM0.W[s >> 3] >> (4 * (s & 7)) & 7)));
    SB();
  };
  auto sample = [&]() { if (timer) lag += (int)(((u32)wall_clock64() - t0) << 8) - due + ((int)__builtin_amdgcn_s_getreg(7 | (3 << 11)) + 1) * tau; };
  auto report = [&](u32 dst) {
    if (timer) {
      if (lane == 0) sm.mflag[wave] = vote * 65536 + min(max(lag >> 8, 0), 4000);
      lag = 0;
      LDS_BARRIER();
      if (wave == 7) {
        int s = sm.mflag[lane & 7];
        s += __builtin_amdgcn_mov_dpp(s, XOR1, 0xf, 0xf, true); s += __builtin_amdgcn_mov_dpp(s, XOR2, 0xf, 0xf, true); s += __builtin_amdgcn_mov_dpp(s, XOR7, 0xf, 0xf, true);
        if (lane < 8) st8_sc1(wsr, dst + lane * 128 + wi * 8, s, tag);
      }
    }
    SB();
  };
  float l0 = 0.f, l1 = 0.f;
  u32x2 gmd[2], tbg;
  int t1 = 0;
#pragma unroll
  for (int i = SK0; i < SK1; ++i) kr[i] = u32x4{0u, 0u, 0u, 0u};
  const int first = SK0 + NL - nl;
  auto left_out = [&](int i) { int f = first; asm volatile("" : "+s"(f)); return i < f; };
  sfor<1, NL>([&](auto ic) {
    constexpr int i = ic.v;
    if constexpr (i >= SK0 && i < SK1) { if (left_out(i)) return true; }
    const u32 so = __builtin_amdgcn_readlane(sot, i);
    gate();
    if constexpr ((i >= TBS && i <= TBP) || (i >= RPS && i <= RPP)) sample();
    SB();
    if constexpr (i >= SK0 && i < SK1) ld16_into(kr[i], rk, lane * 16, so); else kr[i] = ld16<AUX_NT>(rk, lane * 16, so);
    if constexpr (i == SMO) small_ops();
    if constexpr (i == SMO + 1) { if (voter && (xcd & 3) == 0) __builtin_amdgcn_raw_buffer_store_b32(0u, wsr, pbo + (lane >> 5) * 8192 + (lane & 31) * 4, 0, AUX_SC1); }
    if constexpr (i == SK1) tbg = ld8<AUX_SC1>(wsr, wave == 7 ? G_VT + xcd * 128 + c * 8 : OOB, 0);
    if constexpr (i == TBL) tbg = ld8<AUX_SC1>(wsr, wave == 7 ? G_TB + xcd * 128 + c * 8 : OOB, 0);
    if constexpr (i == QP) { gmd[0] = ld8<AUX_SC1>(wsr, part == 1 ? G_MOD + tid * 8 : OOB, 0); gmd[1] = ld8<AUX_SC1>(wsr, part == 1 ? G_MOD + tid * 8 : OOB, 4096); }
    SB();
    SB();
    if (i >= 4) limit(ic);
    if (i == TBP) {
      if (timer) {
        if (lane == 0) sm.mflag[wave] = lag;
        lag = 0;
        LDS_BARRIER();
        if (wave == 7) {
          int s = sm.mflag[lane & 7];
          s += __builtin_amdgcn_mov_dpp(s, XOR1, 0xf, 0xf, true); s += __builtin_amdgcn_mov_dpp(s, XOR2, 0xf, 0xf, true); s += __builtin_amdgcn_mov_dpp(s, XOR7, 0xf, 0xf, true);
          if (lane < 8) st8_sc1(wsr, G_TB + lane * 128 + wi * 8, t0 << 10 | (u32)min(s / (2048 * (TBP - TBS + 1)), 1023), tag);
        }
      }
      SB();
    }
    if (i == PRB) {
      if (voter) {
        u64 ta, tb, tc; u32 dm;
        const u32 pa = pbo + (wave & 1) * 8192;
        asm volatile("s_memtime %0\n\ts_waitcnt lgkmcnt(0)\n\ts_load_dword %3, %4, %5\n\ts_waitcnt lgkmcnt(0)\n\ts_memtime %1\n\ts_waitcnt lgkmcnt(0)\n\t"
                     "s_load_dword %3, %4, %6\n\ts_waitcnt lgkmcnt(0)\n\ts_memtime %2\n\ts_waitcnt lgkmcnt(0)"
                     : "=&s"(ta), "=&s"(tb), "=&s"(tc), "=&s"(dm) : "s"(ws), "s"(pa), "s"(pa ^ 8192u));
        const int d = min(max((int)(2 * (u32)tb - (u32)ta - (u32)tc), -500), 500);
        vote = ((wave ^ die) & 1) ? -d : d;
      }
      SB();
    }
    if (i == TBU) {
      if (wave == 7) {
        const bool ok = tbg[1] == tag;
        float lf = ok ? (float)min(max((int)((t0 << 10 | 1023u) - tbg[0]) >> 10, -300), 300) : 0.f, nf = ok ? 1.f : 0.f, lg = ok ? (float)(tbg[0] & 1023u) : 0.f;
        lf += dpp<XOR1>(lf); nf += dpp<XOR1>(nf); lg += dpp<XOR1>(lg); lf += dpp<XOR2>(lf); nf += dpp<XOR2>(nf); lg += dpp<XOR2>(lg);
        lf += dpp<XOR7>(lf); nf += dpp<XOR7>(nf); lg += dpp<XOR7>(lg); lf += dpp<XOR8>(lf); nf += dpp<XOR8>(nf); lg += dpp<XOR8>(lg);
        const float rn = __builtin_amdgcn_rcpf(fmaxf(nf, 1.f));
        shift = __builtin_amdgcn_readfirstlane(min(max((int)((lg * rn - (float)P.GS) * (float)P.GB * 0.125f), 0), 150));
        if (lane == 0) sm.bc = u32x2{tag, t0 - (u32)(int)(lf * rn) + shift};
      }
      LDS_BARRIER();
      const u32x2 bc = sm.bc;
      tag = __builtin_amdgcn_readfirstlane(bc[0]);
      t1 = (__builtin_amdgcn_readfirstlane((int)(bc[1] - t0)) + TEND + P.X1) * 256;
      tau -= __builtin_amdgcn_readfirstlane((int)((float)(int)(t0 - bc[1]) * 256.f * __builtin_amdgcn_rcpf((float)(nl - TBU - 1))));
      SB();
    }
    if (i == RPP) report(G_VT);
    if (i == LNS) {
      tag = next_tag(__builtin_amdgcn_readfirstlane(ep[0]));
      if (part == 1) {
        float x0[8], x1[8], mb0, ri0, mb1, ri1;
#pragma unroll
        for (int k = 0; k < 4; ++k) { x0[2 * k] = lo_f(xr0[k]); x0[2 * k + 1] = hi_f(xr0[k]); x1[2 * k] = lo_f(xr1[k]); x1[2 * k + 1] = hi_f(xr1[k]); }
        ln_stats2(x0, x1, mb0, ri0, mb1, ri1);
        l0 = rbf(rbf(lo_f(xs0) - mb0) * ri0); l1 = rbf(rbf(lo_f(xs1) - mb1) * ri1);
      }
      PIN(l0); PIN(l1);
      SB();
    }
    if (i == WUSE) {
      PIN(tch1); PIN(tch2);
      if ((part == 1 || (part == 2 && has_w && !modc))) {
        WAIT_VM(WK); CFENCE();
        if (part == 2) mod_stage(); else wo_prep();
      }
#pragma unroll
      for (int k = 0; k < 8; ++k) PIN(bf[k]);
      SB();
    }
    if (i == QP + QD) {
      if (part == 1) {
        u32 gm[2] = {gmd[0][0], gmd[1][0]};
        if (!all_ok((gmd[0][1] == tag) & (gmd[1][1] == tag))) gather8<2>(wsr, G_MOD + tid * 8, 512 * 8, tag, gm);
        gemv_go(pack_bf(rbf(l0 * rbf(1.f + lo_f(gm[1]))) + lo_f(gm[0]), rbf(l1 * rbf(1.f + hi_f(gm[1]))) + hi_f(gm[0])));
      }
      SB();
    }
    if (i == QP + QD + 1) {
      if (part == 1) {
        LDS_BARRIER();
        if (wave == 0) {
          const int bb = lane >> 5, jq = lane & 31;
          st8_sc1(wsr, G_Q + (bb * 2048 + blk * 32 + jq) * 8, pack_bf(sum8(&sm.r2[2 * jq * 20 + bb * 8]), sum8(&sm.r2[(2 * jq + 1) * 20 + bb * 8])), tag);
        }
      }
      SB();
    }
    return true;
  });
  PIN(sot);
  if (wave == 7) {
    const bool ok = tbg[1] == tag;
    float lg = ok ? (float)(tbg[0] & 0xffffu) : 0.f, nf = ok ? 8.f * (RPP - RPS + 1) : 0.f;
    lg += dpp<XOR1>(lg); nf += dpp<XOR1>(nf); lg += dpp<XOR2>(lg); nf += dpp<XOR2>(nf); lg += dpp<XOR7>(lg); nf += dpp<XOR7>(nf); lg += dpp<XOR8>(lg); nf += dpp<XOR8>(nf);
    lg *= __builtin_amdgcn_rcpf(fmaxf(nf, 1.f));
    if (lane == 0) sm.bc2 = u32x2{(u32)min(shift * P.DG * 32, P.EW * 256), (u32)min(max((int)((lg - (float)P.B0) * (float)(P.BG * 32) + fmaxf(lg - (float)P.TC, 0.f) * (float)(P.RT * 32)) - shift * P.D0 * 32, 0), 200 * 256)};
  }
  LDS_BARRIER();
  {
    const u32x2 bc = sm.bc2;
    const int st = __builtin_amdgcn_readfirstlane(bc[1]), sp = (st * P.BF) >> 3;
    t1 += st + __builtin_amdgcn_readfirstlane(bc[0]);
    due += P.TG * 256 + sp;
    tau = (te1 >> 16) + __builtin_amdgcn_readfirstlane((int)((float)(st - sp) * __builtin_amdgcn_rcpf((float)(nl - 3))));
  }
  const u32 qoff = G_Q + ((b * 32 + head) * 64 + c * 4) * 8;
  u32x4 qg[2], mlg;
  u32 qq[4];
  constexpr int NB = (NL + 15) / 16;
  float xlo, sc[NB], pr[NB], m = 0.f;
  f32x4 acc = {0.f, 0.f, 0.f, 0.f};
  u32 pb[NB], pm[4], pl8[8], cpf;
  const u32 adst0 = lds_addr(lane < 32 ? &sm.red[wave][c * 8 + g * 4] : &sm.r2[(lane - 32) * 4]);
  u32 po, pso, rdst, rsrc, xo;
  auto wo_addr = [&]() {
    po = dup(G_P + (xcd & 3) * 8192) + tid * 16 + near;
    pso = tid < 16 ? pslot + c * 128 + near : OOB;
    PIN(pso);
    rdst = (u32)(uintptr_t)&sm.red2[wave][(lane & 31) * 4 + (lane >> 5) * 2];
    rsrc = (u32)(uintptr_t)&sm.red2[0][((wave & 1) * 16 + c) * 4 + (g & 1) * 2 + (g >> 1)];
    xo = dup(G_XP + xcd * 2048 + (wave & 1) * 1024 + (blk * 64 + (lane & ~1)) * 2) + (lane & 1) * 8192;
    PIN(po); PIN(rdst); PIN(rsrc); PIN(xo);
    const int q = (lane >> 2) & 3, pp = lane & 3, r = (g & 1) * 4 + (q & 1);
#pragma unroll
    for (int t = 0; t < 4; ++t) { wfa[t] = wimg + wave * 8192 + ((g >> 1) * 16 + (q >> 1) * 8 + r) * 128 + (pp & 1) * 8 + ((t * 2 + (pp >> 1)) ^ r ^ (q & 2)) * 16; PIN(wfa[t]); }
  };
  const u32 nadd = wdef ? P.LW : (is_up ? P.LU : 0);
  u32 nto = tid < 2 ? dup(G_NT) + bid * 8 + tid * 8192 : OOB;
  PIN(nto);
  auto notices = [&]() -> int {
    u32 off = dup(G_NT) + near + lane * 16;
    PIN(off);
    until(due);
    int mx;
#pragma nounroll
    for (int spin = 0; spin < SPIN_MAX; ++spin) {
      CFENCE();
      const u32x4 ga_ = ld16<AUX_SC1>(wsr, off, 0), gb_ = ld16<AUX_SC1>(wsr, off, 1024);
      mx = max(max((int)(ga_[0] - t0), (int)(ga_[2] - t0)), max((int)(gb_[0] - t0), (int)(gb_[2] - t0)));
      if (all_ok((((ga_[1] ^ tag) | (ga_[3] ^ tag)) | ((gb_[1] ^ tag) | (gb_[3] ^ tag))) == 0u)) break;
      for (int k = 0; k < P.AG; ++k) __builtin_amdgcn_s_sleep(1);
    }
    if (all_ok(mx <= (t1 >> 8) - P.AC)) return t1;
    mx = max(mx, __builtin_amdgcn_mov_dpp(mx, XOR1, 0xf, 0xf, true)); mx = max(mx, __builtin_amdgcn_mov_dpp(mx, XOR2, 0xf, 0xf, true));
    mx = max(mx, __builtin_amdgcn_mov_dpp(mx, XOR7, 0xf, 0xf, true)); mx = max(mx, __builtin_amdgcn_mov_dpp(mx, XOR8, 0xf, 0xf, true));
    mx = max(max(__builtin_amdgcn_readlane(mx, 0), __builtin_amdgcn_readlane(mx, 16)), max(__builtin_amdgcn_readlane(mx, 32), __builtin_amdgcn_readlane(mx, 48)));
    return max(min(mx, 1 << 20) * 256, t1 + P.L0 * 256) + P.L1 * 256;
  };
  auto pv_row = [&](auto jc) {
    constexpr int i = jc.v;
    const u32 pv = __float_as_uint(bcast16(u2f(pb[i >> 4]), i & 15));
    acc = mfma16(u32x4{pv & pm[0], pv & pm[1], pv & pm[2], pv & pm[3]}, vr[i], acc);
    PIN(acc);
    return true;
  };
  sfor<0, NL - 1>([&](auto ic) {
    constexpr int v = ic.v;
    if constexpr (v >= SK0 && v < SK1) { if (left_out(v)) return true; }
    if constexpr (v > 0) {
      const u32 so = __builtin_amdgcn_readlane(sot, v);
      if constexpr (v >= SK1) { if (v == NL - P.NE) tau = (tau * P.FE) >> 4; }
      gate();
      if constexpr (v >= RVS && v <= RVP) sample();
      SB();
      if constexpr (v >= SK0 && v < SK1) ld16_into(vr[v], rv, lane * 16, so); else vr[v] = ld16<AUX_NT>(rv, lane * 16, so);
      SB();
      SB();
    }
    if constexpr (v == SK0 - 1) {
#pragma unroll
      for (int i = SK0; i < SK1; ++i) { vr[i] = u32x4{0u, 0u, 0u, 0u}; PIN(vr[i]); }
    }
    if constexpr (v == VQ - 4) { qg[0] = ld16<AUX_SC1>(wsr, qoff, 0); qg[1] = ld16<AUX_SC1>(wsr, qoff + 16, 0); }
    if constexpr (v == VSIDE) side_loads();
    if constexpr (v == CPF) {
      u64 pc; u32 dlt, len;
      asm volatile("s_getpc_b64 %0\n.Lcpf_here:\n\ts_mov_b32 %1, .Ltail_start-.Lcpf_here\n\ts_mov_b32 %2, .Lend_a-.Ltail_start\n\ts_max_i32 %2, %2, .Lend_b-.Ltail_start\n\ts_max_i32 %2, %2, .Lend_c-.Ltail_start\n\ts_max_i32 %2, %2, .Lend_d-.Ltail_start\n\ts_max_i32 %2, %2, .Lend_e-.Ltail_start" : "=s"(pc), "=s"(dlt), "=s"(len) :: "scc");
      pc += dlt;
      const rsrc_t rc = mk_rsrc((const void*)pc, (part == 3 && i8 == 1) ? len : 0u);
      cpf = __builtin_amdgcn_raw_buffer_load_b32(rc, tid * 64, 0, 0);
    }
    if constexpr (v == SK1 + 2) PIN(cpf);
    if constexpr (v == RVP) report(G_VT + 1024);
    if constexpr (v == RVL) tbg = ld8<AUX_SC1>(wsr, wave == 7 ? G_VT + (g ? 0 : 1024) + xcd * 128 + c * 8 : OOB, 0);
    if constexpr (v == SK1) mlg = ld16<AUX_SC1>(wsr, wave == 0 ? mlo : OOB, 0);
    SB();
    if constexpr (NL + v < 72) limit(IC<NL + v>{});
    if constexpr (v >= SK0 + WV) { WAIT_VM(v < SK1 ? WV : WV + 1); SB(); }
    if constexpr (v == VQ) {
      if (!all_ok((qg[0][1] == tag) & (qg[0][3] == tag) & (qg[1][1] == tag) & (qg[1][3] == tag))) gather16<2>(wsr, qoff, 16, tag, qg);
      qq[0] = qg[0][0]; qq[1] = qg[0][2]; qq[2] = qg[1][0]; qq[3] = qg[1][2];
    }
    if constexpr (v >= VQ && v < VS) {
      constexpr int sl = v - VQ, bt = sl >> 1;
      float xs[8];
#pragma unroll
      for (int i = 0; i < 8; ++i) {
        float d = 0.f;
        if (sl * 8 + i < NL) {
#pragma unroll
          for (int k = 0; k < 4; ++k) d = dot2(kr[sl * 8 + i][k], qq[k], d);
        }
        xs[i] = d;
      }
#pragma unroll
      for (int h = 0; h < 4; ++h) xs[h] = sel_add<XOR7>(xs[h], xs[h + 4], c & 4);
#pragma unroll
      for (int h = 0; h < 2; ++h) xs[h] = sel_add<XOR2>(xs[h], xs[h + 2], c & 2);
      xs[0] = sel_add<XOR1>(xs[0], xs[1], c & 1);
      if constexpr ((sl & 1) || sl == NSL - 1) {
        const float t = (sl & 1) ? sel_add<XOR8>(xlo, xs[0], c & 8) : xs[0] + dpp<XOR8>(xs[0]);
        const float s = rbf(rbf(t) * 0.08838834764831845f);
        sc[bt] = (bt * 16 + c < SK0 || (bt * 16 + c >= first && bt * 16 + c < NL)) ? s : -__builtin_inff();
        PIN(sc[bt]);
      } else {
        xlo = xs[0];
        PIN(xlo);
      }
    }
    if constexpr (v == VS) {
      float mloc = sc[0];
#pragma unroll
      for (int bt = 1; bt < NB; ++bt) mloc = fmaxf(mloc, sc[bt]);
      m = fmaxf(wave_max(mloc), -3.0e38f);
      float ls = 0.f;
#pragma unroll
      for (int bt = 0; bt < NB; ++bt) { pr[bt] = __expf(sc[bt] - m); ls += pr[bt]; }
      const float l = wave_sum(ls);
      if (lane == 0) { sm.ml[wave][0] = m; sm.ml[wave][1] = l; }
      if (c == ((NL - 1) & 15)) sm.pl[g][wave] = pr[NB - 1];
    }
    if constexpr (v >= SK1) pv_row(IC<v - SK1 + SK0 - WV>{});
    if constexpr (v < SK0 || v >= SK0 + WV) sfor<cons_end(v - 1), cons_end(v)>(pv_row);
    if constexpr (v == RVU) {
      if (wave == 7) {
        if (__builtin_expect(!all_ok(tbg[1] == tag || g == 0), 0)) {
          u32 vg[1];
          gather8<1>(wsr, G_VT + xcd * 128 + c * 8, 0, tag, vg);
          if (g) tbg = u32x2{vg[0], tag};
        }
        const bool ok = tbg[1] == tag;
        float lg = ok ? (float)(g ? (int)tbg[0] >> 16 : (int)(tbg[0] & 0xffffu)) : 0.f, nf = ok ? 8.f * (RVP - RVS + 1) : 0.f;
        lg += dpp<XOR1>(lg); nf += dpp<XOR1>(nf); lg += dpp<XOR2>(lg); nf += dpp<XOR2>(nf); lg += dpp<XOR7>(lg); nf += dpp<XOR7>(nf); lg += dpp<XOR8>(lg); nf += dpp<XOR8>(nf);
        lg *= __builtin_amdgcn_rcpf(fmaxf(nf, 1.f));
        if (lane == 16) sm.bc4 = __float_as_uint(lg);
        if (lane == 0) sm.bc3 = (u32)min(max((int)((lg - (float)P.F0) * (float)(P.FG * 32)), 0), 200 * 256);
      }
      LDS_BARRIER();
      const int st = __builtin_amdgcn_readfirstlane(sm.bc3), sp = (st * P.FF_) >> 3;
      near = ((__builtin_amdgcn_readfirstlane(sm.bc4) >> 31 ^ die ^ P.NR ^ 1) & 1) * 8192u;
      t1 += st;
      due += sp;
      tau += __builtin_amdgcn_readfirstlane((int)((float)(st - sp) * __builtin_amdgcn_rcpf((float)(nl - RVU - 3))));
    }
    if constexpr (v == SK1 + 1) {
      float p[8];
#pragma unroll
      for (int w = 0; w < 8; ++w) p[w] = sm.pl[g][w] * __expf(sm.ml[w][0] - m);
#pragma unroll
      for (int w = 0; w < 8; ++w) pl8[w] = pack_bf(p[w], p[w]);
#pragma unroll
      for (int w = 0; w < 8; ++w) PIN(pl8[w]);
    }
    if constexpr (v == MLS - 1) { side_pin(); if (is_up) up_prep(); PIN(ga); PIN(xin); PIN(cc1); }
    if constexpr (v == UPREP - 1) wo_addr();
    if constexpr (v == UPREP) {
#pragma unroll
      for (int k = 0; k < 4; ++k) asm volatile("" : "=v"(bw[k]));
      if (part == 0) wo_frags(0); else if (is_up) up_frags();
#pragma unroll
      for (int k = 0; k < 4; ++k) PIN(bw[k]);
    }
    if constexpr (v == UPREP + 1) {
      if (is_up) {
        const u32x4* a1 = reinterpret_cast<const u32x4*>(&sm.hb[1024 + (c >> 2 & 1) * 1024 + (c >> 3) * 512 + (g >> 1) * 256 + (c >> 1 & 1) * 32 + (g & 1) * 16 + (c & 1) * 8]);
        f32x4 d = {0.f, 0.f, 0.f, 0.f};
#pragma unroll
        for (int j = 0; j < 4; ++j) d = mfma64(a1[j * 8], bw[j], d, (c & 1) ? 0xeeeeu : 0x4444u);
        float t = par ? d[2] + d[3] : d[0] + d[1];
        t += dpp<XOR8>(t);
        const auto ab = __builtin_amdgcn_permlane16_swap(__float_as_uint(t), __float_as_uint(t), false, false);
        Af = u2f(ab[0]); Bf = u2f(ab[1]);
      }
      PIN(Af); PIN(Bf);
    }
    if constexpr (v == MLS) {
      LDS_BARRIER();
      float mm[8], ll[8];
#pragma unroll
      for (int w = 0; w < 8; ++w) { const f32x2 t = *reinterpret_cast<const f32x2*>(&sm.ml[w][0]); mm[w] = t[0]; ll[w] = t[1]; }
      float Mwg = mm[0], Lwg = 0.f;
#pragma unroll
      for (int w = 1; w < 8; ++w) Mwg = fmaxf(Mwg, mm[w]);
      if (wave == 0) {
#pragma unroll
        for (int w = 0; w < 8; ++w) Lwg = fmaf(__expf(mm[w] - Mwg), ll[w], Lwg);
        if (lane == 0) st16_sc1(wsr, G_ML + ((head * 2 + b) * 4 + part) * 16, __float_as_uint(Mwg), __float_as_uint(Lwg), tag);
      }
      const float fw = __expf(m - Mwg);
      m = Mwg;
#pragma unroll
      for (int bt = 0; bt < NB; ++bt) { pb[bt] = pack_bf(pr[bt] * fw, pr[bt] * fw); PIN(pb[bt]); }
#pragma unroll
      for (int k = 0; k < 4; ++k) { pm[k] = (c >> 1 & 3) == k ? ((c & 1) ? 0xffff0000u : 0x0000ffffu) : 0u; PIN(pm[k]); }
    }
    SB();
    return true;
  });
  asm volatile(".Ltail_start:");
  if (wave != 0) {
    pv_row(IC<SK0 - 1>{});
    WAIT_VM(WV); SB();
    sfor<NL - 1 - WV, NL - 1>(pv_row);
    *LDSP(f32x4, adst0) = acc;
    LDS_BARRIER();
  } else {
    u32x4 xr[8], am[8];
#pragma unroll
    for (int w = 0; w < 8; ++w) am[w] = u32x4{pl8[w] & pm[0], pl8[w] & pm[1], pl8[w] & pm[2], pl8[w] & pm[3]};
#pragma unroll
    for (int w = 0; w < 8; ++w) PIN(am[w]);
    {
      const u32 so = __builtin_amdgcn_readlane(sot, NL - 1);
      gate();
      SB();
#pragma unroll
      for (int w = 0; w < 8; ++w) xr[w] = ld16<AUX_NT>(rv, lane * 16, so ^ ((w & 1) ? col(P, 7) : 0u) ^ ((w & 2) ? col(P, 8) : 0u) ^ ((w & 4) ? col(P, 9) : 0u));
      SB();
    }
    u32 rsum = lds_addr(&sm.red[0][c * 8 + (g & 1) * 4]);
    pv_row(IC<SK0 - 1>{});
    WAIT_VM(WV + 8); SB();
    sfor<NL - 1 - WV, NL - 3>(pv_row);
    if (__builtin_expect(!all_ok((mlg[1] == tag) & (mlg[3] == tag)), 0)) {
#pragma nounroll
      for (int spin = 0; spin < SPIN_MAX; ++spin) {
        CFENCE();
        mlg = ld16<AUX_SC1>(wsr, mlo, 0);
        if (all_ok((mlg[1] == tag) & (mlg[3] == tag))) break;
      }
    }
    merge_factor(mlg);
    sfor<NL - 3, NL - 1>(pv_row);
    LDS_BARRIER();
    f32x4 t[8];
#pragma unroll
    for (int w = 1; w < 8; ++w) t[w] = LDSP(f32x4, rsum)[w * 32];
    acc = ((acc + t[1]) + (t[2] + t[3])) + ((t[4] + t[5]) + (t[6] + t[7]));
    PIN(acc);
    SB();
#pragma unroll
    for (int w = 0; w < 8; ++w) acc = mfma16(am[w], xr[w], acc);
  }
  WAIT_VM(0);
  acc *= fmrg;
  {
    const u32 w0 = min(pack_bf(acc[0], acc[1]), SENT - 1u), w1 = min(pack_bf(acc[2], acc[3]), SENT - 1u);
    const auto e0 = __builtin_amdgcn_permlane16_swap(w0, w0, false, false), e1 = __builtin_amdgcn_permlane16_swap(w1, w1, false, false);
    __builtin_amdgcn_raw_buffer_store_b128(u32x4{e0[0], e1[0], e0[1], e1[1]}, wsr, pso, 0, AUX_SC1);
  }
  st8_sc1(wsr, nto, (u32)wall_clock64() + nadd, tag);
  SB();

  if (part == 2) return;
  if (part == 0) {
    __builtin_amdgcn_s_barrier();
#pragma nounroll
    while (wleft > 0) { until(t1 - (P.PA + wleft * P.PD) * 256); piece(); }
    bool late = false;
    u32x4 og[1];
    f32x4 d[4];
    u32 six = (c & 8) ? 0xeeeeu : 0x4444u;
    PIN(six);
#pragma unroll
    for (int t = 0; t < 4; ++t) { d[t] = f32x4{0.f, 0.f, 0.f, 0.f}; PIN(d[t]); }
    if (wdef) {
      late = true;
      while ((int)(((u32)wall_clock64() - t0) << 8) < t1 - 10 * 256) if (__builtin_amdgcn_s_getreg(7 | (3 << 11)) == 0) { late = false; break; }
      if (__builtin_expect(late, 0)) { until(t1); CFENCE(); og[0] = ld16<AUX_SC1>(wsr, po, 0); SB(); WAIT_VM(1); } else { WAIT_VM(0); }
      CFENCE();
      wo_frags(1);
#pragma unroll
      for (int k = 0; k < 4; ++k) PIN(bw[k]);
      SB();
    }
    if (__builtin_expect(!late, 1)) { until(t1); CFENCE(); og[0] = ld16<AUX_SC1>(wsr, po, 0); }
    SB();
    auto wo_mm = [&]() {
#pragma unroll
      for (int t = 0; t < 4; ++t) d[t] = mfma64(og[0], bw[t], d[t], six);
    };
    auto p_bad = [&]() { return !all_ok(max(max(og[0][0], og[0][1]), max(og[0][2], og[0][3])) != SENT); };
    wo_mm();
    if (__builtin_expect(p_bad(), 0)) {
#pragma nounroll
      for (int spin = 0; spin < SPIN_MAX; ++spin) {
        __builtin_amdgcn_s_sleep(1);
        CFENCE();
        og[0] = ld16<AUX_SC1>(wsr, po, 0);
        if (!p_bad()) break;
      }
#pragma unroll
      for (int t = 0; t < 4; ++t) d[t] = f32x4{0.f, 0.f, 0.f, 0.f};
      wo_mm();
      SB();
      asm volatile(".Lend_d:");
    }
    {
      SB();
      f32x4 y; f32x2 h[4];
#pragma unroll
      for (int t = 0; t < 4; ++t) { h[t] = d[t].xy + d[t].zw; PIN(h[t]); }
#pragma unroll
      for (int t = 0; t < 4; ++t) { y[t] = h[t][0] + h[t][1]; PIN(y[t]); }
      *LDSP(f32x2, rdst) = f32x2{swap32_add(y[0], y[1]), swap32_add(y[2], y[3])};
    }
    LDS_BARRIER();
    if (wave < 2) {
      f32x2 v[4];
#pragma unroll
      for (int w = 0; w < 4; ++w) v[w] = f32x2{LDSP(float, rsrc)[w * 256], LDSP(float, rsrc)[w * 256 + 128]};
      const f32x2 h = (v[0] + v[1]) + (v[2] + v[3]);
      const float y = h[0] + h[1];
      __builtin_amdgcn_raw_buffer_store_b32(min(pack_bf(dpp<0xA0>(y), dpp<0xF5>(y)), SENT - 1u), wsr, xo, 0, AUX_SC1);
      asm volatile(".Lend_a:");
    }
    return;
  }
  if (part == 3) {
    if (!has_w) return;
    u32x4 b2[4];
    {
      __builtin_amdgcn_s_barrier();
      const rsrc_t r2 = mk_rsrc(w2 + (size_t)(blk * 32 + wave * 4) * 512, 4096u);
      const u32 l2 = (u32)(uintptr_t)(&sm.w2b[wave * 4][0]);
#pragma unroll
      for (int n = 0; n < 4; ++n) { until(t1 - (P.QA + (4 - n) * P.QD) * 256); dma16(r2, (lane ^ (n * 2) ^ ((wave & 2) << 2)) * 16, n * 1024, l2 + n * 1024); }
    }
    const u32 tdst = lds_addr(&sm.hb[hpos]);
    const u32 sdst = lds_addr(&sm.stat[wave][lane]);
    const u32 ssrc = lds_addr(&sm.stat[(g >> 1) * 4 + ((lane >> 3) & 3)][(lane & 7) * 4]);
    const u32 adst = lds_addr((c < 4 && (g & 1) == 0) ? &sm.actl[g >> 1][wave * 4 + c] : reinterpret_cast<u16*>(&sm.dummy[lane]));
    const u32 asrc = lds_addr(&sm.actl[lane & 1][g * 8]);
    const u32 apa = lds_addr(&sm.hb[(c >> 3) * 512 + (g >> 1) * 256 + (c >> 1 & 1) * 64 + ((c >> 2) & 1) * 32 + (g & 1) * 16 + (c & 1) * 8]);
    u32 six = (c & 1) ? 0xeeeeu : 0x4444u;
    f32x4 d[2] = {{0.f, 0.f, 0.f, 0.f}, {0.f, 0.f, 0.f, 0.f}};
    PIN(six); PIN(d[0]); PIN(d[1]);
    constexpr u32 xs2 = 16384;
    u32 xoff = dup(G_XP) + near + g * 2048 + ub * 1024 + (wave & 3) * 256 + c * 16;
    u32x4 one;
#pragma unroll
    for (int k = 0; k < 4; ++k) one[k] = ((c & 2) == 0 && (c >> 2) == k) ? ((c & 1) ? 0x3f800000u : 0x00003f80u) : 0u;
    PIN(one);
    u32 ooff = dup(G_OP + (blk * 512 + tid) * 8);
    float Afp = Af, Bfp = Bf;
    PIN(xoff); PIN(ooff); PIN(Afp); PIN(Bfp);
    f32x2 gc = ga * cc1, xc = xin * cc1;
    PIN(ga); PIN(xin); PIN(gc); PIN(xc);
    u32 w2a = (u32)(uintptr_t)(&sm.w2b[0][0]) + (g * 8 + ((lane >> 2) & 3)) * 1024 + (wave ^ (g & 1)) * 128 + (lane & 15) * 8;
    PIN(w2a);
    if (wave == 1) {
      due = t1 + P.N1 * 256;
      const int tn = notices();
      due = tn + P.X2 * 256;
      until(due);
    }
    asm volatile("s_waitcnt vmcnt(0)" ::: "memory");
    __builtin_amdgcn_s_barrier();
    CFENCE();
    u32x4 xg[2] = {ld16<AUX_SC1>(wsr, xoff, 0), ld16<AUX_SC1>(wsr, xoff, xs2)};
    SB();
#pragma unroll
    for (int t = 0; t < 4; ++t) {
      const u32x2 lo = tr_read(w2a ^ (t * 32)), hi = tr_read((w2a ^ (t * 32)) + 4096);
      b2[t] = u32x4{lo[0], lo[1], hi[0], hi[1]};
    }
#pragma unroll
    for (int t = 0; t < 4; ++t) PIN(b2[t]);
    SB();
    const f32x4 z4 = {0.f, 0.f, 0.f, 0.f};
    auto xp_bad = [&]() { return !all_ok(max(max(max(xg[0][0], xg[0][1]), max(xg[0][2], xg[0][3])), max(max(xg[1][0], xg[1][1]), max(xg[1][2], xg[1][3]))) != SENT); };
    f32x4 v = mfma16(one, xg[1], mfma16(one, xg[0], z4));
    if (__builtin_expect(xp_bad(), 0)) {
#pragma nounroll
      for (int spin = 0; spin < SPIN_MAX; ++spin) {
        __builtin_amdgcn_s_sleep(1);
        CFENCE();
        xg[0] = ld16<AUX_SC1>(wsr, xoff, 0); xg[1] = ld16<AUX_SC1>(wsr, xoff, xs2);
        if (!xp_bad()) break;
      }
      v = mfma16(one, xg[1], mfma16(one, xg[0], z4));
      SB();
      asm volatile(".Lend_e:");
    }
    {
      const f32x2 a = v.xy, hc = __builtin_elementwise_fma(gc, a, xc), x1 = __builtin_elementwise_fma(ga, a, xin);
      *LDSP(u32, tdst) = pack_bf(hc[0], hc[1]);
      *LDSP(float, sdst) = swap32_add(x1[0] + x1[1], fmaf(x1[0], x1[0], x1[1] * x1[1]));
    }
    LDS_BARRIER();
    {
      const f32x4 s4 = LDSP(f32x4, ssrc)[0], q4 = LDSP(f32x4, ssrc)[8];
      SB();
      const u32x4 af0 = LDSP(u32x4, apa)[0], af1 = LDSP(u32x4, apa)[16];
      SB();
      const f32x2 s2 = s4.xy + s4.zw, q2 = q4.xy + q4.zw;
      float s = s2[0] + s2[1], q = q2[0] + q2[1];
      SB();
      d[0] = mfma64(af0, bw[0], d[0], six);
      SB();
      s = swap16_add(s, q);
      SB();
      d[1] = mfma64(af0, bw[1], d[1], six);
      SB();
      s += dpp<XOR1>(s); s += dpp<XOR2>(s);
      SB();
      d[0] = mfma64(af1, bw[2], d[0], six);
      SB();
      s += dpp<XOR7>(s); s += dpp<XOR8>(s);
      SB();
      d[1] = mfma64(af1, bw[3], d[1], six);
      SB();
      const auto sq = __builtin_amdgcn_permlane16_swap(__float_as_uint(s), __float_as_uint(s), false, false);
      const float mean = u2f(sq[0]) * (1.f / 512.f);
      const float var = fmaf(u2f(sq[1]), 1.f / 512.f, -mean * mean);
      float ri = __builtin_amdgcn_rsqf(fmaxf(var, 0.f) + 1e-5f);
      PIN(ri); PIN(d[0]); PIN(d[1]);
      SB();
      float y0 = d[0][0] + d[0][1], y1 = d[1][2] + d[1][3], mr = -ri * mean;
      PIN(y0); PIN(y1); PIN(mr);
      float y = y0 + y1, cf = fmaf(mr, Afp, Bfp), rk = ri * -1.44269504f;
      PIN(y); PIN(cf); PIN(rk);
      y = __builtin_bit_cast(float, __builtin_amdgcn_update_dpp(__builtin_bit_cast(int, y), __builtin_bit_cast(int, y), XOR8, 0xa, 0xf, false));
      const float ck = cf * -1.44269504f;
      y = swap16_add(y, y);
      const float ex = __builtin_amdgcn_exp2f(fmaf(y, rk, ck));
      y = fmaf(y, ri, cf);
      const float gu = y * dpp<0x104>(y);
      *LDSP(u16, adst) = (u16)pack_bf(gu * __builtin_amdgcn_rcpf(1.f + ex), 0.f);
    }
    LDS_BARRIER();
    {
      const u32x4 af = *LDSP(u32x4, asrc);
      const f32x4 z = {0.f, 0.f, 0.f, 0.f};
      f32x4 d0 = mfma16(af, b2[0], z), d1 = mfma16(af, b2[1], z), d2 = mfma16(af, b2[2], z), d3 = mfma16(af, b2[3], z);
      asm volatile("" : "+v"(d0), "+v"(d1), "+v"(d2), "+v"(d3));
      const bool g1 = g & 1, g2 = g & 2;
      const float e0 = g1 ? d1[0] : d0[0], e1 = g1 ? d1[1] : d0[1], f0 = g1 ? d3[0] : d2[0], f1 = g1 ? d3[1] : d2[1];
      const u32x2 ov = {pack_bf(g2 ? f0 : e0, g2 ? f1 : e1), tag};
      __builtin_amdgcn_raw_buffer_store_b64(ov, wsr, ooff, near ^ 8192u, AUX_SC1);
      __builtin_amdgcn_raw_buffer_store_b64(ov, wsr, ooff, near, AUX_SC1);
    }
    asm volatile(".Lend_b:");
    return;
  }
  if (wave == 0) {
    if (!all_ok((sg[0][1] == tag) & (sg[0][3] == tag) & (sg[1][1] == tag) & (sg[1][3] == tag))) {
      u32x4 t[2];
      gather16<2>(wsr, G_MOD + (1024 + fd) * 8, 1536 * 8, tag, t);
      sg[0] = t[0]; sg[1] = t[1];
    }
    u32 xa;
    {
      due = t1 + P.N2 * 256;
      const int tn = notices();
      due = tn + P.X3 * 256;
      until(due);
    }
    const bool hb = g & 1;
#pragma nounroll
    for (int spin = 0; spin < SPIN_MAX; ++spin) {
      CFENCE();
      xa = __builtin_amdgcn_raw_buffer_load_b32(wsr, dup(G_XP) + near + (g >> 1) * 16384 + (c >> 2) * 2048 + (g & 1) * 1024 + fd * 2, 0, AUX_SC1);
      if (all_ok(xa != SENT)) break;
      __builtin_amdgcn_s_sleep(1);
    }
    float a0 = lo_f(xa), a1 = hi_f(xa);
    a0 += dpp<0x124>(a0); a1 += dpp<0x124>(a1);
    a0 += dpp<XOR8>(a0); a1 += dpp<XOR8>(a1);
    a0 = swap32_add(a0, a0); a1 = swap32_add(a1, a1);
    f32x2 xv = {rbf(lo_f(sx2) + rbf((hb ? hi_f(sg[0][0]) : lo_f(sg[0][0])) * rbf(a0))), rbf(hi_f(sx2) + rbf((hb ? hi_f(sg[0][2]) : lo_f(sg[0][2])) * rbf(a1)))};
    f32x2 gv = {hb ? hi_f(sg[1][0]) : lo_f(sg[1][0]), hb ? hi_f(sg[1][2]) : lo_f(sg[1][2])};
    PIN(xv); PIN(gv);
    if (wi == 0 && lane == 0) __builtin_amdgcn_raw_buffer_store_b32(tag, wsr, WS_EPOCH, 0, AUX_SC1);
    due = max(due + P.X4 * 256, (int)(((u32)wall_clock64() - t0) << 8) + P.GX * 256);
    const bool last = lane < (NUP - 32) * 4;
    u32 o0 = dup(G_OP + (lane >> 2) * 4096 + fd * 8) + near, o2 = last ? o0 : OOB, tg2 = last ? tag : 0u;
    const rsrc_t rout = mk_rsrc(out, 2048u);
    u32 oo = (lane & 44) == 0 ? (g * 512 + fd) * 2 : OOB;
    const u32 pk = (c & 10) ? 0u : ((c & 4) ? 0x3f800000u : 0x00003f80u);
    u32x4 one = {(c & 1) ? 0u : pk, 0u, (c & 1) ? pk : 0u, 0u};
    PIN(o0); PIN(o2); PIN(tg2); PIN(oo); PIN(one);
    __builtin_amdgcn_raw_buffer_store_b32(pack_bf(xv[0], xv[1]), rout, oo, 0, AUX_SC1);
    u32 s1_ = 131072, s2_ = 262144;
    asm volatile("" : "+s"(s1_), "+s"(s2_));
    until(due);
    f32x4 sv;
#pragma nounroll
    for (int spin = 0; spin < SPIN_MAX; ++spin) {
      CFENCE();
      const u32x4 og[3] = {ld16<AUX_SC1>(wsr, o0, 0), ld16<AUX_SC1>(wsr, o0, s1_), ld16<AUX_SC1>(wsr, o2, s2_)};
      sv = f32x4{0.f, 0.f, 0.f, 0.f};
#pragma unroll
      for (int k = 0; k < 3; ++k) sv = mfma16(one, og[k], sv);
      SB();
      u32 bad = ((og[0][1] ^ tag) | (og[0][3] ^ tag) | (og[1][1] ^ tag)) | ((og[1][3] ^ tag) | (og[2][1] ^ tg2) | (og[2][3] ^ tg2));
      PIN(bad);
      SB();
      float s0 = sv[0] + dpp<0x124>(sv[0]), s1 = sv[1] + dpp<0x124>(sv[1]);
      PIN(s0); PIN(s1);
      s0 += dpp<XOR8>(s0); s1 += dpp<XOR8>(s1);
      PIN(s0); PIN(s1);
      __builtin_amdgcn_raw_buffer_store_b32(pack_bf(fmaf(gv[0], s0, xv[0]), fmaf(gv[1], s1, xv[1])), rout, oo, 0, AUX_SC1);
      SB();
      if (all_ok(bad == 0u)) break;
      __builtin_amdgcn_s_sleep(1);
    }
    asm volatile(".Lend_c:");
  }
}

extern "C" void kernel_launch(void* const* d_in, const int* in_sizes, int n_in,
                              void* d_out, int out_size, void* d_ws, size_t ws_size,
                              hipStream_t stream) {
  fused_kernel<<<256, 512, 0, stream>>>((const u16*)d_in[1], (const u16*)d_in[2], (const u16*)d_in[4], (const u16*)d_in[3], (const u16*)d_in[10], (const u16*)d_in[6], (const u16*)d_in[7],
                                        (unsigned char*)d_ws, (const u16*)d_in[0], (const u16*)d_in[9], (const u16*)d_in[8], (u16*)d_out);
}
```

```cpp
#define PROBE_MUL 10
#include <hip/hip_runtime.h>
#include <stdint.h>

typedef unsigned int u32;
typedef unsigned short u16;
typedef unsigned long long u64;
typedef __attribute__((ext_vector_type(4))) unsigned u32x4;
typedef __attribute__((ext_vector_type(2))) unsigned u32x2;
typedef __attribute__((ext_vector_type(2))) __bf16 bf16x2;
typedef __attribute__((ext_vector_type(4))) float f32x4;
typedef __attribute__((ext_vector_type(2))) float f32x2;
typedef __amdgpu_buffer_rsrc_t rsrc_t;

constexpr int NH = 32, SK = 16384, DH = 128, WIN = 4096, FF = 1376;
struct Prm { int E0, ES, E1, E2, E2C, EU, TEND, DT_UP, L0, L1, X1, X2, X3, X4, NE, FE, M0, M1, M2, M3, M4, GS, GX, N1, N2, GA, WD, DT_WO, LW, LU, EW, PM, NF, NR, C0, A0, AG, AC, TS, TW, TK, TG, GB, B0, BG, BF, D0, DG, TC, LM, C1, F0, FG, FF_, PA, PD, QA, QD, RT, SA, SG, SC, W[9]; };
constexpr Prm PRM0 = {32, 20, 32, 32, 60, 60, 1968, 76, 12, 27, 75, 63, 100, 75, 3, 8, 0x2511f55b, 0x2b7425d5, 0x6017665, 0x17e0b9ec, 0x31944305, 62, 30, 29, 62, 50, 4, 106, 50, 28, 10, 0x0, 12, 14, 3, 36, 4, 4, 509, 215, 80, -2, 6, 69, 10, 8, 8, 6, 87, 120, 2, 65, 6, 4, 52, 14, 9, 18, 12, 45, 10, 22, 0x77777777, 0x77777777, 0x77777777, 0x77777777, 0x77777777, 0x44444477, 0x44444444, 0x44444444, 0x44444444};
constexpr int NL = 37;
constexpr int SK0 = 21, SK1 = 32;
constexpr int NCMIN = (NL - SK1 + SK0) * 8;
constexpr int WK = 8, WV = 5;
constexpr int WUSE = 12, LNS = 7;
constexpr int QP = 14, QD = 4;
constexpr int VQ = 6;
constexpr int NSL = (NL + 7) / 8;
constexpr int VS = VQ + NSL;
constexpr int MLS = VS + 2;
constexpr int VSIDE = MLS - 5, UPREP = MLS + 6;
constexpr int CPF = 17;
constexpr int SMO = 1;
constexpr int RVS = 5, RVP = 8, RVL = 14, RVU = 18;
constexpr int RPS = 23, RPP = 27;
constexpr int PRB = 12;
constexpr int TBS = 7, TBP = 10, TBL = 13, TBU = 17;
constexpr int window(int s, int w) {
  int n = w;
  for (int u = s - w + 1; u <= s; ++u) n += u == SMO ? 6 : (u == TBL || u == SK1 || u == NL + CPF || u == NL + RVL || u == NL + SK1 ? 1 : (u == QP || u == NL + VQ - 4 ? 2 : (u == NL + VSIDE ? 4 : 0)));
  return n;
}
constexpr int cons_end(int v) {
  const int a = v - WV + 1, c = (v - MLS) * 4;
  const int m = a < c ? a : c;
  return m < 0 ? 0 : (m > NL ? NL : m);
}
constexpr u32 col(const Prm& P, int j) { const int m[6] = {P.M0, P.M1, P.M2, P.M3, P.M4, P.PM}; return (u32)(m[j / 3] >> (j % 3 * 10) & 1023) << 10; }
constexpr u32 first_col(const Prm& P, int k) {
  if (k < 4) return col(P, 12 + k) | 1u << (24 + k);
  if (k < 6) return col(P, k - 4);
  if (k < 8) return col(P, 4 + k) | 1u << (16 + k);
  if (k < 11) return col(P, k - 1);
  return (col(P, 2) ^ col(P, 3) ^ col(P, 4) ^ col(P, 5) ^ col(P, 6) ^ col(P, 10) ^ col(P, 11)) | 0xf00000u;
}
constexpr bool one_to_one(const Prm& P) {
  u32 c[10] = {};
  for (int j = 0; j < 10; ++j) c[j] = col(P, j);
  for (int j = 0; j < 10; ++j) {
    if (c[j] == 0) return false;
    const u32 low = c[j] & (0u - c[j]);
    for (int k = j + 1; k < 10; ++k) if (c[k] & low) c[k] ^= c[j];
  }
  return true;
}
static_assert(one_to_one(PRM0), "every KB of the window exactly once");
struct Tab { u32 e[512]; };
constexpr Tab make_tab(const Prm& P) {
  Tab T{};
  for (int bid = 0; bid < 256; ++bid) {
    const int xcd = bid & 7, part = bid >> 4 & 3, i8 = (bid >> 3 & 1) | bid >> 6 << 1, wi = i8 * 8 + xcd;
    const bool has_mod = wi < 48, has_up = wi >= 21;
    const int cb0 = (i8 + P.C0) & 7, cb1 = (i8 + P.C1) & 7, ms = wi - 20;
    const int mi = wi < 8 ? wi : (wi < 16 ? 8 + ((xcd + 4) & 7) : (wi < 20 ? (wi - 14) * 8 + 3 : 16 + ms / 7 * 8 + ms % 7 + (ms % 7 >= 3 ? 1 : 0)));
    const int ui = xcd * 5 + (xcd > 5 ? xcd - 5 : 0) + (i8 == 6 ? 0 : (xcd >= 5 ? 1 : 0) + (i8 == 7 ? 4 : i8 - 2));
    const bool edge = i8 >= 6;
    const int wd = cb0 == 3 ? 0 : P.WD;
    const int e[4] = {P.E0 - 4 * wd + (cb0 == 3 ? P.ES : 0), P.E1 + (cb1 == 3 ? P.ES : 0), has_mod ? (wi < 16 ? P.E2C : P.E2) + ((mi & 7) == 3 ? P.ES : 0) : 0, has_up ? P.EU : 0};
    int tw[4] = {}, win[4] = {}, span[4] = {}, k0[4] = {};
    for (int k = 0; k < 4; ++k) {
      const int np = k == 0 ? 8 - wd : 8;
      tw[k] = np ? e[k] * P.TW / (16 * np) : 0;
      k0[k] = P.NF + tw[k] * np / 2 > P.TK ? P.NF + tw[k] * np / 2 : P.TK;
      span[k] = P.TEND - k0[k] - P.TG - (k == 0 ? (wd ? P.DT_WO : P.A0) : (k == 3 && has_up ? P.DT_UP : 0));
      win[k] = span[k] - (k == 3 && has_up ? P.GA + (edge ? P.LM : 0) : 0);
    }
    int n[4] = {NCMIN / 8, NCMIN / 8, NCMIN / 8, NCMIN / 8};
    for (int left = 128 - NCMIN / 2; left > 0; --left) {
      int best = -1;
      for (int k = 3; k >= 0; --k)
        if (n[k] < NL && (best < 0 || n[k] * win[best] < n[best] * win[k])) best = k;
      ++n[best];
    }
    const int c[4] = {0, n[0] - 1, n[0] + n[1] - 2, n[0] + n[1] + n[2] - 3};
    const int nl = n[part];
    const bool has_w = part < 2 || (part == 2 ? has_mod : has_up);
    const int blk = !has_w ? 0 : (part == 0 ? cb0 : (part == 1 ? xcd * 8 + cb1 : (part == 2 ? mi : ui)));
    T.e[2 * bid] = (u32)(c[part] | nl << 7 | blk << 13 | (has_w ? 1 : 0) << 19 | (part == 0 ? wd : 0) << 20) | (u32)tw[part] << 24;
    T.e[2 * bid + 1] = (u32)(span[part] * P.TS / (4 * (nl - 1)) | span[part] * (1024 - P.TS) / (4 * (nl - 1)) << 16);
  }
  return T;
}
constexpr int step_range(const Prm& P, bool mx_) {
  int mx = 0, mn = 1 << 20;
  const Tab T = make_tab(P);
  for (int bid = 0; bid < 256; ++bid) { const int n = T.e[2 * bid] >> 7 & 63; mx = n > mx ? n : mx; mn = n < mn ? n : mn; }
  return mx_ ? mx : mn;
}
extern "C" __attribute__((used, visibility("protected"))) __constant__ const Tab wg_tab = make_tab(PRM0);
static_assert(step_range(PRM0, true) <= NL && step_range(PRM0, false) >= NCMIN / 8, "a wave's key range does not fit its load steps");
static_assert(RVU < UPREP && UPREP + 1 < SK0 && QP + QD + 1 < SK0 && cons_end(SK0 - 1) == SK0 - WV && NL - SK1 == WV, "steps every wave runs");
constexpr bool timers_long(const Prm& P) {
  const Tab T = make_tab(P);
  for (int bid = 48; bid < 64; ++bid) if (SK0 + NL - (int)(T.e[2 * bid] >> 7 & 63) > RPS) return false;
  return true;
}
static_assert(timers_long(PRM0), "steps of the second report");
constexpr int NUP = FF / 32;
constexpr int SPIN_MAX = 1 << 17;

constexpr u32 WS_EPOCH = 0;
constexpr u32 G_TB = 1024;
constexpr u32 G_MOD = 4096;
constexpr u32 G_Q = G_MOD + 3072 * 8;
constexpr u32 G_ML = G_Q + 4096 * 8;
constexpr u32 G_U = G_ML + 256 * 16;
constexpr u32 G_P = 0;
constexpr u32 G_XP = G_P + 128 * 256;
constexpr u32 G_OP = G_XP + 8 * 512 * 4;
constexpr u32 G_NT = G_OP + 43 * 512 * 8;
constexpr u32 G_PB = G_OP + 44 * 512 * 8;
constexpr u32 G_VT = G_U + 2 * (G_PB + 8192);
constexpr u32 WS_END = G_VT + 2048;
__device__ __forceinline__ u32 dup(u32 o) { return G_U + ((o >> 13) << 14) + (o & 8191u); }
constexpr u32 SENT = 0xffffffffu;
constexpr u32 OOB = 0x40000000u;
#define WST 0

constexpr int AUX_NT = 2, AUX_SC1 = 16;

__device__ __forceinline__ float lo_f(u32 v) { return __uint_as_float(v << 16); }
__device__ __forceinline__ float hi_f(u32 v) { return __uint_as_float(v & 0xffff0000u); }
__device__ __forceinline__ float rbf(float x) { return (float)(__bf16)x; }
__device__ __forceinline__ u32 pack_bf(float lo, float hi) {
  bf16x2 r; r[0] = (__bf16)lo; r[1] = (__bf16)hi; return __builtin_bit_cast(u32, r);
}
__device__ __forceinline__ float dot2(u32 k, u32 q, float acc) {
  return __builtin_amdgcn_fdot2_f32_bf16(__builtin_bit_cast(bf16x2, k), __builtin_bit_cast(bf16x2, q), acc, false);
}
template <int CTRL> __device__ __forceinline__ float dpp(float x) {
  return __builtin_bit_cast(float, __builtin_amdgcn_mov_dpp(__builtin_bit_cast(int, x), CTRL, 0xf, 0xf, true));
}
constexpr int XOR1 = 0xB1, XOR2 = 0x4E, XOR7 = 0x141, XOR8 = 0x128;
__device__ __forceinline__ float swap32_add(float a, float b) {
  auto t = __builtin_amdgcn_permlane32_swap(__float_as_uint(a), __float_as_uint(b), false, false);
  return __uint_as_float(t[0]) + __uint_as_float(t[1]);
}
__device__ __forceinline__ float swap16_add(float a, float b) {
  auto t = __builtin_amdgcn_permlane16_swap(__float_as_uint(a), __float_as_uint(b), false, false);
  return __uint_as_float(t[0]) + __uint_as_float(t[1]);
}
__device__ __forceinline__ float wave_sum(float x) {
  x += dpp<XOR1>(x); x += dpp<XOR2>(x); x += dpp<XOR7>(x); x += dpp<XOR8>(x);
  x = swap16_add(x, x);
  return swap32_add(x, x);
}
__device__ __forceinline__ float wave_max(float x) {
  x = fmaxf(x, dpp<XOR1>(x)); x = fmaxf(x, dpp<XOR2>(x)); x = fmaxf(x, dpp<XOR7>(x)); x = fmaxf(x, dpp<XOR8>(x));
  auto s = __builtin_amdgcn_permlane16_swap(__float_as_uint(x), __float_as_uint(x), false, false);
  x = fmaxf(__uint_as_float(s[0]), __uint_as_float(s[1]));
  auto t = __builtin_amdgcn_permlane32_swap(__float_as_uint(x), __float_as_uint(x), false, false);
  return fmaxf(__uint_as_float(t[0]), __uint_as_float(t[1]));
}
template <int CTRL> __device__ __forceinline__ float sel_add(float lo, float hi, bool up) {
  const float t1 = lo + dpp<CTRL>(lo), t2 = hi + dpp<CTRL>(hi);
  return up ? t2 : t1;
}
template <int I> __device__ __forceinline__ float bc16(float x) {
  return __builtin_bit_cast(float, __builtin_amdgcn_update_dpp(0, __builtin_bit_cast(int, x), 0x150 + I, 0xf, 0xf, false));
}
__device__ __forceinline__ float bcast16(float x, int i) {
  switch (i) {
    case 0: return bc16<0>(x); case 1: return bc16<1>(x); case 2: return bc16<2>(x); case 3: return bc16<3>(x);
    case 4: return bc16<4>(x); case 5: return bc16<5>(x); case 6: return bc16<6>(x); case 7: return bc16<7>(x);
    case 8: return bc16<8>(x); case 9: return bc16<9>(x); case 10: return bc16<10>(x); case 11: return bc16<11>(x);
    case 12: return bc16<12>(x); case 13: return bc16<13>(x); case 14: return bc16<14>(x); default: return bc16<15>(x);
  }
}
__device__ __forceinline__ float silu_f(float x) { return x * __builtin_amdgcn_rcpf(1.f + __expf(-x)); }

template <int AUX> __device__ __forceinline__ u32x4 ld16(rsrc_t r, u32 voff, u32 soff) {
  return __builtin_amdgcn_raw_buffer_load_b128(r, voff, soff, AUX);
}
template <int AUX> __device__ __forceinline__ u32x2 ld8(rsrc_t r, u32 voff, u32 soff) {
  return __builtin_amdgcn_raw_buffer_load_b64(r, voff, soff, AUX);
}
__device__ __forceinline__ void st8_sc1(rsrc_t r, u32 voff, u32 v, u32 tag) {
  __builtin_amdgcn_raw_buffer_store_b64(u32x2{v, tag}, r, voff, 0, AUX_SC1);
}
__device__ __forceinline__ void st16_sc1(rsrc_t r, u32 voff, u32 v0, u32 v1, u32 tag) {
  __builtin_amdgcn_raw_buffer_store_b128(u32x4{v0, tag, v1, tag}, r, voff, 0, AUX_SC1);
}
__device__ __forceinline__ rsrc_t mk_rsrc(const void* p, u32 bytes) {
  return __builtin_amdgcn_make_buffer_rsrc(const_cast<void*>(p), 0, bytes, 0x00020000);
}
#define WAIT_VM(n) __builtin_amdgcn_s_waitcnt(((n) & 15) | (((n) >> 4) << 14) | (7 << 4) | (15 << 8))
#define SB() __builtin_amdgcn_sched_barrier(0)
#define CFENCE() asm volatile("" ::: "memory")
__device__ __forceinline__ bool all_ok(bool ok) { return __builtin_amdgcn_ballot_w64(!ok) == 0ull; }

template <int N> __device__ __forceinline__ void gather16(rsrc_t ws, u32 voff, u32 stride, u32 tag, u32x4 (&g)[N]) {
#pragma nounroll
  for (int spin = 0; spin < SPIN_MAX; ++spin) {
    CFENCE();
    bool ok = true;
#pragma unroll
    for (int i = 0; i < N; ++i) g[i] = ld16<AUX_SC1>(ws, voff + i * stride, 0);
#pragma unroll
    for (int i = 0; i < N; ++i) ok &= (g[i][1] == tag) & (g[i][3] == tag);
    if (all_ok(ok)) break;
    __builtin_amdgcn_s_sleep(8);
  }
}
template <int N> __device__ __forceinline__ void gather8(rsrc_t ws, u32 voff, u32 stride, u32 tag, u32 (&g)[N], bool act = true) {
#pragma nounroll
  for (int spin = 0; spin < SPIN_MAX; ++spin) {
    CFENCE();
    bool ok = true;
    u32x2 t[N];
#pragma unroll
    for (int i = 0; i < N; ++i) t[i] = ld8<AUX_SC1>(ws, voff + i * stride, 0);
#pragma unroll
    for (int i = 0; i < N; ++i) { ok &= (t[i][1] == tag); g[i] = t[i][0]; }
    if (all_ok(ok | !act)) break;
    __builtin_amdgcn_s_sleep(8);
  }
}

struct Smem {
  float red[8][128];
  float ml[8][2];
  float r2[64 * 20];
  u16 hb[3 * 1024];
  float stat[8][64];
  float red2[8][128];
  u32 dummy[64];
  u32x2 bc, bc2;
  u32 bc3, bc4;
  u32x4 zero[4];
  float pl[4][8];
  u32 mflag[8];
  u32x4 wbuf[8][8][64];
  __attribute__((aligned(256))) u16 w2b[32][512];
  u16 actl[2][32];
};

#define PROBE_K 8
#define PROBE_OP 0
#define PROBE_PART -1
#define STAMP(k) do { if ((k) == PROBE_K && tid == 0) pst = wall_clock64(); } while (0)
#define WSTAMP(k) do {} while (0)
#define FINISH() do { STAMP(8); if (tid == 0) { __hip_atomic_store(reinterpret_cast<u64*>(ws + (1u << 20)) + bid * 2, pt0, __ATOMIC_RELAXED, __HIP_MEMORY_SCOPE_AGENT); \
   __hip_atomic_store(reinterpret_cast<u64*>(ws + (1u << 20)) + bid * 2 + 1, (PROBE_PART < 0 || part == PROBE_PART) ? pst : 0ull, __ATOMIC_RELAXED, __HIP_MEMORY_SCOPE_AGENT); } \
   if (bid == 8 && wave == 0) probe_end(ws, pt0, lane); return; } while (0)
#define PIN(x) asm volatile("" : "+v"(x))
#define LDS_BARRIER() do { __builtin_amdgcn_s_waitcnt(0xc07f); __builtin_amdgcn_s_barrier(); CFENCE(); } while (0)
#define WAVE_SYNC() do { __builtin_amdgcn_fence(__ATOMIC_RELEASE, "wavefront"); __builtin_amdgcn_wave_barrier(); __builtin_amdgcn_fence(__ATOMIC_ACQUIRE, "wavefront"); } while (0)

__device__ __forceinline__ void dma16(rsrc_t r, u32 voff, u32 soff, u32 lds_dst) {
  u32 keep;
  asm volatile("s_mov_b32 %0, m0\n\ts_mov_b32 m0, %4\n\ts_nop 4\n\tbuffer_load_dwordx4 %1, %2, %3 offen nt lds\n\ts_mov_b32 m0, %0"
               : "=&s"(keep) : "v"(voff), "s"(r), "s"(soff), "s"(lds_dst) : "memory");
}
__device__ __forceinline__ void ld16_into(u32x4& d, rsrc_t r, u32 voff, u32 soff) {
  asm volatile("s_nop 4\n\tbuffer_load_dwordx4 %0, %1, %2, %3 offen nt" : "+v"(d) : "v"(voff), "s"(r), "s"(soff) : "memory");
}
__device__ __forceinline__ u32 perm_lo(u32 a, u32 b) { return __builtin_amdgcn_perm(b, a, 0x05040100u); }
__device__ __forceinline__ u32 perm_hi(u32 a, u32 b) { return __builtin_amdgcn_perm(b, a, 0x07060302u); }
__device__ __forceinline__ float rdl(float x, int l) { return __int_as_float(__builtin_amdgcn_readlane(__float_as_int(x), l)); }

typedef __attribute__((ext_vector_type(8))) __bf16 bf16x8;
__device__ __forceinline__ f32x4 mfma16(u32x4 a, u32x4 b, f32x4 c) {
  return __builtin_amdgcn_mfma_f32_16x16x32_bf16(__builtin_bit_cast(bf16x8, a), __builtin_bit_cast(bf16x8, b), c, 0, 0, 0);
}
typedef __attribute__((ext_vector_type(8))) unsigned u32x8;
typedef __attribute__((ext_vector_type(16))) __bf16 bf16x16;
__device__ __forceinline__ f32x4 mfma64(u32x4 a, u32x8 b, f32x4 c, u32 idx) {
  return __builtin_amdgcn_smfmac_f32_16x16x64_bf16(__builtin_bit_cast(bf16x8, a), __builtin_bit_cast(bf16x16, b), c, idx, 0, 0);
}
__device__ __forceinline__ float sum8(const float* p) {
  const f32x4 a = *reinterpret_cast<const f32x4*>(p), b = *reinterpret_cast<const f32x4*>(p + 4);
  return ((a[0] + b[0]) + (a[1] + b[1])) + ((a[2] + b[2]) + (a[3] + b[3]));
}
__device__ __forceinline__ float ln_ri(float var) {
  return __builtin_amdgcn_rcpf(rbf(__builtin_amdgcn_sqrtf(rbf(rbf(fmaxf(var, 0.f)) + 1e-5f))));
}
__device__ __forceinline__ void ln_stats2(const float (&x0)[8], const float (&x1)[8], float& mb0, float& ri0, float& mb1, float& ri1) {
  const float k0 = rdl(x0[0], 0), k1 = rdl(x1[0], 0);
  float s0 = 0.f, s1 = 0.f, q0 = 0.f, q1 = 0.f;
#pragma unroll
  for (int k = 0; k < 8; ++k) {
    const float d0 = x0[k] - k0, d1 = x1[k] - k1;
    s0 += d0; s1 += d1; q0 = fmaf(d0, d0, q0); q1 = fmaf(d1, d1, q1);
  }
  float t = swap16_add(swap32_add(s0, s1), swap32_add(q0, q1));
  t += dpp<XOR1>(t); t += dpp<XOR2>(t); t += dpp<XOR7>(t); t += dpp<XOR8>(t);
  auto u = __builtin_amdgcn_permlane16_swap(__float_as_uint(t), __float_as_uint(t), false, false);
  const float ms = __uint_as_float(u[0]) * (1.f / 512.f), mq = __uint_as_float(u[1]) * (1.f / 512.f);
  const float ri = ln_ri(fmaf(-ms, ms, mq));
  mb0 = rbf(k0 + rdl(ms, 0)); mb1 = rbf(k1 + rdl(ms, 32));
  ri0 = rdl(ri, 0); ri1 = rdl(ri, 32);
}
template <int I> struct IC { static constexpr int v = I; };
template <int I, int N, class F> __device__ __forceinline__ void sfor(F&& f) {
  if constexpr (I < N) { if (f(IC<I>{})) sfor<I + 1, N>(f); }
}
typedef __attribute__((ext_vector_type(4))) short s16x4;
typedef __attribute__((address_space(3))) s16x4* lds_s16x4p;
__device__ __forceinline__ u32x2 tr_read(u32 lds_addr) {
  return __builtin_bit_cast(u32x2, __builtin_amdgcn_ds_read_tr16_b64_v4i16((lds_s16x4p)lds_addr));
}
__device__ __forceinline__ u32 next_tag(u32 e) { return ((e | 0x40004000u) + 1u) & ~0x40004000u; }
__device__ __forceinline__ float u2f(u32 v) { return __uint_as_float(v); }
#define LDSP(T, a) ((__attribute__((address_space(3))) T*)(a))
template <class T> __device__ __forceinline__ u32 lds_addr(T* p) { u32 a = (u32)(uintptr_t)p; PIN(a); return a; }

#define TAB_LOAD "s_add_u32 s30, s28, wg_tab@rel32@lo+4\ns_addc_u32 s31, s29, wg_tab@rel32@hi+12\nglobal_load_dwordx2 %[te], %[va], s[30:31]\n"
#define FC(k) ".Lfc" #k
#define FC_REGS
#define FX(r, bit, k) "s_bitcmp1_b32 %[" #r "], " #bit "\ns_cselect_b32 %[sb], " FC(k) ", 0\ns_xor_b32 %[sa], %[sa], %[sb]\n"
__device__ __forceinline__ void probe_end(unsigned char* ws, u64 pt0, int lane) {
  while (wall_clock64() < pt0 + 5500ull) __builtin_amdgcn_s_sleep(8);
  u64 a[4], c2[4];
  for (int i = 0; i < 4; ++i) {
    a[i] = __hip_atomic_load(reinterpret_cast<u64*>(ws + (1u << 20)) + (i * 64 + lane) * 2, __ATOMIC_RELAXED, __HIP_MEMORY_SCOPE_AGENT);
    c2[i] = __hip_atomic_load(reinterpret_cast<u64*>(ws + (1u << 20)) + (i * 64 + lane) * 2 + 1, __ATOMIC_RELAXED, __HIP_MEMORY_SCOPE_AGENT);
  }
  int mn0 = 1 << 30, mx1 = -(1 << 30), mn1 = 1 << 30, sum = 0, cnt = 0;
  for (int i = 0; i < 4; ++i) {
    const int d0 = (int)(a[i] - pt0);
    mn0 = min(mn0, d0);
    if (c2[i] != 0ull) { const int d1 = (int)(c2[i] - pt0); mx1 = max(mx1, d1); mn1 = min(mn1, d1); sum += d1; cnt += 1; }
  }
  for (int o = 1; o < 64; o <<= 1) { mn0 = min(mn0, __shfl_xor(mn0, o)); mx1 = max(mx1, __shfl_xor(mx1, o)); mn1 = min(mn1, __shfl_xor(mn1, o)); sum += __shfl_xor(sum, o); cnt += __shfl_xor(cnt, o); }
  int X = PROBE_OP == 0 ? mx1 : (PROBE_OP == 1 ? sum / max(cnt, 1) : mn1);
  if (PROBE_K == 102) X = mn0;
  X = mn0 + (X - mn0) * PROBE_MUL;
  const u64 tg = pt0 + (u64)(long long)(X + 8000);
  for (int i = 0; i < 400000 && wall_clock64() < tg; ++i) __builtin_amdgcn_s_sleep(1);
}
__global__ void __launch_bounds__(512, 2) fused_kernel(const u16* kc_, const u16* vc_, const u16* wo_, const u16* wq_, const u16* wmod_, const u16* w1_, const u16* w3_,
                                                       unsigned char* ws_, const u16* x_, const u16* cnd_, const u16* w2_, u16* out_) {
  __shared__ __attribute__((aligned(256))) Smem sm;
  const u64 a_ka = (u64)__builtin_amdgcn_kernarg_segment_ptr();
  const int bid = blockIdx.x, tid = threadIdx.x;
  u64 wa, t64; u32x2 te; int tid0;
  u32 ar[14];
  u32 tch2, sa_, sb_, va_;
  u32x4 kr[NL], vr[NL];
  asm volatile(".set .Lfc0, %0\n.set .Lfc1, %1\n.set .Lfc2, %2\n.set .Lfc3, %3\n.set .Lfc4, %4\n.set .Lfc5, %5\n.set .Lfc6, %6\n.set .Lfc7, %7\n.set .Lfc8, %8\n.set .Lfc9, %9\n.set .Lfc10, %10\n.set .Lfc11, %11"
    :: "n"(first_col(PRM0, 0)), "n"(first_col(PRM0, 1)), "n"(first_col(PRM0, 2)), "n"(first_col(PRM0, 3)), "n"(first_col(PRM0, 4)), "n"(first_col(PRM0, 5)),
       "n"(first_col(PRM0, 6)), "n"(first_col(PRM0, 7)), "n"(first_col(PRM0, 8)), "n"(first_col(PRM0, 9)), "n"(first_col(PRM0, 10)), "n"(first_col(PRM0, 11)));
  asm volatile(
    "s_load_dwordx16 s[4:19], %[ka], 0x0\ns_load_dwordx8 s[20:27], %[ka], 0x40\n"
    "s_memrealtime %[t]\n"
    "v_lshlrev_b32 %[va], 3, %[bid]\n"
    "s_getpc_b64 s[28:29]\n.Lcpf_first:\n"
    TAB_LOAD
    "s_cmp_gt_u32 %[bid], 15\ns_cbranch_scc1 .Lno_cpf\n"
    "s_mov_b32 %[sa], .Lend_a-.Lcpf_first\ns_max_i32 %[sa], %[sa], .Lend_b-.Lcpf_first\ns_max_i32 %[sa], %[sa], .Lend_c-.Lcpf_first\ns_max_i32 %[sa], %[sa], .Lend_d-.Lcpf_first\ns_max_i32 %[sa], %[sa], .Lend_e-.Lcpf_first\n"
    "v_lshlrev_b32 %[va], 6, %[tid]\nv_min_u32 %[va], %[sa], %[va]\nglobal_load_dword %[pf], %[va], s[28:29]\n"
    ".Lno_cpf:\n"
    "v_readfirstlane_b32 %[t0], %[tid]\nv_and_b32 %[va], 63, %[tid]\n"
    "s_mov_b32 %[sa], " FC(11) "\n"
    FX(bid, 0, 0) FX(bid, 1, 1) FX(bid, 2, 2) FX(bid, 3, 3) FX(bid, 4, 4) FX(bid, 5, 5) FX(bid, 6, 6) FX(bid, 7, 7) FX(t0, 6, 8) FX(t0, 7, 9) FX(t0, 8, 10)
    "v_lshl_add_u32 %[va], %[va], 4, %[sa]\n"
    "s_waitcnt lgkmcnt(0)\nglobal_load_dwordx4 %[k0], %[va], s[4:5] nt\nglobal_load_dwordx4 %[v0], %[va], s[6:7] nt\n"
    "s_bitcmp1_b32 %[t0], 6\ns_cselect_b64 s[14:15], s[16:17], s[14:15]\n"
    "s_bitcmp1_b32 %[bid], 4\ns_cselect_b64 s[8:9], s[10:11], s[8:9]\ns_cselect_b64 s[12:13], s[14:15], s[12:13]\ns_bitcmp1_b32 %[bid], 5\ns_cselect_b64 %[wa], s[12:13], s[8:9]"
    : "=&{s4}"(ar[0]), "=&{s5}"(ar[1]), "=&{s6}"(ar[2]), "=&{s7}"(ar[3]), "=&{s18}"(ar[4]), "=&{s19}"(ar[5]), "=&{s20}"(ar[6]), "=&{s21}"(ar[7]), "=&{s22}"(ar[8]), "=&{s23}"(ar[9]),
      "=&{s24}"(ar[10]), "=&{s25}"(ar[11]), "=&{s26}"(ar[12]), "=&{s27}"(ar[13]), [wa] "=&s"(wa), [t0] "=&s"(tid0), [t] "=&s"(t64), [te] "=&v"(te), [sa] "=&s"(sa_), [sb] "=&s"(sb_),
      [va] "=&v"(va_), [pf] "=&v"(tch2), [k0] "=&v"(kr[0]), [v0] "=&v"(vr[0])
    : [ka] "s"(a_ka), [bid] "s"(bid), [tid] "v"(tid) : "s8", "s9", "s10", "s11", "s12", "s13", "s14", "s15", "s16", "s17", "s28", "s29", "s30", "s31", "scc", "memory" FC_REGS);
  asm volatile("" : "+s"(ar[0]), "+s"(ar[1]), "+s"(ar[2]), "+s"(ar[3]), "+s"(ar[4]), "+s"(ar[5]), "+s"(ar[6]), "+s"(ar[7]), "+s"(ar[8]), "+s"(ar[9]), "+s"(ar[10]), "+s"(ar[11]), "+s"(ar[12]), "+s"(ar[13]));
  asm volatile("" : "+s"(wa), "+s"(t64), "+s"(tid0), "+s"(sa_));
  const u32 tka = (u32)wall_clock64();
  __builtin_assume(tid >= 0 && tid < 512 && tid0 >= 0 && tid0 < 512 && bid >= 0 && bid < 256);
  constexpr Prm P = PRM0;
  const u64 pt0 = t64; u64 pst = (PROBE_K == 100 || PROBE_K == 102) ? pt0 : 0;
  const u32 t0 = tka + min(max((int)(tka - (u32)t64) - P.SA, 0) * P.SG >> 4, P.SC);
  const int TEND = P.TEND;
  const int xcd = bid & 7, part = bid >> 4 & 3;
  const int lane = tid & 63, wave = tid0 >> 6;
  SB();
  if ((bid >> 4) == 2) {
    const u32 o = tid < 384 ? G_U + (bid & 15) * 6144 + tid * 16 : OOB;
    __builtin_amdgcn_raw_buffer_store_b128(u32x4{SENT, SENT, SENT, SENT}, mk_rsrc((void*)(ar[4] | (u64)ar[5] << 32), WS_END), o, 0, AUX_SC1);
  }
  SB();
  const bool p3 = part == 3;
  u32 pitch = (u32)(0x005600c001000020ull >> (part * 16) & 0xffff) * 32u;
  const int sh = p3 ? 2 : 3;
  u32 wv0 = (u32)((p3 ? (wave >> 1) * 128 : wave * 64) + (part == 0 ? xcd * 512 : 0) + (lane >> sh)) * pitch + ((lane & (p3 ? 3 : 7)) ^ (lane >> (p3 ? 5 : 3))) * 16u;
  const u32 wimg = (u32)(uintptr_t)(&sm.wbuf[0][0][0]);
  u32 wsz = (part == 0 ? 4096u : 512u) * pitch, wld = wimg + (p3 ? (wave & 1) * 32768 + (wave >> 1) * 8192 : wave * 8192);
  PIN(wv0);
  asm volatile("" : "+s"(wsz), "+s"(wld), "+s"(pitch));
  auto karg = [&](int i) { return ar[2 * i] | (u64)ar[2 * i + 1] << 32; };
  const u64 a_kc = karg(0), a_vc = karg(1);
  unsigned char* ws = (unsigned char*)karg(2);
  const u16 *x = (const u16*)karg(3), *cnd = (const u16*)karg(4), *w2 = (const u16*)karg(5);
  u16* out = (u16*)karg(6);
  const rsrc_t wsr = mk_rsrc(ws, WS_END);
  asm volatile("s_waitcnt vmcnt(3)\n\ts_cmp_eq_u32 %1, 2\n\ts_cbranch_scc1 1f\n\ts_waitcnt vmcnt(2)\n1:" : "+v"(te) : "s"(bid >> 4) : "scc");
  const u32 te0 = __builtin_amdgcn_readfirstlane(te[0]), te1 = __builtin_amdgcn_readfirstlane(te[1]);
  const int bl0 = te0 & 127, nl = te0 >> 7 & 63, blk = te0 >> 13 & 63;
  const bool has_w = te0 >> 19 & 1;
  const rsrc_t rw = mk_rsrc((const void*)wa, wsz);
  const u32 wstep = pitch << (6 - sh), wflip = (p3 || part == 0) ? 32u : 0u, wflip0 = part == 0 ? 32u : 0u;
  u32 wso = blk << (sh + 4);
  int wleft = 8;
  auto piece = [&]() {
    dma16(rw, wv0, wso, wld);
    wso += wstep; wld += 1024;
    wv0 ^= (wleft & 1) ? wflip : wflip0;
    --wleft;
  };
  SB();
  STAMP(23);
  const int b = bid >> 3 & 1, head = xcd * 4 + (bid >> 6 ^ 3);
  const int g = lane >> 4, c = lane & 15;
  const int i8 = b | bid >> 6 << 1, wi = i8 * 8 + xcd;
  const bool is_up = p3 && has_w, modc = part == 2 && wi < 16;
  const size_t base = ((size_t)(b * NH + head) * SK + (SK - WIN)) * DH;
  const rsrc_t rk = mk_rsrc((const u16*)a_kc + base, WIN * DH * 2), rv = mk_rsrc((const u16*)a_vc + base, WIN * DH * 2);
  int tau = te1 & 0xffff;
  const int tauw = te0 >> 24 << 7, wdef = te0 >> 20 & 15;
  int due = P.NF * 256, now = 0;
  auto gate = [&]() {
    while (now < due) { now = (int)(((u32)wall_clock64() - t0) << 8); if (now < due) __builtin_amdgcn_s_sleep(1); }
    due += tau;
  };
  auto until = [&](int t) { while ((int)(((u32)wall_clock64() - t0) << 8) < t); };
  const u32 pslot = dup(G_P + (head & 15) * 2048 + (b * 4 + part) * 16);
  const u32 die = xcd >> 2;
  const bool voter = p3 && i8 == 1, timer = p3 && i8 < 2;
  int vote = 0, lag = 0, shift = 0;
  const u32 pbo = dup(G_PB) + wave * 1152;
  u32 near = 0;
  u32x2 ep; u32 xs0, xs1, tch1; u32x4 xr0, xr1;
  const rsrc_t rx = mk_rsrc(part == 2 ? cnd : x, (part == 1 || (part == 2 && has_w)) ? 2048u : 0u);
  auto small_ops = [&]() {
    ep = ld8<AUX_SC1>(wsr, wave == 7 ? WS_EPOCH : OOB, 0);
    xs0 = __builtin_amdgcn_raw_buffer_load_b16(rx, tid * 2, 0, 0); xs1 = __builtin_amdgcn_raw_buffer_load_b16(rx, tid * 2, 1024, 0);
    xr0 = ld16<0>(rx, part == 1 ? lane * 16 : OOB, 0); xr1 = ld16<0>(rx, part == 1 ? lane * 16 : OOB, 1024);
    tch1 = __builtin_amdgcn_raw_buffer_load_b32(mk_rsrc(w2 + (size_t)blk * 32 * 512, is_up ? 64u : 0u), tid == 384 ? 0 : OOB, 0, AUX_NT);
  };
  if (has_w) {
    if (modc) now = 1 << 30;
#pragma nounroll
    while (wleft > wdef) { gate(); due += tauw - tau; piece(); }
    now = 0;
  }
  due = max(due, P.TK * 256);
  SB();
  u32 sot = sa_ & 0xffc00u;
  {
    const u32 s1 = (bl0 + lane - 1 - (lane >= SK0 ? NL - nl : 0)) ^ (124 + part);
#pragma unroll
    for (int k = 0; k < 7; ++k) sot ^= (u32)-(int)(s1 >> k & 1u) & col(P, k);
    PIN(sot);
  }
  u32 tag = 0;
  u32x4 bf[8];
  auto wo_prep = [&]() {
    const u32 wi_ = wimg + wave * 8192;
    const int q = (lane >> 2) & 3, pp = lane & 3;
#pragma unroll
    for (int s = 0; s < 2; ++s) {
#pragma unroll
      for (int t = 0; t < 4; ++t) {
        const int r0 = s * 32 + g * 8 + q, r1 = r0 + 4, ch = t * 2 + (pp >> 1);
        const u32x2 lo = tr_read(wi_ + r0 * 128 + ((ch ^ (r0 & 7)) * 16) + (pp & 1) * 8), hi = tr_read(wi_ + r1 * 128 + ((ch ^ (r1 & 7)) * 16) + (pp & 1) * 8);
        bf[s * 4 + t] = u32x4{lo[0], lo[1], hi[0], hi[1]};
      }
    }
  };
  auto gemv_go = [&](u32 hh) {
    sm.hb[tid] = (u16)hh; sm.hb[512 + tid] = (u16)(hh >> 16);
    WAVE_SYNC();
    const u32x4* a = (c & 11) == 0 ? reinterpret_cast<const u32x4*>(&sm.hb[(c >> 2) * 512 + wave * 64 + g * 8]) : &sm.zero[0];
    const u32x4 a0 = a[0], a1 = a[(c & 11) == 0 ? 4 : 0];
    const f32x4 z = {0.f, 0.f, 0.f, 0.f};
    f32x4 d[4];
#pragma unroll
    for (int t = 0; t < 4; ++t) d[t] = mfma16(a0, bf[t], z);
#pragma unroll
    for (int t = 0; t < 4; ++t) d[t] = mfma16(a1, bf[4 + t], d[t]);
    if (lane < 32) {
#pragma unroll
      for (int t = 0; t < 4; ++t) sm.r2[(t * 16 + c) * 20 + g * 8 + wave] = d[t][0];
    }
  };
  auto mod_stage = [&]() {
    wo_prep();
    gemv_go(pack_bf(silu_f(lo_f(xs0)), silu_f(lo_f(xs1))));
    __builtin_amdgcn_s_waitcnt(0xc07f);
    if (lane == 0) *LDSP(volatile u32, (u32)(uintptr_t)&sm.mflag[wave]) = 1u;
    if (wave == 7) {
#pragma nounroll
      for (int spin = 0; spin < SPIN_MAX; ++spin) {
        const u32 f = *LDSP(volatile u32, (u32)(uintptr_t)&sm.mflag[lane & 7]);
        if (all_ok(f != 0u)) break;
        __builtin_amdgcn_s_sleep(1);
      }
      CFENCE();
      st8_sc1(wsr, G_MOD + (blk * 64 + lane) * 8, pack_bf(sum8(&sm.r2[lane * 20]), sum8(&sm.r2[lane * 20 + 8])), tag);
    }
    STAMP(9);
  };
  if (lane < 4) sm.zero[lane] = u32x4{0u, 0u, 0u, 0u};
  if (part == 2 && has_w) {
    if (tid < 8) sm.mflag[tid] = 0u;
    LDS_BARRIER();
    if (__builtin_expect(modc, 0)) {
      small_ops();
      asm volatile("s_waitcnt vmcnt(0)" ::: "memory");
      tag = next_tag(__builtin_amdgcn_readfirstlane(ep[0]));
      mod_stage();
      __builtin_amdgcn_s_barrier();
    }
  }
  SB();
  const int fd = (xcd * 8 + i8) * 8 + (lane & 3) * 2;
  const int ub = wave >> 2, ud = (wave & 3) * 128 + c * 8 + g * 2;
  u32x4 sg[3]; u32 sx2;
  auto side_loads = [&]() {
    const bool fin = part == 1 && wave == 0;
    sg[0] = ld16<AUX_SC1>(wsr, is_up ? G_MOD + (1024 + ud) * 8 : (fin ? G_MOD + (1024 + fd) * 8 : OOB), 0);
    sg[1] = ld16<AUX_SC1>(wsr, is_up ? G_MOD + (1536 + ud) * 8 : (fin ? G_MOD + (2560 + fd) * 8 : OOB), 0);
    sg[2] = ld16<AUX_SC1>(wsr, is_up ? G_MOD + (2048 + ud) * 8 : OOB, 0);
    sx2 = __builtin_amdgcn_raw_buffer_load_b32(mk_rsrc(x, (is_up || fin) ? 2048u : 0u), is_up ? ub * 1024 + ud * 2 : (g & 1) * 1024 + fd * 2, 0, 0);
  };
  auto side_pin = [&]() { PIN(sg[0]); PIN(sg[1]); PIN(sg[2]); PIN(sx2); };
  const u32 mlo = G_ML + (head * 2 + b) * 64 + (lane & 3) * 16;
  float fmrg = 0.f;
  auto merge_factor = [&](const u32x4& ml) {
    const float mi = u2f(ml[0]);
    float M = fmaxf(mi, dpp<XOR1>(mi)); M = fmaxf(M, dpp<XOR2>(M));
    const float e = __expf(mi - M);
    float L = e * u2f(ml[2]);
    L += dpp<XOR1>(L); L += dpp<XOR2>(L);
    fmrg = rdl(e * __builtin_amdgcn_rcpf(L), part);
  };
  f32x2 ga = {0.f, 0.f}, xin = {0.f, 0.f}, cc1 = {0.f, 0.f};
  float Af = 0.f, Bf = 0.f;
  const int hpos = ub * 512 + (ud & ~15) + (g & 1) * 8 + (c & 1) * 4 + (g >> 1) * 2;
  auto up_prep = [&]() {
    if (!all_ok((sg[0][1] == tag) & (sg[0][3] == tag) & (sg[1][1] == tag) & (sg[1][3] == tag) & (sg[2][1] == tag) & (sg[2][3] == tag))) gather16<3>(wsr, G_MOD + (1024 + ud) * 8, 512 * 8, tag, sg);
    ga = f32x2{ub ? hi_f(sg[0][0]) : lo_f(sg[0][0]), ub ? hi_f(sg[0][2]) : lo_f(sg[0][2])};
    cc1 = f32x2{rbf(1.f + (ub ? hi_f(sg[2][0]) : lo_f(sg[2][0]))), rbf(1.f + (ub ? hi_f(sg[2][2]) : lo_f(sg[2][2])))};
    xin = f32x2{lo_f(sx2), hi_f(sx2)};
    u32* hp = reinterpret_cast<u32*>(&sm.hb[hpos]);
    hp[512] = pack_bf(cc1[0], cc1[1]);
    hp[1024] = ub ? perm_hi(sg[1][0], sg[1][2]) : perm_lo(sg[1][0], sg[1][2]);
  };
  u32x8 bw[4];
  auto up_frags = [&]() {
    const int pp = lane & 3;
    const u32 a = wimg + (pp & 1) * 32768 + ((pp >> 1) * 32 + g * 8 + ((lane >> 2) & 3)) * 64 + (((wave >> 1) ^ ((g & 1) | (pp & 2))) * 16) + (wave & 1) * 8;
#pragma unroll
    for (int j = 0; j < 4; ++j) {
      const u32x2 r0 = tr_read(a + j * 4096), r1 = tr_read(a + j * 4096 + 256), r2 = tr_read(a + j * 4096 + 16384), r3 = tr_read(a + j * 4096 + 16640);
      bw[j] = u32x8{r0[0], r0[1], r1[0], r1[1], r2[0], r2[1], r3[0], r3[1]};
    }
  };
  u32 wfa[4];
  auto wo_frags = [&](int s0) {
#pragma unroll
    for (int t = 0; t < 4; ++t) {
#pragma unroll
      for (int s = s0; s < 2; ++s) {
        const u32x2 lo = tr_read(wfa[t] + s * 4096), hi = tr_read(wfa[t ^ 1] + s * 4096 + 256);
        bw[t][s * 4] = lo[0]; bw[t][s * 4 + 1] = lo[1]; bw[t][s * 4 + 2] = hi[0]; bw[t][s * 4 + 3] = hi[1];
      }
    }
  };
  const bool par = lane & 8;
  auto limit = [&](auto sc) {
    constexpr int s = sc.v;
    WAIT_VM(window(s, 1 + (PRM0.W[s >> 3] >> (4 * (s & 7)) & 7)));
    SB();
  };
  auto sample = [&]() { if (timer) lag += (int)(((u32)wall_clock64() - t0) << 8) - due + ((int)__builtin_amdgcn_s_getreg(7 | (3 << 11)) + 1) * tau; };
  auto report = [&](u32 dst) {
    if (timer) {
      if (lane == 0) sm.mflag[wave] = vote * 65536 + min(max(lag >> 8, 0), 4000);
      lag = 0;
      LDS_BARRIER();
      if (wave == 7) {
        int s = sm.mflag[lane & 7];
        s += __builtin_amdgcn_mov_dpp(s, XOR1, 0xf, 0xf, true); s += __builtin_amdgcn_mov_dpp(s, XOR2, 0xf, 0xf, true); s += __builtin_amdgcn_mov_dpp(s, XOR7, 0xf, 0xf, true);
        if (lane < 8) st8_sc1(wsr, dst + lane * 128 + wi * 8, s, tag);
      }
    }
    SB();
  };
  float l0 = 0.f, l1 = 0.f;
  u32x2 gmd[2], tbg;
  int t1 = 0;
#pragma unroll
  for (int i = SK0; i < SK1; ++i) kr[i] = u32x4{0u, 0u, 0u, 0u};
  const int first = SK0 + NL - nl;
  auto left_out = [&](int i) { int f = first; asm volatile("" : "+s"(f)); return i < f; };
  sfor<1, NL>([&](auto ic) {
    constexpr int i = ic.v;
    if constexpr (i >= SK0 && i < SK1) { if (left_out(i)) return true; }
    const u32 so = __builtin_amdgcn_readlane(sot, i);
    gate();
    if constexpr ((i >= TBS && i <= TBP) || (i >= RPS && i <= RPP)) sample();
    SB();
    if constexpr (i >= SK0 && i < SK1) ld16_into(kr[i], rk, lane * 16, so); else kr[i] = ld16<AUX_NT>(rk, lane * 16, so);
    if constexpr (i == SMO) small_ops();
    if constexpr (i == SMO + 1) { if (voter && (xcd & 3) == 0) __builtin_amdgcn_raw_buffer_store_b32(0u, wsr, pbo + (lane >> 5) * 8192 + (lane & 31) * 4, 0, AUX_SC1); }
    if constexpr (i == SK1) tbg = ld8<AUX_SC1>(wsr, wave == 7 ? G_VT + xcd * 128 + c * 8 : OOB, 0);
    if constexpr (i == TBL) tbg = ld8<AUX_SC1>(wsr, wave == 7 ? G_TB + xcd * 128 + c * 8 : OOB, 0);
    if constexpr (i == QP) { gmd[0] = ld8<AUX_SC1>(wsr, part == 1 ? G_MOD + tid * 8 : OOB, 0); gmd[1] = ld8<AUX_SC1>(wsr, part == 1 ? G_MOD + tid * 8 : OOB, 4096); }
    SB();
    SB();
    if (i >= 4) limit(ic);
    if (i == 8) STAMP(12);
    if (i == 20) STAMP(21);
    if (i == 32) STAMP(22);
    if (i == TBP) {
      if (timer) {
        if (lane == 0) sm.mflag[wave] = lag;
        lag = 0;
        LDS_BARRIER();
        if (wave == 7) {
          int s = sm.mflag[lane & 7];
          s += __builtin_amdgcn_mov_dpp(s, XOR1, 0xf, 0xf, true); s += __builtin_amdgcn_mov_dpp(s, XOR2, 0xf, 0xf, true); s += __builtin_amdgcn_mov_dpp(s, XOR7, 0xf, 0xf, true);
          if (lane < 8) st8_sc1(wsr, G_TB + lane * 128 + wi * 8, t0 << 10 | (u32)min(s / (2048 * (TBP - TBS + 1)), 1023), tag);
        }
      }
      SB();
    }
    if (i == PRB) {
      if (voter) {
        u64 ta, tb, tc; u32 dm;
        const u32 pa = pbo + (wave & 1) * 8192;
        asm volatile("s_memtime %0\n\ts_waitcnt lgkmcnt(0)\n\ts_load_dword %3, %4, %5\n\ts_waitcnt lgkmcnt(0)\n\ts_memtime %1\n\ts_waitcnt lgkmcnt(0)\n\t"
                     "s_load_dword %3, %4, %6\n\ts_waitcnt lgkmcnt(0)\n\ts_memtime %2\n\ts_waitcnt lgkmcnt(0)"
                     : "=&s"(ta), "=&s"(tb), "=&s"(tc), "=&s"(dm) : "s"(ws), "s"(pa), "s"(pa ^ 8192u));
        const int d = min(max((int)(2 * (u32)tb - (u32)ta - (u32)tc), -500), 500);
        vote = ((wave ^ die) & 1) ? -d : d;
        STAMP(26);
      }
      SB();
    }
    if (i == TBU) {
      if (wave == 7) {
        const bool ok = tbg[1] == tag;
        float lf = ok ? (float)min(max((int)((t0 << 10 | 1023u) - tbg[0]) >> 10, -300), 300) : 0.f, nf = ok ? 1.f : 0.f, lg = ok ? (float)(tbg[0] & 1023u) : 0.f;
        lf += dpp<XOR1>(lf); nf += dpp<XOR1>(nf); lg += dpp<XOR1>(lg); lf += dpp<XOR2>(lf); nf += dpp<XOR2>(nf); lg += dpp<XOR2>(lg);
        lf += dpp<XOR7>(lf); nf += dpp<XOR7>(nf); lg += dpp<XOR7>(lg); lf += dpp<XOR8>(lf); nf += dpp<XOR8>(nf); lg += dpp<XOR8>(lg);
        const float rn = __builtin_amdgcn_rcpf(fmaxf(nf, 1.f));
        shift = __builtin_amdgcn_readfirstlane(min(max((int)((lg * rn - (float)P.GS) * (float)P.GB * 0.125f), 0), 150));
        if (lane == 0) sm.bc = u32x2{tag, t0 - (u32)(int)(lf * rn) + shift};
      }
      LDS_BARRIER();
      const u32x2 bc = sm.bc;
      tag = __builtin_amdgcn_readfirstlane(bc[0]);
      t1 = (__builtin_amdgcn_readfirstlane((int)(bc[1] - t0)) + TEND + P.X1) * 256;
      tau -= __builtin_amdgcn_readfirstlane((int)((float)(int)(t0 - bc[1]) * 256.f * __builtin_amdgcn_rcpf((float)(nl - TBU - 1))));
      SB();
    }
    if (i == RPP) report(G_VT);
    if (i == LNS) {
      tag = next_tag(__builtin_amdgcn_readfirstlane(ep[0]));
      if (part == 1) {
        float x0[8], x1[8], mb0, ri0, mb1, ri1;
#pragma unroll
        for (int k = 0; k < 4; ++k) { x0[2 * k] = lo_f(xr0[k]); x0[2 * k + 1] = hi_f(xr0[k]); x1[2 * k] = lo_f(xr1[k]); x1[2 * k + 1] = hi_f(xr1[k]); }
        ln_stats2(x0, x1, mb0, ri0, mb1, ri1);
        l0 = rbf(rbf(lo_f(xs0) - mb0) * ri0); l1 = rbf(rbf(lo_f(xs1) - mb1) * ri1);
      }
      PIN(l0); PIN(l1);
      SB();
    }
    if (i == WUSE) {
      PIN(tch1); PIN(tch2);
      if ((part == 1 || (part == 2 && has_w && !modc))) {
        WAIT_VM(WK); CFENCE();
        if (part == 2) mod_stage(); else wo_prep();
      }
#pragma unroll
      for (int k = 0; k < 8; ++k) PIN(bf[k]);
      SB();
    }
    if (i == QP + QD) {
      if (part == 1) {
        u32 gm[2] = {gmd[0][0], gmd[1][0]};
        if (!all_ok((gmd[0][1] == tag) & (gmd[1][1] == tag))) { STAMP(11); gather8<2>(wsr, G_MOD + tid * 8, 512 * 8, tag, gm); }
        STAMP(5);
        gemv_go(pack_bf(rbf(l0 * rbf(1.f + lo_f(gm[1]))) + lo_f(gm[0]), rbf(l1 * rbf(1.f + hi_f(gm[1]))) + hi_f(gm[0])));
      }
      SB();
    }
    if (i == QP + QD + 1) {
      if (part == 1) {
        LDS_BARRIER();
        if (wave == 0) {
          const int bb = lane >> 5, jq = lane & 31;
          st8_sc1(wsr, G_Q + (bb * 2048 + blk * 32 + jq) * 8, pack_bf(sum8(&sm.r2[2 * jq * 20 + bb * 8]), sum8(&sm.r2[(2 * jq + 1) * 20 + bb * 8])), tag);
        }
        STAMP(6);
      }
      SB();
    }
    return true;
  });
  PIN(sot);
  if (wave == 7) {
    const bool ok = tbg[1] == tag;
    float lg = ok ? (float)(tbg[0] & 0xffffu) : 0.f, nf = ok ? 8.f * (RPP - RPS + 1) : 0.f;
    lg += dpp<XOR1>(lg); nf += dpp<XOR1>(nf); lg += dpp<XOR2>(lg); nf += dpp<XOR2>(nf); lg += dpp<XOR7>(lg); nf += dpp<XOR7>(nf); lg += dpp<XOR8>(lg); nf += dpp<XOR8>(nf);
    lg *= __builtin_amdgcn_rcpf(fmaxf(nf, 1.f));
    if (lane == 0) sm.bc2 = u32x2{(u32)min(shift * P.DG * 32, P.EW * 256), (u32)min(max((int)((lg - (float)P.B0) * (float)(P.BG * 32) + fmaxf(lg - (float)P.TC, 0.f) * (float)(P.RT * 32)) - shift * P.D0 * 32, 0), 200 * 256)};
  }
  LDS_BARRIER();
  {
    const u32x2 bc = sm.bc2;
    const int st = __builtin_amdgcn_readfirstlane(bc[1]), sp = (st * P.BF) >> 3;
    t1 += st + __builtin_amdgcn_readfirstlane(bc[0]);
    due += P.TG * 256 + sp;
    tau = (te1 >> 16) + __builtin_amdgcn_readfirstlane((int)((float)(st - sp) * __builtin_amdgcn_rcpf((float)(nl - 3))));
  }
  const u32 qoff = G_Q + ((b * 32 + head) * 64 + c * 4) * 8;
  u32x4 qg[2], mlg;
  u32 qq[4];
  constexpr int NB = (NL + 15) / 16;
  float xlo, sc[NB], pr[NB], m = 0.f;
  f32x4 acc = {0.f, 0.f, 0.f, 0.f};
  u32 pb[NB], pm[4], pl8[8], cpf;
  const u32 adst0 = lds_addr(lane < 32 ? &sm.red[wave][c * 8 + g * 4] : &sm.r2[(lane - 32) * 4]);
  u32 po, pso, rdst, rsrc, xo;
  auto wo_addr = [&]() {
    po = dup(G_P + (xcd & 3) * 8192) + tid * 16 + near;
    pso = tid < 16 ? pslot + c * 128 + near : OOB;
    PIN(pso);
    rdst = (u32)(uintptr_t)&sm.red2[wave][(lane & 31) * 4 + (lane >> 5) * 2];
    rsrc = (u32)(uintptr_t)&sm.red2[0][((wave & 1) * 16 + c) * 4 + (g & 1) * 2 + (g >> 1)];
    xo = dup(G_XP + xcd * 2048 + (wave & 1) * 1024 + (blk * 64 + (lane & ~1)) * 2) + (lane & 1) * 8192;
    PIN(po); PIN(rdst); PIN(rsrc); PIN(xo);
    const int q = (lane >> 2) & 3, pp = lane & 3, r = (g & 1) * 4 + (q & 1);
#pragma unroll
    for (int t = 0; t < 4; ++t) { wfa[t] = wimg + wave * 8192 + ((g >> 1) * 16 + (q >> 1) * 8 + r) * 128 + (pp & 1) * 8 + ((t * 2 + (pp >> 1)) ^ r ^ (q & 2)) * 16; PIN(wfa[t]); }
  };
  const u32 nadd = wdef ? P.LW : (is_up ? P.LU : 0);
  u32 nto = tid < 2 ? dup(G_NT) + bid * 8 + tid * 8192 : OOB;
  PIN(nto);
  auto notices = [&]() -> int {
    u32 off = dup(G_NT) + near + lane * 16;
    PIN(off);
    until(due);
    int mx;
#pragma nounroll
    for (int spin = 0; spin < SPIN_MAX; ++spin) {
      CFENCE();
      const u32x4 ga_ = ld16<AUX_SC1>(wsr, off, 0), gb_ = ld16<AUX_SC1>(wsr, off, 1024);
      mx = max(max((int)(ga_[0] - t0), (int)(ga_[2] - t0)), max((int)(gb_[0] - t0), (int)(gb_[2] - t0)));
      if (all_ok((((ga_[1] ^ tag) | (ga_[3] ^ tag)) | ((gb_[1] ^ tag) | (gb_[3] ^ tag))) == 0u)) break;
      for (int k = 0; k < P.AG; ++k) __builtin_amdgcn_s_sleep(1);
    }
    if (all_ok(mx <= (t1 >> 8) - P.AC)) return t1;
    mx = max(mx, __builtin_amdgcn_mov_dpp(mx, XOR1, 0xf, 0xf, true)); mx = max(mx, __builtin_amdgcn_mov_dpp(mx, XOR2, 0xf, 0xf, true));
    mx = max(mx, __builtin_amdgcn_mov_dpp(mx, XOR7, 0xf, 0xf, true)); mx = max(mx, __builtin_amdgcn_mov_dpp(mx, XOR8, 0xf, 0xf, true));
    mx = max(max(__builtin_amdgcn_readlane(mx, 0), __builtin_amdgcn_readlane(mx, 16)), max(__builtin_amdgcn_readlane(mx, 32), __builtin_amdgcn_readlane(mx, 48)));
    return max(min(mx, 1 << 20) * 256, t1 + P.L0 * 256) + P.L1 * 256;
  };
  auto pv_row = [&](auto jc) {
    constexpr int i = jc.v;
    const u32 pv = __float_as_uint(bcast16(u2f(pb[i >> 4]), i & 15));
    acc = mfma16(u32x4{pv & pm[0], pv & pm[1], pv & pm[2], pv & pm[3]}, vr[i], acc);
    PIN(acc);
    return true;
  };
  sfor<0, NL - 1>([&](auto ic) {
    constexpr int v = ic.v;
    if constexpr (v >= SK0 && v < SK1) { if (left_out(v)) return true; }
    if constexpr (v > 0) {
      const u32 so = __builtin_amdgcn_readlane(sot, v);
      if constexpr (v >= SK1) { if (v == NL - P.NE) tau = (tau * P.FE) >> 4; }
      gate();
      if constexpr (v >= RVS && v <= RVP) sample();
      SB();
      if constexpr (v >= SK0 && v < SK1) ld16_into(vr[v], rv, lane * 16, so); else vr[v] = ld16<AUX_NT>(rv, lane * 16, so);
      SB();
      SB();
    }
    if constexpr (v == SK0 - 1) {
#pragma unroll
      for (int i = SK0; i < SK1; ++i) { vr[i] = u32x4{0u, 0u, 0u, 0u}; PIN(vr[i]); }
    }
    if constexpr (v == VQ - 4) { qg[0] = ld16<AUX_SC1>(wsr, qoff, 0); qg[1] = ld16<AUX_SC1>(wsr, qoff + 16, 0); }
    if constexpr (v == VSIDE) side_loads();
    if constexpr (v == CPF) {
      u64 pc; u32 dlt, len;
      asm volatile("s_getpc_b64 %0\n.Lcpf_here:\n\ts_mov_b32 %1, .Ltail_start-.Lcpf_here\n\ts_mov_b32 %2, .Lend_a-.Ltail_start\n\ts_max_i32 %2, %2, .Lend_b-.Ltail_start\n\ts_max_i32 %2, %2, .Lend_c-.Ltail_start\n\ts_max_i32 %2, %2, .Lend_d-.Ltail_start\n\ts_max_i32 %2, %2, .Lend_e-.Ltail_start" : "=s"(pc), "=s"(dlt), "=s"(len) :: "scc");
      pc += dlt;
      const rsrc_t rc = mk_rsrc((const void*)pc, (part == 3 && i8 == 1) ? len : 0u);
      cpf = __builtin_amdgcn_raw_buffer_load_b32(rc, tid * 64, 0, 0);
    }
    if constexpr (v == SK1 + 2) PIN(cpf);
    if constexpr (v == RVP) report(G_VT + 1024);
    if constexpr (v == RVL) tbg = ld8<AUX_SC1>(wsr, wave == 7 ? G_VT + (g ? 0 : 1024) + xcd * 128 + c * 8 : OOB, 0);
    if constexpr (v == SK1) mlg = ld16<AUX_SC1>(wsr, wave == 0 ? mlo : OOB, 0);
    SB();
    if constexpr (NL + v < 72) limit(IC<NL + v>{});
    if constexpr (v >= SK0 + WV) { WAIT_VM(v < SK1 ? WV : WV + 1); SB(); }
    if constexpr (v == VQ) {
      if (!all_ok((qg[0][1] == tag) & (qg[0][3] == tag) & (qg[1][1] == tag) & (qg[1][3] == tag))) { STAMP(20); gather16<2>(wsr, qoff, 16, tag, qg); }
      STAMP(1); WSTAMP(1);
      qq[0] = qg[0][0]; qq[1] = qg[0][2]; qq[2] = qg[1][0]; qq[3] = qg[1][2];
    }
    if constexpr (v >= VQ && v < VS) {
      constexpr int sl = v - VQ, bt = sl >> 1;
      float xs[8];
#pragma unroll
      for (int i = 0; i < 8; ++i) {
        float d = 0.f;
        if (sl * 8 + i < NL) {
#pragma unroll
          for (int k = 0; k < 4; ++k) d = dot2(kr[sl * 8 + i][k], qq[k], d);
        }
        xs[i] = d;
      }
#pragma unroll
      for (int h = 0; h < 4; ++h) xs[h] = sel_add<XOR7>(xs[h], xs[h + 4], c & 4);
#pragma unroll
      for (int h = 0; h < 2; ++h) xs[h] = sel_add<XOR2>(xs[h], xs[h + 2], c & 2);
      xs[0] = sel_add<XOR1>(xs[0], xs[1], c & 1);
      if constexpr ((sl & 1) || sl == NSL - 1) {
        const float t = (sl & 1) ? sel_add<XOR8>(xlo, xs[0], c & 8) : xs[0] + dpp<XOR8>(xs[0]);
        const float s = rbf(rbf(t) * 0.08838834764831845f);
        sc[bt] = (bt * 16 + c < SK0 || (bt * 16 + c >= first && bt * 16 + c < NL)) ? s : -__builtin_inff();
        PIN(sc[bt]);
      } else {
        xlo = xs[0];
        PIN(xlo);
      }
    }
    if constexpr (v == VS) {
      float mloc = sc[0];
#pragma unroll
      for (int bt = 1; bt < NB; ++bt) mloc = fmaxf(mloc, sc[bt]);
      m = fmaxf(wave_max(mloc), -3.0e38f);
      float ls = 0.f;
#pragma unroll
      for (int bt = 0; bt < NB; ++bt) { pr[bt] = __expf(sc[bt] - m); ls += pr[bt]; }
      const float l = wave_sum(ls);
      if (lane == 0) { sm.ml[wave][0] = m; sm.ml[wave][1] = l; }
      if (c == ((NL - 1) & 15)) sm.pl[g][wave] = pr[NB - 1];
      STAMP(2); WSTAMP(2);
    }
    if constexpr (v >= SK1) pv_row(IC<v - SK1 + SK0 - WV>{});
    if constexpr (v < SK0 || v >= SK0 + WV) sfor<cons_end(v - 1), cons_end(v)>(pv_row);
    if constexpr (v == RVU) {
      if (wave == 7) {
        if (__builtin_expect(!all_ok(tbg[1] == tag || g == 0), 0)) {
          u32 vg[1];
          gather8<1>(wsr, G_VT + xcd * 128 + c * 8, 0, tag, vg);
          if (g) tbg = u32x2{vg[0], tag};
        }
        const bool ok = tbg[1] == tag;
        float lg = ok ? (float)(g ? (int)tbg[0] >> 16 : (int)(tbg[0] & 0xffffu)) : 0.f, nf = ok ? 8.f * (RVP - RVS + 1) : 0.f;
        lg += dpp<XOR1>(lg); nf += dpp<XOR1>(nf); lg += dpp<XOR2>(lg); nf += dpp<XOR2>(nf); lg += dpp<XOR7>(lg); nf += dpp<XOR7>(nf); lg += dpp<XOR8>(lg); nf += dpp<XOR8>(nf);
        lg *= __builtin_amdgcn_rcpf(fmaxf(nf, 1.f));
        if (lane == 16) sm.bc4 = __float_as_uint(lg);
        if (lane == 0) sm.bc3 = (u32)min(max((int)((lg - (float)P.F0) * (float)(P.FG * 32)), 0), 200 * 256);
      }
      LDS_BARRIER();
      const int st = __builtin_amdgcn_readfirstlane(sm.bc3), sp = (st * P.FF_) >> 3;
      near = ((__builtin_amdgcn_readfirstlane(sm.bc4) >> 31 ^ die ^ P.NR ^ 1) & 1) * 8192u;
      t1 += st;
      due += sp;
      tau += __builtin_amdgcn_readfirstlane((int)((float)(st - sp) * __builtin_amdgcn_rcpf((float)(nl - RVU - 3))));
    }
    if constexpr (v == SK1 + 1) {
      float p[8];
#pragma unroll
      for (int w = 0; w < 8; ++w) p[w] = sm.pl[g][w] * __expf(sm.ml[w][0] - m);
#pragma unroll
      for (int w = 0; w < 8; ++w) pl8[w] = pack_bf(p[w], p[w]);
#pragma unroll
      for (int w = 0; w < 8; ++w) PIN(pl8[w]);
    }
    if constexpr (v == MLS - 1) { side_pin(); if (is_up) up_prep(); PIN(ga); PIN(xin); PIN(cc1); }
    if constexpr (v == UPREP - 1) wo_addr();
    if constexpr (v == UPREP) {
#pragma unroll
      for (int k = 0; k < 4; ++k) asm volatile("" : "=v"(bw[k]));
      if (part == 0) wo_frags(0); else if (is_up) up_frags();
#pragma unroll
      for (int k = 0; k < 4; ++k) PIN(bw[k]);
    }
    if constexpr (v == UPREP + 1) {
      if (is_up) {
        const u32x4* a1 = reinterpret_cast<const u32x4*>(&sm.hb[1024 + (c >> 2 & 1) * 1024 + (c >> 3) * 512 + (g >> 1) * 256 + (c >> 1 & 1) * 32 + (g & 1) * 16 + (c & 1) * 8]);
        f32x4 d = {0.f, 0.f, 0.f, 0.f};
#pragma unroll
        for (int j = 0; j < 4; ++j) d = mfma64(a1[j * 8], bw[j], d, (c & 1) ? 0xeeeeu : 0x4444u);
        float t = par ? d[2] + d[3] : d[0] + d[1];
        t += dpp<XOR8>(t);
        const auto ab = __builtin_amdgcn_permlane16_swap(__float_as_uint(t), __float_as_uint(t), false, false);
        Af = u2f(ab[0]); Bf = u2f(ab[1]);
      }
      PIN(Af); PIN(Bf);
    }
    if (v == 10 || v == 16) STAMP(15 + v / 6);
    if (v == 20) STAMP(18);
    if (v == 32) STAMP(19);
    if constexpr (v == MLS) {
      LDS_BARRIER();
      float mm[8], ll[8];
#pragma unroll
      for (int w = 0; w < 8; ++w) { const f32x2 t = *reinterpret_cast<const f32x2*>(&sm.ml[w][0]); mm[w] = t[0]; ll[w] = t[1]; }
      float Mwg = mm[0], Lwg = 0.f;
#pragma unroll
      for (int w = 1; w < 8; ++w) Mwg = fmaxf(Mwg, mm[w]);
      if (wave == 0) {
#pragma unroll
        for (int w = 0; w < 8; ++w) Lwg = fmaf(__expf(mm[w] - Mwg), ll[w], Lwg);
        if (lane == 0) st16_sc1(wsr, G_ML + ((head * 2 + b) * 4 + part) * 16, __float_as_uint(Mwg), __float_as_uint(Lwg), tag);
      }
      const float fw = __expf(m - Mwg);
      m = Mwg;
#pragma unroll
      for (int bt = 0; bt < NB; ++bt) { pb[bt] = pack_bf(pr[bt] * fw, pr[bt] * fw); PIN(pb[bt]); }
#pragma unroll
      for (int k = 0; k < 4; ++k) { pm[k] = (c >> 1 & 3) == k ? ((c & 1) ? 0xffff0000u : 0x0000ffffu) : 0u; PIN(pm[k]); }
    }
    SB();
    return true;
  });
  asm volatile(".Ltail_start:");
  if (wave != 0) {
    pv_row(IC<SK0 - 1>{});
    WAIT_VM(WV); SB();
    sfor<NL - 1 - WV, NL - 1>(pv_row);
    *LDSP(f32x4, adst0) = acc;
    WSTAMP(14);
    LDS_BARRIER();
  } else {
    u32x4 xr[8], am[8];
#pragma unroll
    for (int w = 0; w < 8; ++w) am[w] = u32x4{pl8[w] & pm[0], pl8[w] & pm[1], pl8[w] & pm[2], pl8[w] & pm[3]};
#pragma unroll
    for (int w = 0; w < 8; ++w) PIN(am[w]);
    {
      const u32 so = __builtin_amdgcn_readlane(sot, NL - 1);
      gate();
      SB();
#pragma unroll
      for (int w = 0; w < 8; ++w) xr[w] = ld16<AUX_NT>(rv, lane * 16, so ^ ((w & 1) ? col(P, 7) : 0u) ^ ((w & 2) ? col(P, 8) : 0u) ^ ((w & 4) ? col(P, 9) : 0u));
      SB();
    }
    u32 rsum = lds_addr(&sm.red[0][c * 8 + (g & 1) * 4]);
    pv_row(IC<SK0 - 1>{});
    WAIT_VM(WV + 8); SB();
    sfor<NL - 1 - WV, NL - 3>(pv_row);
    if (__builtin_expect(!all_ok((mlg[1] == tag) & (mlg[3] == tag)), 0)) {
#pragma nounroll
      for (int spin = 0; spin < SPIN_MAX; ++spin) {
        CFENCE();
        mlg = ld16<AUX_SC1>(wsr, mlo, 0);
        if (all_ok((mlg[1] == tag) & (mlg[3] == tag))) break;
      }
    }
    merge_factor(mlg);
    sfor<NL - 3, NL - 1>(pv_row);
    STAMP(14); WSTAMP(14);
    LDS_BARRIER();
    STAMP(15);
    f32x4 t[8];
#pragma unroll
    for (int w = 1; w < 8; ++w) t[w] = LDSP(f32x4, rsum)[w * 32];
    acc = ((acc + t[1]) + (t[2] + t[3])) + ((t[4] + t[5]) + (t[6] + t[7]));
    PIN(acc);
    STAMP(3);
    SB();
#pragma unroll
    for (int w = 0; w < 8; ++w) acc = mfma16(am[w], xr[w], acc);
  }
  WAIT_VM(0);
  acc *= fmrg;
  {
    const u32 w0 = min(pack_bf(acc[0], acc[1]), SENT - 1u), w1 = min(pack_bf(acc[2], acc[3]), SENT - 1u);
    const auto e0 = __builtin_amdgcn_permlane16_swap(w0, w0, false, false), e1 = __builtin_amdgcn_permlane16_swap(w1, w1, false, false);
    __builtin_amdgcn_raw_buffer_store_b128(u32x4{e0[0], e1[0], e0[1], e1[1]}, wsr, pso, 0, AUX_SC1);
  }
  st8_sc1(wsr, nto, (u32)wall_clock64() + nadd, tag);
  SB();
  STAMP(4);

  if (part == 2) FINISH();
  if (part == 0) {
    __builtin_amdgcn_s_barrier();
#pragma nounroll
    while (wleft > 0) { until(t1 - (P.PA + wleft * P.PD) * 256); piece(); }
    bool late = false;
    u32x4 og[1];
    f32x4 d[4];
    u32 six = (c & 8) ? 0xeeeeu : 0x4444u;
    PIN(six);
#pragma unroll
    for (int t = 0; t < 4; ++t) { d[t] = f32x4{0.f, 0.f, 0.f, 0.f}; PIN(d[t]); }
    if (wdef) {
      late = true;
      while ((int)(((u32)wall_clock64() - t0) << 8) < t1 - 10 * 256) if (__builtin_amdgcn_s_getreg(7 | (3 << 11)) == 0) { late = false; break; }
      if (__builtin_expect(late, 0)) { until(t1); CFENCE(); og[0] = ld16<AUX_SC1>(wsr, po, 0); SB(); WAIT_VM(1); } else { WAIT_VM(0); }
      CFENCE();
      wo_frags(1);
#pragma unroll
      for (int k = 0; k < 4; ++k) PIN(bw[k]);
      SB();
    }
    if (__builtin_expect(!late, 1)) { until(t1); CFENCE(); og[0] = ld16<AUX_SC1>(wsr, po, 0); }
    STAMP(11);
    SB();
    auto wo_mm = [&]() {
#pragma unroll
      for (int t = 0; t < 4; ++t) d[t] = mfma64(og[0], bw[t], d[t], six);
    };
    auto p_bad = [&]() { return !all_ok(max(max(og[0][0], og[0][1]), max(og[0][2], og[0][3])) != SENT); };
    wo_mm();
    if (__builtin_expect(p_bad(), 0)) {
#pragma nounroll
      for (int spin = 0; spin < SPIN_MAX; ++spin) {
        __builtin_amdgcn_s_sleep(1);
        CFENCE();
        og[0] = ld16<AUX_SC1>(wsr, po, 0);
        if (!p_bad()) break;
      }
#pragma unroll
      for (int t = 0; t < 4; ++t) d[t] = f32x4{0.f, 0.f, 0.f, 0.f};
      wo_mm();
      SB();
      asm volatile(".Lend_d:");
    }
    STAMP(5); WSTAMP(5);
    {
      SB();
      f32x4 y; f32x2 h[4];
#pragma unroll
      for (int t = 0; t < 4; ++t) { h[t] = d[t].xy + d[t].zw; PIN(h[t]); }
#pragma unroll
      for (int t = 0; t < 4; ++t) { y[t] = h[t][0] + h[t][1]; PIN(y[t]); }
      *LDSP(f32x2, rdst) = f32x2{swap32_add(y[0], y[1]), swap32_add(y[2], y[3])};
    }
    STAMP(16);
    LDS_BARRIER();
    STAMP(17);
    if (wave < 2) {
      f32x2 v[4];
#pragma unroll
      for (int w = 0; w < 4; ++w) v[w] = f32x2{LDSP(float, rsrc)[w * 256], LDSP(float, rsrc)[w * 256 + 128]};
      const f32x2 h = (v[0] + v[1]) + (v[2] + v[3]);
      const float y = h[0] + h[1];
      __builtin_amdgcn_raw_buffer_store_b32(min(pack_bf(dpp<0xA0>(y), dpp<0xF5>(y)), SENT - 1u), wsr, xo, 0, AUX_SC1);
      asm volatile(".Lend_a:");
    }
    STAMP(6);
    FINISH();
  }
  if (part == 3) {
    if (!has_w) FINISH();
    u32x4 b2[4];
    {
      __builtin_amdgcn_s_barrier();
      const rsrc_t r2 = mk_rsrc(w2 + (size_t)(blk * 32 + wave * 4) * 512, 4096u);
      const u32 l2 = (u32)(uintptr_t)(&sm.w2b[wave * 4][0]);
#pragma unroll
      for (int n = 0; n < 4; ++n) { until(t1 - (P.QA + (4 - n) * P.QD) * 256); dma16(r2, (lane ^ (n * 2) ^ ((wave & 2) << 2)) * 16, n * 1024, l2 + n * 1024); }
    }
    const u32 tdst = lds_addr(&sm.hb[hpos]);
    const u32 sdst = lds_addr(&sm.stat[wave][lane]);
    const u32 ssrc = lds_addr(&sm.stat[(g >> 1) * 4 + ((lane >> 3) & 3)][(lane & 7) * 4]);
    const u32 adst = lds_addr((c < 4 && (g & 1) == 0) ? &sm.actl[g >> 1][wave * 4 + c] : reinterpret_cast<u16*>(&sm.dummy[lane]));
    const u32 asrc = lds_addr(&sm.actl[lane & 1][g * 8]);
    const u32 apa = lds_addr(&sm.hb[(c >> 3) * 512 + (g >> 1) * 256 + (c >> 1 & 1) * 64 + ((c >> 2) & 1) * 32 + (g & 1) * 16 + (c & 1) * 8]);
    u32 six = (c & 1) ? 0xeeeeu : 0x4444u;
    f32x4 d[2] = {{0.f, 0.f, 0.f, 0.f}, {0.f, 0.f, 0.f, 0.f}};
    PIN(six); PIN(d[0]); PIN(d[1]);
    constexpr u32 xs2 = 16384;
    u32 xoff = dup(G_XP) + near + g * 2048 + ub * 1024 + (wave & 3) * 256 + c * 16;
    u32x4 one;
#pragma unroll
    for (int k = 0; k < 4; ++k) one[k] = ((c & 2) == 0 && (c >> 2) == k) ? ((c & 1) ? 0x3f800000u : 0x00003f80u) : 0u;
    PIN(one);
    u32 ooff = dup(G_OP + (blk * 512 + tid) * 8);
    float Afp = Af, Bfp = Bf;
    PIN(xoff); PIN(ooff); PIN(Afp); PIN(Bfp);
    f32x2 gc = ga * cc1, xc = xin * cc1;
    PIN(ga); PIN(xin); PIN(gc); PIN(xc);
    u32 w2a = (u32)(uintptr_t)(&sm.w2b[0][0]) + (g * 8 + ((lane >> 2) & 3)) * 1024 + (wave ^ (g & 1)) * 128 + (lane & 15) * 8;
    PIN(w2a);
    if (wave == 1) {
      due = t1 + P.N1 * 256;
      const int tn = notices();
      due = tn + P.X2 * 256;
      until(due);
    }
    asm volatile("s_waitcnt vmcnt(0)" ::: "memory");
    STAMP(9);
    __builtin_amdgcn_s_barrier();
    STAMP(11);
    CFENCE();
    u32x4 xg[2] = {ld16<AUX_SC1>(wsr, xoff, 0), ld16<AUX_SC1>(wsr, xoff, xs2)};
    SB();
#pragma unroll
    for (int t = 0; t < 4; ++t) {
      const u32x2 lo = tr_read(w2a ^ (t * 32)), hi = tr_read((w2a ^ (t * 32)) + 4096);
      b2[t] = u32x4{lo[0], lo[1], hi[0], hi[1]};
    }
#pragma unroll
    for (int t = 0; t < 4; ++t) PIN(b2[t]);
    SB();
    const f32x4 z4 = {0.f, 0.f, 0.f, 0.f};
    auto xp_bad = [&]() { return !all_ok(max(max(max(xg[0][0], xg[0][1]), max(xg[0][2], xg[0][3])), max(max(xg[1][0], xg[1][1]), max(xg[1][2], xg[1][3]))) != SENT); };
    f32x4 v = mfma16(one, xg[1], mfma16(one, xg[0], z4));
    if (__builtin_expect(xp_bad(), 0)) {
#pragma nounroll
      for (int spin = 0; spin < SPIN_MAX; ++spin) {
        __builtin_amdgcn_s_sleep(1);
        CFENCE();
        xg[0] = ld16<AUX_SC1>(wsr, xoff, 0); xg[1] = ld16<AUX_SC1>(wsr, xoff, xs2);
        if (!xp_bad()) break;
      }
      v = mfma16(one, xg[1], mfma16(one, xg[0], z4));
      SB();
      asm volatile(".Lend_e:");
    }
    STAMP(5); WSTAMP(5);
    {
      const f32x2 a = v.xy, hc = __builtin_elementwise_fma(gc, a, xc), x1 = __builtin_elementwise_fma(ga, a, xin);
      *LDSP(u32, tdst) = pack_bf(hc[0], hc[1]);
      *LDSP(float, sdst) = swap32_add(x1[0] + x1[1], fmaf(x1[0], x1[0], x1[1] * x1[1]));
    }
    STAMP(16);
    LDS_BARRIER();
    STAMP(17);
    {
      const f32x4 s4 = LDSP(f32x4, ssrc)[0], q4 = LDSP(f32x4, ssrc)[8];
      SB();
      const u32x4 af0 = LDSP(u32x4, apa)[0], af1 = LDSP(u32x4, apa)[16];
      SB();
      const f32x2 s2 = s4.xy + s4.zw, q2 = q4.xy + q4.zw;
      float s = s2[0] + s2[1], q = q2[0] + q2[1];
      SB();
      d[0] = mfma64(af0, bw[0], d[0], six);
      SB();
      s = swap16_add(s, q);
      SB();
      d[1] = mfma64(af0, bw[1], d[1], six);
      SB();
      s += dpp<XOR1>(s); s += dpp<XOR2>(s);
      SB();
      d[0] = mfma64(af1, bw[2], d[0], six);
      SB();
      s += dpp<XOR7>(s); s += dpp<XOR8>(s);
      SB();
      d[1] = mfma64(af1, bw[3], d[1], six);
      SB();
      const auto sq = __builtin_amdgcn_permlane16_swap(__float_as_uint(s), __float_as_uint(s), false, false);
      const float mean = u2f(sq[0]) * (1.f / 512.f);
      const float var = fmaf(u2f(sq[1]), 1.f / 512.f, -mean * mean);
      float ri = __builtin_amdgcn_rsqf(fmaxf(var, 0.f) + 1e-5f);
      PIN(ri); PIN(d[0]); PIN(d[1]);
      SB();
      float y0 = d[0][0] + d[0][1], y1 = d[1][2] + d[1][3], mr = -ri * mean;
      PIN(y0); PIN(y1); PIN(mr);
      float y = y0 + y1, cf = fmaf(mr, Afp, Bfp), rk = ri * -1.44269504f;
      PIN(y); PIN(cf); PIN(rk);
      y = __builtin_bit_cast(float, __builtin_amdgcn_update_dpp(__builtin_bit_cast(int, y), __builtin_bit_cast(int, y), XOR8, 0xa, 0xf, false));
      const float ck = cf * -1.44269504f;
      y = swap16_add(y, y);
      const float ex = __builtin_amdgcn_exp2f(fmaf(y, rk, ck));
      y = fmaf(y, ri, cf);
      const float gu = y * dpp<0x104>(y);
      *LDSP(u16, adst) = (u16)pack_bf(gu * __builtin_amdgcn_rcpf(1.f + ex), 0.f);
    }
    STAMP(18);
    LDS_BARRIER();
    STAMP(10);
    {
      const u32x4 af = *LDSP(u32x4, asrc);
      const f32x4 z = {0.f, 0.f, 0.f, 0.f};
      f32x4 d0 = mfma16(af, b2[0], z), d1 = mfma16(af, b2[1], z), d2 = mfma16(af, b2[2], z), d3 = mfma16(af, b2[3], z);
      asm volatile("" : "+v"(d0), "+v"(d1), "+v"(d2), "+v"(d3));
      const bool g1 = g & 1, g2 = g & 2;
      const float e0 = g1 ? d1[0] : d0[0], e1 = g1 ? d1[1] : d0[1], f0 = g1 ? d3[0] : d2[0], f1 = g1 ? d3[1] : d2[1];
      const u32x2 ov = {pack_bf(g2 ? f0 : e0, g2 ? f1 : e1), tag};
      __builtin_amdgcn_raw_buffer_store_b64(ov, wsr, ooff, near ^ 8192u, AUX_SC1);
      __builtin_amdgcn_raw_buffer_store_b64(ov, wsr, ooff, near, AUX_SC1);
    }
    asm volatile(".Lend_b:");
    STAMP(6);
    FINISH();
  }
  if (wave == 0) {
    if (!all_ok((sg[0][1] == tag) & (sg[0][3] == tag) & (sg[1][1] == tag) & (sg[1][3] == tag))) {
      u32x4 t[2];
      gather16<2>(wsr, G_MOD + (1024 + fd) * 8, 1536 * 8, tag, t);
      sg[0] = t[0]; sg[1] = t[1];
    }
    u32 xa;
    {
      due = t1 + P.N2 * 256;
      const int tn = notices();
      due = tn + P.X3 * 256;
      until(due);
    }
    const bool hb = g & 1;
#pragma nounroll
    for (int spin = 0; spin < SPIN_MAX; ++spin) {
      CFENCE();
      xa = __builtin_amdgcn_raw_buffer_load_b32(wsr, dup(G_XP) + near + (g >> 1) * 16384 + (c >> 2) * 2048 + (g & 1) * 1024 + fd * 2, 0, AUX_SC1);
      if (all_ok(xa != SENT)) break;
      __builtin_amdgcn_s_sleep(1);
    }
    float a0 = lo_f(xa), a1 = hi_f(xa);
    a0 += dpp<0x124>(a0); a1 += dpp<0x124>(a1);
    a0 += dpp<XOR8>(a0); a1 += dpp<XOR8>(a1);
    a0 = swap32_add(a0, a0); a1 = swap32_add(a1, a1);
    f32x2 xv = {rbf(lo_f(sx2) + rbf((hb ? hi_f(sg[0][0]) : lo_f(sg[0][0])) * rbf(a0))), rbf(hi_f(sx2) + rbf((hb ? hi_f(sg[0][2]) : lo_f(sg[0][2])) * rbf(a1)))};
    f32x2 gv = {hb ? hi_f(sg[1][0]) : lo_f(sg[1][0]), hb ? hi_f(sg[1][2]) : lo_f(sg[1][2])};
    PIN(xv); PIN(gv);
    if (wi == 0 && lane == 0) __builtin_amdgcn_raw_buffer_store_b32(tag, wsr, WS_EPOCH, 0, AUX_SC1);
    STAMP(10);
    due = max(due + P.X4 * 256, (int)(((u32)wall_clock64() - t0) << 8) + P.GX * 256);
    const bool last = lane < (NUP - 32) * 4;
    u32 o0 = dup(G_OP + (lane >> 2) * 4096 + fd * 8) + near, o2 = last ? o0 : OOB, tg2 = last ? tag : 0u;
    const rsrc_t rout = mk_rsrc(out, 2048u);
    u32 oo = (lane & 44) == 0 ? (g * 512 + fd) * 2 : OOB;
    const u32 pk = (c & 10) ? 0u : ((c & 4) ? 0x3f800000u : 0x00003f80u);
    u32x4 one = {(c & 1) ? 0u : pk, 0u, (c & 1) ? pk : 0u, 0u};
    PIN(o0); PIN(o2); PIN(tg2); PIN(oo); PIN(one);
    __builtin_amdgcn_raw_buffer_store_b32(pack_bf(xv[0], xv[1]), rout, oo, 0, AUX_SC1);
    u32 s1_ = 131072, s2_ = 262144;
    asm volatile("" : "+s"(s1_), "+s"(s2_));
    until(due);
    f32x4 sv;
#pragma nounroll
    for (int spin = 0; spin < SPIN_MAX; ++spin) {
      CFENCE();
      const u32x4 og[3] = {ld16<AUX_SC1>(wsr, o0, 0), ld16<AUX_SC1>(wsr, o0, s1_), ld16<AUX_SC1>(wsr, o2, s2_)};
      sv = f32x4{0.f, 0.f, 0.f, 0.f};
#pragma unroll
      for (int k = 0; k < 3; ++k) sv = mfma16(one, og[k], sv);
      SB();
      u32 bad = ((og[0][1] ^ tag) | (og[0][3] ^ tag) | (og[1][1] ^ tag)) | ((og[1][3] ^ tag) | (og[2][1] ^ tg2) | (og[2][3] ^ tg2));
      PIN(bad);
      SB();
      float s0 = sv[0] + dpp<0x124>(sv[0]), s1 = sv[1] + dpp<0x124>(sv[1]);
      PIN(s0); PIN(s1);
      s0 += dpp<XOR8>(s0); s1 += dpp<XOR8>(s1);
      PIN(s0); PIN(s1);
      __builtin_amdgcn_raw_buffer_store_b32(pack_bf(fmaf(gv[0], s0, xv[0]), fmaf(gv[1], s1, xv[1])), rout, oo, 0, AUX_SC1);
      SB();
      if (all_ok(bad == 0u)) break;
      __builtin_amdgcn_s_sleep(1);
    }
    STAMP(7);
    asm volatile(".Lend_c:");
  }
  FINISH();
}

extern "C" void kernel_launch(void* const* d_in, const int* in_sizes, int n_in,
                              void* d_out, int out_size, void* d_ws, size_t ws_size,
                              hipStream_t stream) {
  fused_kernel<<<256, 512, 0, stream>>>((const u16*)d_in[1], (const u16*)d_in[2], (const u16*)d_in[4], (const u16*)d_in[3], (const u16*)d_in[10], (const u16*)d_in[6], (const u16*)d_in[7],
                                        (unsigned char*)d_ws, (const u16*)d_in[0], (const u16*)d_in[9], (const u16*)d_in[8], (u16*)d_out);
}
```

```cpp
#include <hip/hip_runtime.h>
#include <stdint.h>


typedef unsigned int u32;
typedef unsigned short u16;
typedef unsigned long long u64;
typedef __attribute__((ext_vector_type(4))) unsigned u32x4;
typedef __attribute__((ext_vector_type(2))) unsigned u32x2;
typedef __attribute__((ext_vector_type(2))) __bf16 bf16x2;
typedef __attribute__((ext_vector_type(4))) float f32x4;
typedef __attribute__((ext_vector_type(2))) float f32x2;
typedef __amdgpu_buffer_rsrc_t rsrc_t;

constexpr int NH = 32, SK = 16384, DH = 128, WIN = 4096, FF = 1376;
struct Prm { int E0, ES, E1, E2, E2C, EU, TEND, DT_UP, L0, L1, X1, X2, X3, X4, NE, FE, M0, M1, M2, M3, M4, GS, GX, N1, N2, GA, WD, DT_WO, LW, LU, EW, PM, NF, NR, C0, A0, AG, AC, TS, TW, TK, TG, GB, B0, BG, BF, D0, DG, TC, LM, C1, F0, FG, FF_, PA, PD, QA, QD, RT, SA, SG, SC, W[9]; };
constexpr Prm PRM0 = {32, 20, 32, 32, 60, 60, 1960, 76, 12, 27, 75, 63, 100, 75, 3, 8, 0x2511f55b, 0x2b7425d5, 0x6017665, 0x17e0b9ec, 0x31944305, 62, 30, 29, 62, 50, 4, 106, 50, 28, 10, 0x0, 12, 14, 3, 36, 4, 4, 509, 215, 80, -2, 6, 69, 10, 8, 8, 6, 87, 120, 2, 58, 9, 4, 52, 14, 9, 18, 12, 45, 10, 16, 0x77777777, 0x77777777, 0x77777777, 0x77777777, 0x77777777, 0x44444477, 0x44444444, 0x44444444, 0x44444444};
constexpr int NL = 37;
constexpr int SK0 = 21, SK1 = 32;
constexpr int NCMIN = (NL - SK1 + SK0) * 8;
constexpr int WK = 8, WV = 5;
constexpr int WUSE = 12, LNS = 7;
constexpr int QP = 14, QD = 4;
constexpr int VQ = 6;
constexpr int NSL = (NL + 7) / 8;
constexpr int VS = VQ + NSL;
constexpr int MLS = VS + 2;
constexpr int VSIDE = MLS - 5, UPREP = MLS + 6;
constexpr int CPF = 17;
constexpr int SMO = 1;
constexpr int RVS = 5, RVP = 8, RVL = 14, RVU = 18;
constexpr int RPS = 23, RPP = 27;
constexpr int PRB = 12;
constexpr int TBS = 7, TBP = 10, TBL = 13, TBU = 17;
constexpr int window(int s, int w) {
  int n = w;
  for (int u = s - w + 1; u <= s; ++u) n += u == SMO ? 6 : (u == TBL || u == SK1 || u == NL + CPF || u == NL + RVL || u == NL + SK1 ? 1 : (u == QP || u == NL + VQ - 4 ? 2 : (u == NL + VSIDE ? 4 : 0)));
  return n;
}
constexpr int cons_end(int v) {
  const int a = v - WV + 1, c = (v - MLS) * 4;
  const int m = a < c ? a : c;
  return m < 0 ? 0 : (m > NL ? NL : m);
}
constexpr u32 col(const Prm& P, int j) { const int m[6] = {P.M0, P.M1, P.M2, P.M3, P.M4, P.PM}; return (u32)(m[j / 3] >> (j % 3 * 10) & 1023) << 10; }
constexpr u32 first_col(const Prm& P, int k) {
  if (k < 4) return col(P, 12 + k) | 1u << (24 + k);
  if (k < 6) return col(P, k - 4);
  if (k < 8) return col(P, 4 + k) | 1u << (16 + k);
  if (k < 11) return col(P, k - 1);
  return (col(P, 2) ^ col(P, 3) ^ col(P, 4) ^ col(P, 5) ^ col(P, 6) ^ col(P, 10) ^ col(P, 11)) | 0xf00000u;
}
constexpr bool one_to_one(const Prm& P) {
  u32 c[10] = {};
  for (int j = 0; j < 10; ++j) c[j] = col(P, j);
  for (int j = 0; j < 10; ++j) {
    if (c[j] == 0) return false;
    const u32 low = c[j] & (0u - c[j]);
    for (int k = j + 1; k < 10; ++k) if (c[k] & low) c[k] ^= c[j];
  }
  return true;
}
static_assert(one_to_one(PRM0), "every KB of the window exactly once");
struct Tab { u32 e[512]; };
constexpr Tab make_tab(const Prm& P) {
  Tab T{};
  for (int bid = 0; bid < 256; ++bid) {
    const int xcd = bid & 7, part = bid >> 4 & 3, i8 = (bid >> 3 & 1) | bid >> 6 << 1, wi = i8 * 8 + xcd;
    const bool has_mod = wi < 48, has_up = wi >= 21;
    const int cb0 = (i8 + P.C0) & 7, cb1 = (i8 + P.C1) & 7, ms = wi - 20;
    const int mi = wi < 8 ? wi : (wi < 16 ? 8 + ((xcd + 4) & 7) : (wi < 20 ? (wi - 14) * 8 + 3 : 16 + ms / 7 * 8 + ms % 7 + (ms % 7 >= 3 ? 1 : 0)));
    const int ui = xcd * 5 + (xcd > 5 ? xcd - 5 : 0) + (i8 == 6 ? 0 : (xcd >= 5 ? 1 : 0) + (i8 == 7 ? 4 : i8 - 2));
    const bool edge = i8 >= 6;
    const int wd = cb0 == 3 ? 0 : P.WD;
    const int e[4] = {P.E0 - 4 * wd + (cb0 == 3 ? P.ES : 0), P.E1 + (cb1 == 3 ? P.ES : 0), has_mod ? (wi < 16 ? P.E2C : P.E2) + ((mi & 7) == 3 ? P.ES : 0) : 0, has_up ? P.EU : 0};
    int tw[4] = {}, win[4] = {}, span[4] = {}, k0[4] = {};
    for (int k = 0; k < 4; ++k) {
      const int np = k == 0 ? 8 - wd : 8;
      tw[k] = np ? e[k] * P.TW / (16 * np) : 0;
      k0[k] = P.NF + tw[k] * np / 2 > P.TK ? P.NF + tw[k] * np / 2 : P.TK;
      span[k] = P.TEND - k0[k] - P.TG - (k == 0 ? (wd ? P.DT_WO : P.A0) : (k == 3 && has_up ? P.DT_UP : 0));
      win[k] = span[k] - (k == 3 && has_up ? P.GA + (edge ? P.LM : 0) : 0);
    }
    int n[4] = {NCMIN / 8, NCMIN / 8, NCMIN / 8, NCMIN / 8};
    for (int left = 128 - NCMIN / 2; left > 0; --left) {
      int best = -1;
      for (int k = 3; k >= 0; --k)
        if (n[k] < NL && (best < 0 || n[k] * win[best] < n[best] * win[k])) best = k;
      ++n[best];
    }
    const int c[4] = {0, n[0] - 1, n[0] + n[1] - 2, n[0] + n[1] + n[2] - 3};
    const int nl = n[part];
    const bool has_w = part < 2 || (part == 2 ? has_mod : has_up);
    const int blk = !has_w ? 0 : (part == 0 ? cb0 : (part == 1 ? xcd * 8 + cb1 : (part == 2 ? mi : ui)));
    T.e[2 * bid] = (u32)(c[part] | nl << 7 | blk << 13 | (has_w ? 1 : 0) << 19 | (part == 0 ? wd : 0) << 20) | (u32)tw[part] << 24;
    T.e[2 * bid + 1] = (u32)(span[part] * P.TS / (4 * (nl - 1)) | span[part] * (1024 - P.TS) / (4 * (nl - 1)) << 16);
  }
  return T;
}
constexpr int step_range(const Prm& P, bool mx_) {
  int mx = 0, mn = 1 << 20;
  const Tab T = make_tab(P);
  for (int bid = 0; bid < 256; ++bid) { const int n = T.e[2 * bid] >> 7 & 63; mx = n > mx ? n : mx; mn = n < mn ? n : mn; }
  return mx_ ? mx : mn;
}
extern "C" __attribute__((used, visibility("protected"))) __constant__ const Tab wg_tab = make_tab(PRM0);
static_assert(step_range(PRM0, true) <= NL && step_range(PRM0, false) >= NCMIN / 8, "a wave's key range does not fit its load steps");
static_assert(RVU < UPREP && UPREP + 1 < SK0 && QP + QD + 1 < SK0 && cons_end(SK0 - 1) == SK0 - WV && NL - SK1 == WV, "steps every wave runs");
constexpr bool timers_long(const Prm& P) {
  const Tab T = make_tab(P);
  for (int bid = 48; bid < 64; ++bid) if (SK0 + NL - (int)(T.e[2 * bid] >> 7 & 63) > RPS) return false;
  return true;
}
static_assert(timers_long(PRM0), "steps of the second report");
constexpr int NUP = FF / 32;
constexpr int SPIN_MAX = 1 << 17;

constexpr u32 WS_EPOCH = 0;
constexpr u32 G_TB = 1024;
constexpr u32 G_MOD = 4096;
constexpr u32 G_Q = G_MOD + 3072 * 8;
constexpr u32 G_ML = G_Q + 4096 * 8;
constexpr u32 G_U = G_ML + 256 * 16;
constexpr u32 G_P = 0;
constexpr u32 G_XP = G_P + 128 * 256;
constexpr u32 G_OP = G_XP + 8 * 512 * 4;
constexpr u32 G_NT = G_OP + 43 * 512 * 8;
constexpr u32 G_PB = G_OP + 44 * 512 * 8;
constexpr u32 G_VT = G_U + 2 * (G_PB + 8192);
constexpr u32 WS_END = G_VT + 2048;
__device__ __forceinline__ u32 dup(u32 o) { return G_U + ((o >> 13) << 14) + (o & 8191u); }
constexpr u32 SENT = 0xffffffffu;
constexpr u32 OOB = 0x40000000u;

constexpr int AUX_NT = 2, AUX_SC1 = 16;

__device__ __forceinline__ float lo_f(u32 v) { return __uint_as_float(v << 16); }
__device__ __forceinline__ float hi_f(u32 v) { return __uint_as_float(v & 0xffff0000u); }
__device__ __forceinline__ float rbf(float x) { return (float)(__bf16)x; }
__device__ __forceinline__ u32 pack_bf(float lo, float hi) {
  bf16x2 r; r[0] = (__bf16)lo; r[1] = (__bf16)hi; return __builtin_bit_cast(u32, r);
}
__device__ __forceinline__ float dot2(u32 k, u32 q, float acc) {
  return __builtin_amdgcn_fdot2_f32_bf16(__builtin_bit_cast(bf16x2, k), __builtin_bit_cast(bf16x2, q), acc, false);
}
template <int CTRL> __device__ __forceinline__ float dpp(float x) {
  return __builtin_bit_cast(float, __builtin_amdgcn_mov_dpp(__builtin_bit_cast(int, x), CTRL, 0xf, 0xf, true));
}
constexpr int XOR1 = 0xB1, XOR2 = 0x4E, XOR7 = 0x141, XOR8 = 0x128;
__device__ __forceinline__ float swap32_add(float a, float b) {
  auto t = __builtin_amdgcn_permlane32_swap(__float_as_uint(a), __float_as_uint(b), false, false);
  return __uint_as_float(t[0]) + __uint_as_float(t[1]);
}
__device__ __forceinline__ float swap16_add(float a, float b) {
  auto t = __builtin_amdgcn_permlane16_swap(__float_as_uint(a), __float_as_uint(b), false, false);
  return __uint_as_float(t[0]) + __uint_as_float(t[1]);
}
__device__ __forceinline__ float wave_sum(float x) {
  x += dpp<XOR1>(x); x += dpp<XOR2>(x); x += dpp<XOR7>(x); x += dpp<XOR8>(x);
  x = swap16_add(x, x);
  return swap32_add(x, x);
}
__device__ __forceinline__ float wave_max(float x) {
  x = fmaxf(x, dpp<XOR1>(x)); x = fmaxf(x, dpp<XOR2>(x)); x = fmaxf(x, dpp<XOR7>(x)); x = fmaxf(x, dpp<XOR8>(x));
  auto s = __builtin_amdgcn_permlane16_swap(__float_as_uint(x), __float_as_uint(x), false, false);
  x = fmaxf(__uint_as_float(s[0]), __uint_as_float(s[1]));
  auto t = __builtin_amdgcn_permlane32_swap(__float_as_uint(x), __float_as_uint(x), false, false);
  return fmaxf(__uint_as_float(t[0]), __uint_as_float(t[1]));
}
template <int CTRL> __device__ __forceinline__ float sel_add(float lo, float hi, bool up) {
  const float t1 = lo + dpp<CTRL>(lo), t2 = hi + dpp<CTRL>(hi);
  return up ? t2 : t1;
}
template <int I> __device__ __forceinline__ float bc16(float x) {
  return __builtin_bit_cast(float, __builtin_amdgcn_update_dpp(0, __builtin_bit_cast(int, x), 0x150 + I, 0xf, 0xf, false));
}
__device__ __forceinline__ float bcast16(float x, int i) {
  switch (i) {
    case 0: return bc16<0>(x); case 1: return bc16<1>(x); case 2: return bc16<2>(x); case 3: return bc16<3>(x);
    case 4: return bc16<4>(x); case 5: return bc16<5>(x); case 6: return bc16<6>(x); case 7: return bc16<7>(x);
    case 8: return bc16<8>(x); case 9: return bc16<9>(x); case 10: return bc16<10>(x); case 11: return bc16<11>(x);
    case 12: return bc16<12>(x); case 13: return bc16<13>(x); case 14: return bc16<14>(x); default: return bc16<15>(x);
  }
}
__device__ __forceinline__ float silu_f(float x) { return x * __builtin_amdgcn_rcpf(1.f + __expf(-x)); }

template <int AUX> __device__ __forceinline__ u32x4 ld16(rsrc_t r, u32 voff, u32 soff) {
  return __builtin_amdgcn_raw_buffer_load_b128(r, voff, soff, AUX);
}
template <int AUX> __device__ __forceinline__ u32x2 ld8(rsrc_t r, u32 voff, u32 soff) {
  return __builtin_amdgcn_raw_buffer_load_b64(r, voff, soff, AUX);
}
__device__ __forceinline__ void st8_sc1(rsrc_t r, u32 voff, u32 v, u32 tag) {
  __builtin_amdgcn_raw_buffer_store_b64(u32x2{v, tag}, r, voff, 0, AUX_SC1);
}
__device__ __forceinline__ void st16_sc1(rsrc_t r, u32 voff, u32 v0, u32 v1, u32 tag) {
  __builtin_amdgcn_raw_buffer_store_b128(u32x4{v0, tag, v1, tag}, r, voff, 0, AUX_SC1);
}
__device__ __forceinline__ rsrc_t mk_rsrc(const void* p, u32 bytes) {
  return __builtin_amdgcn_make_buffer_rsrc(const_cast<void*>(p), 0, bytes, 0x00020000);
}
#define WAIT_VM(n) __builtin_amdgcn_s_waitcnt(((n) & 15) | (((n) >> 4) << 14) | (7 << 4) | (15 << 8))
#define SB() __builtin_amdgcn_sched_barrier(0)
#define CFENCE() asm volatile("" ::: "memory")
__device__ __forceinline__ bool all_ok(bool ok) { return __builtin_amdgcn_ballot_w64(!ok) == 0ull; }

template <int N> __device__ __forceinline__ void gather16(rsrc_t ws, u32 voff, u32 stride, u32 tag, u32x4 (&g)[N]) {
#pragma nounroll
  for (int spin = 0; spin < SPIN_MAX; ++spin) {
    CFENCE();
    bool ok = true;
#pragma unroll
    for (int i = 0; i < N; ++i) g[i] = ld16<AUX_SC1>(ws, voff + i * stride, 0);
#pragma unroll
    for (int i = 0; i < N; ++i) ok &= (g[i][1] == tag) & (g[i][3] == tag);
    if (all_ok(ok)) break;
    __builtin_amdgcn_s_sleep(8);
  }
}
template <int N> __device__ __forceinline__ void gather8(rsrc_t ws, u32 voff, u32 stride, u32 tag, u32 (&g)[N], bool act = true) {
#pragma nounroll
  for (int spin = 0; spin < SPIN_MAX; ++spin) {
    CFENCE();
    bool ok = true;
    u32x2 t[N];
#pragma unroll
    for (int i = 0; i < N; ++i) t[i] = ld8<AUX_SC1>(ws, voff + i * stride, 0);
#pragma unroll
    for (int i = 0; i < N; ++i) { ok &= (t[i][1] == tag); g[i] = t[i][0]; }
    if (all_ok(ok | !act)) break;
    __builtin_amdgcn_s_sleep(8);
  }
}

struct Smem {
  float red[8][128];
  float ml[8][2];
  float r2[64 * 20];
  u16 hb[3 * 1024];
  float stat[8][64];
  float red2[8][128];
  u32 dummy[64];
  u32x2 bc, bc2;
  u32 bc3, bc4;
  u32x4 zero[4];
  float pl[4][8];
  u32 mflag[8];
  u32x4 wbuf[8][8][64];
  __attribute__((aligned(256))) u16 w2b[32][512];
  u16 actl[2][32];
};

#define PIN(x) asm volatile("" : "+v"(x))
#define LDS_BARRIER() do { __builtin_amdgcn_s_waitcnt(0xc07f); __builtin_amdgcn_s_barrier(); CFENCE(); } while (0)
#define WAVE_SYNC() do { __builtin_amdgcn_fence(__ATOMIC_RELEASE, "wavefront"); __builtin_amdgcn_wave_barrier(); __builtin_amdgcn_fence(__ATOMIC_ACQUIRE, "wavefront"); } while (0)

__device__ __forceinline__ void dma16(rsrc_t r, u32 voff, u32 soff, u32 lds_dst) {
  u32 keep;
  asm volatile("s_mov_b32 %0, m0\n\ts_mov_b32 m0, %4\n\ts_nop 4\n\tbuffer_load_dwordx4 %1, %2, %3 offen nt lds\n\ts_mov_b32 m0, %0"
               : "=&s"(keep) : "v"(voff), "s"(r), "s"(soff), "s"(lds_dst) : "memory");
}
__device__ __forceinline__ void ld16_into(u32x4& d, rsrc_t r, u32 voff, u32 soff) {
  asm volatile("s_nop 4\n\tbuffer_load_dwordx4 %0, %1, %2, %3 offen nt" : "+v"(d) : "v"(voff), "s"(r), "s"(soff) : "memory");
}
__device__ __forceinline__ u32 perm_lo(u32 a, u32 b) { return __builtin_amdgcn_perm(b, a, 0x05040100u); }
__device__ __forceinline__ u32 perm_hi(u32 a, u32 b) { return __builtin_amdgcn_perm(b, a, 0x07060302u); }
__device__ __forceinline__ float rdl(float x, int l) { return __int_as_float(__builtin_amdgcn_readlane(__float_as_int(x), l)); }

typedef __attribute__((ext_vector_type(8))) __bf16 bf16x8;
__device__ __forceinline__ f32x4 mfma16(u32x4 a, u32x4 b, f32x4 c) {
  return __builtin_amdgcn_mfma_f32_16x16x32_bf16(__builtin_bit_cast(bf16x8, a), __builtin_bit_cast(bf16x8, b), c, 0, 0, 0);
}
typedef __attribute__((ext_vector_type(8))) unsigned u32x8;
typedef __attribute__((ext_vector_type(16))) __bf16 bf16x16;
__device__ __forceinline__ f32x4 mfma64(u32x4 a, u32x8 b, f32x4 c, u32 idx) {
  return __builtin_amdgcn_smfmac_f32_16x16x64_bf16(__builtin_bit_cast(bf16x8, a), __builtin_bit_cast(bf16x16, b), c, idx, 0, 0);
}
__device__ __forceinline__ float sum8(const float* p) {
  const f32x4 a = *reinterpret_cast<const f32x4*>(p), b = *reinterpret_cast<const f32x4*>(p + 4);
  return ((a[0] + b[0]) + (a[1] + b[1])) + ((a[2] + b[2]) + (a[3] + b[3]));
}
__device__ __forceinline__ float ln_ri(float var) {
  return __builtin_amdgcn_rcpf(rbf(__builtin_amdgcn_sqrtf(rbf(rbf(fmaxf(var, 0.f)) + 1e-5f))));
}
__device__ __forceinline__ void ln_stats2(const float (&x0)[8], const float (&x1)[8], float& mb0, float& ri0, float& mb1, float& ri1) {
  const float k0 = rdl(x0[0], 0), k1 = rdl(x1[0], 0);
  float s0 = 0.f, s1 = 0.f, q0 = 0.f, q1 = 0.f;
#pragma unroll
  for (int k = 0; k < 8; ++k) {
    const float d0 = x0[k] - k0, d1 = x1[k] - k1;
    s0 += d0; s1 += d1; q0 = fmaf(d0, d0, q0); q1 = fmaf(d1, d1, q1);
  }
  float t = swap16_add(swap32_add(s0, s1), swap32_add(q0, q1));
  t += dpp<XOR1>(t); t += dpp<XOR2>(t); t += dpp<XOR7>(t); t += dpp<XOR8>(t);
  auto u = __builtin_amdgcn_permlane16_swap(__float_as_uint(t), __float_as_uint(t), false, false);
  const float ms = __uint_as_float(u[0]) * (1.f / 512.f), mq = __uint_as_float(u[1]) * (1.f / 512.f);
  const float ri = ln_ri(fmaf(-ms, ms, mq));
  mb0 = rbf(k0 + rdl(ms, 0)); mb1 = rbf(k1 + rdl(ms, 32));
  ri0 = rdl(ri, 0); ri1 = rdl(ri, 32);
}
template <int I> struct IC { static constexpr int v = I; };
template <int I, int N, class F> __device__ __forceinline__ void sfor(F&& f) {
  if constexpr (I < N) { if (f(IC<I>{})) sfor<I + 1, N>(f); }
}
typedef __attribute__((ext_vector_type(4))) short s16x4;
typedef __attribute__((address_space(3))) s16x4* lds_s16x4p;
__device__ __forceinline__ u32x2 tr_read(u32 lds_addr) {
  return __builtin_bit_cast(u32x2, __builtin_amdgcn_ds_read_tr16_b64_v4i16((lds_s16x4p)lds_addr));
}
__device__ __forceinline__ u32 next_tag(u32 e) { return ((e | 0x40004000u) + 1u) & ~0x40004000u; }
__device__ __forceinline__ float u2f(u32 v) { return __uint_as_float(v); }
#define LDSP(T, a) ((__attribute__((address_space(3))) T*)(a))
template <class T> __device__ __forceinline__ u32 lds_addr(T* p) { u32 a = (u32)(uintptr_t)p; PIN(a); return a; }

#define TAB_LOAD "s_add_u32 s30, s28, wg_tab@rel32@lo+4\ns_addc_u32 s31, s29, wg_tab@rel32@hi+12\nglobal_load_dwordx2 %[te], %[va], s[30:31]\n"
#define FC(k) ".Lfc" #k
#define FX(r, bit, k) "s_bitcmp1_b32 %[" #r "], " #bit "\ns_cselect_b32 %[sb], " FC(k) ", 0\ns_xor_b32 %[sa], %[sa], %[sb]\n"
__global__ void __launch_bounds__(512, 2) fused_kernel(const u16* kc_, const u16* vc_, const u16* wo_, const u16* wq_, const u16* wmod_, const u16* w1_, const u16* w3_,
                                                       unsigned char* ws_, const u16* x_, const u16* cnd_, const u16* w2_, u16* out_) {
  __shared__ __attribute__((aligned(256))) Smem sm;
  const u64 a_ka = (u64)__builtin_amdgcn_kernarg_segment_ptr();
  const int bid = blockIdx.x, tid = threadIdx.x;
  u64 wa, t64; u32x2 te; int tid0;
  u32 ar[14];
  u32 tch2, sa_, sb_, va_;
  u32x4 kr[NL], vr[NL];
  asm volatile(".set .Lfc0, %0\n.set .Lfc1, %1\n.set .Lfc2, %2\n.set .Lfc3, %3\n.set .Lfc4, %4\n.set .Lfc5, %5\n.set .Lfc6, %6\n.set .Lfc7, %7\n.set .Lfc8, %8\n.set .Lfc9, %9\n.set .Lfc10, %10\n.set .Lfc11, %11"
    :: "n"(first_col(PRM0, 0)), "n"(first_col(PRM0, 1)), "n"(first_col(PRM0, 2)), "n"(first_col(PRM0, 3)), "n"(first_col(PRM0, 4)), "n"(first_col(PRM0, 5)),
       "n"(first_col(PRM0, 6)), "n"(first_col(PRM0, 7)), "n"(first_col(PRM0, 8)), "n"(first_col(PRM0, 9)), "n"(first_col(PRM0, 10)), "n"(first_col(PRM0, 11)));
  asm volatile(
    "s_load_dwordx16 s[4:19], %[ka], 0x0\ns_load_dwordx8 s[20:27], %[ka], 0x40\n"
    "s_memrealtime %[t]\n"
    "v_lshlrev_b32 %[va], 3, %[bid]\n"
    "s_getpc_b64 s[28:29]\n.Lcpf_first:\n"
    TAB_LOAD
    "s_cmp_gt_u32 %[bid], 15\ns_cbranch_scc1 .Lno_cpf\n"
    "s_mov_b32 %[sa], .Lend_a-.Lcpf_first\ns_max_i32 %[sa], %[sa], .Lend_b-.Lcpf_first\ns_max_i32 %[sa], %[sa], .Lend_c-.Lcpf_first\ns_max_i32 %[sa], %[sa], .Lend_d-.Lcpf_first\ns_max_i32 %[sa], %[sa], .Lend_e-.Lcpf_first\n"
    "v_lshlrev_b32 %[va], 6, %[tid]\nv_min_u32 %[va], %[sa], %[va]\nglobal_load_dword %[pf], %[va], s[28:29]\n"
    ".Lno_cpf:\n"
    "v_readfirstlane_b32 %[t0], %[tid]\nv_and_b32 %[va], 63, %[tid]\n"
    "s_mov_b32 %[sa], " FC(11) "\n"
    FX(bid, 0, 0) FX(bid, 1, 1) FX(bid, 2, 2) FX(bid, 3, 3) FX(bid, 4, 4) FX(bid, 5, 5) FX(bid, 6, 6) FX(bid, 7, 7) FX(t0, 6, 8) FX(t0, 7, 9) FX(t0, 8, 10)
    "v_lshl_add_u32 %[va], %[va], 4, %[sa]\n"
    "s_waitcnt lgkmcnt(0)\nglobal_load_dwordx4 %[k0], %[va], s[4:5] nt\nglobal_load_dwordx4 %[v0], %[va], s[6:7] nt\n"
    "s_bitcmp1_b32 %[t0], 6\ns_cselect_b64 s[14:15], s[16:17], s[14:15]\n"
    "s_bitcmp1_b32 %[bid], 4\ns_cselect_b64 s[8:9], s[10:11], s[8:9]\ns_cselect_b64 s[12:13], s[14:15], s[12:13]\ns_bitcmp1_b32 %[bid], 5\ns_cselect_b64 %[wa], s[12:13], s[8:9]"
    : "=&{s4}"(ar[0]), "=&{s5}"(ar[1]), "=&{s6}"(ar[2]), "=&{s7}"(ar[3]), "=&{s18}"(ar[4]), "=&{s19}"(ar[5]), "=&{s20}"(ar[6]), "=&{s21}"(ar[7]), "=&{s22}"(ar[8]), "=&{s23}"(ar[9]),
      "=&{s24}"(ar[10]), "=&{s25}"(ar[11]), "=&{s26}"(ar[12]), "=&{s27}"(ar[13]), [wa] "=&s"(wa), [t0] "=&s"(tid0), [t] "=&s"(t64), [te] "=&v"(te), [sa] "=&s"(sa_), [sb] "=&s"(sb_),
      [va] "=&v"(va_), [pf] "=&v"(tch2), [k0] "=&v"(kr[0]), [v0] "=&v"(vr[0])
    : [ka] "s"(a_ka), [bid] "s"(bid), [tid] "v"(tid) : "s8", "s9", "s10", "s11", "s12", "s13", "s14", "s15", "s16", "s17", "s28", "s29", "s30", "s31", "scc", "memory");
  asm volatile("" : "+s"(ar[0]), "+s"(ar[1]), "+s"(ar[2]), "+s"(ar[3]), "+s"(ar[4]), "+s"(ar[5]), "+s"(ar[6]), "+s"(ar[7]), "+s"(ar[8]), "+s"(ar[9]), "+s"(ar[10]), "+s"(ar[11]), "+s"(ar[12]), "+s"(ar[13]));
  asm volatile("" : "+s"(wa), "+s"(t64), "+s"(tid0), "+s"(sa_));
  const u32 tka = (u32)wall_clock64();
  __builtin_assume(tid >= 0 && tid < 512 && tid0 >= 0 && tid0 < 512 && bid >= 0 && bid < 256);
  constexpr Prm P = PRM0;
  const u32 t0 = tka + min(max((int)(tka - (u32)t64) - P.SA, 0) * P.SG >> 4, P.SC);
  const int TEND = P.TEND;
  const int xcd = bid & 7, part = bid >> 4 & 3;
  const int lane = tid & 63, wave = tid0 >> 6;
  SB();
  if ((bid >> 4) == 2) {
    const u32 o = tid < 384 ? G_U + (bid & 15) * 6144 + tid * 16 : OOB;
    __builtin_amdgcn_raw_buffer_store_b128(u32x4{SENT, SENT, SENT, SENT}, mk_rsrc((void*)(ar[4] | (u64)ar[5] << 32), WS_END), o, 0, AUX_SC1);
  }
  SB();
  const bool p3 = part == 3;
  u32 pitch = (u32)(0x005600c001000020ull >> (part * 16) & 0xffff) * 32u;
  const int sh = p3 ? 2 : 3;
  u32 wv0 = (u32)((p3 ? (wave >> 1) * 128 : wave * 64) + (part == 0 ? xcd * 512 : 0) + (lane >> sh)) * pitch + ((lane & (p3 ? 3 : 7)) ^ (lane >> (p3 ? 5 : 3))) * 16u;
  const u32 wimg = (u32)(uintptr_t)(&sm.wbuf[0][0][0]);
  u32 wsz = (part == 0 ? 4096u : 512u) * pitch, wld = wimg + (p3 ? (wave & 1) * 32768 + (wave >> 1) * 8192 : wave * 8192);
  PIN(wv0);
  asm volatile("" : "+s"(wsz), "+s"(wld), "+s"(pitch));
  auto karg = [&](int i) { return ar[2 * i] | (u64)ar[2 * i + 1] << 32; };
  const u64 a_kc = karg(0), a_vc = karg(1);
  unsigned char* ws = (unsigned char*)karg(2);
  const u16 *x = (const u16*)karg(3), *cnd = (const u16*)karg(4), *w2 = (const u16*)karg(5);
  u16* out = (u16*)karg(6);
  const rsrc_t wsr = mk_rsrc(ws, WS_END);
  asm volatile("s_waitcnt vmcnt(3)\n\ts_cmp_eq_u32 %1, 2\n\ts_cbranch_scc1 1f\n\ts_waitcnt vmcnt(2)\n1:" : "+v"(te) : "s"(bid >> 4) : "scc");
  const u32 te0 = __builtin_amdgcn_readfirstlane(te[0]), te1 = __builtin_amdgcn_readfirstlane(te[1]);
  const int bl0 = te0 & 127, nl = te0 >> 7 & 63, blk = te0 >> 13 & 63;
  const bool has_w = te0 >> 19 & 1;
  const rsrc_t rw = mk_rsrc((const void*)wa, wsz);
  const u32 wstep = pitch << (6 - sh), wflip = (p3 || part == 0) ? 32u : 0u, wflip0 = part == 0 ? 32u : 0u;
  u32 wso = blk << (sh + 4);
  int wleft = 8;
  auto piece = [&]() {
    dma16(rw, wv0, wso, wld);
    wso += wstep; wld += 1024;
    wv0 ^= (wleft & 1) ? wflip : wflip0;
    --wleft;
  };
  SB();
  const int b = bid >> 3 & 1, head = xcd * 4 + (bid >> 6 ^ 3);
  const int g = lane >> 4, c = lane & 15;
  const int i8 = b | bid >> 6 << 1, wi = i8 * 8 + xcd;
  const bool is_up = p3 && has_w, modc = part == 2 && wi < 16;
  const size_t base = ((size_t)(b * NH + head) * SK + (SK - WIN)) * DH;
  const rsrc_t rk = mk_rsrc((const u16*)a_kc + base, WIN * DH * 2), rv = mk_rsrc((const u16*)a_vc + base, WIN * DH * 2);
  int tau = te1 & 0xffff;
  const int tauw = te0 >> 24 << 7, wdef = te0 >> 20 & 15;
  int due = P.NF * 256, now = 0;
  auto gate = [&]() {
    while (now < due) { now = (int)(((u32)wall_clock64() - t0) << 8); if (now < due) __builtin_amdgcn_s_sleep(1); }
    due += tau;
  };
  auto until = [&](int t) { while ((int)(((u32)wall_clock64() - t0) << 8) < t); };
  const u32 pslot = dup(G_P + (head & 15) * 2048 + (b * 4 + part) * 16);
  const u32 die = xcd >> 2;
  const bool voter = p3 && i8 == 1, timer = p3 && i8 < 2;
  int vote = 0, lag = 0, shift = 0;
  const u32 pbo = dup(G_PB) + wave * 1152;
  u32 near = 0;
  u32x2 ep; u32 xs0, xs1, tch1; u32x4 xr0, xr1;
  const rsrc_t rx = mk_rsrc(part == 2 ? cnd : x, (part == 1 || (part == 2 && has_w)) ? 2048u : 0u);
  auto small_ops = [&]() {
    ep = ld8<AUX_SC1>(wsr, wave == 7 ? WS_EPOCH : OOB, 0);
    xs0 = __builtin_amdgcn_raw_buffer_load_b16(rx, tid * 2, 0, 0); xs1 = __builtin_amdgcn_raw_buffer_load_b16(rx, tid * 2, 1024, 0);
    xr0 = ld16<0>(rx, part == 1 ? lane * 16 : OOB, 0); xr1 = ld16<0>(rx, part == 1 ? lane * 16 : OOB, 1024);
    tch1 = __builtin_amdgcn_raw_buffer_load_b32(mk_rsrc(w2 + (size_t)blk * 32 * 512, is_up ? 64u : 0u), tid == 384 ? 0 : OOB, 0, AUX_NT);
  };
  if (has_w) {
    if (modc) now = 1 << 30;
#pragma nounroll
    while (wleft > wdef) { gate(); due += tauw - tau; piece(); }
    now = 0;
  }
  due = max(due, P.TK * 256);
  SB();
  u32 sot = sa_ & 0xffc00u;
  {
    const u32 s1 = (bl0 + lane - 1 - (lane >= SK0 ? NL - nl : 0)) ^ (124 + part);
#pragma unroll
    for (int k = 0; k < 7; ++k) sot ^= (u32)-(int)(s1 >> k & 1u) & col(P, k);
    PIN(sot);
  }
  u32 tag = 0;
  u32x4 bf[8];
  auto wo_prep = [&]() {
    const u32 wi_ = wimg + wave * 8192;
    const int q = (lane >> 2) & 3, pp = lane & 3;
#pragma unroll
    for (int s = 0; s < 2; ++s) {
#pragma unroll
      for (int t = 0; t < 4; ++t) {
        const int r0 = s * 32 + g * 8 + q, r1 = r0 + 4, ch = t * 2 + (pp >> 1);
        const u32x2 lo = tr_read(wi_ + r0 * 128 + ((ch ^ (r0 & 7)) * 16) + (pp & 1) * 8), hi = tr_read(wi_ + r1 * 128 + ((ch ^ (r1 & 7)) * 16) + (pp & 1) * 8);
        bf[s * 4 + t] = u32x4{lo[0], lo[1], hi[0], hi[1]};
      }
    }
  };
  auto gemv_go = [&](u32 hh) {
    sm.hb[tid] = (u16)hh; sm.hb[512 + tid] = (u16)(hh >> 16);
    WAVE_SYNC();
    const u32x4* a = (c & 11) == 0 ? reinterpret_cast<const u32x4*>(&sm.hb[(c >> 2) * 512 + wave * 64 + g * 8]) : &sm.zero[0];
    const u32x4 a0 = a[0], a1 = a[(c & 11) == 0 ? 4 : 0];
    const f32x4 z = {0.f, 0.f, 0.f, 0.f};
    f32x4 d[4];
#pragma unroll
    for (int t = 0; t < 4; ++t) d[t] = mfma16(a0, bf[t], z);
#pragma unroll
    for (int t = 0; t < 4; ++t) d[t] = mfma16(a1, bf[4 + t], d[t]);
    if (lane < 32) {
#pragma unroll
      for (int t = 0; t < 4; ++t) sm.r2[(t * 16 + c) * 20 + g * 8 + wave] = d[t][0];
    }
  };
  auto mod_stage = [&]() {
    wo_prep();
    gemv_go(pack_bf(silu_f(lo_f(xs0)), silu_f(lo_f(xs1))));
    __builtin_amdgcn_s_waitcnt(0xc07f);
    if (lane == 0) *LDSP(volatile u32, (u32)(uintptr_t)&sm.mflag[wave]) = 1u;
    if (wave == 7) {
#pragma nounroll
      for (int spin = 0; spin < SPIN_MAX; ++spin) {
        const u32 f = *LDSP(volatile u32, (u32)(uintptr_t)&sm.mflag[lane & 7]);
        if (all_ok(f != 0u)) break;
        __builtin_amdgcn_s_sleep(1);
      }
      CFENCE();
      st8_sc1(wsr, G_MOD + (blk * 64 + lane) * 8, pack_bf(sum8(&sm.r2[lane * 20]), sum8(&sm.r2[lane * 20 + 8])), tag);
    }
  };
  if (lane < 4) sm.zero[lane] = u32x4{0u, 0u, 0u, 0u};
  if (part == 2 && has_w) {
    if (tid < 8) sm.mflag[tid] = 0u;
    LDS_BARRIER();
    if (__builtin_expect(modc, 0)) {
      small_ops();
      asm volatile("s_waitcnt vmcnt(0)" ::: "memory");
      tag = next_tag(__builtin_amdgcn_readfirstlane(ep[0]));
      mod_stage();
      __builtin_amdgcn_s_barrier();
    }
  }
  SB();
  const int fd = (xcd * 8 + i8) * 8 + (lane & 3) * 2;
  const int ub = wave >> 2, ud = (wave & 3) * 128 + c * 8 + g * 2;
  u32x4 sg[3]; u32 sx2;
  auto side_loads = [&]() {
    const bool fin = part == 1 && wave == 0;
    sg[0] = ld16<AUX_SC1>(wsr, is_up ? G_MOD + (1024 + ud) * 8 : (fin ? G_MOD + (1024 + fd) * 8 : OOB), 0);
    sg[1] = ld16<AUX_SC1>(wsr, is_up ? G_MOD + (1536 + ud) * 8 : (fin ? G_MOD + (2560 + fd) * 8 : OOB), 0);
    sg[2] = ld16<AUX_SC1>(wsr, is_up ? G_MOD + (2048 + ud) * 8 : OOB, 0);
    sx2 = __builtin_amdgcn_raw_buffer_load_b32(mk_rsrc(x, (is_up || fin) ? 2048u : 0u), is_up ? ub * 1024 + ud * 2 : (g & 1) * 1024 + fd * 2, 0, 0);
  };
  auto side_pin = [&]() { PIN(sg[0]); PIN(sg[1]); PIN(sg[2]); PIN(sx2); };
  const u32 mlo = G_ML + (head * 2 + b) * 64 + (lane & 3) * 16;
  float fmrg = 0.f;
  auto merge_factor = [&](const u32x4& ml) {
    const float mi = u2f(ml[0]);
    float M = fmaxf(mi, dpp<XOR1>(mi)); M = fmaxf(M, dpp<XOR2>(M));
    const float e = __expf(mi - M);
    float L = e * u2f(ml[2]);
    L += dpp<XOR1>(L); L += dpp<XOR2>(L);
    fmrg = rdl(e * __builtin_amdgcn_rcpf(L), part);
  };
  f32x2 ga = {0.f, 0.f}, xin = {0.f, 0.f}, cc1 = {0.f, 0.f};
  float Af = 0.f, Bf = 0.f;
  const int hpos = ub * 512 + (ud & ~15) + (g & 1) * 8 + (c & 1) * 4 + (g >> 1) * 2;
  auto up_prep = [&]() {
    if (!all_ok((sg[0][1] == tag) & (sg[0][3] == tag) & (sg[1][1] == tag) & (sg[1][3] == tag) & (sg[2][1] == tag) & (sg[2][3] == tag))) gather16<3>(wsr, G_MOD + (1024 + ud) * 8, 512 * 8, tag, sg);
    ga = f32x2{ub ? hi_f(sg[0][0]) : lo_f(sg[0][0]), ub ? hi_f(sg[0][2]) : lo_f(sg[0][2])};
    cc1 = f32x2{rbf(1.f + (ub ? hi_f(sg[2][0]) : lo_f(sg[2][0]))), rbf(1.f + (ub ? hi_f(sg[2][2]) : lo_f(sg[2][2])))};
    xin = f32x2{lo_f(sx2), hi_f(sx2)};
    u32* hp = reinterpret_cast<u32*>(&sm.hb[hpos]);
    hp[512] = pack_bf(cc1[0], cc1[1]);
    hp[1024] = ub ? perm_hi(sg[1][0], sg[1][2]) : perm_lo(sg[1][0], sg[1][2]);
  };
  u32x8 bw[4];
  auto up_frags = [&]() {
    const int pp = lane & 3;
    const u32 a = wimg + (pp & 1) * 32768 + ((pp >> 1) * 32 + g * 8 + ((lane >> 2) & 3)) * 64 + (((wave >> 1) ^ ((g & 1) | (pp & 2))) * 16) + (wave & 1) * 8;
#pragma unroll
    for (int j = 0; j < 4; ++j) {
      const u32x2 r0 = tr_read(a + j * 4096), r1 = tr_read(a + j * 4096 + 256), r2 = tr_read(a + j * 4096 + 16384), r3 = tr_read(a + j * 4096 + 16640);
      bw[j] = u32x8{r0[0], r0[1], r1[0], r1[1], r2[0], r2[1], r3[0], r3[1]};
    }
  };
  u32 wfa[4];
  auto wo_frags = [&](int s0) {
#pragma unroll
    for (int t = 0; t < 4; ++t) {
#pragma unroll
      for (int s = s0; s < 2; ++s) {
        const u32x2 lo = tr_read(wfa[t] + s * 4096), hi = tr_read(wfa[t ^ 1] + s * 4096 + 256);
        bw[t][s * 4] = lo[0]; bw[t][s * 4 + 1] = lo[1]; bw[t][s * 4 + 2] = hi[0]; bw[t][s * 4 + 3] = hi[1];
      }
    }
  };
  const bool par = lane & 8;
  auto limit = [&](auto sc) {
    constexpr int s = sc.v;
    WAIT_VM(window(s, 1 + (PRM0.W[s >> 3] >> (4 * (s & 7)) & 7)));
    SB();
  };
  auto sample = [&]() { if (timer) lag += (int)(((u32)wall_clock64() - t0) << 8) - due + ((int)__builtin_amdgcn_s_getreg(7 | (3 << 11)) + 1) * tau; };
  auto report = [&](u32 dst) {
    if (timer) {
      if (lane == 0) sm.mflag[wave] = vote * 65536 + min(max(lag >> 8, 0), 4000);
      lag = 0;
      LDS_BARRIER();
      if (wave == 7) {
        int s = sm.mflag[lane & 7];
        s += __builtin_amdgcn_mov_dpp(s, XOR1, 0xf, 0xf, true); s += __builtin_amdgcn_mov_dpp(s, XOR2, 0xf, 0xf, true); s += __builtin_amdgcn_mov_dpp(s, XOR7, 0xf, 0xf, true);
        if (lane < 8) st8_sc1(wsr, dst + lane * 128 + wi * 8, s, tag);
      }
    }
    SB();
  };
  float l0 = 0.f, l1 = 0.f;
  u32x2 gmd[2], tbg;
  int t1 = 0;
#pragma unroll
  for (int i = SK0; i < SK1; ++i) kr[i] = u32x4{0u, 0u, 0u, 0u};
  const int first = SK0 + NL - nl;
  auto left_out = [&](int i) { int f = first; asm volatile("" : "+s"(f)); return i < f; };
  sfor<1, NL>([&](auto ic) {
    constexpr int i = ic.v;
    if constexpr (i >= SK0 && i < SK1) { if (left_out(i)) return true; }
    const u32 so = __builtin_amdgcn_readlane(sot, i);
    gate();
    if constexpr ((i >= TBS && i <= TBP) || (i >= RPS && i <= RPP)) sample();
    SB();
    if constexpr (i >= SK0 && i < SK1) ld16_into(kr[i], rk, lane * 16, so); else kr[i] = ld16<AUX_NT>(rk, lane * 16, so);
    if constexpr (i == SMO) small_ops();
    if constexpr (i == SMO + 1) { if (voter && (xcd & 3) == 0) __builtin_amdgcn_raw_buffer_store_b32(0u, wsr, pbo + (lane >> 5) * 8192 + (lane & 31) * 4, 0, AUX_SC1); }
    if constexpr (i == SK1) tbg = ld8<AUX_SC1>(wsr, wave == 7 ? G_VT + xcd * 128 + c * 8 : OOB, 0);
    if constexpr (i == TBL) tbg = ld8<AUX_SC1>(wsr, wave == 7 ? G_TB + xcd * 128 + c * 8 : OOB, 0);
    if constexpr (i == QP) { gmd[0] = ld8<AUX_SC1>(wsr, part == 1 ? G_MOD + tid * 8 : OOB, 0); gmd[1] = ld8<AUX_SC1>(wsr, part == 1 ? G_MOD + tid * 8 : OOB, 4096); }
    SB();
    SB();
    if (i >= 4) limit(ic);
    if (i == TBP) {
      if (timer) {
        if (lane == 0) sm.mflag[wave] = lag;
        lag = 0;
        LDS_BARRIER();
        if (wave == 7) {
          int s = sm.mflag[lane & 7];
          s += __builtin_amdgcn_mov_dpp(s, XOR1, 0xf, 0xf, true); s += __builtin_amdgcn_mov_dpp(s, XOR2, 0xf, 0xf, true); s += __builtin_amdgcn_mov_dpp(s, XOR7, 0xf, 0xf, true);
          if (lane < 8) st8_sc1(wsr, G_TB + lane * 128 + wi * 8, t0 << 10 | (u32)min(s / (2048 * (TBP - TBS + 1)), 1023), tag);
        }
      }
      SB();
    }
    if (i == PRB) {
      if (voter) {
        u64 ta, tb, tc; u32 dm;
        const u32 pa = pbo + (wave & 1) * 8192;
        asm volatile("s_memtime %0\n\ts_waitcnt lgkmcnt(0)\n\ts_load_dword %3, %4, %5\n\ts_waitcnt lgkmcnt(0)\n\ts_memtime %1\n\ts_waitcnt lgkmcnt(0)\n\t"
                     "s_load_dword %3, %4, %6\n\ts_waitcnt lgkmcnt(0)\n\ts_memtime %2\n\ts_waitcnt lgkmcnt(0)"
                     : "=&s"(ta), "=&s"(tb), "=&s"(tc), "=&s"(dm) : "s"(ws), "s"(pa), "s"(pa ^ 8192u));
        const int d = min(max((int)(2 * (u32)tb - (u32)ta - (u32)tc), -500), 500);
        vote = ((wave ^ die) & 1) ? -d : d;
      }
      SB();
    }
    if (i == TBU) {
      if (wave == 7) {
        const bool ok = tbg[1] == tag;
        float lf = ok ? (float)min(max((int)((t0 << 10 | 1023u) - tbg[0]) >> 10, -300), 300) : 0.f, nf = ok ? 1.f : 0.f, lg = ok ? (float)(tbg[0] & 1023u) : 0.f;
        lf += dpp<XOR1>(lf); nf += dpp<XOR1>(nf); lg += dpp<XOR1>(lg); lf += dpp<XOR2>(lf); nf += dpp<XOR2>(nf); lg += dpp<XOR2>(lg);
        lf += dpp<XOR7>(lf); nf += dpp<XOR7>(nf); lg += dpp<XOR7>(lg); lf += dpp<XOR8>(lf); nf += dpp<XOR8>(nf); lg += dpp<XOR8>(lg);
        const float rn = __builtin_amdgcn_rcpf(fmaxf(nf, 1.f));
        shift = __builtin_amdgcn_readfirstlane(min(max((int)((lg * rn - (float)P.GS) * (float)P.GB * 0.125f), 0), 150));
        if (lane == 0) sm.bc = u32x2{tag, t0 - (u32)(int)(lf * rn) + shift};
      }
      LDS_BARRIER();
      const u32x2 bc = sm.bc;
      tag = __builtin_amdgcn_readfirstlane(bc[0]);
      t1 = (__builtin_amdgcn_readfirstlane((int)(bc[1] - t0)) + TEND + P.X1) * 256;
      tau -= __builtin_amdgcn_readfirstlane((int)((float)(int)(t0 - bc[1]) * 256.f * __builtin_amdgcn_rcpf((float)(nl - TBU - 1))));
      SB();
    }
    if (i == RPP) report(G_VT);
    if (i == LNS) {
      tag = next_tag(__builtin_amdgcn_readfirstlane(ep[0]));
      if (part == 1) {
        float x0[8], x1[8], mb0, ri0, mb1, ri1;
#pragma unroll
        for (int k = 0; k < 4; ++k) { x0[2 * k] = lo_f(xr0[k]); x0[2 * k + 1] = hi_f(xr0[k]); x1[2 * k] = lo_f(xr1[k]); x1[2 * k + 1] = hi_f(xr1[k]); }
        ln_stats2(x0, x1, mb0, ri0, mb1, ri1);
        l0 = rbf(rbf(lo_f(xs0) - mb0) * ri0); l1 = rbf(rbf(lo_f(xs1) - mb1) * ri1);
      }
      PIN(l0); PIN(l1);
      SB();
    }
    if (i == WUSE) {
      PIN(tch1); PIN(tch2);
      if ((part == 1 || (part == 2 && has_w && !modc))) {
        WAIT_VM(WK); CFENCE();
        if (part == 2) mod_stage(); else wo_prep();
      }
#pragma unroll
      for (int k = 0; k < 8; ++k) PIN(bf[k]);
      SB();
    }
    if (i == QP + QD) {
      if (part == 1) {
        u32 gm[2] = {gmd[0][0], gmd[1][0]};
        if (!all_ok((gmd[0][1] == tag) & (gmd[1][1] == tag))) gather8<2>(wsr, G_MOD + tid * 8, 512 * 8, tag, gm);
        gemv_go(pack_bf(rbf(l0 * rbf(1.f + lo_f(gm[1]))) + lo_f(gm[0]), rbf(l1 * rbf(1.f + hi_f(gm[1]))) + hi_f(gm[0])));
      }
      SB();
    }
    if (i == QP + QD + 1) {
      if (part == 1) {
        LDS_BARRIER();
        if (wave == 0) {
          const int bb = lane >> 5, jq = lane & 31;
          st8_sc1(wsr, G_Q + (bb * 2048 + blk * 32 + jq) * 8, pack_bf(sum8(&sm.r2[2 * jq * 20 + bb * 8]), sum8(&sm.r2[(2 * jq + 1) * 20 + bb * 8])), tag);
        }
      }
      SB();
    }
    return true;
  });
  PIN(sot);
  if (wave == 7) {
    const bool ok = tbg[1] == tag;
    float lg = ok ? (float)(tbg[0] & 0xffffu) : 0.f, nf = ok ? 8.f * (RPP - RPS + 1) : 0.f;
    lg += dpp<XOR1>(lg); nf += dpp<XOR1>(nf); lg += dpp<XOR2>(lg); nf += dpp<XOR2>(nf); lg += dpp<XOR7>(lg); nf += dpp<XOR7>(nf); lg += dpp<XOR8>(lg); nf += dpp<XOR8>(nf);
    lg *= __builtin_amdgcn_rcpf(fmaxf(nf, 1.f));
    if (lane == 0) sm.bc2 = u32x2{(u32)min(shift * P.DG * 32, P.EW * 256), (u32)min(max((int)((lg - (float)P.B0) * (float)(P.BG * 32) + fmaxf(lg - (float)P.TC, 0.f) * (float)(P.RT * 32)) - shift * P.D0 * 32, 0), 200 * 256)};
  }
  LDS_BARRIER();
  {
    const u32x2 bc = sm.bc2;
    const int st = __builtin_amdgcn_readfirstlane(bc[1]), sp = (st * P.BF) >> 3;
    t1 += st + __builtin_amdgcn_readfirstlane(bc[0]);
    due += P.TG * 256 + sp;
    tau = (te1 >> 16) + __builtin_amdgcn_readfirstlane((int)((float)(st - sp) * __builtin_amdgcn_rcpf((float)(nl - 3))));
  }
  const u32 qoff = G_Q + ((b * 32 + head) * 64 + c * 4) * 8;
  u32x4 qg[2], mlg;
  u32 qq[4];
  constexpr int NB = (NL + 15) / 16;
  float xlo, sc[NB], pr[NB], m = 0.f;
  f32x4 acc = {0.f, 0.f, 0.f, 0.f};
  u32 pb[NB], pm[4], pl8[8], cpf;
  const u32 adst0 = lds_addr(lane < 32 ? &sm.red[wave][c * 8 + g * 4] : &sm.r2[(lane - 32) * 4]);
  u32 po, pso, rdst, rsrc, xo;
  auto wo_addr = [&]() {
    po = dup(G_P + (xcd & 3) * 8192) + tid * 16 + near;
    pso = tid < 16 ? pslot + c * 128 + near : OOB;
    PIN(pso);
    rdst = (u32)(uintptr_t)&sm.red2[wave][(lane & 31) * 4 + (lane >> 5) * 2];
    rsrc = (u32)(uintptr_t)&sm.red2[0][((wave & 1) * 16 + c) * 4 + (g & 1) * 2 + (g >> 1)];
    xo = dup(G_XP + xcd * 2048 + (wave & 1) * 1024 + (blk * 64 + (lane & ~1)) * 2) + (lane & 1) * 8192;
    PIN(po); PIN(rdst); PIN(rsrc); PIN(xo);
    const int q = (lane >> 2) & 3, pp = lane & 3, r = (g & 1) * 4 + (q & 1);
#pragma unroll
    for (int t = 0; t < 4; ++t) { wfa[t] = wimg + wave * 8192 + ((g >> 1) * 16 + (q >> 1) * 8 + r) * 128 + (pp & 1) * 8 + ((t * 2 + (pp >> 1)) ^ r ^ (q & 2)) * 16; PIN(wfa[t]); }
  };
  const u32 nadd = wdef ? P.LW : (is_up ? P.LU : 0);
  u32 nto = tid < 2 ? dup(G_NT) + bid * 8 + tid * 8192 : OOB;
  PIN(nto);
  auto notices = [&]() -> int {
    u32 off = dup(G_NT) + near + lane * 16;
    PIN(off);
    until(due);
    int mx;
#pragma nounroll
    for (int spin = 0; spin < SPIN_MAX; ++spin) {
      CFENCE();
      const u32x4 ga_ = ld16<AUX_SC1>(wsr, off, 0), gb_ = ld16<AUX_SC1>(wsr, off, 1024);
      mx = max(max((int)(ga_[0] - t0), (int)(ga_[2] - t0)), max((int)(gb_[0] - t0), (int)(gb_[2] - t0)));
      if (all_ok((((ga_[1] ^ tag) | (ga_[3] ^ tag)) | ((gb_[1] ^ tag) | (gb_[3] ^ tag))) == 0u)) break;
      for (int k = 0; k < P.AG; ++k) __builtin_amdgcn_s_sleep(1);
    }
    if (all_ok(mx <= (t1 >> 8) - P.AC)) return t1;
    mx = max(mx, __builtin_amdgcn_mov_dpp(mx, XOR1, 0xf, 0xf, true)); mx = max(mx, __builtin_amdgcn_mov_dpp(mx, XOR2, 0xf, 0xf, true));
    mx = max(mx, __builtin_amdgcn_mov_dpp(mx, XOR7, 0xf, 0xf, true)); mx = max(mx, __builtin_amdgcn_mov_dpp(mx, XOR8, 0xf, 0xf, true));
    mx = max(max(__builtin_amdgcn_readlane(mx, 0), __builtin_amdgcn_readlane(mx, 16)), max(__builtin_amdgcn_readlane(mx, 32), __builtin_amdgcn_readlane(mx, 48)));
    return max(min(mx, 1 << 20) * 256, t1 + P.L0 * 256) + P.L1 * 256;
  };
  auto pv_row = [&](auto jc) {
    constexpr int i = jc.v;
    const u32 pv = __float_as_uint(bcast16(u2f(pb[i >> 4]), i & 15));
    acc = mfma16(u32x4{pv & pm[0], pv & pm[1], pv & pm[2], pv & pm[3]}, vr[i], acc);
    PIN(acc);
    return true;
  };
  sfor<0, NL - 1>([&](auto ic) {
    constexpr int v = ic.v;
    if constexpr (v >= SK0 && v < SK1) { if (left_out(v)) return true; }
    if constexpr (v > 0) {
      const u32 so = __builtin_amdgcn_readlane(sot, v);
      if constexpr (v >= SK1) { if (v == NL - P.NE) tau = (tau * P.FE) >> 4; }
      gate();
      if constexpr (v >= RVS && v <= RVP) sample();
      SB();
      if constexpr (v >= SK0 && v < SK1) ld16_into(vr[v], rv, lane * 16, so); else vr[v] = ld16<AUX_NT>(rv, lane * 16, so);
      SB();
      SB();
    }
    if constexpr (v == SK0 - 1) {
#pragma unroll
      for (int i = SK0; i < SK1; ++i) { vr[i] = u32x4{0u, 0u, 0u, 0u}; PIN(vr[i]); }
    }
    if constexpr (v == VQ - 4) { qg[0] = ld16<AUX_SC1>(wsr, qoff, 0); qg[1] = ld16<AUX_SC1>(wsr, qoff + 16, 0); }
    if constexpr (v == VSIDE) side_loads();
    if constexpr (v == CPF) {
      u64 pc; u32 dlt, len;
      asm volatile("s_getpc_b64 %0\n.Lcpf_here:\n\ts_mov_b32 %1, .Ltail_start-.Lcpf_here\n\ts_mov_b32 %2, .Lend_a-.Ltail_start\n\ts_max_i32 %2, %2, .Lend_b-.Ltail_start\n\ts_max_i32 %2, %2, .Lend_c-.Ltail_start\n\ts_max_i32 %2, %2, .Lend_d-.Ltail_start\n\ts_max_i32 %2, %2, .Lend_e-.Ltail_start" : "=s"(pc), "=s"(dlt), "=s"(len) :: "scc");
      pc += dlt;
      const rsrc_t rc = mk_rsrc((const void*)pc, (part == 3 && i8 == 1) ? len : 0u);
      cpf = __builtin_amdgcn_raw_buffer_load_b32(rc, tid * 64, 0, 0);
    }
    if constexpr (v == SK1 + 2) PIN(cpf);
    if constexpr (v == RVP) report(G_VT + 1024);
    if constexpr (v == RVL) tbg = ld8<AUX_SC1>(wsr, wave == 7 ? G_VT + (g ? 0 : 1024) + xcd * 128 + c * 8 : OOB, 0);
    if constexpr (v == SK1) mlg = ld16<AUX_SC1>(wsr, wave == 0 ? mlo : OOB, 0);
    SB();
    if constexpr (NL + v < 72) limit(IC<NL + v>{});
    if constexpr (v >= SK0 + WV) { WAIT_VM(v < SK1 ? WV : WV + 1); SB(); }
    if constexpr (v == VQ) {
      if (!all_ok((qg[0][1] == tag) & (qg[0][3] == tag) & (qg[1][1] == tag) & (qg[1][3] == tag))) gather16<2>(wsr, qoff, 16, tag, qg);
      qq[0] = qg[0][0]; qq[1] = qg[0][2]; qq[2] = qg[1][0]; qq[3] = qg[1][2];
    }
    if constexpr (v >= VQ && v < VS) {
      constexpr int sl = v - VQ, bt = sl >> 1;
      float xs[8];
#pragma unroll
      for (int i = 0; i < 8; ++i) {
        float d = 0.f;
        if (sl * 8 + i < NL) {
#pragma unroll
          for (int k = 0; k < 4; ++k) d = dot2(kr[sl * 8 + i][k], qq[k], d);
        }
        xs[i] = d;
      }
#pragma unroll
      for (int h = 0; h < 4; ++h) xs[h] = sel_add<XOR7>(xs[h], xs[h + 4], c & 4);
#pragma unroll
      for (int h = 0; h < 2; ++h) xs[h] = sel_add<XOR2>(xs[h], xs[h + 2], c & 2);
      xs[0] = sel_add<XOR1>(xs[0], xs[1], c & 1);
      if constexpr ((sl & 1) || sl == NSL - 1) {
        const float t = (sl & 1) ? sel_add<XOR8>(xlo, xs[0], c & 8) : xs[0] + dpp<XOR8>(xs[0]);
        const float s = rbf(rbf(t) * 0.08838834764831845f);
        sc[bt] = (bt * 16 + c < SK0 || (bt * 16 + c >= first && bt * 16 + c < NL)) ? s : -__builtin_inff();
        PIN(sc[bt]);
      } else {
        xlo = xs[0];
        PIN(xlo);
      }
    }
    if constexpr (v == VS) {
      float mloc = sc[0];
#pragma unroll
      for (int bt = 1; bt < NB; ++bt) mloc = fmaxf(mloc, sc[bt]);
      m = fmaxf(wave_max(mloc), -3.0e38f);
      float ls = 0.f;
#pragma unroll
      for (int bt = 0; bt < NB; ++bt) { pr[bt] = __expf(sc[bt] - m); ls += pr[bt]; }
      const float l = wave_sum(ls);
      if (lane == 0) { sm.ml[wave][0] = m; sm.ml[wave][1] = l; }
      if (c == ((NL - 1) & 15)) sm.pl[g][wave] = pr[NB - 1];
    }
    if constexpr (v >= SK1) pv_row(IC<v - SK1 + SK0 - WV>{});
    if constexpr (v < SK0 || v >= SK0 + WV) sfor<cons_end(v - 1), cons_end(v)>(pv_row);
    if constexpr (v == RVU) {
      if (wave == 7) {
        if (__builtin_expect(!all_ok(tbg[1] == tag || g == 0), 0)) {
          u32 vg[1];
          gather8<1>(wsr, G_VT + xcd * 128 + c * 8, 0, tag, vg);
          if (g) tbg = u32x2{vg[0], tag};
        }
        const bool ok = tbg[1] == tag;
        float lg = ok ? (float)(g ? (int)tbg[0] >> 16 : (int)(tbg[0] & 0xffffu)) : 0.f, nf = ok ? 8.f * (RVP - RVS + 1) : 0.f;
        lg += dpp<XOR1>(lg); nf += dpp<XOR1>(nf); lg += dpp<XOR2>(lg); nf += dpp<XOR2>(nf); lg += dpp<XOR7>(lg); nf += dpp<XOR7>(nf); lg += dpp<XOR8>(lg); nf += dpp<XOR8>(nf);
        lg *= __builtin_amdgcn_rcpf(fmaxf(nf, 1.f));
        if (lane == 16) sm.bc4 = __float_as_uint(lg);
        if (lane == 0) sm.bc3 = (u32)min(max((int)((lg - (float)P.F0) * (float)(P.FG * 32)), 0), 200 * 256);
      }
      LDS_BARRIER();
      const int st = __builtin_amdgcn_readfirstlane(sm.bc3), sp = (st * P.FF_) >> 3;
      near = ((__builtin_amdgcn_readfirstlane(sm.bc4) >> 31 ^ die ^ P.NR ^ 1) & 1) * 8192u;
      t1 += st;
      due += sp;
      tau += __builtin_amdgcn_readfirstlane((int)((float)(st - sp) * __builtin_amdgcn_rcpf((float)(nl - RVU - 3))));
    }
    if constexpr (v == SK1 + 1) {
      float p[8];
#pragma unroll
      for (int w = 0; w < 8; ++w) p[w] = sm.pl[g][w] * __expf(sm.ml[w][0] - m);
#pragma unroll
      for (int w = 0; w < 8; ++w) pl8[w] = pack_bf(p[w], p[w]);
#pragma unroll
      for (int w = 0; w < 8; ++w) PIN(pl8[w]);
    }
    if constexpr (v == MLS - 1) { side_pin(); if (is_up) up_prep(); PIN(ga); PIN(xin); PIN(cc1); }
    if constexpr (v == UPREP - 1) wo_addr();
    if constexpr (v == UPREP) {
#pragma unroll
      for (int k = 0; k < 4; ++k) asm volatile("" : "=v"(bw[k]));
      if (part == 0) wo_frags(0); else if (is_up) up_frags();
#pragma unroll
      for (int k = 0; k < 4; ++k) PIN(bw[k]);
    }
    if constexpr (v == UPREP + 1) {
      if (is_up) {
        const u32x4* a1 = reinterpret_cast<const u32x4*>(&sm.hb[1024 + (c >> 2 & 1) * 1024 + (c >> 3) * 512 + (g >> 1) * 256 + (c >> 1 & 1) * 32 + (g & 1) * 16 + (c & 1) * 8]);
        f32x4 d = {0.f, 0.f, 0.f, 0.f};
#pragma unroll
        for (int j = 0; j < 4; ++j) d = mfma64(a1[j * 8], bw[j], d, (c & 1) ? 0xeeeeu : 0x4444u);
        float t = par ? d[2] + d[3] : d[0] + d[1];
        t += dpp<XOR8>(t);
        const auto ab = __builtin_amdgcn_permlane16_swap(__float_as_uint(t), __float_as_uint(t), false, false);
        Af = u2f(ab[0]); Bf = u2f(ab[1]);
      }
      PIN(Af); PIN(Bf);
    }
    if constexpr (v == MLS) {
      LDS_BARRIER();
      float mm[8], ll[8];
#pragma unroll
      for (int w = 0; w < 8; ++w) { const f32x2 t = *reinterpret_cast<const f32x2*>(&sm.ml[w][0]); mm[w] = t[0]; ll[w] = t[1]; }
      float Mwg = mm[0], Lwg = 0.f;
#pragma unroll
      for (int w = 1; w < 8; ++w) Mwg = fmaxf(Mwg, mm[w]);
      if (wave == 0) {
#pragma unroll
        for (int w = 0; w < 8; ++w) Lwg = fmaf(__expf(mm[w] - Mwg), ll[w], Lwg);
        if (lane == 0) st16_sc1(wsr, G_ML + ((head * 2 + b) * 4 + part) * 16, __float_as_uint(Mwg), __float_as_uint(Lwg), tag);
      }
      const float fw = __expf(m - Mwg);
      m = Mwg;
#pragma unroll
      for (int bt = 0; bt < NB; ++bt) { pb[bt] = pack_bf(pr[bt] * fw, pr[bt] * fw); PIN(pb[bt]); }
#pragma unroll
      for (int k = 0; k < 4; ++k) { pm[k] = (c >> 1 & 3) == k ? ((c & 1) ? 0xffff0000u : 0x0000ffffu) : 0u; PIN(pm[k]); }
    }
    SB();
    return true;
  });
  asm volatile(".Ltail_start:");
  if (wave != 0) {
    pv_row(IC<SK0 - 1>{});
    WAIT_VM(WV); SB();
    sfor<NL - 1 - WV, NL - 1>(pv_row);
    *LDSP(f32x4, adst0) = acc;
    LDS_BARRIER();
  } else {
    u32x4 xr[8], am[8];
#pragma unroll
    for (int w = 0; w < 8; ++w) am[w] = u32x4{pl8[w] & pm[0], pl8[w] & pm[1], pl8[w] & pm[2], pl8[w] & pm[3]};
#pragma unroll
    for (int w = 0; w < 8; ++w) PIN(am[w]);
    {
      const u32 so = __builtin_amdgcn_readlane(sot, NL - 1);
      gate();
      SB();
#pragma unroll
      for (int w = 0; w < 8; ++w) xr[w] = ld16<AUX_NT>(rv, lane * 16, so ^ ((w & 1) ? col(P, 7) : 0u) ^ ((w & 2) ? col(P, 8) : 0u) ^ ((w & 4) ? col(P, 9) : 0u));
      SB();
    }
    u32 rsum = lds_addr(&sm.red[0][c * 8 + (g & 1) * 4]);
    pv_row(IC<SK0 - 1>{});
    WAIT_VM(WV + 8); SB();
    sfor<NL - 1 - WV, NL - 3>(pv_row);
    if (__builtin_expect(!all_ok((mlg[1] == tag) & (mlg[3] == tag)), 0)) {
#pragma nounroll
      for (int spin = 0; spin < SPIN_MAX; ++spin) {
        CFENCE();
        mlg = ld16<AUX_SC1>(wsr, mlo, 0);
        if (all_ok((mlg[1] == tag) & (mlg[3] == tag))) break;
      }
    }
    merge_factor(mlg);
    sfor<NL - 3, NL - 1>(pv_row);
    LDS_BARRIER();
    f32x4 t[8];
#pragma unroll
    for (int w = 1; w < 8; ++w) t[w] = LDSP(f32x4, rsum)[w * 32];
    acc = ((acc + t[1]) + (t[2] + t[3])) + ((t[4] + t[5]) + (t[6] + t[7]));
    PIN(acc);
    SB();
#pragma unroll
    for (int w = 0; w < 8; ++w) acc = mfma16(am[w], xr[w], acc);
  }
  WAIT_VM(0);
  acc *= fmrg;
  {
    const u32 w0 = min(pack_bf(acc[0], acc[1]), SENT - 1u), w1 = min(pack_bf(acc[2], acc[3]), SENT - 1u);
    const auto e0 = __builtin_amdgcn_permlane16_swap(w0, w0, false, false), e1 = __builtin_amdgcn_permlane16_swap(w1, w1, false, false);
    __builtin_amdgcn_raw_buffer_store_b128(u32x4{e0[0], e1[0], e0[1], e1[1]}, wsr, pso, 0, AUX_SC1);
  }
  st8_sc1(wsr, nto, (u32)wall_clock64() + nadd, tag);
  SB();

  if (part == 2) return;
  if (part == 0) {
    __builtin_amdgcn_s_barrier();
#pragma nounroll
    while (wleft > 0) { until(t1 - (P.PA + wleft * P.PD) * 256); piece(); }
    bool late = false;
    u32x4 og[1];
    f32x4 d[4];
    u32 six = (c & 8) ? 0xeeeeu : 0x4444u;
    PIN(six);
#pragma unroll
    for (int t = 0; t < 4; ++t) { d[t] = f32x4{0.f, 0.f, 0.f, 0.f}; PIN(d[t]); }
    if (wdef) {
      late = true;
      while ((int)(((u32)wall_clock64() - t0) << 8) < t1 - 10 * 256) if (__builtin_amdgcn_s_getreg(7 | (3 << 11)) == 0) { late = false; break; }
      if (__builtin_expect(late, 0)) { until(t1); CFENCE(); og[0] = ld16<AUX_SC1>(wsr, po, 0); SB(); WAIT_VM(1); } else { WAIT_VM(0); }
      CFENCE();
      wo_frags(1);
#pragma unroll
      for (int k = 0; k < 4; ++k) PIN(bw[k]);
      SB();
    }
    if (__builtin_expect(!late, 1)) { until(t1); CFENCE(); og[0] = ld16<AUX_SC1>(wsr, po, 0); }
    SB();
    auto wo_mm = [&]() {
#pragma unroll
      for (int t = 0; t < 4; ++t) d[t] = mfma64(og[0], bw[t], d[t], six);
    };
    auto p_bad = [&]() { return !all_ok(max(max(og[0][0], og[0][1]), max(og[0][2], og[0][3])) != SENT); };
    wo_mm();
    if (__builtin_expect(p_bad(), 0)) {
#pragma nounroll
      for (int spin = 0; spin < SPIN_MAX; ++spin) {
        __builtin_amdgcn_s_sleep(1);
        CFENCE();
        og[0] = ld16<AUX_SC1>(wsr, po, 0);
        if (!p_bad()) break;
      }
#pragma unroll
      for (int t = 0; t < 4; ++t) d[t] = f32x4{0.f, 0.f, 0.f, 0.f};
      wo_mm();
      SB();
      asm volatile(".Lend_d:");
    }
    {
      SB();
      f32x4 y; f32x2 h[4];
#pragma unroll
      for (int t = 0; t < 4; ++t) { h[t] = d[t].xy + d[t].zw; PIN(h[t]); }
#pragma unroll
      for (int t = 0; t < 4; ++t) { y[t] = h[t][0] + h[t][1]; PIN(y[t]); }
      *LDSP(f32x2, rdst) = f32x2{swap32_add(y[0], y[1]), swap32_add(y[2], y[3])};
    }
    LDS_BARRIER();
    if (wave < 2) {
      f32x2 v[4];
#pragma unroll
      for (int w = 0; w < 4; ++w) v[w] = f32x2{LDSP(float, rsrc)[w * 256], LDSP(float, rsrc)[w * 256 + 128]};
      const f32x2 h = (v[0] + v[1]) + (v[2] + v[3]);
      const float y = h[0] + h[1];
      __builtin_amdgcn_raw_buffer_store_b32(min(pack_bf(dpp<0xA0>(y), dpp<0xF5>(y)), SENT - 1u), wsr, xo, 0, AUX_SC1);
      asm volatile(".Lend_a:");
    }
    return;
  }
  if (part == 3) {
    if (!has_w) return;
    u32x4 b2[4];
    {
      __builtin_amdgcn_s_barrier();
      const rsrc_t r2 = mk_rsrc(w2 + (size_t)(blk * 32 + wave * 4) * 512, 4096u);
      const u32 l2 = (u32)(uintptr_t)(&sm.w2b[wave * 4][0]);
#pragma unroll
      for (int n = 0; n < 4; ++n) { until(t1 - (P.QA + (4 - n) * P.QD) * 256); dma16(r2, (lane ^ (n * 2) ^ ((wave & 2) << 2)) * 16, n * 1024, l2 + n * 1024); }
    }
    const u32 tdst = lds_addr(&sm.hb[hpos]);
    const u32 sdst = lds_addr(&sm.stat[wave][lane]);
    const u32 ssrc = lds_addr(&sm.stat[(g >> 1) * 4 + ((lane >> 3) & 3)][(lane & 7) * 4]);
    const u32 adst = lds_addr((c < 4 && (g & 1) == 0) ? &sm.actl[g >> 1][wave * 4 + c] : reinterpret_cast<u16*>(&sm.dummy[lane]));
    const u32 asrc = lds_addr(&sm.actl[lane & 1][g * 8]);
    const u32 apa = lds_addr(&sm.hb[(c >> 3) * 512 + (g >> 1) * 256 + (c >> 1 & 1) * 64 + ((c >> 2) & 1) * 32 + (g & 1) * 16 + (c & 1) * 8]);
    u32 six = (c & 1) ? 0xeeeeu : 0x4444u;
    f32x4 d[2] = {{0.f, 0.f, 0.f, 0.f}, {0.f, 0.f, 0.f, 0.f}};
    PIN(six); PIN(d[0]); PIN(d[1]);
    constexpr u32 xs2 = 16384;
    u32 xoff = dup(G_XP) + near + g * 2048 + ub * 1024 + (wave & 3) * 256 + c * 16;
    u32x4 one;
#pragma unroll
    for (int k = 0; k < 4; ++k) one[k] = ((c & 2) == 0 && (c >> 2) == k) ? ((c & 1) ? 0x3f800000u : 0x00003f80u) : 0u;
    PIN(one);
    u32 ooff = dup(G_OP + (blk * 512 + tid) * 8);
    float Afp = Af, Bfp = Bf;
    PIN(xoff); PIN(ooff); PIN(Afp); PIN(Bfp);
    f32x2 gc = ga * cc1, xc = xin * cc1;
    PIN(ga); PIN(xin); PIN(gc); PIN(xc);
    u32 w2a = (u32)(uintptr_t)(&sm.w2b[0][0]) + (g * 8 + ((lane >> 2) & 3)) * 1024 + (wave ^ (g & 1)) * 128 + (lane & 15) * 8;
    PIN(w2a);
    if (wave == 1) {
      due = t1 + P.N1 * 256;
      const int tn = notices();
      due = tn + P.X2 * 256;
      until(due);
    }
    asm volatile("s_waitcnt vmcnt(0)" ::: "memory");
    __builtin_amdgcn_s_barrier();
    CFENCE();
    u32x4 xg[2] = {ld16<AUX_SC1>(wsr, xoff, 0), ld16<AUX_SC1>(wsr, xoff, xs2)};
    SB();
#pragma unroll
    for (int t = 0; t < 4; ++t) {
      const u32x2 lo = tr_read(w2a ^ (t * 32)), hi = tr_read((w2a ^ (t * 32)) + 4096);
      b2[t] = u32x4{lo[0], lo[1], hi[0], hi[1]};
    }
#pragma unroll
    for (int t = 0; t < 4; ++t) PIN(b2[t]);
    SB();
    const f32x4 z4 = {0.f, 0.f, 0.f, 0.f};
    auto xp_bad = [&]() { return !all_ok(max(max(max(xg[0][0], xg[0][1]), max(xg[0][2], xg[0][3])), max(max(xg[1][0], xg[1][1]), max(xg[1][2], xg[1][3]))) != SENT); };
    f32x4 v = mfma16(one, xg[1], mfma16(one, xg[0], z4));
    if (__builtin_expect(xp_bad(), 0)) {
#pragma nounroll
      for (int spin = 0; spin < SPIN_MAX; ++spin) {
        __builtin_amdgcn_s_sleep(1);
        CFENCE();
        xg[0] = ld16<AUX_SC1>(wsr, xoff, 0); xg[1] = ld16<AUX_SC1>(wsr, xoff, xs2);
        if (!xp_bad()) break;
      }
      v = mfma16(one, xg[1], mfma16(one, xg[0], z4));
      SB();
      asm volatile(".Lend_e:");
    }
    {
      const f32x2 a = v.xy, hc = __builtin_elementwise_fma(gc, a, xc), x1 = __builtin_elementwise_fma(ga, a, xin);
      *LDSP(u32, tdst) = pack_bf(hc[0], hc[1]);
      *LDSP(float, sdst) = swap32_add(x1[0] + x1[1], fmaf(x1[0], x1[0], x1[1] * x1[1]));
    }
    LDS_BARRIER();
    {
      const f32x4 s4 = LDSP(f32x4, ssrc)[0], q4 = LDSP(f32x4, ssrc)[8];
      SB();
      const u32x4 af0 = LDSP(u32x4, apa)[0], af1 = LDSP(u32x4, apa)[16];
      SB();
      const f32x2 s2 = s4.xy + s4.zw, q2 = q4.xy + q4.zw;
      float s = s2[0] + s2[1], q = q2[0] + q2[1];
      SB();
      d[0] = mfma64(af0, bw[0], d[0], six);
      SB();
      s = swap16_add(s, q);
      SB();
      d[1] = mfma64(af0, bw[1], d[1], six);
      SB();
      s += dpp<XOR1>(s); s += dpp<XOR2>(s);
      SB();
      d[0] = mfma64(af1, bw[2], d[0], six);
      SB();
      s += dpp<XOR7>(s); s += dpp<XOR8>(s);
      SB();
      d[1] = mfma64(af1, bw[3], d[1], six);
      SB();
      const auto sq = __builtin_amdgcn_permlane16_swap(__float_as_uint(s), __float_as_uint(s), false, false);
      const float mean = u2f(sq[0]) * (1.f / 512.f);
      const float var = fmaf(u2f(sq[1]), 1.f / 512.f, -mean * mean);
      float ri = __builtin_amdgcn_rsqf(fmaxf(var, 0.f) + 1e-5f);
      PIN(ri); PIN(d[0]); PIN(d[1]);
      SB();
      float y0 = d[0][0] + d[0][1], y1 = d[1][2] + d[1][3], mr = -ri * mean;
      PIN(y0); PIN(y1); PIN(mr);
      float y = y0 + y1, cf = fmaf(mr, Afp, Bfp), rk = ri * -1.44269504f;
      PIN(y); PIN(cf); PIN(rk);
      y = __builtin_bit_cast(float, __builtin_amdgcn_update_dpp(__builtin_bit_cast(int, y), __builtin_bit_cast(int, y), XOR8, 0xa, 0xf, false));
      const float ck = cf * -1.44269504f;
      y = swap16_add(y, y);
      const float ex = __builtin_amdgcn_exp2f(fmaf(y, rk, ck));
      y = fmaf(y, ri, cf);
      const float gu = y * dpp<0x104>(y);
      *LDSP(u16, adst) = (u16)pack_bf(gu * __builtin_amdgcn_rcpf(1.f + ex), 0.f);
    }
    LDS_BARRIER();
    {
      const u32x4 af = *LDSP(u32x4, asrc);
      const f32x4 z = {0.f, 0.f, 0.f, 0.f};
      f32x4 d0 = mfma16(af, b2[0], z), d1 = mfma16(af, b2[1], z), d2 = mfma16(af, b2[2], z), d3 = mfma16(af, b2[3], z);
      asm volatile("" : "+v"(d0), "+v"(d1), "+v"(d2), "+v"(d3));
      const bool g1 = g & 1, g2 = g & 2;
      const float e0 = g1 ? d1[0] : d0[0], e1 = g1 ? d1[1] : d0[1], f0 = g1 ? d3[0] : d2[0], f1 = g1 ? d3[1] : d2[1];
      const u32x2 ov = {pack_bf(g2 ? f0 : e0, g2 ? f1 : e1), tag};
      __builtin_amdgcn_raw_buffer_store_b64(ov, wsr, ooff, near ^ 8192u, AUX_SC1);
      __builtin_amdgcn_raw_buffer_store_b64(ov, wsr, ooff, near, AUX_SC1);
    }
    asm volatile(".Lend_b:");
    return;
  }
  if (wave == 0) {
    if (!all_ok((sg[0][1] == tag) & (sg[0][3] == tag) & (sg[1][1] == tag) & (sg[1][3] == tag))) {
      u32x4 t[2];
      gather16<2>(wsr, G_MOD + (1024 + fd) * 8, 1536 * 8, tag, t);
      sg[0] = t[0]; sg[1] = t[1];
    }
    u32 xa;
    {
      due = t1 + P.N2 * 256;
      const int tn = notices();
      due = tn + P.X3 * 256;
      until(due);
    }
    const bool hb = g & 1;
#pragma nounroll
    for (int spin = 0; spin < SPIN_MAX; ++spin) {
      CFENCE();
      xa = __builtin_amdgcn_raw_buffer_load_b32(wsr, dup(G_XP) + near + (g >> 1) * 16384 + (c >> 2) * 2048 + (g & 1) * 1024 + fd * 2, 0, AUX_SC1);
      if (all_ok(xa != SENT)) break;
      __builtin_amdgcn_s_sleep(1);
    }
    float a0 = lo_f(xa), a1 = hi_f(xa);
    a0 += dpp<0x124>(a0); a1 += dpp<0x124>(a1);
    a0 += dpp<XOR8>(a0); a1 += dpp<XOR8>(a1);
    a0 = swap32_add(a0, a0); a1 = swap32_add(a1, a1);
    f32x2 xv = {rbf(lo_f(sx2) + rbf((hb ? hi_f(sg[0][0]) : lo_f(sg[0][0])) * rbf(a0))), rbf(hi_f(sx2) + rbf((hb ? hi_f(sg[0][2]) : lo_f(sg[0][2])) * rbf(a1)))};
    f32x2 gv = {hb ? hi_f(sg[1][0]) : lo_f(sg[1][0]), hb ? hi_f(sg[1][2]) : lo_f(sg[1][2])};
    PIN(xv); PIN(gv);
    if (wi == 0 && lane == 0) __builtin_amdgcn_raw_buffer_store_b32(tag, wsr, WS_EPOCH, 0, AUX_SC1);
    due = max(due + P.X4 * 256, (int)(((u32)wall_clock64() - t0) << 8) + P.GX * 256);
    const bool last = lane < (NUP - 32) * 4;
    u32 o0 = dup(G_OP + (lane >> 2) * 4096 + fd * 8) + near, o2 = last ? o0 : OOB, tg2 = last ? tag : 0u;
    const rsrc_t rout = mk_rsrc(out, 2048u);
    u32 oo = (lane & 44) == 0 ? (g * 512 + fd) * 2 : OOB;
    const u32 pk = (c & 10) ? 0u : ((c & 4) ? 0x3f800000u : 0x00003f80u);
    u32x4 one = {(c & 1) ? 0u : pk, 0u, (c & 1) ? pk : 0u, 0u};
    PIN(o0); PIN(o2); PIN(tg2); PIN(oo); PIN(one);
    __builtin_amdgcn_raw_buffer_store_b32(pack_bf(xv[0], xv[1]), rout, oo, 0, AUX_SC1);
    u32 s1_ = 131072, s2_ = 262144;
    asm volatile("" : "+s"(s1_), "+s"(s2_));
    until(due);
    f32x4 sv;
#pragma nounroll
    for (int spin = 0; spin < SPIN_MAX; ++spin) {
      CFENCE();
      const u32x4 og[3] = {ld16<AUX_SC1>(wsr, o0, 0), ld16<AUX_SC1>(wsr, o0, s1_), ld16<AUX_SC1>(wsr, o2, s2_)};
      sv = f32x4{0.f, 0.f, 0.f, 0.f};
#pragma unroll
      for (int k = 0; k < 3; ++k) sv = mfma16(one, og[k], sv);
      SB();
      u32 bad = ((og[0][1] ^ tag) | (og[0][3] ^ tag) | (og[1][1] ^ tag)) | ((og[1][3] ^ tag) | (og[2][1] ^ tg2) | (og[2][3] ^ tg2));
      PIN(bad);
      SB();
      float s0 = sv[0] + dpp<0x124>(sv[0]), s1 = sv[1] + dpp<0x124>(sv[1]);
      PIN(s0); PIN(s1);
      s0 += dpp<XOR8>(s0); s1 += dpp<XOR8>(s1);
      PIN(s0); PIN(s1);
      __builtin_amdgcn_raw_buffer_store_b32(pack_bf(fmaf(gv[0], s0, xv[0]), fmaf(gv[1], s1, xv[1])), rout, oo, 0, AUX_SC1);
      SB();
      if (all_ok(bad == 0u)) break;
      __builtin_amdgcn_s_sleep(1);
    }
    asm volatile(".Lend_c:");
  }
}

extern "C" void kernel_launch(void* const* d_in, const int* in_sizes, int n_in,
                              void* d_out, int out_size, void* d_ws, size_t ws_size,
                              hipStream_t stream) {
  fused_kernel<<<256, 512, 0, stream>>>((const u16*)d_in[1], (const u16*)d_in[2], (const u16*)d_in[4], (const u16*)d_in[3], (const u16*)d_in[10], (const u16*)d_in[6], (const u16*)d_in[7],
                                        (unsigned char*)d_ws, (const u16*)d_in[0], (const u16*)d_in[9], (const u16*)d_in[8], (u16*)d_out);
}
```

```cpp
#define PROBE_MUL 10
#include <hip/hip_runtime.h>
#include <stdint.h>

typedef unsigned int u32;
typedef unsigned short u16;
typedef unsigned long long u64;
typedef __attribute__((ext_vector_type(4))) unsigned u32x4;
typedef __attribute__((ext_vector_type(2))) unsigned u32x2;
typedef __attribute__((ext_vector_type(2))) __bf16 bf16x2;
typedef __attribute__((ext_vector_type(4))) float f32x4;
typedef __attribute__((ext_vector_type(2))) float f32x2;
typedef __amdgpu_buffer_rsrc_t rsrc_t;

constexpr int NH = 32, SK = 16384, DH = 128, WIN = 4096, FF = 1376;
struct Prm { int E0, ES, E1, E2, E2C, EU, TEND, DT_UP, L0, L1, X1, X2, X3, X4, NE, FE, M0, M1, M2, M3, M4, GS, GX, N1, N2, GA, WD, DT_WO, LW, LU, EW, PM, NF, NR, C0, A0, AG, AC, TS, TW, TK, TG, GB, B0, BG, BF, D0, DG, TC, LM, C1, F0, FG, FF_, PA, PD, QA, QD, RT, SA, SG, SC, W[9]; };
constexpr Prm PRM0 = {32, 20, 32, 32, 60, 60, 1968, 76, 12, 27, 75, 63, 100, 75, 3, 8, 0x2511f55b, 0x2b7425d5, 0x6017665, 0x17e0b9ec, 0x31944305, 62, 30, 29, 62, 50, 4, 106, 50, 28, 10, 0x0, 12, 14, 3, 36, 4, 4, 509, 215, 80, -2, 6, 69, 10, 8, 8, 6, 87, 120, 2, 65, 6, 4, 52, 14, 9, 18, 12, 45, 14, 32, 0x77777777, 0x77777777, 0x77777777, 0x77777777, 0x77777777, 0x44444477, 0x44444444, 0x44444444, 0x44444444};
constexpr int NL = 37;
constexpr int SK0 = 21, SK1 = 32;
constexpr int NCMIN = (NL - SK1 + SK0) * 8;
constexpr int WK = 8, WV = 5;
constexpr int WUSE = 12, LNS = 7;
constexpr int QP = 14, QD = 4;
constexpr int VQ = 6;
constexpr int NSL = (NL + 7) / 8;
constexpr int VS = VQ + NSL;
constexpr int MLS = VS + 2;
constexpr int VSIDE = MLS - 5, UPREP = MLS + 6;
constexpr int CPF = 17;
constexpr int SMO = 1;
constexpr int RVS = 5, RVP = 8, RVL = 14, RVU = 18;
constexpr int RPS = 23, RPP = 27;
constexpr int PRB = 12;
constexpr int TBS = 7, TBP = 10, TBL = 13, TBU = 17;
constexpr int window(int s, int w) {
  int n = w;
  for (int u = s - w + 1; u <= s; ++u) n += u == SMO ? 6 : (u == TBL || u == SK1 || u == NL + CPF || u == NL + RVL || u == NL + SK1 ? 1 : (u == QP || u == NL + VQ - 4 ? 2 : (u == NL + VSIDE ? 4 : 0)));
  return n;
}
constexpr int cons_end(int v) {
  const int a = v - WV + 1, c = (v - MLS) * 4;
  const int m = a < c ? a : c;
  return m < 0 ? 0 : (m > NL ? NL : m);
}
constexpr u32 col(const Prm& P, int j) { const int m[6] = {P.M0, P.M1, P.M2, P.M3, P.M4, P.PM}; return (u32)(m[j / 3] >> (j % 3 * 10) & 1023) << 10; }
constexpr u32 first_col(const Prm& P, int k) {
  if (k < 4) return col(P, 12 + k) | 1u << (24 + k);
  if (k < 6) return col(P, k - 4);
  if (k < 8) return col(P, 4 + k) | 1u << (16 + k);
  if (k < 11) return col(P, k - 1);
  return (col(P, 2) ^ col(P, 3) ^ col(P, 4) ^ col(P, 5) ^ col(P, 6) ^ col(P, 10) ^ col(P, 11)) | 0xf00000u;
}
constexpr bool one_to_one(const Prm& P) {
  u32 c[10] = {};
  for (int j = 0; j < 10; ++j) c[j] = col(P, j);
  for (int j = 0; j < 10; ++j) {
    if (c[j] == 0) return false;
    const u32 low = c[j] & (0u - c[j]);
    for (int k = j + 1; k < 10; ++k) if (c[k] & low) c[k] ^= c[j];
  }
  return true;
}
static_assert(one_to_one(PRM0), "every KB of the window exactly once");
struct Tab { u32 e[512]; };
constexpr Tab make_tab(const Prm& P) {
  Tab T{};
  for (int bid = 0; bid < 256; ++bid) {
    const int xcd = bid & 7, part = bid >> 4 & 3, i8 = (bid >> 3 & 1) | bid >> 6 << 1, wi = i8 * 8 + xcd;
    const bool has_mod = wi < 48, has_up = wi >= 21;
    const int cb0 = (i8 + P.C0) & 7, cb1 = (i8 + P.C1) & 7, ms = wi - 20;
    const int mi = wi < 8 ? wi : (wi < 16 ? 8 + ((xcd + 4) & 7) : (wi < 20 ? (wi - 14) * 8 + 3 : 16 + ms / 7 * 8 + ms % 7 + (ms % 7 >= 3 ? 1 : 0)));
    const int ui = xcd * 5 + (xcd > 5 ? xcd - 5 : 0) + (i8 == 6 ? 0 : (xcd >= 5 ? 1 : 0) + (i8 == 7 ? 4 : i8 - 2));
    const bool edge = i8 >= 6;
    const int wd = cb0 == 3 ? 0 : P.WD;
    const int e[4] = {P.E0 - 4 * wd + (cb0 == 3 ? P.ES : 0), P.E1 + (cb1 == 3 ? P.ES : 0), has_mod ? (wi < 16 ? P.E2C : P.E2) + ((mi & 7) == 3 ? P.ES : 0) : 0, has_up ? P.EU : 0};
    int tw[4] = {}, win[4] = {}, span[4] = {}, k0[4] = {};
    for (int k = 0; k < 4; ++k) {
      const int np = k == 0 ? 8 - wd : 8;
      tw[k] = np ? e[k] * P.TW / (16 * np) : 0;
      k0[k] = P.NF + tw[k] * np / 2 > P.TK ? P.NF + tw[k] * np / 2 : P.TK;
      span[k] = P.TEND - k0[k] - P.TG - (k == 0 ? (wd ? P.DT_WO : P.A0) : (k == 3 && has_up ? P.DT_UP : 0));
      win[k] = span[k] - (k == 3 && has_up ? P.GA + (edge ? P.LM : 0) : 0);
    }
    int n[4] = {NCMIN / 8, NCMIN / 8, NCMIN / 8, NCMIN / 8};
    for (int left = 128 - NCMIN / 2; left > 0; --left) {
      int best = -1;
      for (int k = 3; k >= 0; --k)
        if (n[k] < NL && (best < 0 || n[k] * win[best] < n[best] * win[k])) best = k;
      ++n[best];
    }
    const int c[4] = {0, n[0] - 1, n[0] + n[1] - 2, n[0] + n[1] + n[2] - 3};
    const int nl = n[part];
    const bool has_w = part < 2 || (part == 2 ? has_mod : has_up);
    const int blk = !has_w ? 0 : (part == 0 ? cb0 : (part == 1 ? xcd * 8 + cb1 : (part == 2 ? mi : ui)));
    T.e[2 * bid] = (u32)(c[part] | nl << 7 | blk << 13 | (has_w ? 1 : 0) << 19 | (part == 0 ? wd : 0) << 20) | (u32)tw[part] << 24;
    T.e[2 * bid + 1] = (u32)(span[part] * P.TS / (4 * (nl - 1)) | span[part] * (1024 - P.TS) / (4 * (nl - 1)) << 16);
  }
  return T;
}
constexpr int step_range(const Prm& P, bool mx_) {
  int mx = 0, mn = 1 << 20;
  const Tab T = make_tab(P);
  for (int bid = 0; bid < 256; ++bid) { const int n = T.e[2 * bid] >> 7 & 63; mx = n > mx ? n : mx; mn = n < mn ? n : mn; }
  return mx_ ? mx : mn;
}
extern "C" __attribute__((used, visibility("protected"))) __constant__ const Tab wg_tab = make_tab(PRM0);
static_assert(step_range(PRM0, true) <= NL && step_range(PRM0, false) >= NCMIN / 8, "a wave's key range does not fit its load steps");
static_assert(RVU < UPREP && UPREP + 1 < SK0 && QP + QD + 1 < SK0 && cons_end(SK0 - 1) == SK0 - WV && NL - SK1 == WV, "steps every wave runs");
constexpr bool timers_long(const Prm& P) {
  const Tab T = make_tab(P);
  for (int bid = 48; bid < 64; ++bid) if (SK0 + NL - (int)(T.e[2 * bid] >> 7 & 63) > RPS) return false;
  return true;
}
static_assert(timers_long(PRM0), "steps of the second report");
constexpr int NUP = FF / 32;
constexpr int SPIN_MAX = 1 << 17;

constexpr u32 WS_EPOCH = 0;
constexpr u32 G_TB = 1024;
constexpr u32 G_MOD = 4096;
constexpr u32 G_Q = G_MOD + 3072 * 8;
constexpr u32 G_ML = G_Q + 4096 * 8;
constexpr u32 G_U = G_ML + 256 * 16;
constexpr u32 G_P = 0;
constexpr u32 G_XP = G_P + 128 * 256;
constexpr u32 G_OP = G_XP + 8 * 512 * 4;
constexpr u32 G_NT = G_OP + 43 * 512 * 8;
constexpr u32 G_PB = G_OP + 44 * 512 * 8;
constexpr u32 G_VT = G_U + 2 * (G_PB + 8192);
constexpr u32 WS_END = G_VT + 2048;
__device__ __forceinline__ u32 dup(u32 o) { return G_U + ((o >> 13) << 14) + (o & 8191u); }
constexpr u32 SENT = 0xffffffffu;
constexpr u32 OOB = 0x40000000u;
#define WST 0

constexpr int AUX_NT = 2, AUX_SC1 = 16;

__device__ __forceinline__ float lo_f(u32 v) { return __uint_as_float(v << 16); }
__device__ __forceinline__ float hi_f(u32 v) { return __uint_as_float(v & 0xffff0000u); }
__device__ __forceinline__ float rbf(float x) { return (float)(__bf16)x; }
__device__ __forceinline__ u32 pack_bf(float lo, float hi) {
  bf16x2 r; r[0] = (__bf16)lo; r[1] = (__bf16)hi; return __builtin_bit_cast(u32, r);
}
__device__ __forceinline__ float dot2(u32 k, u32 q, float acc) {
  return __builtin_amdgcn_fdot2_f32_bf16(__builtin_bit_cast(bf16x2, k), __builtin_bit_cast(bf16x2, q), acc, false);
}
template <int CTRL> __device__ __forceinline__ float dpp(float x) {
  return __builtin_bit_cast(float, __builtin_amdgcn_mov_dpp(__builtin_bit_cast(int, x), CTRL, 0xf, 0xf, true));
}
constexpr int XOR1 = 0xB1, XOR2 = 0x4E, XOR7 = 0x141, XOR8 = 0x128;
__device__ __forceinline__ float swap32_add(float a, float b) {
  auto t = __builtin_amdgcn_permlane32_swap(__float_as_uint(a), __float_as_uint(b), false, false);
  return __uint_as_float(t[0]) + __uint_as_float(t[1]);
}
__device__ __forceinline__ float swap16_add(float a, float b) {
  auto t = __builtin_amdgcn_permlane16_swap(__float_as_uint(a), __float_as_uint(b), false, false);
  return __uint_as_float(t[0]) + __uint_as_float(t[1]);
}
__device__ __forceinline__ float wave_sum(float x) {
  x += dpp<XOR1>(x); x += dpp<XOR2>(x); x += dpp<XOR7>(x); x += dpp<XOR8>(x);
  x = swap16_add(x, x);
  return swap32_add(x, x);
}
__device__ __forceinline__ float wave_max(float x) {
  x = fmaxf(x, dpp<XOR1>(x)); x = fmaxf(x, dpp<XOR2>(x)); x = fmaxf(x, dpp<XOR7>(x)); x = fmaxf(x, dpp<XOR8>(x));
  auto s = __builtin_amdgcn_permlane16_swap(__float_as_uint(x), __float_as_uint(x), false, false);
  x = fmaxf(__uint_as_float(s[0]), __uint_as_float(s[1]));
  auto t = __builtin_amdgcn_permlane32_swap(__float_as_uint(x), __float_as_uint(x), false, false);
  return fmaxf(__uint_as_float(t[0]), __uint_as_float(t[1]));
}
template <int CTRL> __device__ __forceinline__ float sel_add(float lo, float hi, bool up) {
  const float t1 = lo + dpp<CTRL>(lo), t2 = hi + dpp<CTRL>(hi);
  return up ? t2 : t1;
}
template <int I> __device__ __forceinline__ float bc16(float x) {
  return __builtin_bit_cast(float, __builtin_amdgcn_update_dpp(0, __builtin_bit_cast(int, x), 0x150 + I, 0xf, 0xf, false));
}
__device__ __forceinline__ float bcast16(float x, int i) {
  switch (i) {
    case 0: return bc16<0>(x); case 1: return bc16<1>(x); case 2: return bc16<2>(x); case 3: return bc16<3>(x);
    case 4: return bc16<4>(x); case 5: return bc16<5>(x); case 6: return bc16<6>(x); case 7: return bc16<7>(x);
    case 8: return bc16<8>(x); case 9: return bc16<9>(x); case 10: return bc16<10>(x); case 11: return bc16<11>(x);
    case 12: return bc16<12>(x); case 13: return bc16<13>(x); case 14: return bc16<14>(x); default: return bc16<15>(x);
  }
}
__device__ __forceinline__ float silu_f(float x) { return x * __builtin_amdgcn_rcpf(1.f + __expf(-x)); }

template <int AUX> __device__ __forceinline__ u32x4 ld16(rsrc_t r, u32 voff, u32 soff) {
  return __builtin_amdgcn_raw_buffer_load_b128(r, voff, soff, AUX);
}
template <int AUX> __device__ __forceinline__ u32x2 ld8(rsrc_t r, u32 voff, u32 soff) {
  return __builtin_amdgcn_raw_buffer_load_b64(r, voff, soff, AUX);
}
__device__ __forceinline__ void st8_sc1(rsrc_t r, u32 voff, u32 v, u32 tag) {
  __builtin_amdgcn_raw_buffer_store_b64(u32x2{v, tag}, r, voff, 0, AUX_SC1);
}
__device__ __forceinline__ void st16_sc1(rsrc_t r, u32 voff, u32 v0, u32 v1, u32 tag) {
  __builtin_amdgcn_raw_buffer_store_b128(u32x4{v0, tag, v1, tag}, r, voff, 0, AUX_SC1);
}
__device__ __forceinline__ rsrc_t mk_rsrc(const void* p, u32 bytes) {
  return __builtin_amdgcn_make_buffer_rsrc(const_cast<void*>(p), 0, bytes, 0x00020000);
}
#define WAIT_VM(n) __builtin_amdgcn_s_waitcnt(((n) & 15) | (((n) >> 4) << 14) | (7 << 4) | (15 << 8))
#define SB() __builtin_amdgcn_sched_barrier(0)
#define CFENCE() asm volatile("" ::: "memory")
__device__ __forceinline__ bool all_ok(bool ok) { return __builtin_amdgcn_ballot_w64(!ok) == 0ull; }

template <int N> __device__ __forceinline__ void gather16(rsrc_t ws, u32 voff, u32 stride, u32 tag, u32x4 (&g)[N]) {
#pragma nounroll
  for (int spin = 0; spin < SPIN_MAX; ++spin) {
    CFENCE();
    bool ok = true;
#pragma unroll
    for (int i = 0; i < N; ++i) g[i] = ld16<AUX_SC1>(ws, voff + i * stride, 0);
#pragma unroll
    for (int i = 0; i < N; ++i) ok &= (g[i][1] == tag) & (g[i][3] == tag);
    if (all_ok(ok)) break;
    __builtin_amdgcn_s_sleep(8);
  }
}
template <int N> __device__ __forceinline__ void gather8(rsrc_t ws, u32 voff, u32 stride, u32 tag, u32 (&g)[N], bool act = true) {
#pragma nounroll
  for (int spin = 0; spin < SPIN_MAX; ++spin) {
    CFENCE();
    bool ok = true;
    u32x2 t[N];
#pragma unroll
    for (int i = 0; i < N; ++i) t[i] = ld8<AUX_SC1>(ws, voff + i * stride, 0);
#pragma unroll
    for (int i = 0; i < N; ++i) { ok &= (t[i][1] == tag); g[i] = t[i][0]; }
    if (all_ok(ok | !act)) break;
    __builtin_amdgcn_s_sleep(8);
  }
}

struct Smem {
  float red[8][128];
  float ml[8][2];
  float r2[64 * 20];
  u16 hb[3 * 1024];
  float stat[8][64];
  float red2[8][128];
  u32 dummy[64];
  u32x2 bc, bc2;
  u32 bc3, bc4;
  u32x4 zero[4];
  float pl[4][8];
  u32 mflag[8];
  u32x4 wbuf[8][8][64];
  __attribute__((aligned(256))) u16 w2b[32][512];
  u16 actl[2][32];
};

#define PROBE_K 8
#define PROBE_OP 0
#define PROBE_PART -1
#define STAMP(k) do { if ((k) == PROBE_K && tid == 0) pst = wall_clock64(); } while (0)
#define WSTAMP(k) do {} while (0)
#define FINISH() do { STAMP(8); if (tid == 0) { __hip_atomic_store(reinterpret_cast<u64*>(ws + (1u << 20)) + bid * 2, pt0, __ATOMIC_RELAXED, __HIP_MEMORY_SCOPE_AGENT); \
   __hip_atomic_store(reinterpret_cast<u64*>(ws + (1u << 20)) + bid * 2 + 1, (PROBE_PART < 0 || part == PROBE_PART) ? pst : 0ull, __ATOMIC_RELAXED, __HIP_MEMORY_SCOPE_AGENT); } \
   if (bid == 8 && wave == 0) probe_end(ws, pt0, lane); return; } while (0)
#define PIN(x) asm volatile("" : "+v"(x))
#define LDS_BARRIER() do { __builtin_amdgcn_s_waitcnt(0xc07f); __builtin_amdgcn_s_barrier(); CFENCE(); } while (0)
#define WAVE_SYNC() do { __builtin_amdgcn_fence(__ATOMIC_RELEASE, "wavefront"); __builtin_amdgcn_wave_barrier(); __builtin_amdgcn_fence(__ATOMIC_ACQUIRE, "wavefront"); } while (0)

__device__ __forceinline__ void dma16(rsrc_t r, u32 voff, u32 soff, u32 lds_dst) {
  u32 keep;
  asm volatile("s_mov_b32 %0, m0\n\ts_mov_b32 m0, %4\n\ts_nop 4\n\tbuffer_load_dwordx4 %1, %2, %3 offen nt lds\n\ts_mov_b32 m0, %0"
               : "=&s"(keep) : "v"(voff), "s"(r), "s"(soff), "s"(lds_dst) : "memory");
}
__device__ __forceinline__ void ld16_into(u32x4& d, rsrc_t r, u32 voff, u32 soff) {
  asm volatile("s_nop 4\n\tbuffer_load_dwordx4 %0, %1, %2, %3 offen nt" : "+v"(d) : "v"(voff), "s"(r), "s"(soff) : "memory");
}
__device__ __forceinline__ u32 perm_lo(u32 a, u32 b) { return __builtin_amdgcn_perm(b, a, 0x05040100u); }
__device__ __forceinline__ u32 perm_hi(u32 a, u32 b) { return __builtin_amdgcn_perm(b, a, 0x07060302u); }
__device__ __forceinline__ float rdl(float x, int l) { return __int_as_float(__builtin_amdgcn_readlane(__float_as_int(x), l)); }

typedef __attribute__((ext_vector_type(8))) __bf16 bf16x8;
__device__ __forceinline__ f32x4 mfma16(u32x4 a, u32x4 b, f32x4 c) {
  return __builtin_amdgcn_mfma_f32_16x16x32_bf16(__builtin_bit_cast(bf16x8, a), __builtin_bit_cast(bf16x8, b), c, 0, 0, 0);
}
typedef __attribute__((ext_vector_type(8))) unsigned u32x8;
typedef __attribute__((ext_vector_type(16))) __bf16 bf16x16;
__device__ __forceinline__ f32x4 mfma64(u32x4 a, u32x8 b, f32x4 c, u32 idx) {
  return __builtin_amdgcn_smfmac_f32_16x16x64_bf16(__builtin_bit_cast(bf16x8, a), __builtin_bit_cast(bf16x16, b), c, idx, 0, 0);
}
__device__ __forceinline__ float sum8(const float* p) {
  const f32x4 a = *reinterpret_cast<const f32x4*>(p), b = *reinterpret_cast<const f32x4*>(p + 4);
  return ((a[0] + b[0]) + (a[1] + b[1])) + ((a[2] + b[2]) + (a[3] + b[3]));
}
__device__ __forceinline__ float ln_ri(float var) {
  return __builtin_amdgcn_rcpf(rbf(__builtin_amdgcn_sqrtf(rbf(rbf(fmaxf(var, 0.f)) + 1e-5f))));
}
__device__ __forceinline__ void ln_stats2(const float (&x0)[8], const float (&x1)[8], float& mb0, float& ri0, float& mb1, float& ri1) {
  const float k0 = rdl(x0[0], 0), k1 = rdl(x1[0], 0);
  float s0 = 0.f, s1 = 0.f, q0 = 0.f, q1 = 0.f;
#pragma unroll
  for (int k = 0; k < 8; ++k) {
    const float d0 = x0[k] - k0, d1 = x1[k] - k1;
    s0 += d0; s1 += d1; q0 = fmaf(d0, d0, q0); q1 = fmaf(d1, d1, q1);
  }
  float t = swap16_add(swap32_add(s0, s1), swap32_add(q0, q1));
  t += dpp<XOR1>(t); t += dpp<XOR2>(t); t += dpp<XOR7>(t); t += dpp<XOR8>(t);
  auto u = __builtin_amdgcn_permlane16_swap(__float_as_uint(t), __float_as_uint(t), false, false);
  const float ms = __uint_as_float(u[0]) * (1.f / 512.f), mq = __uint_as_float(u[1]) * (1.f / 512.f);
  const float ri = ln_ri(fmaf(-ms, ms, mq));
  mb0 = rbf(k0 + rdl(ms, 0)); mb1 = rbf(k1 + rdl(ms, 32));
  ri0 = rdl(ri, 0); ri1 = rdl(ri, 32);
}
template <int I> struct IC { static constexpr int v = I; };
template <int I, int N, class F> __device__ __forceinline__ void sfor(F&& f) {
  if constexpr (I < N) { if (f(IC<I>{})) sfor<I + 1, N>(f); }
}
typedef __attribute__((ext_vector_type(4))) short s16x4;
typedef __attribute__((address_space(3))) s16x4* lds_s16x4p;
__device__ __forceinline__ u32x2 tr_read(u32 lds_addr) {
  return __builtin_bit_cast(u32x2, __builtin_amdgcn_ds_read_tr16_b64_v4i16((lds_s16x4p)lds_addr));
}
__device__ __forceinline__ u32 next_tag(u32 e) { return ((e | 0x40004000u) + 1u) & ~0x40004000u; }
__device__ __forceinline__ float u2f(u32 v) { return __uint_as_float(v); }
#define LDSP(T, a) ((__attribute__((address_space(3))) T*)(a))
template <class T> __device__ __forceinline__ u32 lds_addr(T* p) { u32 a = (u32)(uintptr_t)p; PIN(a); return a; }

#define TAB_LOAD "s_add_u32 s30, s28, wg_tab@rel32@lo+4\ns_addc_u32 s31, s29, wg_tab@rel32@hi+12\nglobal_load_dwordx2 %[te], %[va], s[30:31]\n"
#define FC(k) ".Lfc" #k
#define FC_REGS
#define FX(r, bit, k) "s_bitcmp1_b32 %[" #r "], " #bit "\ns_cselect_b32 %[sb], " FC(k) ", 0\ns_xor_b32 %[sa], %[sa], %[sb]\n"
__device__ __forceinline__ void probe_end(unsigned char* ws, u64 pt0, int lane) {
  while (wall_clock64() < pt0 + 5500ull) __builtin_amdgcn_s_sleep(8);
  u64 a[4], c2[4];
  for (int i = 0; i < 4; ++i) {
    a[i] = __hip_atomic_load(reinterpret_cast<u64*>(ws + (1u << 20)) + (i * 64 + lane) * 2, __ATOMIC_RELAXED, __HIP_MEMORY_SCOPE_AGENT);
    c2[i] = __hip_atomic_load(reinterpret_cast<u64*>(ws + (1u << 20)) + (i * 64 + lane) * 2 + 1, __ATOMIC_RELAXED, __HIP_MEMORY_SCOPE_AGENT);
  }
  int mn0 = 1 << 30, mx1 = -(1 << 30), mn1 = 1 << 30, sum = 0, cnt = 0;
  for (int i = 0; i < 4; ++i) {
    const int d0 = (int)(a[i] - pt0);
    mn0 = min(mn0, d0);
    if (c2[i] != 0ull) { const int d1 = (int)(c2[i] - pt0); mx1 = max(mx1, d1); mn1 = min(mn1, d1); sum += d1; cnt += 1; }
  }
  for (int o = 1; o < 64; o <<= 1) { mn0 = min(mn0, __shfl_xor(mn0, o)); mx1 = max(mx1, __shfl_xor(mx1, o)); mn1 = min(mn1, __shfl_xor(mn1, o)); sum += __shfl_xor(sum, o); cnt += __shfl_xor(cnt, o); }
  int X = PROBE_OP == 0 ? mx1 : (PROBE_OP == 1 ? sum / max(cnt, 1) : mn1);
  if (PROBE_K == 102) X = mn0;
  X = mn0 + (X - mn0) * PROBE_MUL;
  const u64 tg = pt0 + (u64)(long long)(X + 8000);
  for (int i = 0; i < 400000 && wall_clock64() < tg; ++i) __builtin_amdgcn_s_sleep(1);
}
__global__ void __launch_bounds__(512, 2) fused_kernel(const u16* kc_, const u16* vc_, const u16* wo_, const u16* wq_, const u16* wmod_, const u16* w1_, const u16* w3_,
                                                       unsigned char* ws_, const u16* x_, const u16* cnd_, const u16* w2_, u16* out_) {
  __shared__ __attribute__((aligned(256))) Smem sm;
  const u64 a_ka = (u64)__builtin_amdgcn_kernarg_segment_ptr();
  const int bid = blockIdx.x, tid = threadIdx.x;
  u64 wa, t64; u32x2 te; int tid0;
  u32 ar[14];
  u32 tch2, sa_, sb_, va_;
  u32x4 kr[NL], vr[NL];
  asm volatile(".set .Lfc0, %0\n.set .Lfc1, %1\n.set .Lfc2, %2\n.set .Lfc3, %3\n.set .Lfc4, %4\n.set .Lfc5, %5\n.set .Lfc6, %6\n.set .Lfc7, %7\n.set .Lfc8, %8\n.set .Lfc9, %9\n.set .Lfc10, %10\n.set .Lfc11, %11"
    :: "n"(first_col(PRM0, 0)), "n"(first_col(PRM0, 1)), "n"(first_col(PRM0, 2)), "n"(first_col(PRM0, 3)), "n"(first_col(PRM0, 4)), "n"(first_col(PRM0, 5)),
       "n"(first_col(PRM0, 6)), "n"(first_col(PRM0, 7)), "n"(first_col(PRM0, 8)), "n"(first_col(PRM0, 9)), "n"(first_col(PRM0, 10)), "n"(first_col(PRM0, 11)));
  asm volatile(
    "s_load_dwordx16 s[4:19], %[ka], 0x0\ns_load_dwordx8 s[20:27], %[ka], 0x40\n"
    "s_memrealtime %[t]\n"
    "v_lshlrev_b32 %[va], 3, %[bid]\n"
    "s_getpc_b64 s[28:29]\n.Lcpf_first:\n"
    TAB_LOAD
    "s_cmp_gt_u32 %[bid], 15\ns_cbranch_scc1 .Lno_cpf\n"
    "s_mov_b32 %[sa], .Lend_a-.Lcpf_first\ns_max_i32 %[sa], %[sa], .Lend_b-.Lcpf_first\ns_max_i32 %[sa], %[sa], .Lend_c-.Lcpf_first\ns_max_i32 %[sa], %[sa], .Lend_d-.Lcpf_first\ns_max_i32 %[sa], %[sa], .Lend_e-.Lcpf_first\n"
    "v_lshlrev_b32 %[va], 6, %[tid]\nv_min_u32 %[va], %[sa], %[va]\nglobal_load_dword %[pf], %[va], s[28:29]\n"
    ".Lno_cpf:\n"
    "v_readfirstlane_b32 %[t0], %[tid]\nv_and_b32 %[va], 63, %[tid]\n"
    "s_mov_b32 %[sa], " FC(11) "\n"
    FX(bid, 0, 0) FX(bid, 1, 1) FX(bid, 2, 2) FX(bid, 3, 3) FX(bid, 4, 4) FX(bid, 5, 5) FX(bid, 6, 6) FX(bid, 7, 7) FX(t0, 6, 8) FX(t0, 7, 9) FX(t0, 8, 10)
    "v_lshl_add_u32 %[va], %[va], 4, %[sa]\n"
    "s_waitcnt lgkmcnt(0)\nglobal_load_dwordx4 %[k0], %[va], s[4:5] nt\nglobal_load_dwordx4 %[v0], %[va], s[6:7] nt\n"
    "s_bitcmp1_b32 %[t0], 6\ns_cselect_b64 s[14:15], s[16:17], s[14:15]\n"
    "s_bitcmp1_b32 %[bid], 4\ns_cselect_b64 s[8:9], s[10:11], s[8:9]\ns_cselect_b64 s[12:13], s[14:15], s[12:13]\ns_bitcmp1_b32 %[bid], 5\ns_cselect_b64 %[wa], s[12:13], s[8:9]"
    : "=&{s4}"(ar[0]), "=&{s5}"(ar[1]), "=&{s6}"(ar[2]), "=&{s7}"(ar[3]), "=&{s18}"(ar[4]), "=&{s19}"(ar[5]), "=&{s20}"(ar[6]), "=&{s21}"(ar[7]), "=&{s22}"(ar[8]), "=&{s23}"(ar[9]),
      "=&{s24}"(ar[10]), "=&{s25}"(ar[11]), "=&{s26}"(ar[12]), "=&{s27}"(ar[13]), [wa] "=&s"(wa), [t0] "=&s"(tid0), [t] "=&s"(t64), [te] "=&v"(te), [sa] "=&s"(sa_), [sb] "=&s"(sb_),
      [va] "=&v"(va_), [pf] "=&v"(tch2), [k0] "=&v"(kr[0]), [v0] "=&v"(vr[0])
    : [ka] "s"(a_ka), [bid] "s"(bid), [tid] "v"(tid) : "s8", "s9", "s10", "s11", "s12", "s13", "s14", "s15", "s16", "s17", "s28", "s29", "s30", "s31", "scc", "memory" FC_REGS);
  asm volatile("" : "+s"(ar[0]), "+s"(ar[1]), "+s"(ar[2]), "+s"(ar[3]), "+s"(ar[4]), "+s"(ar[5]), "+s"(ar[6]), "+s"(ar[7]), "+s"(ar[8]), "+s"(ar[9]), "+s"(ar[10]), "+s"(ar[11]), "+s"(ar[12]), "+s"(ar[13]));
  asm volatile("" : "+s"(wa), "+s"(t64), "+s"(tid0), "+s"(sa_));
  const u32 tka = (u32)wall_clock64();
  __builtin_assume(tid >= 0 && tid < 512 && tid0 >= 0 && tid0 < 512 && bid >= 0 && bid < 256);
  constexpr Prm P = PRM0;
  const u64 pt0 = t64; u64 pst = (PROBE_K == 100 || PROBE_K == 102) ? pt0 : 0;
  const u32 t0 = tka + min(max((int)(tka - (u32)t64) - P.SA, 0) * P.SG >> 4, P.SC);
  const int TEND = P.TEND;
  const int xcd = bid & 7, part = bid >> 4 & 3;
  const int lane = tid & 63, wave = tid0 >> 6;
  SB();
  if ((bid >> 4) == 2) {
    const u32 o = tid < 384 ? G_U + (bid & 15) * 6144 + tid * 16 : OOB;
    __builtin_amdgcn_raw_buffer_store_b128(u32x4{SENT, SENT, SENT, SENT}, mk_rsrc((void*)(ar[4] | (u64)ar[5] << 32), WS_END), o, 0, AUX_SC1);
  }
  SB();
  const bool p3 = part == 3;
  u32 pitch = (u32)(0x005600c001000020ull >> (part * 16) & 0xffff) * 32u;
  const int sh = p3 ? 2 : 3;
  u32 wv0 = (u32)((p3 ? (wave >> 1) * 128 : wave * 64) + (part == 0 ? xcd * 512 : 0) + (lane >> sh)) * pitch + ((lane & (p3 ? 3 : 7)) ^ (lane >> (p3 ? 5 : 3))) * 16u;
  const u32 wimg = (u32)(uintptr_t)(&sm.wbuf[0][0][0]);
  u32 wsz = (part == 0 ? 4096u : 512u) * pitch, wld = wimg + (p3 ? (wave & 1) * 32768 + (wave >> 1) * 8192 : wave * 8192);
  PIN(wv0);
  asm volatile("" : "+s"(wsz), "+s"(wld), "+s"(pitch));
  auto karg = [&](int i) { return ar[2 * i] | (u64)ar[2 * i + 1] << 32; };
  const u64 a_kc = karg(0), a_vc = karg(1);
  unsigned char* ws = (unsigned char*)karg(2);
  const u16 *x = (const u16*)karg(3), *cnd = (const u16*)karg(4), *w2 = (const u16*)karg(5);
  u16* out = (u16*)karg(6);
  const rsrc_t wsr = mk_rsrc(ws, WS_END);
  asm volatile("s_waitcnt vmcnt(3)\n\ts_cmp_eq_u32 %1, 2\n\ts_cbranch_scc1 1f\n\ts_waitcnt vmcnt(2)\n1:" : "+v"(te) : "s"(bid >> 4) : "scc");
  const u32 te0 = __builtin_amdgcn_readfirstlane(te[0]), te1 = __builtin_amdgcn_readfirstlane(te[1]);
  const int bl0 = te0 & 127, nl = te0 >> 7 & 63, blk = te0 >> 13 & 63;
  const bool has_w = te0 >> 19 & 1;
  const rsrc_t rw = mk_rsrc((const void*)wa, wsz);
  const u32 wstep = pitch << (6 - sh), wflip = (p3 || part == 0) ? 32u : 0u, wflip0 = part == 0 ? 32u : 0u;
  u32 wso = blk << (sh + 4);
  int wleft = 8;
  auto piece = [&]() {
    dma16(rw, wv0, wso, wld);
    wso += wstep; wld += 1024;
    wv0 ^= (wleft & 1) ? wflip : wflip0;
    --wleft;
  };
  SB();
  STAMP(23);
  const int b = bid >> 3 & 1, head = xcd * 4 + (bid >> 6 ^ 3);
  const int g = lane >> 4, c = lane & 15;
  const int i8 = b | bid >> 6 << 1, wi = i8 * 8 + xcd;
  const bool is_up = p3 && has_w, modc = part == 2 && wi < 16;
  const size_t base = ((size_t)(b * NH + head) * SK + (SK - WIN)) * DH;
  const rsrc_t rk = mk_rsrc((const u16*)a_kc + base, WIN * DH * 2), rv = mk_rsrc((const u16*)a_vc + base, WIN * DH * 2);
  int tau = te1 & 0xffff;
  const int tauw = te0 >> 24 << 7, wdef = te0 >> 20 & 15;
  int due = P.NF * 256, now = 0;
  auto gate = [&]() {
    while (now < due) { now = (int)(((u32)wall_clock64() - t0) << 8); if (now < due) __builtin_amdgcn_s_sleep(1); }
    due += tau;
  };
  auto until = [&](int t) { while ((int)(((u32)wall_clock64() - t0) << 8) < t); };
  const u32 pslot = dup(G_P + (head & 15) * 2048 + (b * 4 + part) * 16);
  const u32 die = xcd >> 2;
  const bool voter = p3 && i8 == 1, timer = p3 && i8 < 2;
  int vote = 0, lag = 0, shift = 0;
  const u32 pbo = dup(G_PB) + wave * 1152;
  u32 near = 0;
  u32x2 ep; u32 xs0, xs1, tch1; u32x4 xr0, xr1;
  const rsrc_t rx = mk_rsrc(part == 2 ? cnd : x, (part == 1 || (part == 2 && has_w)) ? 2048u : 0u);
  auto small_ops = [&]() {
    ep = ld8<AUX_SC1>(wsr, wave == 7 ? WS_EPOCH : OOB, 0);
    xs0 = __builtin_amdgcn_raw_buffer_load_b16(rx, tid * 2, 0, 0); xs1 = __builtin_amdgcn_raw_buffer_load_b16(rx, tid * 2, 1024, 0);
    xr0 = ld16<0>(rx, part == 1 ? lane * 16 : OOB, 0); xr1 = ld16<0>(rx, part == 1 ? lane * 16 : OOB, 1024);
    tch1 = __builtin_amdgcn_raw_buffer_load_b32(mk_rsrc(w2 + (size_t)blk * 32 * 512, is_up ? 64u : 0u), tid == 384 ? 0 : OOB, 0, AUX_NT);
  };
  if (has_w) {
    if (modc) now = 1 << 30;
#pragma nounroll
    while (wleft > wdef) { gate(); due += tauw - tau; piece(); }
    now = 0;
  }
  due = max(due, P.TK * 256);
  SB();
  u32 sot = sa_ & 0xffc00u;
  {
    const u32 s1 = (bl0 + lane - 1 - (lane >= SK0 ? NL - nl : 0)) ^ (124 + part);
#pragma unroll
    for (int k = 0; k < 7; ++k) sot ^= (u32)-(int)(s1 >> k & 1u) & col(P, k);
    PIN(sot);
  }
  u32 tag = 0;
  u32x4 bf[8];
  auto wo_prep = [&]() {
    const u32 wi_ = wimg + wave * 8192;
    const int q = (lane >> 2) & 3, pp = lane & 3;
#pragma unroll
    for (int s = 0; s < 2; ++s) {
#pragma unroll
      for (int t = 0; t < 4; ++t) {
        const int r0 = s * 32 + g * 8 + q, r1 = r0 + 4, ch = t * 2 + (pp >> 1);
        const u32x2 lo = tr_read(wi_ + r0 * 128 + ((ch ^ (r0 & 7)) * 16) + (pp & 1) * 8), hi = tr_read(wi_ + r1 * 128 + ((ch ^ (r1 & 7)) * 16) + (pp & 1) * 8);
        bf[s * 4 + t] = u32x4{lo[0], lo[1], hi[0], hi[1]};
      }
    }
  };
  auto gemv_go = [&](u32 hh) {
    sm.hb[tid] = (u16)hh; sm.hb[512 + tid] = (u16)(hh >> 16);
    WAVE_SYNC();
    const u32x4* a = (c & 11) == 0 ? reinterpret_cast<const u32x4*>(&sm.hb[(c >> 2) * 512 + wave * 64 + g * 8]) : &sm.zero[0];
    const u32x4 a0 = a[0], a1 = a[(c & 11) == 0 ? 4 : 0];
    const f32x4 z = {0.f, 0.f, 0.f, 0.f};
    f32x4 d[4];
#pragma unroll
    for (int t = 0; t < 4; ++t) d[t] = mfma16(a0, bf[t], z);
#pragma unroll
    for (int t = 0; t < 4; ++t) d[t] = mfma16(a1, bf[4 + t], d[t]);
    if (lane < 32) {
#pragma unroll
      for (int t = 0; t < 4; ++t) sm.r2[(t * 16 + c) * 20 + g * 8 + wave] = d[t][0];
    }
  };
  auto mod_stage = [&]() {
    wo_prep();
    gemv_go(pack_bf(silu_f(lo_f(xs0)), silu_f(lo_f(xs1))));
    __builtin_amdgcn_s_waitcnt(0xc07f);
    if (lane == 0) *LDSP(volatile u32, (u32)(uintptr_t)&sm.mflag[wave]) = 1u;
    if (wave == 7) {
#pragma nounroll
      for (int spin = 0; spin < SPIN_MAX; ++spin) {
        const u32 f = *LDSP(volatile u32, (u32)(uintptr_t)&sm.mflag[lane & 7]);
        if (all_ok(f != 0u)) break;
        __builtin_amdgcn_s_sleep(1);
      }
      CFENCE();
      st8_sc1(wsr, G_MOD + (blk * 64 + lane) * 8, pack_bf(sum8(&sm.r2[lane * 20]), sum8(&sm.r2[lane * 20 + 8])), tag);
    }
    STAMP(9);
  };
  if (lane < 4) sm.zero[lane] = u32x4{0u, 0u, 0u, 0u};
  if (part == 2 && has_w) {
    if (tid < 8) sm.mflag[tid] = 0u;
    LDS_BARRIER();
    if (__builtin_expect(modc, 0)) {
      small_ops();
      asm volatile("s_waitcnt vmcnt(0)" ::: "memory");
      tag = next_tag(__builtin_amdgcn_readfirstlane(ep[0]));
      mod_stage();
      __builtin_amdgcn_s_barrier();
    }
  }
  SB();
  const int fd = (xcd * 8 + i8) * 8 + (lane & 3) * 2;
  const int ub = wave >> 2, ud = (wave & 3) * 128 + c * 8 + g * 2;
  u32x4 sg[3]; u32 sx2;
  auto side_loads = [&]() {
    const bool fin = part == 1 && wave == 0;
    sg[0] = ld16<AUX_SC1>(wsr, is_up ? G_MOD + (1024 + ud) * 8 : (fin ? G_MOD + (1024 + fd) * 8 : OOB), 0);
    sg[1] = ld16<AUX_SC1>(wsr, is_up ? G_MOD + (1536 + ud) * 8 : (fin ? G_MOD + (2560 + fd) * 8 : OOB), 0);
    sg[2] = ld16<AUX_SC1>(wsr, is_up ? G_MOD + (2048 + ud) * 8 : OOB, 0);
    sx2 = __builtin_amdgcn_raw_buffer_load_b32(mk_rsrc(x, (is_up || fin) ? 2048u : 0u), is_up ? ub * 1024 + ud * 2 : (g & 1) * 1024 + fd * 2, 0, 0);
  };
  auto side_pin = [&]() { PIN(sg[0]); PIN(sg[1]); PIN(sg[2]); PIN(sx2); };
  const u32 mlo = G_ML + (head * 2 + b) * 64 + (lane & 3) * 16;
  float fmrg = 0.f;
  auto merge_factor = [&](const u32x4& ml) {
    const float mi = u2f(ml[0]);
    float M = fmaxf(mi, dpp<XOR1>(mi)); M = fmaxf(M, dpp<XOR2>(M));
    const float e = __expf(mi - M);
    float L = e * u2f(ml[2]);
    L += dpp<XOR1>(L); L += dpp<XOR2>(L);
    fmrg = rdl(e * __builtin_amdgcn_rcpf(L), part);
  };
  f32x2 ga = {0.f, 0.f}, xin = {0.f, 0.f}, cc1 = {0.f, 0.f};
  float Af = 0.f, Bf = 0.f;
  const int hpos = ub * 512 + (ud & ~15) + (g & 1) * 8 + (c & 1) * 4 + (g >> 1) * 2;
  auto up_prep = [&]() {
    if (!all_ok((sg[0][1] == tag) & (sg[0][3] == tag) & (sg[1][1] == tag) & (sg[1][3] == tag) & (sg[2][1] == tag) & (sg[2][3] == tag))) gather16<3>(wsr, G_MOD + (1024 + ud) * 8, 512 * 8, tag, sg);
    ga = f32x2{ub ? hi_f(sg[0][0]) : lo_f(sg[0][0]), ub ? hi_f(sg[0][2]) : lo_f(sg[0][2])};
    cc1 = f32x2{rbf(1.f + (ub ? hi_f(sg[2][0]) : lo_f(sg[2][0]))), rbf(1.f + (ub ? hi_f(sg[2][2]) : lo_f(sg[2][2])))};
    xin = f32x2{lo_f(sx2), hi_f(sx2)};
    u32* hp = reinterpret_cast<u32*>(&sm.hb[hpos]);
    hp[512] = pack_bf(cc1[0], cc1[1]);
    hp[1024] = ub ? perm_hi(sg[1][0], sg[1][2]) : perm_lo(sg[1][0], sg[1][2]);
  };
  u32x8 bw[4];
  auto up_frags = [&]() {
    const int pp = lane & 3;
    const u32 a = wimg + (pp & 1) * 32768 + ((pp >> 1) * 32 + g * 8 + ((lane >> 2) & 3)) * 64 + (((wave >> 1) ^ ((g & 1) | (pp & 2))) * 16) + (wave & 1) * 8;
#pragma unroll
    for (int j = 0; j < 4; ++j) {
      const u32x2 r0 = tr_read(a + j * 4096), r1 = tr_read(a + j * 4096 + 256), r2 = tr_read(a + j * 4096 + 16384), r3 = tr_read(a + j * 4096 + 16640);
      bw[j] = u32x8{r0[0], r0[1], r1[0], r1[1], r2[0], r2[1], r3[0], r3[1]};
    }
  };
  u32 wfa[4];
  auto wo_frags = [&](int s0) {
#pragma unroll
    for (int t = 0; t < 4; ++t) {
#pragma unroll
      for (int s = s0; s < 2; ++s) {
        const u32x2 lo = tr_read(wfa[t] + s * 4096), hi = tr_read(wfa[t ^ 1] + s * 4096 + 256);
        bw[t][s * 4] = lo[0]; bw[t][s * 4 + 1] = lo[1]; bw[t][s * 4 + 2] = hi[0]; bw[t][s * 4 + 3] = hi[1];
      }
    }
  };
  const bool par = lane & 8;
  auto limit = [&](auto sc) {
    constexpr int s = sc.v;
    WAIT_VM(window(s, 1 + (PRM0.W[s >> 3] >> (4 * (s & 7)) & 7)));
    SB();
  };
  auto sample = [&]() { if (timer) lag += (int)(((u32)wall_clock64() - t0) << 8) - due + ((int)__builtin_amdgcn_s_getreg(7 | (3 << 11)) + 1) * tau; };
  auto report = [&](u32 dst) {
    if (timer) {
      if (lane == 0) sm.mflag[wave] = vote * 65536 + min(max(lag >> 8, 0), 4000);
      lag = 0;
      LDS_BARRIER();
      if (wave == 7) {
        int s = sm.mflag[lane & 7];
        s += __builtin_amdgcn_mov_dpp(s, XOR1, 0xf, 0xf, true); s += __builtin_amdgcn_mov_dpp(s, XOR2, 0xf, 0xf, true); s += __builtin_amdgcn_mov_dpp(s, XOR7, 0xf, 0xf, true);
        if (lane < 8) st8_sc1(wsr, dst + lane * 128 + wi * 8, s, tag);
      }
    }
    SB();
  };
  float l0 = 0.f, l1 = 0.f;
  u32x2 gmd[2], tbg;
  int t1 = 0;
#pragma unroll
  for (int i = SK0; i < SK1; ++i) kr[i] = u32x4{0u, 0u, 0u, 0u};
  const int first = SK0 + NL - nl;
  auto left_out = [&](int i) { int f = first; asm volatile("" : "+s"(f)); return i < f; };
  sfor<1, NL>([&](auto ic) {
    constexpr int i = ic.v;
    if constexpr (i >= SK0 && i < SK1) { if (left_out(i)) return true; }
    const u32 so = __builtin_amdgcn_readlane(sot, i);
    gate();
    if constexpr ((i >= TBS && i <= TBP) || (i >= RPS && i <= RPP)) sample();
    SB();
    if constexpr (i >= SK0 && i < SK1) ld16_into(kr[i], rk, lane * 16, so); else kr[i] = ld16<AUX_NT>(rk, lane * 16, so);
    if constexpr (i == SMO) small_ops();
    if constexpr (i == SMO + 1) { if (voter && (xcd & 3) == 0) __builtin_amdgcn_raw_buffer_store_b32(0u, wsr, pbo + (lane >> 5) * 8192 + (lane & 31) * 4, 0, AUX_SC1); }
    if constexpr (i == SK1) tbg = ld8<AUX_SC1>(wsr, wave == 7 ? G_VT + xcd * 128 + c * 8 : OOB, 0);
    if constexpr (i == TBL) tbg = ld8<AUX_SC1>(wsr, wave == 7 ? G_TB + xcd * 128 + c * 8 : OOB, 0);
    if constexpr (i == QP) { gmd[0] = ld8<AUX_SC1>(wsr, part == 1 ? G_MOD + tid * 8 : OOB, 0); gmd[1] = ld8<AUX_SC1>(wsr, part == 1 ? G_MOD + tid * 8 : OOB, 4096); }
    SB();
    SB();
    if (i >= 4) limit(ic);
    if (i == 8) STAMP(12);
    if (i == 20) STAMP(21);
    if (i == 32) STAMP(22);
    if (i == TBP) {
      if (timer) {
        if (lane == 0) sm.mflag[wave] = lag;
        lag = 0;
        LDS_BARRIER();
        if (wave == 7) {
          int s = sm.mflag[lane & 7];
          s += __builtin_amdgcn_mov_dpp(s, XOR1, 0xf, 0xf, true); s += __builtin_amdgcn_mov_dpp(s, XOR2, 0xf, 0xf, true); s += __builtin_amdgcn_mov_dpp(s, XOR7, 0xf, 0xf, true);
          if (lane < 8) st8_sc1(wsr, G_TB + lane * 128 + wi * 8, t0 << 10 | (u32)min(s / (2048 * (TBP - TBS + 1)), 1023), tag);
        }
      }
      SB();
    }
    if (i == PRB) {
      if (voter) {
        u64 ta, tb, tc; u32 dm;
        const u32 pa = pbo + (wave & 1) * 8192;
        asm volatile("s_memtime %0\n\ts_waitcnt lgkmcnt(0)\n\ts_load_dword %3, %4, %5\n\ts_waitcnt lgkmcnt(0)\n\ts_memtime %1\n\ts_waitcnt lgkmcnt(0)\n\t"
                     "s_load_dword %3, %4, %6\n\ts_waitcnt lgkmcnt(0)\n\ts_memtime %2\n\ts_waitcnt lgkmcnt(0)"
                     : "=&s"(ta), "=&s"(tb), "=&s"(tc), "=&s"(dm) : "s"(ws), "s"(pa), "s"(pa ^ 8192u));
        const int d = min(max((int)(2 * (u32)tb - (u32)ta - (u32)tc), -500), 500);
        vote = ((wave ^ die) & 1) ? -d : d;
        STAMP(26);
      }
      SB();
    }
    if (i == TBU) {
      if (wave == 7) {
        const bool ok = tbg[1] == tag;
        float lf = ok ? (float)min(max((int)((t0 << 10 | 1023u) - tbg[0]) >> 10, -300), 300) : 0.f, nf = ok ? 1.f : 0.f, lg = ok ? (float)(tbg[0] & 1023u) : 0.f;
        lf += dpp<XOR1>(lf); nf += dpp<XOR1>(nf); lg += dpp<XOR1>(lg); lf += dpp<XOR2>(lf); nf += dpp<XOR2>(nf); lg += dpp<XOR2>(lg);
        lf += dpp<XOR7>(lf); nf += dpp<XOR7>(nf); lg += dpp<XOR7>(lg); lf += dpp<XOR8>(lf); nf += dpp<XOR8>(nf); lg += dpp<XOR8>(lg);
        const float rn = __builtin_amdgcn_rcpf(fmaxf(nf, 1.f));
        shift = __builtin_amdgcn_readfirstlane(min(max((int)((lg * rn - (float)P.GS) * (float)P.GB * 0.125f), 0), 150));
        if (lane == 0) sm.bc = u32x2{tag, t0 - (u32)(int)(lf * rn) + shift};
      }
      LDS_BARRIER();
      const u32x2 bc = sm.bc;
      tag = __builtin_amdgcn_readfirstlane(bc[0]);
      t1 = (__builtin_amdgcn_readfirstlane((int)(bc[1] - t0)) + TEND + P.X1) * 256;
      tau -= __builtin_amdgcn_readfirstlane((int)((float)(int)(t0 - bc[1]) * 256.f * __builtin_amdgcn_rcpf((float)(nl - TBU - 1))));
      SB();
    }
    if (i == RPP) report(G_VT);
    if (i == LNS) {
      tag = next_tag(__builtin_amdgcn_readfirstlane(ep[0]));
      if (part == 1) {
        float x0[8], x1[8], mb0, ri0, mb1, ri1;
#pragma unroll
        for (int k = 0; k < 4; ++k) { x0[2 * k] = lo_f(xr0[k]); x0[2 * k + 1] = hi_f(xr0[k]); x1[2 * k] = lo_f(xr1[k]); x1[2 * k + 1] = hi_f(xr1[k]); }
        ln_stats2(x0, x1, mb0, ri0, mb1, ri1);
        l0 = rbf(rbf(lo_f(xs0) - mb0) * ri0); l1 = rbf(rbf(lo_f(xs1) - mb1) * ri1);
      }
      PIN(l0); PIN(l1);
      SB();
    }
    if (i == WUSE) {
      PIN(tch1); PIN(tch2);
      if ((part == 1 || (part == 2 && has_w && !modc))) {
        WAIT_VM(WK); CFENCE();
        if (part == 2) mod_stage(); else wo_prep();
      }
#pragma unroll
      for (int k = 0; k < 8; ++k) PIN(bf[k]);
      SB();
    }
    if (i == QP + QD) {
      if (part == 1) {
        u32 gm[2] = {gmd[0][0], gmd[1][0]};
        if (!all_ok((gmd[0][1] == tag) & (gmd[1][1] == tag))) { STAMP(11); gather8<2>(wsr, G_MOD + tid * 8, 512 * 8, tag, gm); }
        STAMP(5);
        gemv_go(pack_bf(rbf(l0 * rbf(1.f + lo_f(gm[1]))) + lo_f(gm[0]), rbf(l1 * rbf(1.f + hi_f(gm[1]))) + hi_f(gm[0])));
      }
      SB();
    }
    if (i == QP + QD + 1) {
      if (part == 1) {
        LDS_BARRIER();
        if (wave == 0) {
          const int bb = lane >> 5, jq = lane & 31;
          st8_sc1(wsr, G_Q + (bb * 2048 + blk * 32 + jq) * 8, pack_bf(sum8(&sm.r2[2 * jq * 20 + bb * 8]), sum8(&sm.r2[(2 * jq + 1) * 20 + bb * 8])), tag);
        }
        STAMP(6);
      }
      SB();
    }
    return true;
  });
  PIN(sot);
  if (wave == 7) {
    const bool ok = tbg[1] == tag;
    float lg = ok ? (float)(tbg[0] & 0xffffu) : 0.f, nf = ok ? 8.f * (RPP - RPS + 1) : 0.f;
    lg += dpp<XOR1>(lg); nf += dpp<XOR1>(nf); lg += dpp<XOR2>(lg); nf += dpp<XOR2>(nf); lg += dpp<XOR7>(lg); nf += dpp<XOR7>(nf); lg += dpp<XOR8>(lg); nf += dpp<XOR8>(nf);
    lg *= __builtin_amdgcn_rcpf(fmaxf(nf, 1.f));
    if (lane == 0) sm.bc2 = u32x2{(u32)min(shift * P.DG * 32, P.EW * 256), (u32)min(max((int)((lg - (float)P.B0) * (float)(P.BG * 32) + fmaxf(lg - (float)P.TC, 0.f) * (float)(P.RT * 32)) - shift * P.D0 * 32, 0), 200 * 256)};
  }
  LDS_BARRIER();
  {
    const u32x2 bc = sm.bc2;
    const int st = __builtin_amdgcn_readfirstlane(bc[1]), sp = (st * P.BF) >> 3;
    t1 += st + __builtin_amdgcn_readfirstlane(bc[0]);
    due += P.TG * 256 + sp;
    tau = (te1 >> 16) + __builtin_amdgcn_readfirstlane((int)((float)(st - sp) * __builtin_amdgcn_rcpf((float)(nl - 3))));
  }
  const u32 qoff = G_Q + ((b * 32 + head) * 64 + c * 4) * 8;
  u32x4 qg[2], mlg;
  u32 qq[4];
  constexpr int NB = (NL + 15) / 16;
  float xlo, sc[NB], pr[NB], m = 0.f;
  f32x4 acc = {0.f, 0.f, 0.f, 0.f};
  u32 pb[NB], pm[4], pl8[8], cpf;
  const u32 adst0 = lds_addr(lane < 32 ? &sm.red[wave][c * 8 + g * 4] : &sm.r2[(lane - 32) * 4]);
  u32 po, pso, rdst, rsrc, xo;
  auto wo_addr = [&]() {
    po = dup(G_P + (xcd & 3) * 8192) + tid * 16 + near;
    pso = tid < 16 ? pslot + c * 128 + near : OOB;
    PIN(pso);
    rdst = (u32)(uintptr_t)&sm.red2[wave][(lane & 31) * 4 + (lane >> 5) * 2];
    rsrc = (u32)(uintptr_t)&sm.red2[0][((wave & 1) * 16 + c) * 4 + (g & 1) * 2 + (g >> 1)];
    xo = dup(G_XP + xcd * 2048 + (wave & 1) * 1024 + (blk * 64 + (lane & ~1)) * 2) + (lane & 1) * 8192;
    PIN(po); PIN(rdst); PIN(rsrc); PIN(xo);
    const int q = (lane >> 2) & 3, pp = lane & 3, r = (g & 1) * 4 + (q & 1);
#pragma unroll
    for (int t = 0; t < 4; ++t) { wfa[t] = wimg + wave * 8192 + ((g >> 1) * 16 + (q >> 1) * 8 + r) * 128 + (pp & 1) * 8 + ((t * 2 + (pp >> 1)) ^ r ^ (q & 2)) * 16; PIN(wfa[t]); }
  };
  const u32 nadd = wdef ? P.LW : (is_up ? P.LU : 0);
  u32 nto = tid < 2 ? dup(G_NT) + bid * 8 + tid * 8192 : OOB;
  PIN(nto);
  auto notices = [&]() -> int {
    u32 off = dup(G_NT) + near + lane * 16;
    PIN(off);
    until(due);
    int mx;
#pragma nounroll
    for (int spin = 0; spin < SPIN_MAX; ++spin) {
      CFENCE();
      const u32x4 ga_ = ld16<AUX_SC1>(wsr, off, 0), gb_ = ld16<AUX_SC1>(wsr, off, 1024);
      mx = max(max((int)(ga_[0] - t0), (int)(ga_[2] - t0)), max((int)(gb_[0] - t0), (int)(gb_[2] - t0)));
      if (all_ok((((ga_[1] ^ tag) | (ga_[3] ^ tag)) | ((gb_[1] ^ tag) | (gb_[3] ^ tag))) == 0u)) break;
      for (int k = 0; k < P.AG; ++k) __builtin_amdgcn_s_sleep(1);
    }
    if (all_ok(mx <= (t1 >> 8) - P.AC)) return t1;
    mx = max(mx, __builtin_amdgcn_mov_dpp(mx, XOR1, 0xf, 0xf, true)); mx = max(mx, __builtin_amdgcn_mov_dpp(mx, XOR2, 0xf, 0xf, true));
    mx = max(mx, __builtin_amdgcn_mov_dpp(mx, XOR7, 0xf, 0xf, true)); mx = max(mx, __builtin_amdgcn_mov_dpp(mx, XOR8, 0xf, 0xf, true));
    mx = max(max(__builtin_amdgcn_readlane(mx, 0), __builtin_amdgcn_readlane(mx, 16)), max(__builtin_amdgcn_readlane(mx, 32), __builtin_amdgcn_readlane(mx, 48)));
    return max(min(mx, 1 << 20) * 256, t1 + P.L0 * 256) + P.L1 * 256;
  };
  auto pv_row = [&](auto jc) {
    constexpr int i = jc.v;
    const u32 pv = __float_as_uint(bcast16(u2f(pb[i >> 4]), i & 15));
    acc = mfma16(u32x4{pv & pm[0], pv & pm[1], pv & pm[2], pv & pm[3]}, vr[i], acc);
    PIN(acc);
    return true;
  };
  sfor<0, NL - 1>([&](auto ic) {
    constexpr int v = ic.v;
    if constexpr (v >= SK0 && v < SK1) { if (left_out(v)) return true; }
    if constexpr (v > 0) {
      const u32 so = __builtin_amdgcn_readlane(sot, v);
      if constexpr (v >= SK1) { if (v == NL - P.NE) tau = (tau * P.FE) >> 4; }
      gate();
      if constexpr (v >= RVS && v <= RVP) sample();
      SB();
      if constexpr (v >= SK0 && v < SK1) ld16_into(vr[v], rv, lane * 16, so); else vr[v] = ld16<AUX_NT>(rv, lane * 16, so);
      SB();
      SB();
    }
    if constexpr (v == SK0 - 1) {
#pragma unroll
      for (int i = SK0; i < SK1; ++i) { vr[i] = u32x4{0u, 0u, 0u, 0u}; PIN(vr[i]); }
    }
    if constexpr (v == VQ - 4) { qg[0] = ld16<AUX_SC1>(wsr, qoff, 0); qg[1] = ld16<AUX_SC1>(wsr, qoff + 16, 0); }
    if constexpr (v == VSIDE) side_loads();
    if constexpr (v == CPF) {
      u64 pc; u32 dlt, len;
      asm volatile("s_getpc_b64 %0\n.Lcpf_here:\n\ts_mov_b32 %1, .Ltail_start-.Lcpf_here\n\ts_mov_b32 %2, .Lend_a-.Ltail_start\n\ts_max_i32 %2, %2, .Lend_b-.Ltail_start\n\ts_max_i32 %2, %2, .Lend_c-.Ltail_start\n\ts_max_i32 %2, %2, .Lend_d-.Ltail_start\n\ts_max_i32 %2, %2, .Lend_e-.Ltail_start" : "=s"(pc), "=s"(dlt), "=s"(len) :: "scc");
      pc += dlt;
      const rsrc_t rc = mk_rsrc((const void*)pc, (part == 3 && i8 == 1) ? len : 0u);
      cpf = __builtin_amdgcn_raw_buffer_load_b32(rc, tid * 64, 0, 0);
    }
    if constexpr (v == SK1 + 2) PIN(cpf);
    if constexpr (v == RVP) report(G_VT + 1024);
    if constexpr (v == RVL) tbg = ld8<AUX_SC1>(wsr, wave == 7 ? G_VT + (g ? 0 : 1024) + xcd * 128 + c * 8 : OOB, 0);
    if constexpr (v == SK1) mlg = ld16<AUX_SC1>(wsr, wave == 0 ? mlo : OOB, 0);
    SB();
    if constexpr (NL + v < 72) limit(IC<NL + v>{});
    if constexpr (v >= SK0 + WV) { WAIT_VM(v < SK1 ? WV : WV + 1); SB(); }
    if constexpr (v == VQ) {
      if (!all_ok((qg[0][1] == tag) & (qg[0][3] == tag) & (qg[1][1] == tag) & (qg[1][3] == tag))) { STAMP(20); gather16<2>(wsr, qoff, 16, tag, qg); }
      STAMP(1); WSTAMP(1);
      qq[0] = qg[0][0]; qq[1] = qg[0][2]; qq[2] = qg[1][0]; qq[3] = qg[1][2];
    }
    if constexpr (v >= VQ && v < VS) {
      constexpr int sl = v - VQ, bt = sl >> 1;
      float xs[8];
#pragma unroll
      for (int i = 0; i < 8; ++i) {
        float d = 0.f;
        if (sl * 8 + i < NL) {
#pragma unroll
          for (int k = 0; k < 4; ++k) d = dot2(kr[sl * 8 + i][k], qq[k], d);
        }
        xs[i] = d;
      }
#pragma unroll
      for (int h = 0; h < 4; ++h) xs[h] = sel_add<XOR7>(xs[h], xs[h + 4], c & 4);
#pragma unroll
      for (int h = 0; h < 2; ++h) xs[h] = sel_add<XOR2>(xs[h], xs[h + 2], c & 2);
      xs[0] = sel_add<XOR1>(xs[0], xs[1], c & 1);
      if constexpr ((sl & 1) || sl == NSL - 1) {
        const float t = (sl & 1) ? sel_add<XOR8>(xlo, xs[0], c & 8) : xs[0] + dpp<XOR8>(xs[0]);
        const float s = rbf(rbf(t) * 0.08838834764831845f);
        sc[bt] = (bt * 16 + c < SK0 || (bt * 16 + c >= first && bt * 16 + c < NL)) ? s : -__builtin_inff();
        PIN(sc[bt]);
      } else {
        xlo = xs[0];
        PIN(xlo);
      }
    }
    if constexpr (v == VS) {
      float mloc = sc[0];
#pragma unroll
      for (int bt = 1; bt < NB; ++bt) mloc = fmaxf(mloc, sc[bt]);
      m = fmaxf(wave_max(mloc), -3.0e38f);
      float ls = 0.f;
#pragma unroll
      for (int bt = 0; bt < NB; ++bt) { pr[bt] = __expf(sc[bt] - m); ls += pr[bt]; }
      const float l = wave_sum(ls);
      if (lane == 0) { sm.ml[wave][0] = m; sm.ml[wave][1] = l; }
      if (c == ((NL - 1) & 15)) sm.pl[g][wave] = pr[NB - 1];
      STAMP(2); WSTAMP(2);
    }
    if constexpr (v >= SK1) pv_row(IC<v - SK1 + SK0 - WV>{});
    if constexpr (v < SK0 || v >= SK0 + WV) sfor<cons_end(v - 1), cons_end(v)>(pv_row);
    if constexpr (v == RVU) {
      if (wave == 7) {
        if (__builtin_expect(!all_ok(tbg[1] == tag || g == 0), 0)) {
          u32 vg[1];
          gather8<1>(wsr, G_VT + xcd * 128 + c * 8, 0, tag, vg);
          if (g) tbg = u32x2{vg[0], tag};
        }
        const bool ok = tbg[1] == tag;
        float lg = ok ? (float)(g ? (int)tbg[0] >> 16 : (int)(tbg[0] & 0xffffu)) : 0.f, nf = ok ? 8.f * (RVP - RVS + 1) : 0.f;
        lg += dpp<XOR1>(lg); nf += dpp<XOR1>(nf); lg += dpp<XOR2>(lg); nf += dpp<XOR2>(nf); lg += dpp<XOR7>(lg); nf += dpp<XOR7>(nf); lg += dpp<XOR8>(lg); nf += dpp<XOR8>(nf);
        lg *= __builtin_amdgcn_rcpf(fmaxf(nf, 1.f));
        if (lane == 16) sm.bc4 = __float_as_uint(lg);
        if (lane == 0) sm.bc3 = (u32)min(max((int)((lg - (float)P.F0) * (float)(P.FG * 32)), 0), 200 * 256);
      }
      LDS_BARRIER();
      const int st = __builtin_amdgcn_readfirstlane(sm.bc3), sp = (st * P.FF_) >> 3;
      near = ((__builtin_amdgcn_readfirstlane(sm.bc4) >> 31 ^ die ^ P.NR ^ 1) & 1) * 8192u;
      t1 += st;
      due += sp;
      tau += __builtin_amdgcn_readfirstlane((int)((float)(st - sp) * __builtin_amdgcn_rcpf((float)(nl - RVU - 3))));
    }
    if constexpr (v == SK1 + 1) {
      float p[8];
#pragma unroll
      for (int w = 0; w < 8; ++w) p[w] = sm.pl[g][w] * __expf(sm.ml[w][0] - m);
#pragma unroll
      for (int w = 0; w < 8; ++w) pl8[w] = pack_bf(p[w], p[w]);
#pragma unroll
      for (int w = 0; w < 8; ++w) PIN(pl8[w]);
    }
    if constexpr (v == MLS - 1) { side_pin(); if (is_up) up_prep(); PIN(ga); PIN(xin); PIN(cc1); }
    if constexpr (v == UPREP - 1) wo_addr();
    if constexpr (v == UPREP) {
#pragma unroll
      for (int k = 0; k < 4; ++k) asm volatile("" : "=v"(bw[k]));
      if (part == 0) wo_frags(0); else if (is_up) up_frags();
#pragma unroll
      for (int k = 0; k < 4; ++k) PIN(bw[k]);
    }
    if constexpr (v == UPREP + 1) {
      if (is_up) {
        const u32x4* a1 = reinterpret_cast<const u32x4*>(&sm.hb[1024 + (c >> 2 & 1) * 1024 + (c >> 3) * 512 + (g >> 1) * 256 + (c >> 1 & 1) * 32 + (g & 1) * 16 + (c & 1) * 8]);
        f32x4 d = {0.f, 0.f, 0.f, 0.f};
#pragma unroll
        for (int j = 0; j < 4; ++j) d = mfma64(a1[j * 8], bw[j], d, (c & 1) ? 0xeeeeu : 0x4444u);
        float t = par ? d[2] + d[3] : d[0] + d[1];
        t += dpp<XOR8>(t);
        const auto ab = __builtin_amdgcn_permlane16_swap(__float_as_uint(t), __float_as_uint(t), false, false);
        Af = u2f(ab[0]); Bf = u2f(ab[1]);
      }
      PIN(Af); PIN(Bf);
    }
    if (v == 10 || v == 16) STAMP(15 + v / 6);
    if (v == 20) STAMP(18);
    if (v == 32) STAMP(19);
    if constexpr (v == MLS) {
      LDS_BARRIER();
      float mm[8], ll[8];
#pragma unroll
      for (int w = 0; w < 8; ++w) { const f32x2 t = *reinterpret_cast<const f32x2*>(&sm.ml[w][0]); mm[w] = t[0]; ll[w] = t[1]; }
      float Mwg = mm[0], Lwg = 0.f;
#pragma unroll
      for (int w = 1; w < 8; ++w) Mwg = fmaxf(Mwg, mm[w]);
      if (wave == 0) {
#pragma unroll
        for (int w = 0; w < 8; ++w) Lwg = fmaf(__expf(mm[w] - Mwg), ll[w], Lwg);
        if (lane == 0) st16_sc1(wsr, G_ML + ((head * 2 + b) * 4 + part) * 16, __float_as_uint(Mwg), __float_as_uint(Lwg), tag);
      }
      const float fw = __expf(m - Mwg);
      m = Mwg;
#pragma unroll
      for (int bt = 0; bt < NB; ++bt) { pb[bt] = pack_bf(pr[bt] * fw, pr[bt] * fw); PIN(pb[bt]); }
#pragma unroll
      for (int k = 0; k < 4; ++k) { pm[k] = (c >> 1 & 3) == k ? ((c & 1) ? 0xffff0000u : 0x0000ffffu) : 0u; PIN(pm[k]); }
    }
    SB();
    return true;
  });
  asm volatile(".Ltail_start:");
  if (wave != 0) {
    pv_row(IC<SK0 - 1>{});
    WAIT_VM(WV); SB();
    sfor<NL - 1 - WV, NL - 1>(pv_row);
    *LDSP(f32x4, adst0) = acc;
    WSTAMP(14);
    LDS_BARRIER();
  } else {
    u32x4 xr[8], am[8];
#pragma unroll
    for (int w = 0; w < 8; ++w) am[w] = u32x4{pl8[w] & pm[0], pl8[w] & pm[1], pl8[w] & pm[2], pl8[w] & pm[3]};
#pragma unroll
    for (int w = 0; w < 8; ++w) PIN(am[w]);
    {
      const u32 so = __builtin_amdgcn_readlane(sot, NL - 1);
      gate();
      SB();
#pragma unroll
      for (int w = 0; w < 8; ++w) xr[w] = ld16<AUX_NT>(rv, lane * 16, so ^ ((w & 1) ? col(P, 7) : 0u) ^ ((w & 2) ? col(P, 8) : 0u) ^ ((w & 4) ? col(P, 9) : 0u));
      SB();
    }
    u32 rsum = lds_addr(&sm.red[0][c * 8 + (g & 1) * 4]);
    pv_row(IC<SK0 - 1>{});
    WAIT_VM(WV + 8); SB();
    sfor<NL - 1 - WV, NL - 3>(pv_row);
    if (__builtin_expect(!all_ok((mlg[1] == tag) & (mlg[3] == tag)), 0)) {
#pragma nounroll
      for (int spin = 0; spin < SPIN_MAX; ++spin) {
        CFENCE();
        mlg = ld16<AUX_SC1>(wsr, mlo, 0);
        if (all_ok((mlg[1] == tag) & (mlg[3] == tag))) break;
      }
    }
    merge_factor(mlg);
    sfor<NL - 3, NL - 1>(pv_row);
    STAMP(14); WSTAMP(14);
    LDS_BARRIER();
    STAMP(15);
    f32x4 t[8];
#pragma unroll
    for (int w = 1; w < 8; ++w) t[w] = LDSP(f32x4, rsum)[w * 32];
    acc = ((acc + t[1]) + (t[2] + t[3])) + ((t[4] + t[5]) + (t[6] + t[7]));
    PIN(acc);
    STAMP(3);
    SB();
#pragma unroll
    for (int w = 0; w < 8; ++w) acc = mfma16(am[w], xr[w], acc);
  }
  WAIT_VM(0);
  acc *= fmrg;
  {
    const u32 w0 = min(pack_bf(acc[0], acc[1]), SENT - 1u), w1 = min(pack_bf(acc[2], acc[3]), SENT - 1u);
    const auto e0 = __builtin_amdgcn_permlane16_swap(w0, w0, false, false), e1 = __builtin_amdgcn_permlane16_swap(w1, w1, false, false);
    __builtin_amdgcn_raw_buffer_store_b128(u32x4{e0[0], e1[0], e0[1], e1[1]}, wsr, pso, 0, AUX_SC1);
  }
  st8_sc1(wsr, nto, (u32)wall_clock64() + nadd, tag);
  SB();
  STAMP(4);

  if (part == 2) FINISH();
  if (part == 0) {
    __builtin_amdgcn_s_barrier();
#pragma nounroll
    while (wleft > 0) { until(t1 - (P.PA + wleft * P.PD) * 256); piece(); }
    bool late = false;
    u32x4 og[1];
    f32x4 d[4];
    u32 six = (c & 8) ? 0xeeeeu : 0x4444u;
    PIN(six);
#pragma unroll
    for (int t = 0; t < 4; ++t) { d[t] = f32x4{0.f, 0.f, 0.f, 0.f}; PIN(d[t]); }
    if (wdef) {
      late = true;
      while ((int)(((u32)wall_clock64() - t0) << 8) < t1 - 10 * 256) if (__builtin_amdgcn_s_getreg(7 | (3 << 11)) == 0) { late = false; break; }
      if (__builtin_expect(late, 0)) { until(t1); CFENCE(); og[0] = ld16<AUX_SC1>(wsr, po, 0); SB(); WAIT_VM(1); } else { WAIT_VM(0); }
      CFENCE();
      wo_frags(1);
#pragma unroll
      for (int k = 0; k < 4; ++k) PIN(bw[k]);
      SB();
    }
    if (__builtin_expect(!late, 1)) { until(t1); CFENCE(); og[0] = ld16<AUX_SC1>(wsr, po, 0); }
    STAMP(11);
    SB();
    auto wo_mm = [&]() {
#pragma unroll
      for (int t = 0; t < 4; ++t) d[t] = mfma64(og[0], bw[t], d[t], six);
    };
    auto p_bad = [&]() { return !all_ok(max(max(og[0][0], og[0][1]), max(og[0][2], og[0][3])) != SENT); };
    wo_mm();
    if (__builtin_expect(p_bad(), 0)) {
#pragma nounroll
      for (int spin = 0; spin < SPIN_MAX; ++spin) {
        __builtin_amdgcn_s_sleep(1);
        CFENCE();
        og[0] = ld16<AUX_SC1>(wsr, po, 0);
        if (!p_bad()) break;
      }
#pragma unroll
      for (int t = 0; t < 4; ++t) d[t] = f32x4{0.f, 0.f, 0.f, 0.f};
      wo_mm();
      SB();
      asm volatile(".Lend_d:");
    }
    STAMP(5); WSTAMP(5);
    {
      SB();
      f32x4 y; f32x2 h[4];
#pragma unroll
      for (int t = 0; t < 4; ++t) { h[t] = d[t].xy + d[t].zw; PIN(h[t]); }
#pragma unroll
      for (int t = 0; t < 4; ++t) { y[t] = h[t][0] + h[t][1]; PIN(y[t]); }
      *LDSP(f32x2, rdst) = f32x2{swap32_add(y[0], y[1]), swap32_add(y[2], y[3])};
    }
    STAMP(16);
    LDS_BARRIER();
    STAMP(17);
    if (wave < 2) {
      f32x2 v[4];
#pragma unroll
      for (int w = 0; w < 4; ++w) v[w] = f32x2{LDSP(float, rsrc)[w * 256], LDSP(float, rsrc)[w * 256 + 128]};
      const f32x2 h = (v[0] + v[1]) + (v[2] + v[3]);
      const float y = h[0] + h[1];
      __builtin_amdgcn_raw_buffer_store_b32(min(pack_bf(dpp<0xA0>(y), dpp<0xF5>(y)), SENT - 1u), wsr, xo, 0, AUX_SC1);
      asm volatile(".Lend_a:");
    }
    STAMP(6);
    FINISH();
  }
  if (part == 3) {
    if (!has_w) FINISH();
    u32x4 b2[4];
    {
      __builtin_amdgcn_s_barrier();
      const rsrc_t r2 = mk_rsrc(w2 + (size_t)(blk * 32 + wave * 4) * 512, 4096u);
      const u32 l2 = (u32)(uintptr_t)(&sm.w2b[wave * 4][0]);
#pragma unroll
      for (int n = 0; n < 4; ++n) { until(t1 - (P.QA + (4 - n) * P.QD) * 256); dma16(r2, (lane ^ (n * 2) ^ ((wave & 2) << 2)) * 16, n * 1024, l2 + n * 1024); }
    }
    const u32 tdst = lds_addr(&sm.hb[hpos]);
    const u32 sdst = lds_addr(&sm.stat[wave][lane]);
    const u32 ssrc = lds_addr(&sm.stat[(g >> 1) * 4 + ((lane >> 3) & 3)][(lane & 7) * 4]);
    const u32 adst = lds_addr((c < 4 && (g & 1) == 0) ? &sm.actl[g >> 1][wave * 4 + c] : reinterpret_cast<u16*>(&sm.dummy[lane]));
    const u32 asrc = lds_addr(&sm.actl[lane & 1][g * 8]);
    const u32 apa = lds_addr(&sm.hb[(c >> 3) * 512 + (g >> 1) * 256 + (c >> 1 & 1) * 64 + ((c >> 2) & 1) * 32 + (g & 1) * 16 + (c & 1) * 8]);
    u32 six = (c & 1) ? 0xeeeeu : 0x4444u;
    f32x4 d[2] = {{0.f, 0.f, 0.f, 0.f}, {0.f, 0.f, 0.f, 0.f}};
    PIN(six); PIN(d[0]); PIN(d[1]);
    constexpr u32 xs2 = 16384;
    u32 xoff = dup(G_XP) + near + g * 2048 + ub * 1024 + (wave & 3) * 256 + c * 16;
    u32x4 one;
#pragma unroll
    for (int k = 0; k < 4; ++k) one[k] = ((c & 2) == 0 && (c >> 2) == k) ? ((c & 1) ? 0x3f800000u : 0x00003f80u) : 0u;
    PIN(one);
    u32 ooff = dup(G_OP + (blk * 512 + tid) * 8);
    float Afp = Af, Bfp = Bf;
    PIN(xoff); PIN(ooff); PIN(Afp); PIN(Bfp);
    f32x2 gc = ga * cc1, xc = xin * cc1;
    PIN(ga); PIN(xin); PIN(gc); PIN(xc);
    u32 w2a = (u32)(uintptr_t)(&sm.w2b[0][0]) + (g * 8 + ((lane >> 2) & 3)) * 1024 + (wave ^ (g & 1)) * 128 + (lane & 15) * 8;
    PIN(w2a);
    if (wave == 1) {
      due = t1 + P.N1 * 256;
      const int tn = notices();
      due = tn + P.X2 * 256;
      until(due);
    }
    asm volatile("s_waitcnt vmcnt(0)" ::: "memory");
    STAMP(9);
    __builtin_amdgcn_s_barrier();
    STAMP(11);
    CFENCE();
    u32x4 xg[2] = {ld16<AUX_SC1>(wsr, xoff, 0), ld16<AUX_SC1>(wsr, xoff, xs2)};
    SB();
#pragma unroll
    for (int t = 0; t < 4; ++t) {
      const u32x2 lo = tr_read(w2a ^ (t * 32)), hi = tr_read((w2a ^ (t * 32)) + 4096);
      b2[t] = u32x4{lo[0], lo[1], hi[0], hi[1]};
    }
#pragma unroll
    for (int t = 0; t < 4; ++t) PIN(b2[t]);
    SB();
    const f32x4 z4 = {0.f, 0.f, 0.f, 0.f};
    auto xp_bad = [&]() { return !all_ok(max(max(max(xg[0][0], xg[0][1]), max(xg[0][2], xg[0][3])), max(max(xg[1][0], xg[1][1]), max(xg[1][2], xg[1][3]))) != SENT); };
    f32x4 v = mfma16(one, xg[1], mfma16(one, xg[0], z4));
    if (__builtin_expect(xp_bad(), 0)) {
#pragma nounroll
      for (int spin = 0; spin < SPIN_MAX; ++spin) {
        __builtin_amdgcn_s_sleep(1);
        CFENCE();
        xg[0] = ld16<AUX_SC1>(wsr, xoff, 0); xg[1] = ld16<AUX_SC1>(wsr, xoff, xs2);
        if (!xp_bad()) break;
      }
      v = mfma16(one, xg[1], mfma16(one, xg[0], z4));
      SB();
      asm volatile(".Lend_e:");
    }
    STAMP(5); WSTAMP(5);
    {
      const f32x2 a = v.xy, hc = __builtin_elementwise_fma(gc, a, xc), x1 = __builtin_elementwise_fma(ga, a, xin);
      *LDSP(u32, tdst) = pack_bf(hc[0], hc[1]);
      *LDSP(float, sdst) = swap32_add(x1[0] + x1[1], fmaf(x1[0], x1[0], x1[1] * x1[1]));
    }
    STAMP(16);
    LDS_BARRIER();
    STAMP(17);
    {
      const f32x4 s4 = LDSP(f32x4, ssrc)[0], q4 = LDSP(f32x4, ssrc)[8];
      SB();
      const u32x4 af0 = LDSP(u32x4, apa)[0], af1 = LDSP(u32x4, apa)[16];
      SB();
      const f32x2 s2 = s4.xy + s4.zw, q2 = q4.xy + q4.zw;
      float s = s2[0] + s2[1], q = q2[0] + q2[1];
      SB();
      d[0] = mfma64(af0, bw[0], d[0], six);
      SB();
      s = swap16_add(s, q);
      SB();
      d[1] = mfma64(af0, bw[1], d[1], six);
      SB();
      s += dpp<XOR1>(s); s += dpp<XOR2>(s);
      SB();
      d[0] = mfma64(af1, bw[2], d[0], six);
      SB();
      s += dpp<XOR7>(s); s += dpp<XOR8>(s);
      SB();
      d[1] = mfma64(af1, bw[3], d[1], six);
      SB();
      const auto sq = __builtin_amdgcn_permlane16_swap(__float_as_uint(s), __float_as_uint(s), false, false);
      const float mean = u2f(sq[0]) * (1.f / 512.f);
      const float var = fmaf(u2f(sq[1]), 1.f / 512.f, -mean * mean);
      float ri = __builtin_amdgcn_rsqf(fmaxf(var, 0.f) + 1e-5f);
      PIN(ri); PIN(d[0]); PIN(d[1]);
      SB();
      float y0 = d[0][0] + d[0][1], y1 = d[1][2] + d[1][3], mr = -ri * mean;
      PIN(y0); PIN(y1); PIN(mr);
      float y = y0 + y1, cf = fmaf(mr, Afp, Bfp), rk = ri * -1.44269504f;
      PIN(y); PIN(cf); PIN(rk);
      y = __builtin_bit_cast(float, __builtin_amdgcn_update_dpp(__builtin_bit_cast(int, y), __builtin_bit_cast(int, y), XOR8, 0xa, 0xf, false));
      const float ck = cf * -1.44269504f;
      y = swap16_add(y, y);
      const float ex = __builtin_amdgcn_exp2f(fmaf(y, rk, ck));
      y = fmaf(y, ri, cf);
      const float gu = y * dpp<0x104>(y);
      *LDSP(u16, adst) = (u16)pack_bf(gu * __builtin_amdgcn_rcpf(1.f + ex), 0.f);
    }
    STAMP(18);
    LDS_BARRIER();
    STAMP(10);
    {
      const u32x4 af = *LDSP(u32x4, asrc);
      const f32x4 z = {0.f, 0.f, 0.f, 0.f};
      f32x4 d0 = mfma16(af, b2[0], z), d1 = mfma16(af, b2[1], z), d2 = mfma16(af, b2[2], z), d3 = mfma16(af, b2[3], z);
      asm volatile("" : "+v"(d0), "+v"(d1), "+v"(d2), "+v"(d3));
      const bool g1 = g & 1, g2 = g & 2;
      const float e0 = g1 ? d1[0] : d0[0], e1 = g1 ? d1[1] : d0[1], f0 = g1 ? d3[0] : d2[0], f1 = g1 ? d3[1] : d2[1];
      const u32x2 ov = {pack_bf(g2 ? f0 : e0, g2 ? f1 : e1), tag};
      __builtin_amdgcn_raw_buffer_store_b64(ov, wsr, ooff, near ^ 8192u, AUX_SC1);
      __builtin_amdgcn_raw_buffer_store_b64(ov, wsr, ooff, near, AUX_SC1);
    }
    asm volatile(".Lend_b:");
    STAMP(6);
    FINISH();
  }
  if (wave == 0) {
    if (!all_ok((sg[0][1] == tag) & (sg[0][3] == tag) & (sg[1][1] == tag) & (sg[1][3] == tag))) {
      u32x4 t[2];
      gather16<2>(wsr, G_MOD + (1024 + fd) * 8, 1536 * 8, tag, t);
      sg[0] = t[0]; sg[1] = t[1];
    }
    u32 xa;
    {
      due = t1 + P.N2 * 256;
      const int tn = notices();
      due = tn + P.X3 * 256;
      until(due);
    }
    const bool hb = g & 1;
#pragma nounroll
    for (int spin = 0; spin < SPIN_MAX; ++spin) {
      CFENCE();
      xa = __builtin_amdgcn_raw_buffer_load_b32(wsr, dup(G_XP) + near + (g >> 1) * 16384 + (c >> 2) * 2048 + (g & 1) * 1024 + fd * 2, 0, AUX_SC1);
      if (all_ok(xa != SENT)) break;
      __builtin_amdgcn_s_sleep(1);
    }
    float a0 = lo_f(xa), a1 = hi_f(xa);
    a0 += dpp<0x124>(a0); a1 += dpp<0x124>(a1);
    a0 += dpp<XOR8>(a0); a1 += dpp<XOR8>(a1);
    a0 = swap32_add(a0, a0); a1 = swap32_add(a1, a1);
    f32x2 xv = {rbf(lo_f(sx2) + rbf((hb ? hi_f(sg[0][0]) : lo_f(sg[0][0])) * rbf(a0))), rbf(hi_f(sx2) + rbf((hb ? hi_f(sg[0][2]) : lo_f(sg[0][2])) * rbf(a1)))};
    f32x2 gv = {hb ? hi_f(sg[1][0]) : lo_f(sg[1][0]), hb ? hi_f(sg[1][2]) : lo_f(sg[1][2])};
    PIN(xv); PIN(gv);
    if (wi == 0 && lane == 0) __builtin_amdgcn_raw_buffer_store_b32(tag, wsr, WS_EPOCH, 0, AUX_SC1);
    STAMP(10);
    due = max(due + P.X4 * 256, (int)(((u32)wall_clock64() - t0) << 8) + P.GX * 256);
    const bool last = lane < (NUP - 32) * 4;
    u32 o0 = dup(G_OP + (lane >> 2) * 4096 + fd * 8) + near, o2 = last ? o0 : OOB, tg2 = last ? tag : 0u;
    const rsrc_t rout = mk_rsrc(out, 2048u);
    u32 oo = (lane & 44) == 0 ? (g * 512 + fd) * 2 : OOB;
    const u32 pk = (c & 10) ? 0u : ((c & 4) ? 0x3f800000u : 0x00003f80u);
    u32x4 one = {(c & 1) ? 0u : pk, 0u, (c & 1) ? pk : 0u, 0u};
    PIN(o0); PIN(o2); PIN(tg2); PIN(oo); PIN(one);
    __builtin_amdgcn_raw_buffer_store_b32(pack_bf(xv[0], xv[1]), rout, oo, 0, AUX_SC1);
    u32 s1_ = 131072, s2_ = 262144;
    asm volatile("" : "+s"(s1_), "+s"(s2_));
    until(due);
    f32x4 sv;
#pragma nounroll
    for (int spin = 0; spin < SPIN_MAX; ++spin) {
      CFENCE();
      const u32x4 og[3] = {ld16<AUX_SC1>(wsr, o0, 0), ld16<AUX_SC1>(wsr, o0, s1_), ld16<AUX_SC1>(wsr, o2, s2_)};
      sv = f32x4{0.f, 0.f, 0.f, 0.f};
#pragma unroll
      for (int k = 0; k < 3; ++k) sv = mfma16(one, og[k], sv);
      SB();
      u32 bad = ((og[0][1] ^ tag) | (og[0][3] ^ tag) | (og[1][1] ^ tag)) | ((og[1][3] ^ tag) | (og[2][1] ^ tg2) | (og[2][3] ^ tg2));
      PIN(bad);
      SB();
      float s0 = sv[0] + dpp<0x124>(sv[0]), s1 = sv[1] + dpp<0x124>(sv[1]);
      PIN(s0); PIN(s1);
      s0 += dpp<XOR8>(s0); s1 += dpp<XOR8>(s1);
      PIN(s0); PIN(s1);
      __builtin_amdgcn_raw_buffer_store_b32(pack_bf(fmaf(gv[0], s0, xv[0]), fmaf(gv[1], s1, xv[1])), rout, oo, 0, AUX_SC1);
      SB();
      if (all_ok(bad == 0u)) break;
      __builtin_amdgcn_s_sleep(1);
    }
    STAMP(7);
    asm volatile(".Lend_c:");
  }
  FINISH();
}

extern "C" void kernel_launch(void* const* d_in, const int* in_sizes, int n_in,
                              void* d_out, int out_size, void* d_ws, size_t ws_size,
                              hipStream_t stream) {
  fused_kernel<<<256, 512, 0, stream>>>((const u16*)d_in[1], (const u16*)d_in[2], (const u16*)d_in[4], (const u16*)d_in[3], (const u16*)d_in[10], (const u16*)d_in[6], (const u16*)d_in[7],
                                        (unsigned char*)d_ws, (const u16*)d_in[0], (const u16*)d_in[9], (const u16*)d_in[8], (u16*)d_out);
}
```

```cpp
#include <hip/hip_runtime.h>
#include <stdint.h>


typedef unsigned int u32;
typedef unsigned short u16;
typedef unsigned long long u64;
typedef __attribute__((ext_vector_type(4))) unsigned u32x4;
typedef __attribute__((ext_vector_type(2))) unsigned u32x2;
typedef __attribute__((ext_vector_type(2))) __bf16 bf16x2;
typedef __attribute__((ext_vector_type(4))) float f32x4;
typedef __attribute__((ext_vector_type(2))) float f32x2;
typedef __amdgpu_buffer_rsrc_t rsrc_t;

constexpr int NH = 32, SK = 16384, DH = 128, WIN = 4096, FF = 1376;
struct Prm { int E0, ES, E1, E2, E2C, EU, TEND, DT_UP, L0, L1, X1, X2, X3, X4, NE, FE, M0, M1, M2, M3, M4, GS, GX, N1, N2, GA, WD, DT_WO, LW, LU, EW, PM, NF, NR, C0, A0, AG, AC, TS, TW, TK, TG, GB, B0, BG, BF, D0, DG, TC, LM, C1, F0, FG, FF_, PA, PD, QA, QD, RT, SA, SG, SC, W[9]; };
constexpr Prm PRM0 = {32, 20, 32, 32, 60, 60, 1960, 76, 12, 27, 75, 63, 100, 75, 3, 8, 0x2511f55b, 0x2b7425d5, 0x6017665, 0x17e0b9ec, 0x31944305, 62, 30, 29, 62, 50, 4, 106, 50, 28, 10, 0x0, 12, 14, 3, 36, 4, 4, 509, 215, 80, -2, 6, 69, 10, 8, 8, 6, 87, 120, 2, 58, 9, 4, 52, 14, 9, 18, 12, 45, 10, 21, 0x77777777, 0x77777777, 0x77777777, 0x77777777, 0x77777777, 0x44444477, 0x44444444, 0x44444444, 0x44444444};
constexpr int NL = 37;
constexpr int SK0 = 21, SK1 = 32;
constexpr int NCMIN = (NL - SK1 + SK0) * 8;
constexpr int WK = 8, WV = 5;
constexpr int WUSE = 12, LNS = 7;
constexpr int QP = 14, QD = 4;
constexpr int VQ = 6;
constexpr int NSL = (NL + 7) / 8;
constexpr int VS = VQ + NSL;
constexpr int MLS = VS + 2;
constexpr int VSIDE = MLS - 5, UPREP = MLS + 6;
constexpr int CPF = 17;
constexpr int SMO = 1;
constexpr int RVS = 5, RVP = 8, RVL = 14, RVU = 18;
constexpr int RPS = 23, RPP = 27;
constexpr int PRB = 12;
constexpr int TBS = 7, TBP = 10, TBL = 13, TBU = 17;
constexpr int window(int s, int w) {
  int n = w;
  for (int u = s - w + 1; u <= s; ++u) n += u == SMO ? 6 : (u == TBL || u == SK1 || u == NL + CPF || u == NL + RVL || u == NL + SK1 ? 1 : (u == QP || u == NL + VQ - 4 ? 2 : (u == NL + VSIDE ? 4 : 0)));
  return n;
}
constexpr int cons_end(int v) {
  const int a = v - WV + 1, c = (v - MLS) * 4;
  const int m = a < c ? a : c;
  return m < 0 ? 0 : (m > NL ? NL : m);
}
constexpr u32 col(const Prm& P, int j) { const int m[6] = {P.M0, P.M1, P.M2, P.M3, P.M4, P.PM}; return (u32)(m[j / 3] >> (j % 3 * 10) & 1023) << 10; }
constexpr u32 first_col(const Prm& P, int k) {
  if (k < 4) return col(P, 12 + k) | 1u << (24 + k);
  if (k < 6) return col(P, k - 4);
  if (k < 8) return col(P, 4 + k) | 1u << (16 + k);
  if (k < 11) return col(P, k - 1);
  return (col(P, 2) ^ col(P, 3) ^ col(P, 4) ^ col(P, 5) ^ col(P, 6) ^ col(P, 10) ^ col(P, 11)) | 0xf00000u;
}
constexpr bool one_to_one(const Prm& P) {
  u32 c[10] = {};
  for (int j = 0; j < 10; ++j) c[j] = col(P, j);
  for (int j = 0; j < 10; ++j) {
    if (c[j] == 0) return false;
    const u32 low = c[j] & (0u - c[j]);
    for (int k = j + 1; k < 10; ++k) if (c[k] & low) c[k] ^= c[j];
  }
  return true;
}
static_assert(one_to_one(PRM0), "every KB of the window exactly once");
struct Tab { u32 e[512]; };
constexpr Tab make_tab(const Prm& P) {
  Tab T{};
  for (int bid = 0; bid < 256; ++bid) {
    const int xcd = bid & 7, part = bid >> 4 & 3, i8 = (bid >> 3 & 1) | bid >> 6 << 1, wi = i8 * 8 + xcd;
    const bool has_mod = wi < 48, has_up = wi >= 21;
    const int cb0 = (i8 + P.C0) & 7, cb1 = (i8 + P.C1) & 7, ms = wi - 20;
    const int mi = wi < 8 ? wi : (wi < 16 ? 8 + ((xcd + 4) & 7) : (wi < 20 ? (wi - 14) * 8 + 3 : 16 + ms / 7 * 8 + ms % 7 + (ms % 7 >= 3 ? 1 : 0)));
    const int ui = xcd * 5 + (xcd > 5 ? xcd - 5 : 0) + (i8 == 6 ? 0 : (xcd >= 5 ? 1 : 0) + (i8 == 7 ? 4 : i8 - 2));
    const bool edge = i8 >= 6;
    const int wd = cb0 == 3 ? 0 : P.WD;
    const int e[4] = {P.E0 - 4 * wd + (cb0 == 3 ? P.ES : 0), P.E1 + (cb1 == 3 ? P.ES : 0), has_mod ? (wi < 16 ? P.E2C : P.E2) + ((mi & 7) == 3 ? P.ES : 0) : 0, has_up ? P.EU : 0};
    int tw[4] = {}, win[4] = {}, span[4] = {}, k0[4] = {};
    for (int k = 0; k < 4; ++k) {
      const int np = k == 0 ? 8 - wd : 8;
      tw[k] = np ? e[k] * P.TW / (16 * np) : 0;
      k0[k] = P.NF + tw[k] * np / 2 > P.TK ? P.NF + tw[k] * np / 2 : P.TK;
      span[k] = P.TEND - k0[k] - P.TG - (k == 0 ? (wd ? P.DT_WO : P.A0) : (k == 3 && has_up ? P.DT_UP : 0));
      win[k] = span[k] - (k == 3 && has_up ? P.GA + (edge ? P.LM : 0) : 0);
    }
    int n[4] = {NCMIN / 8, NCMIN / 8, NCMIN / 8, NCMIN / 8};
    for (int left = 128 - NCMIN / 2; left > 0; --left) {
      int best = -1;
      for (int k = 3; k >= 0; --k)
        if (n[k] < NL && (best < 0 || n[k] * win[best] < n[best] * win[k])) best = k;
      ++n[best];
    }
    const int c[4] = {0, n[0] - 1, n[0] + n[1] - 2, n[0] + n[1] + n[2] - 3};
    const int nl = n[part];
    const bool has_w = part < 2 || (part == 2 ? has_mod : has_up);
    const int blk = !has_w ? 0 : (part == 0 ? cb0 : (part == 1 ? xcd * 8 + cb1 : (part == 2 ? mi : ui)));
    T.e[2 * bid] = (u32)(c[part] | nl << 7 | blk << 13 | (has_w ? 1 : 0) << 19 | (part == 0 ? wd : 0) << 20) | (u32)tw[part] << 24;
    T.e[2 * bid + 1] = (u32)(span[part] * P.TS / (4 * (nl - 1)) | span[part] * (1024 - P.TS) / (4 * (nl - 1)) << 16);
  }
  return T;
}
constexpr int step_range(const Prm& P, bool mx_) {
  int mx = 0, mn = 1 << 20;
  const Tab T = make_tab(P);
  for (int bid = 0; bid < 256; ++bid) { const int n = T.e[2 * bid] >> 7 & 63; mx = n > mx ? n : mx; mn = n < mn ? n : mn; }
  return mx_ ? mx : mn;
}
extern "C" __attribute__((used, visibility("protected"))) __constant__ const Tab wg_tab = make_tab(PRM0);
static_assert(step_range(PRM0, true) <= NL && step_range(PRM0, false) >= NCMIN / 8, "a wave's key range does not fit its load steps");
static_assert(RVU < UPREP && UPREP + 1 < SK0 && QP + QD + 1 < SK0 && cons_end(SK0 - 1) == SK0 - WV && NL - SK1 == WV, "steps every wave runs");
constexpr bool timers_long(const Prm& P) {
  const Tab T = make_tab(P);
  for (int bid = 48; bid < 64; ++bid) if (SK0 + NL - (int)(T.e[2 * bid] >> 7 & 63) > RPS) return false;
  return true;
}
static_assert(timers_long(PRM0), "steps of the second report");
constexpr int NUP = FF / 32;
constexpr int SPIN_MAX = 1 << 17;

constexpr u32 WS_EPOCH = 0;
constexpr u32 G_TB = 1024;
constexpr u32 G_MOD = 4096;
constexpr u32 G_Q = G_MOD + 3072 * 8;
constexpr u32 G_ML = G_Q + 4096 * 8;
constexpr u32 G_U = G_ML + 256 * 16;
constexpr u32 G_P = 0;
constexpr u32 G_XP = G_P + 128 * 256;
constexpr u32 G_OP = G_XP + 8 * 512 * 4;
constexpr u32 G_NT = G_OP + 43 * 512 * 8;
constexpr u32 G_PB = G_OP + 44 * 512 * 8;
constexpr u32 G_VT = G_U + 2 * (G_PB + 8192);
constexpr u32 WS_END = G_VT + 2048;
__device__ __forceinline__ u32 dup(u32 o) { return G_U + ((o >> 13) << 14) + (o & 8191u); }
constexpr u32 SENT = 0xffffffffu;
constexpr u32 OOB = 0x40000000u;

constexpr int AUX_NT = 2, AUX_SC1 = 16;

__device__ __forceinline__ float lo_f(u32 v) { return __uint_as_float(v << 16); }
__device__ __forceinline__ float hi_f(u32 v) { return __uint_as_float(v & 0xffff0000u); }
__device__ __forceinline__ float rbf(float x) { return (float)(__bf16)x; }
__device__ __forceinline__ u32 pack_bf(float lo, float hi) {
  bf16x2 r; r[0] = (__bf16)lo; r[1] = (__bf16)hi; return __builtin_bit_cast(u32, r);
}
__device__ __forceinline__ float dot2(u32 k, u32 q, float acc) {
  return __builtin_amdgcn_fdot2_f32_bf16(__builtin_bit_cast(bf16x2, k), __builtin_bit_cast(bf16x2, q), acc, false);
}
template <int CTRL> __device__ __forceinline__ float dpp(float x) {
  return __builtin_bit_cast(float, __builtin_amdgcn_mov_dpp(__builtin_bit_cast(int, x), CTRL, 0xf, 0xf, true));
}
constexpr int XOR1 = 0xB1, XOR2 = 0x4E, XOR7 = 0x141, XOR8 = 0x128;
__device__ __forceinline__ float swap32_add(float a, float b) {
  auto t = __builtin_amdgcn_permlane32_swap(__float_as_uint(a), __float_as_uint(b), false, false);
  return __uint_as_float(t[0]) + __uint_as_float(t[1]);
}
__device__ __forceinline__ float swap16_add(float a, float b) {
  auto t = __builtin_amdgcn_permlane16_swap(__float_as_uint(a), __float_as_uint(b), false, false);
  return __uint_as_float(t[0]) + __uint_as_float(t[1]);
}
__device__ __forceinline__ float wave_sum(float x) {
  x += dpp<XOR1>(x); x += dpp<XOR2>(x); x += dpp<XOR7>(x); x += dpp<XOR8>(x);
  x = swap16_add(x, x);
  return swap32_add(x, x);
}
__device__ __forceinline__ float wave_max(float x) {
  x = fmaxf(x, dpp<XOR1>(x)); x = fmaxf(x, dpp<XOR2>(x)); x = fmaxf(x, dpp<XOR7>(x)); x = fmaxf(x, dpp<XOR8>(x));
  auto s = __builtin_amdgcn_permlane16_swap(__float_as_uint(x), __float_as_uint(x), false, false);
  x = fmaxf(__uint_as_float(s[0]), __uint_as_float(s[1]));
  auto t = __builtin_amdgcn_permlane32_swap(__float_as_uint(x), __float_as_uint(x), false, false);
  return fmaxf(__uint_as_float(t[0]), __uint_as_float(t[1]));
}
template <int CTRL> __device__ __forceinline__ float sel_add(float lo, float hi, bool up) {
  const float t1 = lo + dpp<CTRL>(lo), t2 = hi + dpp<CTRL>(hi);
  return up ? t2 : t1;
}
template <int I> __device__ __forceinline__ float bc16(float x) {
  return __builtin_bit_cast(float, __builtin_amdgcn_update_dpp(0, __builtin_bit_cast(int, x), 0x150 + I, 0xf, 0xf, false));
}
__device__ __forceinline__ float bcast16(float x, int i) {
  switch (i) {
    case 0: return bc16<0>(x); case 1: return bc16<1>(x); case 2: return bc16<2>(x); case 3: return bc16<3>(x);
    case 4: return bc16<4>(x); case 5: return bc16<5>(x); case 6: return bc16<6>(x); case 7: return bc16<7>(x);
    case 8: return bc16<8>(x); case 9: return bc16<9>(x); case 10: return bc16<10>(x); case 11: return bc16<11>(x);
    case 12: return bc16<12>(x); case 13: return bc16<13>(x); case 14: return bc16<14>(x); default: return bc16<15>(x);
  }
}
__device__ __forceinline__ float silu_f(float x) { return x * __builtin_amdgcn_rcpf(1.f + __expf(-x)); }

template <int AUX> __device__ __forceinline__ u32x4 ld16(rsrc_t r, u32 voff, u32 soff) {
  return __builtin_amdgcn_raw_buffer_load_b128(r, voff, soff, AUX);
}
template <int AUX> __device__ __forceinline__ u32x2 ld8(rsrc_t r, u32 voff, u32 soff) {
  return __builtin_amdgcn_raw_buffer_load_b64(r, voff, soff, AUX);
}
__device__ __forceinline__ void st8_sc1(rsrc_t r, u32 voff, u32 v, u32 tag) {
  __builtin_amdgcn_raw_buffer_store_b64(u32x2{v, tag}, r, voff, 0, AUX_SC1);
}
__device__ __forceinline__ void st16_sc1(rsrc_t r, u32 voff, u32 v0, u32 v1, u32 tag) {
  __builtin_amdgcn_raw_buffer_store_b128(u32x4{v0, tag, v1, tag}, r, voff, 0, AUX_SC1);
}
__device__ __forceinline__ rsrc_t mk_rsrc(const void* p, u32 bytes) {
  return __builtin_amdgcn_make_buffer_rsrc(const_cast<void*>(p), 0, bytes, 0x00020000);
}
#define WAIT_VM(n) __builtin_amdgcn_s_waitcnt(((n) & 15) | (((n) >> 4) << 14) | (7 << 4) | (15 << 8))
#define SB() __builtin_amdgcn_sched_barrier(0)
#define CFENCE() asm volatile("" ::: "memory")
__device__ __forceinline__ bool all_ok(bool ok) { return __builtin_amdgcn_ballot_w64(!ok) == 0ull; }

template <int N> __device__ __forceinline__ void gather16(rsrc_t ws, u32 voff, u32 stride, u32 tag, u32x4 (&g)[N]) {
#pragma nounroll
  for (int spin = 0; spin < SPIN_MAX; ++spin) {
    CFENCE();
    bool ok = true;
#pragma unroll
    for (int i = 0; i < N; ++i) g[i] = ld16<AUX_SC1>(ws, voff + i * stride, 0);
#pragma unroll
    for (int i = 0; i < N; ++i) ok &= (g[i][1] == tag) & (g[i][3] == tag);
    if (all_ok(ok)) break;
    __builtin_amdgcn_s_sleep(8);
  }
}
template <int N> __device__ __forceinline__ void gather8(rsrc_t ws, u32 voff, u32 stride, u32 tag, u32 (&g)[N], bool act = true) {
#pragma nounroll
  for (int spin = 0; spin < SPIN_MAX; ++spin) {
    CFENCE();
    bool ok = true;
    u32x2 t[N];
#pragma unroll
    for (int i = 0; i < N; ++i) t[i] = ld8<AUX_SC1>(ws, voff + i * stride, 0);
#pragma unroll
    for (int i = 0; i < N; ++i) { ok &= (t[i][1] == tag); g[i] = t[i][0]; }
    if (all_ok(ok | !act)) break;
    __builtin_amdgcn_s_sleep(8);
  }
}

struct Smem {
  float red[8][128];
  float ml[8][2];
  float r2[64 * 20];
  u16 hb[3 * 1024];
  float stat[8][64];
  float red2[8][128];
  u32 dummy[64];
  u32x2 bc, bc2;
  u32 bc3, bc4;
  u32x4 zero[4];
  float pl[4][8];
  u32 mflag[8];
  u32x4 wbuf[8][8][64];
  __attribute__((aligned(256))) u16 w2b[32][512];
  u16 actl[2][32];
};

#define PIN(x) asm volatile("" : "+v"(x))
#define LDS_BARRIER() do { __builtin_amdgcn_s_waitcnt(0xc07f); __builtin_amdgcn_s_barrier(); CFENCE(); } while (0)
#define WAVE_SYNC() do { __builtin_amdgcn_fence(__ATOMIC_RELEASE, "wavefront"); __builtin_amdgcn_wave_barrier(); __builtin_amdgcn_fence(__ATOMIC_ACQUIRE, "wavefront"); } while (0)

__device__ __forceinline__ void dma16(rsrc_t r, u32 voff, u32 soff, u32 lds_dst) {
  u32 keep;
  asm volatile("s_mov_b32 %0, m0\n\ts_mov_b32 m0, %4\n\ts_nop 4\n\tbuffer_load_dwordx4 %1, %2, %3 offen nt lds\n\ts_mov_b32 m0, %0"
               : "=&s"(keep) : "v"(voff), "s"(r), "s"(soff), "s"(lds_dst) : "memory");
}
__device__ __forceinline__ void ld16_into(u32x4& d, rsrc_t r, u32 voff, u32 soff) {
  asm volatile("s_nop 4\n\tbuffer_load_dwordx4 %0, %1, %2, %3 offen nt" : "+v"(d) : "v"(voff), "s"(r), "s"(soff) : "memory");
}
__device__ __forceinline__ u32 perm_lo(u32 a, u32 b) { return __builtin_amdgcn_perm(b, a, 0x05040100u); }
__device__ __forceinline__ u32 perm_hi(u32 a, u32 b) { return __builtin_amdgcn_perm(b, a, 0x07060302u); }
__device__ __forceinline__ float rdl(float x, int l) { return __int_as_float(__builtin_amdgcn_readlane(__float_as_int(x), l)); }

typedef __attribute__((ext_vector_type(8))) __bf16 bf16x8;
__device__ __forceinline__ f32x4 mfma16(u32x4 a, u32x4 b, f32x4 c) {
  return __builtin_amdgcn_mfma_f32_16x16x32_bf16(__builtin_bit_cast(bf16x8, a), __builtin_bit_cast(bf16x8, b), c, 0, 0, 0);
}
typedef __attribute__((ext_vector_type(8))) unsigned u32x8;
typedef __attribute__((ext_vector_type(16))) __bf16 bf16x16;
__device__ __forceinline__ f32x4 mfma64(u32x4 a, u32x8 b, f32x4 c, u32 idx) {
  return __builtin_amdgcn_smfmac_f32_16x16x64_bf16(__builtin_bit_cast(bf16x8, a), __builtin_bit_cast(bf16x16, b), c, idx, 0, 0);
}
__device__ __forceinline__ float sum8(const float* p) {
  const f32x4 a = *reinterpret_cast<const f32x4*>(p), b = *reinterpret_cast<const f32x4*>(p + 4);
  return ((a[0] + b[0]) + (a[1] + b[1])) + ((a[2] + b[2]) + (a[3] + b[3]));
}
__device__ __forceinline__ float ln_ri(float var) {
  return __builtin_amdgcn_rcpf(rbf(__builtin_amdgcn_sqrtf(rbf(rbf(fmaxf(var, 0.f)) + 1e-5f))));
}
__device__ __forceinline__ void ln_stats2(const float (&x0)[8], const float (&x1)[8], float& mb0, float& ri0, float& mb1, float& ri1) {
  const float k0 = rdl(x0[0], 0), k1 = rdl(x1[0], 0);
  float s0 = 0.f, s1 = 0.f, q0 = 0.f, q1 = 0.f;
#pragma unroll
  for (int k = 0; k < 8; ++k) {
    const float d0 = x0[k] - k0, d1 = x1[k] - k1;
    s0 += d0; s1 += d1; q0 = fmaf(d0, d0, q0); q1 = fmaf(d1, d1, q1);
  }
  float t = swap16_add(swap32_add(s0, s1), swap32_add(q0, q1));
  t += dpp<XOR1>(t); t += dpp<XOR2>(t); t += dpp<XOR7>(t); t += dpp<XOR8>(t);
  auto u = __builtin_amdgcn_permlane16_swap(__float_as_uint(t), __float_as_uint(t), false, false);
  const float ms = __uint_as_float(u[0]) * (1.f / 512.f), mq = __uint_as_float(u[1]) * (1.f / 512.f);
  const float ri = ln_ri(fmaf(-ms, ms, mq));
  mb0 = rbf(k0 + rdl(ms, 0)); mb1 = rbf(k1 + rdl(ms, 32));
  ri0 = rdl(ri, 0); ri1 = rdl(ri, 32);
}
template <int I> struct IC { static constexpr int v = I; };
template <int I, int N, class F> __device__ __forceinline__ void sfor(F&& f) {
  if constexpr (I < N) { if (f(IC<I>{})) sfor<I + 1, N>(f); }
}
typedef __attribute__((ext_vector_type(4))) short s16x4;
typedef __attribute__((address_space(3))) s16x4* lds_s16x4p;
__device__ __forceinline__ u32x2 tr_read(u32 lds_addr) {
  return __builtin_bit_cast(u32x2, __builtin_amdgcn_ds_read_tr16_b64_v4i16((lds_s16x4p)lds_addr));
}
__device__ __forceinline__ u32 next_tag(u32 e) { return ((e | 0x40004000u) + 1u) & ~0x40004000u; }
__device__ __forceinline__ float u2f(u32 v) { return __uint_as_float(v); }
#define LDSP(T, a) ((__attribute__((address_space(3))) T*)(a))
template <class T> __device__ __forceinline__ u32 lds_addr(T* p) { u32 a = (u32)(uintptr_t)p; PIN(a); return a; }

#define TAB_LOAD "s_add_u32 s30, s28, wg_tab@rel32@lo+4\ns_addc_u32 s31, s29, wg_tab@rel32@hi+12\nglobal_load_dwordx2 %[te], %[va], s[30:31]\n"
#define FC(k) ".Lfc" #k
#define FX(r, bit, k) "s_bitcmp1_b32 %[" #r "], " #bit "\ns_cselect_b32 %[sb], " FC(k) ", 0\ns_xor_b32 %[sa], %[sa], %[sb]\n"
__global__ void __launch_bounds__(512, 2) fused_kernel(const u16* kc_, const u16* vc_, const u16* wo_, const u16* wq_, const u16* wmod_, const u16* w1_, const u16* w3_,
                                                       unsigned char* ws_, const u16* x_, const u16* cnd_, const u16* w2_, u16* out_) {
  __shared__ __attribute__((aligned(256))) Smem sm;
  const u64 a_ka = (u64)__builtin_amdgcn_kernarg_segment_ptr();
  const int bid = blockIdx.x, tid = threadIdx.x;
  u64 wa, t64; u32x2 te; int tid0;
  u32 ar[14];
  u32 tch2, sa_, sb_, va_;
  u32x4 kr[NL], vr[NL];
  asm volatile(".set .Lfc0, %0\n.set .Lfc1, %1\n.set .Lfc2, %2\n.set .Lfc3, %3\n.set .Lfc4, %4\n.set .Lfc5, %5\n.set .Lfc6, %6\n.set .Lfc7, %7\n.set .Lfc8, %8\n.set .Lfc9, %9\n.set .Lfc10, %10\n.set .Lfc11, %11"
    :: "n"(first_col(PRM0, 0)), "n"(first_col(PRM0, 1)), "n"(first_col(PRM0, 2)), "n"(first_col(PRM0, 3)), "n"(first_col(PRM0, 4)), "n"(first_col(PRM0, 5)),
       "n"(first_col(PRM0, 6)), "n"(first_col(PRM0, 7)), "n"(first_col(PRM0, 8)), "n"(first_col(PRM0, 9)), "n"(first_col(PRM0, 10)), "n"(first_col(PRM0, 11)));
  asm volatile(
    "s_load_dwordx16 s[4:19], %[ka], 0x0\ns_load_dwordx8 s[20:27], %[ka], 0x40\n"
    "s_memrealtime %[t]\n"
    "v_lshlrev_b32 %[va], 3, %[bid]\n"
    "s_getpc_b64 s[28:29]\n.Lcpf_first:\n"
    TAB_LOAD
    "s_cmp_gt_u32 %[bid], 15\ns_cbranch_scc1 .Lno_cpf\n"
    "s_mov_b32 %[sa], .Lend_a-.Lcpf_first\ns_max_i32 %[sa], %[sa], .Lend_b-.Lcpf_first\ns_max_i32 %[sa], %[sa], .Lend_c-.Lcpf_first\ns_max_i32 %[sa], %[sa], .Lend_d-.Lcpf_first\ns_max_i32 %[sa], %[sa], .Lend_e-.Lcpf_first\n"
    "v_lshlrev_b32 %[va], 6, %[tid]\nv_min_u32 %[va], %[sa], %[va]\nglobal_load_dword %[pf], %[va], s[28:29]\n"
    ".Lno_cpf:\n"
    "v_readfirstlane_b32 %[t0], %[tid]\nv_and_b32 %[va], 63, %[tid]\n"
    "s_mov_b32 %[sa], " FC(11) "\n"
    FX(bid, 0, 0) FX(bid, 1, 1) FX(bid, 2, 2) FX(bid, 3, 3) FX(bid, 4, 4) FX(bid, 5, 5) FX(bid, 6, 6) FX(bid, 7, 7) FX(t0, 6, 8) FX(t0, 7, 9) FX(t0, 8, 10)
    "v_lshl_add_u32 %[va], %[va], 4, %[sa]\n"
    "s_waitcnt lgkmcnt(0)\nglobal_load_dwordx4 %[k0], %[va], s[4:5] nt\nglobal_load_dwordx4 %[v0], %[va], s[6:7] nt\n"
    "s_bitcmp1_b32 %[t0], 6\ns_cselect_b64 s[14:15], s[16:17], s[14:15]\n"
    "s_bitcmp1_b32 %[bid], 4\ns_cselect_b64 s[8:9], s[10:11], s[8:9]\ns_cselect_b64 s[12:13], s[14:15], s[12:13]\ns_bitcmp1_b32 %[bid], 5\ns_cselect_b64 %[wa], s[12:13], s[8:9]"
    : "=&{s4}"(ar[0]), "=&{s5}"(ar[1]), "=&{s6}"(ar[2]), "=&{s7}"(ar[3]), "=&{s18}"(ar[4]), "=&{s19}"(ar[5]), "=&{s20}"(ar[6]), "=&{s21}"(ar[7]), "=&{s22}"(ar[8]), "=&{s23}"(ar[9]),
      "=&{s24}"(ar[10]), "=&{s25}"(ar[11]), "=&{s26}"(ar[12]), "=&{s27}"(ar[13]), [wa] "=&s"(wa), [t0] "=&s"(tid0), [t] "=&s"(t64), [te] "=&v"(te), [sa] "=&s"(sa_), [sb] "=&s"(sb_),
      [va] "=&v"(va_), [pf] "=&v"(tch2), [k0] "=&v"(kr[0]), [v0] "=&v"(vr[0])
    : [ka] "s"(a_ka), [bid] "s"(bid), [tid] "v"(tid) : "s8", "s9", "s10", "s11", "s12", "s13", "s14", "s15", "s16", "s17", "s28", "s29", "s30", "s31", "scc", "memory");
  asm volatile("" : "+s"(ar[0]), "+s"(ar[1]), "+s"(ar[2]), "+s"(ar[3]), "+s"(ar[4]), "+s"(ar[5]), "+s"(ar[6]), "+s"(ar[7]), "+s"(ar[8]), "+s"(ar[9]), "+s"(ar[10]), "+s"(ar[11]), "+s"(ar[12]), "+s"(ar[13]));
  asm volatile("" : "+s"(wa), "+s"(t64), "+s"(tid0), "+s"(sa_));
  const u32 tka = (u32)wall_clock64();
  __builtin_assume(tid >= 0 && tid < 512 && tid0 >= 0 && tid0 < 512 && bid >= 0 && bid < 256);
  constexpr Prm P = PRM0;
  const u32 t0 = tka + min(max((int)(tka - (u32)t64) - P.SA, 0) * P.SG >> 4, P.SC);
  const int TEND = P.TEND;
  const int xcd = bid & 7, part = bid >> 4 & 3;
  const int lane = tid & 63, wave = tid0 >> 6;
  SB();
  if ((bid >> 4) == 2) {
    const u32 o = tid < 384 ? G_U + (bid & 15) * 6144 + tid * 16 : OOB;
    __builtin_amdgcn_raw_buffer_store_b128(u32x4{SENT, SENT, SENT, SENT}, mk_rsrc((void*)(ar[4] | (u64)ar[5] << 32), WS_END), o, 0, AUX_SC1);
  }
  SB();
  const bool p3 = part == 3;
  u32 pitch = (u32)(0x005600c001000020ull >> (part * 16) & 0xffff) * 32u;
  const int sh = p3 ? 2 : 3;
  u32 wv0 = (u32)((p3 ? (wave >> 1) * 128 : wave * 64) + (part == 0 ? xcd * 512 : 0) + (lane >> sh)) * pitch + ((lane & (p3 ? 3 : 7)) ^ (lane >> (p3 ? 5 : 3))) * 16u;
  const u32 wimg = (u32)(uintptr_t)(&sm.wbuf[0][0][0]);
  u32 wsz = (part == 0 ? 4096u : 512u) * pitch, wld = wimg + (p3 ? (wave & 1) * 32768 + (wave >> 1) * 8192 : wave * 8192);
  PIN(wv0);
  asm volatile("" : "+s"(wsz), "+s"(wld), "+s"(pitch));
  auto karg = [&](int i) { return ar[2 * i] | (u64)ar[2 * i + 1] << 32; };
  const u64 a_kc = karg(0), a_vc = karg(1);
  unsigned char* ws = (unsigned char*)karg(2);
  const u16 *x = (const u16*)karg(3), *cnd = (const u16*)karg(4), *w2 = (const u16*)karg(5);
  u16* out = (u16*)karg(6);
  const rsrc_t wsr = mk_rsrc(ws, WS_END);
  asm volatile("s_waitcnt vmcnt(3)\n\ts_cmp_eq_u32 %1, 2\n\ts_cbranch_scc1 1f\n\ts_waitcnt vmcnt(2)\n1:" : "+v"(te) : "s"(bid >> 4) : "scc");
  const u32 te0 = __builtin_amdgcn_readfirstlane(te[0]), te1 = __builtin_amdgcn_readfirstlane(te[1]);
  const int bl0 = te0 & 127, nl = te0 >> 7 & 63, blk = te0 >> 13 & 63;
  const bool has_w = te0 >> 19 & 1;
  const rsrc_t rw = mk_rsrc((const void*)wa, wsz);
  const u32 wstep = pitch << (6 - sh), wflip = (p3 || part == 0) ? 32u : 0u, wflip0 = part == 0 ? 32u : 0u;
  u32 wso = blk << (sh + 4);
  int wleft = 8;
  auto piece = [&]() {
    dma16(rw, wv0, wso, wld);
    wso += wstep; wld += 1024;
    wv0 ^= (wleft & 1) ? wflip : wflip0;
    --wleft;
  };
  SB();
  const int b = bid >> 3 & 1, head = xcd * 4 + (bid >> 6 ^ 3);
  const int g = lane >> 4, c = lane & 15;
  const int i8 = b | bid >> 6 << 1, wi = i8 * 8 + xcd;
  const bool is_up = p3 && has_w, modc = part == 2 && wi < 16;
  const size_t base = ((size_t)(b * NH + head) * SK + (SK - WIN)) * DH;
  const rsrc_t rk = mk_rsrc((const u16*)a_kc + base, WIN * DH * 2), rv = mk_rsrc((const u16*)a_vc + base, WIN * DH * 2);
  int tau = te1 & 0xffff;
  const int tauw = te0 >> 24 << 7, wdef = te0 >> 20 & 15;
  int due = P.NF * 256, now = 0;
  auto gate = [&]() {
    while (now < due) { now = (int)(((u32)wall_clock64() - t0) << 8); if (now < due) __builtin_amdgcn_s_sleep(1); }
    due += tau;
  };
  auto until = [&](int t) { while ((int)(((u32)wall_clock64() - t0) << 8) < t); };
  const u32 pslot = dup(G_P + (head & 15) * 2048 + (b * 4 + part) * 16);
  const u32 die = xcd >> 2;
  const bool voter = p3 && i8 == 1, timer = p3 && i8 < 2;
  int vote = 0, lag = 0, shift = 0;
  const u32 pbo = dup(G_PB) + wave * 1152;
  u32 near = 0;
  u32x2 ep; u32 xs0, xs1, tch1; u32x4 xr0, xr1;
  const rsrc_t rx = mk_rsrc(part == 2 ? cnd : x, (part == 1 || (part == 2 && has_w)) ? 2048u : 0u);
  auto small_ops = [&]() {
    ep = ld8<AUX_SC1>(wsr, wave == 7 ? WS_EPOCH : OOB, 0);
    xs0 = __builtin_amdgcn_raw_buffer_load_b16(rx, tid * 2, 0, 0); xs1 = __builtin_amdgcn_raw_buffer_load_b16(rx, tid * 2, 1024, 0);
    xr0 = ld16<0>(rx, part == 1 ? lane * 16 : OOB, 0); xr1 = ld16<0>(rx, part == 1 ? lane * 16 : OOB, 1024);
    tch1 = __builtin_amdgcn_raw_buffer_load_b32(mk_rsrc(w2 + (size_t)blk * 32 * 512, is_up ? 64u : 0u), tid == 384 ? 0 : OOB, 0, AUX_NT);
  };
  if (has_w) {
    if (modc) now = 1 << 30;
#pragma nounroll
    while (wleft > wdef) { gate(); due += tauw - tau; piece(); }
    now = 0;
  }
  due = max(due, P.TK * 256);
  SB();
  u32 sot = sa_ & 0xffc00u;
  {
    const u32 s1 = (bl0 + lane - 1 - (lane >= SK0 ? NL - nl : 0)) ^ (124 + part);
#pragma unroll
    for (int k = 0; k < 7; ++k) sot ^= (u32)-(int)(s1 >> k & 1u) & col(P, k);
    PIN(sot);
  }
  u32 tag = 0;
  u32x4 bf[8];
  auto wo_prep = [&]() {
    const u32 wi_ = wimg + wave * 8192;
    const int q = (lane >> 2) & 3, pp = lane & 3;
#pragma unroll
    for (int s = 0; s < 2; ++s) {
#pragma unroll
      for (int t = 0; t < 4; ++t) {
        const int r0 = s * 32 + g * 8 + q, r1 = r0 + 4, ch = t * 2 + (pp >> 1);
        const u32x2 lo = tr_read(wi_ + r0 * 128 + ((ch ^ (r0 & 7)) * 16) + (pp & 1) * 8), hi = tr_read(wi_ + r1 * 128 + ((ch ^ (r1 & 7)) * 16) + (pp & 1) * 8);
        bf[s * 4 + t] = u32x4{lo[0], lo[1], hi[0], hi[1]};
      }
    }
  };
  auto gemv_go = [&](u32 hh) {
    sm.hb[tid] = (u16)hh; sm.hb[512 + tid] = (u16)(hh >> 16);
    WAVE_SYNC();
    const u32x4* a = (c & 11) == 0 ? reinterpret_cast<const u32x4*>(&sm.hb[(c >> 2) * 512 + wave * 64 + g * 8]) : &sm.zero[0];
    const u32x4 a0 = a[0], a1 = a[(c & 11) == 0 ? 4 : 0];
    const f32x4 z = {0.f, 0.f, 0.f, 0.f};
    f32x4 d[4];
#pragma unroll
    for (int t = 0; t < 4; ++t) d[t] = mfma16(a0, bf[t], z);
#pragma unroll
    for (int t = 0; t < 4; ++t) d[t] = mfma16(a1, bf[4 + t], d[t]);
    if (lane < 32) {
#pragma unroll
      for (int t = 0; t < 4; ++t) sm.r2[(t * 16 + c) * 20 + g * 8 + wave] = d[t][0];
    }
  };
  auto mod_stage = [&]() {
    wo_prep();
    gemv_go(pack_bf(silu_f(lo_f(xs0)), silu_f(lo_f(xs1))));
    __builtin_amdgcn_s_waitcnt(0xc07f);
    if (lane == 0) *LDSP(volatile u32, (u32)(uintptr_t)&sm.mflag[wave]) = 1u;
    if (wave == 7) {
#pragma nounroll
      for (int spin = 0; spin < SPIN_MAX; ++spin) {
        const u32 f = *LDSP(volatile u32, (u32)(uintptr_t)&sm.mflag[lane & 7]);
        if (all_ok(f != 0u)) break;
        __builtin_amdgcn_s_sleep(1);
      }
      CFENCE();
      st8_sc1(wsr, G_MOD + (blk * 64 + lane) * 8, pack_bf(sum8(&sm.r2[lane * 20]), sum8(&sm.r2[lane * 20 + 8])), tag);
    }
  };
  if (lane < 4) sm.zero[lane] = u32x4{0u, 0u, 0u, 0u};
  if (part == 2 && has_w) {
    if (tid < 8) sm.mflag[tid] = 0u;
    LDS_BARRIER();
    if (__builtin_expect(modc, 0)) {
      small_ops();
      asm volatile("s_waitcnt vmcnt(0)" ::: "memory");
      tag = next_tag(__builtin_amdgcn_readfirstlane(ep[0]));
      mod_stage();
      __builtin_amdgcn_s_barrier();
    }
  }
  SB();
  const int fd = (xcd * 8 + i8) * 8 + (lane & 3) * 2;
  const int ub = wave >> 2, ud = (wave & 3) * 128 + c * 8 + g * 2;
  u32x4 sg[3]; u32 sx2;
  auto side_loads = [&]() {
    const bool fin = part == 1 && wave == 0;
    sg[0] = ld16<AUX_SC1>(wsr, is_up ? G_MOD + (1024 + ud) * 8 : (fin ? G_MOD + (1024 + fd) * 8 : OOB), 0);
    sg[1] = ld16<AUX_SC1>(wsr, is_up ? G_MOD + (1536 + ud) * 8 : (fin ? G_MOD + (2560 + fd) * 8 : OOB), 0);
    sg[2] = ld16<AUX_SC1>(wsr, is_up ? G_MOD + (2048 + ud) * 8 : OOB, 0);
    sx2 = __builtin_amdgcn_raw_buffer_load_b32(mk_rsrc(x, (is_up || fin) ? 2048u : 0u), is_up ? ub * 1024 + ud * 2 : (g & 1) * 1024 + fd * 2, 0, 0);
  };
  auto side_pin = [&]() { PIN(sg[0]); PIN(sg[1]); PIN(sg[2]); PIN(sx2); };
  const u32 mlo = G_ML + (head * 2 + b) * 64 + (lane & 3) * 16;
  float fmrg = 0.f;
  auto merge_factor = [&](const u32x4& ml) {
    const float mi = u2f(ml[0]);
    float M = fmaxf(mi, dpp<XOR1>(mi)); M = fmaxf(M, dpp<XOR2>(M));
    const float e = __expf(mi - M);
    float L = e * u2f(ml[2]);
    L += dpp<XOR1>(L); L += dpp<XOR2>(L);
    fmrg = rdl(e * __builtin_amdgcn_rcpf(L), part);
  };
  f32x2 ga = {0.f, 0.f}, xin = {0.f, 0.f}, cc1 = {0.f, 0.f};
  float Af = 0.f, Bf = 0.f;
  const int hpos = ub * 512 + (ud & ~15) + (g & 1) * 8 + (c & 1) * 4 + (g >> 1) * 2;
  auto up_prep = [&]() {
    if (!all_ok((sg[0][1] == tag) & (sg[0][3] == tag) & (sg[1][1] == tag) & (sg[1][3] == tag) & (sg[2][1] == tag) & (sg[2][3] == tag))) gather16<3>(wsr, G_MOD + (1024 + ud) * 8, 512 * 8, tag, sg);
    ga = f32x2{ub ? hi_f(sg[0][0]) : lo_f(sg[0][0]), ub ? hi_f(sg[0][2]) : lo_f(sg[0][2])};
    cc1 = f32x2{rbf(1.f + (ub ? hi_f(sg[2][0]) : lo_f(sg[2][0]))), rbf(1.f + (ub ? hi_f(sg[2][2]) : lo_f(sg[2][2])))};
    xin = f32x2{lo_f(sx2), hi_f(sx2)};
    u32* hp = reinterpret_cast<u32*>(&sm.hb[hpos]);
    hp[512] = pack_bf(cc1[0], cc1[1]);
    hp[1024] = ub ? perm_hi(sg[1][0], sg[1][2]) : perm_lo(sg[1][0], sg[1][2]);
  };
  u32x8 bw[4];
  auto up_frags = [&]() {
    const int pp = lane & 3;
    const u32 a = wimg + (pp & 1) * 32768 + ((pp >> 1) * 32 + g * 8 + ((lane >> 2) & 3)) * 64 + (((wave >> 1) ^ ((g & 1) | (pp & 2))) * 16) + (wave & 1) * 8;
#pragma unroll
    for (int j = 0; j < 4; ++j) {
      const u32x2 r0 = tr_read(a + j * 4096), r1 = tr_read(a + j * 4096 + 256), r2 = tr_read(a + j * 4096 + 16384), r3 = tr_read(a + j * 4096 + 16640);
      bw[j] = u32x8{r0[0], r0[1], r1[0], r1[1], r2[0], r2[1], r3[0], r3[1]};
    }
  };
  u32 wfa[4];
  auto wo_frags = [&](int s0) {
#pragma unroll
    for (int t = 0; t < 4; ++t) {
#pragma unroll
      for (int s = s0; s < 2; ++s) {
        const u32x2 lo = tr_read(wfa[t] + s * 4096), hi = tr_read(wfa[t ^ 1] + s * 4096 + 256);
        bw[t][s * 4] = lo[0]; bw[t][s * 4 + 1] = lo[1]; bw[t][s * 4 + 2] = hi[0]; bw[t][s * 4 + 3] = hi[1];
      }
    }
  };
  const bool par = lane & 8;
  auto limit = [&](auto sc) {
    constexpr int s = sc.v;
    WAIT_VM(window(s, 1 + (PRM0.W[s >> 3] >> (4 * (s & 7)) & 7)));
    SB();
  };
  auto sample = [&]() { if (timer) lag += (int)(((u32)wall_clock64() - t0) << 8) - due + ((int)__builtin_amdgcn_s_getreg(7 | (3 << 11)) + 1) * tau; };
  auto report = [&](u32 dst) {
    if (timer) {
      if (lane == 0) sm.mflag[wave] = vote * 65536 + min(max(lag >> 8, 0), 4000);
      lag = 0;
      LDS_BARRIER();
      if (wave == 7) {
        int s = sm.mflag[lane & 7];
        s += __builtin_amdgcn_mov_dpp(s, XOR1, 0xf, 0xf, true); s += __builtin_amdgcn_mov_dpp(s, XOR2, 0xf, 0xf, true); s += __builtin_amdgcn_mov_dpp(s, XOR7, 0xf, 0xf, true);
        if (lane < 8) st8_sc1(wsr, dst + lane * 128 + wi * 8, s, tag);
      }
    }
    SB();
  };
  float l0 = 0.f, l1 = 0.f;
  u32x2 gmd[2], tbg;
  int t1 = 0;
#pragma unroll
  for (int i = SK0; i < SK1; ++i) kr[i] = u32x4{0u, 0u, 0u, 0u};
  const int first = SK0 + NL - nl;
  auto left_out = [&](int i) { int f = first; asm volatile("" : "+s"(f)); return i < f; };
  sfor<1, NL>([&](auto ic) {
    constexpr int i = ic.v;
    if constexpr (i >= SK0 && i < SK1) { if (left_out(i)) return true; }
    const u32 so = __builtin_amdgcn_readlane(sot, i);
    gate();
    if constexpr ((i >= TBS && i <= TBP) || (i >= RPS && i <= RPP)) sample();
    SB();
    if constexpr (i >= SK0 && i < SK1) ld16_into(kr[i], rk, lane * 16, so); else kr[i] = ld16<AUX_NT>(rk, lane * 16, so);
    if constexpr (i == SMO) small_ops();
    if constexpr (i == SMO + 1) { if (voter && (xcd & 3) == 0) __builtin_amdgcn_raw_buffer_store_b32(0u, wsr, pbo + (lane >> 5) * 8192 + (lane & 31) * 4, 0, AUX_SC1); }
    if constexpr (i == SK1) tbg = ld8<AUX_SC1>(wsr, wave == 7 ? G_VT + xcd * 128 + c * 8 : OOB, 0);
    if constexpr (i == TBL) tbg = ld8<AUX_SC1>(wsr, wave == 7 ? G_TB + xcd * 128 + c * 8 : OOB, 0);
    if constexpr (i == QP) { gmd[0] = ld8<AUX_SC1>(wsr, part == 1 ? G_MOD + tid * 8 : OOB, 0); gmd[1] = ld8<AUX_SC1>(wsr, part == 1 ? G_MOD + tid * 8 : OOB, 4096); }
    SB();
    SB();
    if (i >= 4) limit(ic);
    if (i == TBP) {
      if (timer) {
        if (lane == 0) sm.mflag[wave] = lag;
        lag = 0;
        LDS_BARRIER();
        if (wave == 7) {
          int s = sm.mflag[lane & 7];
          s += __builtin_amdgcn_mov_dpp(s, XOR1, 0xf, 0xf, true); s += __builtin_amdgcn_mov_dpp(s, XOR2, 0xf, 0xf, true); s += __builtin_amdgcn_mov_dpp(s, XOR7, 0xf, 0xf, true);
          if (lane < 8) st8_sc1(wsr, G_TB + lane * 128 + wi * 8, t0 << 10 | (u32)min(s / (2048 * (TBP - TBS + 1)), 1023), tag);
        }
      }
      SB();
    }
    if (i == PRB) {
      if (voter) {
        u64 ta, tb, tc; u32 dm;
        const u32 pa = pbo + (wave & 1) * 8192;
        asm volatile("s_memtime %0\n\ts_waitcnt lgkmcnt(0)\n\ts_load_dword %3, %4, %5\n\ts_waitcnt lgkmcnt(0)\n\ts_memtime %1\n\ts_waitcnt lgkmcnt(0)\n\t"
                     "s_load_dword %3, %4, %6\n\ts_waitcnt lgkmcnt(0)\n\ts_memtime %2\n\ts_waitcnt lgkmcnt(0)"
                     : "=&s"(ta), "=&s"(tb), "=&s"(tc), "=&s"(dm) : "s"(ws), "s"(pa), "s"(pa ^ 8192u));
        const int d = min(max((int)(2 * (u32)tb - (u32)ta - (u32)tc), -500), 500);
        vote = ((wave ^ die) & 1) ? -d : d;
      }
      SB();
    }
    if (i == TBU) {
      if (wave == 7) {
        const bool ok = tbg[1] == tag;
        float lf = ok ? (float)min(max((int)((t0 << 10 | 1023u) - tbg[0]) >> 10, -300), 300) : 0.f, nf = ok ? 1.f : 0.f, lg = ok ? (float)(tbg[0] & 1023u) : 0.f;
        lf += dpp<XOR1>(lf); nf += dpp<XOR1>(nf); lg += dpp<XOR1>(lg); lf += dpp<XOR2>(lf); nf += dpp<XOR2>(nf); lg += dpp<XOR2>(lg);
        lf += dpp<XOR7>(lf); nf += dpp<XOR7>(nf); lg += dpp<XOR7>(lg); lf += dpp<XOR8>(lf); nf += dpp<XOR8>(nf); lg += dpp<XOR8>(lg);
        const float rn = __builtin_amdgcn_rcpf(fmaxf(nf, 1.f));
        shift = __builtin_amdgcn_readfirstlane(min(max((int)((lg * rn - (float)P.GS) * (float)P.GB * 0.125f), 0), 150));
        if (lane == 0) sm.bc = u32x2{tag, t0 - (u32)(int)(lf * rn) + shift};
      }
      LDS_BARRIER();
      const u32x2 bc = sm.bc;
      tag = __builtin_amdgcn_readfirstlane(bc[0]);
      t1 = (__builtin_amdgcn_readfirstlane((int)(bc[1] - t0)) + TEND + P.X1) * 256;
      tau -= __builtin_amdgcn_readfirstlane((int)((float)(int)(t0 - bc[1]) * 256.f * __builtin_amdgcn_rcpf((float)(nl - TBU - 1))));
      SB();
    }
    if (i == RPP) report(G_VT);
    if (i == LNS) {
      tag = next_tag(__builtin_amdgcn_readfirstlane(ep[0]));
      if (part == 1) {
        float x0[8], x1[8], mb0, ri0, mb1, ri1;
#pragma unroll
        for (int k = 0; k < 4; ++k) { x0[2 * k] = lo_f(xr0[k]); x0[2 * k + 1] = hi_f(xr0[k]); x1[2 * k] = lo_f(xr1[k]); x1[2 * k + 1] = hi_f(xr1[k]); }
        ln_stats2(x0, x1, mb0, ri0, mb1, ri1);
        l0 = rbf(rbf(lo_f(xs0) - mb0) * ri0); l1 = rbf(rbf(lo_f(xs1) - mb1) * ri1);
      }
      PIN(l0); PIN(l1);
      SB();
    }
    if (i == WUSE) {
      PIN(tch1); PIN(tch2);
      if ((part == 1 || (part == 2 && has_w && !modc))) {
        WAIT_VM(WK); CFENCE();
        if (part == 2) mod_stage(); else wo_prep();
      }
#pragma unroll
      for (int k = 0; k < 8; ++k) PIN(bf[k]);
      SB();
    }
    if (i == QP + QD) {
      if (part == 1) {
        u32 gm[2] = {gmd[0][0], gmd[1][0]};
        if (!all_ok((gmd[0][1] == tag) & (gmd[1][1] == tag))) gather8<2>(wsr, G_MOD + tid * 8, 512 * 8, tag, gm);
        gemv_go(pack_bf(rbf(l0 * rbf(1.f + lo_f(gm[1]))) + lo_f(gm[0]), rbf(l1 * rbf(1.f + hi_f(gm[1]))) + hi_f(gm[0])));
      }
      SB();
    }
    if (i == QP + QD + 1) {
      if (part == 1) {
        LDS_BARRIER();
        if (wave == 0) {
          const int bb = lane >> 5, jq = lane & 31;
          st8_sc1(wsr, G_Q + (bb * 2048 + blk * 32 + jq) * 8, pack_bf(sum8(&sm.r2[2 * jq * 20 + bb * 8]), sum8(&sm.r2[(2 * jq + 1) * 20 + bb * 8])), tag);
        }
      }
      SB();
    }
    return true;
  });
  PIN(sot);
  if (wave == 7) {
    const bool ok = tbg[1] == tag;
    float lg = ok ? (float)(tbg[0] & 0xffffu) : 0.f, nf = ok ? 8.f * (RPP - RPS + 1) : 0.f;
    lg += dpp<XOR1>(lg); nf += dpp<XOR1>(nf); lg += dpp<XOR2>(lg); nf += dpp<XOR2>(nf); lg += dpp<XOR7>(lg); nf += dpp<XOR7>(nf); lg += dpp<XOR8>(lg); nf += dpp<XOR8>(nf);
    lg *= __builtin_amdgcn_rcpf(fmaxf(nf, 1.f));
    if (lane == 0) sm.bc2 = u32x2{(u32)min(shift * P.DG * 32, P.EW * 256), (u32)min(max((int)((lg - (float)P.B0) * (float)(P.BG * 32) + fmaxf(lg - (float)P.TC, 0.f) * (float)(P.RT * 32)) - shift * P.D0 * 32, 0), 200 * 256)};
  }
  LDS_BARRIER();
  {
    const u32x2 bc = sm.bc2;
    const int st = __builtin_amdgcn_readfirstlane(bc[1]), sp = (st * P.BF) >> 3;
    t1 += st + __builtin_amdgcn_readfirstlane(bc[0]);
    due += P.TG * 256 + sp;
    tau = (te1 >> 16) + __builtin_amdgcn_readfirstlane((int)((float)(st - sp) * __builtin_amdgcn_rcpf((float)(nl - 3))));
  }
  const u32 qoff = G_Q + ((b * 32 + head) * 64 + c * 4) * 8;
  u32x4 qg[2], mlg;
  u32 qq[4];
  constexpr int NB = (NL + 15) / 16;
  float xlo, sc[NB], pr[NB], m = 0.f;
  f32x4 acc = {0.f, 0.f, 0.f, 0.f};
  u32 pb[NB], pm[4], pl8[8], cpf;
  const u32 adst0 = lds_addr(lane < 32 ? &sm.red[wave][c * 8 + g * 4] : &sm.r2[(lane - 32) * 4]);
  u32 po, pso, rdst, rsrc, xo;
  auto wo_addr = [&]() {
    po = dup(G_P + (xcd & 3) * 8192) + tid * 16 + near;
    pso = tid < 16 ? pslot + c * 128 + near : OOB;
    PIN(pso);
    rdst = (u32)(uintptr_t)&sm.red2[wave][(lane & 31) * 4 + (lane >> 5) * 2];
    rsrc = (u32)(uintptr_t)&sm.red2[0][((wave & 1) * 16 + c) * 4 + (g & 1) * 2 + (g >> 1)];
    xo = dup(G_XP + xcd * 2048 + (wave & 1) * 1024 + (blk * 64 + (lane & ~1)) * 2) + (lane & 1) * 8192;
    PIN(po); PIN(rdst); PIN(rsrc); PIN(xo);
    const int q = (lane >> 2) & 3, pp = lane & 3, r = (g & 1) * 4 + (q & 1);
#pragma unroll
    for (int t = 0; t < 4; ++t) { wfa[t] = wimg + wave * 8192 + ((g >> 1) * 16 + (q >> 1) * 8 + r) * 128 + (pp & 1) * 8 + ((t * 2 + (pp >> 1)) ^ r ^ (q & 2)) * 16; PIN(wfa[t]); }
  };
  const u32 nadd = wdef ? P.LW : (is_up ? P.LU : 0);
  u32 nto = tid < 2 ? dup(G_NT) + bid * 8 + tid * 8192 : OOB;
  PIN(nto);
  auto notices = [&]() -> int {
    u32 off = dup(G_NT) + near + lane * 16;
    PIN(off);
    until(due);
    int mx;
#pragma nounroll
    for (int spin = 0; spin < SPIN_MAX; ++spin) {
      CFENCE();
      const u32x4 ga_ = ld16<AUX_SC1>(wsr, off, 0), gb_ = ld16<AUX_SC1>(wsr, off, 1024);
      mx = max(max((int)(ga_[0] - t0), (int)(ga_[2] - t0)), max((int)(gb_[0] - t0), (int)(gb_[2] - t0)));
      if (all_ok((((ga_[1] ^ tag) | (ga_[3] ^ tag)) | ((gb_[1] ^ tag) | (gb_[3] ^ tag))) == 0u)) break;
      for (int k = 0; k < P.AG; ++k) __builtin_amdgcn_s_sleep(1);
    }
    if (all_ok(mx <= (t1 >> 8) - P.AC)) return t1;
    mx = max(mx, __builtin_amdgcn_mov_dpp(mx, XOR1, 0xf, 0xf, true)); mx = max(mx, __builtin_amdgcn_mov_dpp(mx, XOR2, 0xf, 0xf, true));
    mx = max(mx, __builtin_amdgcn_mov_dpp(mx, XOR7, 0xf, 0xf, true)); mx = max(mx, __builtin_amdgcn_mov_dpp(mx, XOR8, 0xf, 0xf, true));
    mx = max(max(__builtin_amdgcn_readlane(mx, 0), __builtin_amdgcn_readlane(mx, 16)), max(__builtin_amdgcn_readlane(mx, 32), __builtin_amdgcn_readlane(mx, 48)));
    return max(min(mx, 1 << 20) * 256, t1 + P.L0 * 256) + P.L1 * 256;
  };
  auto pv_row = [&](auto jc) {
    constexpr int i = jc.v;
    const u32 pv = __float_as_uint(bcast16(u2f(pb[i >> 4]), i & 15));
    acc = mfma16(u32x4{pv & pm[0], pv & pm[1], pv & pm[2], pv & pm[3]}, vr[i], acc);
    PIN(acc);
    return true;
  };
  sfor<0, NL - 1>([&](auto ic) {
    constexpr int v = ic.v;
    if constexpr (v >= SK0 && v < SK1) { if (left_out(v)) return true; }
    if constexpr (v > 0) {
      const u32 so = __builtin_amdgcn_readlane(sot, v);
      if constexpr (v >= SK1) { if (v == NL - P.NE) tau = (tau * P.FE) >> 4; }
      gate();
      if constexpr (v >= RVS && v <= RVP) sample();
      SB();
      if constexpr (v >= SK0 && v < SK1) ld16_into(vr[v], rv, lane * 16, so); else vr[v] = ld16<AUX_NT>(rv, lane * 16, so);
      SB();
      SB();
    }
    if constexpr (v == SK0 - 1) {
#pragma unroll
      for (int i = SK0; i < SK1; ++i) { vr[i] = u32x4{0u, 0u, 0u, 0u}; PIN(vr[i]); }
    }
    if constexpr (v == VQ - 4) { qg[0] = ld16<AUX_SC1>(wsr, qoff, 0); qg[1] = ld16<AUX_SC1>(wsr, qoff + 16, 0); }
    if constexpr (v == VSIDE) side_loads();
    if constexpr (v == CPF) {
      u64 pc; u32 dlt, len;
      asm volatile("s_getpc_b64 %0\n.Lcpf_here:\n\ts_mov_b32 %1, .Ltail_start-.Lcpf_here\n\ts_mov_b32 %2, .Lend_a-.Ltail_start\n\ts_max_i32 %2, %2, .Lend_b-.Ltail_start\n\ts_max_i32 %2, %2, .Lend_c-.Ltail_start\n\ts_max_i32 %2, %2, .Lend_d-.Ltail_start\n\ts_max_i32 %2, %2, .Lend_e-.Ltail_start" : "=s"(pc), "=s"(dlt), "=s"(len) :: "scc");
      pc += dlt;
      const rsrc_t rc = mk_rsrc((const void*)pc, (part == 3 && i8 == 1) ? len : 0u);
      cpf = __builtin_amdgcn_raw_buffer_load_b32(rc, tid * 64, 0, 0);
    }
    if constexpr (v == SK1 + 2) PIN(cpf);
    if constexpr (v == RVP) report(G_VT + 1024);
    if constexpr (v == RVL) tbg = ld8<AUX_SC1>(wsr, wave == 7 ? G_VT + (g ? 0 : 1024) + xcd * 128 + c * 8 : OOB, 0);
    if constexpr (v == SK1) mlg = ld16<AUX_SC1>(wsr, wave == 0 ? mlo : OOB, 0);
    SB();
    if constexpr (NL + v < 72) limit(IC<NL + v>{});
    if constexpr (v >= SK0 + WV) { WAIT_VM(v < SK1 ? WV : WV + 1); SB(); }
    if constexpr (v == VQ) {
      if (!all_ok((qg[0][1] == tag) & (qg[0][3] == tag) & (qg[1][1] == tag) & (qg[1][3] == tag))) gather16<2>(wsr, qoff, 16, tag, qg);
      qq[0] = qg[0][0]; qq[1] = qg[0][2]; qq[2] = qg[1][0]; qq[3] = qg[1][2];
    }
    if constexpr (v >= VQ && v < VS) {
      constexpr int sl = v - VQ, bt = sl >> 1;
      float xs[8];
#pragma unroll
      for (int i = 0; i < 8; ++i) {
        float d = 0.f;
        if (sl * 8 + i < NL) {
#pragma unroll
          for (int k = 0; k < 4; ++k) d = dot2(kr[sl * 8 + i][k], qq[k], d);
        }
        xs[i] = d;
      }
#pragma unroll
      for (int h = 0; h < 4; ++h) xs[h] = sel_add<XOR7>(xs[h], xs[h + 4], c & 4);
#pragma unroll
      for (int h = 0; h < 2; ++h) xs[h] = sel_add<XOR2>(xs[h], xs[h + 2], c & 2);
      xs[0] = sel_add<XOR1>(xs[0], xs[1], c & 1);
      if constexpr ((sl & 1) || sl == NSL - 1) {
        const float t = (sl & 1) ? sel_add<XOR8>(xlo, xs[0], c & 8) : xs[0] + dpp<XOR8>(xs[0]);
        const float s = rbf(rbf(t) * 0.08838834764831845f);
        sc[bt] = (bt * 16 + c < SK0 || (bt * 16 + c >= first && bt * 16 + c < NL)) ? s : -__builtin_inff();
        PIN(sc[bt]);
      } else {
        xlo = xs[0];
        PIN(xlo);
      }
    }
    if constexpr (v == VS) {
      float mloc = sc[0];
#pragma unroll
      for (int bt = 1; bt < NB; ++bt) mloc = fmaxf(mloc, sc[bt]);
      m = fmaxf(wave_max(mloc), -3.0e38f);
      float ls = 0.f;
#pragma unroll
      for (int bt = 0; bt < NB; ++bt) { pr[bt] = __expf(sc[bt] - m); ls += pr[bt]; }
      const float l = wave_sum(ls);
      if (lane == 0) { sm.ml[wave][0] = m; sm.ml[wave][1] = l; }
      if (c == ((NL - 1) & 15)) sm.pl[g][wave] = pr[NB - 1];
    }
    if constexpr (v >= SK1) pv_row(IC<v - SK1 + SK0 - WV>{});
    if constexpr (v < SK0 || v >= SK0 + WV) sfor<cons_end(v - 1), cons_end(v)>(pv_row);
    if constexpr (v == RVU) {
      if (wave == 7) {
        if (__builtin_expect(!all_ok(tbg[1] == tag || g == 0), 0)) {
          u32 vg[1];
          gather8<1>(wsr, G_VT + xcd * 128 + c * 8, 0, tag, vg);
          if (g) tbg = u32x2{vg[0], tag};
        }
        const bool ok = tbg[1] == tag;
        float lg = ok ? (float)(g ? (int)tbg[0] >> 16 : (int)(tbg[0] & 0xffffu)) : 0.f, nf = ok ? 8.f * (RVP - RVS + 1) : 0.f;
        lg += dpp<XOR1>(lg); nf += dpp<XOR1>(nf); lg += dpp<XOR2>(lg); nf += dpp<XOR2>(nf); lg += dpp<XOR7>(lg); nf += dpp<XOR7>(nf); lg += dpp<XOR8>(lg); nf += dpp<XOR8>(nf);
        lg *= __builtin_amdgcn_rcpf(fmaxf(nf, 1.f));
        if (lane == 16) sm.bc4 = __float_as_uint(lg);
        if (lane == 0) sm.bc3 = (u32)min(max((int)((lg - (float)P.F0) * (float)(P.FG * 32)), 0), 200 * 256);
      }
      LDS_BARRIER();
      const int st = __builtin_amdgcn_readfirstlane(sm.bc3), sp = (st * P.FF_) >> 3;
      near = ((__builtin_amdgcn_readfirstlane(sm.bc4) >> 31 ^ die ^ P.NR ^ 1) & 1) * 8192u;
      t1 += st;
      due += sp;
      tau += __builtin_amdgcn_readfirstlane((int)((float)(st - sp) * __builtin_amdgcn_rcpf((float)(nl - RVU - 3))));
    }
    if constexpr (v == SK1 + 1) {
      float p[8];
#pragma unroll
      for (int w = 0; w < 8; ++w) p[w] = sm.pl[g][w] * __expf(sm.ml[w][0] - m);
#pragma unroll
      for (int w = 0; w < 8; ++w) pl8[w] = pack_bf(p[w], p[w]);
#pragma unroll
      for (int w = 0; w < 8; ++w) PIN(pl8[w]);
    }
    if constexpr (v == MLS - 1) { side_pin(); if (is_up) up_prep(); PIN(ga); PIN(xin); PIN(cc1); }
    if constexpr (v == UPREP - 1) wo_addr();
    if constexpr (v == UPREP) {
#pragma unroll
      for (int k = 0; k < 4; ++k) asm volatile("" : "=v"(bw[k]));
      if (part == 0) wo_frags(0); else if (is_up) up_frags();
#pragma unroll
      for (int k = 0; k < 4; ++k) PIN(bw[k]);
    }
    if constexpr (v == UPREP + 1) {
      if (is_up) {
        const u32x4* a1 = reinterpret_cast<const u32x4*>(&sm.hb[1024 + (c >> 2 & 1) * 1024 + (c >> 3) * 512 + (g >> 1) * 256 + (c >> 1 & 1) * 32 + (g & 1) * 16 + (c & 1) * 8]);
        f32x4 d = {0.f, 0.f, 0.f, 0.f};
#pragma unroll
        for (int j = 0; j < 4; ++j) d = mfma64(a1[j * 8], bw[j], d, (c & 1) ? 0xeeeeu : 0x4444u);
        float t = par ? d[2] + d[3] : d[0] + d[1];
        t += dpp<XOR8>(t);
        const auto ab = __builtin_amdgcn_permlane16_swap(__float_as_uint(t), __float_as_uint(t), false, false);
        Af = u2f(ab[0]); Bf = u2f(ab[1]);
      }
      PIN(Af); PIN(Bf);
    }
    if constexpr (v == MLS) {
      LDS_BARRIER();
      float mm[8], ll[8];
#pragma unroll
      for (int w = 0; w < 8; ++w) { const f32x2 t = *reinterpret_cast<const f32x2*>(&sm.ml[w][0]); mm[w] = t[0]; ll[w] = t[1]; }
      float Mwg = mm[0], Lwg = 0.f;
#pragma unroll
      for (int w = 1; w < 8; ++w) Mwg = fmaxf(Mwg, mm[w]);
      if (wave == 0) {
#pragma unroll
        for (int w = 0; w < 8; ++w) Lwg = fmaf(__expf(mm[w] - Mwg), ll[w], Lwg);
        if (lane == 0) st16_sc1(wsr, G_ML + ((head * 2 + b) * 4 + part) * 16, __float_as_uint(Mwg), __float_as_uint(Lwg), tag);
      }
      const float fw = __expf(m - Mwg);
      m = Mwg;
#pragma unroll
      for (int bt = 0; bt < NB; ++bt) { pb[bt] = pack_bf(pr[bt] * fw, pr[bt] * fw); PIN(pb[bt]); }
#pragma unroll
      for (int k = 0; k < 4; ++k) { pm[k] = (c >> 1 & 3) == k ? ((c & 1) ? 0xffff0000u : 0x0000ffffu) : 0u; PIN(pm[k]); }
    }
    SB();
    return true;
  });
  asm volatile(".Ltail_start:");
  if (wave != 0) {
    pv_row(IC<SK0 - 1>{});
    WAIT_VM(WV); SB();
    sfor<NL - 1 - WV, NL - 1>(pv_row);
    *LDSP(f32x4, adst0) = acc;
    LDS_BARRIER();
  } else {
    u32x4 xr[8], am[8];
#pragma unroll
    for (int w = 0; w < 8; ++w) am[w] = u32x4{pl8[w] & pm[0], pl8[w] & pm[1], pl8[w] & pm[2], pl8[w] & pm[3]};
#pragma unroll
    for (int w = 0; w < 8; ++w) PIN(am[w]);
    {
      const u32 so = __builtin_amdgcn_readlane(sot, NL - 1);
      gate();
      SB();
#pragma unroll
      for (int w = 0; w < 8; ++w) xr[w] = ld16<AUX_NT>(rv, lane * 16, so ^ ((w & 1) ? col(P, 7) : 0u) ^ ((w & 2) ? col(P, 8) : 0u) ^ ((w & 4) ? col(P, 9) : 0u));
      SB();
    }
    u32 rsum = lds_addr(&sm.red[0][c * 8 + (g & 1) * 4]);
    pv_row(IC<SK0 - 1>{});
    WAIT_VM(WV + 8); SB();
    sfor<NL - 1 - WV, NL - 3>(pv_row);
    if (__builtin_expect(!all_ok((mlg[1] == tag) & (mlg[3] == tag)), 0)) {
#pragma nounroll
      for (int spin = 0; spin < SPIN_MAX; ++spin) {
        CFENCE();
        mlg = ld16<AUX_SC1>(wsr, mlo, 0);
        if (all_ok((mlg[1] == tag) & (mlg[3] == tag))) break;
      }
    }
    merge_factor(mlg);
    sfor<NL - 3, NL - 1>(pv_row);
    LDS_BARRIER();
    f32x4 t[8];
#pragma unroll
    for (int w = 1; w < 8; ++w) t[w] = LDSP(f32x4, rsum)[w * 32];
    acc = ((acc + t[1]) + (t[2] + t[3])) + ((t[4] + t[5]) + (t[6] + t[7]));
    PIN(acc);
    SB();
#pragma unroll
    for (int w = 0; w < 8; ++w) acc = mfma16(am[w], xr[w], acc);
  }
  WAIT_VM(0);
  acc *= fmrg;
  {
    const u32 w0 = min(pack_bf(acc[0], acc[1]), SENT - 1u), w1 = min(pack_bf(acc[2], acc[3]), SENT - 1u);
    const auto e0 = __builtin_amdgcn_permlane16_swap(w0, w0, false, false), e1 = __builtin_amdgcn_permlane16_swap(w1, w1, false, false);
    __builtin_amdgcn_raw_buffer_store_b128(u32x4{e0[0], e1[0], e0[1], e1[1]}, wsr, pso, 0, AUX_SC1);
  }
  st8_sc1(wsr, nto, (u32)wall_clock64() + nadd, tag);
  SB();

  if (part == 2) return;
  if (part == 0) {
    __builtin_amdgcn_s_barrier();
#pragma nounroll
    while (wleft > 0) { until(t1 - (P.PA + wleft * P.PD) * 256); piece(); }
    bool late = false;
    u32x4 og[1];
    f32x4 d[4];
    u32 six = (c & 8) ? 0xeeeeu : 0x4444u;
    PIN(six);
#pragma unroll
    for (int t = 0; t < 4; ++t) { d[t] = f32x4{0.f, 0.f, 0.f, 0.f}; PIN(d[t]); }
    if (wdef) {
      late = true;
      while ((int)(((u32)wall_clock64() - t0) << 8) < t1 - 10 * 256) if (__builtin_amdgcn_s_getreg(7 | (3 << 11)) == 0) { late = false; break; }
      if (__builtin_expect(late, 0)) { until(t1); CFENCE(); og[0] = ld16<AUX_SC1>(wsr, po, 0); SB(); WAIT_VM(1); } else { WAIT_VM(0); }
      CFENCE();
      wo_frags(1);
#pragma unroll
      for (int k = 0; k < 4; ++k) PIN(bw[k]);
      SB();
    }
    if (__builtin_expect(!late, 1)) { until(t1); CFENCE(); og[0] = ld16<AUX_SC1>(wsr, po, 0); }
    SB();
    auto wo_mm = [&]() {
#pragma unroll
      for (int t = 0; t < 4; ++t) d[t] = mfma64(og[0], bw[t], d[t], six);
    };
    auto p_bad = [&]() { return !all_ok(max(max(og[0][0], og[0][1]), max(og[0][2], og[0][3])) != SENT); };
    wo_mm();
    if (__builtin_expect(p_bad(), 0)) {
#pragma nounroll
      for (int spin = 0; spin < SPIN_MAX; ++spin) {
        __builtin_amdgcn_s_sleep(1);
        CFENCE();
        og[0] = ld16<AUX_SC1>(wsr, po, 0);
        if (!p_bad()) break;
      }
#pragma unroll
      for (int t = 0; t < 4; ++t) d[t] = f32x4{0.f, 0.f, 0.f, 0.f};
      wo_mm();
      SB();
      asm volatile(".Lend_d:");
    }
    {
      SB();
      f32x4 y; f32x2 h[4];
#pragma unroll
      for (int t = 0; t < 4; ++t) { h[t] = d[t].xy + d[t].zw; PIN(h[t]); }
#pragma unroll
      for (int t = 0; t < 4; ++t) { y[t] = h[t][0] + h[t][1]; PIN(y[t]); }
      *LDSP(f32x2, rdst) = f32x2{swap32_add(y[0], y[1]), swap32_add(y[2], y[3])};
    }
    LDS_BARRIER();
    if (wave < 2) {
      f32x2 v[4];
#pragma unroll
      for (int w = 0; w < 4; ++w) v[w] = f32x2{LDSP(float, rsrc)[w * 256], LDSP(float, rsrc)[w * 256 + 128]};
      const f32x2 h = (v[0] + v[1]) + (v[2] + v[3]);
      const float y = h[0] + h[1];
      __builtin_amdgcn_raw_buffer_store_b32(min(pack_bf(dpp<0xA0>(y), dpp<0xF5>(y)), SENT - 1u), wsr, xo, 0, AUX_SC1);
      asm volatile(".Lend_a:");
    }
    return;
  }
  if (part == 3) {
    if (!has_w) return;
    u32x4 b2[4];
    {
      __builtin_amdgcn_s_barrier();
      const rsrc_t r2 = mk_rsrc(w2 + (size_t)(blk * 32 + wave * 4) * 512, 4096u);
      const u32 l2 = (u32)(uintptr_t)(&sm.w2b[wave * 4][0]);
#pragma unroll
      for (int n = 0; n < 4; ++n) { until(t1 - (P.QA + (4 - n) * P.QD) * 256); dma16(r2, (lane ^ (n * 2) ^ ((wave & 2) << 2)) * 16, n * 1024, l2 + n * 1024); }
    }
    const u32 tdst = lds_addr(&sm.hb[hpos]);
    const u32 sdst = lds_addr(&sm.stat[wave][lane]);
    const u32 ssrc = lds_addr(&sm.stat[(g >> 1) * 4 + ((lane >> 3) & 3)][(lane & 7) * 4]);
    const u32 adst = lds_addr((c < 4 && (g & 1) == 0) ? &sm.actl[g >> 1][wave * 4 + c] : reinterpret_cast<u16*>(&sm.dummy[lane]));
    const u32 asrc = lds_addr(&sm.actl[lane & 1][g * 8]);
    const u32 apa = lds_addr(&sm.hb[(c >> 3) * 512 + (g >> 1) * 256 + (c >> 1 & 1) * 64 + ((c >> 2) & 1) * 32 + (g & 1) * 16 + (c & 1) * 8]);
    u32 six = (c & 1) ? 0xeeeeu : 0x4444u;
    f32x4 d[2] = {{0.f, 0.f, 0.f, 0.f}, {0.f, 0.f, 0.f, 0.f}};
    PIN(six); PIN(d[0]); PIN(d[1]);
    constexpr u32 xs2 = 16384;
    u32 xoff = dup(G_XP) + near + g * 2048 + ub * 1024 + (wave & 3) * 256 + c * 16;
    u32x4 one;
#pragma unroll
    for (int k = 0; k < 4; ++k) one[k] = ((c & 2) == 0 && (c >> 2) == k) ? ((c & 1) ? 0x3f800000u : 0x00003f80u) : 0u;
    PIN(one);
    u32 ooff = dup(G_OP + (blk * 512 + tid) * 8);
    float Afp = Af, Bfp = Bf;
    PIN(xoff); PIN(ooff); PIN(Afp); PIN(Bfp);
    f32x2 gc = ga * cc1, xc = xin * cc1;
    PIN(ga); PIN(xin); PIN(gc); PIN(xc);
    u32 w2a = (u32)(uintptr_t)(&sm.w2b[0][0]) + (g * 8 + ((lane >> 2) & 3)) * 1024 + (wave ^ (g & 1)) * 128 + (lane & 15) * 8;
    PIN(w2a);
    if (wave == 1) {
      due = t1 + P.N1 * 256;
      const int tn = notices();
      due = tn + P.X2 * 256;
      until(due);
    }
    asm volatile("s_waitcnt vmcnt(0)" ::: "memory");
    __builtin_amdgcn_s_barrier();
    CFENCE();
    u32x4 xg[2] = {ld16<AUX_SC1>(wsr, xoff, 0), ld16<AUX_SC1>(wsr, xoff, xs2)};
    SB();
#pragma unroll
    for (int t = 0; t < 4; ++t) {
      const u32x2 lo = tr_read(w2a ^ (t * 32)), hi = tr_read((w2a ^ (t * 32)) + 4096);
      b2[t] = u32x4{lo[0], lo[1], hi[0], hi[1]};
    }
#pragma unroll
    for (int t = 0; t < 4; ++t) PIN(b2[t]);
    SB();
    const f32x4 z4 = {0.f, 0.f, 0.f, 0.f};
    auto xp_bad = [&]() { return !all_ok(max(max(max(xg[0][0], xg[0][1]), max(xg[0][2], xg[0][3])), max(max(xg[1][0], xg[1][1]), max(xg[1][2], xg[1][3]))) != SENT); };
    f32x4 v = mfma16(one, xg[1], mfma16(one, xg[0], z4));
    if (__builtin_expect(xp_bad(), 0)) {
#pragma nounroll
      for (int spin = 0; spin < SPIN_MAX; ++spin) {
        __builtin_amdgcn_s_sleep(1);
        CFENCE();
        xg[0] = ld16<AUX_SC1>(wsr, xoff, 0); xg[1] = ld16<AUX_SC1>(wsr, xoff, xs2);
        if (!xp_bad()) break;
      }
      v = mfma16(one, xg[1], mfma16(one, xg[0], z4));
      SB();
      asm volatile(".Lend_e:");
    }
    {
      const f32x2 a = v.xy, hc = __builtin_elementwise_fma(gc, a, xc), x1 = __builtin_elementwise_fma(ga, a, xin);
      *LDSP(u32, tdst) = pack_bf(hc[0], hc[1]);
      *LDSP(float, sdst) = swap32_add(x1[0] + x1[1], fmaf(x1[0], x1[0], x1[1] * x1[1]));
    }
    LDS_BARRIER();
    {
      const f32x4 s4 = LDSP(f32x4, ssrc)[0], q4 = LDSP(f32x4, ssrc)[8];
      SB();
      const u32x4 af0 = LDSP(u32x4, apa)[0], af1 = LDSP(u32x4, apa)[16];
      SB();
      const f32x2 s2 = s4.xy + s4.zw, q2 = q4.xy + q4.zw;
      float s = s2[0] + s2[1], q = q2[0] + q2[1];
      SB();
      d[0] = mfma64(af0, bw[0], d[0], six);
      SB();
      s = swap16_add(s, q);
      SB();
      d[1] = mfma64(af0, bw[1], d[1], six);
      SB();
      s += dpp<XOR1>(s); s += dpp<XOR2>(s);
      SB();
      d[0] = mfma64(af1, bw[2], d[0], six);
      SB();
      s += dpp<XOR7>(s); s += dpp<XOR8>(s);
      SB();
      d[1] = mfma64(af1, bw[3], d[1], six);
      SB();
      const auto sq = __builtin_amdgcn_permlane16_swap(__float_as_uint(s), __float_as_uint(s), false, false);
      const float mean = u2f(sq[0]) * (1.f / 512.f);
      const float var = fmaf(u2f(sq[1]), 1.f / 512.f, -mean * mean);
      float ri = __builtin_amdgcn_rsqf(fmaxf(var, 0.f) + 1e-5f);
      PIN(ri); PIN(d[0]); PIN(d[1]);
      SB();
      float y0 = d[0][0] + d[0][1], y1 = d[1][2] + d[1][3], mr = -ri * mean;
      PIN(y0); PIN(y1); PIN(mr);
      float y = y0 + y1, cf = fmaf(mr, Afp, Bfp), rk = ri * -1.44269504f;
      PIN(y); PIN(cf); PIN(rk);
      y = __builtin_bit_cast(float, __builtin_amdgcn_update_dpp(__builtin_bit_cast(int, y), __builtin_bit_cast(int, y), XOR8, 0xa, 0xf, false));
      const float ck = cf * -1.44269504f;
      y = swap16_add(y, y);
      const float ex = __builtin_amdgcn_exp2f(fmaf(y, rk, ck));
      y = fmaf(y, ri, cf);
      const float gu = y * dpp<0x104>(y);
      *LDSP(u16, adst) = (u16)pack_bf(gu * __builtin_amdgcn_rcpf(1.f + ex), 0.f);
    }
    LDS_BARRIER();
    {
      const u32x4 af = *LDSP(u32x4, asrc);
      const f32x4 z = {0.f, 0.f, 0.f, 0.f};
      f32x4 d0 = mfma16(af, b2[0], z), d1 = mfma16(af, b2[1], z), d2 = mfma16(af, b2[2], z), d3 = mfma16(af, b2[3], z);
      asm volatile("" : "+v"(d0), "+v"(d1), "+v"(d2), "+v"(d3));
      const bool g1 = g & 1, g2 = g & 2;
      const float e0 = g1 ? d1[0] : d0[0], e1 = g1 ? d1[1] : d0[1], f0 = g1 ? d3[0] : d2[0], f1 = g1 ? d3[1] : d2[1];
      const u32x2 ov = {pack_bf(g2 ? f0 : e0, g2 ? f1 : e1), tag};
      __builtin_amdgcn_raw_buffer_store_b64(ov, wsr, ooff, near ^ 8192u, AUX_SC1);
      __builtin_amdgcn_raw_buffer_store_b64(ov, wsr, ooff, near, AUX_SC1);
    }
    asm volatile(".Lend_b:");
    return;
  }
  if (wave == 0) {
    if (!all_ok((sg[0][1] == tag) & (sg[0][3] == tag) & (sg[1][1] == tag) & (sg[1][3] == tag))) {
      u32x4 t[2];
      gather16<2>(wsr, G_MOD + (1024 + fd) * 8, 1536 * 8, tag, t);
      sg[0] = t[0]; sg[1] = t[1];
    }
    u32 xa;
    {
      due = t1 + P.N2 * 256;
      const int tn = notices();
      due = tn + P.X3 * 256;
      until(due);
    }
    const bool hb = g & 1;
#pragma nounroll
    for (int spin = 0; spin < SPIN_MAX; ++spin) {
      CFENCE();
      xa = __builtin_amdgcn_raw_buffer_load_b32(wsr, dup(G_XP) + near + (g >> 1) * 16384 + (c >> 2) * 2048 + (g & 1) * 1024 + fd * 2, 0, AUX_SC1);
      if (all_ok(xa != SENT)) break;
      __builtin_amdgcn_s_sleep(1);
    }
    float a0 = lo_f(xa), a1 = hi_f(xa);
    a0 += dpp<0x124>(a0); a1 += dpp<0x124>(a1);
    a0 += dpp<XOR8>(a0); a1 += dpp<XOR8>(a1);
    a0 = swap32_add(a0, a0); a1 = swap32_add(a1, a1);
    f32x2 xv = {rbf(lo_f(sx2) + rbf((hb ? hi_f(sg[0][0]) : lo_f(sg[0][0])) * rbf(a0))), rbf(hi_f(sx2) + rbf((hb ? hi_f(sg[0][2]) : lo_f(sg[0][2])) * rbf(a1)))};
    f32x2 gv = {hb ? hi_f(sg[1][0]) : lo_f(sg[1][0]), hb ? hi_f(sg[1][2]) : lo_f(sg[1][2])};
    PIN(xv); PIN(gv);
    if (wi == 0 && lane == 0) __builtin_amdgcn_raw_buffer_store_b32(tag, wsr, WS_EPOCH, 0, AUX_SC1);
    due = max(due + P.X4 * 256, (int)(((u32)wall_clock64() - t0) << 8) + P.GX * 256);
    const bool last = lane < (NUP - 32) * 4;
    u32 o0 = dup(G_OP + (lane >> 2) * 4096 + fd * 8) + near, o2 = last ? o0 : OOB, tg2 = last ? tag : 0u;
    const rsrc_t rout = mk_rsrc(out, 2048u);
    u32 oo = (lane & 44) == 0 ? (g * 512 + fd) * 2 : OOB;
    const u32 pk = (c & 10) ? 0u : ((c & 4) ? 0x3f800000u : 0x00003f80u);
    u32x4 one = {(c & 1) ? 0u : pk, 0u, (c & 1) ? pk : 0u, 0u};
    PIN(o0); PIN(o2); PIN(tg2); PIN(oo); PIN(one);
    __builtin_amdgcn_raw_buffer_store_b32(pack_bf(xv[0], xv[1]), rout, oo, 0, AUX_SC1);
    u32 s1_ = 131072, s2_ = 262144;
    asm volatile("" : "+s"(s1_), "+s"(s2_));
    until(due);
    f32x4 sv;
#pragma nounroll
    for (int spin = 0; spin < SPIN_MAX; ++spin) {
      CFENCE();
      const u32x4 og[3] = {ld16<AUX_SC1>(wsr, o0, 0), ld16<AUX_SC1>(wsr, o0, s1_), ld16<AUX_SC1>(wsr, o2, s2_)};
      sv = f32x4{0.f, 0.f, 0.f, 0.f};
#pragma unroll
      for (int k = 0; k < 3; ++k) sv = mfma16(one, og[k], sv);
      SB();
      u32 bad = ((og[0][1] ^ tag) | (og[0][3] ^ tag) | (og[1][1] ^ tag)) | ((og[1][3] ^ tag) | (og[2][1] ^ tg2) | (og[2][3] ^ tg2));
      PIN(bad);
      SB();
      float s0 = sv[0] + dpp<0x124>(sv[0]), s1 = sv[1] + dpp<0x124>(sv[1]);
      PIN(s0); PIN(s1);
      s0 += dpp<XOR8>(s0); s1 += dpp<XOR8>(s1);
      PIN(s0); PIN(s1);
      __builtin_amdgcn_raw_buffer_store_b32(pack_bf(fmaf(gv[0], s0, xv[0]), fmaf(gv[1], s1, xv[1])), rout, oo, 0, AUX_SC1);
      SB();
      if (all_ok(bad == 0u)) break;
      __builtin_amdgcn_s_sleep(1);
    }
    asm volatile(".Lend_c:");
  }
}

extern "C" void kernel_launch(void* const* d_in, const int* in_sizes, int n_in,
                              void* d_out, int out_size, void* d_ws, size_t ws_size,
                              hipStream_t stream) {
  fused_kernel<<<256, 512, 0, stream>>>((const u16*)d_in[1], (const u16*)d_in[2], (const u16*)d_in[4], (const u16*)d_in[3], (const u16*)d_in[10], (const u16*)d_in[6], (const u16*)d_in[7],
                                        (unsigned char*)d_ws, (const u16*)d_in[0], (const u16*)d_in[9], (const u16*)d_in[8], (u16*)d_out);
}
```

```cpp
#include <hip/hip_runtime.h>
#include <stdint.h>


typedef unsigned int u32;
typedef unsigned short u16;
typedef unsigned long long u64;
typedef __attribute__((ext_vector_type(4))) unsigned u32x4;
typedef __attribute__((ext_vector_type(2))) unsigned u32x2;
typedef __attribute__((ext_vector_type(2))) __bf16 bf16x2;
typedef __attribute__((ext_vector_type(4))) float f32x4;
typedef __attribute__((ext_vector_type(2))) float f32x2;
typedef __amdgpu_buffer_rsrc_t rsrc_t;

constexpr int NH = 32, SK = 16384, DH = 128, WIN = 4096, FF = 1376;
struct Prm { int E0, ES, E1, E2, E2C, EU, TEND, DT_UP, L0, L1, X1, X2, X3, X4, NE, FE, M0, M1, M2, M3, M4, GS, GX, N1, N2, GA, WD, DT_WO, LW, LU, EW, PM, NF, NR, C0, A0, AG, AC, TS, TW, TK, TG, GB, B0, BG, BF, D0, DG, TC, LM, C1, F0, FG, FF_, PA, PD, QA, QD, RT, SA, SG, SC, PV, W[9]; };
constexpr Prm PRM0 = {32, 20, 32, 32, 60, 60, 1960, 76, 12, 27, 75, 63, 96, 72, 3, 8, 0x2511f55b, 0x2b7425d5, 0x6017665, 0x17e0b9ec, 0x31944305, 62, 28, 29, 58, 50, 4, 106, 50, 28, 10, 0x0, 12, 14, 3, 36, 4, 4, 509, 215, 80, -2, 6, 69, 10, 8, 8, 6, 87, 120, 2, 58, 9, 4, 52, 14, 9, 18, 12, 45, 10, 21, 0, 0x77777777, 0x77777777, 0x77777777, 0x77777777, 0x77777777, 0x44444477, 0x44444444, 0x44444444, 0x44444444};
constexpr int NL = 37;
constexpr int SK0 = 21, SK1 = 32;
constexpr int NCMIN = (NL - SK1 + SK0) * 8;
constexpr int WK = 8, WV = 5;
constexpr int WUSE = 12, LNS = 7;
constexpr int QP = 14, QD = 4;
constexpr int VQ = 6;
constexpr int NSL = (NL + 7) / 8;
constexpr int VS = VQ + NSL;
constexpr int MLS = VS + 2;
constexpr int VSIDE = MLS - 5, UPREP = MLS + 6;
constexpr int CPF = 17;
constexpr int SMO = 1;
constexpr int RVS = 5, RVP = 8, RVL = 14, RVU = 18;
constexpr int RPS = 23, RPP = 27;
constexpr int PRB = 12;
constexpr int TBS = 7, TBP = 10, TBL = 13, TBU = 17;
constexpr int window(int s, int w) {
  int n = w;
  for (int u = s - w + 1; u <= s; ++u) n += u == SMO ? 6 : (u == TBL || u == SK1 || u == NL + CPF || u == NL + RVL || u == NL + SK1 ? 1 : (u == QP || u == NL + VQ - 4 ? 2 : (u == NL + VSIDE ? 4 : 0)));
  return n;
}
constexpr int cons_end(int v) {
  const int a = v - WV + 1, c = (v - MLS) * 4;
  const int m = a < c ? a : c;
  return m < 0 ? 0 : (m > NL ? NL : m);
}
constexpr u32 col(const Prm& P, int j) { const int m[6] = {P.M0, P.M1, P.M2, P.M3, P.M4, P.PM}; return (u32)(m[j / 3] >> (j % 3 * 10) & 1023) << 10; }
constexpr u32 first_col(const Prm& P, int k) {
  if (k < 4) return col(P, 12 + k) | 1u << (24 + k);
  if (k < 6) return col(P, k - 4);
  if (k < 8) return col(P, 4 + k) | 1u << (16 + k);
  if (k < 11) return col(P, k - 1);
  return (col(P, 2) ^ col(P, 3) ^ col(P, 4) ^ col(P, 5) ^ col(P, 6) ^ col(P, 10) ^ col(P, 11)) | 0xf00000u;
}
constexpr bool one_to_one(const Prm& P) {
  u32 c[10] = {};
  for (int j = 0; j < 10; ++j) c[j] = col(P, j);
  for (int j = 0; j < 10; ++j) {
    if (c[j] == 0) return false;
    const u32 low = c[j] & (0u - c[j]);
    for (int k = j + 1; k < 10; ++k) if (c[k] & low) c[k] ^= c[j];
  }
  return true;
}
static_assert(one_to_one(PRM0), "every KB of the window exactly once");
struct Tab { u32 e[512]; };
constexpr Tab make_tab(const Prm& P) {
  Tab T{};
  for (int bid = 0; bid < 256; ++bid) {
    const int xcd = bid & 7, part = bid >> 4 & 3, i8 = (bid >> 3 & 1) | bid >> 6 << 1, wi = i8 * 8 + xcd;
    const bool has_mod = wi < 48, has_up = wi >= 21;
    const int cb0 = (i8 + P.C0) & 7, cb1 = (i8 + P.C1) & 7, ms = wi - 20;
    const int mi = wi < 8 ? wi : (wi < 16 ? 8 + ((xcd + 4) & 7) : (wi < 20 ? (wi - 14) * 8 + 3 : 16 + ms / 7 * 8 + ms % 7 + (ms % 7 >= 3 ? 1 : 0)));
    const int ui = xcd * 5 + (xcd > 5 ? xcd - 5 : 0) + (i8 == 6 ? 0 : (xcd >= 5 ? 1 : 0) + (i8 == 7 ? 4 : i8 - 2));
    const bool edge = i8 >= 6;
    const int wd = cb0 == 3 ? 0 : P.WD;
    const int e[4] = {P.E0 - 4 * wd + (cb0 == 3 ? P.ES : 0), P.E1 + (cb1 == 3 ? P.ES : 0), has_mod ? (wi < 16 ? P.E2C : P.E2) + ((mi & 7) == 3 ? P.ES : 0) : 0, has_up ? P.EU : 0};
    int tw[4] = {}, win[4] = {}, span[4] = {}, k0[4] = {};
    for (int k = 0; k < 4; ++k) {
      const int np = k == 0 ? 8 - wd : 8;
      tw[k] = np ? e[k] * P.TW / (16 * np) : 0;
      k0[k] = P.NF + tw[k] * np / 2 > P.TK ? P.NF + tw[k] * np / 2 : P.TK;
      span[k] = P.TEND - k0[k] - P.TG - (k == 0 ? (wd ? P.DT_WO : P.A0) : (k == 3 && has_up ? P.DT_UP : 0));
      win[k] = span[k] - (k == 3 && has_up ? P.GA + (edge ? P.LM : 0) : 0);
    }
    int n[4] = {NCMIN / 8, NCMIN / 8, NCMIN / 8, NCMIN / 8};
    for (int left = 128 - NCMIN / 2; left > 0; --left) {
      int best = -1;
      for (int k = 3; k >= 0; --k)
        if (n[k] < NL && (best < 0 || n[k] * win[best] < n[best] * win[k])) best = k;
      ++n[best];
    }
    const int c[4] = {0, n[0] - 1, n[0] + n[1] - 2, n[0] + n[1] + n[2] - 3};
    const int nl = n[part];
    const bool has_w = part < 2 || (part == 2 ? has_mod : has_up);
    const int blk = !has_w ? 0 : (part == 0 ? cb0 : (part == 1 ? xcd * 8 + cb1 : (part == 2 ? mi : ui)));
    T.e[2 * bid] = (u32)(c[part] | nl << 7 | blk << 13 | (has_w ? 1 : 0) << 19 | (part == 0 ? wd : 0) << 20) | (u32)tw[part] << 24;
    T.e[2 * bid + 1] = (u32)(span[part] * P.TS / (4 * (nl - 1)) | span[part] * (1024 - P.TS) / (4 * (nl - 1)) << 16);
  }
  return T;
}
constexpr int step_range(const Prm& P, bool mx_) {
  int mx = 0, mn = 1 << 20;
  const Tab T = make_tab(P);
  for (int bid = 0; bid < 256; ++bid) { const int n = T.e[2 * bid] >> 7 & 63; mx = n > mx ? n : mx; mn = n < mn ? n : mn; }
  return mx_ ? mx : mn;
}
extern "C" __attribute__((used, visibility("protected"))) __constant__ const Tab wg_tab = make_tab(PRM0);
static_assert(step_range(PRM0, true) <= NL && step_range(PRM0, false) >= NCMIN / 8, "a wave's key range does not fit its load steps");
static_assert(RVU < UPREP && UPREP + 1 < SK0 && QP + QD + 1 < SK0 && cons_end(SK0 - 1) == SK0 - WV && NL - SK1 == WV, "steps every wave runs");
constexpr bool timers_long(const Prm& P) {
  const Tab T = make_tab(P);
  for (int bid = 48; bid < 64; ++bid) if (SK0 + NL - (int)(T.e[2 * bid] >> 7 & 63) > RPS) return false;
  return true;
}
static_assert(timers_long(PRM0), "steps of the second report");
static_assert(PRM0.WD <= 4 && PRM0.NE <= NL - SK1, "late pieces: the second half of the block at most");
constexpr int NUP = FF / 32;
constexpr int SPIN_MAX = 1 << 17;

constexpr u32 WS_EPOCH = 0;
constexpr u32 G_TB = 1024;
constexpr u32 G_MOD = 4096;
constexpr u32 G_Q = G_MOD + 3072 * 8;
constexpr u32 G_ML = G_Q + 4096 * 8;
constexpr u32 G_U = G_ML + 256 * 16;
constexpr u32 G_P = 0;
constexpr u32 G_XP = G_P + 128 * 256;
constexpr u32 G_OP = G_XP + 8 * 512 * 4;
constexpr u32 G_NT = G_OP + 43 * 512 * 8;
constexpr u32 G_PB = G_OP + 44 * 512 * 8;
constexpr u32 G_VT = G_U + 2 * (G_PB + 8192);
constexpr u32 WS_END = G_VT + 2048;
__device__ __forceinline__ u32 dup(u32 o) { return G_U + ((o >> 13) << 14) + (o & 8191u); }
constexpr u32 SENT = 0xffffffffu;
constexpr u32 OOB = 0x40000000u;

constexpr int AUX_NT = 2, AUX_SC1 = 16;

__device__ __forceinline__ float lo_f(u32 v) { return __uint_as_float(v << 16); }
__device__ __forceinline__ float hi_f(u32 v) { return __uint_as_float(v & 0xffff0000u); }
__device__ __forceinline__ float rbf(float x) { return (float)(__bf16)x; }
__device__ __forceinline__ u32 pack_bf(float lo, float hi) {
  bf16x2 r; r[0] = (__bf16)lo; r[1] = (__bf16)hi; return __builtin_bit_cast(u32, r);
}
__device__ __forceinline__ float dot2(u32 k, u32 q, float acc) {
  return __builtin_amdgcn_fdot2_f32_bf16(__builtin_bit_cast(bf16x2, k), __builtin_bit_cast(bf16x2, q), acc, false);
}
template <int CTRL> __device__ __forceinline__ float dpp(float x) {
  return __builtin_bit_cast(float, __builtin_amdgcn_mov_dpp(__builtin_bit_cast(int, x), CTRL, 0xf, 0xf, true));
}
constexpr int XOR1 = 0xB1, XOR2 = 0x4E, XOR7 = 0x141, XOR8 = 0x128;
__device__ __forceinline__ float swap32_add(float a, float b) {
  auto t = __builtin_amdgcn_permlane32_swap(__float_as_uint(a), __float_as_uint(b), false, false);
  return __uint_as_float(t[0]) + __uint_as_float(t[1]);
}
__device__ __forceinline__ float swap16_add(float a, float b) {
  auto t = __builtin_amdgcn_permlane16_swap(__float_as_uint(a), __float_as_uint(b), false, false);
  return __uint_as_float(t[0]) + __uint_as_float(t[1]);
}
__device__ __forceinline__ float wave_sum(float x) {
  x += dpp<XOR1>(x); x += dpp<XOR2>(x); x += dpp<XOR7>(x); x += dpp<XOR8>(x);
  x = swap16_add(x, x);
  return swap32_add(x, x);
}
__device__ __forceinline__ float wave_max(float x) {
  x = fmaxf(x, dpp<XOR1>(x)); x = fmaxf(x, dpp<XOR2>(x)); x = fmaxf(x, dpp<XOR7>(x)); x = fmaxf(x, dpp<XOR8>(x));
  auto s = __builtin_amdgcn_permlane16_swap(__float_as_uint(x), __float_as_uint(x), false, false);
  x = fmaxf(__uint_as_float(s[0]), __uint_as_float(s[1]));
  auto t = __builtin_amdgcn_permlane32_swap(__float_as_uint(x), __float_as_uint(x), false, false);
  return fmaxf(__uint_as_float(t[0]), __uint_as_float(t[1]));
}
template <int CTRL> __device__ __forceinline__ float sel_add(float lo, float hi, bool up) {
  const float t1 = lo + dpp<CTRL>(lo), t2 = hi + dpp<CTRL>(hi);
  return up ? t2 : t1;
}
template <int I> __device__ __forceinline__ float bc16(float x) {
  return __builtin_bit_cast(float, __builtin_amdgcn_update_dpp(0, __builtin_bit_cast(int, x), 0x150 + I, 0xf, 0xf, false));
}
__device__ __forceinline__ float bcast16(float x, int i) {
  switch (i) {
    case 0: return bc16<0>(x); case 1: return bc16<1>(x); case 2: return bc16<2>(x); case 3: return bc16<3>(x);
    case 4: return bc16<4>(x); case 5: return bc16<5>(x); case 6: return bc16<6>(x); case 7: return bc16<7>(x);
    case 8: return bc16<8>(x); case 9: return bc16<9>(x); case 10: return bc16<10>(x); case 11: return bc16<11>(x);
    case 12: return bc16<12>(x); case 13: return bc16<13>(x); case 14: return bc16<14>(x); default: return bc16<15>(x);
  }
}
__device__ __forceinline__ float silu_f(float x) { return x * __builtin_amdgcn_rcpf(1.f + __expf(-x)); }

template <int AUX> __device__ __forceinline__ u32x4 ld16(rsrc_t r, u32 voff, u32 soff) {
  return __builtin_amdgcn_raw_buffer_load_b128(r, voff, soff, AUX);
}
template <int AUX> __device__ __forceinline__ u32x2 ld8(rsrc_t r, u32 voff, u32 soff) {
  return __builtin_amdgcn_raw_buffer_load_b64(r, voff, soff, AUX);
}
__device__ __forceinline__ void st8_sc1(rsrc_t r, u32 voff, u32 v, u32 tag) {
  __builtin_amdgcn_raw_buffer_store_b64(u32x2{v, tag}, r, voff, 0, AUX_SC1);
}
__device__ __forceinline__ void st16_sc1(rsrc_t r, u32 voff, u32 v0, u32 v1, u32 tag) {
  __builtin_amdgcn_raw_buffer_store_b128(u32x4{v0, tag, v1, tag}, r, voff, 0, AUX_SC1);
}
__device__ __forceinline__ rsrc_t mk_rsrc(const void* p, u32 bytes) {
  return __builtin_amdgcn_make_buffer_rsrc(const_cast<void*>(p), 0, bytes, 0x00020000);
}
#define WAIT_VM(n) __builtin_amdgcn_s_waitcnt(((n) & 15) | (((n) >> 4) << 14) | (7 << 4) | (15 << 8))
#define SB() __builtin_amdgcn_sched_barrier(0)
#define CFENCE() asm volatile("" ::: "memory")
__device__ __forceinline__ bool all_ok(bool ok) { return __builtin_amdgcn_ballot_w64(!ok) == 0ull; }

template <int N> __device__ __forceinline__ void gather16(rsrc_t ws, u32 voff, u32 stride, u32 tag, u32x4 (&g)[N]) {
#pragma nounroll
  for (int spin = 0; spin < SPIN_MAX; ++spin) {
    CFENCE();
    bool ok = true;
#pragma unroll
    for (int i = 0; i < N; ++i) g[i] = ld16<AUX_SC1>(ws, voff + i * stride, 0);
#pragma unroll
    for (int i = 0; i < N; ++i) ok &= (g[i][1] == tag) & (g[i][3] == tag);
    if (all_ok(ok)) break;
    __builtin_amdgcn_s_sleep(8);
  }
}
template <int N> __device__ __forceinline__ void gather8(rsrc_t ws, u32 voff, u32 stride, u32 tag, u32 (&g)[N], bool act = true) {
#pragma nounroll
  for (int spin = 0; spin < SPIN_MAX; ++spin) {
    CFENCE();
    bool ok = true;
    u32x2 t[N];
#pragma unroll
    for (int i = 0; i < N; ++i) t[i] = ld8<AUX_SC1>(ws, voff + i * stride, 0);
#pragma unroll
    for (int i = 0; i < N; ++i) { ok &= (t[i][1] == tag); g[i] = t[i][0]; }
    if (all_ok(ok | !act)) break;
    __builtin_amdgcn_s_sleep(8);
  }
}

struct Smem {
  float red[8][128];
  float ml[8][2];
  float r2[64 * 20];
  u16 hb[3 * 1024];
  float stat[8][64];
  float red2[8][128];
  u32 dummy[64];
  u32x2 bc, bc2;
  u32 bc3, bc4;
  u32x4 zero[4];
  float pl[4][8];
  u32 mflag[8];
  u32x4 wbuf[8][8][64];
  __attribute__((aligned(256))) u16 w2b[32][512];
  u16 actl[2][32];
};

#define PIN(x) asm volatile("" : "+v"(x))
#define LDS_BARRIER() do { __builtin_amdgcn_s_waitcnt(0xc07f); __builtin_amdgcn_s_barrier(); CFENCE(); } while (0)
#define WAVE_SYNC() do { __builtin_amdgcn_fence(__ATOMIC_RELEASE, "wavefront"); __builtin_amdgcn_wave_barrier(); __builtin_amdgcn_fence(__ATOMIC_ACQUIRE, "wavefront"); } while (0)

__device__ __forceinline__ void dma16(rsrc_t r, u32 voff, u32 soff, u32 lds_dst) {
  u32 keep;
  asm volatile("s_mov_b32 %0, m0\n\ts_mov_b32 m0, %4\n\ts_nop 4\n\tbuffer_load_dwordx4 %1, %2, %3 offen nt lds\n\ts_mov_b32 m0, %0"
               : "=&s"(keep) : "v"(voff), "s"(r), "s"(soff), "s"(lds_dst) : "memory");
}
__device__ __forceinline__ void ld16_into(u32x4& d, rsrc_t r, u32 voff, u32 soff) {
  asm volatile("s_nop 4\n\tbuffer_load_dwordx4 %0, %1, %2, %3 offen nt" : "+v"(d) : "v"(voff), "s"(r), "s"(soff) : "memory");
}
__device__ __forceinline__ u32 perm_lo(u32 a, u32 b) { return __builtin_amdgcn_perm(b, a, 0x05040100u); }
__device__ __forceinline__ u32 perm_hi(u32 a, u32 b) { return __builtin_amdgcn_perm(b, a, 0x07060302u); }
__device__ __forceinline__ float rdl(float x, int l) { return __int_as_float(__builtin_amdgcn_readlane(__float_as_int(x), l)); }

typedef __attribute__((ext_vector_type(8))) __bf16 bf16x8;
__device__ __forceinline__ f32x4 mfma16(u32x4 a, u32x4 b, f32x4 c) {
  return __builtin_amdgcn_mfma_f32_16x16x32_bf16(__builtin_bit_cast(bf16x8, a), __builtin_bit_cast(bf16x8, b), c, 0, 0, 0);
}
typedef __attribute__((ext_vector_type(8))) unsigned u32x8;
typedef __attribute__((ext_vector_type(16))) __bf16 bf16x16;
__device__ __forceinline__ f32x4 mfma64(u32x4 a, u32x8 b, f32x4 c, u32 idx) {
  return __builtin_amdgcn_smfmac_f32_16x16x64_bf16(__builtin_bit_cast(bf16x8, a), __builtin_bit_cast(bf16x16, b), c, idx, 0, 0);
}
__device__ __forceinline__ float sum8(const float* p) {
  const f32x4 a = *reinterpret_cast<const f32x4*>(p), b = *reinterpret_cast<const f32x4*>(p + 4);
  return ((a[0] + b[0]) + (a[1] + b[1])) + ((a[2] + b[2]) + (a[3] + b[3]));
}
__device__ __forceinline__ float ln_ri(float var) {
  return __builtin_amdgcn_rcpf(rbf(__builtin_amdgcn_sqrtf(rbf(rbf(fmaxf(var, 0.f)) + 1e-5f))));
}
__device__ __forceinline__ void ln_stats2(const float (&x0)[8], const float (&x1)[8], float& mb0, float& ri0, float& mb1, float& ri1) {
  const float k0 = rdl(x0[0], 0), k1 = rdl(x1[0], 0);
  float s0 = 0.f, s1 = 0.f, q0 = 0.f, q1 = 0.f;
#pragma unroll
  for (int k = 0; k < 8; ++k) {
    const float d0 = x0[k] - k0, d1 = x1[k] - k1;
    s0 += d0; s1 += d1; q0 = fmaf(d0, d0, q0); q1 = fmaf(d1, d1, q1);
  }
  float t = swap16_add(swap32_add(s0, s1), swap32_add(q0, q1));
  t += dpp<XOR1>(t); t += dpp<XOR2>(t); t += dpp<XOR7>(t); t += dpp<XOR8>(t);
  auto u = __builtin_amdgcn_permlane16_swap(__float_as_uint(t), __float_as_uint(t), false, false);
  const float ms = __uint_as_float(u[0]) * (1.f / 512.f), mq = __uint_as_float(u[1]) * (1.f / 512.f);
  const float ri = ln_ri(fmaf(-ms, ms, mq));
  mb0 = rbf(k0 + rdl(ms, 0)); mb1 = rbf(k1 + rdl(ms, 32));
  ri0 = rdl(ri, 0); ri1 = rdl(ri, 32);
}
template <int I> struct IC { static constexpr int v = I; };
template <int I, int N, class F> __device__ __forceinline__ void sfor(F&& f) {
  if constexpr (I < N) { if (f(IC<I>{})) sfor<I + 1, N>(f); }
}
typedef __attribute__((ext_vector_type(4))) short s16x4;
typedef __attribute__((address_space(3))) s16x4* lds_s16x4p;
__device__ __forceinline__ u32x2 tr_read(u32 lds_addr) {
  return __builtin_bit_cast(u32x2, __builtin_amdgcn_ds_read_tr16_b64_v4i16((lds_s16x4p)lds_addr));
}
__device__ __forceinline__ u32 next_tag(u32 e) { return ((e | 0x40004000u) + 1u) & ~0x40004000u; }
__device__ __forceinline__ float u2f(u32 v) { return __uint_as_float(v); }
#define LDSP(T, a) ((__attribute__((address_space(3))) T*)(a))
template <class T> __device__ __forceinline__ u32 lds_addr(T* p) { u32 a = (u32)(uintptr_t)p; PIN(a); return a; }

#define TAB_LOAD "s_add_u32 s30, s28, wg_tab@rel32@lo+4\ns_addc_u32 s31, s29, wg_tab@rel32@hi+12\nglobal_load_dwordx2 %[te], %[va], s[30:31]\n"
#define FC(k) ".Lfc" #k
#define FX(r, bit, k) "s_bitcmp1_b32 %[" #r "], " #bit "\ns_cselect_b32 %[sb], " FC(k) ", 0\ns_xor_b32 %[sa], %[sa], %[sb]\n"
__global__ void __launch_bounds__(512, 2) fused_kernel(const u16* kc_, const u16* vc_, const u16* wo_, const u16* wq_, const u16* wmod_, const u16* w1_, const u16* w3_,
                                                       unsigned char* ws_, const u16* x_, const u16* cnd_, const u16* w2_, u16* out_) {
  __shared__ __attribute__((aligned(256))) Smem sm;
  const u64 a_ka = (u64)__builtin_amdgcn_kernarg_segment_ptr();
  const int bid = blockIdx.x, tid = threadIdx.x;
  u64 wa, t64; u32x2 te; int tid0;
  u32 ar[14];
  u32 tch2, sa_, sb_, va_;
  u32x4 kr[NL], vr[NL];
  asm volatile(".set .Lfc0, %0\n.set .Lfc1, %1\n.set .Lfc2, %2\n.set .Lfc3, %3\n.set .Lfc4, %4\n.set .Lfc5, %5\n.set .Lfc6, %6\n.set .Lfc7, %7\n.set .Lfc8, %8\n.set .Lfc9, %9\n.set .Lfc10, %10\n.set .Lfc11, %11"
    :: "n"(first_col(PRM0, 0)), "n"(first_col(PRM0, 1)), "n"(first_col(PRM0, 2)), "n"(first_col(PRM0, 3)), "n"(first_col(PRM0, 4)), "n"(first_col(PRM0, 5)),
       "n"(first_col(PRM0, 6)), "n"(first_col(PRM0, 7)), "n"(first_col(PRM0, 8)), "n"(first_col(PRM0, 9)), "n"(first_col(PRM0, 10)), "n"(first_col(PRM0, 11)));
  asm volatile(
    "s_load_dwordx16 s[4:19], %[ka], 0x0\ns_load_dwordx8 s[20:27], %[ka], 0x40\n"
    "s_memrealtime %[t]\n"
    "v_lshlrev_b32 %[va], 3, %[bid]\n"
    "s_getpc_b64 s[28:29]\n.Lcpf_first:\n"
    TAB_LOAD
    "s_cmp_gt_u32 %[bid], 15\ns_cbranch_scc1 .Lno_cpf\n"
    "s_mov_b32 %[sa], .Lend_a-.Lcpf_first\ns_max_i32 %[sa], %[sa], .Lend_b-.Lcpf_first\ns_max_i32 %[sa], %[sa], .Lend_c-.Lcpf_first\ns_max_i32 %[sa], %[sa], .Lend_d-.Lcpf_first\ns_max_i32 %[sa], %[sa], .Lend_e-.Lcpf_first\n"
    "v_lshlrev_b32 %[va], 6, %[tid]\nv_min_u32 %[va], %[sa], %[va]\nglobal_load_dword %[pf], %[va], s[28:29]\n"
    ".Lno_cpf:\n"
    "v_readfirstlane_b32 %[t0], %[tid]\nv_and_b32 %[va], 63, %[tid]\n"
    "s_mov_b32 %[sa], " FC(11) "\n"
    FX(bid, 0, 0) FX(bid, 1, 1) FX(bid, 2, 2) FX(bid, 3, 3) FX(bid, 4, 4) FX(bid, 5, 5) FX(bid, 6, 6) FX(bid, 7, 7) FX(t0, 6, 8) FX(t0, 7, 9) FX(t0, 8, 10)
    "v_lshl_add_u32 %[va], %[va], 4, %[sa]\n"
    "s_waitcnt lgkmcnt(0)\nglobal_load_dwordx4 %[k0], %[va], s[4:5] nt\nglobal_load_dwordx4 %[v0], %[va], s[6:7] nt\n"
    "s_bitcmp1_b32 %[t0], 6\ns_cselect_b64 s[14:15], s[16:17], s[14:15]\n"
    "s_bitcmp1_b32 %[bid], 4\ns_cselect_b64 s[8:9], s[10:11], s[8:9]\ns_cselect_b64 s[12:13], s[14:15], s[12:13]\ns_bitcmp1_b32 %[bid], 5\ns_cselect_b64 %[wa], s[12:13], s[8:9]"
    : "=&{s4}"(ar[0]), "=&{s5}"(ar[1]), "=&{s6}"(ar[2]), "=&{s7}"(ar[3]), "=&{s18}"(ar[4]), "=&{s19}"(ar[5]), "=&{s20}"(ar[6]), "=&{s21}"(ar[7]), "=&{s22}"(ar[8]), "=&{s23}"(ar[9]),
      "=&{s24}"(ar[10]), "=&{s25}"(ar[11]), "=&{s26}"(ar[12]), "=&{s27}"(ar[13]), [wa] "=&s"(wa), [t0] "=&s"(tid0), [t] "=&s"(t64), [te] "=&v"(te), [sa] "=&s"(sa_), [sb] "=&s"(sb_),
      [va] "=&v"(va_), [pf] "=&v"(tch2), [k0] "=&v"(kr[0]), [v0] "=&v"(vr[0])
    : [ka] "s"(a_ka), [bid] "s"(bid), [tid] "v"(tid) : "s8", "s9", "s10", "s11", "s12", "s13", "s14", "s15", "s16", "s17", "s28", "s29", "s30", "s31", "scc", "memory");
  asm volatile("" : "+s"(ar[0]), "+s"(ar[1]), "+s"(ar[2]), "+s"(ar[3]), "+s"(ar[4]), "+s"(ar[5]), "+s"(ar[6]), "+s"(ar[7]), "+s"(ar[8]), "+s"(ar[9]), "+s"(ar[10]), "+s"(ar[11]), "+s"(ar[12]), "+s"(ar[13]));
  asm volatile("" : "+s"(wa), "+s"(t64), "+s"(tid0), "+s"(sa_));
  const u32 tka = (u32)wall_clock64();
  __builtin_assume(tid >= 0 && tid < 512 && tid0 >= 0 && tid0 < 512 && bid >= 0 && bid < 256);
  constexpr Prm P = PRM0;
  const u32 t0 = tka + min(max((int)(tka - (u32)t64) - P.SA, 0) * P.SG >> 4, P.SC);
  const int TEND = P.TEND;
  const int xcd = bid & 7, part = bid >> 4 & 3;
  const int lane = tid & 63, wave = tid0 >> 6;
  SB();
  if ((bid >> 4) == 2) {
    const u32 o = tid < 384 ? G_U + (bid & 15) * 6144 + tid * 16 : OOB;
    __builtin_amdgcn_raw_buffer_store_b128(u32x4{SENT, SENT, SENT, SENT}, mk_rsrc((void*)(ar[4] | (u64)ar[5] << 32), WS_END), o, 0, AUX_SC1);
  }
  SB();
  const bool p3 = part == 3;
  u32 pitch = (u32)(0x005600c001000020ull >> (part * 16) & 0xffff) * 32u;
  const int sh = p3 ? 2 : 3;
  u32 wv0 = (u32)((p3 ? (wave >> 1) * 128 : wave * 64) + (part == 0 ? xcd * 512 : 0) + (lane >> sh)) * pitch + ((lane & (p3 ? 3 : 7)) ^ (lane >> (p3 ? 5 : 3))) * 16u;
  const u32 wimg = (u32)(uintptr_t)(&sm.wbuf[0][0][0]);
  u32 wsz = (part == 0 ? 4096u : 512u) * pitch, wld = wimg + (p3 ? (wave & 1) * 32768 + (wave >> 1) * 8192 : wave * 8192);
  PIN(wv0);
  asm volatile("" : "+s"(wsz), "+s"(wld), "+s"(pitch));
  auto karg = [&](int i) { return ar[2 * i] | (u64)ar[2 * i + 1] << 32; };
  const u64 a_kc = karg(0), a_vc = karg(1);
  unsigned char* ws = (unsigned char*)karg(2);
  const u16 *x = (const u16*)karg(3), *cnd = (const u16*)karg(4), *w2 = (const u16*)karg(5);
  u16* out = (u16*)karg(6);
  const rsrc_t wsr = mk_rsrc(ws, WS_END);
  asm volatile("s_waitcnt vmcnt(3)\n\ts_cmp_eq_u32 %1, 2\n\ts_cbranch_scc1 1f\n\ts_waitcnt vmcnt(2)\n1:" : "+v"(te) : "s"(bid >> 4) : "scc");
  const u32 te0 = __builtin_amdgcn_readfirstlane(te[0]), te1 = __builtin_amdgcn_readfirstlane(te[1]);
  const int bl0 = te0 & 127, nl = te0 >> 7 & 63, blk = te0 >> 13 & 63;
  const bool has_w = te0 >> 19 & 1;
  const rsrc_t rw = mk_rsrc((const void*)wa, wsz);
  const u32 wstep = pitch << (6 - sh), wflip = (p3 || part == 0) ? 32u : 0u, wflip0 = part == 0 ? 32u : 0u;
  u32 wso = blk << (sh + 4);
  int wleft = 8;
  auto piece = [&]() {
    dma16(rw, wv0, wso, wld);
    wso += wstep; wld += 1024;
    wv0 ^= (wleft & 1) ? wflip : wflip0;
    --wleft;
  };
  SB();
  const int b = bid >> 3 & 1, head = xcd * 4 + (bid >> 6 ^ 3);
  const int g = lane >> 4, c = lane & 15;
  const int i8 = b | bid >> 6 << 1, wi = i8 * 8 + xcd;
  const bool is_up = p3 && has_w, modc = part == 2 && wi < 16;
  const size_t base = ((size_t)(b * NH + head) * SK + (SK - WIN)) * DH;
  const rsrc_t rk = mk_rsrc((const u16*)a_kc + base, WIN * DH * 2), rv = mk_rsrc((const u16*)a_vc + base, WIN * DH * 2);
  int tau = te1 & 0xffff;
  const int tauw = te0 >> 24 << 7, wdef = te0 >> 20 & 15;
  int due = P.NF * 256, now = 0;
  auto gate = [&]() {
    while (now < due) { now = (int)(((u32)wall_clock64() - t0) << 8); if (now < due) __builtin_amdgcn_s_sleep(1); }
    due += tau;
  };
  auto until = [&](int t) { while ((int)(((u32)wall_clock64() - t0) << 8) < t); };
  const u32 pslot = dup(G_P + (head & 15) * 2048 + (b * 4 + part) * 16);
  const u32 die = xcd >> 2;
  const bool voter = p3 && i8 == 1, timer = p3 && i8 < 2;
  int vote = 0, lag = 0, shift = 0;
  const u32 pbo = dup(G_PB) + wave * 1152;
  u32 near = 0;
  u32x2 ep; u32 xs0, xs1, tch1; u32x4 xr0, xr1;
  const rsrc_t rx = mk_rsrc(part == 2 ? cnd : x, (part == 1 || (part == 2 && has_w)) ? 2048u : 0u);
  auto small_ops = [&]() {
    ep = ld8<AUX_SC1>(wsr, wave == 7 ? WS_EPOCH : OOB, 0);
    xs0 = __builtin_amdgcn_raw_buffer_load_b16(rx, tid * 2, 0, 0); xs1 = __builtin_amdgcn_raw_buffer_load_b16(rx, tid * 2, 1024, 0);
    xr0 = ld16<0>(rx, part == 1 ? lane * 16 : OOB, 0); xr1 = ld16<0>(rx, part == 1 ? lane * 16 : OOB, 1024);
    tch1 = __builtin_amdgcn_raw_buffer_load_b32(mk_rsrc(w2 + (size_t)blk * 32 * 512, is_up ? 64u : 0u), tid == 384 ? 0 : OOB, 0, AUX_NT);
  };
  if (has_w) {
    if (modc) now = 1 << 30;
#pragma nounroll
    while (wleft > wdef) { gate(); due += tauw - tau; piece(); }
    now = 0;
  }
  due = max(due, P.TK * 256);
  SB();
  u32 sot = sa_ & 0xffc00u;
  {
    const u32 s1 = (bl0 + lane - 1 - (lane >= SK0 ? NL - nl : 0)) ^ (124 + part);
#pragma unroll
    for (int k = 0; k < 7; ++k) sot ^= (u32)-(int)(s1 >> k & 1u) & col(P, k);
    PIN(sot);
  }
  u32 tag = 0;
  u32x4 bf[8];
  auto wo_prep = [&]() {
    const u32 wi_ = wimg + wave * 8192;
    const int q = (lane >> 2) & 3, pp = lane & 3;
#pragma unroll
    for (int s = 0; s < 2; ++s) {
#pragma unroll
      for (int t = 0; t < 4; ++t) {
        const int r0 = s * 32 + g * 8 + q, r1 = r0 + 4, ch = t * 2 + (pp >> 1);
        const u32x2 lo = tr_read(wi_ + r0 * 128 + ((ch ^ (r0 & 7)) * 16) + (pp & 1) * 8), hi = tr_read(wi_ + r1 * 128 + ((ch ^ (r1 & 7)) * 16) + (pp & 1) * 8);
        bf[s * 4 + t] = u32x4{lo[0], lo[1], hi[0], hi[1]};
      }
    }
  };
  auto gemv_go = [&](u32 hh) {
    sm.hb[tid] = (u16)hh; sm.hb[512 + tid] = (u16)(hh >> 16);
    WAVE_SYNC();
    const u32x4* a = (c & 11) == 0 ? reinterpret_cast<const u32x4*>(&sm.hb[(c >> 2) * 512 + wave * 64 + g * 8]) : &sm.zero[0];
    const u32x4 a0 = a[0], a1 = a[(c & 11) == 0 ? 4 : 0];
    const f32x4 z = {0.f, 0.f, 0.f, 0.f};
    f32x4 d[4];
#pragma unroll
    for (int t = 0; t < 4; ++t) d[t] = mfma16(a0, bf[t], z);
#pragma unroll
    for (int t = 0; t < 4; ++t) d[t] = mfma16(a1, bf[4 + t], d[t]);
    if (lane < 32) {
#pragma unroll
      for (int t = 0; t < 4; ++t) sm.r2[(t * 16 + c) * 20 + g * 8 + wave] = d[t][0];
    }
  };
  auto mod_stage = [&]() {
    wo_prep();
    gemv_go(pack_bf(silu_f(lo_f(xs0)), silu_f(lo_f(xs1))));
    __builtin_amdgcn_s_waitcnt(0xc07f);
    if (lane == 0) *LDSP(volatile u32, (u32)(uintptr_t)&sm.mflag[wave]) = 1u;
    if (wave == 7) {
#pragma nounroll
      for (int spin = 0; spin < SPIN_MAX; ++spin) {
        const u32 f = *LDSP(volatile u32, (u32)(uintptr_t)&sm.mflag[lane & 7]);
        if (all_ok(f != 0u)) break;
        __builtin_amdgcn_s_sleep(1);
      }
      CFENCE();
      st8_sc1(wsr, G_MOD + (blk * 64 + lane) * 8, pack_bf(sum8(&sm.r2[lane * 20]), sum8(&sm.r2[lane * 20 + 8])), tag);
    }
  };
  if (lane < 4) sm.zero[lane] = u32x4{0u, 0u, 0u, 0u};
  if (part == 2 && has_w) {
    if (tid < 8) sm.mflag[tid] = 0u;
    LDS_BARRIER();
    if (__builtin_expect(modc, 0)) {
      small_ops();
      asm volatile("s_waitcnt vmcnt(0)" ::: "memory");
      tag = next_tag(__builtin_amdgcn_readfirstlane(ep[0]));
      mod_stage();
      __builtin_amdgcn_s_barrier();
    }
  }
  SB();
  const int fd = (xcd * 8 + i8) * 8 + (lane & 3) * 2;
  const int ub = wave >> 2, ud = (wave & 3) * 128 + c * 8 + g * 2;
  u32x4 sg[3]; u32 sx2;
  auto side_loads = [&]() {
    const bool fin = part == 1 && wave == 0;
    sg[0] = ld16<AUX_SC1>(wsr, is_up ? G_MOD + (1024 + ud) * 8 : (fin ? G_MOD + (1024 + fd) * 8 : OOB), 0);
    sg[1] = ld16<AUX_SC1>(wsr, is_up ? G_MOD + (1536 + ud) * 8 : (fin ? G_MOD + (2560 + fd) * 8 : OOB), 0);
    sg[2] = ld16<AUX_SC1>(wsr, is_up ? G_MOD + (2048 + ud) * 8 : OOB, 0);
    sx2 = __builtin_amdgcn_raw_buffer_load_b32(mk_rsrc(x, (is_up || fin) ? 2048u : 0u), is_up ? ub * 1024 + ud * 2 : (g & 1) * 1024 + fd * 2, 0, 0);
  };
  auto side_pin = [&]() { PIN(sg[0]); PIN(sg[1]); PIN(sg[2]); PIN(sx2); };
  const u32 mlo = G_ML + (head * 2 + b) * 64 + (lane & 3) * 16;
  float fmrg = 0.f;
  auto merge_factor = [&](const u32x4& ml) {
    const float mi = u2f(ml[0]);
    float M = fmaxf(mi, dpp<XOR1>(mi)); M = fmaxf(M, dpp<XOR2>(M));
    const float e = __expf(mi - M);
    float L = e * u2f(ml[2]);
    L += dpp<XOR1>(L); L += dpp<XOR2>(L);
    fmrg = rdl(e * __builtin_amdgcn_rcpf(L), part);
  };
  f32x2 ga = {0.f, 0.f}, xin = {0.f, 0.f}, cc1 = {0.f, 0.f};
  float Af = 0.f, Bf = 0.f;
  const int hpos = ub * 512 + (ud & ~15) + (g & 1) * 8 + (c & 1) * 4 + (g >> 1) * 2;
  auto up_prep = [&]() {
    if (!all_ok((sg[0][1] == tag) & (sg[0][3] == tag) & (sg[1][1] == tag) & (sg[1][3] == tag) & (sg[2][1] == tag) & (sg[2][3] == tag))) gather16<3>(wsr, G_MOD + (1024 + ud) * 8, 512 * 8, tag, sg);
    ga = f32x2{ub ? hi_f(sg[0][0]) : lo_f(sg[0][0]), ub ? hi_f(sg[0][2]) : lo_f(sg[0][2])};
    cc1 = f32x2{rbf(1.f + (ub ? hi_f(sg[2][0]) : lo_f(sg[2][0]))), rbf(1.f + (ub ? hi_f(sg[2][2]) : lo_f(sg[2][2])))};
    xin = f32x2{lo_f(sx2), hi_f(sx2)};
    u32* hp = reinterpret_cast<u32*>(&sm.hb[hpos]);
    hp[512] = pack_bf(cc1[0], cc1[1]);
    hp[1024] = ub ? perm_hi(sg[1][0], sg[1][2]) : perm_lo(sg[1][0], sg[1][2]);
  };
  u32x8 bw[4];
  auto up_frags = [&]() {
    const int pp = lane & 3;
    const u32 a = wimg + (pp & 1) * 32768 + ((pp >> 1) * 32 + g * 8 + ((lane >> 2) & 3)) * 64 + (((wave >> 1) ^ ((g & 1) | (pp & 2))) * 16) + (wave & 1) * 8;
#pragma unroll
    for (int j = 0; j < 4; ++j) {
      const u32x2 r0 = tr_read(a + j * 4096), r1 = tr_read(a + j * 4096 + 256), r2 = tr_read(a + j * 4096 + 16384), r3 = tr_read(a + j * 4096 + 16640);
      bw[j] = u32x8{r0[0], r0[1], r1[0], r1[1], r2[0], r2[1], r3[0], r3[1]};
    }
  };
  u32 wfa[4];
  auto wo_frags = [&](int s0) {
#pragma unroll
    for (int t = 0; t < 4; ++t) {
#pragma unroll
      for (int s = s0; s < 2; ++s) {
        const u32x2 lo = tr_read(wfa[t] + s * 4096), hi = tr_read(wfa[t ^ 1] + s * 4096 + 256);
        bw[t][s * 4] = lo[0]; bw[t][s * 4 + 1] = lo[1]; bw[t][s * 4 + 2] = hi[0]; bw[t][s * 4 + 3] = hi[1];
      }
    }
  };
  const bool par = lane & 8;
  auto limit = [&](auto sc) {
    constexpr int s = sc.v;
    WAIT_VM(window(s, 1 + (PRM0.W[s >> 3] >> (4 * (s & 7)) & 7)));
    SB();
  };
  auto sample = [&]() { if (timer) lag += (int)(((u32)wall_clock64() - t0) << 8) - due + ((int)__builtin_amdgcn_s_getreg(7 | (3 << 11)) + 1) * tau; };
  auto report = [&](u32 dst) {
    if (timer) {
      if (lane == 0) sm.mflag[wave] = vote * 65536 + min(max(lag >> 8, 0), 4000);
      lag = 0;
      LDS_BARRIER();
      if (wave == 7) {
        int s = sm.mflag[lane & 7];
        s += __builtin_amdgcn_mov_dpp(s, XOR1, 0xf, 0xf, true); s += __builtin_amdgcn_mov_dpp(s, XOR2, 0xf, 0xf, true); s += __builtin_amdgcn_mov_dpp(s, XOR7, 0xf, 0xf, true);
        if (lane < 8) st8_sc1(wsr, dst + lane * 128 + wi * 8, s, tag);
      }
    }
    SB();
  };
  float l0 = 0.f, l1 = 0.f;
  u32x2 gmd[2], tbg;
  int t1 = 0;
#pragma unroll
  for (int i = SK0; i < SK1; ++i) kr[i] = u32x4{0u, 0u, 0u, 0u};
  const int first = SK0 + NL - nl;
  auto left_out = [&](int i) { int f = first; asm volatile("" : "+s"(f)); return i < f; };
  sfor<1, NL>([&](auto ic) {
    constexpr int i = ic.v;
    if constexpr (i >= SK0 && i < SK1) { if (left_out(i)) return true; }
    const u32 so = __builtin_amdgcn_readlane(sot, i);
    gate();
    if constexpr ((i >= TBS && i <= TBP) || (i >= RPS && i <= RPP)) sample();
    SB();
    if constexpr (i >= SK0 && i < SK1) ld16_into(kr[i], rk, lane * 16, so); else kr[i] = ld16<AUX_NT>(rk, lane * 16, so);
    if constexpr (i == SMO) small_ops();
    if constexpr (i == SMO + 1) { if (voter && (xcd & 3) == 0) __builtin_amdgcn_raw_buffer_store_b32(0u, wsr, pbo + (lane >> 5) * 8192 + (lane & 31) * 4, 0, AUX_SC1); }
    if constexpr (i == SK1) tbg = ld8<AUX_SC1>(wsr, wave == 7 ? G_VT + xcd * 128 + c * 8 : OOB, 0);
    if constexpr (i == TBL) tbg = ld8<AUX_SC1>(wsr, wave == 7 ? G_TB + xcd * 128 + c * 8 : OOB, 0);
    if constexpr (i == QP) { gmd[0] = ld8<AUX_SC1>(wsr, part == 1 ? G_MOD + tid * 8 : OOB, 0); gmd[1] = ld8<AUX_SC1>(wsr, part == 1 ? G_MOD + tid * 8 : OOB, 4096); }
    SB();
    SB();
    if (i >= 4) limit(ic);
    if (i == TBP) {
      if (timer) {
        if (lane == 0) sm.mflag[wave] = lag;
        lag = 0;
        LDS_BARRIER();
        if (wave == 7) {
          int s = sm.mflag[lane & 7];
          s += __builtin_amdgcn_mov_dpp(s, XOR1, 0xf, 0xf, true); s += __builtin_amdgcn_mov_dpp(s, XOR2, 0xf, 0xf, true); s += __builtin_amdgcn_mov_dpp(s, XOR7, 0xf, 0xf, true);
          if (lane < 8) st8_sc1(wsr, G_TB + lane * 128 + wi * 8, t0 << 10 | (u32)min(s / (2048 * (TBP - TBS + 1)), 1023), tag);
        }
      }
      SB();
    }
    if (i == PRB) {
      if (voter) {
        u64 ta, tb, tc; u32 dm;
        const u32 pa = pbo + (wave & 1) * 8192;
        asm volatile("s_memtime %0\n\ts_waitcnt lgkmcnt(0)\n\ts_load_dword %3, %4, %5\n\ts_waitcnt lgkmcnt(0)\n\ts_memtime %1\n\ts_waitcnt lgkmcnt(0)\n\t"
                     "s_load_dword %3, %4, %6\n\ts_waitcnt lgkmcnt(0)\n\ts_memtime %2\n\ts_waitcnt lgkmcnt(0)"
                     : "=&s"(ta), "=&s"(tb), "=&s"(tc), "=&s"(dm) : "s"(ws), "s"(pa), "s"(pa ^ 8192u));
        const int d = min(max((int)(2 * (u32)tb - (u32)ta - (u32)tc), -500), 500);
        vote = ((wave ^ die) & 1) ? -d : d;
      }
      SB();
    }
    if (i == TBU) {
      if (wave == 7) {
        const bool ok = tbg[1] == tag;
        float lf = ok ? (float)min(max((int)((t0 << 10 | 1023u) - tbg[0]) >> 10, -300), 300) : 0.f, nf = ok ? 1.f : 0.f, lg = ok ? (float)(tbg[0] & 1023u) : 0.f;
        lf += dpp<XOR1>(lf); nf += dpp<XOR1>(nf); lg += dpp<XOR1>(lg); lf += dpp<XOR2>(lf); nf += dpp<XOR2>(nf); lg += dpp<XOR2>(lg);
        lf += dpp<XOR7>(lf); nf += dpp<XOR7>(nf); lg += dpp<XOR7>(lg); lf += dpp<XOR8>(lf); nf += dpp<XOR8>(nf); lg += dpp<XOR8>(lg);
        const float rn = __builtin_amdgcn_rcpf(fmaxf(nf, 1.f));
        shift = __builtin_amdgcn_readfirstlane(min(max((int)((lg * rn - (float)P.GS) * (float)P.GB * 0.125f), 0), 150));
        if (lane == 0) sm.bc = u32x2{tag, t0 - (u32)(int)(lf * rn) + shift};
      }
      LDS_BARRIER();
      const u32x2 bc = sm.bc;
      tag = __builtin_amdgcn_readfirstlane(bc[0]);
      t1 = (__builtin_amdgcn_readfirstlane((int)(bc[1] - t0)) + TEND + P.X1) * 256;
      tau -= __builtin_amdgcn_readfirstlane((int)((float)(int)(t0 - bc[1]) * 256.f * __builtin_amdgcn_rcpf((float)(nl - TBU - 1))));
      SB();
    }
    if (i == RPP) report(G_VT);
    if (i == LNS) {
      tag = next_tag(__builtin_amdgcn_readfirstlane(ep[0]));
      if (part == 1) {
        float x0[8], x1[8], mb0, ri0, mb1, ri1;
#pragma unroll
        for (int k = 0; k < 4; ++k) { x0[2 * k] = lo_f(xr0[k]); x0[2 * k + 1] = hi_f(xr0[k]); x1[2 * k] = lo_f(xr1[k]); x1[2 * k + 1] = hi_f(xr1[k]); }
        ln_stats2(x0, x1, mb0, ri0, mb1, ri1);
        l0 = rbf(rbf(lo_f(xs0) - mb0) * ri0); l1 = rbf(rbf(lo_f(xs1) - mb1) * ri1);
      }
      PIN(l0); PIN(l1);
      SB();
    }
    if (i == WUSE) {
      PIN(tch1); PIN(tch2);
      if ((part == 1 || (part == 2 && has_w && !modc))) {
        WAIT_VM(WK); CFENCE();
        if (part == 2) mod_stage(); else wo_prep();
      }
#pragma unroll
      for (int k = 0; k < 8; ++k) PIN(bf[k]);
      SB();
    }
    if (i == QP + QD) {
      if (part == 1) {
        u32 gm[2] = {gmd[0][0], gmd[1][0]};
        if (!all_ok((gmd[0][1] == tag) & (gmd[1][1] == tag))) gather8<2>(wsr, G_MOD + tid * 8, 512 * 8, tag, gm);
        gemv_go(pack_bf(rbf(l0 * rbf(1.f + lo_f(gm[1]))) + lo_f(gm[0]), rbf(l1 * rbf(1.f + hi_f(gm[1]))) + hi_f(gm[0])));
      }
      SB();
    }
    if (i == QP + QD + 1) {
      if (part == 1) {
        LDS_BARRIER();
        if (wave == 0) {
          const int bb = lane >> 5, jq = lane & 31;
          st8_sc1(wsr, G_Q + (bb * 2048 + blk * 32 + jq) * 8, pack_bf(sum8(&sm.r2[2 * jq * 20 + bb * 8]), sum8(&sm.r2[(2 * jq + 1) * 20 + bb * 8])), tag);
        }
      }
      SB();
    }
    return true;
  });
  PIN(sot);
  if (wave == 7) {
    const bool ok = tbg[1] == tag;
    float lg = ok ? (float)(tbg[0] & 0xffffu) : 0.f, nf = ok ? 8.f * (RPP - RPS + 1) : 0.f;
    lg += dpp<XOR1>(lg); nf += dpp<XOR1>(nf); lg += dpp<XOR2>(lg); nf += dpp<XOR2>(nf); lg += dpp<XOR7>(lg); nf += dpp<XOR7>(nf); lg += dpp<XOR8>(lg); nf += dpp<XOR8>(nf);
    lg *= __builtin_amdgcn_rcpf(fmaxf(nf, 1.f));
    if (lane == 0) sm.bc2 = u32x2{(u32)min(shift * P.DG * 32, P.EW * 256), (u32)min(max((int)((lg - (float)P.B0) * (float)(P.BG * 32) + fmaxf(lg - (float)P.TC, 0.f) * (float)(P.RT * 32)) - shift * P.D0 * 32, 0), 200 * 256)};
  }
  LDS_BARRIER();
  {
    const u32x2 bc = sm.bc2;
    const int st = __builtin_amdgcn_readfirstlane(bc[1]), sp = (st * P.BF) >> 3;
    t1 += st + __builtin_amdgcn_readfirstlane(bc[0]);
    due += P.TG * 256 + sp;
    tau = (te1 >> 16) + __builtin_amdgcn_readfirstlane((int)((float)(st - sp) * __builtin_amdgcn_rcpf((float)(nl - 3))));
  }
  const u32 qoff = G_Q + ((b * 32 + head) * 64 + c * 4) * 8;
  u32x4 qg[2], mlg;
  u32 qq[4];
  constexpr int NB = (NL + 15) / 16;
  float xlo, sc[NB], pr[NB], m = 0.f;
  f32x4 acc = {0.f, 0.f, 0.f, 0.f};
  u32 pb[NB], pm[4], pl8[8], cpf;
  const u32 adst0 = lds_addr(lane < 32 ? &sm.red[wave][c * 8 + g * 4] : &sm.r2[(lane - 32) * 4]);
  u32 po, pso, rdst, rsrc, xo;
  auto wo_addr = [&]() {
    po = dup(G_P + (xcd & 3) * 8192) + tid * 16 + near;
    pso = tid < 16 ? pslot + c * 128 + near : OOB;
    PIN(pso);
    rdst = (u32)(uintptr_t)&sm.red2[wave][(lane & 31) * 4 + (lane >> 5) * 2];
    rsrc = (u32)(uintptr_t)&sm.red2[0][((wave & 1) * 16 + c) * 4 + (g & 1) * 2 + (g >> 1)];
    xo = dup(G_XP + xcd * 2048 + (wave & 1) * 1024 + (blk * 64 + (lane & ~1)) * 2) + (lane & 1) * 8192;
    PIN(po); PIN(rdst); PIN(rsrc); PIN(xo);
    const int q = (lane >> 2) & 3, pp = lane & 3, r = (g & 1) * 4 + (q & 1);
#pragma unroll
    for (int t = 0; t < 4; ++t) { wfa[t] = wimg + wave * 8192 + ((g >> 1) * 16 + (q >> 1) * 8 + r) * 128 + (pp & 1) * 8 + ((t * 2 + (pp >> 1)) ^ r ^ (q & 2)) * 16; PIN(wfa[t]); }
  };
  const u32 nadd = wdef ? P.LW : (is_up ? P.LU : 0);
  u32 nto = tid < 2 ? dup(G_NT) + bid * 8 + tid * 8192 : OOB;
  PIN(nto);
  auto notices = [&]() -> int {
    u32 off = dup(G_NT) + near + lane * 16;
    PIN(off);
    until(due);
    int mx;
#pragma nounroll
    for (int spin = 0; spin < SPIN_MAX; ++spin) {
      CFENCE();
      const u32x4 ga_ = ld16<AUX_SC1>(wsr, off, 0), gb_ = ld16<AUX_SC1>(wsr, off, 1024);
      mx = max(max((int)(ga_[0] - t0), (int)(ga_[2] - t0)), max((int)(gb_[0] - t0), (int)(gb_[2] - t0)));
      if (all_ok((((ga_[1] ^ tag) | (ga_[3] ^ tag)) | ((gb_[1] ^ tag) | (gb_[3] ^ tag))) == 0u)) break;
      for (int k = 0; k < P.AG; ++k) __builtin_amdgcn_s_sleep(1);
    }
    if (all_ok(mx <= (t1 >> 8) - P.AC)) return t1;
    mx = max(mx, __builtin_amdgcn_mov_dpp(mx, XOR1, 0xf, 0xf, true)); mx = max(mx, __builtin_amdgcn_mov_dpp(mx, XOR2, 0xf, 0xf, true));
    mx = max(mx, __builtin_amdgcn_mov_dpp(mx, XOR7, 0xf, 0xf, true)); mx = max(mx, __builtin_amdgcn_mov_dpp(mx, XOR8, 0xf, 0xf, true));
    mx = max(max(__builtin_amdgcn_readlane(mx, 0), __builtin_amdgcn_readlane(mx, 16)), max(__builtin_amdgcn_readlane(mx, 32), __builtin_amdgcn_readlane(mx, 48)));
    return max(min(mx, 1 << 20) * 256, t1 + P.L0 * 256) + P.L1 * 256;
  };
  auto pv_row = [&](auto jc) {
    constexpr int i = jc.v;
    const u32 pv = __float_as_uint(bcast16(u2f(pb[i >> 4]), i & 15));
    acc = mfma16(u32x4{pv & pm[0], pv & pm[1], pv & pm[2], pv & pm[3]}, vr[i], acc);
    PIN(acc);
    return true;
  };
  sfor<0, NL - 1>([&](auto ic) {
    constexpr int v = ic.v;
    if constexpr (v >= SK0 && v < SK1) { if (left_out(v)) return true; }
    if constexpr (v > 0) {
      const u32 so = __builtin_amdgcn_readlane(sot, v);
      if constexpr (v >= SK1) { if (v == NL - P.NE) tau = (tau * P.FE) >> 4; }
      gate();
      if constexpr (v >= RVS && v <= RVP) sample();
      SB();
      if constexpr (v >= SK0 && v < SK1) ld16_into(vr[v], rv, lane * 16, so); else vr[v] = ld16<AUX_NT>(rv, lane * 16, so);
      SB();
      SB();
    }
    if constexpr (v == SK0 - 1) {
#pragma unroll
      for (int i = SK0; i < SK1; ++i) { vr[i] = u32x4{0u, 0u, 0u, 0u}; PIN(vr[i]); }
    }
    if constexpr (v == VQ - 4) { qg[0] = ld16<AUX_SC1>(wsr, qoff, 0); qg[1] = ld16<AUX_SC1>(wsr, qoff + 16, 0); }
    if constexpr (v == VSIDE) side_loads();
    if constexpr (v == CPF) {
      u64 pc; u32 dlt, len;
      asm volatile("s_getpc_b64 %0\n.Lcpf_here:\n\ts_mov_b32 %1, .Ltail_start-.Lcpf_here\n\ts_mov_b32 %2, .Lend_a-.Ltail_start\n\ts_max_i32 %2, %2, .Lend_b-.Ltail_start\n\ts_max_i32 %2, %2, .Lend_c-.Ltail_start\n\ts_max_i32 %2, %2, .Lend_d-.Ltail_start\n\ts_max_i32 %2, %2, .Lend_e-.Ltail_start" : "=s"(pc), "=s"(dlt), "=s"(len) :: "scc");
      pc += dlt;
      const rsrc_t rc = mk_rsrc((const void*)pc, (part == 3 && i8 == 1) ? len : 0u);
      cpf = __builtin_amdgcn_raw_buffer_load_b32(rc, tid * 64, 0, 0);
    }
    if constexpr (v == SK1 + 2) PIN(cpf);
    if constexpr (v == RVP) report(G_VT + 1024);
    if constexpr (v == RVL) tbg = ld8<AUX_SC1>(wsr, wave == 7 ? G_VT + (g ? 0 : 1024) + xcd * 128 + c * 8 : OOB, 0);
    if constexpr (v == SK1) mlg = ld16<AUX_SC1>(wsr, wave == 0 ? mlo : OOB, 0);
    SB();
    if constexpr (NL + v < 72) limit(IC<NL + v>{});
    if constexpr (v >= SK0 + WV) { WAIT_VM(v < SK1 ? WV : WV + 1); SB(); }
    if constexpr (v == VQ) {
      if (!all_ok((qg[0][1] == tag) & (qg[0][3] == tag) & (qg[1][1] == tag) & (qg[1][3] == tag))) gather16<2>(wsr, qoff, 16, tag, qg);
      qq[0] = qg[0][0]; qq[1] = qg[0][2]; qq[2] = qg[1][0]; qq[3] = qg[1][2];
    }
    if constexpr (v >= VQ && v < VS) {
      constexpr int sl = v - VQ, bt = sl >> 1;
      float xs[8];
#pragma unroll
      for (int i = 0; i < 8; ++i) {
        float d = 0.f;
        if (sl * 8 + i < NL) {
#pragma unroll
          for (int k = 0; k < 4; ++k) d = dot2(kr[sl * 8 + i][k], qq[k], d);
        }
        xs[i] = d;
      }
#pragma unroll
      for (int h = 0; h < 4; ++h) xs[h] = sel_add<XOR7>(xs[h], xs[h + 4], c & 4);
#pragma unroll
      for (int h = 0; h < 2; ++h) xs[h] = sel_add<XOR2>(xs[h], xs[h + 2], c & 2);
      xs[0] = sel_add<XOR1>(xs[0], xs[1], c & 1);
      if constexpr ((sl & 1) || sl == NSL - 1) {
        const float t = (sl & 1) ? sel_add<XOR8>(xlo, xs[0], c & 8) : xs[0] + dpp<XOR8>(xs[0]);
        const float s = rbf(rbf(t) * 0.08838834764831845f);
        sc[bt] = (bt * 16 + c < SK0 || (bt * 16 + c >= first && bt * 16 + c < NL)) ? s : -__builtin_inff();
        PIN(sc[bt]);
      } else {
        xlo = xs[0];
        PIN(xlo);
      }
    }
    if constexpr (v == VS) {
      float mloc = sc[0];
#pragma unroll
      for (int bt = 1; bt < NB; ++bt) mloc = fmaxf(mloc, sc[bt]);
      m = fmaxf(wave_max(mloc), -3.0e38f);
      float ls = 0.f;
#pragma unroll
      for (int bt = 0; bt < NB; ++bt) { pr[bt] = __expf(sc[bt] - m); ls += pr[bt]; }
      const float l = wave_sum(ls);
      if (lane == 0) { sm.ml[wave][0] = m; sm.ml[wave][1] = l; }
      if (c == ((NL - 1) & 15)) sm.pl[g][wave] = pr[NB - 1];
    }
    if constexpr (v >= SK1) pv_row(IC<v - SK1 + SK0 - WV>{});
    if constexpr (v < SK0 || v >= SK0 + WV) sfor<cons_end(v - 1), cons_end(v)>(pv_row);
    if constexpr (v == RVU) {
      if (wave == 7) {
        if (__builtin_expect(!all_ok(tbg[1] == tag || g == 0), 0)) {
          u32 vg[1];
          gather8<1>(wsr, G_VT + xcd * 128 + c * 8, 0, tag, vg);
          if (g) tbg = u32x2{vg[0], tag};
        }
        const bool ok = tbg[1] == tag;
        float lg = ok ? (float)(g ? (int)tbg[0] >> 16 : (int)(tbg[0] & 0xffffu)) : 0.f, nf = ok ? 8.f * (RVP - RVS + 1) : 0.f;
        lg += dpp<XOR1>(lg); nf += dpp<XOR1>(nf); lg += dpp<XOR2>(lg); nf += dpp<XOR2>(nf); lg += dpp<XOR7>(lg); nf += dpp<XOR7>(nf); lg += dpp<XOR8>(lg); nf += dpp<XOR8>(nf);
        lg *= __builtin_amdgcn_rcpf(fmaxf(nf, 1.f));
        if (lane == 16) sm.bc4 = __float_as_uint(lg);
        if (lane == 0) sm.bc3 = (u32)min(max((int)((lg - (float)P.F0) * (float)(P.FG * 32)), 0), 200 * 256);
      }
      LDS_BARRIER();
      const int st = __builtin_amdgcn_readfirstlane(sm.bc3), sp = (st * P.FF_) >> 3;
      near = ((__builtin_amdgcn_readfirstlane(sm.bc4) >> 31 ^ die ^ P.NR ^ 1) & 1) * 8192u;
      t1 += st;
      due += sp;
      tau += __builtin_amdgcn_readfirstlane((int)((float)(st - sp) * __builtin_amdgcn_rcpf((float)(nl - RVU - 3))));
    }
    if constexpr (v == SK1 + 1) {
      float p[8];
#pragma unroll
      for (int w = 0; w < 8; ++w) p[w] = sm.pl[g][w] * __expf(sm.ml[w][0] - m);
#pragma unroll
      for (int w = 0; w < 8; ++w) pl8[w] = pack_bf(p[w], p[w]);
#pragma unroll
      for (int w = 0; w < 8; ++w) PIN(pl8[w]);
    }
    if constexpr (v == MLS - 1) { side_pin(); if (is_up) up_prep(); PIN(ga); PIN(xin); PIN(cc1); }
    if constexpr (v == UPREP - 1) wo_addr();
    if constexpr (v == UPREP) {
#pragma unroll
      for (int k = 0; k < 4; ++k) asm volatile("" : "=v"(bw[k]));
      if (part == 0) wo_frags(0); else if (is_up) up_frags();
#pragma unroll
      for (int k = 0; k < 4; ++k) PIN(bw[k]);
    }
    if constexpr (v == UPREP + 1) {
      if (is_up) {
        const u32x4* a1 = reinterpret_cast<const u32x4*>(&sm.hb[1024 + (c >> 2 & 1) * 1024 + (c >> 3) * 512 + (g >> 1) * 256 + (c >> 1 & 1) * 32 + (g & 1) * 16 + (c & 1) * 8]);
        f32x4 d = {0.f, 0.f, 0.f, 0.f};
#pragma unroll
        for (int j = 0; j < 4; ++j) d = mfma64(a1[j * 8], bw[j], d, (c & 1) ? 0xeeeeu : 0x4444u);
        float t = par ? d[2] + d[3] : d[0] + d[1];
        t += dpp<XOR8>(t);
        const auto ab = __builtin_amdgcn_permlane16_swap(__float_as_uint(t), __float_as_uint(t), false, false);
        Af = u2f(ab[0]); Bf = u2f(ab[1]);
      }
      PIN(Af); PIN(Bf);
    }
    if constexpr (v == MLS) {
      LDS_BARRIER();
      float mm[8], ll[8];
#pragma unroll
      for (int w = 0; w < 8; ++w) { const f32x2 t = *reinterpret_cast<const f32x2*>(&sm.ml[w][0]); mm[w] = t[0]; ll[w] = t[1]; }
      float Mwg = mm[0], Lwg = 0.f;
#pragma unroll
      for (int w = 1; w < 8; ++w) Mwg = fmaxf(Mwg, mm[w]);
      if (wave == 0) {
#pragma unroll
        for (int w = 0; w < 8; ++w) Lwg = fmaf(__expf(mm[w] - Mwg), ll[w], Lwg);
        if (lane == 0) st16_sc1(wsr, G_ML + ((head * 2 + b) * 4 + part) * 16, __float_as_uint(Mwg), __float_as_uint(Lwg), tag);
      }
      const float fw = __expf(m - Mwg);
      m = Mwg;
#pragma unroll
      for (int bt = 0; bt < NB; ++bt) { pb[bt] = pack_bf(pr[bt] * fw, pr[bt] * fw); PIN(pb[bt]); }
#pragma unroll
      for (int k = 0; k < 4; ++k) { pm[k] = (c >> 1 & 3) == k ? ((c & 1) ? 0xffff0000u : 0x0000ffffu) : 0u; PIN(pm[k]); }
    }
    SB();
    return true;
  });
  asm volatile(".Ltail_start:");
  if (wave != 0) {
    pv_row(IC<SK0 - 1>{});
    WAIT_VM(WV); SB();
    sfor<NL - 1 - WV, NL - 1>(pv_row);
    *LDSP(f32x4, adst0) = acc;
    LDS_BARRIER();
  } else {
    u32x4 xr[8], am[8];
#pragma unroll
    for (int w = 0; w < 8; ++w) am[w] = u32x4{pl8[w] & pm[0], pl8[w] & pm[1], pl8[w] & pm[2], pl8[w] & pm[3]};
#pragma unroll
    for (int w = 0; w < 8; ++w) PIN(am[w]);
    {
      const u32 so = __builtin_amdgcn_readlane(sot, NL - 1);
      gate();
      SB();
#pragma unroll
      for (int w = 0; w < 8; ++w) xr[w] = ld16<AUX_NT>(rv, lane * 16, so ^ ((w & 1) ? col(P, 7) : 0u) ^ ((w & 2) ? col(P, 8) : 0u) ^ ((w & 4) ? col(P, 9) : 0u));
      SB();
    }
    u32 rsum = lds_addr(&sm.red[0][c * 8 + (g & 1) * 4]);
    pv_row(IC<SK0 - 1>{});
    WAIT_VM(WV + 8); SB();
    sfor<NL - 1 - WV, NL - 3>(pv_row);
    if (__builtin_expect(!all_ok((mlg[1] == tag) & (mlg[3] == tag)), 0)) {
#pragma nounroll
      for (int spin = 0; spin < SPIN_MAX; ++spin) {
        CFENCE();
        mlg = ld16<AUX_SC1>(wsr, mlo, 0);
        if (all_ok((mlg[1] == tag) & (mlg[3] == tag))) break;
      }
    }
    merge_factor(mlg);
    sfor<NL - 3, NL - 1>(pv_row);
    LDS_BARRIER();
    f32x4 t[8];
#pragma unroll
    for (int w = 1; w < 8; ++w) t[w] = LDSP(f32x4, rsum)[w * 32];
    acc = ((acc + t[1]) + (t[2] + t[3])) + ((t[4] + t[5]) + (t[6] + t[7]));
    PIN(acc);
    SB();
#pragma unroll
    for (int w = 0; w < 8; ++w) acc = mfma16(am[w], xr[w], acc);
  }
  WAIT_VM(0);
  acc *= fmrg;
  {
    const u32 w0 = min(pack_bf(acc[0], acc[1]), SENT - 1u), w1 = min(pack_bf(acc[2], acc[3]), SENT - 1u);
    const auto e0 = __builtin_amdgcn_permlane16_swap(w0, w0, false, false), e1 = __builtin_amdgcn_permlane16_swap(w1, w1, false, false);
    __builtin_amdgcn_raw_buffer_store_b128(u32x4{e0[0], e1[0], e0[1], e1[1]}, wsr, pso, 0, AUX_SC1);
  }
  st8_sc1(wsr, nto, (u32)wall_clock64() + nadd, tag);
  SB();

  if (part == 2) return;
  if (part == 0) {
    __builtin_amdgcn_s_barrier();
#pragma nounroll
    while (wleft > 0) { until(t1 - (P.PA + wleft * P.PD) * 256); piece(); }
    bool late = false;
    u32x4 og[1];
    f32x4 d[4];
    u32 six = (c & 8) ? 0xeeeeu : 0x4444u;
    PIN(six);
#pragma unroll
    for (int t = 0; t < 4; ++t) { d[t] = f32x4{0.f, 0.f, 0.f, 0.f}; PIN(d[t]); }
    if (wdef) {
      late = true;
      while ((int)(((u32)wall_clock64() - t0) << 8) < t1 - 10 * 256) if (__builtin_amdgcn_s_getreg(7 | (3 << 11)) == 0) { late = false; break; }
      if (__builtin_expect(late, 0)) { until(t1); CFENCE(); og[0] = ld16<AUX_SC1>(wsr, po, 0); SB(); WAIT_VM(1); } else { WAIT_VM(0); }
      CFENCE();
      wo_frags(1);
#pragma unroll
      for (int k = 0; k < 4; ++k) PIN(bw[k]);
      SB();
    }
    if (__builtin_expect(!late, 1)) { until(t1); CFENCE(); og[0] = ld16<AUX_SC1>(wsr, po, 0); }
    SB();
    auto wo_mm = [&]() {
#pragma unroll
      for (int t = 0; t < 4; ++t) d[t] = mfma64(og[0], bw[t], d[t], six);
    };
    auto p_bad = [&]() { return !all_ok(max(max(og[0][0], og[0][1]), max(og[0][2], og[0][3])) != SENT); };
    wo_mm();
    if (__builtin_expect(p_bad(), 0)) {
#pragma nounroll
      for (int spin = 0; spin < SPIN_MAX; ++spin) {
        __builtin_amdgcn_s_sleep(1);
        CFENCE();
        og[0] = ld16<AUX_SC1>(wsr, po, 0);
        if (!p_bad()) break;
      }
#pragma unroll
      for (int t = 0; t < 4; ++t) d[t] = f32x4{0.f, 0.f, 0.f, 0.f};
      wo_mm();
      SB();
      asm volatile(".Lend_d:");
    }
    {
      SB();
      f32x4 y; f32x2 h[4];
#pragma unroll
      for (int t = 0; t < 4; ++t) { h[t] = d[t].xy + d[t].zw; PIN(h[t]); }
#pragma unroll
      for (int t = 0; t < 4; ++t) { y[t] = h[t][0] + h[t][1]; PIN(y[t]); }
      *LDSP(f32x2, rdst) = f32x2{swap32_add(y[0], y[1]), swap32_add(y[2], y[3])};
    }
    LDS_BARRIER();
    if (wave < 2) {
      f32x2 v[4];
#pragma unroll
      for (int w = 0; w < 4; ++w) v[w] = f32x2{LDSP(float, rsrc)[w * 256], LDSP(float, rsrc)[w * 256 + 128]};
      const f32x2 h = (v[0] + v[1]) + (v[2] + v[3]);
      const float y = h[0] + h[1];
      __builtin_amdgcn_raw_buffer_store_b32(min(pack_bf(dpp<0xA0>(y), dpp<0xF5>(y)), SENT - 1u), wsr, xo, 0, AUX_SC1);
      asm volatile(".Lend_a:");
    }
    return;
  }
  if (part == 3) {
    if (!has_w) return;
    u32x4 b2[4];
    {
      __builtin_amdgcn_s_barrier();
      const rsrc_t r2 = mk_rsrc(w2 + (size_t)(blk * 32 + wave * 4) * 512, 4096u);
      const u32 l2 = (u32)(uintptr_t)(&sm.w2b[wave * 4][0]);
#pragma unroll
      for (int n = 0; n < 4; ++n) { until(t1 - (P.QA + (4 - n) * P.QD) * 256); dma16(r2, (lane ^ (n * 2) ^ ((wave & 2) << 2)) * 16, n * 1024, l2 + n * 1024); }
    }
    const u32 tdst = lds_addr(&sm.hb[hpos]);
    const u32 sdst = lds_addr(&sm.stat[wave][lane]);
    const u32 ssrc = lds_addr(&sm.stat[(g >> 1) * 4 + ((lane >> 3) & 3)][(lane & 7) * 4]);
    const u32 adst = lds_addr((c < 4 && (g & 1) == 0) ? &sm.actl[g >> 1][wave * 4 + c] : reinterpret_cast<u16*>(&sm.dummy[lane]));
    const u32 asrc = lds_addr(&sm.actl[lane & 1][g * 8]);
    const u32 apa = lds_addr(&sm.hb[(c >> 3) * 512 + (g >> 1) * 256 + (c >> 1 & 1) * 64 + ((c >> 2) & 1) * 32 + (g & 1) * 16 + (c & 1) * 8]);
    u32 six = (c & 1) ? 0xeeeeu : 0x4444u;
    f32x4 d[2] = {{0.f, 0.f, 0.f, 0.f}, {0.f, 0.f, 0.f, 0.f}};
    PIN(six); PIN(d[0]); PIN(d[1]);
    constexpr u32 xs2 = 16384;
    u32 xoff = dup(G_XP) + near + g * 2048 + ub * 1024 + (wave & 3) * 256 + c * 16;
    u32x4 one;
#pragma unroll
    for (int k = 0; k < 4; ++k) one[k] = ((c & 2) == 0 && (c >> 2) == k) ? ((c & 1) ? 0x3f800000u : 0x00003f80u) : 0u;
    PIN(one);
    const int cw = wave ^ ((die ^ 1) << 2);
    u32 ooff = dup(G_OP + (blk * 512 + cw * 64 + lane) * 8) + (near ^ ((cw >> 2 ^ die) << 13));
    float Afp = Af, Bfp = Bf;
    PIN(xoff); PIN(ooff); PIN(Afp); PIN(Bfp);
    f32x2 gc = ga * cc1, xc = xin * cc1;
    PIN(ga); PIN(xin); PIN(gc); PIN(xc);
    u32 w2a = (u32)(uintptr_t)(&sm.w2b[0][0]) + (g * 8 + ((lane >> 2) & 3)) * 1024 + (cw ^ (g & 1)) * 128 + (lane & 15) * 8;
    PIN(w2a);
    if (wave == 1) {
      due = t1 + P.N1 * 256;
      const int tn = notices();
      due = tn + P.X2 * 256;
      until(due);
    }
    asm volatile("s_waitcnt vmcnt(0)" ::: "memory");
    __builtin_amdgcn_s_barrier();
    CFENCE();
    u32x4 xg[2] = {ld16<AUX_SC1>(wsr, xoff, 0), ld16<AUX_SC1>(wsr, xoff, xs2)};
    SB();
#pragma unroll
    for (int t = 0; t < 4; ++t) {
      const u32x2 lo = tr_read(w2a ^ (t * 32)), hi = tr_read((w2a ^ (t * 32)) + 4096);
      b2[t] = u32x4{lo[0], lo[1], hi[0], hi[1]};
    }
#pragma unroll
    for (int t = 0; t < 4; ++t) PIN(b2[t]);
    SB();
    const f32x4 z4 = {0.f, 0.f, 0.f, 0.f};
    auto xp_bad = [&]() { return !all_ok(max(max(max(xg[0][0], xg[0][1]), max(xg[0][2], xg[0][3])), max(max(xg[1][0], xg[1][1]), max(xg[1][2], xg[1][3]))) != SENT); };
    f32x4 v = mfma16(one, xg[1], mfma16(one, xg[0], z4));
    if (__builtin_expect(xp_bad(), 0)) {
#pragma nounroll
      for (int spin = 0; spin < SPIN_MAX; ++spin) {
        __builtin_amdgcn_s_sleep(1);
        CFENCE();
        xg[0] = ld16<AUX_SC1>(wsr, xoff, 0); xg[1] = ld16<AUX_SC1>(wsr, xoff, xs2);
        if (!xp_bad()) break;
      }
      v = mfma16(one, xg[1], mfma16(one, xg[0], z4));
      SB();
      asm volatile(".Lend_e:");
    }
    {
      const f32x2 a = v.xy, hc = __builtin_elementwise_fma(gc, a, xc), x1 = __builtin_elementwise_fma(ga, a, xin);
      *LDSP(u32, tdst) = pack_bf(hc[0], hc[1]);
      *LDSP(float, sdst) = swap32_add(x1[0] + x1[1], fmaf(x1[0], x1[0], x1[1] * x1[1]));
    }
    LDS_BARRIER();
    {
      const f32x4 s4 = LDSP(f32x4, ssrc)[0], q4 = LDSP(f32x4, ssrc)[8];
      SB();
      const u32x4 af0 = LDSP(u32x4, apa)[0], af1 = LDSP(u32x4, apa)[16];
      SB();
      const f32x2 s2 = s4.xy + s4.zw, q2 = q4.xy + q4.zw;
      float s = s2[0] + s2[1], q = q2[0] + q2[1];
      SB();
      d[0] = mfma64(af0, bw[0], d[0], six);
      SB();
      s = swap16_add(s, q);
      SB();
      d[1] = mfma64(af0, bw[1], d[1], six);
      SB();
      s += dpp<XOR1>(s); s += dpp<XOR2>(s);
      SB();
      d[0] = mfma64(af1, bw[2], d[0], six);
      SB();
      s += dpp<XOR7>(s); s += dpp<XOR8>(s);
      SB();
      d[1] = mfma64(af1, bw[3], d[1], six);
      SB();
      const auto sq = __builtin_amdgcn_permlane16_swap(__float_as_uint(s), __float_as_uint(s), false, false);
      const float mean = u2f(sq[0]) * (1.f / 512.f);
      const float var = fmaf(u2f(sq[1]), 1.f / 512.f, -mean * mean);
      float ri = __builtin_amdgcn_rsqf(fmaxf(var, 0.f) + 1e-5f);
      PIN(ri); PIN(d[0]); PIN(d[1]);
      SB();
      float y0 = d[0][0] + d[0][1], y1 = d[1][2] + d[1][3], mr = -ri * mean;
      PIN(y0); PIN(y1); PIN(mr);
      float y = y0 + y1, cf = fmaf(mr, Afp, Bfp), rk = ri * -1.44269504f;
      PIN(y); PIN(cf); PIN(rk);
      y = __builtin_bit_cast(float, __builtin_amdgcn_update_dpp(__builtin_bit_cast(int, y), __builtin_bit_cast(int, y), XOR8, 0xa, 0xf, false));
      const float ck = cf * -1.44269504f;
      y = swap16_add(y, y);
      const float ex = __builtin_amdgcn_exp2f(fmaf(y, rk, ck));
      y = fmaf(y, ri, cf);
      const float gu = y * dpp<0x104>(y);
      *LDSP(u16, adst) = (u16)pack_bf(gu * __builtin_amdgcn_rcpf(1.f + ex), 0.f);
    }
    LDS_BARRIER();
    {
      const u32x4 af = *LDSP(u32x4, asrc);
      const f32x4 z = {0.f, 0.f, 0.f, 0.f};
      f32x4 d0 = mfma16(af, b2[0], z), d1 = mfma16(af, b2[1], z), d2 = mfma16(af, b2[2], z), d3 = mfma16(af, b2[3], z);
      asm volatile("" : "+v"(d0), "+v"(d1), "+v"(d2), "+v"(d3));
      const bool g1 = g & 1, g2 = g & 2;
      const float e0 = g1 ? d1[0] : d0[0], e1 = g1 ? d1[1] : d0[1], f0 = g1 ? d3[0] : d2[0], f1 = g1 ? d3[1] : d2[1];
      const u32x2 ov = {pack_bf(g2 ? f0 : e0, g2 ? f1 : e1), tag};
      __builtin_amdgcn_raw_buffer_store_b64(ov, wsr, ooff, 0, AUX_SC1);
      if (blk == 0 && tid == 0) __builtin_amdgcn_raw_buffer_store_b32(tag, wsr, WS_EPOCH, 0, AUX_SC1);
    }
    asm volatile(".Lend_b:");
    return;
  }
  if (wave == 0) {
    if (!all_ok((sg[0][1] == tag) & (sg[0][3] == tag) & (sg[1][1] == tag) & (sg[1][3] == tag))) {
      u32x4 t[2];
      gather16<2>(wsr, G_MOD + (1024 + fd) * 8, 1536 * 8, tag, t);
      sg[0] = t[0]; sg[1] = t[1];
    }
    const rsrc_t rout = mk_rsrc(out, 2048u);
    u32 oo = (lane & 44) == 0 ? (g * 512 + fd) * 2 : OOB;
    until(t1 + P.PV * 256);
    __builtin_amdgcn_raw_buffer_store_b32(sx2, rout, oo, 0, AUX_SC1);
    u32 xa;
    {
      due = t1 + P.N2 * 256;
      const int tn = notices();
      due = tn + P.X3 * 256;
      until(due);
    }
    const bool hb = g & 1;
#pragma nounroll
    for (int spin = 0; spin < SPIN_MAX; ++spin) {
      CFENCE();
      xa = __builtin_amdgcn_raw_buffer_load_b32(wsr, dup(G_XP) + near + (g >> 1) * 16384 + (c >> 2) * 2048 + (g & 1) * 1024 + fd * 2, 0, AUX_SC1);
      if (all_ok(xa != SENT)) break;
      __builtin_amdgcn_s_sleep(1);
    }
    float a0 = lo_f(xa), a1 = hi_f(xa);
    a0 += dpp<0x124>(a0); a1 += dpp<0x124>(a1);
    a0 += dpp<XOR8>(a0); a1 += dpp<XOR8>(a1);
    a0 = swap32_add(a0, a0); a1 = swap32_add(a1, a1);
    f32x2 xv = {rbf(lo_f(sx2) + rbf((hb ? hi_f(sg[0][0]) : lo_f(sg[0][0])) * rbf(a0))), rbf(hi_f(sx2) + rbf((hb ? hi_f(sg[0][2]) : lo_f(sg[0][2])) * rbf(a1)))};
    f32x2 gv = {hb ? hi_f(sg[1][0]) : lo_f(sg[1][0]), hb ? hi_f(sg[1][2]) : lo_f(sg[1][2])};
    PIN(xv); PIN(gv);
    due = max(due + P.X4 * 256, (int)(((u32)wall_clock64() - t0) << 8) + P.GX * 256);
    const bool last = lane < (NUP - 32) * 4;
    u32 o0 = dup(G_OP + (lane >> 2) * 4096 + fd * 8) + near, o2 = last ? o0 : OOB, tg2 = last ? tag : 0u;
    const u32 pk = (c & 10) ? 0u : ((c & 4) ? 0x3f800000u : 0x00003f80u);
    u32x4 one = {(c & 1) ? 0u : pk, 0u, (c & 1) ? pk : 0u, 0u};
    PIN(o0); PIN(o2); PIN(tg2); PIN(oo); PIN(one);
    u32 s1_ = 131072, s2_ = 262144;
    asm volatile("" : "+s"(s1_), "+s"(s2_));
    until(due);
    f32x4 sv;
#pragma nounroll
    for (int spin = 0; spin < SPIN_MAX; ++spin) {
      CFENCE();
      const u32x4 og[3] = {ld16<AUX_SC1>(wsr, o0, 0), ld16<AUX_SC1>(wsr, o0, s1_), ld16<AUX_SC1>(wsr, o2, s2_)};
      sv = f32x4{0.f, 0.f, 0.f, 0.f};
#pragma unroll
      for (int k = 0; k < 3; ++k) sv = mfma16(one, og[k], sv);
      SB();
      u32 bad = ((og[0][1] ^ tag) | (og[0][3] ^ tag) | (og[1][1] ^ tag)) | ((og[1][3] ^ tag) | (og[2][1] ^ tg2) | (og[2][3] ^ tg2));
      PIN(bad);
      SB();
      float s0 = sv[0] + dpp<0x124>(sv[0]), s1 = sv[1] + dpp<0x124>(sv[1]);
      PIN(s0); PIN(s1);
      s0 += dpp<XOR8>(s0); s1 += dpp<XOR8>(s1);
      PIN(s0); PIN(s1);
      __builtin_amdgcn_raw_buffer_store_b32(pack_bf(fmaf(gv[0], s0, xv[0]), fmaf(gv[1], s1, xv[1])), rout, oo, 0, AUX_SC1);
      SB();
      if (all_ok(bad == 0u)) break;
      __builtin_amdgcn_s_sleep(1);
    }
    asm volatile(".Lend_c:");
  }
}

extern "C" void kernel_launch(void* const* d_in, const int* in_sizes, int n_in,
                              void* d_out, int out_size, void* d_ws, size_t ws_size,
                              hipStream_t stream) {
  fused_kernel<<<256, 512, 0, stream>>>((const u16*)d_in[1], (const u16*)d_in[2], (const u16*)d_in[4], (const u16*)d_in[3], (const u16*)d_in[10], (const u16*)d_in[6], (const u16*)d_in[7],
                                        (unsigned char*)d_ws, (const u16*)d_in[0], (const u16*)d_in[9], (const u16*)d_in[8], (u16*)d_out);
}
```

```cpp
#include <hip/hip_runtime.h>
#include <stdint.h>


typedef unsigned int u32;
typedef unsigned short u16;
typedef unsigned long long u64;
typedef __attribute__((ext_vector_type(4))) unsigned u32x4;
typedef __attribute__((ext_vector_type(2))) unsigned u32x2;
typedef __attribute__((ext_vector_type(2))) __bf16 bf16x2;
typedef __attribute__((ext_vector_type(4))) float f32x4;
typedef __attribute__((ext_vector_type(2))) float f32x2;
typedef __amdgpu_buffer_rsrc_t rsrc_t;

constexpr int NH = 32, SK = 16384, DH = 128, WIN = 4096, FF = 1376;
struct Prm { int E0, ES, E1, E2, E2C, EU, TEND, DT_UP, L0, L1, X1, X2, X3, X4, NE, FE, M0, M1, M2, M3, M4, GS, GX, N1, N2, GA, WD, DT_WO, LW, LU, EW, PM, NF, NR, C0, A0, AG, AC, TS, TW, TK, TG, GB, B0, BG, BF, D0, DG, TC, LM, C1, F0, FG, FF_, PA, PD, QA, QD, RT, SA, SG, SC, PV, W[9]; };
constexpr Prm PRM0 = {32, 20, 32, 32, 60, 60, 1960, 76, 12, 27, 75, 61, 96, 63, 3, 8, 0x2511f55b, 0x2b7425d5, 0x6017665, 0x17e0b9ec, 0x31944305, 62, 18, 27, 58, 50, 4, 106, 50, 28, 10, 0x0, 12, 14, 3, 36, 4, 4, 509, 215, 80, -2, 6, 69, 10, 8, 8, 6, 87, 120, 2, 58, 9, 4, 52, 14, 9, 18, 12, 45, 10, 21, 0, 0x77777777, 0x77777777, 0x77777777, 0x77777777, 0x77777777, 0x44444477, 0x44444444, 0x44444444, 0x44444444};
constexpr int NL = 37;
constexpr int SK0 = 21, SK1 = 32;
constexpr int NCMIN = (NL - SK1 + SK0) * 8;
constexpr int WK = 8, WV = 5;
constexpr int WUSE = 12, LNS = 7;
constexpr int QP = 14, QD = 4;
constexpr int VQ = 6;
constexpr int NSL = (NL + 7) / 8;
constexpr int VS = VQ + NSL;
constexpr int MLS = VS + 2;
constexpr int VSIDE = MLS - 5, UPREP = MLS + 6;
constexpr int CPF = 17;
constexpr int SMO = 1;
constexpr int RVS = 5, RVP = 8, RVL = 14, RVU = 18;
constexpr int RPS = 23, RPP = 27;
constexpr int PRB = 12;
constexpr int TBS = 7, TBP = 10, TBL = 13, TBU = 17;
constexpr int window(int s, int w) {
  int n = w;
  for (int u = s - w + 1; u <= s; ++u) n += u == SMO ? 6 : (u == TBL || u == SK1 || u == NL + CPF || u == NL + RVL || u == NL + SK1 ? 1 : (u == QP || u == NL + VQ - 4 ? 2 : (u == NL + VSIDE ? 4 : 0)));
  return n;
}
constexpr int cons_end(int v) {
  const int a = v - WV + 1, c = (v - MLS) * 4;
  const int m = a < c ? a : c;
  return m < 0 ? 0 : (m > NL ? NL : m);
}
constexpr u32 col(const Prm& P, int j) { const int m[6] = {P.M0, P.M1, P.M2, P.M3, P.M4, P.PM}; return (u32)(m[j / 3] >> (j % 3 * 10) & 1023) << 10; }
constexpr u32 first_col(const Prm& P, int k) {
  if (k < 4) return col(P, 12 + k) | 1u << (24 + k);
  if (k < 6) return col(P, k - 4);
  if (k < 8) return col(P, 4 + k) | 1u << (16 + k);
  if (k < 11) return col(P, k - 1);
  return (col(P, 2) ^ col(P, 3) ^ col(P, 4) ^ col(P, 5) ^ col(P, 6) ^ col(P, 10) ^ col(P, 11)) | 0xf00000u;
}
constexpr bool one_to_one(const Prm& P) {
  u32 c[10] = {};
  for (int j = 0; j < 10; ++j) c[j] = col(P, j);
  for (int j = 0; j < 10; ++j) {
    if (c[j] == 0) return false;
    const u32 low = c[j] & (0u - c[j]);
    for (int k = j + 1; k < 10; ++k) if (c[k] & low) c[k] ^= c[j];
  }
  return true;
}
static_assert(one_to_one(PRM0), "every KB of the window exactly once");
struct Tab { u32 e[512]; };
constexpr Tab make_tab(const Prm& P) {
  Tab T{};
  for (int bid = 0; bid < 256; ++bid) {
    const int xcd = bid & 7, part = bid >> 4 & 3, i8 = (bid >> 3 & 1) | bid >> 6 << 1, wi = i8 * 8 + xcd;
    const bool has_mod = wi < 48, has_up = wi >= 21;
    const int cb0 = (i8 + P.C0) & 7, cb1 = (i8 + P.C1) & 7, ms = wi - 20;
    const int mi = wi < 8 ? wi : (wi < 16 ? 8 + ((xcd + 4) & 7) : (wi < 20 ? (wi - 14) * 8 + 3 : 16 + ms / 7 * 8 + ms % 7 + (ms % 7 >= 3 ? 1 : 0)));
    const int ui = xcd * 5 + (xcd > 5 ? xcd - 5 : 0) + (i8 == 6 ? 0 : (xcd >= 5 ? 1 : 0) + (i8 == 7 ? 4 : i8 - 2));
    const bool edge = i8 >= 6;
    const int wd = cb0 == 3 ? 0 : P.WD;
    const int e[4] = {P.E0 - 4 * wd + (cb0 == 3 ? P.ES : 0), P.E1 + (cb1 == 3 ? P.ES : 0), has_mod ? (wi < 16 ? P.E2C : P.E2) + ((mi & 7) == 3 ? P.ES : 0) : 0, has_up ? P.EU : 0};
    int tw[4] = {}, win[4] = {}, span[4] = {}, k0[4] = {};
    for (int k = 0; k < 4; ++k) {
      const int np = k == 0 ? 8 - wd : 8;
      tw[k] = np ? e[k] * P.TW / (16 * np) : 0;
      k0[k] = P.NF + tw[k] * np / 2 > P.TK ? P.NF + tw[k] * np / 2 : P.TK;
      span[k] = P.TEND - k0[k] - P.TG - (k == 0 ? (wd ? P.DT_WO : P.A0) : (k == 3 && has_up ? P.DT_UP : 0));
      win[k] = span[k] - (k == 3 && has_up ? P.GA + (edge ? P.LM : 0) : 0);
    }
    int n[4] = {NCMIN / 8, NCMIN / 8, NCMIN / 8, NCMIN / 8};
    for (int left = 128 - NCMIN / 2; left > 0; --left) {
      int best = -1;
      for (int k = 3; k >= 0; --k)
        if (n[k] < NL && (best < 0 || n[k] * win[best] < n[best] * win[k])) best = k;
      ++n[best];
    }
    const int c[4] = {0, n[0] - 1, n[0] + n[1] - 2, n[0] + n[1] + n[2] - 3};
    const int nl = n[part];
    const bool has_w = part < 2 || (part == 2 ? has_mod : has_up);
    const int blk = !has_w ? 0 : (part == 0 ? cb0 : (part == 1 ? xcd * 8 + cb1 : (part == 2 ? mi : ui)));
    T.e[2 * bid] = (u32)(c[part] | nl << 7 | blk << 13 | (has_w ? 1 : 0) << 19 | (part == 0 ? wd : 0) << 20) | (u32)tw[part] << 24;
    T.e[2 * bid + 1] = (u32)(span[part] * P.TS / (4 * (nl - 1)) | span[part] * (1024 - P.TS) / (4 * (nl - 1)) << 16);
  }
  return T;
}
constexpr int step_range(const Prm& P, bool mx_) {
  int mx = 0, mn = 1 << 20;
  const Tab T = make_tab(P);
  for (int bid = 0; bid < 256; ++bid) { const int n = T.e[2 * bid] >> 7 & 63; mx = n > mx ? n : mx; mn = n < mn ? n : mn; }
  return mx_ ? mx : mn;
}
extern "C" __attribute__((used, visibility("protected"))) __constant__ const Tab wg_tab = make_tab(PRM0);
static_assert(step_range(PRM0, true) <= NL && step_range(PRM0, false) >= NCMIN / 8, "a wave's key range does not fit its load steps");
static_assert(RVU < UPREP && UPREP + 1 < SK0 && QP + QD + 1 < SK0 && cons_end(SK0 - 1) == SK0 - WV && NL - SK1 == WV, "steps every wave runs");
constexpr bool timers_long(const Prm& P) {
  const Tab T = make_tab(P);
  for (int bid = 48; bid < 64; ++bid) if (SK0 + NL - (int)(T.e[2 * bid] >> 7 & 63) > RPS) return false;
  return true;
}
static_assert(timers_long(PRM0), "steps of the second report");
static_assert(PRM0.WD <= 4 && PRM0.NE <= NL - SK1, "late pieces: the second half of the block at most");
constexpr int NUP = FF / 32;
constexpr int SPIN_MAX = 1 << 17;

constexpr u32 WS_EPOCH = 0;
constexpr u32 G_TB = 1024;
constexpr u32 G_MOD = 4096;
constexpr u32 G_Q = G_MOD + 3072 * 8;
constexpr u32 G_ML = G_Q + 4096 * 8;
constexpr u32 G_U = G_ML + 256 * 16;
constexpr u32 G_P = 0;
constexpr u32 G_XP = G_P + 128 * 256;
constexpr u32 G_OP = G_XP + 8 * 512 * 4;
constexpr u32 G_NT = G_OP + 43 * 512 * 8;
constexpr u32 G_PB = G_OP + 44 * 512 * 8;
constexpr u32 G_VT = G_U + 2 * (G_PB + 8192);
constexpr u32 WS_END = G_VT + 2048;
__device__ __forceinline__ u32 dup(u32 o) { return G_U + ((o >> 13) << 14) + (o & 8191u); }
constexpr u32 SENT = 0xffffffffu;
constexpr u32 OOB = 0x40000000u;

constexpr int AUX_NT = 2, AUX_SC1 = 16;

__device__ __forceinline__ float lo_f(u32 v) { return __uint_as_float(v << 16); }
__device__ __forceinline__ float hi_f(u32 v) { return __uint_as_float(v & 0xffff0000u); }
__device__ __forceinline__ float rbf(float x) { return (float)(__bf16)x; }
__device__ __forceinline__ u32 pack_bf(float lo, float hi) {
  bf16x2 r; r[0] = (__bf16)lo; r[1] = (__bf16)hi; return __builtin_bit_cast(u32, r);
}
__device__ __forceinline__ float dot2(u32 k, u32 q, float acc) {
  return __builtin_amdgcn_fdot2_f32_bf16(__builtin_bit_cast(bf16x2, k), __builtin_bit_cast(bf16x2, q), acc, false);
}
template <int CTRL> __device__ __forceinline__ float dpp(float x) {
  return __builtin_bit_cast(float, __builtin_amdgcn_mov_dpp(__builtin_bit_cast(int, x), CTRL, 0xf, 0xf, true));
}
constexpr int XOR1 = 0xB1, XOR2 = 0x4E, XOR7 = 0x141, XOR8 = 0x128;
__device__ __forceinline__ float swap32_add(float a, float b) {
  auto t = __builtin_amdgcn_permlane32_swap(__float_as_uint(a), __float_as_uint(b), false, false);
  return __uint_as_float(t[0]) + __uint_as_float(t[1]);
}
__device__ __forceinline__ float swap16_add(float a, float b) {
  auto t = __builtin_amdgcn_permlane16_swap(__float_as_uint(a), __float_as_uint(b), false, false);
  return __uint_as_float(t[0]) + __uint_as_float(t[1]);
}
__device__ __forceinline__ float wave_sum(float x) {
  x += dpp<XOR1>(x); x += dpp<XOR2>(x); x += dpp<XOR7>(x); x += dpp<XOR8>(x);
  x = swap16_add(x, x);
  return swap32_add(x, x);
}
__device__ __forceinline__ float wave_max(float x) {
  x = fmaxf(x, dpp<XOR1>(x)); x = fmaxf(x, dpp<XOR2>(x)); x = fmaxf(x, dpp<XOR7>(x)); x = fmaxf(x, dpp<XOR8>(x));
  auto s = __builtin_amdgcn_permlane16_swap(__float_as_uint(x), __float_as_uint(x), false, false);
  x = fmaxf(__uint_as_float(s[0]), __uint_as_float(s[1]));
  auto t = __builtin_amdgcn_permlane32_swap(__float_as_uint(x), __float_as_uint(x), false, false);
  return fmaxf(__uint_as_float(t[0]), __uint_as_float(t[1]));
}
template <int CTRL> __device__ __forceinline__ float sel_add(float lo, float hi, bool up) {
  const float t1 = lo + dpp<CTRL>(lo), t2 = hi + dpp<CTRL>(hi);
  return up ? t2 : t1;
}
template <int I> __device__ __forceinline__ float bc16(float x) {
  return __builtin_bit_cast(float, __builtin_amdgcn_update_dpp(0, __builtin_bit_cast(int, x), 0x150 + I, 0xf, 0xf, false));
}
__device__ __forceinline__ float bcast16(float x, int i) {
  switch (i) {
    case 0: return bc16<0>(x); case 1: return bc16<1>(x); case 2: return bc16<2>(x); case 3: return bc16<3>(x);
    case 4: return bc16<4>(x); case 5: return bc16<5>(x); case 6: return bc16<6>(x); case 7: return bc16<7>(x);
    case 8: return bc16<8>(x); case 9: return bc16<9>(x); case 10: return bc16<10>(x); case 11: return bc16<11>(x);
    case 12: return bc16<12>(x); case 13: return bc16<13>(x); case 14: return bc16<14>(x); default: return bc16<15>(x);
  }
}
__device__ __forceinline__ float silu_f(float x) { return x * __builtin_amdgcn_rcpf(1.f + __expf(-x)); }

template <int AUX> __device__ __forceinline__ u32x4 ld16(rsrc_t r, u32 voff, u32 soff) {
  return __builtin_amdgcn_raw_buffer_load_b128(r, voff, soff, AUX);
}
template <int AUX> __device__ __forceinline__ u32x2 ld8(rsrc_t r, u32 voff, u32 soff) {
  return __builtin_amdgcn_raw_buffer_load_b64(r, voff, soff, AUX);
}
__device__ __forceinline__ void st8_sc1(rsrc_t r, u32 voff, u32 v, u32 tag) {
  __builtin_amdgcn_raw_buffer_store_b64(u32x2{v, tag}, r, voff, 0, AUX_SC1);
}
__device__ __forceinline__ void st16_sc1(rsrc_t r, u32 voff, u32 v0, u32 v1, u32 tag) {
  __builtin_amdgcn_raw_buffer_store_b128(u32x4{v0, tag, v1, tag}, r, voff, 0, AUX_SC1);
}
__device__ __forceinline__ rsrc_t mk_rsrc(const void* p, u32 bytes) {
  return __builtin_amdgcn_make_buffer_rsrc(const_cast<void*>(p), 0, bytes, 0x00020000);
}
#define WAIT_VM(n) __builtin_amdgcn_s_waitcnt(((n) & 15) | (((n) >> 4) << 14) | (7 << 4) | (15 << 8))
#define SB() __builtin_amdgcn_sched_barrier(0)
#define CFENCE() asm volatile("" ::: "memory")
__device__ __forceinline__ bool all_ok(bool ok) { return __builtin_amdgcn_ballot_w64(!ok) == 0ull; }

template <int N> __device__ __forceinline__ void gather16(rsrc_t ws, u32 voff, u32 stride, u32 tag, u32x4 (&g)[N]) {
#pragma nounroll
  for (int spin = 0; spin < SPIN_MAX; ++spin) {
    CFENCE();
    bool ok = true;
#pragma unroll
    for (int i = 0; i < N; ++i) g[i] = ld16<AUX_SC1>(ws, voff + i * stride, 0);
#pragma unroll
    for (int i = 0; i < N; ++i) ok &= (g[i][1] == tag) & (g[i][3] == tag);
    if (all_ok(ok)) break;
    __builtin_amdgcn_s_sleep(8);
  }
}
template <int N> __device__ __forceinline__ void gather8(rsrc_t ws, u32 voff, u32 stride, u32 tag, u32 (&g)[N], bool act = true) {
#pragma nounroll
  for (int spin = 0; spin < SPIN_MAX; ++spin) {
    CFENCE();
    bool ok = true;
    u32x2 t[N];
#pragma unroll
    for (int i = 0; i < N; ++i) t[i] = ld8<AUX_SC1>(ws, voff + i * stride, 0);
#pragma unroll
    for (int i = 0; i < N; ++i) { ok &= (t[i][1] == tag); g[i] = t[i][0]; }
    if (all_ok(ok | !act)) break;
    __builtin_amdgcn_s_sleep(8);
  }
}

struct Smem {
  float red[8][128];
  float ml[8][2];
  float r2[64 * 20];
  u16 hb[3 * 1024];
  float stat[2][64];
  float red2[8][128];
  u32 dummy[64];
  u32x2 bc, bc2;
  u32 bc3, bc4;
  u32x4 zero[4];
  float pl[4][8];
  u32 mflag[8];
  u32x4 wbuf[8][8][64];
  __attribute__((aligned(256))) u16 w2b[32][512];
  u16 actl[2][32];
};

#define PIN(x) asm volatile("" : "+v"(x))
#define LDS_BARRIER() do { __builtin_amdgcn_s_waitcnt(0xc07f); __builtin_amdgcn_s_barrier(); CFENCE(); } while (0)
#define WAVE_SYNC() do { __builtin_amdgcn_fence(__ATOMIC_RELEASE, "wavefront"); __builtin_amdgcn_wave_barrier(); __builtin_amdgcn_fence(__ATOMIC_ACQUIRE, "wavefront"); } while (0)

__device__ __forceinline__ void dma16(rsrc_t r, u32 voff, u32 soff, u32 lds_dst) {
  u32 keep;
  asm volatile("s_mov_b32 %0, m0\n\ts_mov_b32 m0, %4\n\ts_nop 4\n\tbuffer_load_dwordx4 %1, %2, %3 offen nt lds\n\ts_mov_b32 m0, %0"
               : "=&s"(keep) : "v"(voff), "s"(r), "s"(soff), "s"(lds_dst) : "memory");
}
__device__ __forceinline__ void ld16_into(u32x4& d, rsrc_t r, u32 voff, u32 soff) {
  asm volatile("s_nop 4\n\tbuffer_load_dwordx4 %0, %1, %2, %3 offen nt" : "+v"(d) : "v"(voff), "s"(r), "s"(soff) : "memory");
}
__device__ __forceinline__ u32 perm_lo(u32 a, u32 b) { return __builtin_amdgcn_perm(b, a, 0x05040100u); }
__device__ __forceinline__ u32 perm_hi(u32 a, u32 b) { return __builtin_amdgcn_perm(b, a, 0x07060302u); }
__device__ __forceinline__ float rdl(float x, int l) { return __int_as_float(__builtin_amdgcn_readlane(__float_as_int(x), l)); }

typedef __attribute__((ext_vector_type(8))) __bf16 bf16x8;
__device__ __forceinline__ f32x4 mfma16(u32x4 a, u32x4 b, f32x4 c) {
  return __builtin_amdgcn_mfma_f32_16x16x32_bf16(__builtin_bit_cast(bf16x8, a), __builtin_bit_cast(bf16x8, b), c, 0, 0, 0);
}
typedef __attribute__((ext_vector_type(8))) unsigned u32x8;
typedef __attribute__((ext_vector_type(16))) __bf16 bf16x16;
__device__ __forceinline__ f32x4 mfma64(u32x4 a, u32x8 b, f32x4 c, u32 idx) {
  return __builtin_amdgcn_smfmac_f32_16x16x64_bf16(__builtin_bit_cast(bf16x8, a), __builtin_bit_cast(bf16x16, b), c, idx, 0, 0);
}
__device__ __forceinline__ float sum8(const float* p) {
  const f32x4 a = *reinterpret_cast<const f32x4*>(p), b = *reinterpret_cast<const f32x4*>(p + 4);
  return ((a[0] + b[0]) + (a[1] + b[1])) + ((a[2] + b[2]) + (a[3] + b[3]));
}
__device__ __forceinline__ float ln_ri(float var) {
  return __builtin_amdgcn_rcpf(rbf(__builtin_amdgcn_sqrtf(rbf(rbf(fmaxf(var, 0.f)) + 1e-5f))));
}
__device__ __forceinline__ void ln_stats2(const float (&x0)[8], const float (&x1)[8], float& mb0, float& ri0, float& mb1, float& ri1) {
  const float k0 = rdl(x0[0], 0), k1 = rdl(x1[0], 0);
  float s0 = 0.f, s1 = 0.f, q0 = 0.f, q1 = 0.f;
#pragma unroll
  for (int k = 0; k < 8; ++k) {
    const float d0 = x0[k] - k0, d1 = x1[k] - k1;
    s0 += d0; s1 += d1; q0 = fmaf(d0, d0, q0); q1 = fmaf(d1, d1, q1);
  }
  float t = swap16_add(swap32_add(s0, s1), swap32_add(q0, q1));
  t += dpp<XOR1>(t); t += dpp<XOR2>(t); t += dpp<XOR7>(t); t += dpp<XOR8>(t);
  auto u = __builtin_amdgcn_permlane16_swap(__float_as_uint(t), __float_as_uint(t), false, false);
  const float ms = __uint_as_float(u[0]) * (1.f / 512.f), mq = __uint_as_float(u[1]) * (1.f / 512.f);
  const float ri = ln_ri(fmaf(-ms, ms, mq));
  mb0 = rbf(k0 + rdl(ms, 0)); mb1 = rbf(k1 + rdl(ms, 32));
  ri0 = rdl(ri, 0); ri1 = rdl(ri, 32);
}
template <int I> struct IC { static constexpr int v = I; };
template <int I, int N, class F> __device__ __forceinline__ void sfor(F&& f) {
  if constexpr (I < N) { if (f(IC<I>{})) sfor<I + 1, N>(f); }
}
typedef __attribute__((ext_vector_type(4))) short s16x4;
typedef __attribute__((address_space(3))) s16x4* lds_s16x4p;
__device__ __forceinline__ u32x2 tr_read(u32 lds_addr) {
  return __builtin_bit_cast(u32x2, __builtin_amdgcn_ds_read_tr16_b64_v4i16((lds_s16x4p)lds_addr));
}
__device__ __forceinline__ u32 next_tag(u32 e) { return ((e | 0x40004000u) + 1u) & ~0x40004000u; }
__device__ __forceinline__ float u2f(u32 v) { return __uint_as_float(v); }
#define LDSP(T, a) ((__attribute__((address_space(3))) T*)(a))
template <class T> __device__ __forceinline__ u32 lds_addr(T* p) { u32 a = (u32)(uintptr_t)p; PIN(a); return a; }

#define TAB_LOAD "s_add_u32 s30, s28, wg_tab@rel32@lo+4\ns_addc_u32 s31, s29, wg_tab@rel32@hi+12\nglobal_load_dwordx2 %[te], %[va], s[30:31]\n"
#define FC(k) ".Lfc" #k
#define FX(r, bit, k) "s_bitcmp1_b32 %[" #r "], " #bit "\ns_cselect_b32 %[sb], " FC(k) ", 0\ns_xor_b32 %[sa], %[sa], %[sb]\n"
__global__ void __launch_bounds__(512, 2) fused_kernel(const u16* kc_, const u16* vc_, const u16* wo_, const u16* wq_, const u16* wmod_, const u16* w1_, const u16* w3_,
                                                       unsigned char* ws_, const u16* x_, const u16* cnd_, const u16* w2_, u16* out_) {
  __shared__ __attribute__((aligned(256))) Smem sm;
  const u64 a_ka = (u64)__builtin_amdgcn_kernarg_segment_ptr();
  const int bid = blockIdx.x;
  int tid = threadIdx.x;
  u64 wa, t64; u32x2 te; int tid0;
  u32 ar[14];
  u32 tch2, sa_, sb_, va_;
  u32x4 kr[NL], vr[NL];
  asm volatile(".set .Lfc0, %0\n.set .Lfc1, %1\n.set .Lfc2, %2\n.set .Lfc3, %3\n.set .Lfc4, %4\n.set .Lfc5, %5\n.set .Lfc6, %6\n.set .Lfc7, %7\n.set .Lfc8, %8\n.set .Lfc9, %9\n.set .Lfc10, %10\n.set .Lfc11, %11"
    :: "n"(first_col(PRM0, 0)), "n"(first_col(PRM0, 1)), "n"(first_col(PRM0, 2)), "n"(first_col(PRM0, 3)), "n"(first_col(PRM0, 4)), "n"(first_col(PRM0, 5)),
       "n"(first_col(PRM0, 6)), "n"(first_col(PRM0, 7)), "n"(first_col(PRM0, 8)), "n"(first_col(PRM0, 9)), "n"(first_col(PRM0, 10)), "n"(first_col(PRM0, 11)));
  asm volatile(
    "s_load_dwordx16 s[4:19], %[ka], 0x0\ns_load_dwordx8 s[20:27], %[ka], 0x40\n"
    "s_memrealtime %[t]\n"
    "v_lshlrev_b32 %[va], 3, %[bid]\n"
    "s_getpc_b64 s[28:29]\n.Lcpf_first:\n"
    TAB_LOAD
    "s_cmp_gt_u32 %[bid], 15\ns_cbranch_scc1 .Lno_cpf\n"
    "s_mov_b32 %[sa], .Lend_a-.Lcpf_first\ns_max_i32 %[sa], %[sa], .Lend_b-.Lcpf_first\ns_max_i32 %[sa], %[sa], .Lend_c-.Lcpf_first\ns_max_i32 %[sa], %[sa], .Lend_d-.Lcpf_first\ns_max_i32 %[sa], %[sa], .Lend_e-.Lcpf_first\n"
    "v_lshlrev_b32 %[va], 6, %[tid]\nv_min_u32 %[va], %[sa], %[va]\nglobal_load_dword %[pf], %[va], s[28:29]\n"
    ".Lno_cpf:\n"
    "v_readfirstlane_b32 %[t0], %[tid]\nv_and_b32 %[va], 63, %[tid]\n"
    "s_mov_b32 %[sa], " FC(11) "\n"
    FX(bid, 0, 0) FX(bid, 1, 1) FX(bid, 2, 2) FX(bid, 3, 3) FX(bid, 4, 4) FX(bid, 5, 5) FX(bid, 6, 6) FX(bid, 7, 7) FX(t0, 6, 8) FX(t0, 7, 9) FX(t0, 8, 10)
    "v_lshl_add_u32 %[va], %[va], 4, %[sa]\n"
    "s_waitcnt lgkmcnt(0)\nglobal_load_dwordx4 %[k0], %[va], s[4:5] nt\nglobal_load_dwordx4 %[v0], %[va], s[6:7] nt\n"
    "s_bitcmp1_b32 %[t0], 6\ns_cselect_b64 s[14:15], s[16:17], s[14:15]\n"
    "s_bitcmp1_b32 %[bid], 4\ns_cselect_b64 s[8:9], s[10:11], s[8:9]\ns_cselect_b64 s[12:13], s[14:15], s[12:13]\ns_bitcmp1_b32 %[bid], 5\ns_cselect_b64 %[wa], s[12:13], s[8:9]"
    : "=&{s4}"(ar[0]), "=&{s5}"(ar[1]), "=&{s6}"(ar[2]), "=&{s7}"(ar[3]), "=&{s18}"(ar[4]), "=&{s19}"(ar[5]), "=&{s20}"(ar[6]), "=&{s21}"(ar[7]), "=&{s22}"(ar[8]), "=&{s23}"(ar[9]),
      "=&{s24}"(ar[10]), "=&{s25}"(ar[11]), "=&{s26}"(ar[12]), "=&{s27}"(ar[13]), [wa] "=&s"(wa), [t0] "=&s"(tid0), [t] "=&s"(t64), [te] "=&v"(te), [sa] "=&s"(sa_), [sb] "=&s"(sb_),
      [va] "=&v"(va_), [pf] "=&v"(tch2), [k0] "=&v"(kr[0]), [v0] "=&v"(vr[0])
    : [ka] "s"(a_ka), [bid] "s"(bid), [tid] "v"(tid) : "s8", "s9", "s10", "s11", "s12", "s13", "s14", "s15", "s16", "s17", "s28", "s29", "s30", "s31", "scc", "memory");
  asm volatile("" : "+s"(ar[0]), "+s"(ar[1]), "+s"(ar[2]), "+s"(ar[3]), "+s"(ar[4]), "+s"(ar[5]), "+s"(ar[6]), "+s"(ar[7]), "+s"(ar[8]), "+s"(ar[9]), "+s"(ar[10]), "+s"(ar[11]), "+s"(ar[12]), "+s"(ar[13]));
  asm volatile("" : "+s"(wa), "+s"(t64), "+s"(tid0), "+s"(sa_));
  const u32 tka = (u32)wall_clock64();
  __builtin_assume(tid >= 0 && tid < 512 && tid0 >= 0 && tid0 < 512 && bid >= 0 && bid < 256);
  constexpr Prm P = PRM0;
  const u32 t0 = tka + min(max((int)(tka - (u32)t64) - P.SA, 0) * P.SG >> 4, P.SC);
  const int TEND = P.TEND;
  const int xcd = bid & 7, part = bid >> 4 & 3;
  int lane = tid & 63;
  const int wave = tid0 >> 6;
  SB();
  if ((bid >> 4) == 2) {
    const u32 o = tid < 384 ? G_U + (bid & 15) * 6144 + tid * 16 : OOB;
    __builtin_amdgcn_raw_buffer_store_b128(u32x4{SENT, SENT, SENT, SENT}, mk_rsrc((void*)(ar[4] | (u64)ar[5] << 32), WS_END), o, 0, AUX_SC1);
  }
  SB();
  const bool p3 = part == 3;
  u32 pitch = (u32)(0x005600c001000020ull >> (part * 16) & 0xffff) * 32u;
  const int sh = p3 ? 2 : 3;
  u32 wv0 = (u32)((p3 ? (wave >> 1) * 128 : wave * 64) + (part == 0 ? xcd * 512 : 0) + (lane >> sh)) * pitch + ((lane & (p3 ? 3 : 7)) ^ (lane >> (p3 ? 5 : 3))) * 16u;
  const u32 wimg = (u32)(uintptr_t)(&sm.wbuf[0][0][0]);
  u32 wsz = (part == 0 ? 4096u : 512u) * pitch, wld = wimg + (p3 ? (wave & 1) * 32768 + (wave >> 1) * 8192 : wave * 8192);
  PIN(wv0);
  asm volatile("" : "+s"(wsz), "+s"(wld), "+s"(pitch));
  auto karg = [&](int i) { return ar[2 * i] | (u64)ar[2 * i + 1] << 32; };
  const u64 a_kc = karg(0), a_vc = karg(1);
  unsigned char* ws = (unsigned char*)karg(2);
  const u16 *x = (const u16*)karg(3), *cnd = (const u16*)karg(4), *w2 = (const u16*)karg(5);
  u16* out = (u16*)karg(6);
  const rsrc_t wsr = mk_rsrc(ws, WS_END);
  asm volatile("s_waitcnt vmcnt(3)\n\ts_cmp_eq_u32 %1, 2\n\ts_cbranch_scc1 1f\n\ts_waitcnt vmcnt(2)\n1:" : "+v"(te) : "s"(bid >> 4) : "scc");
  const u32 te0 = __builtin_amdgcn_readfirstlane(te[0]), te1 = __builtin_amdgcn_readfirstlane(te[1]);
  const int bl0 = te0 & 127, nl = te0 >> 7 & 63, blk = te0 >> 13 & 63;
  const bool has_w = te0 >> 19 & 1;
  const rsrc_t rw = mk_rsrc((const void*)wa, wsz);
  const u32 wstep = pitch << (6 - sh), wflip = (p3 || part == 0) ? 32u : 0u, wflip0 = part == 0 ? 32u : 0u;
  u32 wso = blk << (sh + 4);
  int wleft = 8;
  auto piece = [&]() {
    dma16(rw, wv0, wso, wld);
    wso += wstep; wld += 1024;
    wv0 ^= (wleft & 1) ? wflip : wflip0;
    --wleft;
  };
  SB();
  const int b = bid >> 3 & 1, head = xcd * 4 + (bid >> 6 ^ 3);
  int g = lane >> 4, c = lane & 15;
  const int i8 = b | bid >> 6 << 1, wi = i8 * 8 + xcd;
  const bool is_up = p3 && has_w, modc = part == 2 && wi < 16;
  const size_t base = ((size_t)(b * NH + head) * SK + (SK - WIN)) * DH;
  const rsrc_t rk = mk_rsrc((const u16*)a_kc + base, WIN * DH * 2), rv = mk_rsrc((const u16*)a_vc + base, WIN * DH * 2);
  int tau = te1 & 0xffff;
  const int tauw = te0 >> 24 << 7, wdef = te0 >> 20 & 15;
  int due = P.NF * 256, now = 0;
  auto gate = [&]() {
    while (now < due) { now = (int)(((u32)wall_clock64() - t0) << 8); if (now < due) __builtin_amdgcn_s_sleep(1); }
    due += tau;
  };
  auto until = [&](int t) { while ((int)(((u32)wall_clock64() - t0) << 8) < t); };
  const u32 pslot = dup(G_P + (head & 15) * 2048 + (b * 4 + part) * 16);
  const u32 die = xcd >> 2;
  const bool voter = p3 && i8 == 1, timer = p3 && i8 < 2;
  int vote = 0, lag = 0, shift = 0;
  const u32 pbo = dup(G_PB) + wave * 1152;
  u32 near = 0;
  u32x2 ep; u32 xs0, xs1, tch1; u32x4 xr0, xr1;
  const rsrc_t rx = mk_rsrc(part == 2 ? cnd : x, (part == 1 || (part == 2 && has_w)) ? 2048u : 0u);
  auto small_ops = [&]() {
    ep = ld8<AUX_SC1>(wsr, wave == 7 ? WS_EPOCH : OOB, 0);
    xs0 = __builtin_amdgcn_raw_buffer_load_b16(rx, tid * 2, 0, 0); xs1 = __builtin_amdgcn_raw_buffer_load_b16(rx, tid * 2, 1024, 0);
    xr0 = ld16<0>(rx, part == 1 ? lane * 16 : OOB, 0); xr1 = ld16<0>(rx, part == 1 ? lane * 16 : OOB, 1024);
    tch1 = __builtin_amdgcn_raw_buffer_load_b32(mk_rsrc(w2 + (size_t)blk * 32 * 512, is_up ? 64u : 0u), tid == 384 ? 0 : OOB, 0, AUX_NT);
  };
  if (has_w) {
    if (modc) now = 1 << 30;
#pragma nounroll
    while (wleft > wdef) { gate(); due += tauw - tau; piece(); }
    now = 0;
  }
  due = max(due, P.TK * 256);
  SB();
  u32 sot = sa_ & 0xffc00u;
  {
    const u32 s1 = (bl0 + lane - 1 - (lane >= SK0 ? NL - nl : 0)) ^ (124 + part);
#pragma unroll
    for (int k = 0; k < 7; ++k) sot ^= (u32)-(int)(s1 >> k & 1u) & col(P, k);
    PIN(sot);
  }
  u32 tag = 0;
  u32x4 bf[8];
  auto wo_prep = [&]() {
    const u32 wi_ = wimg + wave * 8192;
    const int q = (lane >> 2) & 3, pp = lane & 3;
#pragma unroll
    for (int s = 0; s < 2; ++s) {
#pragma unroll
      for (int t = 0; t < 4; ++t) {
        const int r0 = s * 32 + g * 8 + q, r1 = r0 + 4, ch = t * 2 + (pp >> 1);
        const u32x2 lo = tr_read(wi_ + r0 * 128 + ((ch ^ (r0 & 7)) * 16) + (pp & 1) * 8), hi = tr_read(wi_ + r1 * 128 + ((ch ^ (r1 & 7)) * 16) + (pp & 1) * 8);
        bf[s * 4 + t] = u32x4{lo[0], lo[1], hi[0], hi[1]};
      }
    }
  };
  auto gemv_go = [&](u32 hh) {
    sm.hb[tid] = (u16)hh; sm.hb[512 + tid] = (u16)(hh >> 16);
    WAVE_SYNC();
    const u32x4* a = (c & 11) == 0 ? reinterpret_cast<const u32x4*>(&sm.hb[(c >> 2) * 512 + wave * 64 + g * 8]) : &sm.zero[0];
    const u32x4 a0 = a[0], a1 = a[(c & 11) == 0 ? 4 : 0];
    const f32x4 z = {0.f, 0.f, 0.f, 0.f};
    f32x4 d[4];
#pragma unroll
    for (int t = 0; t < 4; ++t) d[t] = mfma16(a0, bf[t], z);
#pragma unroll
    for (int t = 0; t < 4; ++t) d[t] = mfma16(a1, bf[4 + t], d[t]);
    if (lane < 32) {
#pragma unroll
      for (int t = 0; t < 4; ++t) sm.r2[(t * 16 + c) * 20 + g * 8 + wave] = d[t][0];
    }
  };
  auto mod_stage = [&]() {
    wo_prep();
    gemv_go(pack_bf(silu_f(lo_f(xs0)), silu_f(lo_f(xs1))));
    __builtin_amdgcn_s_waitcnt(0xc07f);
    if (lane == 0) *LDSP(volatile u32, (u32)(uintptr_t)&sm.mflag[wave]) = 1u;
    if (wave == 7) {
#pragma nounroll
      for (int spin = 0; spin < SPIN_MAX; ++spin) {
        const u32 f = *LDSP(volatile u32, (u32)(uintptr_t)&sm.mflag[lane & 7]);
        if (all_ok(f != 0u)) break;
        __builtin_amdgcn_s_sleep(1);
      }
      CFENCE();
      st8_sc1(wsr, G_MOD + (blk * 64 + lane) * 8, pack_bf(sum8(&sm.r2[lane * 20]), sum8(&sm.r2[lane * 20 + 8])), tag);
    }
  };
  if (lane < 4) sm.zero[lane] = u32x4{0u, 0u, 0u, 0u};
  if (part == 2 && has_w) {
    if (tid < 8) sm.mflag[tid] = 0u;
    LDS_BARRIER();
    if (__builtin_expect(modc, 0)) {
      small_ops();
      asm volatile("s_waitcnt vmcnt(0)" ::: "memory");
      tag = next_tag(__builtin_amdgcn_readfirstlane(ep[0]));
      mod_stage();
      __builtin_amdgcn_s_barrier();
    }
  }
  SB();
  int fd = (xcd * 8 + i8) * 8 + (lane & 3) * 2;
  const int ub = wave >> 2, ud = (wave & 3) * 128 + c * 8 + g * 2;
  u32x4 sg[3]; u32 sx2;
  auto side_loads = [&]() {
    const bool fin = part == 1 && wave == 0;
    sg[0] = ld16<AUX_SC1>(wsr, is_up ? G_MOD + (1024 + ud) * 8 : (fin ? G_MOD + (1024 + fd) * 8 : OOB), 0);
    sg[1] = ld16<AUX_SC1>(wsr, is_up ? G_MOD + (1536 + ud) * 8 : (fin ? G_MOD + (2560 + fd) * 8 : OOB), 0);
    sg[2] = ld16<AUX_SC1>(wsr, is_up ? G_MOD + (2048 + ud) * 8 : OOB, 0);
    sx2 = __builtin_amdgcn_raw_buffer_load_b32(mk_rsrc(x, (is_up || fin) ? 2048u : 0u), is_up ? ub * 1024 + ud * 2 : (g & 1) * 1024 + fd * 2, 0, 0);
  };
  auto side_pin = [&]() { PIN(sg[0]); PIN(sg[1]); PIN(sg[2]); PIN(sx2); };
  const u32 mlo = G_ML + (head * 2 + b) * 64 + (lane & 3) * 16;
  float fmrg = 0.f;
  auto merge_factor = [&](const u32x4& ml) {
    const float mi = u2f(ml[0]);
    float M = fmaxf(mi, dpp<XOR1>(mi)); M = fmaxf(M, dpp<XOR2>(M));
    const float e = __expf(mi - M);
    float L = e * u2f(ml[2]);
    L += dpp<XOR1>(L); L += dpp<XOR2>(L);
    fmrg = rdl(e * __builtin_amdgcn_rcpf(L), part);
  };
  f32x2 ga = {0.f, 0.f}, xin = {0.f, 0.f}, cc1 = {0.f, 0.f};
  float Af = 0.f, Bf = 0.f;
  int hpos = ub * 512 + (ud & ~15) + (g & 1) * 8 + (c & 1) * 4 + (g >> 1) * 2;
  auto up_prep = [&]() {
    if (!all_ok((sg[0][1] == tag) & (sg[0][3] == tag) & (sg[1][1] == tag) & (sg[1][3] == tag) & (sg[2][1] == tag) & (sg[2][3] == tag))) gather16<3>(wsr, G_MOD + (1024 + ud) * 8, 512 * 8, tag, sg);
    ga = f32x2{ub ? hi_f(sg[0][0]) : lo_f(sg[0][0]), ub ? hi_f(sg[0][2]) : lo_f(sg[0][2])};
    cc1 = f32x2{rbf(1.f + (ub ? hi_f(sg[2][0]) : lo_f(sg[2][0]))), rbf(1.f + (ub ? hi_f(sg[2][2]) : lo_f(sg[2][2])))};
    xin = f32x2{lo_f(sx2), hi_f(sx2)};
    u32* hp = reinterpret_cast<u32*>(&sm.hb[hpos]);
    hp[512] = pack_bf(cc1[0], cc1[1]);
    hp[1024] = ub ? perm_hi(sg[1][0], sg[1][2]) : perm_lo(sg[1][0], sg[1][2]);
  };
  u32x8 bw[4];
  auto up_frags = [&]() {
    const int pp = lane & 3;
    const u32 a = wimg + (pp & 1) * 32768 + ((pp >> 1) * 32 + g * 8 + ((lane >> 2) & 3)) * 64 + (((wave >> 1) ^ ((g & 1) | (pp & 2))) * 16) + (wave & 1) * 8;
#pragma unroll
    for (int j = 0; j < 4; ++j) {
      const u32x2 r0 = tr_read(a + j * 4096), r1 = tr_read(a + j * 4096 + 256), r2 = tr_read(a + j * 4096 + 16384), r3 = tr_read(a + j * 4096 + 16640);
      bw[j] = u32x8{r0[0], r0[1], r1[0], r1[1], r2[0], r2[1], r3[0], r3[1]};
    }
  };
  u32 wfa[4];
  auto wo_frags = [&](int s0) {
#pragma unroll
    for (int t = 0; t < 4; ++t) {
#pragma unroll
      for (int s = s0; s < 2; ++s) {
        const u32x2 lo = tr_read(wfa[t] + s * 4096), hi = tr_read(wfa[t ^ 1] + s * 4096 + 256);
        bw[t][s * 4] = lo[0]; bw[t][s * 4 + 1] = lo[1]; bw[t][s * 4 + 2] = hi[0]; bw[t][s * 4 + 3] = hi[1];
      }
    }
  };
  const bool par = lane & 8;
  auto limit = [&](auto sc) {
    constexpr int s = sc.v;
    WAIT_VM(window(s, 1 + (PRM0.W[s >> 3] >> (4 * (s & 7)) & 7)));
    SB();
  };
  auto sample = [&]() { if (timer) lag += (int)(((u32)wall_clock64() - t0) << 8) - due + ((int)__builtin_amdgcn_s_getreg(7 | (3 << 11)) + 1) * tau; };
  auto report = [&](u32 dst) {
    if (timer) {
      if (lane == 0) sm.mflag[wave] = vote * 65536 + min(max(lag >> 8, 0), 4000);
      lag = 0;
      LDS_BARRIER();
      if (wave == 7) {
        int s = sm.mflag[lane & 7];
        s += __builtin_amdgcn_mov_dpp(s, XOR1, 0xf, 0xf, true); s += __builtin_amdgcn_mov_dpp(s, XOR2, 0xf, 0xf, true); s += __builtin_amdgcn_mov_dpp(s, XOR7, 0xf, 0xf, true);
        if (lane < 8) st8_sc1(wsr, dst + lane * 128 + wi * 8, s, tag);
      }
    }
    SB();
  };
  float l0 = 0.f, l1 = 0.f;
  u32x2 gmd[2], tbg;
  int t1 = 0;
#pragma unroll
  for (int i = SK0; i < SK1; ++i) kr[i] = u32x4{0u, 0u, 0u, 0u};
  const int first = SK0 + NL - nl;
  auto left_out = [&](int i) { int f = first; asm volatile("" : "+s"(f)); return i < f; };
  sfor<1, NL>([&](auto ic) {
    constexpr int i = ic.v;
    if constexpr (i >= SK0 && i < SK1) { if (left_out(i)) return true; }
    const u32 so = __builtin_amdgcn_readlane(sot, i);
    gate();
    if constexpr ((i >= TBS && i <= TBP) || (i >= RPS && i <= RPP)) sample();
    SB();
    if constexpr (i >= SK0 && i < SK1) ld16_into(kr[i], rk, lane * 16, so); else kr[i] = ld16<AUX_NT>(rk, lane * 16, so);
    if constexpr (i == SMO) small_ops();
    if constexpr (i == SMO + 1) { if (voter && (xcd & 3) == 0) __builtin_amdgcn_raw_buffer_store_b32(0u, wsr, pbo + (lane >> 5) * 8192 + (lane & 31) * 4, 0, AUX_SC1); }
    if constexpr (i == SK1) tbg = ld8<AUX_SC1>(wsr, wave == 7 ? G_VT + xcd * 128 + c * 8 : OOB, 0);
    if constexpr (i == TBL) tbg = ld8<AUX_SC1>(wsr, wave == 7 ? G_TB + xcd * 128 + c * 8 : OOB, 0);
    if constexpr (i == QP) { gmd[0] = ld8<AUX_SC1>(wsr, part == 1 ? G_MOD + tid * 8 : OOB, 0); gmd[1] = ld8<AUX_SC1>(wsr, part == 1 ? G_MOD + tid * 8 : OOB, 4096); }
    SB();
    SB();
    if (i >= 4) limit(ic);
    if (i == TBP) {
      if (timer) {
        if (lane == 0) sm.mflag[wave] = lag;
        lag = 0;
        LDS_BARRIER();
        if (wave == 7) {
          int s = sm.mflag[lane & 7];
          s += __builtin_amdgcn_mov_dpp(s, XOR1, 0xf, 0xf, true); s += __builtin_amdgcn_mov_dpp(s, XOR2, 0xf, 0xf, true); s += __builtin_amdgcn_mov_dpp(s, XOR7, 0xf, 0xf, true);
          if (lane < 8) st8_sc1(wsr, G_TB + lane * 128 + wi * 8, t0 << 10 | (u32)min(s / (2048 * (TBP - TBS + 1)), 1023), tag);
        }
      }
      SB();
    }
    if (i == PRB) {
      if (voter) {
        u64 ta, tb, tc; u32 dm;
        const u32 pa = pbo + (wave & 1) * 8192;
        asm volatile("s_memtime %0\n\ts_waitcnt lgkmcnt(0)\n\ts_load_dword %3, %4, %5\n\ts_waitcnt lgkmcnt(0)\n\ts_memtime %1\n\ts_waitcnt lgkmcnt(0)\n\t"
                     "s_load_dword %3, %4, %6\n\ts_waitcnt lgkmcnt(0)\n\ts_memtime %2\n\ts_waitcnt lgkmcnt(0)"
                     : "=&s"(ta), "=&s"(tb), "=&s"(tc), "=&s"(dm) : "s"(ws), "s"(pa), "s"(pa ^ 8192u));
        const int d = min(max((int)(2 * (u32)tb - (u32)ta - (u32)tc), -500), 500);
        vote = ((wave ^ die) & 1) ? -d : d;
      }
      SB();
    }
    if (i == TBU) {
      if (wave == 7) {
        const bool ok = tbg[1] == tag;
        float lf = ok ? (float)min(max((int)((t0 << 10 | 1023u) - tbg[0]) >> 10, -300), 300) : 0.f, nf = ok ? 1.f : 0.f, lg = ok ? (float)(tbg[0] & 1023u) : 0.f;
        lf += dpp<XOR1>(lf); nf += dpp<XOR1>(nf); lg += dpp<XOR1>(lg); lf += dpp<XOR2>(lf); nf += dpp<XOR2>(nf); lg += dpp<XOR2>(lg);
        lf += dpp<XOR7>(lf); nf += dpp<XOR7>(nf); lg += dpp<XOR7>(lg); lf += dpp<XOR8>(lf); nf += dpp<XOR8>(nf); lg += dpp<XOR8>(lg);
        const float rn = __builtin_amdgcn_rcpf(fmaxf(nf, 1.f));
        shift = __builtin_amdgcn_readfirstlane(min(max((int)((lg * rn - (float)P.GS) * (float)P.GB * 0.125f), 0), 150));
        if (lane == 0) sm.bc = u32x2{tag, t0 - (u32)(int)(lf * rn) + shift};
      }
      LDS_BARRIER();
      const u32x2 bc = sm.bc;
      tag = __builtin_amdgcn_readfirstlane(bc[0]);
      t1 = (__builtin_amdgcn_readfirstlane((int)(bc[1] - t0)) + TEND + P.X1) * 256;
      tau -= __builtin_amdgcn_readfirstlane((int)((float)(int)(t0 - bc[1]) * 256.f * __builtin_amdgcn_rcpf((float)(nl - TBU - 1))));
      SB();
    }
    if (i == RPP) report(G_VT);
    if (i == LNS) {
      tag = next_tag(__builtin_amdgcn_readfirstlane(ep[0]));
      if (part == 1) {
        float x0[8], x1[8], mb0, ri0, mb1, ri1;
#pragma unroll
        for (int k = 0; k < 4; ++k) { x0[2 * k] = lo_f(xr0[k]); x0[2 * k + 1] = hi_f(xr0[k]); x1[2 * k] = lo_f(xr1[k]); x1[2 * k + 1] = hi_f(xr1[k]); }
        ln_stats2(x0, x1, mb0, ri0, mb1, ri1);
        l0 = rbf(rbf(lo_f(xs0) - mb0) * ri0); l1 = rbf(rbf(lo_f(xs1) - mb1) * ri1);
      }
      PIN(l0); PIN(l1);
      SB();
    }
    if (i == WUSE) {
      PIN(tch1); PIN(tch2);
      if ((part == 1 || (part == 2 && has_w && !modc))) {
        WAIT_VM(WK); CFENCE();
        if (part == 2) mod_stage(); else wo_prep();
      }
#pragma unroll
      for (int k = 0; k < 8; ++k) PIN(bf[k]);
      SB();
    }
    if (i == QP + QD) {
      if (part == 1) {
        u32 gm[2] = {gmd[0][0], gmd[1][0]};
        if (!all_ok((gmd[0][1] == tag) & (gmd[1][1] == tag))) gather8<2>(wsr, G_MOD + tid * 8, 512 * 8, tag, gm);
        gemv_go(pack_bf(rbf(l0 * rbf(1.f + lo_f(gm[1]))) + lo_f(gm[0]), rbf(l1 * rbf(1.f + hi_f(gm[1]))) + hi_f(gm[0])));
      }
      SB();
    }
    if (i == QP + QD + 1) {
      if (part == 1) {
        LDS_BARRIER();
        if (wave == 0) {
          const int bb = lane >> 5, jq = lane & 31;
          st8_sc1(wsr, G_Q + (bb * 2048 + blk * 32 + jq) * 8, pack_bf(sum8(&sm.r2[2 * jq * 20 + bb * 8]), sum8(&sm.r2[(2 * jq + 1) * 20 + bb * 8])), tag);
        }
      }
      SB();
    }
    return true;
  });
  PIN(sot);
  if (wave == 7) {
    const bool ok = tbg[1] == tag;
    float lg = ok ? (float)(tbg[0] & 0xffffu) : 0.f, nf = ok ? 8.f * (RPP - RPS + 1) : 0.f;
    lg += dpp<XOR1>(lg); nf += dpp<XOR1>(nf); lg += dpp<XOR2>(lg); nf += dpp<XOR2>(nf); lg += dpp<XOR7>(lg); nf += dpp<XOR7>(nf); lg += dpp<XOR8>(lg); nf += dpp<XOR8>(nf);
    lg *= __builtin_amdgcn_rcpf(fmaxf(nf, 1.f));
    if (lane == 0) sm.bc2 = u32x2{(u32)min(shift * P.DG * 32, P.EW * 256), (u32)min(max((int)((lg - (float)P.B0) * (float)(P.BG * 32) + fmaxf(lg - (float)P.TC, 0.f) * (float)(P.RT * 32)) - shift * P.D0 * 32, 0), 200 * 256)};
  }
  LDS_BARRIER();
  {
    const u32x2 bc = sm.bc2;
    const int st = __builtin_amdgcn_readfirstlane(bc[1]), sp = (st * P.BF) >> 3;
    t1 += st + __builtin_amdgcn_readfirstlane(bc[0]);
    due += P.TG * 256 + sp;
    tau = (te1 >> 16) + __builtin_amdgcn_readfirstlane((int)((float)(st - sp) * __builtin_amdgcn_rcpf((float)(nl - 3))));
  }
  const u32 qoff = G_Q + ((b * 32 + head) * 64 + c * 4) * 8;
  u32x4 qg[2], mlg;
  u32 qq[4];
  constexpr int NB = (NL + 15) / 16;
  float xlo, sc[NB], pr[NB], m = 0.f;
  f32x4 acc = {0.f, 0.f, 0.f, 0.f};
  u32 pb[NB], pm[4], pl8[8], cpf;
  const u32 adst0 = lds_addr(lane < 32 ? &sm.red[wave][c * 8 + g * 4] : &sm.r2[(lane - 32) * 4]);
  u32 po, pso, rdst, rsrc, xo;
  auto wo_addr = [&]() {
    po = dup(G_P + (xcd & 3) * 8192) + tid * 16 + near;
    pso = tid < 16 ? pslot + c * 128 + near : OOB;
    PIN(pso);
    rdst = (u32)(uintptr_t)&sm.red2[wave][(lane & 31) * 4 + (lane >> 5) * 2];
    rsrc = (u32)(uintptr_t)&sm.red2[0][((wave & 1) * 16 + c) * 4 + (g & 1) * 2 + (g >> 1)];
    xo = dup(G_XP + xcd * 2048 + (wave & 1) * 1024 + (blk * 64 + (lane & ~1)) * 2) + (lane & 1) * 8192;
    PIN(po); PIN(rdst); PIN(rsrc); PIN(xo);
    const int q = (lane >> 2) & 3, pp = lane & 3, r = (g & 1) * 4 + (q & 1);
#pragma unroll
    for (int t = 0; t < 4; ++t) { wfa[t] = wimg + wave * 8192 + ((g >> 1) * 16 + (q >> 1) * 8 + r) * 128 + (pp & 1) * 8 + ((t * 2 + (pp >> 1)) ^ r ^ (q & 2)) * 16; PIN(wfa[t]); }
  };
  const u32 nadd = wdef ? P.LW : (is_up ? P.LU : 0);
  u32 nto = tid < 2 ? dup(G_NT) + bid * 8 + tid * 8192 : OOB;
  PIN(nto);
  auto notices = [&]() -> int {
    u32 off = dup(G_NT) + near + lane * 16;
    PIN(off);
    until(due);
    int mx;
#pragma nounroll
    for (int spin = 0; spin < SPIN_MAX; ++spin) {
      CFENCE();
      const u32x4 ga_ = ld16<AUX_SC1>(wsr, off, 0), gb_ = ld16<AUX_SC1>(wsr, off, 1024);
      mx = max(max((int)(ga_[0] - t0), (int)(ga_[2] - t0)), max((int)(gb_[0] - t0), (int)(gb_[2] - t0)));
      if (all_ok((((ga_[1] ^ tag) | (ga_[3] ^ tag)) | ((gb_[1] ^ tag) | (gb_[3] ^ tag))) == 0u)) break;
      for (int k = 0; k < P.AG; ++k) __builtin_amdgcn_s_sleep(1);
    }
    if (all_ok(mx <= (t1 >> 8) - P.AC)) return t1;
    mx = max(mx, __builtin_amdgcn_mov_dpp(mx, XOR1, 0xf, 0xf, true)); mx = max(mx, __builtin_amdgcn_mov_dpp(mx, XOR2, 0xf, 0xf, true));
    mx = max(mx, __builtin_amdgcn_mov_dpp(mx, XOR7, 0xf, 0xf, true)); mx = max(mx, __builtin_amdgcn_mov_dpp(mx, XOR8, 0xf, 0xf, true));
    mx = max(max(__builtin_amdgcn_readlane(mx, 0), __builtin_amdgcn_readlane(mx, 16)), max(__builtin_amdgcn_readlane(mx, 32), __builtin_amdgcn_readlane(mx, 48)));
    return max(min(mx, 1 << 20) * 256, t1 + P.L0 * 256) + P.L1 * 256;
  };
  auto pv_row = [&](auto jc) {
    constexpr int i = jc.v;
    const u32 pv = __float_as_uint(bcast16(u2f(pb[i >> 4]), i & 15));
    acc = mfma16(u32x4{pv & pm[0], pv & pm[1], pv & pm[2], pv & pm[3]}, vr[i], acc);
    PIN(acc);
    return true;
  };
  sfor<0, NL - 1>([&](auto ic) {
    constexpr int v = ic.v;
    if constexpr (v >= SK0 && v < SK1) { if (left_out(v)) return true; }
    if constexpr (v > 0) {
      const u32 so = __builtin_amdgcn_readlane(sot, v);
      if constexpr (v >= SK1) { if (v == NL - P.NE) tau = (tau * P.FE) >> 4; }
      gate();
      if constexpr (v >= RVS && v <= RVP) sample();
      SB();
      if constexpr (v >= SK0 && v < SK1) ld16_into(vr[v], rv, lane * 16, so); else vr[v] = ld16<AUX_NT>(rv, lane * 16, so);
      SB();
      SB();
    }
    if constexpr (v == SK0 - 1) {
#pragma unroll
      for (int i = SK0; i < SK1; ++i) { vr[i] = u32x4{0u, 0u, 0u, 0u}; PIN(vr[i]); }
    }
    if constexpr (v == VQ - 4) { qg[0] = ld16<AUX_SC1>(wsr, qoff, 0); qg[1] = ld16<AUX_SC1>(wsr, qoff + 16, 0); }
    if constexpr (v == VSIDE) side_loads();
    if constexpr (v == CPF) {
      u64 pc; u32 dlt, len;
      asm volatile("s_getpc_b64 %0\n.Lcpf_here:\n\ts_mov_b32 %1, .Ltail_start-.Lcpf_here\n\ts_mov_b32 %2, .Lend_a-.Ltail_start\n\ts_max_i32 %2, %2, .Lend_b-.Ltail_start\n\ts_max_i32 %2, %2, .Lend_c-.Ltail_start\n\ts_max_i32 %2, %2, .Lend_d-.Ltail_start\n\ts_max_i32 %2, %2, .Lend_e-.Ltail_start" : "=s"(pc), "=s"(dlt), "=s"(len) :: "scc");
      pc += dlt;
      const rsrc_t rc = mk_rsrc((const void*)pc, (part == 3 && i8 == 1) ? len : 0u);
      cpf = __builtin_amdgcn_raw_buffer_load_b32(rc, tid * 64, 0, 0);
    }
    if constexpr (v == SK1 + 2) PIN(cpf);
    if constexpr (v == RVP) report(G_VT + 1024);
    if constexpr (v == RVL) tbg = ld8<AUX_SC1>(wsr, wave == 7 ? G_VT + (g ? 0 : 1024) + xcd * 128 + c * 8 : OOB, 0);
    if constexpr (v == SK1) mlg = ld16<AUX_SC1>(wsr, wave == 0 ? mlo : OOB, 0);
    SB();
    if constexpr (NL + v < 72) limit(IC<NL + v>{});
    if constexpr (v >= SK0 + WV) { WAIT_VM(v < SK1 ? WV : WV + 1); SB(); }
    if constexpr (v == VQ) {
      if (!all_ok((qg[0][1] == tag) & (qg[0][3] == tag) & (qg[1][1] == tag) & (qg[1][3] == tag))) gather16<2>(wsr, qoff, 16, tag, qg);
      qq[0] = qg[0][0]; qq[1] = qg[0][2]; qq[2] = qg[1][0]; qq[3] = qg[1][2];
    }
    if constexpr (v >= VQ && v < VS) {
      constexpr int sl = v - VQ, bt = sl >> 1;
      float xs[8];
#pragma unroll
      for (int i = 0; i < 8; ++i) {
        float d = 0.f;
        if (sl * 8 + i < NL) {
#pragma unroll
          for (int k = 0; k < 4; ++k) d = dot2(kr[sl * 8 + i][k], qq[k], d);
        }
        xs[i] = d;
      }
#pragma unroll
      for (int h = 0; h < 4; ++h) xs[h] = sel_add<XOR7>(xs[h], xs[h + 4], c & 4);
#pragma unroll
      for (int h = 0; h < 2; ++h) xs[h] = sel_add<XOR2>(xs[h], xs[h + 2], c & 2);
      xs[0] = sel_add<XOR1>(xs[0], xs[1], c & 1);
      if constexpr ((sl & 1) || sl == NSL - 1) {
        const float t = (sl & 1) ? sel_add<XOR8>(xlo, xs[0], c & 8) : xs[0] + dpp<XOR8>(xs[0]);
        const float s = rbf(rbf(t) * 0.08838834764831845f);
        sc[bt] = (bt * 16 + c < SK0 || (bt * 16 + c >= first && bt * 16 + c < NL)) ? s : -__builtin_inff();
        PIN(sc[bt]);
      } else {
        xlo = xs[0];
        PIN(xlo);
      }
    }
    if constexpr (v == VS) {
      float mloc = sc[0];
#pragma unroll
      for (int bt = 1; bt < NB; ++bt) mloc = fmaxf(mloc, sc[bt]);
      m = fmaxf(wave_max(mloc), -3.0e38f);
      float ls = 0.f;
#pragma unroll
      for (int bt = 0; bt < NB; ++bt) { pr[bt] = __expf(sc[bt] - m); ls += pr[bt]; }
      const float l = wave_sum(ls);
      if (lane == 0) { sm.ml[wave][0] = m; sm.ml[wave][1] = l; }
      if (c == ((NL - 1) & 15)) sm.pl[g][wave] = pr[NB - 1];
    }
    if constexpr (v >= SK1) pv_row(IC<v - SK1 + SK0 - WV>{});
    if constexpr (v < SK0 || v >= SK0 + WV) sfor<cons_end(v - 1), cons_end(v)>(pv_row);
    if constexpr (v == RVU) {
      if (wave == 7) {
        if (__builtin_expect(!all_ok(tbg[1] == tag || g == 0), 0)) {
          u32 vg[1];
          gather8<1>(wsr, G_VT + xcd * 128 + c * 8, 0, tag, vg);
          if (g) tbg = u32x2{vg[0], tag};
        }
        const bool ok = tbg[1] == tag;
        float lg = ok ? (float)(g ? (int)tbg[0] >> 16 : (int)(tbg[0] & 0xffffu)) : 0.f, nf = ok ? 8.f * (RVP - RVS + 1) : 0.f;
        lg += dpp<XOR1>(lg); nf += dpp<XOR1>(nf); lg += dpp<XOR2>(lg); nf += dpp<XOR2>(nf); lg += dpp<XOR7>(lg); nf += dpp<XOR7>(nf); lg += dpp<XOR8>(lg); nf += dpp<XOR8>(nf);
        lg *= __builtin_amdgcn_rcpf(fmaxf(nf, 1.f));
        if (lane == 16) sm.bc4 = __float_as_uint(lg);
        if (lane == 0) sm.bc3 = (u32)min(max((int)((lg - (float)P.F0) * (float)(P.FG * 32)), 0), 200 * 256);
      }
      LDS_BARRIER();
      const int st = __builtin_amdgcn_readfirstlane(sm.bc3), sp = (st * P.FF_) >> 3;
      near = ((__builtin_amdgcn_readfirstlane(sm.bc4) >> 31 ^ die ^ P.NR ^ 1) & 1) * 8192u;
      t1 += st;
      due += sp;
      tau += __builtin_amdgcn_readfirstlane((int)((float)(st - sp) * __builtin_amdgcn_rcpf((float)(nl - RVU - 3))));
    }
    if constexpr (v == SK1 + 1) {
      float p[8];
#pragma unroll
      for (int w = 0; w < 8; ++w) p[w] = sm.pl[g][w] * __expf(sm.ml[w][0] - m);
#pragma unroll
      for (int w = 0; w < 8; ++w) pl8[w] = pack_bf(p[w], p[w]);
#pragma unroll
      for (int w = 0; w < 8; ++w) PIN(pl8[w]);
    }
    if constexpr (v == MLS - 1) { side_pin(); if (is_up) up_prep(); PIN(ga); PIN(xin); PIN(cc1); }
    if constexpr (v == UPREP - 1) wo_addr();
    if constexpr (v == UPREP) {
#pragma unroll
      for (int k = 0; k < 4; ++k) asm volatile("" : "=v"(bw[k]));
      if (part == 0) wo_frags(0); else if (is_up) up_frags();
#pragma unroll
      for (int k = 0; k < 4; ++k) PIN(bw[k]);
    }
    if constexpr (v == UPREP + 1) {
      if (is_up) {
        const u32x4* a1 = reinterpret_cast<const u32x4*>(&sm.hb[1024 + (c >> 2 & 1) * 1024 + (c >> 3) * 512 + (g >> 1) * 256 + (c >> 1 & 1) * 32 + (g & 1) * 16 + (c & 1) * 8]);
        f32x4 d = {0.f, 0.f, 0.f, 0.f};
#pragma unroll
        for (int j = 0; j < 4; ++j) d = mfma64(a1[j * 8], bw[j], d, (c & 1) ? 0xeeeeu : 0x4444u);
        float t = par ? d[2] + d[3] : d[0] + d[1];
        t += dpp<XOR8>(t);
        const auto ab = __builtin_amdgcn_permlane16_swap(__float_as_uint(t), __float_as_uint(t), false, false);
        Af = u2f(ab[0]); Bf = u2f(ab[1]);
      }
      PIN(Af); PIN(Bf);
    }
    if constexpr (v == MLS) {
      LDS_BARRIER();
      float mm[8], ll[8];
#pragma unroll
      for (int w = 0; w < 8; ++w) { const f32x2 t = *reinterpret_cast<const f32x2*>(&sm.ml[w][0]); mm[w] = t[0]; ll[w] = t[1]; }
      float Mwg = mm[0], Lwg = 0.f;
#pragma unroll
      for (int w = 1; w < 8; ++w) Mwg = fmaxf(Mwg, mm[w]);
      if (wave == 0) {
#pragma unroll
        for (int w = 0; w < 8; ++w) Lwg = fmaf(__expf(mm[w] - Mwg), ll[w], Lwg);
        if (lane == 0) st16_sc1(wsr, G_ML + ((head * 2 + b) * 4 + part) * 16, __float_as_uint(Mwg), __float_as_uint(Lwg), tag);
      }
      const float fw = __expf(m - Mwg);
      m = Mwg;
#pragma unroll
      for (int bt = 0; bt < NB; ++bt) { pb[bt] = pack_bf(pr[bt] * fw, pr[bt] * fw); PIN(pb[bt]); }
#pragma unroll
      for (int k = 0; k < 4; ++k) { pm[k] = (c >> 1 & 3) == k ? ((c & 1) ? 0xffff0000u : 0x0000ffffu) : 0u; PIN(pm[k]); }
    }
    SB();
    return true;
  });
  asm volatile(".Ltail_start:");
  if (wave != 0) {
    pv_row(IC<SK0 - 1>{});
    WAIT_VM(WV); SB();
    sfor<NL - 1 - WV, NL - 1>(pv_row);
    *LDSP(f32x4, adst0) = acc;
    LDS_BARRIER();
  } else {
    u32x4 xr[8], am[8];
#pragma unroll
    for (int w = 0; w < 8; ++w) am[w] = u32x4{pl8[w] & pm[0], pl8[w] & pm[1], pl8[w] & pm[2], pl8[w] & pm[3]};
#pragma unroll
    for (int w = 0; w < 8; ++w) PIN(am[w]);
    {
      const u32 so = __builtin_amdgcn_readlane(sot, NL - 1);
      gate();
      SB();
#pragma unroll
      for (int w = 0; w < 8; ++w) xr[w] = ld16<AUX_NT>(rv, lane * 16, so ^ ((w & 1) ? col(P, 7) : 0u) ^ ((w & 2) ? col(P, 8) : 0u) ^ ((w & 4) ? col(P, 9) : 0u));
      SB();
    }
    u32 rsum = lds_addr(&sm.red[0][c * 8 + (g & 1) * 4]);
    pv_row(IC<SK0 - 1>{});
    WAIT_VM(WV + 8); SB();
    sfor<NL - 1 - WV, NL - 3>(pv_row);
    if (__builtin_expect(!all_ok((mlg[1] == tag) & (mlg[3] == tag)), 0)) {
#pragma nounroll
      for (int spin = 0; spin < SPIN_MAX; ++spin) {
        CFENCE();
        mlg = ld16<AUX_SC1>(wsr, mlo, 0);
        if (all_ok((mlg[1] == tag) & (mlg[3] == tag))) break;
      }
    }
    merge_factor(mlg);
    sfor<NL - 3, NL - 1>(pv_row);
    LDS_BARRIER();
    f32x4 t[8];
#pragma unroll
    for (int w = 1; w < 8; ++w) t[w] = LDSP(f32x4, rsum)[w * 32];
    acc = ((acc + t[1]) + (t[2] + t[3])) + ((t[4] + t[5]) + (t[6] + t[7]));
    PIN(acc);
    SB();
#pragma unroll
    for (int w = 0; w < 8; ++w) acc = mfma16(am[w], xr[w], acc);
  }
  WAIT_VM(0);
  acc *= fmrg;
  {
    const u32 w0 = min(pack_bf(acc[0], acc[1]), SENT - 1u), w1 = min(pack_bf(acc[2], acc[3]), SENT - 1u);
    const auto e0 = __builtin_amdgcn_permlane16_swap(w0, w0, false, false), e1 = __builtin_amdgcn_permlane16_swap(w1, w1, false, false);
    __builtin_amdgcn_raw_buffer_store_b128(u32x4{e0[0], e1[0], e0[1], e1[1]}, wsr, pso, 0, AUX_SC1);
  }
  st8_sc1(wsr, nto, (u32)wall_clock64() + nadd, tag);
  SB();

  PIN(tid);
  lane = tid & 63; g = lane >> 4; c = lane & 15;
  fd = (xcd * 8 + i8) * 8 + (lane & 3) * 2;
  hpos = ub * 512 + (wave & 3) * 128 + (c >> 1) * 16 + (g & 1) * 8 + (c & 1) * 4 + (g >> 1) * 2;
  if (part == 2) return;
  if (part == 0) {
    __builtin_amdgcn_s_barrier();
#pragma nounroll
    while (wleft > 0) { until(t1 - (P.PA + wleft * P.PD) * 256); piece(); }
    bool late = false;
    u32x4 og[1];
    f32x4 d[4];
    u32 six = (c & 8) ? 0xeeeeu : 0x4444u;
    PIN(six);
#pragma unroll
    for (int t = 0; t < 4; ++t) { d[t] = f32x4{0.f, 0.f, 0.f, 0.f}; PIN(d[t]); }
    if (wdef) {
      late = true;
      while ((int)(((u32)wall_clock64() - t0) << 8) < t1 - 10 * 256) if (__builtin_amdgcn_s_getreg(7 | (3 << 11)) == 0) { late = false; break; }
      if (__builtin_expect(late, 0)) { until(t1); CFENCE(); og[0] = ld16<AUX_SC1>(wsr, po, 0); SB(); WAIT_VM(1); } else { WAIT_VM(0); }
      CFENCE();
      wo_frags(1);
#pragma unroll
      for (int k = 0; k < 4; ++k) PIN(bw[k]);
      SB();
    }
    if (__builtin_expect(!late, 1)) { until(t1); CFENCE(); og[0] = ld16<AUX_SC1>(wsr, po, 0); }
    SB();
    auto wo_mm = [&]() {
#pragma unroll
      for (int t = 0; t < 4; ++t) d[t] = mfma64(og[0], bw[t], d[t], six);
    };
    auto p_bad = [&]() { return !all_ok(max(max(og[0][0], og[0][1]), max(og[0][2], og[0][3])) != SENT); };
    wo_mm();
    if (__builtin_expect(p_bad(), 0)) {
#pragma nounroll
      for (int spin = 0; spin < SPIN_MAX; ++spin) {
        __builtin_amdgcn_s_sleep(1);
        CFENCE();
        og[0] = ld16<AUX_SC1>(wsr, po, 0);
        if (!p_bad()) break;
      }
#pragma unroll
      for (int t = 0; t < 4; ++t) d[t] = f32x4{0.f, 0.f, 0.f, 0.f};
      wo_mm();
#pragma unroll
      for (int t = 0; t < 4; ++t) { d[t] += f32x4{0.f, 0.f, 0.f, 0.f}; PIN(d[t]); }
      SB();
      asm volatile(".Lend_d:");
    }
    {
      SB();
      f32x4 y; f32x2 h[4];
#pragma unroll
      for (int t = 0; t < 4; ++t) { h[t] = d[t].xy + d[t].zw; PIN(h[t]); }
#pragma unroll
      for (int t = 0; t < 4; ++t) { y[t] = h[t][0] + h[t][1]; PIN(y[t]); }
      *LDSP(f32x2, rdst) = f32x2{swap32_add(y[0], y[1]), swap32_add(y[2], y[3])};
    }
    LDS_BARRIER();
    if (wave < 2) {
      f32x2 v[4];
#pragma unroll
      for (int w = 0; w < 4; ++w) v[w] = f32x2{LDSP(float, rsrc)[w * 256], LDSP(float, rsrc)[w * 256 + 128]};
      const f32x2 h = (v[0] + v[1]) + (v[2] + v[3]);
      const float y = h[0] + h[1];
      __builtin_amdgcn_raw_buffer_store_b32(min(pack_bf(dpp<0xA0>(y), dpp<0xF5>(y)), SENT - 1u), wsr, xo, 0, AUX_SC1);
      asm volatile(".Lend_a:");
    }
    return;
  }
  if (part == 3) {
    if (!has_w) return;
    u32x8 b2[2];
    {
      __builtin_amdgcn_s_barrier();
      const rsrc_t r2 = mk_rsrc(w2 + (size_t)(blk * 32 + wave * 4) * 512, 4096u);
      const u32 l2 = (u32)(uintptr_t)(&sm.w2b[wave * 4][0]);
#pragma unroll
      for (int n = 0; n < 4; ++n) { until(t1 - (P.QA + (4 - n) * P.QD) * 256); dma16(r2, (lane ^ (n * 2) ^ ((wave & 2) << 2)) * 16, n * 1024, l2 + n * 1024); }
    }
    const u32 tdst = lds_addr(&sm.hb[hpos]);
    const u32 sdst = lds_addr((lane & 7) == 0 ? &sm.stat[0][(wave >> 2) * 32 + (lane >> 5) * 16 + (wave & 3) * 4 + (lane >> 3 & 3)] : &sm.stat[1][lane]);
    const u32 ssrc = lds_addr(&sm.stat[0][lane]);
    const u32 adst = lds_addr((c < 4 && (g & 1) == 0) ? &sm.actl[g >> 1][(wave >> 2) * 16 + (c >> 1) * 8 + (wave & 3) * 2 + (c & 1)] : reinterpret_cast<u16*>(&sm.dummy[lane]));
    const u16* act8 = &sm.actl[c >> 1 & 1][(g & 1) * 16 + (c & 1) * 8];
    const u32 asrc0 = lds_addr((c >> 2) == (g >> 1) ? act8 : reinterpret_cast<const u16*>(sm.zero)), asrc1 = lds_addr((c >> 2) == 2 + (g >> 1) ? act8 : reinterpret_cast<const u16*>(sm.zero));
    f32x4 dn = {0.f, 0.f, 0.f, 0.f};
    PIN(dn);
    const u32 apa = lds_addr(&sm.hb[(c >> 3) * 512 + (g >> 1) * 256 + (c >> 1 & 1) * 64 + ((c >> 2) & 1) * 32 + (g & 1) * 16 + (c & 1) * 8]);
    u32 six = (c & 1) ? 0xeeeeu : 0x4444u;
    f32x4 d[2] = {{0.f, 0.f, 0.f, 0.f}, {0.f, 0.f, 0.f, 0.f}};
    PIN(six); PIN(d[0]); PIN(d[1]);
    constexpr u32 xs2 = 16384;
    u32 xoff = dup(G_XP) + near + g * 2048 + ub * 1024 + (wave & 3) * 256 + c * 16;
    u32x4 one;
#pragma unroll
    for (int k = 0; k < 4; ++k) one[k] = ((c & 2) == 0 && (c >> 3) == (k & 1)) ? ((c & 1) ? 0x3f800000u : 0x00003f80u) : 0u;
    u32 oix = (c & 4) ? 0xeeeeu : 0x4444u;
    f32x4 v = {0.f, 0.f, 0.f, 0.f};
    PIN(one); PIN(oix); PIN(v);
    const int cw = wave ^ ((die ^ 1) << 2);
    u32 ooff = dup(G_OP + (blk * 512 + cw * 64 + lane) * 8) + (near ^ ((cw >> 2 ^ die) << 13));
    float Afp = Af, Bfp = Bf;
    PIN(xoff); PIN(ooff); PIN(Afp); PIN(Bfp);
    f32x2 gc = ga * cc1, xc = xin * cc1;
    PIN(ga); PIN(xin); PIN(gc); PIN(xc);
    u32 w2a = (u32)(uintptr_t)(&sm.w2b[0][0]) + (g * 8 + ((lane >> 2) & 3)) * 1024 + (cw ^ (g & 1)) * 128 + (lane & 15) * 8;
    PIN(w2a);
    if (wave == 1) {
      due = t1 + P.N1 * 256;
      const int tn = notices();
      due = tn + P.X2 * 256;
      until(due);
    }
    asm volatile("s_waitcnt vmcnt(0)" ::: "memory");
    __builtin_amdgcn_s_barrier();
    CFENCE();
    u32x4 xg[2] = {ld16<AUX_SC1>(wsr, xoff, 0), ld16<AUX_SC1>(wsr, xoff, xs2)};
    SB();
#pragma unroll
    for (int t = 0; t < 4; ++t) {
      const u32x2 lo = tr_read(w2a ^ (t * 32)), hi = tr_read((w2a ^ (t * 32)) + 4096);
      b2[t >> 1][(t & 1) * 4] = lo[0]; b2[t >> 1][(t & 1) * 4 + 1] = lo[1]; b2[t >> 1][(t & 1) * 4 + 2] = hi[0]; b2[t >> 1][(t & 1) * 4 + 3] = hi[1];
    }
    PIN(b2[0]); PIN(b2[1]);
    SB();
    const f32x4 z4 = {0.f, 0.f, 0.f, 0.f};
    auto xp_bad = [&]() { return !all_ok(max(max(max(xg[0][0], xg[0][1]), max(xg[0][2], xg[0][3])), max(max(xg[1][0], xg[1][1]), max(xg[1][2], xg[1][3]))) != SENT); };
    auto both = [&]() { return u32x8{xg[0][0], xg[0][1], xg[0][2], xg[0][3], xg[1][0], xg[1][1], xg[1][2], xg[1][3]}; };
    v = mfma64(one, both(), v, oix);
    if (__builtin_expect(xp_bad(), 0)) {
#pragma nounroll
      for (int spin = 0; spin < SPIN_MAX; ++spin) {
        __builtin_amdgcn_s_sleep(1);
        CFENCE();
        xg[0] = ld16<AUX_SC1>(wsr, xoff, 0); xg[1] = ld16<AUX_SC1>(wsr, xoff, xs2);
        if (!xp_bad()) break;
      }
      v = mfma64(one, both(), z4, oix);
      v += z4;
      PIN(v);
      SB();
      asm volatile(".Lend_e:");
    }
    {
      PIN(v);
      const f32x2 a = v.xy, hc = __builtin_elementwise_fma(gc, a, xc), x1 = __builtin_elementwise_fma(ga, a, xin);
      *LDSP(u32, tdst) = pack_bf(hc[0], hc[1]);
      float u = swap32_add(x1[0] + x1[1], fmaf(x1[0], x1[0], x1[1] * x1[1]));
      u += dpp<XOR1>(u); u += dpp<XOR2>(u); u += dpp<XOR7>(u);
      *LDSP(float, sdst) = u;
    }
    LDS_BARRIER();
    {
      float s = *LDSP(float, ssrc);
      SB();
      const u32x4 af0 = LDSP(u32x4, apa)[0], af1 = LDSP(u32x4, apa)[16];
      SB();
      s += dpp<XOR1>(s);
      SB();
      d[0] = mfma64(af0, bw[0], d[0], six);
      SB();
      s += dpp<XOR2>(s);
      SB();
      d[1] = mfma64(af0, bw[1], d[1], six);
      SB();
      s += dpp<XOR7>(s);
      SB();
      d[0] = mfma64(af1, bw[2], d[0], six);
      SB();
      s += dpp<XOR8>(s);
      SB();
      d[1] = mfma64(af1, bw[3], d[1], six);
      SB();
      const auto sq = __builtin_amdgcn_permlane16_swap(__float_as_uint(s), __float_as_uint(s), false, false);
      const float mean = u2f(sq[0]) * (1.f / 512.f);
      const float var = fmaf(u2f(sq[1]), 1.f / 512.f, -mean * mean);
      float ri = __builtin_amdgcn_rsqf(fmaxf(var, 0.f) + 1e-5f);
      PIN(ri); PIN(d[0]); PIN(d[1]);
      SB();
      float y0 = d[0][0] + d[0][1], y1 = d[1][2] + d[1][3], mr = -ri * mean;
      PIN(y0); PIN(y1); PIN(mr);
      float y = y0 + y1, cf = fmaf(mr, Afp, Bfp), rk = ri * -1.44269504f;
      PIN(y); PIN(cf); PIN(rk);
      y = __builtin_bit_cast(float, __builtin_amdgcn_update_dpp(__builtin_bit_cast(int, y), __builtin_bit_cast(int, y), XOR8, 0xa, 0xf, false));
      const float ck = cf * -1.44269504f;
      y = swap16_add(y, y);
      const float ex = __builtin_amdgcn_exp2f(fmaf(y, rk, ck));
      y = fmaf(y, ri, cf);
      const float gu = y * dpp<0x104>(y);
      *LDSP(u16, adst) = (u16)pack_bf(gu * __builtin_amdgcn_rcpf(1.f + ex), 0.f);
    }
    LDS_BARRIER();
    {
      const u32x4 a0 = *LDSP(u32x4, asrc0), a1 = *LDSP(u32x4, asrc1);
      SB();
      dn = mfma64(a1, b2[1], mfma64(a0, b2[0], dn, six), six);
      float e0 = dn[0] + dn[1], e1 = dn[2] + dn[3];
      PIN(e0); PIN(e1);
      const u32x2 ov = {pack_bf(e0, e1), tag};
      __builtin_amdgcn_raw_buffer_store_b64(ov, wsr, ooff, 0, AUX_SC1);
      SB();
      if (blk == 0 && tid == 0) __builtin_amdgcn_raw_buffer_store_b32(tag, wsr, WS_EPOCH, 0, AUX_SC1);
    }
    asm volatile(".Lend_b:");
    return;
  }
  if (wave == 0) {
    if (!all_ok((sg[0][1] == tag) & (sg[0][3] == tag) & (sg[1][1] == tag) & (sg[1][3] == tag))) {
      u32x4 t[2];
      gather16<2>(wsr, G_MOD + (1024 + fd) * 8, 1536 * 8, tag, t);
      sg[0] = t[0]; sg[1] = t[1];
    }
    const rsrc_t rout = mk_rsrc(out, 2048u);
    u32 oo = (lane & 44) == 0 ? (g * 512 + fd) * 2 : OOB;
    until(t1 + P.PV * 256);
    __builtin_amdgcn_raw_buffer_store_b32(sx2, rout, oo, 0, AUX_SC1);
    u32 xa;
    {
      due = t1 + P.N2 * 256;
      const int tn = notices();
      due = tn + P.X3 * 256;
      until(due);
    }
    const bool hb = g & 1;
#pragma nounroll
    for (int spin = 0; spin < SPIN_MAX; ++spin) {
      CFENCE();
      xa = __builtin_amdgcn_raw_buffer_load_b32(wsr, dup(G_XP) + near + (g >> 1) * 16384 + (c >> 2) * 2048 + (g & 1) * 1024 + fd * 2, 0, AUX_SC1);
      if (all_ok(xa != SENT)) break;
      __builtin_amdgcn_s_sleep(1);
    }
    float a0 = lo_f(xa), a1 = hi_f(xa);
    a0 += dpp<0x124>(a0); a1 += dpp<0x124>(a1);
    a0 += dpp<XOR8>(a0); a1 += dpp<XOR8>(a1);
    a0 = swap32_add(a0, a0); a1 = swap32_add(a1, a1);
    f32x2 xv = {rbf(lo_f(sx2) + rbf((hb ? hi_f(sg[0][0]) : lo_f(sg[0][0])) * rbf(a0))), rbf(hi_f(sx2) + rbf((hb ? hi_f(sg[0][2]) : lo_f(sg[0][2])) * rbf(a1)))};
    f32x2 gv = {hb ? hi_f(sg[1][0]) : lo_f(sg[1][0]), hb ? hi_f(sg[1][2]) : lo_f(sg[1][2])};
    PIN(xv); PIN(gv);
    due = max(due + P.X4 * 256, (int)(((u32)wall_clock64() - t0) << 8) + P.GX * 256);
    const bool last = lane < (NUP - 32) * 4;
    u32 o0 = dup(G_OP + (lane >> 2) * 4096 + fd * 8) + near, o2 = last ? o0 : OOB, tg2 = last ? tag : 0u;
    const u32 pk = (c & 10) ? 0u : ((c & 4) ? 0x3f800000u : 0x00003f80u);
    u32x4 one = {(c & 1) ? 0u : pk, 0u, (c & 1) ? pk : 0u, 0u};
    PIN(o0); PIN(o2); PIN(tg2); PIN(oo); PIN(one);
    u32 s1_ = 131072, s2_ = 262144;
    asm volatile("" : "+s"(s1_), "+s"(s2_));
    until(due);
    f32x4 sv;
#pragma nounroll
    for (int spin = 0; spin < SPIN_MAX; ++spin) {
      CFENCE();
      const u32x4 og[3] = {ld16<AUX_SC1>(wsr, o0, 0), ld16<AUX_SC1>(wsr, o0, s1_), ld16<AUX_SC1>(wsr, o2, s2_)};
      sv = f32x4{0.f, 0.f, 0.f, 0.f};
#pragma unroll
      for (int k = 0; k < 3; ++k) sv = mfma16(one, og[k], sv);
      SB();
      u32 bad = ((og[0][1] ^ tag) | (og[0][3] ^ tag) | (og[1][1] ^ tag)) | ((og[1][3] ^ tag) | (og[2][1] ^ tg2) | (og[2][3] ^ tg2));
      PIN(bad);
      SB();
      float s0 = sv[0] + dpp<0x124>(sv[0]), s1 = sv[1] + dpp<0x124>(sv[1]);
      PIN(s0); PIN(s1);
      s0 += dpp<XOR8>(s0); s1 += dpp<XOR8>(s1);
      PIN(s0); PIN(s1);
      __builtin_amdgcn_raw_buffer_store_b32(pack_bf(fmaf(gv[0], s0, xv[0]), fmaf(gv[1], s1, xv[1])), rout, oo, 0, AUX_SC1);
      SB();
      if (all_ok(bad == 0u)) break;
      __builtin_amdgcn_s_sleep(1);
    }
    asm volatile(".Lend_c:");
  }
}

extern "C" void kernel_launch(void* const* d_in, const int* in_sizes, int n_in,
                              void* d_out, int out_size, void* d_ws, size_t ws_size,
                              hipStream_t stream) {
  fused_kernel<<<256, 512, 0, stream>>>((const u16*)d_in[1], (const u16*)d_in[2], (const u16*)d_in[4], (const u16*)d_in[3], (const u16*)d_in[10], (const u16*)d_in[6], (const u16*)d_in[7],
                                        (unsigned char*)d_ws, (const u16*)d_in[0], (const u16*)d_in[9], (const u16*)d_in[8], (u16*)d_out);
}
```

```cpp
#define PROBE_MUL 10
#include <hip/hip_runtime.h>
#include <stdint.h>

typedef unsigned int u32;
typedef unsigned short u16;
typedef unsigned long long u64;
typedef __attribute__((ext_vector_type(4))) unsigned u32x4;
typedef __attribute__((ext_vector_type(2))) unsigned u32x2;
typedef __attribute__((ext_vector_type(2))) __bf16 bf16x2;
typedef __attribute__((ext_vector_type(4))) float f32x4;
typedef __attribute__((ext_vector_type(2))) float f32x2;
typedef __amdgpu_buffer_rsrc_t rsrc_t;

constexpr int NH = 32, SK = 16384, DH = 128, WIN = 4096, FF = 1376;
struct Prm { int E0, ES, E1, E2, E2C, EU, TEND, DT_UP, L0, L1, X1, X2, X3, X4, NE, FE, M0, M1, M2, M3, M4, GS, GX, N1, N2, GA, WD, DT_WO, LW, LU, EW, PM, NF, NR, C0, A0, AG, AC, TS, TW, TK, TG, GB, B0, BG, BF, D0, DG, TC, LM, C1, F0, FG, FF_, PA, PD, QA, QD, RT, SA, SG, SC, W[9]; };
constexpr Prm PRM0 = {32, 20, 32, 32, 60, 60, 2000, 76, 12, 27, 95, 60, 96, 67, 3, 8, 0x2511f55b, 0x2b7425d5, 0x6017665, 0x17e0b9ec, 0x31944305, 62, 28, 26, 58, 50, 4, 106, 50, 28, 10, 0x0, 12, 14, 3, 36, 4, 4, 509, 215, 80, -2, 6, 69, 10, 8, 8, 6, 87, 120, 2, 58, 9, 4, 52, 14, 9, 18, 12, 45, 10, 21, 0x77777777, 0x77777777, 0x77777777, 0x77777777, 0x77777777, 0x44444477, 0x44444444, 0x44444444, 0x44444444};
constexpr int NL = 37;
constexpr int SK0 = 21, SK1 = 32;
constexpr int NCMIN = (NL - SK1 + SK0) * 8;
constexpr int WK = 8, WV = 5;
constexpr int WUSE = 12, LNS = 7;
constexpr int QP = 14, QD = 4;
constexpr int VQ = 6;
constexpr int NSL = (NL + 7) / 8;
constexpr int VS = VQ + NSL;
constexpr int MLS = VS + 2;
constexpr int VSIDE = MLS - 5, UPREP = MLS + 6;
constexpr int CPF = 17;
constexpr int SMO = 1;
constexpr int RVS = 5, RVP = 8, RVL = 14, RVU = 18;
constexpr int RPS = 23, RPP = 27;
constexpr int PRB = 12;
constexpr int TBS = 7, TBP = 10, TBL = 13, TBU = 17;
constexpr int window(int s, int w) {
  int n = w;
  for (int u = s - w + 1; u <= s; ++u) n += u == SMO ? 6 : (u == TBL || u == SK1 || u == NL + CPF || u == NL + RVL || u == NL + SK1 ? 1 : (u == QP || u == NL + VQ - 4 ? 2 : (u == NL + VSIDE ? 4 : 0)));
  return n;
}
constexpr int cons_end(int v) {
  const int a = v - WV + 1, c = (v - MLS) * 4;
  const int m = a < c ? a : c;
  return m < 0 ? 0 : (m > NL ? NL : m);
}
constexpr u32 col(const Prm& P, int j) { const int m[6] = {P.M0, P.M1, P.M2, P.M3, P.M4, P.PM}; return (u32)(m[j / 3] >> (j % 3 * 10) & 1023) << 10; }
constexpr u32 first_col(const Prm& P, int k) {
  if (k < 4) return col(P, 12 + k) | 1u << (24 + k);
  if (k < 6) return col(P, k - 4);
  if (k < 8) return col(P, 4 + k) | 1u << (16 + k);
  if (k < 11) return col(P, k - 1);
  return (col(P, 2) ^ col(P, 3) ^ col(P, 4) ^ col(P, 5) ^ col(P, 6) ^ col(P, 10) ^ col(P, 11)) | 0xf00000u;
}
constexpr bool one_to_one(const Prm& P) {
  u32 c[10] = {};
  for (int j = 0; j < 10; ++j) c[j] = col(P, j);
  for (int j = 0; j < 10; ++j) {
    if (c[j] == 0) return false;
    const u32 low = c[j] & (0u - c[j]);
    for (int k = j + 1; k < 10; ++k) if (c[k] & low) c[k] ^= c[j];
  }
  return true;
}
static_assert(one_to_one(PRM0), "every KB of the window exactly once");
struct Tab { u32 e[512]; };
constexpr Tab make_tab(const Prm& P) {
  Tab T{};
  for (int bid = 0; bid < 256; ++bid) {
    const int xcd = bid & 7, part = bid >> 4 & 3, i8 = (bid >> 3 & 1) | bid >> 6 << 1, wi = i8 * 8 + xcd;
    const bool has_mod = wi < 48, has_up = wi >= 21;
    const int cb0 = (i8 + P.C0) & 7, cb1 = (i8 + P.C1) & 7, ms = wi - 20;
    const int mi = wi < 8 ? wi : (wi < 16 ? 8 + ((xcd + 4) & 7) : (wi < 20 ? (wi - 14) * 8 + 3 : 16 + ms / 7 * 8 + ms % 7 + (ms % 7 >= 3 ? 1 : 0)));
    const int ui = xcd * 5 + (xcd > 5 ? xcd - 5 : 0) + (i8 == 6 ? 0 : (xcd >= 5 ? 1 : 0) + (i8 == 7 ? 4 : i8 - 2));
    const bool edge = i8 >= 6;
    const int wd = cb0 == 3 ? 0 : P.WD;
    const int e[4] = {P.E0 - 4 * wd + (cb0 == 3 ? P.ES : 0), P.E1 + (cb1 == 3 ? P.ES : 0), has_mod ? (wi < 16 ? P.E2C : P.E2) + ((mi & 7) == 3 ? P.ES : 0) : 0, has_up ? P.EU : 0};
    int tw[4] = {}, win[4] = {}, span[4] = {}, k0[4] = {};
    for (int k = 0; k < 4; ++k) {
      const int np = k == 0 ? 8 - wd : 8;
      tw[k] = np ? e[k] * P.TW / (16 * np) : 0;
      k0[k] = P.NF + tw[k] * np / 2 > P.TK ? P.NF + tw[k] * np / 2 : P.TK;
      span[k] = P.TEND - k0[k] - P.TG - (k == 0 ? (wd ? P.DT_WO : P.A0) : (k == 3 && has_up ? P.DT_UP : 0));
      win[k] = span[k] - (k == 3 && has_up ? P.GA + (edge ? P.LM : 0) : 0);
    }
    int n[4] = {NCMIN / 8, NCMIN / 8, NCMIN / 8, NCMIN / 8};
    for (int left = 128 - NCMIN / 2; left > 0; --left) {
      int best = -1;
      for (int k = 3; k >= 0; --k)
        if (n[k] < NL && (best < 0 || n[k] * win[best] < n[best] * win[k])) best = k;
      ++n[best];
    }
    const int c[4] = {0, n[0] - 1, n[0] + n[1] - 2, n[0] + n[1] + n[2] - 3};
    const int nl = n[part];
    const bool has_w = part < 2 || (part == 2 ? has_mod : has_up);
    const int blk = !has_w ? 0 : (part == 0 ? cb0 : (part == 1 ? xcd * 8 + cb1 : (part == 2 ? mi : ui)));
    T.e[2 * bid] = (u32)(c[part] | nl << 7 | blk << 13 | (has_w ? 1 : 0) << 19 | (part == 0 ? wd : 0) << 20) | (u32)tw[part] << 24;
    T.e[2 * bid + 1] = (u32)(span[part] * P.TS / (4 * (nl - 1)) | span[part] * (1024 - P.TS) / (4 * (nl - 1)) << 16);
  }
  return T;
}
constexpr int step_range(const Prm& P, bool mx_) {
  int mx = 0, mn = 1 << 20;
  const Tab T = make_tab(P);
  for (int bid = 0; bid < 256; ++bid) { const int n = T.e[2 * bid] >> 7 & 63; mx = n > mx ? n : mx; mn = n < mn ? n : mn; }
  return mx_ ? mx : mn;
}
extern "C" __attribute__((used, visibility("protected"))) __constant__ const Tab wg_tab = make_tab(PRM0);
static_assert(step_range(PRM0, true) <= NL && step_range(PRM0, false) >= NCMIN / 8, "a wave's key range does not fit its load steps");
static_assert(RVU < UPREP && UPREP + 1 < SK0 && QP + QD + 1 < SK0 && cons_end(SK0 - 1) == SK0 - WV && NL - SK1 == WV, "steps every wave runs");
constexpr bool timers_long(const Prm& P) {
  const Tab T = make_tab(P);
  for (int bid = 48; bid < 64; ++bid) if (SK0 + NL - (int)(T.e[2 * bid] >> 7 & 63) > RPS) return false;
  return true;
}
static_assert(timers_long(PRM0), "steps of the second report");
static_assert(PRM0.WD <= 4 && PRM0.NE <= NL - SK1, "late pieces: the second half of the block at most");
constexpr int NUP = FF / 32;
constexpr int SPIN_MAX = 1 << 17;

constexpr u32 WS_EPOCH = 0;
constexpr u32 G_TB = 1024;
constexpr u32 G_MOD = 4096;
constexpr u32 G_Q = G_MOD + 3072 * 8;
constexpr u32 G_ML = G_Q + 4096 * 8;
constexpr u32 G_U = G_ML + 256 * 16;
constexpr u32 G_P = 0;
constexpr u32 G_XP = G_P + 128 * 256;
constexpr u32 G_OP = G_XP + 8 * 512 * 4;
constexpr u32 G_NT = G_OP + 43 * 512 * 8;
constexpr u32 G_PB = G_OP + 44 * 512 * 8;
constexpr u32 G_VT = G_U + 2 * (G_PB + 8192);
constexpr u32 WS_END = G_VT + 2048;
__device__ __forceinline__ u32 dup(u32 o) { return G_U + ((o >> 13) << 14) + (o & 8191u); }
constexpr u32 SENT = 0xffffffffu;
constexpr u32 OOB = 0x40000000u;
#define WST 0

constexpr int AUX_NT = 2, AUX_SC1 = 16;

__device__ __forceinline__ float lo_f(u32 v) { return __uint_as_float(v << 16); }
__device__ __forceinline__ float hi_f(u32 v) { return __uint_as_float(v & 0xffff0000u); }
__device__ __forceinline__ float rbf(float x) { return (float)(__bf16)x; }
__device__ __forceinline__ u32 pack_bf(float lo, float hi) {
  bf16x2 r; r[0] = (__bf16)lo; r[1] = (__bf16)hi; return __builtin_bit_cast(u32, r);
}
__device__ __forceinline__ float dot2(u32 k, u32 q, float acc) {
  return __builtin_amdgcn_fdot2_f32_bf16(__builtin_bit_cast(bf16x2, k), __builtin_bit_cast(bf16x2, q), acc, false);
}
template <int CTRL> __device__ __forceinline__ float dpp(float x) {
  return __builtin_bit_cast(float, __builtin_amdgcn_mov_dpp(__builtin_bit_cast(int, x), CTRL, 0xf, 0xf, true));
}
constexpr int XOR1 = 0xB1, XOR2 = 0x4E, XOR7 = 0x141, XOR8 = 0x128;
__device__ __forceinline__ float swap32_add(float a, float b) {
  auto t = __builtin_amdgcn_permlane32_swap(__float_as_uint(a), __float_as_uint(b), false, false);
  return __uint_as_float(t[0]) + __uint_as_float(t[1]);
}
__device__ __forceinline__ float swap16_add(float a, float b) {
  auto t = __builtin_amdgcn_permlane16_swap(__float_as_uint(a), __float_as_uint(b), false, false);
  return __uint_as_float(t[0]) + __uint_as_float(t[1]);
}
__device__ __forceinline__ float wave_sum(float x) {
  x += dpp<XOR1>(x); x += dpp<XOR2>(x); x += dpp<XOR7>(x); x += dpp<XOR8>(x);
  x = swap16_add(x, x);
  return swap32_add(x, x);
}
__device__ __forceinline__ float wave_max(float x) {
  x = fmaxf(x, dpp<XOR1>(x)); x = fmaxf(x, dpp<XOR2>(x)); x = fmaxf(x, dpp<XOR7>(x)); x = fmaxf(x, dpp<XOR8>(x));
  auto s = __builtin_amdgcn_permlane16_swap(__float_as_uint(x), __float_as_uint(x), false, false);
  x = fmaxf(__uint_as_float(s[0]), __uint_as_float(s[1]));
  auto t = __builtin_amdgcn_permlane32_swap(__float_as_uint(x), __float_as_uint(x), false, false);
  return fmaxf(__uint_as_float(t[0]), __uint_as_float(t[1]));
}
template <int CTRL> __device__ __forceinline__ float sel_add(float lo, float hi, bool up) {
  const float t1 = lo + dpp<CTRL>(lo), t2 = hi + dpp<CTRL>(hi);
  return up ? t2 : t1;
}
template <int I> __device__ __forceinline__ float bc16(float x) {
  return __builtin_bit_cast(float, __builtin_amdgcn_update_dpp(0, __builtin_bit_cast(int, x), 0x150 + I, 0xf, 0xf, false));
}
__device__ __forceinline__ float bcast16(float x, int i) {
  switch (i) {
    case 0: return bc16<0>(x); case 1: return bc16<1>(x); case 2: return bc16<2>(x); case 3: return bc16<3>(x);
    case 4: return bc16<4>(x); case 5: return bc16<5>(x); case 6: return bc16<6>(x); case 7: return bc16<7>(x);
    case 8: return bc16<8>(x); case 9: return bc16<9>(x); case 10: return bc16<10>(x); case 11: return bc16<11>(x);
    case 12: return bc16<12>(x); case 13: return bc16<13>(x); case 14: return bc16<14>(x); default: return bc16<15>(x);
  }
}
__device__ __forceinline__ float silu_f(float x) { return x * __builtin_amdgcn_rcpf(1.f + __expf(-x)); }

template <int AUX> __device__ __forceinline__ u32x4 ld16(rsrc_t r, u32 voff, u32 soff) {
  return __builtin_amdgcn_raw_buffer_load_b128(r, voff, soff, AUX);
}
template <int AUX> __device__ __forceinline__ u32x2 ld8(rsrc_t r, u32 voff, u32 soff) {
  return __builtin_amdgcn_raw_buffer_load_b64(r, voff, soff, AUX);
}
__device__ __forceinline__ void st8_sc1(rsrc_t r, u32 voff, u32 v, u32 tag) {
  __builtin_amdgcn_raw_buffer_store_b64(u32x2{v, tag}, r, voff, 0, AUX_SC1);
}
__device__ __forceinline__ void st16_sc1(rsrc_t r, u32 voff, u32 v0, u32 v1, u32 tag) {
  __builtin_amdgcn_raw_buffer_store_b128(u32x4{v0, tag, v1, tag}, r, voff, 0, AUX_SC1);
}
__device__ __forceinline__ rsrc_t mk_rsrc(const void* p, u32 bytes) {
  return __builtin_amdgcn_make_buffer_rsrc(const_cast<void*>(p), 0, bytes, 0x00020000);
}
#define WAIT_VM(n) __builtin_amdgcn_s_waitcnt(((n) & 15) | (((n) >> 4) << 14) | (7 << 4) | (15 << 8))
#define SB() __builtin_amdgcn_sched_barrier(0)
#define CFENCE() asm volatile("" ::: "memory")
__device__ __forceinline__ bool all_ok(bool ok) { return __builtin_amdgcn_ballot_w64(!ok) == 0ull; }

template <int N> __device__ __forceinline__ void gather16(rsrc_t ws, u32 voff, u32 stride, u32 tag, u32x4 (&g)[N]) {
#pragma nounroll
  for (int spin = 0; spin < SPIN_MAX; ++spin) {
    CFENCE();
    bool ok = true;
#pragma unroll
    for (int i = 0; i < N; ++i) g[i] = ld16<AUX_SC1>(ws, voff + i * stride, 0);
#pragma unroll
    for (int i = 0; i < N; ++i) ok &= (g[i][1] == tag) & (g[i][3] == tag);
    if (all_ok(ok)) break;
    __builtin_amdgcn_s_sleep(8);
  }
}
template <int N> __device__ __forceinline__ void gather8(rsrc_t ws, u32 voff, u32 stride, u32 tag, u32 (&g)[N], bool act = true) {
#pragma nounroll
  for (int spin = 0; spin < SPIN_MAX; ++spin) {
    CFENCE();
    bool ok = true;
    u32x2 t[N];
#pragma unroll
    for (int i = 0; i < N; ++i) t[i] = ld8<AUX_SC1>(ws, voff + i * stride, 0);
#pragma unroll
    for (int i = 0; i < N; ++i) { ok &= (t[i][1] == tag); g[i] = t[i][0]; }
    if (all_ok(ok | !act)) break;
    __builtin_amdgcn_s_sleep(8);
  }
}

struct Smem {
  float red[8][128];
  float ml[8][2];
  float r2[64 * 20];
  u16 hb[3 * 1024];
  float stat[8][64];
  float red2[8][128];
  u32 dummy[64];
  u32x2 bc, bc2;
  u32 bc3, bc4;
  u32x4 zero[4];
  float pl[4][8];
  u32 mflag[8];
  u32x4 wbuf[8][8][64];
  __attribute__((aligned(256))) u16 w2b[32][512];
  u16 actl[2][32];
};

#define PROBE_K 8
#define PROBE_OP 0
#define PROBE_PART -1
#define STAMP(k) do { if ((k) == PROBE_K && tid == 0) pst = wall_clock64(); } while (0)
#define WSTAMP(k) do {} while (0)
#define FINISH() do { STAMP(8); if (tid == 0) { __hip_atomic_store(reinterpret_cast<u64*>(ws + (1u << 20)) + bid * 2, pt0 + (u32)(t0 - (u32)pt0) + (t1 >> 8), __ATOMIC_RELAXED, __HIP_MEMORY_SCOPE_AGENT); \
   __hip_atomic_store(reinterpret_cast<u64*>(ws + (1u << 20)) + bid * 2 + 1, (PROBE_PART < 0 || part == PROBE_PART) ? pst : 0ull, __ATOMIC_RELAXED, __HIP_MEMORY_SCOPE_AGENT); } \
   if (bid == 8 && wave == 0) probe_end(ws, pt0, lane); return; } while (0)
#define PIN(x) asm volatile("" : "+v"(x))
#define LDS_BARRIER() do { __builtin_amdgcn_s_waitcnt(0xc07f); __builtin_amdgcn_s_barrier(); CFENCE(); } while (0)
#define WAVE_SYNC() do { __builtin_amdgcn_fence(__ATOMIC_RELEASE, "wavefront"); __builtin_amdgcn_wave_barrier(); __builtin_amdgcn_fence(__ATOMIC_ACQUIRE, "wavefront"); } while (0)

__device__ __forceinline__ void dma16(rsrc_t r, u32 voff, u32 soff, u32 lds_dst) {
  u32 keep;
  asm volatile("s_mov_b32 %0, m0\n\ts_mov_b32 m0, %4\n\ts_nop 4\n\tbuffer_load_dwordx4 %1, %2, %3 offen nt lds\n\ts_mov_b32 m0, %0"
               : "=&s"(keep) : "v"(voff), "s"(r), "s"(soff), "s"(lds_dst) : "memory");
}
__device__ __forceinline__ void ld16_into(u32x4& d, rsrc_t r, u32 voff, u32 soff) {
  asm volatile("s_nop 4\n\tbuffer_load_dwordx4 %0, %1, %2, %3 offen nt" : "+v"(d) : "v"(voff), "s"(r), "s"(soff) : "memory");
}
__device__ __forceinline__ u32 perm_lo(u32 a, u32 b) { return __builtin_amdgcn_perm(b, a, 0x05040100u); }
__device__ __forceinline__ u32 perm_hi(u32 a, u32 b) { return __builtin_amdgcn_perm(b, a, 0x07060302u); }
__device__ __forceinline__ float rdl(float x, int l) { return __int_as_float(__builtin_amdgcn_readlane(__float_as_int(x), l)); }

typedef __attribute__((ext_vector_type(8))) __bf16 bf16x8;
__device__ __forceinline__ f32x4 mfma16(u32x4 a, u32x4 b, f32x4 c) {
  return __builtin_amdgcn_mfma_f32_16x16x32_bf16(__builtin_bit_cast(bf16x8, a), __builtin_bit_cast(bf16x8, b), c, 0, 0, 0);
}
typedef __attribute__((ext_vector_type(8))) unsigned u32x8;
typedef __attribute__((ext_vector_type(16))) __bf16 bf16x16;
__device__ __forceinline__ f32x4 mfma64(u32x4 a, u32x8 b, f32x4 c, u32 idx) {
  return __builtin_amdgcn_smfmac_f32_16x16x64_bf16(__builtin_bit_cast(bf16x8, a), __builtin_bit_cast(bf16x16, b), c, idx, 0, 0);
}
__device__ __forceinline__ float sum8(const float* p) {
  const f32x4 a = *reinterpret_cast<const f32x4*>(p), b = *reinterpret_cast<const f32x4*>(p + 4);
  return ((a[0] + b[0]) + (a[1] + b[1])) + ((a[2] + b[2]) + (a[3] + b[3]));
}
__device__ __forceinline__ float ln_ri(float var) {
  return __builtin_amdgcn_rcpf(rbf(__builtin_amdgcn_sqrtf(rbf(rbf(fmaxf(var, 0.f)) + 1e-5f))));
}
__device__ __forceinline__ void ln_stats2(const float (&x0)[8], const float (&x1)[8], float& mb0, float& ri0, float& mb1, float& ri1) {
  const float k0 = rdl(x0[0], 0), k1 = rdl(x1[0], 0);
  float s0 = 0.f, s1 = 0.f, q0 = 0.f, q1 = 0.f;
#pragma unroll
  for (int k = 0; k < 8; ++k) {
    const float d0 = x0[k] - k0, d1 = x1[k] - k1;
    s0 += d0; s1 += d1; q0 = fmaf(d0, d0, q0); q1 = fmaf(d1, d1, q1);
  }
  float t = swap16_add(swap32_add(s0, s1), swap32_add(q0, q1));
  t += dpp<XOR1>(t); t += dpp<XOR2>(t); t += dpp<XOR7>(t); t += dpp<XOR8>(t);
  auto u = __builtin_amdgcn_permlane16_swap(__float_as_uint(t), __float_as_uint(t), false, false);
  const float ms = __uint_as_float(u[0]) * (1.f / 512.f), mq = __uint_as_float(u[1]) * (1.f / 512.f);
  const float ri = ln_ri(fmaf(-ms, ms, mq));
  mb0 = rbf(k0 + rdl(ms, 0)); mb1 = rbf(k1 + rdl(ms, 32));
  ri0 = rdl(ri, 0); ri1 = rdl(ri, 32);
}
template <int I> struct IC { static constexpr int v = I; };
template <int I, int N, class F> __device__ __forceinline__ void sfor(F&& f) {
  if constexpr (I < N) { if (f(IC<I>{})) sfor<I + 1, N>(f); }
}
typedef __attribute__((ext_vector_type(4))) short s16x4;
typedef __attribute__((address_space(3))) s16x4* lds_s16x4p;
__device__ __forceinline__ u32x2 tr_read(u32 lds_addr) {
  return __builtin_bit_cast(u32x2, __builtin_amdgcn_ds_read_tr16_b64_v4i16((lds_s16x4p)lds_addr));
}
__device__ __forceinline__ u32 next_tag(u32 e) { return ((e | 0x40004000u) + 1u) & ~0x40004000u; }
__device__ __forceinline__ float u2f(u32 v) { return __uint_as_float(v); }
#define LDSP(T, a) ((__attribute__((address_space(3))) T*)(a))
template <class T> __device__ __forceinline__ u32 lds_addr(T* p) { u32 a = (u32)(uintptr_t)p; PIN(a); return a; }

#define TAB_LOAD "s_add_u32 s30, s28, wg_tab@rel32@lo+4\ns_addc_u32 s31, s29, wg_tab@rel32@hi+12\nglobal_load_dwordx2 %[te], %[va], s[30:31]\n"
#define FC(k) ".Lfc" #k
#define FC_REGS
#define FX(r, bit, k) "s_bitcmp1_b32 %[" #r "], " #bit "\ns_cselect_b32 %[sb], " FC(k) ", 0\ns_xor_b32 %[sa], %[sa], %[sb]\n"
__device__ __forceinline__ void probe_end(unsigned char* ws, u64 pt0, int lane) {
  while (wall_clock64() < pt0 + 5500ull) __builtin_amdgcn_s_sleep(8);
  u64 a[4], c2[4];
  for (int i = 0; i < 4; ++i) {
    a[i] = __hip_atomic_load(reinterpret_cast<u64*>(ws + (1u << 20)) + (i * 64 + lane) * 2, __ATOMIC_RELAXED, __HIP_MEMORY_SCOPE_AGENT);
    c2[i] = __hip_atomic_load(reinterpret_cast<u64*>(ws + (1u << 20)) + (i * 64 + lane) * 2 + 1, __ATOMIC_RELAXED, __HIP_MEMORY_SCOPE_AGENT);
  }
  int mn0 = 1 << 30, mx1 = -(1 << 30), mn1 = 1 << 30, sum = 0, cnt = 0;
  for (int i = 0; i < 4; ++i) {
    const int d0 = (int)(a[i] - pt0);
    mn0 = min(mn0, d0);
    if (c2[i] != 0ull) { const int d1 = (int)(c2[i] - pt0); mx1 = max(mx1, d1); mn1 = min(mn1, d1); sum += d1; cnt += 1; }
  }
  for (int o = 1; o < 64; o <<= 1) { mn0 = min(mn0, __shfl_xor(mn0, o)); mx1 = max(mx1, __shfl_xor(mx1, o)); mn1 = min(mn1, __shfl_xor(mn1, o)); sum += __shfl_xor(sum, o); cnt += __shfl_xor(cnt, o); }
  int X = PROBE_OP == 0 ? mx1 : (PROBE_OP == 1 ? sum / max(cnt, 1) : mn1);
  if (PROBE_K == 102) X = mn0;
  X = mn0 + (X - mn0) * PROBE_MUL;
  const u64 tg = pt0 + (u64)(long long)(X + 8000);
  for (int i = 0; i < 400000 && wall_clock64() < tg; ++i) __builtin_amdgcn_s_sleep(1);
}
__global__ void __launch_bounds__(512, 2) fused_kernel(const u16* kc_, const u16* vc_, const u16* wo_, const u16* wq_, const u16* wmod_, const u16* w1_, const u16* w3_,
                                                       unsigned char* ws_, const u16* x_, const u16* cnd_, const u16* w2_, u16* out_) {
  __shared__ __attribute__((aligned(256))) Smem sm;
  const u64 a_ka = (u64)__builtin_amdgcn_kernarg_segment_ptr();
  const int bid = blockIdx.x, tid = threadIdx.x;
  u64 wa, t64; u32x2 te; int tid0;
  u32 ar[14];
  u32 tch2, sa_, sb_, va_;
  u32x4 kr[NL], vr[NL];
  asm volatile(".set .Lfc0, %0\n.set .Lfc1, %1\n.set .Lfc2, %2\n.set .Lfc3, %3\n.set .Lfc4, %4\n.set .Lfc5, %5\n.set .Lfc6, %6\n.set .Lfc7, %7\n.set .Lfc8, %8\n.set .Lfc9, %9\n.set .Lfc10, %10\n.set .Lfc11, %11"
    :: "n"(first_col(PRM0, 0)), "n"(first_col(PRM0, 1)), "n"(first_col(PRM0, 2)), "n"(first_col(PRM0, 3)), "n"(first_col(PRM0, 4)), "n"(first_col(PRM0, 5)),
       "n"(first_col(PRM0, 6)), "n"(first_col(PRM0, 7)), "n"(first_col(PRM0, 8)), "n"(first_col(PRM0, 9)), "n"(first_col(PRM0, 10)), "n"(first_col(PRM0, 11)));
  asm volatile(
    "s_load_dwordx16 s[4:19], %[ka], 0x0\ns_load_dwordx8 s[20:27], %[ka], 0x40\n"
    "s_memrealtime %[t]\n"
    "v_lshlrev_b32 %[va], 3, %[bid]\n"
    "s_getpc_b64 s[28:29]\n.Lcpf_first:\n"
    TAB_LOAD
    "s_cmp_gt_u32 %[bid], 15\ns_cbranch_scc1 .Lno_cpf\n"
    "s_mov_b32 %[sa], .Lend_a-.Lcpf_first\ns_max_i32 %[sa], %[sa], .Lend_b-.Lcpf_first\ns_max_i32 %[sa], %[sa], .Lend_c-.Lcpf_first\ns_max_i32 %[sa], %[sa], .Lend_d-.Lcpf_first\ns_max_i32 %[sa], %[sa], .Lend_e-.Lcpf_first\n"
    "v_lshlrev_b32 %[va], 6, %[tid]\nv_min_u32 %[va], %[sa], %[va]\nglobal_load_dword %[pf], %[va], s[28:29]\n"
    ".Lno_cpf:\n"
    "v_readfirstlane_b32 %[t0], %[tid]\nv_and_b32 %[va], 63, %[tid]\n"
    "s_mov_b32 %[sa], " FC(11) "\n"
    FX(bid, 0, 0) FX(bid, 1, 1) FX(bid, 2, 2) FX(bid, 3, 3) FX(bid, 4, 4) FX(bid, 5, 5) FX(bid, 6, 6) FX(bid, 7, 7) FX(t0, 6, 8) FX(t0, 7, 9) FX(t0, 8, 10)
    "v_lshl_add_u32 %[va], %[va], 4, %[sa]\n"
    "s_waitcnt lgkmcnt(0)\nglobal_load_dwordx4 %[k0], %[va], s[4:5] nt\nglobal_load_dwordx4 %[v0], %[va], s[6:7] nt\n"
    "s_bitcmp1_b32 %[t0], 6\ns_cselect_b64 s[14:15], s[16:17], s[14:15]\n"
    "s_bitcmp1_b32 %[bid], 4\ns_cselect_b64 s[8:9], s[10:11], s[8:9]\ns_cselect_b64 s[12:13], s[14:15], s[12:13]\ns_bitcmp1_b32 %[bid], 5\ns_cselect_b64 %[wa], s[12:13], s[8:9]"
    : "=&{s4}"(ar[0]), "=&{s5}"(ar[1]), "=&{s6}"(ar[2]), "=&{s7}"(ar[3]), "=&{s18}"(ar[4]), "=&{s19}"(ar[5]), "=&{s20}"(ar[6]), "=&{s21}"(ar[7]), "=&{s22}"(ar[8]), "=&{s23}"(ar[9]),
      "=&{s24}"(ar[10]), "=&{s25}"(ar[11]), "=&{s26}"(ar[12]), "=&{s27}"(ar[13]), [wa] "=&s"(wa), [t0] "=&s"(tid0), [t] "=&s"(t64), [te] "=&v"(te), [sa] "=&s"(sa_), [sb] "=&s"(sb_),
      [va] "=&v"(va_), [pf] "=&v"(tch2), [k0] "=&v"(kr[0]), [v0] "=&v"(vr[0])
    : [ka] "s"(a_ka), [bid] "s"(bid), [tid] "v"(tid) : "s8", "s9", "s10", "s11", "s12", "s13", "s14", "s15", "s16", "s17", "s28", "s29", "s30", "s31", "scc", "memory" FC_REGS);
  asm volatile("" : "+s"(ar[0]), "+s"(ar[1]), "+s"(ar[2]), "+s"(ar[3]), "+s"(ar[4]), "+s"(ar[5]), "+s"(ar[6]), "+s"(ar[7]), "+s"(ar[8]), "+s"(ar[9]), "+s"(ar[10]), "+s"(ar[11]), "+s"(ar[12]), "+s"(ar[13]));
  asm volatile("" : "+s"(wa), "+s"(t64), "+s"(tid0), "+s"(sa_));
  const u32 tka = (u32)wall_clock64();
  __builtin_assume(tid >= 0 && tid < 512 && tid0 >= 0 && tid0 < 512 && bid >= 0 && bid < 256);
  constexpr Prm P = PRM0;
  const u64 pt0 = t64; u64 pst = (PROBE_K == 100 || PROBE_K == 102) ? pt0 : 0;
  const u32 t0 = tka + min(max((int)(tka - (u32)t64) - P.SA, 0) * P.SG >> 4, P.SC);
  const int TEND = P.TEND;
  const int xcd = bid & 7, part = bid >> 4 & 3;
  const int lane = tid & 63, wave = tid0 >> 6;
  SB();
  if ((bid >> 4) == 2) {
    const u32 o = tid < 384 ? G_U + (bid & 15) * 6144 + tid * 16 : OOB;
    __builtin_amdgcn_raw_buffer_store_b128(u32x4{SENT, SENT, SENT, SENT}, mk_rsrc((void*)(ar[4] | (u64)ar[5] << 32), WS_END), o, 0, AUX_SC1);
  }
  SB();
  const bool p3 = part == 3;
  u32 pitch = (u32)(0x005600c001000020ull >> (part * 16) & 0xffff) * 32u;
  const int sh = p3 ? 2 : 3;
  u32 wv0 = (u32)((p3 ? (wave >> 1) * 128 : wave * 64) + (part == 0 ? xcd * 512 : 0) + (lane >> sh)) * pitch + ((lane & (p3 ? 3 : 7)) ^ (lane >> (p3 ? 5 : 3))) * 16u;
  const u32 wimg = (u32)(uintptr_t)(&sm.wbuf[0][0][0]);
  u32 wsz = (part == 0 ? 4096u : 512u) * pitch, wld = wimg + (p3 ? (wave & 1) * 32768 + (wave >> 1) * 8192 : wave * 8192);
  PIN(wv0);
  asm volatile("" : "+s"(wsz), "+s"(wld), "+s"(pitch));
  auto karg = [&](int i) { return ar[2 * i] | (u64)ar[2 * i + 1] << 32; };
  const u64 a_kc = karg(0), a_vc = karg(1);
  unsigned char* ws = (unsigned char*)karg(2);
  const u16 *x = (const u16*)karg(3), *cnd = (const u16*)karg(4), *w2 = (const u16*)karg(5);
  u16* out = (u16*)karg(6);
  const rsrc_t wsr = mk_rsrc(ws, WS_END);
  asm volatile("s_waitcnt vmcnt(3)\n\ts_cmp_eq_u32 %1, 2\n\ts_cbranch_scc1 1f\n\ts_waitcnt vmcnt(2)\n1:" : "+v"(te) : "s"(bid >> 4) : "scc");
  const u32 te0 = __builtin_amdgcn_readfirstlane(te[0]), te1 = __builtin_amdgcn_readfirstlane(te[1]);
  const int bl0 = te0 & 127, nl = te0 >> 7 & 63, blk = te0 >> 13 & 63;
  const bool has_w = te0 >> 19 & 1;
  const rsrc_t rw = mk_rsrc((const void*)wa, wsz);
  const u32 wstep = pitch << (6 - sh), wflip = (p3 || part == 0) ? 32u : 0u, wflip0 = part == 0 ? 32u : 0u;
  u32 wso = blk << (sh + 4);
  int wleft = 8;
  auto piece = [&]() {
    dma16(rw, wv0, wso, wld);
    wso += wstep; wld += 1024;
    wv0 ^= (wleft & 1) ? wflip : wflip0;
    --wleft;
  };
  SB();
  STAMP(23);
  const int b = bid >> 3 & 1, head = xcd * 4 + (bid >> 6 ^ 3);
  const int g = lane >> 4, c = lane & 15;
  const int i8 = b | bid >> 6 << 1, wi = i8 * 8 + xcd;
  const bool is_up = p3 && has_w, modc = part == 2 && wi < 16;
  const size_t base = ((size_t)(b * NH + head) * SK + (SK - WIN)) * DH;
  const rsrc_t rk = mk_rsrc((const u16*)a_kc + base, WIN * DH * 2), rv = mk_rsrc((const u16*)a_vc + base, WIN * DH * 2);
  int tau = te1 & 0xffff;
  const int tauw = te0 >> 24 << 7, wdef = te0 >> 20 & 15;
  int due = P.NF * 256, now = 0;
  auto gate = [&]() {
    while (now < due) { now = (int)(((u32)wall_clock64() - t0) << 8); if (now < due) __builtin_amdgcn_s_sleep(1); }
    due += tau;
  };
  auto until = [&](int t) { while ((int)(((u32)wall_clock64() - t0) << 8) < t); };
  const u32 pslot = dup(G_P + (head & 15) * 2048 + (b * 4 + part) * 16);
  const u32 die = xcd >> 2;
  const bool voter = p3 && i8 == 1, timer = p3 && i8 < 2;
  int vote = 0, lag = 0, shift = 0;
  const u32 pbo = dup(G_PB) + wave * 1152;
  u32 near = 0;
  u32x2 ep; u32 xs0, xs1, tch1; u32x4 xr0, xr1;
  const rsrc_t rx = mk_rsrc(part == 2 ? cnd : x, (part == 1 || (part == 2 && has_w)) ? 2048u : 0u);
  auto small_ops = [&]() {
    ep = ld8<AUX_SC1>(wsr, wave == 7 ? WS_EPOCH : OOB, 0);
    xs0 = __builtin_amdgcn_raw_buffer_load_b16(rx, tid * 2, 0, 0); xs1 = __builtin_amdgcn_raw_buffer_load_b16(rx, tid * 2, 1024, 0);
    xr0 = ld16<0>(rx, part == 1 ? lane * 16 : OOB, 0); xr1 = ld16<0>(rx, part == 1 ? lane * 16 : OOB, 1024);
    tch1 = __builtin_amdgcn_raw_buffer_load_b32(mk_rsrc(w2 + (size_t)blk * 32 * 512, is_up ? 64u : 0u), tid == 384 ? 0 : OOB, 0, AUX_NT);
  };
  if (has_w) {
    if (modc) now = 1 << 30;
#pragma nounroll
    while (wleft > wdef) { gate(); due += tauw - tau; piece(); }
    now = 0;
  }
  due = max(due, P.TK * 256);
  SB();
  u32 sot = sa_ & 0xffc00u;
  {
    const u32 s1 = (bl0 + lane - 1 - (lane >= SK0 ? NL - nl : 0)) ^ (124 + part);
#pragma unroll
    for (int k = 0; k < 7; ++k) sot ^= (u32)-(int)(s1 >> k & 1u) & col(P, k);
    PIN(sot);
  }
  u32 tag = 0;
  u32x4 bf[8];
  auto wo_prep = [&]() {
    const u32 wi_ = wimg + wave * 8192;
    const int q = (lane >> 2) & 3, pp = lane & 3;
#pragma unroll
    for (int s = 0; s < 2; ++s) {
#pragma unroll
      for (int t = 0; t < 4; ++t) {
        const int r0 = s * 32 + g * 8 + q, r1 = r0 + 4, ch = t * 2 + (pp >> 1);
        const u32x2 lo = tr_read(wi_ + r0 * 128 + ((ch ^ (r0 & 7)) * 16) + (pp & 1) * 8), hi = tr_read(wi_ + r1 * 128 + ((ch ^ (r1 & 7)) * 16) + (pp & 1) * 8);
        bf[s * 4 + t] = u32x4{lo[0], lo[1], hi[0], hi[1]};
      }
    }
  };
  auto gemv_go = [&](u32 hh) {
    sm.hb[tid] = (u16)hh; sm.hb[512 + tid] = (u16)(hh >> 16);
    WAVE_SYNC();
    const u32x4* a = (c & 11) == 0 ? reinterpret_cast<const u32x4*>(&sm.hb[(c >> 2) * 512 + wave * 64 + g * 8]) : &sm.zero[0];
    const u32x4 a0 = a[0], a1 = a[(c & 11) == 0 ? 4 : 0];
    const f32x4 z = {0.f, 0.f, 0.f, 0.f};
    f32x4 d[4];
#pragma unroll
    for (int t = 0; t < 4; ++t) d[t] = mfma16(a0, bf[t], z);
#pragma unroll
    for (int t = 0; t < 4; ++t) d[t] = mfma16(a1, bf[4 + t], d[t]);
    if (lane < 32) {
#pragma unroll
      for (int t = 0; t < 4; ++t) sm.r2[(t * 16 + c) * 20 + g * 8 + wave] = d[t][0];
    }
  };
  auto mod_stage = [&]() {
    wo_prep();
    gemv_go(pack_bf(silu_f(lo_f(xs0)), silu_f(lo_f(xs1))));
    __builtin_amdgcn_s_waitcnt(0xc07f);
    if (lane == 0) *LDSP(volatile u32, (u32)(uintptr_t)&sm.mflag[wave]) = 1u;
    if (wave == 7) {
#pragma nounroll
      for (int spin = 0; spin < SPIN_MAX; ++spin) {
        const u32 f = *LDSP(volatile u32, (u32)(uintptr_t)&sm.mflag[lane & 7]);
        if (all_ok(f != 0u)) break;
        __builtin_amdgcn_s_sleep(1);
      }
      CFENCE();
      st8_sc1(wsr, G_MOD + (blk * 64 + lane) * 8, pack_bf(sum8(&sm.r2[lane * 20]), sum8(&sm.r2[lane * 20 + 8])), tag);
    }
    STAMP(9);
  };
  if (lane < 4) sm.zero[lane] = u32x4{0u, 0u, 0u, 0u};
  if (part == 2 && has_w) {
    if (tid < 8) sm.mflag[tid] = 0u;
    LDS_BARRIER();
    if (__builtin_expect(modc, 0)) {
      small_ops();
      asm volatile("s_waitcnt vmcnt(0)" ::: "memory");
      tag = next_tag(__builtin_amdgcn_readfirstlane(ep[0]));
      mod_stage();
      __builtin_amdgcn_s_barrier();
    }
  }
  SB();
  const int fd = (xcd * 8 + i8) * 8 + (lane & 3) * 2;
  const int ub = wave >> 2, ud = (wave & 3) * 128 + c * 8 + g * 2;
  u32x4 sg[3]; u32 sx2;
  auto side_loads = [&]() {
    const bool fin = part == 1 && wave == 0;
    sg[0] = ld16<AUX_SC1>(wsr, is_up ? G_MOD + (1024 + ud) * 8 : (fin ? G_MOD + (1024 + fd) * 8 : OOB), 0);
    sg[1] = ld16<AUX_SC1>(wsr, is_up ? G_MOD + (1536 + ud) * 8 : (fin ? G_MOD + (2560 + fd) * 8 : OOB), 0);
    sg[2] = ld16<AUX_SC1>(wsr, is_up ? G_MOD + (2048 + ud) * 8 : OOB, 0);
    sx2 = __builtin_amdgcn_raw_buffer_load_b32(mk_rsrc(x, (is_up || fin) ? 2048u : 0u), is_up ? ub * 1024 + ud * 2 : (g & 1) * 1024 + fd * 2, 0, 0);
  };
  auto side_pin = [&]() { PIN(sg[0]); PIN(sg[1]); PIN(sg[2]); PIN(sx2); };
  const u32 mlo = G_ML + (head * 2 + b) * 64 + (lane & 3) * 16;
  float fmrg = 0.f;
  auto merge_factor = [&](const u32x4& ml) {
    const float mi = u2f(ml[0]);
    float M = fmaxf(mi, dpp<XOR1>(mi)); M = fmaxf(M, dpp<XOR2>(M));
    const float e = __expf(mi - M);
    float L = e * u2f(ml[2]);
    L += dpp<XOR1>(L); L += dpp<XOR2>(L);
    fmrg = rdl(e * __builtin_amdgcn_rcpf(L), part);
  };
  f32x2 ga = {0.f, 0.f}, xin = {0.f, 0.f}, cc1 = {0.f, 0.f};
  float Af = 0.f, Bf = 0.f;
  const int hpos = ub * 512 + (ud & ~15) + (g & 1) * 8 + (c & 1) * 4 + (g >> 1) * 2;
  auto up_prep = [&]() {
    if (!all_ok((sg[0][1] == tag) & (sg[0][3] == tag) & (sg[1][1] == tag) & (sg[1][3] == tag) & (sg[2][1] == tag) & (sg[2][3] == tag))) gather16<3>(wsr, G_MOD + (1024 + ud) * 8, 512 * 8, tag, sg);
    ga = f32x2{ub ? hi_f(sg[0][0]) : lo_f(sg[0][0]), ub ? hi_f(sg[0][2]) : lo_f(sg[0][2])};
    cc1 = f32x2{rbf(1.f + (ub ? hi_f(sg[2][0]) : lo_f(sg[2][0]))), rbf(1.f + (ub ? hi_f(sg[2][2]) : lo_f(sg[2][2])))};
    xin = f32x2{lo_f(sx2), hi_f(sx2)};
    u32* hp = reinterpret_cast<u32*>(&sm.hb[hpos]);
    hp[512] = pack_bf(cc1[0], cc1[1]);
    hp[1024] = ub ? perm_hi(sg[1][0], sg[1][2]) : perm_lo(sg[1][0], sg[1][2]);
  };
  u32x8 bw[4];
  auto up_frags = [&]() {
    const int pp = lane & 3;
    const u32 a = wimg + (pp & 1) * 32768 + ((pp >> 1) * 32 + g * 8 + ((lane >> 2) & 3)) * 64 + (((wave >> 1) ^ ((g & 1) | (pp & 2))) * 16) + (wave & 1) * 8;
#pragma unroll
    for (int j = 0; j < 4; ++j) {
      const u32x2 r0 = tr_read(a + j * 4096), r1 = tr_read(a + j * 4096 + 256), r2 = tr_read(a + j * 4096 + 16384), r3 = tr_read(a + j * 4096 + 16640);
      bw[j] = u32x8{r0[0], r0[1], r1[0], r1[1], r2[0], r2[1], r3[0], r3[1]};
    }
  };
  u32 wfa[4];
  auto wo_frags = [&](int s0) {
#pragma unroll
    for (int t = 0; t < 4; ++t) {
#pragma unroll
      for (int s = s0; s < 2; ++s) {
        const u32x2 lo = tr_read(wfa[t] + s * 4096), hi = tr_read(wfa[t ^ 1] + s * 4096 + 256);
        bw[t][s * 4] = lo[0]; bw[t][s * 4 + 1] = lo[1]; bw[t][s * 4 + 2] = hi[0]; bw[t][s * 4 + 3] = hi[1];
      }
    }
  };
  const bool par = lane & 8;
  auto limit = [&](auto sc) {
    constexpr int s = sc.v;
    WAIT_VM(window(s, 1 + (PRM0.W[s >> 3] >> (4 * (s & 7)) & 7)));
    SB();
  };
  auto sample = [&]() { if (timer) lag += (int)(((u32)wall_clock64() - t0) << 8) - due + ((int)__builtin_amdgcn_s_getreg(7 | (3 << 11)) + 1) * tau; };
  auto report = [&](u32 dst) {
    if (timer) {
      if (lane == 0) sm.mflag[wave] = vote * 65536 + min(max(lag >> 8, 0), 4000);
      lag = 0;
      LDS_BARRIER();
      if (wave == 7) {
        int s = sm.mflag[lane & 7];
        s += __builtin_amdgcn_mov_dpp(s, XOR1, 0xf, 0xf, true); s += __builtin_amdgcn_mov_dpp(s, XOR2, 0xf, 0xf, true); s += __builtin_amdgcn_mov_dpp(s, XOR7, 0xf, 0xf, true);
        if (lane < 8) st8_sc1(wsr, dst + lane * 128 + wi * 8, s, tag);
      }
    }
    SB();
  };
  float l0 = 0.f, l1 = 0.f;
  u32x2 gmd[2], tbg;
  int t1 = 0;
#pragma unroll
  for (int i = SK0; i < SK1; ++i) kr[i] = u32x4{0u, 0u, 0u, 0u};
  const int first = SK0 + NL - nl;
  auto left_out = [&](int i) { int f = first; asm volatile("" : "+s"(f)); return i < f; };
  sfor<1, NL>([&](auto ic) {
    constexpr int i = ic.v;
    if constexpr (i >= SK0 && i < SK1) { if (left_out(i)) return true; }
    const u32 so = __builtin_amdgcn_readlane(sot, i);
    gate();
    if constexpr ((i >= TBS && i <= TBP) || (i >= RPS && i <= RPP)) sample();
    SB();
    if constexpr (i >= SK0 && i < SK1) ld16_into(kr[i], rk, lane * 16, so); else kr[i] = ld16<AUX_NT>(rk, lane * 16, so);
    if constexpr (i == SMO) small_ops();
    if constexpr (i == SMO + 1) { if (voter && (xcd & 3) == 0) __builtin_amdgcn_raw_buffer_store_b32(0u, wsr, pbo + (lane >> 5) * 8192 + (lane & 31) * 4, 0, AUX_SC1); }
    if constexpr (i == SK1) tbg = ld8<AUX_SC1>(wsr, wave == 7 ? G_VT + xcd * 128 + c * 8 : OOB, 0);
    if constexpr (i == TBL) tbg = ld8<AUX_SC1>(wsr, wave == 7 ? G_TB + xcd * 128 + c * 8 : OOB, 0);
    if constexpr (i == QP) { gmd[0] = ld8<AUX_SC1>(wsr, part == 1 ? G_MOD + tid * 8 : OOB, 0); gmd[1] = ld8<AUX_SC1>(wsr, part == 1 ? G_MOD + tid * 8 : OOB, 4096); }
    SB();
    SB();
    if (i >= 4) limit(ic);
    if (i == 8) STAMP(12);
    if (i == 20) STAMP(21);
    if (i == 32) STAMP(22);
    if (i == TBP) {
      if (timer) {
        if (lane == 0) sm.mflag[wave] = lag;
        lag = 0;
        LDS_BARRIER();
        if (wave == 7) {
          int s = sm.mflag[lane & 7];
          s += __builtin_amdgcn_mov_dpp(s, XOR1, 0xf, 0xf, true); s += __builtin_amdgcn_mov_dpp(s, XOR2, 0xf, 0xf, true); s += __builtin_amdgcn_mov_dpp(s, XOR7, 0xf, 0xf, true);
          if (lane < 8) st8_sc1(wsr, G_TB + lane * 128 + wi * 8, t0 << 10 | (u32)min(s / (2048 * (TBP - TBS + 1)), 1023), tag);
        }
      }
      SB();
    }
    if (i == PRB) {
      if (voter) {
        u64 ta, tb, tc; u32 dm;
        const u32 pa = pbo + (wave & 1) * 8192;
        asm volatile("s_memtime %0\n\ts_waitcnt lgkmcnt(0)\n\ts_load_dword %3, %4, %5\n\ts_waitcnt lgkmcnt(0)\n\ts_memtime %1\n\ts_waitcnt lgkmcnt(0)\n\t"
                     "s_load_dword %3, %4, %6\n\ts_waitcnt lgkmcnt(0)\n\ts_memtime %2\n\ts_waitcnt lgkmcnt(0)"
                     : "=&s"(ta), "=&s"(tb), "=&s"(tc), "=&s"(dm) : "s"(ws), "s"(pa), "s"(pa ^ 8192u));
        const int d = min(max((int)(2 * (u32)tb - (u32)ta - (u32)tc), -500), 500);
        vote = ((wave ^ die) & 1) ? -d : d;
        STAMP(26);
      }
      SB();
    }
    if (i == TBU) {
      if (wave == 7) {
        const bool ok = tbg[1] == tag;
        float lf = ok ? (float)min(max((int)((t0 << 10 | 1023u) - tbg[0]) >> 10, -300), 300) : 0.f, nf = ok ? 1.f : 0.f, lg = ok ? (float)(tbg[0] & 1023u) : 0.f;
        lf += dpp<XOR1>(lf); nf += dpp<XOR1>(nf); lg += dpp<XOR1>(lg); lf += dpp<XOR2>(lf); nf += dpp<XOR2>(nf); lg += dpp<XOR2>(lg);
        lf += dpp<XOR7>(lf); nf += dpp<XOR7>(nf); lg += dpp<XOR7>(lg); lf += dpp<XOR8>(lf); nf += dpp<XOR8>(nf); lg += dpp<XOR8>(lg);
        const float rn = __builtin_amdgcn_rcpf(fmaxf(nf, 1.f));
        shift = __builtin_amdgcn_readfirstlane(min(max((int)((lg * rn - (float)P.GS) * (float)P.GB * 0.125f), 0), 150));
        if (lane == 0) sm.bc = u32x2{tag, t0 - (u32)(int)(lf * rn) + shift};
      }
      LDS_BARRIER();
      const u32x2 bc = sm.bc;
      tag = __builtin_amdgcn_readfirstlane(bc[0]);
      t1 = (__builtin_amdgcn_readfirstlane((int)(bc[1] - t0)) + TEND + P.X1) * 256;
      tau -= __builtin_amdgcn_readfirstlane((int)((float)(int)(t0 - bc[1]) * 256.f * __builtin_amdgcn_rcpf((float)(nl - TBU - 1))));
      SB();
    }
    if (i == RPP) report(G_VT);
    if (i == LNS) {
      tag = next_tag(__builtin_amdgcn_readfirstlane(ep[0]));
      if (part == 1) {
        float x0[8], x1[8], mb0, ri0, mb1, ri1;
#pragma unroll
        for (int k = 0; k < 4; ++k) { x0[2 * k] = lo_f(xr0[k]); x0[2 * k + 1] = hi_f(xr0[k]); x1[2 * k] = lo_f(xr1[k]); x1[2 * k + 1] = hi_f(xr1[k]); }
        ln_stats2(x0, x1, mb0, ri0, mb1, ri1);
        l0 = rbf(rbf(lo_f(xs0) - mb0) * ri0); l1 = rbf(rbf(lo_f(xs1) - mb1) * ri1);
      }
      PIN(l0); PIN(l1);
      SB();
    }
    if (i == WUSE) {
      PIN(tch1); PIN(tch2);
      if ((part == 1 || (part == 2 && has_w && !modc))) {
        WAIT_VM(WK); CFENCE();
        if (part == 2) mod_stage(); else wo_prep();
      }
#pragma unroll
      for (int k = 0; k < 8; ++k) PIN(bf[k]);
      SB();
    }
    if (i == QP + QD) {
      if (part == 1) {
        u32 gm[2] = {gmd[0][0], gmd[1][0]};
        if (!all_ok((gmd[0][1] == tag) & (gmd[1][1] == tag))) { STAMP(11); gather8<2>(wsr, G_MOD + tid * 8, 512 * 8, tag, gm); }
        STAMP(5);
        gemv_go(pack_bf(rbf(l0 * rbf(1.f + lo_f(gm[1]))) + lo_f(gm[0]), rbf(l1 * rbf(1.f + hi_f(gm[1]))) + hi_f(gm[0])));
      }
      SB();
    }
    if (i == QP + QD + 1) {
      if (part == 1) {
        LDS_BARRIER();
        if (wave == 0) {
          const int bb = lane >> 5, jq = lane & 31;
          st8_sc1(wsr, G_Q + (bb * 2048 + blk * 32 + jq) * 8, pack_bf(sum8(&sm.r2[2 * jq * 20 + bb * 8]), sum8(&sm.r2[(2 * jq + 1) * 20 + bb * 8])), tag);
        }
        STAMP(6);
      }
      SB();
    }
    return true;
  });
  PIN(sot);
  if (wave == 7) {
    const bool ok = tbg[1] == tag;
    float lg = ok ? (float)(tbg[0] & 0xffffu) : 0.f, nf = ok ? 8.f * (RPP - RPS + 1) : 0.f;
    lg += dpp<XOR1>(lg); nf += dpp<XOR1>(nf); lg += dpp<XOR2>(lg); nf += dpp<XOR2>(nf); lg += dpp<XOR7>(lg); nf += dpp<XOR7>(nf); lg += dpp<XOR8>(lg); nf += dpp<XOR8>(nf);
    lg *= __builtin_amdgcn_rcpf(fmaxf(nf, 1.f));
    if (lane == 0) sm.bc2 = u32x2{(u32)min(shift * P.DG * 32, P.EW * 256), (u32)min(max((int)((lg - (float)P.B0) * (float)(P.BG * 32) + fmaxf(lg - (float)P.TC, 0.f) * (float)(P.RT * 32)) - shift * P.D0 * 32, 0), 200 * 256)};
  }
  LDS_BARRIER();
  {
    const u32x2 bc = sm.bc2;
    const int st = __builtin_amdgcn_readfirstlane(bc[1]), sp = (st * P.BF) >> 3;
    t1 += st + __builtin_amdgcn_readfirstlane(bc[0]);
    due += P.TG * 256 + sp;
    tau = (te1 >> 16) + __builtin_amdgcn_readfirstlane((int)((float)(st - sp) * __builtin_amdgcn_rcpf((float)(nl - 3))));
  }
  const u32 qoff = G_Q + ((b * 32 + head) * 64 + c * 4) * 8;
  u32x4 qg[2], mlg;
  u32 qq[4];
  constexpr int NB = (NL + 15) / 16;
  float xlo, sc[NB], pr[NB], m = 0.f;
  f32x4 acc = {0.f, 0.f, 0.f, 0.f};
  u32 pb[NB], pm[4], pl8[8], cpf;
  const u32 adst0 = lds_addr(lane < 32 ? &sm.red[wave][c * 8 + g * 4] : &sm.r2[(lane - 32) * 4]);
  u32 po, pso, rdst, rsrc, xo;
  auto wo_addr = [&]() {
    po = dup(G_P + (xcd & 3) * 8192) + tid * 16 + near;
    pso = tid < 16 ? pslot + c * 128 + near : OOB;
    PIN(pso);
    rdst = (u32)(uintptr_t)&sm.red2[wave][(lane & 31) * 4 + (lane >> 5) * 2];
    rsrc = (u32)(uintptr_t)&sm.red2[0][((wave & 1) * 16 + c) * 4 + (g & 1) * 2 + (g >> 1)];
    xo = dup(G_XP + xcd * 2048 + (wave & 1) * 1024 + (blk * 64 + (lane & ~1)) * 2) + (lane & 1) * 8192;
    PIN(po); PIN(rdst); PIN(rsrc); PIN(xo);
    const int q = (lane >> 2) & 3, pp = lane & 3, r = (g & 1) * 4 + (q & 1);
#pragma unroll
    for (int t = 0; t < 4; ++t) { wfa[t] = wimg + wave * 8192 + ((g >> 1) * 16 + (q >> 1) * 8 + r) * 128 + (pp & 1) * 8 + ((t * 2 + (pp >> 1)) ^ r ^ (q & 2)) * 16; PIN(wfa[t]); }
  };
  const u32 nadd = wdef ? P.LW : (is_up ? P.LU : 0);
  u32 nto = tid < 2 ? dup(G_NT) + bid * 8 + tid * 8192 : OOB;
  PIN(nto);
  auto notices = [&]() -> int {
    u32 off = dup(G_NT) + near + lane * 16;
    PIN(off);
    until(due);
    int mx;
#pragma nounroll
    for (int spin = 0; spin < SPIN_MAX; ++spin) {
      CFENCE();
      const u32x4 ga_ = ld16<AUX_SC1>(wsr, off, 0), gb_ = ld16<AUX_SC1>(wsr, off, 1024);
      mx = max(max((int)(ga_[0] - t0), (int)(ga_[2] - t0)), max((int)(gb_[0] - t0), (int)(gb_[2] - t0)));
      if (all_ok((((ga_[1] ^ tag) | (ga_[3] ^ tag)) | ((gb_[1] ^ tag) | (gb_[3] ^ tag))) == 0u)) break;
      for (int k = 0; k < P.AG; ++k) __builtin_amdgcn_s_sleep(1);
    }
    if (all_ok(mx <= (t1 >> 8) - P.AC)) return t1;
    mx = max(mx, __builtin_amdgcn_mov_dpp(mx, XOR1, 0xf, 0xf, true)); mx = max(mx, __builtin_amdgcn_mov_dpp(mx, XOR2, 0xf, 0xf, true));
    mx = max(mx, __builtin_amdgcn_mov_dpp(mx, XOR7, 0xf, 0xf, true)); mx = max(mx, __builtin_amdgcn_mov_dpp(mx, XOR8, 0xf, 0xf, true));
    mx = max(max(__builtin_amdgcn_readlane(mx, 0), __builtin_amdgcn_readlane(mx, 16)), max(__builtin_amdgcn_readlane(mx, 32), __builtin_amdgcn_readlane(mx, 48)));
    return max(min(mx, 1 << 20) * 256, t1 + P.L0 * 256) + P.L1 * 256;
  };
  auto pv_row = [&](auto jc) {
    constexpr int i = jc.v;
    const u32 pv = __float_as_uint(bcast16(u2f(pb[i >> 4]), i & 15));
    acc = mfma16(u32x4{pv & pm[0], pv & pm[1], pv & pm[2], pv & pm[3]}, vr[i], acc);
    PIN(acc);
    return true;
  };
  sfor<0, NL - 1>([&](auto ic) {
    constexpr int v = ic.v;
    if constexpr (v >= SK0 && v < SK1) { if (left_out(v)) return true; }
    if constexpr (v > 0) {
      const u32 so = __builtin_amdgcn_readlane(sot, v);
      if constexpr (v >= SK1) { if (v == NL - P.NE) tau = (tau * P.FE) >> 4; }
      gate();
      if constexpr (v >= RVS && v <= RVP) sample();
      SB();
      if constexpr (v >= SK0 && v < SK1) ld16_into(vr[v], rv, lane * 16, so); else vr[v] = ld16<AUX_NT>(rv, lane * 16, so);
      SB();
      SB();
    }
    if constexpr (v == SK0 - 1) {
#pragma unroll
      for (int i = SK0; i < SK1; ++i) { vr[i] = u32x4{0u, 0u, 0u, 0u}; PIN(vr[i]); }
    }
    if constexpr (v == VQ - 4) { qg[0] = ld16<AUX_SC1>(wsr, qoff, 0); qg[1] = ld16<AUX_SC1>(wsr, qoff + 16, 0); }
    if constexpr (v == VSIDE) side_loads();
    if constexpr (v == CPF) {
      u64 pc; u32 dlt, len;
      asm volatile("s_getpc_b64 %0\n.Lcpf_here:\n\ts_mov_b32 %1, .Ltail_start-.Lcpf_here\n\ts_mov_b32 %2, .Lend_a-.Ltail_start\n\ts_max_i32 %2, %2, .Lend_b-.Ltail_start\n\ts_max_i32 %2, %2, .Lend_c-.Ltail_start\n\ts_max_i32 %2, %2, .Lend_d-.Ltail_start\n\ts_max_i32 %2, %2, .Lend_e-.Ltail_start" : "=s"(pc), "=s"(dlt), "=s"(len) :: "scc");
      pc += dlt;
      const rsrc_t rc = mk_rsrc((const void*)pc, (part == 3 && i8 == 1) ? len : 0u);
      cpf = __builtin_amdgcn_raw_buffer_load_b32(rc, tid * 64, 0, 0);
    }
    if constexpr (v == SK1 + 2) PIN(cpf);
    if constexpr (v == RVP) report(G_VT + 1024);
    if constexpr (v == RVL) tbg = ld8<AUX_SC1>(wsr, wave == 7 ? G_VT + (g ? 0 : 1024) + xcd * 128 + c * 8 : OOB, 0);
    if constexpr (v == SK1) mlg = ld16<AUX_SC1>(wsr, wave == 0 ? mlo : OOB, 0);
    SB();
    if constexpr (NL + v < 72) limit(IC<NL + v>{});
    if constexpr (v >= SK0 + WV) { WAIT_VM(v < SK1 ? WV : WV + 1); SB(); }
    if constexpr (v == VQ) {
      if (!all_ok((qg[0][1] == tag) & (qg[0][3] == tag) & (qg[1][1] == tag) & (qg[1][3] == tag))) { STAMP(20); gather16<2>(wsr, qoff, 16, tag, qg); }
      STAMP(1); WSTAMP(1);
      qq[0] = qg[0][0]; qq[1] = qg[0][2]; qq[2] = qg[1][0]; qq[3] = qg[1][2];
    }
    if constexpr (v >= VQ && v < VS) {
      constexpr int sl = v - VQ, bt = sl >> 1;
      float xs[8];
#pragma unroll
      for (int i = 0; i < 8; ++i) {
        float d = 0.f;
        if (sl * 8 + i < NL) {
#pragma unroll
          for (int k = 0; k < 4; ++k) d = dot2(kr[sl * 8 + i][k], qq[k], d);
        }
        xs[i] = d;
      }
#pragma unroll
      for (int h = 0; h < 4; ++h) xs[h] = sel_add<XOR7>(xs[h], xs[h + 4], c & 4);
#pragma unroll
      for (int h = 0; h < 2; ++h) xs[h] = sel_add<XOR2>(xs[h], xs[h + 2], c & 2);
      xs[0] = sel_add<XOR1>(xs[0], xs[1], c & 1);
      if constexpr ((sl & 1) || sl == NSL - 1) {
        const float t = (sl & 1) ? sel_add<XOR8>(xlo, xs[0], c & 8) : xs[0] + dpp<XOR8>(xs[0]);
        const float s = rbf(rbf(t) * 0.08838834764831845f);
        sc[bt] = (bt * 16 + c < SK0 || (bt * 16 + c >= first && bt * 16 + c < NL)) ? s : -__builtin_inff();
        PIN(sc[bt]);
      } else {
        xlo = xs[0];
        PIN(xlo);
      }
    }
    if constexpr (v == VS) {
      float mloc = sc[0];
#pragma unroll
      for (int bt = 1; bt < NB; ++bt) mloc = fmaxf(mloc, sc[bt]);
      m = fmaxf(wave_max(mloc), -3.0e38f);
      float ls = 0.f;
#pragma unroll
      for (int bt = 0; bt < NB; ++bt) { pr[bt] = __expf(sc[bt] - m); ls += pr[bt]; }
      const float l = wave_sum(ls);
      if (lane == 0) { sm.ml[wave][0] = m; sm.ml[wave][1] = l; }
      if (c == ((NL - 1) & 15)) sm.pl[g][wave] = pr[NB - 1];
      STAMP(2); WSTAMP(2);
    }
    if constexpr (v >= SK1) pv_row(IC<v - SK1 + SK0 - WV>{});
    if constexpr (v < SK0 || v >= SK0 + WV) sfor<cons_end(v - 1), cons_end(v)>(pv_row);
    if constexpr (v == RVU) {
      if (wave == 7) {
        if (__builtin_expect(!all_ok(tbg[1] == tag || g == 0), 0)) {
          u32 vg[1];
          gather8<1>(wsr, G_VT + xcd * 128 + c * 8, 0, tag, vg);
          if (g) tbg = u32x2{vg[0], tag};
        }
        const bool ok = tbg[1] == tag;
        float lg = ok ? (float)(g ? (int)tbg[0] >> 16 : (int)(tbg[0] & 0xffffu)) : 0.f, nf = ok ? 8.f * (RVP - RVS + 1) : 0.f;
        lg += dpp<XOR1>(lg); nf += dpp<XOR1>(nf); lg += dpp<XOR2>(lg); nf += dpp<XOR2>(nf); lg += dpp<XOR7>(lg); nf += dpp<XOR7>(nf); lg += dpp<XOR8>(lg); nf += dpp<XOR8>(nf);
        lg *= __builtin_amdgcn_rcpf(fmaxf(nf, 1.f));
        if (lane == 16) sm.bc4 = __float_as_uint(lg);
        if (lane == 0) sm.bc3 = (u32)min(max((int)((lg - (float)P.F0) * (float)(P.FG * 32)), 0), 200 * 256);
      }
      LDS_BARRIER();
      const int st = __builtin_amdgcn_readfirstlane(sm.bc3), sp = (st * P.FF_) >> 3;
      near = ((__builtin_amdgcn_readfirstlane(sm.bc4) >> 31 ^ die ^ P.NR ^ 1) & 1) * 8192u;
      t1 += st;
      due += sp;
      tau += __builtin_amdgcn_readfirstlane((int)((float)(st - sp) * __builtin_amdgcn_rcpf((float)(nl - RVU - 3))));
    }
    if constexpr (v == SK1 + 1) {
      float p[8];
#pragma unroll
      for (int w = 0; w < 8; ++w) p[w] = sm.pl[g][w] * __expf(sm.ml[w][0] - m);
#pragma unroll
      for (int w = 0; w < 8; ++w) pl8[w] = pack_bf(p[w], p[w]);
#pragma unroll
      for (int w = 0; w < 8; ++w) PIN(pl8[w]);
    }
    if constexpr (v == MLS - 1) { side_pin(); if (is_up) up_prep(); PIN(ga); PIN(xin); PIN(cc1); }
    if constexpr (v == UPREP - 1) wo_addr();
    if constexpr (v == UPREP) {
#pragma unroll
      for (int k = 0; k < 4; ++k) asm volatile("" : "=v"(bw[k]));
      if (part == 0) wo_frags(0); else if (is_up) up_frags();
#pragma unroll
      for (int k = 0; k < 4; ++k) PIN(bw[k]);
    }
    if constexpr (v == UPREP + 1) {
      if (is_up) {
        const u32x4* a1 = reinterpret_cast<const u32x4*>(&sm.hb[1024 + (c >> 2 & 1) * 1024 + (c >> 3) * 512 + (g >> 1) * 256 + (c >> 1 & 1) * 32 + (g & 1) * 16 + (c & 1) * 8]);
        f32x4 d = {0.f, 0.f, 0.f, 0.f};
#pragma unroll
        for (int j = 0; j < 4; ++j) d = mfma64(a1[j * 8], bw[j], d, (c & 1) ? 0xeeeeu : 0x4444u);
        float t = par ? d[2] + d[3] : d[0] + d[1];
        t += dpp<XOR8>(t);
        const auto ab = __builtin_amdgcn_permlane16_swap(__float_as_uint(t), __float_as_uint(t), false, false);
        Af = u2f(ab[0]); Bf = u2f(ab[1]);
      }
      PIN(Af); PIN(Bf);
    }
    if (v == 10 || v == 16) STAMP(15 + v / 6);
    if (v == 20) STAMP(18);
    if (v == 32) STAMP(19);
    if constexpr (v == MLS) {
      LDS_BARRIER();
      float mm[8], ll[8];
#pragma unroll
      for (int w = 0; w < 8; ++w) { const f32x2 t = *reinterpret_cast<const f32x2*>(&sm.ml[w][0]); mm[w] = t[0]; ll[w] = t[1]; }
      float Mwg = mm[0], Lwg = 0.f;
#pragma unroll
      for (int w = 1; w < 8; ++w) Mwg = fmaxf(Mwg, mm[w]);
      if (wave == 0) {
#pragma unroll
        for (int w = 0; w < 8; ++w) Lwg = fmaf(__expf(mm[w] - Mwg), ll[w], Lwg);
        if (lane == 0) st16_sc1(wsr, G_ML + ((head * 2 + b) * 4 + part) * 16, __float_as_uint(Mwg), __float_as_uint(Lwg), tag);
      }
      const float fw = __expf(m - Mwg);
      m = Mwg;
#pragma unroll
      for (int bt = 0; bt < NB; ++bt) { pb[bt] = pack_bf(pr[bt] * fw, pr[bt] * fw); PIN(pb[bt]); }
#pragma unroll
      for (int k = 0; k < 4; ++k) { pm[k] = (c >> 1 & 3) == k ? ((c & 1) ? 0xffff0000u : 0x0000ffffu) : 0u; PIN(pm[k]); }
    }
    SB();
    return true;
  });
  asm volatile(".Ltail_start:");
  if (wave != 0) {
    pv_row(IC<SK0 - 1>{});
    WAIT_VM(WV); SB();
    sfor<NL - 1 - WV, NL - 1>(pv_row);
    *LDSP(f32x4, adst0) = acc;
    WSTAMP(14);
    LDS_BARRIER();
  } else {
    u32x4 xr[8], am[8];
#pragma unroll
    for (int w = 0; w < 8; ++w) am[w] = u32x4{pl8[w] & pm[0], pl8[w] & pm[1], pl8[w] & pm[2], pl8[w] & pm[3]};
#pragma unroll
    for (int w = 0; w < 8; ++w) PIN(am[w]);
    {
      const u32 so = __builtin_amdgcn_readlane(sot, NL - 1);
      gate();
      SB();
#pragma unroll
      for (int w = 0; w < 8; ++w) xr[w] = ld16<AUX_NT>(rv, lane * 16, so ^ ((w & 1) ? col(P, 7) : 0u) ^ ((w & 2) ? col(P, 8) : 0u) ^ ((w & 4) ? col(P, 9) : 0u));
      SB();
    }
    u32 rsum = lds_addr(&sm.red[0][c * 8 + (g & 1) * 4]);
    pv_row(IC<SK0 - 1>{});
    WAIT_VM(WV + 8); SB();
    sfor<NL - 1 - WV, NL - 3>(pv_row);
    if (__builtin_expect(!all_ok((mlg[1] == tag) & (mlg[3] == tag)), 0)) {
#pragma nounroll
      for (int spin = 0; spin < SPIN_MAX; ++spin) {
        CFENCE();
        mlg = ld16<AUX_SC1>(wsr, mlo, 0);
        if (all_ok((mlg[1] == tag) & (mlg[3] == tag))) break;
      }
    }
    merge_factor(mlg);
    sfor<NL - 3, NL - 1>(pv_row);
    STAMP(14); WSTAMP(14);
    LDS_BARRIER();
    STAMP(15);
    f32x4 t[8];
#pragma unroll
    for (int w = 1; w < 8; ++w) t[w] = LDSP(f32x4, rsum)[w * 32];
    acc = ((acc + t[1]) + (t[2] + t[3])) + ((t[4] + t[5]) + (t[6] + t[7]));
    PIN(acc);
    STAMP(3);
    SB();
#pragma unroll
    for (int w = 0; w < 8; ++w) acc = mfma16(am[w], xr[w], acc);
  }
  WAIT_VM(0);
  acc *= fmrg;
  {
    const u32 w0 = min(pack_bf(acc[0], acc[1]), SENT - 1u), w1 = min(pack_bf(acc[2], acc[3]), SENT - 1u);
    const auto e0 = __builtin_amdgcn_permlane16_swap(w0, w0, false, false), e1 = __builtin_amdgcn_permlane16_swap(w1, w1, false, false);
    __builtin_amdgcn_raw_buffer_store_b128(u32x4{e0[0], e1[0], e0[1], e1[1]}, wsr, pso, 0, AUX_SC1);
  }
  st8_sc1(wsr, nto, (u32)wall_clock64() + nadd, tag);
  SB();
  STAMP(4);

  if (part == 2) FINISH();
  if (part == 0) {
    __builtin_amdgcn_s_barrier();
#pragma nounroll
    while (wleft > 0) { until(t1 - (P.PA + wleft * P.PD) * 256); piece(); }
    bool late = false;
    u32x4 og[1];
    f32x4 d[4];
    u32 six = (c & 8) ? 0xeeeeu : 0x4444u;
    PIN(six);
#pragma unroll
    for (int t = 0; t < 4; ++t) { d[t] = f32x4{0.f, 0.f, 0.f, 0.f}; PIN(d[t]); }
    if (wdef) {
      late = true;
      while ((int)(((u32)wall_clock64() - t0) << 8) < t1 - 10 * 256) if (__builtin_amdgcn_s_getreg(7 | (3 << 11)) == 0) { late = false; break; }
      if (__builtin_expect(late, 0)) { until(t1); CFENCE(); og[0] = ld16<AUX_SC1>(wsr, po, 0); SB(); WAIT_VM(1); } else { WAIT_VM(0); }
      CFENCE();
      wo_frags(1);
#pragma unroll
      for (int k = 0; k < 4; ++k) PIN(bw[k]);
      SB();
    }
    if (__builtin_expect(!late, 1)) { until(t1); CFENCE(); og[0] = ld16<AUX_SC1>(wsr, po, 0); }
    STAMP(11);
    SB();
    auto wo_mm = [&]() {
#pragma unroll
      for (int t = 0; t < 4; ++t) d[t] = mfma64(og[0], bw[t], d[t], six);
    };
    auto p_bad = [&]() { return !all_ok(max(max(og[0][0], og[0][1]), max(og[0][2], og[0][3])) != SENT); };
    wo_mm();
    if (__builtin_expect(p_bad(), 0)) {
#pragma nounroll
      for (int spin = 0; spin < SPIN_MAX; ++spin) {
        __builtin_amdgcn_s_sleep(1);
        CFENCE();
        og[0] = ld16<AUX_SC1>(wsr, po, 0);
        if (!p_bad()) break;
      }
#pragma unroll
      for (int t = 0; t < 4; ++t) d[t] = f32x4{0.f, 0.f, 0.f, 0.f};
      wo_mm();
      SB();
      asm volatile(".Lend_d:");
    }
    STAMP(5); WSTAMP(5);
    {
      SB();
      f32x4 y; f32x2 h[4];
#pragma unroll
      for (int t = 0; t < 4; ++t) { h[t] = d[t].xy + d[t].zw; PIN(h[t]); }
#pragma unroll
      for (int t = 0; t < 4; ++t) { y[t] = h[t][0] + h[t][1]; PIN(y[t]); }
      *LDSP(f32x2, rdst) = f32x2{swap32_add(y[0], y[1]), swap32_add(y[2], y[3])};
    }
    STAMP(16);
    LDS_BARRIER();
    STAMP(17);
    if (wave < 2) {
      f32x2 v[4];
#pragma unroll
      for (int w = 0; w < 4; ++w) v[w] = f32x2{LDSP(float, rsrc)[w * 256], LDSP(float, rsrc)[w * 256 + 128]};
      const f32x2 h = (v[0] + v[1]) + (v[2] + v[3]);
      const float y = h[0] + h[1];
      __builtin_amdgcn_raw_buffer_store_b32(min(pack_bf(dpp<0xA0>(y), dpp<0xF5>(y)), SENT - 1u), wsr, xo, 0, AUX_SC1);
      asm volatile(".Lend_a:");
    }
    STAMP(6);
    FINISH();
  }
  if (part == 3) {
    if (!has_w) FINISH();
    u32x4 b2[4];
    {
      __builtin_amdgcn_s_barrier();
      const rsrc_t r2 = mk_rsrc(w2 + (size_t)(blk * 32 + wave * 4) * 512, 4096u);
      const u32 l2 = (u32)(uintptr_t)(&sm.w2b[wave * 4][0]);
#pragma unroll
      for (int n = 0; n < 4; ++n) { until(t1 - (P.QA + (4 - n) * P.QD) * 256); dma16(r2, (lane ^ (n * 2) ^ ((wave & 2) << 2)) * 16, n * 1024, l2 + n * 1024); }
    }
    const u32 tdst = lds_addr(&sm.hb[hpos]);
    const u32 sdst = lds_addr(&sm.stat[wave][lane]);
    const u32 ssrc = lds_addr(&sm.stat[(g >> 1) * 4 + ((lane >> 3) & 3)][(lane & 7) * 4]);
    const u32 adst = lds_addr((c < 4 && (g & 1) == 0) ? &sm.actl[g >> 1][wave * 4 + c] : reinterpret_cast<u16*>(&sm.dummy[lane]));
    const u32 asrc = lds_addr(&sm.actl[lane & 1][g * 8]);
    const u32 apa = lds_addr(&sm.hb[(c >> 3) * 512 + (g >> 1) * 256 + (c >> 1 & 1) * 64 + ((c >> 2) & 1) * 32 + (g & 1) * 16 + (c & 1) * 8]);
    u32 six = (c & 1) ? 0xeeeeu : 0x4444u;
    f32x4 d[2] = {{0.f, 0.f, 0.f, 0.f}, {0.f, 0.f, 0.f, 0.f}};
    PIN(six); PIN(d[0]); PIN(d[1]);
    constexpr u32 xs2 = 16384;
    u32 xoff = dup(G_XP) + near + g * 2048 + ub * 1024 + (wave & 3) * 256 + c * 16;
    u32x4 one;
#pragma unroll
    for (int k = 0; k < 4; ++k) one[k] = ((c & 2) == 0 && (c >> 2) == k) ? ((c & 1) ? 0x3f800000u : 0x00003f80u) : 0u;
    PIN(one);
    const int cw = wave ^ ((die ^ 1) << 2);
    u32 ooff = dup(G_OP + (blk * 512 + cw * 64 + lane) * 8) + (near ^ ((cw >> 2 ^ die) << 13));
    float Afp = Af, Bfp = Bf;
    PIN(xoff); PIN(ooff); PIN(Afp); PIN(Bfp);
    f32x2 gc = ga * cc1, xc = xin * cc1;
    PIN(ga); PIN(xin); PIN(gc); PIN(xc);
    u32 w2a = (u32)(uintptr_t)(&sm.w2b[0][0]) + (g * 8 + ((lane >> 2) & 3)) * 1024 + (cw ^ (g & 1)) * 128 + (lane & 15) * 8;
    PIN(w2a);
    if (wave == 1) {
      due = t1 + P.N1 * 256;
      const int tn = notices();
      due = tn + P.X2 * 256;
      until(due);
    }
    asm volatile("s_waitcnt vmcnt(0)" ::: "memory");
    STAMP(9);
    __builtin_amdgcn_s_barrier();
    STAMP(11);
    CFENCE();
    u32x4 xg[2] = {ld16<AUX_SC1>(wsr, xoff, 0), ld16<AUX_SC1>(wsr, xoff, xs2)};
    SB();
#pragma unroll
    for (int t = 0; t < 4; ++t) {
      const u32x2 lo = tr_read(w2a ^ (t * 32)), hi = tr_read((w2a ^ (t * 32)) + 4096);
      b2[t] = u32x4{lo[0], lo[1], hi[0], hi[1]};
    }
#pragma unroll
    for (int t = 0; t < 4; ++t) PIN(b2[t]);
    SB();
    const f32x4 z4 = {0.f, 0.f, 0.f, 0.f};
    auto xp_bad = [&]() { return !all_ok(max(max(max(xg[0][0], xg[0][1]), max(xg[0][2], xg[0][3])), max(max(xg[1][0], xg[1][1]), max(xg[1][2], xg[1][3]))) != SENT); };
    f32x4 v = mfma16(one, xg[1], mfma16(one, xg[0], z4));
    if (__builtin_expect(xp_bad(), 0)) {
#pragma nounroll
      for (int spin = 0; spin < SPIN_MAX; ++spin) {
        __builtin_amdgcn_s_sleep(1);
        CFENCE();
        xg[0] = ld16<AUX_SC1>(wsr, xoff, 0); xg[1] = ld16<AUX_SC1>(wsr, xoff, xs2);
        if (!xp_bad()) break;
      }
      v = mfma16(one, xg[1], mfma16(one, xg[0], z4));
      SB();
      asm volatile(".Lend_e:");
    }
    STAMP(5); WSTAMP(5);
    {
      const f32x2 a = v.xy, hc = __builtin_elementwise_fma(gc, a, xc), x1 = __builtin_elementwise_fma(ga, a, xin);
      *LDSP(u32, tdst) = pack_bf(hc[0], hc[1]);
      *LDSP(float, sdst) = swap32_add(x1[0] + x1[1], fmaf(x1[0], x1[0], x1[1] * x1[1]));
    }
    STAMP(16);
    LDS_BARRIER();
    STAMP(17);
    {
      const f32x4 s4 = LDSP(f32x4, ssrc)[0], q4 = LDSP(f32x4, ssrc)[8];
      SB();
      const u32x4 af0 = LDSP(u32x4, apa)[0], af1 = LDSP(u32x4, apa)[16];
      SB();
      const f32x2 s2 = s4.xy + s4.zw, q2 = q4.xy + q4.zw;
      float s = s2[0] + s2[1], q = q2[0] + q2[1];
      SB();
      d[0] = mfma64(af0, bw[0], d[0], six);
      SB();
      s = swap16_add(s, q);
      SB();
      d[1] = mfma64(af0, bw[1], d[1], six);
      SB();
      s += dpp<XOR1>(s); s += dpp<XOR2>(s);
      SB();
      d[0] = mfma64(af1, bw[2], d[0], six);
      SB();
      s += dpp<XOR7>(s); s += dpp<XOR8>(s);
      SB();
      d[1] = mfma64(af1, bw[3], d[1], six);
      SB();
      const auto sq = __builtin_amdgcn_permlane16_swap(__float_as_uint(s), __float_as_uint(s), false, false);
      const float mean = u2f(sq[0]) * (1.f / 512.f);
      const float var = fmaf(u2f(sq[1]), 1.f / 512.f, -mean * mean);
      float ri = __builtin_amdgcn_rsqf(fmaxf(var, 0.f) + 1e-5f);
      PIN(ri); PIN(d[0]); PIN(d[1]);
      SB();
      float y0 = d[0][0] + d[0][1], y1 = d[1][2] + d[1][3], mr = -ri * mean;
      PIN(y0); PIN(y1); PIN(mr);
      float y = y0 + y1, cf = fmaf(mr, Afp, Bfp), rk = ri * -1.44269504f;
      PIN(y); PIN(cf); PIN(rk);
      y = __builtin_bit_cast(float, __builtin_amdgcn_update_dpp(__builtin_bit_cast(int, y), __builtin_bit_cast(int, y), XOR8, 0xa, 0xf, false));
      const float ck = cf * -1.44269504f;
      y = swap16_add(y, y);
      const float ex = __builtin_amdgcn_exp2f(fmaf(y, rk, ck));
      y = fmaf(y, ri, cf);
      const float gu = y * dpp<0x104>(y);
      *LDSP(u16, adst) = (u16)pack_bf(gu * __builtin_amdgcn_rcpf(1.f + ex), 0.f);
    }
    STAMP(18);
    LDS_BARRIER();
    STAMP(10);
    {
      const u32x4 af = *LDSP(u32x4, asrc);
      const f32x4 z = {0.f, 0.f, 0.f, 0.f};
      f32x4 d0 = mfma16(af, b2[0], z), d1 = mfma16(af, b2[1], z), d2 = mfma16(af, b2[2], z), d3 = mfma16(af, b2[3], z);
      asm volatile("" : "+v"(d0), "+v"(d1), "+v"(d2), "+v"(d3));
      const bool g1 = g & 1, g2 = g & 2;
      const float e0 = g1 ? d1[0] : d0[0], e1 = g1 ? d1[1] : d0[1], f0 = g1 ? d3[0] : d2[0], f1 = g1 ? d3[1] : d2[1];
      const u32x2 ov = {pack_bf(g2 ? f0 : e0, g2 ? f1 : e1), tag};
      __builtin_amdgcn_raw_buffer_store_b64(ov, wsr, ooff, 0, AUX_SC1);
    }
    asm volatile(".Lend_b:");
    STAMP(6);
    FINISH();
  }
  if (wave == 0) {
    if (!all_ok((sg[0][1] == tag) & (sg[0][3] == tag) & (sg[1][1] == tag) & (sg[1][3] == tag))) {
      u32x4 t[2];
      gather16<2>(wsr, G_MOD + (1024 + fd) * 8, 1536 * 8, tag, t);
      sg[0] = t[0]; sg[1] = t[1];
    }
    u32 xa;
    {
      due = t1 + P.N2 * 256;
      const int tn = notices();
      due = tn + P.X3 * 256;
      until(due);
    }
    const bool hb = g & 1;
#pragma nounroll
    for (int spin = 0; spin < SPIN_MAX; ++spin) {
      CFENCE();
      xa = __builtin_amdgcn_raw_buffer_load_b32(wsr, dup(G_XP) + near + (g >> 1) * 16384 + (c >> 2) * 2048 + (g & 1) * 1024 + fd * 2, 0, AUX_SC1);
      if (all_ok(xa != SENT)) break;
      __builtin_amdgcn_s_sleep(1);
    }
    float a0 = lo_f(xa), a1 = hi_f(xa);
    a0 += dpp<0x124>(a0); a1 += dpp<0x124>(a1);
    a0 += dpp<XOR8>(a0); a1 += dpp<XOR8>(a1);
    a0 = swap32_add(a0, a0); a1 = swap32_add(a1, a1);
    f32x2 xv = {rbf(lo_f(sx2) + rbf((hb ? hi_f(sg[0][0]) : lo_f(sg[0][0])) * rbf(a0))), rbf(hi_f(sx2) + rbf((hb ? hi_f(sg[0][2]) : lo_f(sg[0][2])) * rbf(a1)))};
    f32x2 gv = {hb ? hi_f(sg[1][0]) : lo_f(sg[1][0]), hb ? hi_f(sg[1][2]) : lo_f(sg[1][2])};
    PIN(xv); PIN(gv);
    if (wi == 0 && lane == 0) __builtin_amdgcn_raw_buffer_store_b32(tag, wsr, WS_EPOCH, 0, AUX_SC1);
    STAMP(10);
    due = max(due + P.X4 * 256, (int)(((u32)wall_clock64() - t0) << 8) + P.GX * 256);
    const bool last = lane < (NUP - 32) * 4;
    u32 o0 = dup(G_OP + (lane >> 2) * 4096 + fd * 8) + near, o2 = last ? o0 : OOB, tg2 = last ? tag : 0u;
    const rsrc_t rout = mk_rsrc(out, 2048u);
    u32 oo = (lane & 44) == 0 ? (g * 512 + fd) * 2 : OOB;
    const u32 pk = (c & 10) ? 0u : ((c & 4) ? 0x3f800000u : 0x00003f80u);
    u32x4 one = {(c & 1) ? 0u : pk, 0u, (c & 1) ? pk : 0u, 0u};
    PIN(o0); PIN(o2); PIN(tg2); PIN(oo); PIN(one);
    __builtin_amdgcn_raw_buffer_store_b32(pack_bf(xv[0], xv[1]), rout, oo, 0, AUX_SC1);
    u32 s1_ = 131072, s2_ = 262144;
    asm volatile("" : "+s"(s1_), "+s"(s2_));
    until(due);
    f32x4 sv;
#pragma nounroll
    for (int spin = 0; spin < SPIN_MAX; ++spin) {
      CFENCE();
      const u32x4 og[3] = {ld16<AUX_SC1>(wsr, o0, 0), ld16<AUX_SC1>(wsr, o0, s1_), ld16<AUX_SC1>(wsr, o2, s2_)};
      sv = f32x4{0.f, 0.f, 0.f, 0.f};
#pragma unroll
      for (int k = 0; k < 3; ++k) sv = mfma16(one, og[k], sv);
      SB();
      u32 bad = ((og[0][1] ^ tag) | (og[0][3] ^ tag) | (og[1][1] ^ tag)) | ((og[1][3] ^ tag) | (og[2][1] ^ tg2) | (og[2][3] ^ tg2));
      PIN(bad);
      SB();
      float s0 = sv[0] + dpp<0x124>(sv[0]), s1 = sv[1] + dpp<0x124>(sv[1]);
      PIN(s0); PIN(s1);
      s0 += dpp<XOR8>(s0); s1 += dpp<XOR8>(s1);
      PIN(s0); PIN(s1);
      __builtin_amdgcn_raw_buffer_store_b32(pack_bf(fmaf(gv[0], s0, xv[0]), fmaf(gv[1], s1, xv[1])), rout, oo, 0, AUX_SC1);
      SB();
      if (all_ok(bad == 0u)) break;
      __builtin_amdgcn_s_sleep(1);
    }
    STAMP(7);
    asm volatile(".Lend_c:");
  }
  FINISH();
}

extern "C" void kernel_launch(void* const* d_in, const int* in_sizes, int n_in,
                              void* d_out, int out_size, void* d_ws, size_t ws_size,
                              hipStream_t stream) {
  fused_kernel<<<256, 512, 0, stream>>>((const u16*)d_in[1], (const u16*)d_in[2], (const u16*)d_in[4], (const u16*)d_in[3], (const u16*)d_in[10], (const u16*)d_in[6], (const u16*)d_in[7],
                                        (unsigned char*)d_ws, (const u16*)d_in[0], (const u16*)d_in[9], (const u16*)d_in[8], (u16*)d_out);
}
```

```cpp
#include <hip/hip_runtime.h>
#include <stdint.h>


typedef unsigned int u32;
typedef unsigned short u16;
typedef unsigned long long u64;
typedef __attribute__((ext_vector_type(4))) unsigned u32x4;
typedef __attribute__((ext_vector_type(2))) unsigned u32x2;
typedef __attribute__((ext_vector_type(2))) __bf16 bf16x2;
typedef __attribute__((ext_vector_type(4))) float f32x4;
typedef __attribute__((ext_vector_type(2))) float f32x2;
typedef __amdgpu_buffer_rsrc_t rsrc_t;

constexpr int NH = 32, SK = 16384, DH = 128, WIN = 4096, FF = 1376;
struct Prm { int E0, ES, E1, E2, E2C, EU, TEND, DT_UP, L0, L1, X1, X2, X3, X4, NE, FE, M0, M1, M2, M3, M4, GS, GX, N1, N2, GA, WD, DT_WO, LW, LU, EW, PM, NF, NR, C0, A0, AG, AC, TS, TW, TK, TG, GB, B0, BG, BF, D0, DG, TC, LM, C1, F0, FG, FF_, PA, PD, QA, QD, RT, SA, SG, SC, PV, W[9]; };
constexpr Prm PRM0 = {32, 20, 32, 32, 60, 60, 1973, 72, 12, 27, 72, 60, 96, 61, 3, 8, 0x2511f55b, 0x2b7425d5, 0x6017665, 0x17e0b9ec, 0x31944305, 60, 18, 26, 58, 65, 4, 100, 46, 28, 4, 0x0, 28, 14, 3, 36, 4, 4, 510, 208, 70, -2, 10, 67, 10, 8, 8, 6, 87, 120, 2, 62, 8, 4, 46, 14, 4, 18, 12, 45, 10, 21, 0, 0x77777777, 0x77777777, 0x77777777, 0x77777777, 0x77777777, 0x44444477, 0x44444444, 0x44444444, 0x44444444};
constexpr int NL = 37;
constexpr int SK0 = 21, SK1 = 32;
constexpr int NCMIN = (NL - SK1 + SK0) * 8;
constexpr int WK = 8, WV = 5;
constexpr int WUSE = 12, LNS = 7;
constexpr int QP = 14, QD = 4;
constexpr int VQ = 6;
constexpr int NSL = (NL + 7) / 8;
constexpr int VS = VQ + NSL;
constexpr int MLS = VS + 2;
constexpr int VSIDE = MLS - 5, UPREP = MLS + 6;
constexpr int CPF = 17;
constexpr int SMO = 1;
constexpr int RVS = 5, RVP = 8, RVL = 14, RVU = 18;
constexpr int RPS = 23, RPP = 27;
constexpr int PRB = 12;
constexpr int TBS = 7, TBP = 10, TBL = 13, TBU = 17;
constexpr int window(int s, int w) {
  int n = w;
  for (int u = s - w + 1; u <= s; ++u) n += u == SMO ? 6 : (u == TBL || u == SK1 || u == NL + CPF || u == NL + RVL || u == NL + SK1 ? 1 : (u == QP || u == NL + VQ - 4 ? 2 : (u == NL + VSIDE ? 4 : 0)));
  return n;
}
constexpr int cons_end(int v) {
  const int a = v - WV + 1, c = (v - MLS) * 4;
  const int m = a < c ? a : c;
  return m < 0 ? 0 : (m > NL ? NL : m);
}
constexpr u32 col(const Prm& P, int j) { const int m[6] = {P.M0, P.M1, P.M2, P.M3, P.M4, P.PM}; return (u32)(m[j / 3] >> (j % 3 * 10) & 1023) << 10; }
constexpr u32 first_col(const Prm& P, int k) {
  if (k < 4) return col(P, 12 + k) | 1u << (24 + k);
  if (k < 6) return col(P, k - 4);
  if (k < 8) return col(P, 4 + k) | 1u << (16 + k);
  if (k < 11) return col(P, k - 1);
  return (col(P, 2) ^ col(P, 3) ^ col(P, 4) ^ col(P, 5) ^ col(P, 6) ^ col(P, 10) ^ col(P, 11)) | 0xf00000u;
}
constexpr bool one_to_one(const Prm& P) {
  u32 c[10] = {};
  for (int j = 0; j < 10; ++j) c[j] = col(P, j);
  for (int j = 0; j < 10; ++j) {
    if (c[j] == 0) return false;
    const u32 low = c[j] & (0u - c[j]);
    for (int k = j + 1; k < 10; ++k) if (c[k] & low) c[k] ^= c[j];
  }
  return true;
}
static_assert(one_to_one(PRM0), "every KB of the window exactly once");
struct Tab { u32 e[512]; };
constexpr Tab make_tab(const Prm& P) {
  Tab T{};
  for (int bid = 0; bid < 256; ++bid) {
    const int xcd = bid & 7, part = bid >> 4 & 3, i8 = (bid >> 3 & 1) | bid >> 6 << 1, wi = i8 * 8 + xcd;
    const bool has_mod = wi < 48, has_up = wi >= 21;
    const int cb0 = (i8 + P.C0) & 7, cb1 = (i8 + P.C1) & 7, ms = wi - 20;
    const int mi = wi < 8 ? wi : (wi < 16 ? 8 + ((xcd + 4) & 7) : (wi < 20 ? (wi - 14) * 8 + 3 : 16 + ms / 7 * 8 + ms % 7 + (ms % 7 >= 3 ? 1 : 0)));
    const int ui = xcd * 5 + (xcd > 5 ? xcd - 5 : 0) + (i8 == 6 ? 0 : (xcd >= 5 ? 1 : 0) + (i8 == 7 ? 4 : i8 - 2));
    const bool edge = i8 >= 6;
    const int wd = cb0 == 3 ? 0 : P.WD;
    const int e[4] = {P.E0 - 4 * wd + (cb0 == 3 ? P.ES : 0), P.E1 + (cb1 == 3 ? P.ES : 0), has_mod ? (wi < 16 ? P.E2C : P.E2) + ((mi & 7) == 3 ? P.ES : 0) : 0, has_up ? P.EU : 0};
    int tw[4] = {}, win[4] = {}, span[4] = {}, k0[4] = {};
    for (int k = 0; k < 4; ++k) {
      const int np = k == 0 ? 8 - wd : 8;
      tw[k] = np ? e[k] * P.TW / (16 * np) : 0;
      k0[k] = P.NF + tw[k] * np / 2 > P.TK ? P.NF + tw[k] * np / 2 : P.TK;
      span[k] = P.TEND - k0[k] - P.TG - (k == 0 ? (wd ? P.DT_WO : P.A0) : (k == 3 && has_up ? P.DT_UP : 0));
      win[k] = span[k] - (k == 3 && has_up ? P.GA + (edge ? P.LM : 0) : 0);
    }
    int n[4] = {NCMIN / 8, NCMIN / 8, NCMIN / 8, NCMIN / 8};
    for (int left = 128 - NCMIN / 2; left > 0; --left) {
      int best = -1;
      for (int k = 3; k >= 0; --k)
        if (n[k] < NL && (best < 0 || n[k] * win[best] < n[best] * win[k])) best = k;
      ++n[best];
    }
    const int c[4] = {0, n[0] - 1, n[0] + n[1] - 2, n[0] + n[1] + n[2] - 3};
    const int nl = n[part];
    const bool has_w = part < 2 || (part == 2 ? has_mod : has_up);
    const int blk = !has_w ? 0 : (part == 0 ? cb0 : (part == 1 ? xcd * 8 + cb1 : (part == 2 ? mi : ui)));
    T.e[2 * bid] = (u32)(c[part] | nl << 7 | blk << 13 | (has_w ? 1 : 0) << 19 | (part == 0 ? wd : 0) << 20) | (u32)tw[part] << 24;
    T.e[2 * bid + 1] = (u32)(span[part] * P.TS / (4 * (nl - 1)) | span[part] * (1024 - P.TS) / (4 * (nl - 1)) << 16);
  }
  return T;
}
constexpr int step_range(const Prm& P, bool mx_) {
  int mx = 0, mn = 1 << 20;
  const Tab T = make_tab(P);
  for (int bid = 0; bid < 256; ++bid) { const int n = T.e[2 * bid] >> 7 & 63; mx = n > mx ? n : mx; mn = n < mn ? n : mn; }
  return mx_ ? mx : mn;
}
extern "C" __attribute__((used, visibility("protected"))) __constant__ const Tab wg_tab = make_tab(PRM0);
static_assert(step_range(PRM0, true) <= NL && step_range(PRM0, false) >= NCMIN / 8, "a wave's key range does not fit its load steps");
static_assert(RVU < UPREP && UPREP + 1 < SK0 && QP + QD + 1 < SK0 && cons_end(SK0 - 1) == SK0 - WV && NL - SK1 == WV, "steps every wave runs");
constexpr bool timers_long(const Prm& P) {
  const Tab T = make_tab(P);
  for (int bid = 48; bid < 64; ++bid) if (SK0 + NL - (int)(T.e[2 * bid] >> 7 & 63) > RPS) return false;
  return true;
}
static_assert(timers_long(PRM0), "steps of the second report");
static_assert(PRM0.WD <= 4 && PRM0.NE <= NL - SK1, "late pieces: the second half of the block at most");
constexpr int NUP = FF / 32;
constexpr int SPIN_MAX = 1 << 17;

constexpr u32 WS_EPOCH = 0;
constexpr u32 G_TB = 1024;
constexpr u32 G_MOD = 4096;
constexpr u32 G_Q = G_MOD + 3072 * 8;
constexpr u32 G_ML = G_Q + 4096 * 8;
constexpr u32 G_U = G_ML + 256 * 16;
constexpr u32 G_P = 0;
constexpr u32 G_XP = G_P + 128 * 256;
constexpr u32 G_OP = G_XP + 8 * 512 * 4;
constexpr u32 G_NT = G_OP + 43 * 512 * 8;
constexpr u32 G_PB = G_OP + 44 * 512 * 8;
constexpr u32 G_VT = G_U + 2 * (G_PB + 8192);
constexpr u32 WS_END = G_VT + 2048;
__device__ __forceinline__ u32 dup(u32 o) { return G_U + ((o >> 13) << 14) + (o & 8191u); }
constexpr u32 SENT = 0xffffffffu;
constexpr u32 OOB = 0x40000000u;

constexpr int AUX_NT = 2, AUX_SC1 = 16;

__device__ __forceinline__ float lo_f(u32 v) { return __uint_as_float(v << 16); }
__device__ __forceinline__ float hi_f(u32 v) { return __uint_as_float(v & 0xffff0000u); }
__device__ __forceinline__ float rbf(float x) { return (float)(__bf16)x; }
__device__ __forceinline__ u32 pack_bf(float lo, float hi) {
  bf16x2 r; r[0] = (__bf16)lo; r[1] = (__bf16)hi; return __builtin_bit_cast(u32, r);
}
__device__ __forceinline__ float dot2(u32 k, u32 q, float acc) {
  return __builtin_amdgcn_fdot2_f32_bf16(__builtin_bit_cast(bf16x2, k), __builtin_bit_cast(bf16x2, q), acc, false);
}
template <int CTRL> __device__ __forceinline__ float dpp(float x) {
  return __builtin_bit_cast(float, __builtin_amdgcn_mov_dpp(__builtin_bit_cast(int, x), CTRL, 0xf, 0xf, true));
}
constexpr int XOR1 = 0xB1, XOR2 = 0x4E, XOR7 = 0x141, XOR8 = 0x128;
__device__ __forceinline__ float swap32_add(float a, float b) {
  auto t = __builtin_amdgcn_permlane32_swap(__float_as_uint(a), __float_as_uint(b), false, false);
  return __uint_as_float(t[0]) + __uint_as_float(t[1]);
}
__device__ __forceinline__ float swap16_add(float a, float b) {
  auto t = __builtin_amdgcn_permlane16_swap(__float_as_uint(a), __float_as_uint(b), false, false);
  return __uint_as_float(t[0]) + __uint_as_float(t[1]);
}
__device__ __forceinline__ float wave_sum(float x) {
  x += dpp<XOR1>(x); x += dpp<XOR2>(x); x += dpp<XOR7>(x); x += dpp<XOR8>(x);
  x = swap16_add(x, x);
  return swap32_add(x, x);
}
__device__ __forceinline__ float wave_max(float x) {
  x = fmaxf(x, dpp<XOR1>(x)); x = fmaxf(x, dpp<XOR2>(x)); x = fmaxf(x, dpp<XOR7>(x)); x = fmaxf(x, dpp<XOR8>(x));
  auto s = __builtin_amdgcn_permlane16_swap(__float_as_uint(x), __float_as_uint(x), false, false);
  x = fmaxf(__uint_as_float(s[0]), __uint_as_float(s[1]));
  auto t = __builtin_amdgcn_permlane32_swap(__float_as_uint(x), __float_as_uint(x), false, false);
  return fmaxf(__uint_as_float(t[0]), __uint_as_float(t[1]));
}
template <int CTRL> __device__ __forceinline__ float sel_add(float lo, float hi, bool up) {
  const float t1 = lo + dpp<CTRL>(lo), t2 = hi + dpp<CTRL>(hi);
  return up ? t2 : t1;
}
template <int I> __device__ __forceinline__ float bc16(float x) {
  return __builtin_bit_cast(float, __builtin_amdgcn_update_dpp(0, __builtin_bit_cast(int, x), 0x150 + I, 0xf, 0xf, false));
}
__device__ __forceinline__ float bcast16(float x, int i) {
  switch (i) {
    case 0: return bc16<0>(x); case 1: return bc16<1>(x); case 2: return bc16<2>(x); case 3: return bc16<3>(x);
    case 4: return bc16<4>(x); case 5: return bc16<5>(x); case 6: return bc16<6>(x); case 7: return bc16<7>(x);
    case 8: return bc16<8>(x); case 9: return bc16<9>(x); case 10: return bc16<10>(x); case 11: return bc16<11>(x);
    case 12: return bc16<12>(x); case 13: return bc16<13>(x); case 14: return bc16<14>(x); default: return bc16<15>(x);
  }
}
__device__ __forceinline__ float silu_f(float x) { return x * __builtin_amdgcn_rcpf(1.f + __expf(-x)); }

template <int AUX> __device__ __forceinline__ u32x4 ld16(rsrc_t r, u32 voff, u32 soff) {
  return __builtin_amdgcn_raw_buffer_load_b128(r, voff, soff, AUX);
}
template <int AUX> __device__ __forceinline__ u32x2 ld8(rsrc_t r, u32 voff, u32 soff) {
  return __builtin_amdgcn_raw_buffer_load_b64(r, voff, soff, AUX);
}
__device__ __forceinline__ void st8_sc1(rsrc_t r, u32 voff, u32 v, u32 tag) {
  __builtin_amdgcn_raw_buffer_store_b64(u32x2{v, tag}, r, voff, 0, AUX_SC1);
}
__device__ __forceinline__ void st16_sc1(rsrc_t r, u32 voff, u32 v0, u32 v1, u32 tag) {
  __builtin_amdgcn_raw_buffer_store_b128(u32x4{v0, tag, v1, tag}, r, voff, 0, AUX_SC1);
}
__device__ __forceinline__ rsrc_t mk_rsrc(const void* p, u32 bytes) {
  return __builtin_amdgcn_make_buffer_rsrc(const_cast<void*>(p), 0, bytes, 0x00020000);
}
#define WAIT_VM(n) __builtin_amdgcn_s_waitcnt(((n) & 15) | (((n) >> 4) << 14) | (7 << 4) | (15 << 8))
#define SB() __builtin_amdgcn_sched_barrier(0)
#define CFENCE() asm volatile("" ::: "memory")
__device__ __forceinline__ bool all_ok(bool ok) { return __builtin_amdgcn_ballot_w64(!ok) == 0ull; }

template <int N> __device__ __forceinline__ void gather16(rsrc_t ws, u32 voff, u32 stride, u32 tag, u32x4 (&g)[N]) {
#pragma nounroll
  for (int spin = 0; spin < SPIN_MAX; ++spin) {
    CFENCE();
    bool ok = true;
#pragma unroll
    for (int i = 0; i < N; ++i) g[i] = ld16<AUX_SC1>(ws, voff + i * stride, 0);
#pragma unroll
    for (int i = 0; i < N; ++i) ok &= (g[i][1] == tag) & (g[i][3] == tag);
    if (all_ok(ok)) break;
    __builtin_amdgcn_s_sleep(8);
  }
}
template <int N> __device__ __forceinline__ void gather8(rsrc_t ws, u32 voff, u32 stride, u32 tag, u32 (&g)[N], bool act = true) {
#pragma nounroll
  for (int spin = 0; spin < SPIN_MAX; ++spin) {
    CFENCE();
    bool ok = true;
    u32x2 t[N];
#pragma unroll
    for (int i = 0; i < N; ++i) t[i] = ld8<AUX_SC1>(ws, voff + i * stride, 0);
#pragma unroll
    for (int i = 0; i < N; ++i) { ok &= (t[i][1] == tag); g[i] = t[i][0]; }
    if (all_ok(ok | !act)) break;
    __builtin_amdgcn_s_sleep(8);
  }
}

struct Smem {
  float red[8][128];
  float ml[8][2];
  float r2[64 * 20];
  u16 hb[3 * 1024];
  float stat[2][64];
  float red2[8][128];
  u32 dummy[64];
  u32x2 bc, bc2;
  u32 bc3, bc4;
  u32x4 zero[4];
  float pl[4][8];
  u32 mflag[8];
  u32x4 wbuf[8][8][64];
  __attribute__((aligned(256))) u16 w2b[32][512];
  u16 actl[2][32];
};

#define PIN(x) asm volatile("" : "+v"(x))
#define LDS_BARRIER() do { __builtin_amdgcn_s_waitcnt(0xc07f); __builtin_amdgcn_s_barrier(); CFENCE(); } while (0)
#define WAVE_SYNC() do { __builtin_amdgcn_fence(__ATOMIC_RELEASE, "wavefront"); __builtin_amdgcn_wave_barrier(); __builtin_amdgcn_fence(__ATOMIC_ACQUIRE, "wavefront"); } while (0)

__device__ __forceinline__ void dma16(rsrc_t r, u32 voff, u32 soff, u32 lds_dst) {
  u32 keep;
  asm volatile("s_mov_b32 %0, m0\n\ts_mov_b32 m0, %4\n\ts_nop 4\n\tbuffer_load_dwordx4 %1, %2, %3 offen nt lds\n\ts_mov_b32 m0, %0"
               : "=&s"(keep) : "v"(voff), "s"(r), "s"(soff), "s"(lds_dst) : "memory");
}
__device__ __forceinline__ void ld16_into(u32x4& d, rsrc_t r, u32 voff, u32 soff) {
  asm volatile("s_nop 4\n\tbuffer_load_dwordx4 %0, %1, %2, %3 offen nt" : "+v"(d) : "v"(voff), "s"(r), "s"(soff) : "memory");
}
__device__ __forceinline__ u32 perm_lo(u32 a, u32 b) { return __builtin_amdgcn_perm(b, a, 0x05040100u); }
__device__ __forceinline__ u32 perm_hi(u32 a, u32 b) { return __builtin_amdgcn_perm(b, a, 0x07060302u); }
__device__ __forceinline__ float rdl(float x, int l) { return __int_as_float(__builtin_amdgcn_readlane(__float_as_int(x), l)); }

typedef __attribute__((ext_vector_type(8))) __bf16 bf16x8;
__device__ __forceinline__ f32x4 mfma16(u32x4 a, u32x4 b, f32x4 c) {
  return __builtin_amdgcn_mfma_f32_16x16x32_bf16(__builtin_bit_cast(bf16x8, a), __builtin_bit_cast(bf16x8, b), c, 0, 0, 0);
}
typedef __attribute__((ext_vector_type(8))) unsigned u32x8;
typedef __attribute__((ext_vector_type(16))) __bf16 bf16x16;
__device__ __forceinline__ f32x4 mfma64(u32x4 a, u32x8 b, f32x4 c, u32 idx) {
  return __builtin_amdgcn_smfmac_f32_16x16x64_bf16(__builtin_bit_cast(bf16x8, a), __builtin_bit_cast(bf16x16, b), c, idx, 0, 0);
}
__device__ __forceinline__ float sum8(const float* p) {
  const f32x4 a = *reinterpret_cast<const f32x4*>(p), b = *reinterpret_cast<const f32x4*>(p + 4);
  return ((a[0] + b[0]) + (a[1] + b[1])) + ((a[2] + b[2]) + (a[3] + b[3]));
}
__device__ __forceinline__ float ln_ri(float var) {
  return __builtin_amdgcn_rcpf(rbf(__builtin_amdgcn_sqrtf(rbf(rbf(fmaxf(var, 0.f)) + 1e-5f))));
}
__device__ __forceinline__ void ln_stats2(const float (&x0)[8], const float (&x1)[8], float& mb0, float& ri0, float& mb1, float& ri1) {
  const float k0 = rdl(x0[0], 0), k1 = rdl(x1[0], 0);
  float s0 = 0.f, s1 = 0.f, q0 = 0.f, q1 = 0.f;
#pragma unroll
  for (int k = 0; k < 8; ++k) {
    const float d0 = x0[k] - k0, d1 = x1[k] - k1;
    s0 += d0; s1 += d1; q0 = fmaf(d0, d0, q0); q1 = fmaf(d1, d1, q1);
  }
  float t = swap16_add(swap32_add(s0, s1), swap32_add(q0, q1));
  t += dpp<XOR1>(t); t += dpp<XOR2>(t); t += dpp<XOR7>(t); t += dpp<XOR8>(t);
  auto u = __builtin_amdgcn_permlane16_swap(__float_as_uint(t), __float_as_uint(t), false, false);
  const float ms = __uint_as_float(u[0]) * (1.f / 512.f), mq = __uint_as_float(u[1]) * (1.f / 512.f);
  const float ri = ln_ri(fmaf(-ms, ms, mq));
  mb0 = rbf(k0 + rdl(ms, 0)); mb1 = rbf(k1 + rdl(ms, 32));
  ri0 = rdl(ri, 0); ri1 = rdl(ri, 32);
}
template <int I> struct IC { static constexpr int v = I; };
template <int I, int N, class F> __device__ __forceinline__ void sfor(F&& f) {
  if constexpr (I < N) { if (f(IC<I>{})) sfor<I + 1, N>(f); }
}
typedef __attribute__((ext_vector_type(4))) short s16x4;
typedef __attribute__((address_space(3))) s16x4* lds_s16x4p;
__device__ __forceinline__ u32x2 tr_read(u32 lds_addr) {
  return __builtin_bit_cast(u32x2, __builtin_amdgcn_ds_read_tr16_b64_v4i16((lds_s16x4p)lds_addr));
}
__device__ __forceinline__ u32 next_tag(u32 e) { return ((e | 0x40004000u) + 1u) & ~0x40004000u; }
__device__ __forceinline__ float u2f(u32 v) { return __uint_as_float(v); }
#define LDSP(T, a) ((__attribute__((address_space(3))) T*)(a))
template <class T> __device__ __forceinline__ u32 lds_addr(T* p) { u32 a = (u32)(uintptr_t)p; PIN(a); return a; }

#define TAB_LOAD "s_add_u32 s30, s28, wg_tab@rel32@lo+4\ns_addc_u32 s31, s29, wg_tab@rel32@hi+12\nglobal_load_dwordx2 %[te], %[va], s[30:31]\n"
#define FC(k) ".Lfc" #k
#define FX(r, bit, k) "s_bitcmp1_b32 %[" #r "], " #bit "\ns_cselect_b32 %[sb], " FC(k) ", 0\ns_xor_b32 %[sa], %[sa], %[sb]\n"
__global__ void __launch_bounds__(512, 2) fused_kernel(const u16* kc_, const u16* vc_, const u16* wo_, const u16* wq_, const u16* wmod_, const u16* w1_, const u16* w3_,
                                                       unsigned char* ws_, const u16* x_, const u16* cnd_, const u16* w2_, u16* out_) {
  __shared__ __attribute__((aligned(256))) Smem sm;
  const u64 a_ka = (u64)__builtin_amdgcn_kernarg_segment_ptr();
  const int bid = blockIdx.x;
  int tid = threadIdx.x;
  u64 wa, t64; u32x2 te; int tid0;
  u32 ar[14];
  u32 tch2, sa_, sb_, va_;
  u32x4 kr[NL], vr[NL];
  asm volatile(".set .Lfc0, %0\n.set .Lfc1, %1\n.set .Lfc2, %2\n.set .Lfc3, %3\n.set .Lfc4, %4\n.set .Lfc5, %5\n.set .Lfc6, %6\n.set .Lfc7, %7\n.set .Lfc8, %8\n.set .Lfc9, %9\n.set .Lfc10, %10\n.set .Lfc11, %11"
    :: "n"(first_col(PRM0, 0)), "n"(first_col(PRM0, 1)), "n"(first_col(PRM0, 2)), "n"(first_col(PRM0, 3)), "n"(first_col(PRM0, 4)), "n"(first_col(PRM0, 5)),
       "n"(first_col(PRM0, 6)), "n"(first_col(PRM0, 7)), "n"(first_col(PRM0, 8)), "n"(first_col(PRM0, 9)), "n"(first_col(PRM0, 10)), "n"(first_col(PRM0, 11)));
  asm volatile(
    "s_load_dwordx16 s[4:19], %[ka], 0x0\ns_load_dwordx8 s[20:27], %[ka], 0x40\n"
    "s_memrealtime %[t]\n"
    "v_lshlrev_b32 %[va], 3, %[bid]\n"
    "s_getpc_b64 s[28:29]\n.Lcpf_first:\n"
    TAB_LOAD
    "s_cmp_gt_u32 %[bid], 15\ns_cbranch_scc1 .Lno_cpf\n"
    "s_mov_b32 %[sa], .Lend_a-.Lcpf_first\ns_max_i32 %[sa], %[sa], .Lend_b-.Lcpf_first\ns_max_i32 %[sa], %[sa], .Lend_c-.Lcpf_first\ns_max_i32 %[sa], %[sa], .Lend_d-.Lcpf_first\ns_max_i32 %[sa], %[sa], .Lend_e-.Lcpf_first\n"
    "v_lshlrev_b32 %[va], 6, %[tid]\nv_min_u32 %[va], %[sa], %[va]\nglobal_load_dword %[pf], %[va], s[28:29]\n"
    ".Lno_cpf:\n"
    "v_readfirstlane_b32 %[t0], %[tid]\nv_and_b32 %[va], 63, %[tid]\n"
    "s_mov_b32 %[sa], " FC(11) "\n"
    FX(bid, 0, 0) FX(bid, 1, 1) FX(bid, 2, 2) FX(bid, 3, 3) FX(bid, 4, 4) FX(bid, 5, 5) FX(bid, 6, 6) FX(bid, 7, 7) FX(t0, 6, 8) FX(t0, 7, 9) FX(t0, 8, 10)
    "v_lshl_add_u32 %[va], %[va], 4, %[sa]\n"
    "s_waitcnt lgkmcnt(0)\nglobal_load_dwordx4 %[k0], %[va], s[4:5] nt\nglobal_load_dwordx4 %[v0], %[va], s[6:7] nt\n"
    "s_bitcmp1_b32 %[t0], 6\ns_cselect_b64 s[14:15], s[16:17], s[14:15]\n"
    "s_bitcmp1_b32 %[bid], 4\ns_cselect_b64 s[8:9], s[10:11], s[8:9]\ns_cselect_b64 s[12:13], s[14:15], s[12:13]\ns_bitcmp1_b32 %[bid], 5\ns_cselect_b64 %[wa], s[12:13], s[8:9]"
    : "=&{s4}"(ar[0]), "=&{s5}"(ar[1]), "=&{s6}"(ar[2]), "=&{s7}"(ar[3]), "=&{s18}"(ar[4]), "=&{s19}"(ar[5]), "=&{s20}"(ar[6]), "=&{s21}"(ar[7]), "=&{s22}"(ar[8]), "=&{s23}"(ar[9]),
      "=&{s24}"(ar[10]), "=&{s25}"(ar[11]), "=&{s26}"(ar[12]), "=&{s27}"(ar[13]), [wa] "=&s"(wa), [t0] "=&s"(tid0), [t] "=&s"(t64), [te] "=&v"(te), [sa] "=&s"(sa_), [sb] "=&s"(sb_),
      [va] "=&v"(va_), [pf] "=&v"(tch2), [k0] "=&v"(kr[0]), [v0] "=&v"(vr[0])
    : [ka] "s"(a_ka), [bid] "s"(bid), [tid] "v"(tid) : "s8", "s9", "s10", "s11", "s12", "s13", "s14", "s15", "s16", "s17", "s28", "s29", "s30", "s31", "scc", "memory");
  asm volatile("" : "+s"(ar[0]), "+s"(ar[1]), "+s"(ar[2]), "+s"(ar[3]), "+s"(ar[4]), "+s"(ar[5]), "+s"(ar[6]), "+s"(ar[7]), "+s"(ar[8]), "+s"(ar[9]), "+s"(ar[10]), "+s"(ar[11]), "+s"(ar[12]), "+s"(ar[13]));
  asm volatile("" : "+s"(wa), "+s"(t64), "+s"(tid0), "+s"(sa_));
  const u32 tka = (u32)wall_clock64();
  __builtin_assume(tid >= 0 && tid < 512 && tid0 >= 0 && tid0 < 512 && bid >= 0 && bid < 256);
  constexpr Prm P = PRM0;
  const u32 t0 = tka + min(max((int)(tka - (u32)t64) - P.SA, 0) * P.SG >> 4, P.SC);
  const int TEND = P.TEND;
  const int xcd = bid & 7, part = bid >> 4 & 3;
  int lane = tid & 63;
  const int wave = tid0 >> 6;
  SB();
  if ((bid >> 4) == 2) {
    const u32 o = tid < 384 ? G_U + (bid & 15) * 6144 + tid * 16 : OOB;
    __builtin_amdgcn_raw_buffer_store_b128(u32x4{SENT, SENT, SENT, SENT}, mk_rsrc((void*)(ar[4] | (u64)ar[5] << 32), WS_END), o, 0, AUX_SC1);
  }
  SB();
  const bool p3 = part == 3;
  u32 pitch = (u32)(0x005600c001000020ull >> (part * 16) & 0xffff) * 32u;
  const int sh = p3 ? 2 : 3;
  u32 wv0 = (u32)((p3 ? (wave >> 1) * 128 : wave * 64) + (part == 0 ? xcd * 512 : 0) + (lane >> sh)) * pitch + ((lane & (p3 ? 3 : 7)) ^ (lane >> (p3 ? 5 : 3))) * 16u;
  const u32 wimg = (u32)(uintptr_t)(&sm.wbuf[0][0][0]);
  u32 wsz = (part == 0 ? 4096u : 512u) * pitch, wld = wimg + (p3 ? (wave & 1) * 32768 + (wave >> 1) * 8192 : wave * 8192);
  PIN(wv0);
  asm volatile("" : "+s"(wsz), "+s"(wld), "+s"(pitch));
  auto karg = [&](int i) { return ar[2 * i] | (u64)ar[2 * i + 1] << 32; };
  const u64 a_kc = karg(0), a_vc = karg(1);
  unsigned char* ws = (unsigned char*)karg(2);
  const u16 *x = (const u16*)karg(3), *cnd = (const u16*)karg(4), *w2 = (const u16*)karg(5);
  u16* out = (u16*)karg(6);
  const rsrc_t wsr = mk_rsrc(ws, WS_END);
  asm volatile("s_waitcnt vmcnt(3)\n\ts_cmp_eq_u32 %1, 2\n\ts_cbranch_scc1 1f\n\ts_waitcnt vmcnt(2)\n1:" : "+v"(te) : "s"(bid >> 4) : "scc");
  const u32 te0 = __builtin_amdgcn_readfirstlane(te[0]), te1 = __builtin_amdgcn_readfirstlane(te[1]);
  const int bl0 = te0 & 127, nl = te0 >> 7 & 63, blk = te0 >> 13 & 63;
  const bool has_w = te0 >> 19 & 1;
  const rsrc_t rw = mk_rsrc((const void*)wa, wsz);
  const u32 wstep = pitch << (6 - sh), wflip = (p3 || part == 0) ? 32u : 0u, wflip0 = part == 0 ? 32u : 0u;
  u32 wso = blk << (sh + 4);
  int wleft = 8;
  auto piece = [&]() {
    dma16(rw, wv0, wso, wld);
    wso += wstep; wld += 1024;
    wv0 ^= (wleft & 1) ? wflip : wflip0;
    --wleft;
  };
  SB();
  const int b = bid >> 3 & 1, head = xcd * 4 + (bid >> 6 ^ 3);
  int g = lane >> 4, c = lane & 15;
  const int i8 = b | bid >> 6 << 1, wi = i8 * 8 + xcd;
  const bool is_up = p3 && has_w, modc = part == 2 && wi < 16;
  const size_t base = ((size_t)(b * NH + head) * SK + (SK - WIN)) * DH;
  const rsrc_t rk = mk_rsrc((const u16*)a_kc + base, WIN * DH * 2), rv = mk_rsrc((const u16*)a_vc + base, WIN * DH * 2);
  int tau = te1 & 0xffff;
  const int tauw = te0 >> 24 << 7, wdef = te0 >> 20 & 15;
  int due = P.NF * 256, now = 0;
  auto gate = [&]() {
    while (now < due) { now = (int)(((u32)wall_clock64() - t0) << 8); if (now < due) __builtin_amdgcn_s_sleep(1); }
    due += tau;
  };
  auto until = [&](int t) { while ((int)(((u32)wall_clock64() - t0) << 8) < t); };
  const u32 pslot = dup(G_P + (head & 15) * 2048 + (b * 4 + part) * 16);
  const u32 die = xcd >> 2;
  const bool voter = p3 && i8 == 1, timer = p3 && i8 < 2;
  int vote = 0, lag = 0, shift = 0;
  const u32 pbo = dup(G_PB) + wave * 1152;
  u32 near = 0;
  u32x2 ep; u32 xs0, xs1, tch1; u32x4 xr0, xr1;
  const rsrc_t rx = mk_rsrc(part == 2 ? cnd : x, (part == 1 || (part == 2 && has_w)) ? 2048u : 0u);
  auto small_ops = [&]() {
    ep = ld8<AUX_SC1>(wsr, wave == 7 ? WS_EPOCH : OOB, 0);
    xs0 = __builtin_amdgcn_raw_buffer_load_b16(rx, tid * 2, 0, 0); xs1 = __builtin_amdgcn_raw_buffer_load_b16(rx, tid * 2, 1024, 0);
    xr0 = ld16<0>(rx, part == 1 ? lane * 16 : OOB, 0); xr1 = ld16<0>(rx, part == 1 ? lane * 16 : OOB, 1024);
    tch1 = __builtin_amdgcn_raw_buffer_load_b32(mk_rsrc(w2 + (size_t)blk * 32 * 512, is_up ? 64u : 0u), tid == 384 ? 0 : OOB, 0, AUX_NT);
  };
  if (has_w) {
    if (modc) now = 1 << 30;
#pragma nounroll
    while (wleft > wdef) { gate(); due += tauw - tau; piece(); }
    now = 0;
  }
  due = max(due, P.TK * 256);
  SB();
  u32 sot = sa_ & 0xffc00u;
  {
    const u32 s1 = (bl0 + lane - 1 - (lane >= SK0 ? NL - nl : 0)) ^ (124 + part);
#pragma unroll
    for (int k = 0; k < 7; ++k) sot ^= (u32)-(int)(s1 >> k & 1u) & col(P, k);
    PIN(sot);
  }
  u32 tag = 0;
  u32x4 bf[8];
  auto wo_prep = [&]() {
    const u32 wi_ = wimg + wave * 8192;
    const int q = (lane >> 2) & 3, pp = lane & 3;
#pragma unroll
    for (int s = 0; s < 2; ++s) {
#pragma unroll
      for (int t = 0; t < 4; ++t) {
        const int r0 = s * 32 + g * 8 + q, r1 = r0 + 4, ch = t * 2 + (pp >> 1);
        const u32x2 lo = tr_read(wi_ + r0 * 128 + ((ch ^ (r0 & 7)) * 16) + (pp & 1) * 8), hi = tr_read(wi_ + r1 * 128 + ((ch ^ (r1 & 7)) * 16) + (pp & 1) * 8);
        bf[s * 4 + t] = u32x4{lo[0], lo[1], hi[0], hi[1]};
      }
    }
  };
  auto gemv_go = [&](u32 hh) {
    sm.hb[tid] = (u16)hh; sm.hb[512 + tid] = (u16)(hh >> 16);
    WAVE_SYNC();
    const u32x4* a = (c & 11) == 0 ? reinterpret_cast<const u32x4*>(&sm.hb[(c >> 2) * 512 + wave * 64 + g * 8]) : &sm.zero[0];
    const u32x4 a0 = a[0], a1 = a[(c & 11) == 0 ? 4 : 0];
    const f32x4 z = {0.f, 0.f, 0.f, 0.f};
    f32x4 d[4];
#pragma unroll
    for (int t = 0; t < 4; ++t) d[t] = mfma16(a0, bf[t], z);
#pragma unroll
    for (int t = 0; t < 4; ++t) d[t] = mfma16(a1, bf[4 + t], d[t]);
    if (lane < 32) {
#pragma unroll
      for (int t = 0; t < 4; ++t) sm.r2[(t * 16 + c) * 20 + g * 8 + wave] = d[t][0];
    }
  };
  auto mod_stage = [&]() {
    wo_prep();
    gemv_go(pack_bf(silu_f(lo_f(xs0)), silu_f(lo_f(xs1))));
    __builtin_amdgcn_s_waitcnt(0xc07f);
    if (lane == 0) *LDSP(volatile u32, (u32)(uintptr_t)&sm.mflag[wave]) = 1u;
    if (wave == 7) {
#pragma nounroll
      for (int spin = 0; spin < SPIN_MAX; ++spin) {
        const u32 f = *LDSP(volatile u32, (u32)(uintptr_t)&sm.mflag[lane & 7]);
        if (all_ok(f != 0u)) break;
        __builtin_amdgcn_s_sleep(1);
      }
      CFENCE();
      st8_sc1(wsr, G_MOD + (blk * 64 + lane) * 8, pack_bf(sum8(&sm.r2[lane * 20]), sum8(&sm.r2[lane * 20 + 8])), tag);
    }
  };
  if (lane < 4) sm.zero[lane] = u32x4{0u, 0u, 0u, 0u};
  if (part == 2 && has_w) {
    if (tid < 8) sm.mflag[tid] = 0u;
    LDS_BARRIER();
    if (__builtin_expect(modc, 0)) {
      small_ops();
      asm volatile("s_waitcnt vmcnt(0)" ::: "memory");
      tag = next_tag(__builtin_amdgcn_readfirstlane(ep[0]));
      mod_stage();
      __builtin_amdgcn_s_barrier();
    }
  }
  SB();
  int fd = (xcd * 8 + i8) * 8 + (lane & 3) * 2;
  const int ub = wave >> 2, ud = (wave & 3) * 128 + c * 8 + g * 2;
  u32x4 sg[3]; u32 sx2;
  auto side_loads = [&]() {
    const bool fin = part == 1 && wave == 0;
    sg[0] = ld16<AUX_SC1>(wsr, is_up ? G_MOD + (1024 + ud) * 8 : (fin ? G_MOD + (1024 + fd) * 8 : OOB), 0);
    sg[1] = ld16<AUX_SC1>(wsr, is_up ? G_MOD + (1536 + ud) * 8 : (fin ? G_MOD + (2560 + fd) * 8 : OOB), 0);
    sg[2] = ld16<AUX_SC1>(wsr, is_up ? G_MOD + (2048 + ud) * 8 : OOB, 0);
    sx2 = __builtin_amdgcn_raw_buffer_load_b32(mk_rsrc(x, (is_up || fin) ? 2048u : 0u), is_up ? ub * 1024 + ud * 2 : (g & 1) * 1024 + fd * 2, 0, 0);
  };
  auto side_pin = [&]() { PIN(sg[0]); PIN(sg[1]); PIN(sg[2]); PIN(sx2); };
  const u32 mlo = G_ML + (head * 2 + b) * 64 + (lane & 3) * 16;
  float fmrg = 0.f;
  auto merge_factor = [&](const u32x4& ml) {
    const float mi = u2f(ml[0]);
    float M = fmaxf(mi, dpp<XOR1>(mi)); M = fmaxf(M, dpp<XOR2>(M));
    const float e = __expf(mi - M);
    float L = e * u2f(ml[2]);
    L += dpp<XOR1>(L); L += dpp<XOR2>(L);
    fmrg = rdl(e * __builtin_amdgcn_rcpf(L), part);
  };
  f32x2 ga = {0.f, 0.f}, xin = {0.f, 0.f}, cc1 = {0.f, 0.f};
  float Af = 0.f, Bf = 0.f;
  int hpos = ub * 512 + (ud & ~15) + (g & 1) * 8 + (c & 1) * 4 + (g >> 1) * 2;
  auto up_prep = [&]() {
    if (!all_ok((sg[0][1] == tag) & (sg[0][3] == tag) & (sg[1][1] == tag) & (sg[1][3] == tag) & (sg[2][1] == tag) & (sg[2][3] == tag))) gather16<3>(wsr, G_MOD + (1024 + ud) * 8, 512 * 8, tag, sg);
    ga = f32x2{ub ? hi_f(sg[0][0]) : lo_f(sg[0][0]), ub ? hi_f(sg[0][2]) : lo_f(sg[0][2])};
    cc1 = f32x2{rbf(1.f + (ub ? hi_f(sg[2][0]) : lo_f(sg[2][0]))), rbf(1.f + (ub ? hi_f(sg[2][2]) : lo_f(sg[2][2])))};
    xin = f32x2{lo_f(sx2), hi_f(sx2)};
    u32* hp = reinterpret_cast<u32*>(&sm.hb[hpos]);
    hp[512] = pack_bf(cc1[0], cc1[1]);
    hp[1024] = ub ? perm_hi(sg[1][0], sg[1][2]) : perm_lo(sg[1][0], sg[1][2]);
  };
  u32x8 bw[4];
  auto up_frags = [&]() {
    const int pp = lane & 3;
    const u32 a = wimg + (pp & 1) * 32768 + ((pp >> 1) * 32 + g * 8 + ((lane >> 2) & 3)) * 64 + (((wave >> 1) ^ ((g & 1) | (pp & 2))) * 16) + (wave & 1) * 8;
#pragma unroll
    for (int j = 0; j < 4; ++j) {
      const u32x2 r0 = tr_read(a + j * 4096), r1 = tr_read(a + j * 4096 + 256), r2 = tr_read(a + j * 4096 + 16384), r3 = tr_read(a + j * 4096 + 16640);
      bw[j] = u32x8{r0[0], r0[1], r1[0], r1[1], r2[0], r2[1], r3[0], r3[1]};
    }
  };
  u32 wfa[4];
  auto wo_frags = [&](int s0) {
#pragma unroll
    for (int t = 0; t < 4; ++t) {
#pragma unroll
      for (int s = s0; s < 2; ++s) {
        const u32x2 lo = tr_read(wfa[t] + s * 4096), hi = tr_read(wfa[t ^ 1] + s * 4096 + 256);
        bw[t][s * 4] = lo[0]; bw[t][s * 4 + 1] = lo[1]; bw[t][s * 4 + 2] = hi[0]; bw[t][s * 4 + 3] = hi[1];
      }
    }
  };
  const bool par = lane & 8;
  auto limit = [&](auto sc) {
    constexpr int s = sc.v;
    WAIT_VM(window(s, 1 + (PRM0.W[s >> 3] >> (4 * (s & 7)) & 7)));
    SB();
  };
  auto sample = [&]() { if (timer) lag += (int)(((u32)wall_clock64() - t0) << 8) - due + ((int)__builtin_amdgcn_s_getreg(7 | (3 << 11)) + 1) * tau; };
  auto report = [&](u32 dst) {
    if (timer) {
      if (lane == 0) sm.mflag[wave] = vote * 65536 + min(max(lag >> 8, 0), 4000);
      lag = 0;
      LDS_BARRIER();
      if (wave == 7) {
        int s = sm.mflag[lane & 7];
        s += __builtin_amdgcn_mov_dpp(s, XOR1, 0xf, 0xf, true); s += __builtin_amdgcn_mov_dpp(s, XOR2, 0xf, 0xf, true); s += __builtin_amdgcn_mov_dpp(s, XOR7, 0xf, 0xf, true);
        if (lane < 8) st8_sc1(wsr, dst + lane * 128 + wi * 8, s, tag);
      }
    }
    SB();
  };
  float l0 = 0.f, l1 = 0.f;
  u32x2 gmd[2], tbg;
  int t1 = 0;
#pragma unroll
  for (int i = SK0; i < SK1; ++i) kr[i] = u32x4{0u, 0u, 0u, 0u};
  const int first = SK0 + NL - nl;
  auto left_out = [&](int i) { int f = first; asm volatile("" : "+s"(f)); return i < f; };
  sfor<1, NL>([&](auto ic) {
    constexpr int i = ic.v;
    if constexpr (i >= SK0 && i < SK1) { if (left_out(i)) return true; }
    const u32 so = __builtin_amdgcn_readlane(sot, i);
    gate();
    if constexpr ((i >= TBS && i <= TBP) || (i >= RPS && i <= RPP)) sample();
    SB();
    if constexpr (i >= SK0 && i < SK1) ld16_into(kr[i], rk, lane * 16, so); else kr[i] = ld16<AUX_NT>(rk, lane * 16, so);
    if constexpr (i == SMO) small_ops();
    if constexpr (i == SMO + 1) { if (voter && (xcd & 3) == 0) __builtin_amdgcn_raw_buffer_store_b32(0u, wsr, pbo + (lane >> 5) * 8192 + (lane & 31) * 4, 0, AUX_SC1); }
    if constexpr (i == SK1) tbg = ld8<AUX_SC1>(wsr, wave == 7 ? G_VT + xcd * 128 + c * 8 : OOB, 0);
    if constexpr (i == TBL) tbg = ld8<AUX_SC1>(wsr, wave == 7 ? G_TB + xcd * 128 + c * 8 : OOB, 0);
    if constexpr (i == QP) { gmd[0] = ld8<AUX_SC1>(wsr, part == 1 ? G_MOD + tid * 8 : OOB, 0); gmd[1] = ld8<AUX_SC1>(wsr, part == 1 ? G_MOD + tid * 8 : OOB, 4096); }
    SB();
    SB();
    if (i >= 4) limit(ic);
    if (i == TBP) {
      if (timer) {
        if (lane == 0) sm.mflag[wave] = lag;
        lag = 0;
        LDS_BARRIER();
        if (wave == 7) {
          int s = sm.mflag[lane & 7];
          s += __builtin_amdgcn_mov_dpp(s, XOR1, 0xf, 0xf, true); s += __builtin_amdgcn_mov_dpp(s, XOR2, 0xf, 0xf, true); s += __builtin_amdgcn_mov_dpp(s, XOR7, 0xf, 0xf, true);
          if (lane < 8) st8_sc1(wsr, G_TB + lane * 128 + wi * 8, t0 << 10 | (u32)min(s / (2048 * (TBP - TBS + 1)), 1023), tag);
        }
      }
      SB();
    }
    if (i == PRB) {
      if (voter) {
        u64 ta, tb, tc; u32 dm;
        const u32 pa = pbo + (wave & 1) * 8192;
        asm volatile("s_memtime %0\n\ts_waitcnt lgkmcnt(0)\n\ts_load_dword %3, %4, %5\n\ts_waitcnt lgkmcnt(0)\n\ts_memtime %1\n\ts_waitcnt lgkmcnt(0)\n\t"
                     "s_load_dword %3, %4, %6\n\ts_waitcnt lgkmcnt(0)\n\ts_memtime %2\n\ts_waitcnt lgkmcnt(0)"
                     : "=&s"(ta), "=&s"(tb), "=&s"(tc), "=&s"(dm) : "s"(ws), "s"(pa), "s"(pa ^ 8192u));
        const int d = min(max((int)(2 * (u32)tb - (u32)ta - (u32)tc), -500), 500);
        vote = ((wave ^ die) & 1) ? -d : d;
      }
      SB();
    }
    if (i == TBU) {
      if (wave == 7) {
        const bool ok = tbg[1] == tag;
        float lf = ok ? (float)min(max((int)((t0 << 10 | 1023u) - tbg[0]) >> 10, -300), 300) : 0.f, nf = ok ? 1.f : 0.f, lg = ok ? (float)(tbg[0] & 1023u) : 0.f;
        lf += dpp<XOR1>(lf); nf += dpp<XOR1>(nf); lg += dpp<XOR1>(lg); lf += dpp<XOR2>(lf); nf += dpp<XOR2>(nf); lg += dpp<XOR2>(lg);
        lf += dpp<XOR7>(lf); nf += dpp<XOR7>(nf); lg += dpp<XOR7>(lg); lf += dpp<XOR8>(lf); nf += dpp<XOR8>(nf); lg += dpp<XOR8>(lg);
        const float rn = __builtin_amdgcn_rcpf(fmaxf(nf, 1.f));
        shift = __builtin_amdgcn_readfirstlane(min(max((int)((lg * rn - (float)P.GS) * (float)P.GB * 0.125f), 0), 150));
        if (lane == 0) sm.bc = u32x2{tag, t0 - (u32)(int)(lf * rn) + shift};
      }
      LDS_BARRIER();
      const u32x2 bc = sm.bc;
      tag = __builtin_amdgcn_readfirstlane(bc[0]);
      t1 = (__builtin_amdgcn_readfirstlane((int)(bc[1] - t0)) + TEND + P.X1) * 256;
      tau -= __builtin_amdgcn_readfirstlane((int)((float)(int)(t0 - bc[1]) * 256.f * __builtin_amdgcn_rcpf((float)(nl - TBU - 1))));
      SB();
    }
    if (i == RPP) report(G_VT);
    if (i == LNS) {
      tag = next_tag(__builtin_amdgcn_readfirstlane(ep[0]));
      if (part == 1) {
        float x0[8], x1[8], mb0, ri0, mb1, ri1;
#pragma unroll
        for (int k = 0; k < 4; ++k) { x0[2 * k] = lo_f(xr0[k]); x0[2 * k + 1] = hi_f(xr0[k]); x1[2 * k] = lo_f(xr1[k]); x1[2 * k + 1] = hi_f(xr1[k]); }
        ln_stats2(x0, x1, mb0, ri0, mb1, ri1);
        l0 = rbf(rbf(lo_f(xs0) - mb0) * ri0); l1 = rbf(rbf(lo_f(xs1) - mb1) * ri1);
      }
      PIN(l0); PIN(l1);
      SB();
    }
    if (i == WUSE) {
      PIN(tch1); PIN(tch2);
      if ((part == 1 || (part == 2 && has_w && !modc))) {
        WAIT_VM(WK); CFENCE();
        if (part == 2) mod_stage(); else wo_prep();
      }
#pragma unroll
      for (int k = 0; k < 8; ++k) PIN(bf[k]);
      SB();
    }
    if (i == QP + QD) {
      if (part == 1) {
        u32 gm[2] = {gmd[0][0], gmd[1][0]};
        if (!all_ok((gmd[0][1] == tag) & (gmd[1][1] == tag))) gather8<2>(wsr, G_MOD + tid * 8, 512 * 8, tag, gm);
        gemv_go(pack_bf(rbf(l0 * rbf(1.f + lo_f(gm[1]))) + lo_f(gm[0]), rbf(l1 * rbf(1.f + hi_f(gm[1]))) + hi_f(gm[0])));
      }
      SB();
    }
    if (i == QP + QD + 1) {
      if (part == 1) {
        LDS_BARRIER();
        if (wave == 0) {
          const int bb = lane >> 5, jq = lane & 31;
          st8_sc1(wsr, G_Q + (bb * 2048 + blk * 32 + jq) * 8, pack_bf(sum8(&sm.r2[2 * jq * 20 + bb * 8]), sum8(&sm.r2[(2 * jq + 1) * 20 + bb * 8])), tag);
        }
      }
      SB();
    }
    return true;
  });
  PIN(sot);
  if (wave == 7) {
    const bool ok = tbg[1] == tag;
    float lg = ok ? (float)(tbg[0] & 0xffffu) : 0.f, nf = ok ? 8.f * (RPP - RPS + 1) : 0.f;
    lg += dpp<XOR1>(lg); nf += dpp<XOR1>(nf); lg += dpp<XOR2>(lg); nf += dpp<XOR2>(nf); lg += dpp<XOR7>(lg); nf += dpp<XOR7>(nf); lg += dpp<XOR8>(lg); nf += dpp<XOR8>(nf);
    lg *= __builtin_amdgcn_rcpf(fmaxf(nf, 1.f));
    if (lane == 0) sm.bc2 = u32x2{(u32)min(shift * P.DG * 32, P.EW * 256), (u32)min(max((int)((lg - (float)P.B0) * (float)(P.BG * 32) + fmaxf(lg - (float)P.TC, 0.f) * (float)(P.RT * 32)) - shift * P.D0 * 32, 0), 200 * 256)};
  }
  LDS_BARRIER();
  {
    const u32x2 bc = sm.bc2;
    const int st = __builtin_amdgcn_readfirstlane(bc[1]), sp = (st * P.BF) >> 3;
    t1 += st + __builtin_amdgcn_readfirstlane(bc[0]);
    due += P.TG * 256 + sp;
    tau = (te1 >> 16) + __builtin_amdgcn_readfirstlane((int)((float)(st - sp) * __builtin_amdgcn_rcpf((float)(nl - 3))));
  }
  const u32 qoff = G_Q + ((b * 32 + head) * 64 + c * 4) * 8;
  u32x4 qg[2], mlg;
  u32 qq[4];
  constexpr int NB = (NL + 15) / 16;
  float xlo, sc[NB], pr[NB], m = 0.f;
  f32x4 acc = {0.f, 0.f, 0.f, 0.f};
  u32 pb[NB], pm[4], pl8[8], cpf;
  const u32 adst0 = lds_addr(lane < 32 ? &sm.red[wave][c * 8 + g * 4] : &sm.r2[(lane - 32) * 4]);
  u32 po, pso, rdst, rsrc, xo;
  auto wo_addr = [&]() {
    po = dup(G_P + (xcd & 3) * 8192) + tid * 16 + near;
    pso = tid < 16 ? pslot + c * 128 + near : OOB;
    PIN(pso);
    rdst = (u32)(uintptr_t)&sm.red2[wave][(lane & 31) * 4 + (lane >> 5) * 2];
    rsrc = (u32)(uintptr_t)&sm.red2[0][((wave & 1) * 16 + c) * 4 + (g & 1) * 2 + (g >> 1)];
    xo = dup(G_XP + xcd * 2048 + (wave & 1) * 1024 + (blk * 64 + (lane & ~1)) * 2) + (lane & 1) * 8192;
    PIN(po); PIN(rdst); PIN(rsrc); PIN(xo);
    const int q = (lane >> 2) & 3, pp = lane & 3, r = (g & 1) * 4 + (q & 1);
#pragma unroll
    for (int t = 0; t < 4; ++t) { wfa[t] = wimg + wave * 8192 + ((g >> 1) * 16 + (q >> 1) * 8 + r) * 128 + (pp & 1) * 8 + ((t * 2 + (pp >> 1)) ^ r ^ (q & 2)) * 16; PIN(wfa[t]); }
  };
  const u32 nadd = wdef ? P.LW : (is_up ? P.LU : 0);
  u32 nto = tid < 2 ? dup(G_NT) + bid * 8 + tid * 8192 : OOB;
  PIN(nto);
  auto notices = [&]() -> int {
    u32 off = dup(G_NT) + near + lane * 16;
    PIN(off);
    until(due);
    int mx;
#pragma nounroll
    for (int spin = 0; spin < SPIN_MAX; ++spin) {
      CFENCE();
      const u32x4 ga_ = ld16<AUX_SC1>(wsr, off, 0), gb_ = ld16<AUX_SC1>(wsr, off, 1024);
      mx = max(max((int)(ga_[0] - t0), (int)(ga_[2] - t0)), max((int)(gb_[0] - t0), (int)(gb_[2] - t0)));
      if (all_ok((((ga_[1] ^ tag) | (ga_[3] ^ tag)) | ((gb_[1] ^ tag) | (gb_[3] ^ tag))) == 0u)) break;
      for (int k = 0; k < P.AG; ++k) __builtin_amdgcn_s_sleep(1);
    }
    if (all_ok(mx <= (t1 >> 8) - P.AC)) return t1;
    mx = max(mx, __builtin_amdgcn_mov_dpp(mx, XOR1, 0xf, 0xf, true)); mx = max(mx, __builtin_amdgcn_mov_dpp(mx, XOR2, 0xf, 0xf, true));
    mx = max(mx, __builtin_amdgcn_mov_dpp(mx, XOR7, 0xf, 0xf, true)); mx = max(mx, __builtin_amdgcn_mov_dpp(mx, XOR8, 0xf, 0xf, true));
    mx = max(max(__builtin_amdgcn_readlane(mx, 0), __builtin_amdgcn_readlane(mx, 16)), max(__builtin_amdgcn_readlane(mx, 32), __builtin_amdgcn_readlane(mx, 48)));
    return max(min(mx, 1 << 20) * 256, t1 + P.L0 * 256) + P.L1 * 256;
  };
  auto pv_row = [&](auto jc) {
    constexpr int i = jc.v;
    const u32 pv = __float_as_uint(bcast16(u2f(pb[i >> 4]), i & 15));
    acc = mfma16(u32x4{pv & pm[0], pv & pm[1], pv & pm[2], pv & pm[3]}, vr[i], acc);
    PIN(acc);
    return true;
  };
  sfor<0, NL - 1>([&](auto ic) {
    constexpr int v = ic.v;
    if constexpr (v >= SK0 && v < SK1) { if (left_out(v)) return true; }
    if constexpr (v > 0) {
      const u32 so = __builtin_amdgcn_readlane(sot, v);
      if constexpr (v >= SK1) { if (v == NL - P.NE) tau = (tau * P.FE) >> 4; }
      gate();
      if constexpr (v >= RVS && v <= RVP) sample();
      SB();
      if constexpr (v >= SK0 && v < SK1) ld16_into(vr[v], rv, lane * 16, so); else vr[v] = ld16<AUX_NT>(rv, lane * 16, so);
      SB();
      SB();
    }
    if constexpr (v == SK0 - 1) {
#pragma unroll
      for (int i = SK0; i < SK1; ++i) { vr[i] = u32x4{0u, 0u, 0u, 0u}; PIN(vr[i]); }
    }
    if constexpr (v == VQ - 4) { qg[0] = ld16<AUX_SC1>(wsr, qoff, 0); qg[1] = ld16<AUX_SC1>(wsr, qoff + 16, 0); }
    if constexpr (v == VSIDE) side_loads();
    if constexpr (v == CPF) {
      u64 pc; u32 dlt, len;
      asm volatile("s_getpc_b64 %0\n.Lcpf_here:\n\ts_mov_b32 %1, .Ltail_start-.Lcpf_here\n\ts_mov_b32 %2, .Lend_a-.Ltail_start\n\ts_max_i32 %2, %2, .Lend_b-.Ltail_start\n\ts_max_i32 %2, %2, .Lend_c-.Ltail_start\n\ts_max_i32 %2, %2, .Lend_d-.Ltail_start\n\ts_max_i32 %2, %2, .Lend_e-.Ltail_start" : "=s"(pc), "=s"(dlt), "=s"(len) :: "scc");
      pc += dlt;
      const rsrc_t rc = mk_rsrc((const void*)pc, (part == 3 && i8 == 1) ? len : 0u);
      cpf = __builtin_amdgcn_raw_buffer_load_b32(rc, tid * 64, 0, 0);
    }
    if constexpr (v == SK1 + 2) PIN(cpf);
    if constexpr (v == RVP) report(G_VT + 1024);
    if constexpr (v == RVL) tbg = ld8<AUX_SC1>(wsr, wave == 7 ? G_VT + (g ? 0 : 1024) + xcd * 128 + c * 8 : OOB, 0);
    if constexpr (v == SK1) mlg = ld16<AUX_SC1>(wsr, wave == 0 ? mlo : OOB, 0);
    SB();
    if constexpr (NL + v < 72) limit(IC<NL + v>{});
    if constexpr (v >= SK0 + WV) { WAIT_VM(v < SK1 ? WV : WV + 1); SB(); }
    if constexpr (v == VQ) {
      if (!all_ok((qg[0][1] == tag) & (qg[0][3] == tag) & (qg[1][1] == tag) & (qg[1][3] == tag))) gather16<2>(wsr, qoff, 16, tag, qg);
      qq[0] = qg[0][0]; qq[1] = qg[0][2]; qq[2] = qg[1][0]; qq[3] = qg[1][2];
    }
    if constexpr (v >= VQ && v < VS) {
      constexpr int sl = v - VQ, bt = sl >> 1;
      float xs[8];
#pragma unroll
      for (int i = 0; i < 8; ++i) {
        float d = 0.f;
        if (sl * 8 + i < NL) {
#pragma unroll
          for (int k = 0; k < 4; ++k) d = dot2(kr[sl * 8 + i][k], qq[k], d);
        }
        xs[i] = d;
      }
#pragma unroll
      for (int h = 0; h < 4; ++h) xs[h] = sel_add<XOR7>(xs[h], xs[h + 4], c & 4);
#pragma unroll
      for (int h = 0; h < 2; ++h) xs[h] = sel_add<XOR2>(xs[h], xs[h + 2], c & 2);
      xs[0] = sel_add<XOR1>(xs[0], xs[1], c & 1);
      if constexpr ((sl & 1) || sl == NSL - 1) {
        const float t = (sl & 1) ? sel_add<XOR8>(xlo, xs[0], c & 8) : xs[0] + dpp<XOR8>(xs[0]);
        const float s = rbf(rbf(t) * 0.08838834764831845f);
        sc[bt] = (bt * 16 + c < SK0 || (bt * 16 + c >= first && bt * 16 + c < NL)) ? s : -__builtin_inff();
        PIN(sc[bt]);
      } else {
        xlo = xs[0];
        PIN(xlo);
      }
    }
    if constexpr (v == VS) {
      float mloc = sc[0];
#pragma unroll
      for (int bt = 1; bt < NB; ++bt) mloc = fmaxf(mloc, sc[bt]);
      m = fmaxf(wave_max(mloc), -3.0e38f);
      float ls = 0.f;
#pragma unroll
      for (int bt = 0; bt < NB; ++bt) { pr[bt] = __expf(sc[bt] - m); ls += pr[bt]; }
      const float l = wave_sum(ls);
      if (lane == 0) { sm.ml[wave][0] = m; sm.ml[wave][1] = l; }
      if (c == ((NL - 1) & 15)) sm.pl[g][wave] = pr[NB - 1];
    }
    if constexpr (v >= SK1) pv_row(IC<v - SK1 + SK0 - WV>{});
    if constexpr (v < SK0 || v >= SK0 + WV) sfor<cons_end(v - 1), cons_end(v)>(pv_row);
    if constexpr (v == RVU) {
      if (wave == 7) {
        if (__builtin_expect(!all_ok(tbg[1] == tag || g == 0), 0)) {
          u32 vg[1];
          gather8<1>(wsr, G_VT + xcd * 128 + c * 8, 0, tag, vg);
          if (g) tbg = u32x2{vg[0], tag};
        }
        const bool ok = tbg[1] == tag;
        float lg = ok ? (float)(g ? (int)tbg[0] >> 16 : (int)(tbg[0] & 0xffffu)) : 0.f, nf = ok ? 8.f * (RVP - RVS + 1) : 0.f;
        lg += dpp<XOR1>(lg); nf += dpp<XOR1>(nf); lg += dpp<XOR2>(lg); nf += dpp<XOR2>(nf); lg += dpp<XOR7>(lg); nf += dpp<XOR7>(nf); lg += dpp<XOR8>(lg); nf += dpp<XOR8>(nf);
        lg *= __builtin_amdgcn_rcpf(fmaxf(nf, 1.f));
        if (lane == 16) sm.bc4 = __float_as_uint(lg);
        if (lane == 0) sm.bc3 = (u32)min(max((int)((lg - (float)P.F0) * (float)(P.FG * 32)), 0), 200 * 256);
      }
      LDS_BARRIER();
      const int st = __builtin_amdgcn_readfirstlane(sm.bc3), sp = (st * P.FF_) >> 3;
      near = ((__builtin_amdgcn_readfirstlane(sm.bc4) >> 31 ^ die ^ P.NR ^ 1) & 1) * 8192u;
      t1 += st;
      due += sp;
      tau += __builtin_amdgcn_readfirstlane((int)((float)(st - sp) * __builtin_amdgcn_rcpf((float)(nl - RVU - 3))));
    }
    if constexpr (v == SK1 + 1) {
      float p[8];
#pragma unroll
      for (int w = 0; w < 8; ++w) p[w] = sm.pl[g][w] * __expf(sm.ml[w][0] - m);
#pragma unroll
      for (int w = 0; w < 8; ++w) pl8[w] = pack_bf(p[w], p[w]);
#pragma unroll
      for (int w = 0; w < 8; ++w) PIN(pl8[w]);
    }
    if constexpr (v == MLS - 1) { side_pin(); if (is_up) up_prep(); PIN(ga); PIN(xin); PIN(cc1); }
    if constexpr (v == UPREP - 1) wo_addr();
    if constexpr (v == UPREP) {
#pragma unroll
      for (int k = 0; k < 4; ++k) asm volatile("" : "=v"(bw[k]));
      if (part == 0) wo_frags(0); else if (is_up) up_frags();
#pragma unroll
      for (int k = 0; k < 4; ++k) PIN(bw[k]);
    }
    if constexpr (v == UPREP + 1) {
      if (is_up) {
        const u32x4* a1 = reinterpret_cast<const u32x4*>(&sm.hb[1024 + (c >> 2 & 1) * 1024 + (c >> 3) * 512 + (g >> 1) * 256 + (c >> 1 & 1) * 32 + (g & 1) * 16 + (c & 1) * 8]);
        f32x4 d = {0.f, 0.f, 0.f, 0.f};
#pragma unroll
        for (int j = 0; j < 4; ++j) d = mfma64(a1[j * 8], bw[j], d, (c & 1) ? 0xeeeeu : 0x4444u);
        float t = par ? d[2] + d[3] : d[0] + d[1];
        t += dpp<XOR8>(t);
        const auto ab = __builtin_amdgcn_permlane16_swap(__float_as_uint(t), __float_as_uint(t), false, false);
        Af = u2f(ab[0]); Bf = u2f(ab[1]);
      }
      PIN(Af); PIN(Bf);
    }
    if constexpr (v == MLS) {
      LDS_BARRIER();
      float mm[8], ll[8];
#pragma unroll
      for (int w = 0; w < 8; ++w) { const f32x2 t = *reinterpret_cast<const f32x2*>(&sm.ml[w][0]); mm[w] = t[0]; ll[w] = t[1]; }
      float Mwg = mm[0], Lwg = 0.f;
#pragma unroll
      for (int w = 1; w < 8; ++w) Mwg = fmaxf(Mwg, mm[w]);
      if (wave == 0) {
#pragma unroll
        for (int w = 0; w < 8; ++w) Lwg = fmaf(__expf(mm[w] - Mwg), ll[w], Lwg);
        if (lane == 0) st16_sc1(wsr, G_ML + ((head * 2 + b) * 4 + part) * 16, __float_as_uint(Mwg), __float_as_uint(Lwg), tag);
      }
      const float fw = __expf(m - Mwg);
      m = Mwg;
#pragma unroll
      for (int bt = 0; bt < NB; ++bt) { pb[bt] = pack_bf(pr[bt] * fw, pr[bt] * fw); PIN(pb[bt]); }
#pragma unroll
      for (int k = 0; k < 4; ++k) { pm[k] = (c >> 1 & 3) == k ? ((c & 1) ? 0xffff0000u : 0x0000ffffu) : 0u; PIN(pm[k]); }
    }
    SB();
    return true;
  });
  asm volatile(".Ltail_start:");
  if (wave != 0) {
    pv_row(IC<SK0 - 1>{});
    WAIT_VM(WV); SB();
    sfor<NL - 1 - WV, NL - 1>(pv_row);
    *LDSP(f32x4, adst0) = acc;
    LDS_BARRIER();
  } else {
    u32x4 xr[8], am[8];
#pragma unroll
    for (int w = 0; w < 8; ++w) am[w] = u32x4{pl8[w] & pm[0], pl8[w] & pm[1], pl8[w] & pm[2], pl8[w] & pm[3]};
#pragma unroll
    for (int w = 0; w < 8; ++w) PIN(am[w]);
    {
      const u32 so = __builtin_amdgcn_readlane(sot, NL - 1);
      gate();
      SB();
#pragma unroll
      for (int w = 0; w < 8; ++w) xr[w] = ld16<AUX_NT>(rv, lane * 16, so ^ ((w & 1) ? col(P, 7) : 0u) ^ ((w & 2) ? col(P, 8) : 0u) ^ ((w & 4) ? col(P, 9) : 0u));
      SB();
    }
    u32 rsum = lds_addr(&sm.red[0][c * 8 + (g & 1) * 4]);
    pv_row(IC<SK0 - 1>{});
    WAIT_VM(WV + 8); SB();
    sfor<NL - 1 - WV, NL - 3>(pv_row);
    if (__builtin_expect(!all_ok((mlg[1] == tag) & (mlg[3] == tag)), 0)) {
#pragma nounroll
      for (int spin = 0; spin < SPIN_MAX; ++spin) {
        CFENCE();
        mlg = ld16<AUX_SC1>(wsr, mlo, 0);
        if (all_ok((mlg[1] == tag) & (mlg[3] == tag))) break;
      }
    }
    merge_factor(mlg);
    sfor<NL - 3, NL - 1>(pv_row);
    LDS_BARRIER();
    f32x4 t[8];
#pragma unroll
    for (int w = 1; w < 8; ++w) t[w] = LDSP(f32x4, rsum)[w * 32];
    acc = ((acc + t[1]) + (t[2] + t[3])) + ((t[4] + t[5]) + (t[6] + t[7]));
    PIN(acc);
    SB();
#pragma unroll
    for (int w = 0; w < 8; ++w) acc = mfma16(am[w], xr[w], acc);
  }
  WAIT_VM(0);
  acc *= fmrg;
  {
    const u32 w0 = min(pack_bf(acc[0], acc[1]), SENT - 1u), w1 = min(pack_bf(acc[2], acc[3]), SENT - 1u);
    const auto e0 = __builtin_amdgcn_permlane16_swap(w0, w0, false, false), e1 = __builtin_amdgcn_permlane16_swap(w1, w1, false, false);
    __builtin_amdgcn_raw_buffer_store_b128(u32x4{e0[0], e1[0], e0[1], e1[1]}, wsr, pso, 0, AUX_SC1);
  }
  st8_sc1(wsr, nto, (u32)wall_clock64() + nadd, tag);
  SB();

  PIN(tid);
  lane = tid & 63; g = lane >> 4; c = lane & 15;
  fd = (xcd * 8 + i8) * 8 + (lane & 3) * 2;
  hpos = ub * 512 + (wave & 3) * 128 + (c >> 1) * 16 + (g & 1) * 8 + (c & 1) * 4 + (g >> 1) * 2;
  if (part == 2) return;
  if (part == 0) {
    __builtin_amdgcn_s_barrier();
#pragma nounroll
    while (wleft > 0) { until(t1 - (P.PA + wleft * P.PD) * 256); piece(); }
    bool late = false;
    u32x4 og[1];
    f32x4 d[4];
    u32 six = (c & 8) ? 0xeeeeu : 0x4444u;
    PIN(six);
#pragma unroll
    for (int t = 0; t < 4; ++t) { d[t] = f32x4{0.f, 0.f, 0.f, 0.f}; PIN(d[t]); }
    if (wdef) {
      late = true;
      while ((int)(((u32)wall_clock64() - t0) << 8) < t1 - 10 * 256) if (__builtin_amdgcn_s_getreg(7 | (3 << 11)) == 0) { late = false; break; }
      if (__builtin_expect(late, 0)) { until(t1); CFENCE(); og[0] = ld16<AUX_SC1>(wsr, po, 0); SB(); WAIT_VM(1); } else { WAIT_VM(0); }
      CFENCE();
      wo_frags(1);
#pragma unroll
      for (int k = 0; k < 4; ++k) PIN(bw[k]);
      SB();
    }
    if (__builtin_expect(!late, 1)) { until(t1); CFENCE(); og[0] = ld16<AUX_SC1>(wsr, po, 0); }
    SB();
    auto wo_mm = [&]() {
#pragma unroll
      for (int t = 0; t < 4; ++t) d[t] = mfma64(og[0], bw[t], d[t], six);
    };
    auto p_bad = [&]() { return !all_ok(max(max(og[0][0], og[0][1]), max(og[0][2], og[0][3])) != SENT); };
    wo_mm();
    if (__builtin_expect(p_bad(), 0)) {
#pragma nounroll
      for (int spin = 0; spin < SPIN_MAX; ++spin) {
        __builtin_amdgcn_s_sleep(1);
        CFENCE();
        og[0] = ld16<AUX_SC1>(wsr, po, 0);
        if (!p_bad()) break;
      }
#pragma unroll
      for (int t = 0; t < 4; ++t) d[t] = f32x4{0.f, 0.f, 0.f, 0.f};
      wo_mm();
#pragma unroll
      for (int t = 0; t < 4; ++t) { d[t] += f32x4{0.f, 0.f, 0.f, 0.f}; PIN(d[t]); }
      SB();
      asm volatile(".Lend_d:");
    }
    {
      SB();
      f32x4 y; f32x2 h[4];
#pragma unroll
      for (int t = 0; t < 4; ++t) { h[t] = d[t].xy + d[t].zw; PIN(h[t]); }
#pragma unroll
      for (int t = 0; t < 4; ++t) { y[t] = h[t][0] + h[t][1]; PIN(y[t]); }
      *LDSP(f32x2, rdst) = f32x2{swap32_add(y[0], y[1]), swap32_add(y[2], y[3])};
    }
    LDS_BARRIER();
    if (wave < 2) {
      f32x2 v[4];
#pragma unroll
      for (int w = 0; w < 4; ++w) v[w] = f32x2{LDSP(float, rsrc)[w * 256], LDSP(float, rsrc)[w * 256 + 128]};
      const f32x2 h = (v[0] + v[1]) + (v[2] + v[3]);
      const float y = h[0] + h[1];
      __builtin_amdgcn_raw_buffer_store_b32(min(pack_bf(dpp<0xA0>(y), dpp<0xF5>(y)), SENT - 1u), wsr, xo, 0, AUX_SC1);
      asm volatile(".Lend_a:");
    }
    return;
  }
  if (part == 3) {
    if (!has_w) return;
    u32x8 b2[2];
    {
      __builtin_amdgcn_s_barrier();
      const rsrc_t r2 = mk_rsrc(w2 + (size_t)(blk * 32 + wave * 4) * 512, 4096u);
      const u32 l2 = (u32)(uintptr_t)(&sm.w2b[wave * 4][0]);
#pragma unroll
      for (int n = 0; n < 4; ++n) { until(t1 - (P.QA + (4 - n) * P.QD) * 256); dma16(r2, (lane ^ (n * 2) ^ ((wave & 2) << 2)) * 16, n * 1024, l2 + n * 1024); }
    }
    const u32 tdst = lds_addr(&sm.hb[hpos]);
    const u32 sdst = lds_addr((lane & 7) == 0 ? &sm.stat[0][(wave >> 2) * 32 + (lane >> 5) * 16 + (wave & 3) * 4 + (lane >> 3 & 3)] : &sm.stat[1][lane]);
    const u32 ssrc = lds_addr(&sm.stat[0][lane]);
    const u32 adst = lds_addr((c < 4 && (g & 1) == 0) ? &sm.actl[g >> 1][(wave >> 2) * 16 + (c >> 1) * 8 + (wave & 3) * 2 + (c & 1)] : reinterpret_cast<u16*>(&sm.dummy[lane]));
    const u16* act8 = &sm.actl[c >> 1 & 1][(g & 1) * 16 + (c & 1) * 8];
    const u32 asrc0 = lds_addr((c >> 2) == (g >> 1) ? act8 : reinterpret_cast<const u16*>(sm.zero)), asrc1 = lds_addr((c >> 2) == 2 + (g >> 1) ? act8 : reinterpret_cast<const u16*>(sm.zero));
    f32x4 dn = {0.f, 0.f, 0.f, 0.f};
    PIN(dn);
    const u32 apa = lds_addr(&sm.hb[(c >> 3) * 512 + (g >> 1) * 256 + (c >> 1 & 1) * 64 + ((c >> 2) & 1) * 32 + (g & 1) * 16 + (c & 1) * 8]);
    u32 six = (c & 1) ? 0xeeeeu : 0x4444u;
    f32x4 d[2] = {{0.f, 0.f, 0.f, 0.f}, {0.f, 0.f, 0.f, 0.f}};
    PIN(six); PIN(d[0]); PIN(d[1]);
    constexpr u32 xs2 = 16384;
    u32 xoff = dup(G_XP) + near + g * 2048 + ub * 1024 + (wave & 3) * 256 + c * 16;
    u32x4 one;
#pragma unroll
    for (int k = 0; k < 4; ++k) one[k] = ((c & 2) == 0 && (c >> 3) == (k & 1)) ? ((c & 1) ? 0x3f800000u : 0x00003f80u) : 0u;
    u32 oix = (c & 4) ? 0xeeeeu : 0x4444u;
    f32x4 v = {0.f, 0.f, 0.f, 0.f};
    PIN(one); PIN(oix); PIN(v);
    const int cw = wave ^ ((die ^ 1) << 2);
    u32 ooff = dup(G_OP + (blk * 512 + cw * 64 + lane) * 8) + (near ^ ((cw >> 2 ^ die) << 13));
    float Afp = Af, Bfp = Bf;
    PIN(xoff); PIN(ooff); PIN(Afp); PIN(Bfp);
    f32x2 gc = ga * cc1, xc = xin * cc1;
    PIN(ga); PIN(xin); PIN(gc); PIN(xc);
    u32 w2a = (u32)(uintptr_t)(&sm.w2b[0][0]) + (g * 8 + ((lane >> 2) & 3)) * 1024 + (cw ^ (g & 1)) * 128 + (lane & 15) * 8;
    PIN(w2a);
    if (wave == 1) {
      due = t1 + P.N1 * 256;
      const int tn = notices();
      due = tn + P.X2 * 256;
      until(due);
    }
    asm volatile("s_waitcnt vmcnt(0)" ::: "memory");
    __builtin_amdgcn_s_barrier();
    CFENCE();
    u32x4 xg[2] = {ld16<AUX_SC1>(wsr, xoff, 0), ld16<AUX_SC1>(wsr, xoff, xs2)};
    SB();
#pragma unroll
    for (int t = 0; t < 4; ++t) {
      const u32x2 lo = tr_read(w2a ^ (t * 32)), hi = tr_read((w2a ^ (t * 32)) + 4096);
      b2[t >> 1][(t & 1) * 4] = lo[0]; b2[t >> 1][(t & 1) * 4 + 1] = lo[1]; b2[t >> 1][(t & 1) * 4 + 2] = hi[0]; b2[t >> 1][(t & 1) * 4 + 3] = hi[1];
    }
    PIN(b2[0]); PIN(b2[1]);
    SB();
    const f32x4 z4 = {0.f, 0.f, 0.f, 0.f};
    auto xp_bad = [&]() { return !all_ok(max(max(max(xg[0][0], xg[0][1]), max(xg[0][2], xg[0][3])), max(max(xg[1][0], xg[1][1]), max(xg[1][2], xg[1][3]))) != SENT); };
    auto both = [&]() { return u32x8{xg[0][0], xg[0][1], xg[0][2], xg[0][3], xg[1][0], xg[1][1], xg[1][2], xg[1][3]}; };
    v = mfma64(one, both(), v, oix);
    if (__builtin_expect(xp_bad(), 0)) {
#pragma nounroll
      for (int spin = 0; spin < SPIN_MAX; ++spin) {
        __builtin_amdgcn_s_sleep(1);
        CFENCE();
        xg[0] = ld16<AUX_SC1>(wsr, xoff, 0); xg[1] = ld16<AUX_SC1>(wsr, xoff, xs2);
        if (!xp_bad()) break;
      }
      v = mfma64(one, both(), z4, oix);
      v += z4;
      PIN(v);
      SB();
      asm volatile(".Lend_e:");
    }
    {
      PIN(v);
      const f32x2 a = v.xy, hc = __builtin_elementwise_fma(gc, a, xc), x1 = __builtin_elementwise_fma(ga, a, xin);
      *LDSP(u32, tdst) = pack_bf(hc[0], hc[1]);
      float u = swap32_add(x1[0] + x1[1], fmaf(x1[0], x1[0], x1[1] * x1[1]));
      u += dpp<XOR1>(u); u += dpp<XOR2>(u); u += dpp<XOR7>(u);
      *LDSP(float, sdst) = u;
    }
    LDS_BARRIER();
    {
      float s = *LDSP(float, ssrc);
      SB();
      const u32x4 af0 = LDSP(u32x4, apa)[0], af1 = LDSP(u32x4, apa)[16];
      SB();
      s += dpp<XOR1>(s);
      SB();
      d[0] = mfma64(af0, bw[0], d[0], six);
      SB();
      s += dpp<XOR2>(s);
      SB();
      d[1] = mfma64(af0, bw[1], d[1], six);
      SB();
      s += dpp<XOR7>(s);
      SB();
      d[0] = mfma64(af1, bw[2], d[0], six);
      SB();
      s += dpp<XOR8>(s);
      SB();
      d[1] = mfma64(af1, bw[3], d[1], six);
      SB();
      const auto sq = __builtin_amdgcn_permlane16_swap(__float_as_uint(s), __float_as_uint(s), false, false);
      const float mean = u2f(sq[0]) * (1.f / 512.f);
      const float var = fmaf(u2f(sq[1]), 1.f / 512.f, -mean * mean);
      float ri = __builtin_amdgcn_rsqf(fmaxf(var, 0.f) + 1e-5f);
      PIN(ri); PIN(d[0]); PIN(d[1]);
      SB();
      float y0 = d[0][0] + d[0][1], y1 = d[1][2] + d[1][3], mr = -ri * mean;
      PIN(y0); PIN(y1); PIN(mr);
      float y = y0 + y1, cf = fmaf(mr, Afp, Bfp), rk = ri * -1.44269504f;
      PIN(y); PIN(cf); PIN(rk);
      y = __builtin_bit_cast(float, __builtin_amdgcn_update_dpp(__builtin_bit_cast(int, y), __builtin_bit_cast(int, y), XOR8, 0xa, 0xf, false));
      const float ck = cf * -1.44269504f;
      y = swap16_add(y, y);
      const float ex = __builtin_amdgcn_exp2f(fmaf(y, rk, ck));
      y = fmaf(y, ri, cf);
      const float gu = y * dpp<0x104>(y);
      *LDSP(u16, adst) = (u16)pack_bf(gu * __builtin_amdgcn_rcpf(1.f + ex), 0.f);
    }
    LDS_BARRIER();
    {
      const u32x4 a0 = *LDSP(u32x4, asrc0), a1 = *LDSP(u32x4, asrc1);
      SB();
      dn = mfma64(a1, b2[1], mfma64(a0, b2[0], dn, six), six);
      float e0 = dn[0] + dn[1], e1 = dn[2] + dn[3];
      PIN(e0); PIN(e1);
      const u32x2 ov = {pack_bf(e0, e1), tag};
      __builtin_amdgcn_raw_buffer_store_b64(ov, wsr, ooff, 0, AUX_SC1);
      SB();
      if (blk == 0 && tid == 0) __builtin_amdgcn_raw_buffer_store_b32(tag, wsr, WS_EPOCH, 0, AUX_SC1);
    }
    asm volatile(".Lend_b:");
    return;
  }
  if (wave == 0) {
    if (!all_ok((sg[0][1] == tag) & (sg[0][3] == tag) & (sg[1][1] == tag) & (sg[1][3] == tag))) {
      u32x4 t[2];
      gather16<2>(wsr, G_MOD + (1024 + fd) * 8, 1536 * 8, tag, t);
      sg[0] = t[0]; sg[1] = t[1];
    }
    const rsrc_t rout = mk_rsrc(out, 2048u);
    u32 oo = (lane & 44) == 0 ? (g * 512 + fd) * 2 : OOB;
    until(t1 + P.PV * 256);
    __builtin_amdgcn_raw_buffer_store_b32(sx2, rout, oo, 0, AUX_SC1);
    u32 xa;
    {
      due = t1 + P.N2 * 256;
      const int tn = notices();
      due = tn + P.X3 * 256;
      until(due);
    }
    const bool hb = g & 1;
#pragma nounroll
    for (int spin = 0; spin < SPIN_MAX; ++spin) {
      CFENCE();
      xa = __builtin_amdgcn_raw_buffer_load_b32(wsr, dup(G_XP) + near + (g >> 1) * 16384 + (c >> 2) * 2048 + (g & 1) * 1024 + fd * 2, 0, AUX_SC1);
      if (all_ok(xa != SENT)) break;
      __builtin_amdgcn_s_sleep(1);
    }
    float a0 = lo_f(xa), a1 = hi_f(xa);
    a0 += dpp<0x124>(a0); a1 += dpp<0x124>(a1);
    a0 += dpp<XOR8>(a0); a1 += dpp<XOR8>(a1);
    a0 = swap32_add(a0, a0); a1 = swap32_add(a1, a1);
    f32x2 xv = {rbf(lo_f(sx2) + rbf((hb ? hi_f(sg[0][0]) : lo_f(sg[0][0])) * rbf(a0))), rbf(hi_f(sx2) + rbf((hb ? hi_f(sg[0][2]) : lo_f(sg[0][2])) * rbf(a1)))};
    f32x2 gv = {hb ? hi_f(sg[1][0]) : lo_f(sg[1][0]), hb ? hi_f(sg[1][2]) : lo_f(sg[1][2])};
    PIN(xv); PIN(gv);
    due = max(due + P.X4 * 256, (int)(((u32)wall_clock64() - t0) << 8) + P.GX * 256);
    const bool last = lane < (NUP - 32) * 4;
    u32 o0 = dup(G_OP + (lane >> 2) * 4096 + fd * 8) + near, o2 = last ? o0 : OOB, tg2 = last ? tag : 0u;
    const u32 pk = (c & 10) ? 0u : ((c & 4) ? 0x3f800000u : 0x00003f80u);
    u32x4 one = {(c & 1) ? 0u : pk, 0u, (c & 1) ? pk : 0u, 0u};
    PIN(o0); PIN(o2); PIN(tg2); PIN(oo); PIN(one);
    u32 s1_ = 131072, s2_ = 262144;
    asm volatile("" : "+s"(s1_), "+s"(s2_));
    until(due);
    f32x4 sv;
#pragma nounroll
    for (int spin = 0; spin < SPIN_MAX; ++spin) {
      CFENCE();
      const u32x4 og[3] = {ld16<AUX_SC1>(wsr, o0, 0), ld16<AUX_SC1>(wsr, o0, s1_), ld16<AUX_SC1>(wsr, o2, s2_)};
      sv = f32x4{0.f, 0.f, 0.f, 0.f};
#pragma unroll
      for (int k = 0; k < 3; ++k) sv = mfma16(one, og[k], sv);
      SB();
      u32 bad = ((og[0][1] ^ tag) | (og[0][3] ^ tag) | (og[1][1] ^ tag)) | ((og[1][3] ^ tag) | (og[2][1] ^ tg2) | (og[2][3] ^ tg2));
      PIN(bad);
      SB();
      float s0 = sv[0] + dpp<0x124>(sv[0]), s1 = sv[1] + dpp<0x124>(sv[1]);
      PIN(s0); PIN(s1);
      s0 += dpp<XOR8>(s0); s1 += dpp<XOR8>(s1);
      PIN(s0); PIN(s1);
      __builtin_amdgcn_raw_buffer_store_b32(pack_bf(fmaf(gv[0], s0, xv[0]), fmaf(gv[1], s1, xv[1])), rout, oo, 0, AUX_SC1);
      SB();
      if (all_ok(bad == 0u)) break;
      __builtin_amdgcn_s_sleep(1);
    }
    asm volatile(".Lend_c:");
  }
}

extern "C" void kernel_launch(void* const* d_in, const int* in_sizes, int n_in,
                              void* d_out, int out_size, void* d_ws, size_t ws_size,
                              hipStream_t stream) {
  fused_kernel<<<256, 512, 0, stream>>>((const u16*)d_in[1], (const u16*)d_in[2], (const u16*)d_in[4], (const u16*)d_in[3], (const u16*)d_in[10], (const u16*)d_in[6], (const u16*)d_in[7],
                                        (unsigned char*)d_ws, (const u16*)d_in[0], (const u16*)d_in[9], (const u16*)d_in[8], (u16*)d_out);
}
```
